```python
import torch

def _softmax(x, axis=-1):
    return torch.softmax(x, dim=axis)
def _gelu(x):
    return torch.nn.functional.gelu(x, approximate="tanh")
def _silu(x):
    return torch.nn.functional.silu(x)
def _sigmoid(x):
    return torch.sigmoid(x)
def _layernorm(x, axis=-1, eps=1e-5):
    m = x.mean(dim=axis, keepdim=True)
    v = x.var(dim=axis, keepdim=True, unbiased=False)
    return (x - m) / torch.sqrt(v + eps)
def _scatter_add(tgt, idx, src, axis=0):
    return tgt.clone().index_add_(axis, idx, src)
def _segment_sum(src, seg, n):
    out = torch.zeros((n,) + src.shape[1:], dtype=src.dtype)
    return out.index_add_(0, seg, src)
def _pad(x, pw):
    flat = tuple(v for pair in reversed(pw) for v in pair)
    return torch.nn.functional.pad(x, flat)

kernel_name = 'standalone__depthwise_separable__vision'

def setup_inputs():

    _w = (lambda a: (lambda t: t.to(torch.bfloat16) if t.is_floating_point() else t)(torch.as_tensor(a)))
    H = W = 384
    C = 384
    k = 1
    return {
        "x": _w((lambda *s: torch.randn(*s, dtype=torch.bfloat16) * 0.05)(1, H, W, C)),
        "dw": _w((lambda *s: torch.randn(*s, dtype=torch.bfloat16) * 0.05)(k, k, C)),
        "pw": _w((lambda *s: torch.randn(*s, dtype=torch.bfloat16) * 0.05)(C, C)),
    }

def reference(x, dw, pw):
    B, H, W, C = x.shape
    k = dw.shape[0]
    pad = k // 2
    xp = _pad(x, ((0,0),(pad,pad),(pad,pad),(0,0)))
    out = torch.zeros_like(x)
    for i in range(k):
        for j in range(k):
            out = out + xp[:, i:i+H, j:j+W, :] * dw[i, j][None, None, None, :]
    return torch.einsum('bhwc,cd->bhwd', out, pw)
```

```cpp
#include <hip/hip_runtime.h>

constexpr unsigned ROWB = 768, TILEB = 12288, NWG = 256;

__global__ __launch_bounds__(256, 1) void dwsep_kernel(const unsigned short* __restrict__ x,
                                                       const unsigned short* __restrict__ dw,
                                                       const unsigned short* __restrict__ pw,
                                                       unsigned short* __restrict__ out, unsigned nbytes,
                                                       unsigned ntrip) {
  __shared__ __attribute__((aligned(256))) char lds[163328];
  const unsigned tid = threadIdx.x, lane = tid & 63u;
  const unsigned q = __builtin_amdgcn_readfirstlane(tid >> 6);
  const unsigned r = lane & 15u, g = lane >> 4;
  const unsigned ldsb = (unsigned)(size_t)lds;
  __amdgpu_buffer_rsrc_t srdW = __builtin_amdgcn_make_buffer_rsrc((void*)pw, 0, 384 * 768, 0x00020000);
  __amdgpu_buffer_rsrc_t srdD = __builtin_amdgcn_make_buffer_rsrc((void*)dw, 0, ROWB, 0x00020000);
  const unsigned stv = q * 3072u + lane * 16u;
  unsigned vx[3], vw[3], str[3];
#pragma unroll
  for (int j = 0; j < 3; ++j) {
    const unsigned r16 = __builtin_amdgcn_ubfe(j == 0 ? 0x10000000u : j == 1 ? 0x20201010u : 0x30303020u, g * 8u, 8u);
    vx[j] = stv ^ (q << 6) ^ r16;
    const unsigned idx = 64u * j + lane, row = (idx * 171u) >> 11;
    vw[j] = row * (ROWB - 192u) + 192u * q + ((lane * 16u) ^ ((row & 8u) << 2));
    str[j] = ldsb + stv + (q << 6) + r16;
  }
  const unsigned tq = (lane >> 2) & 3u, tp = lane & 3u;
  const unsigned tb0 = ldsb + 6144u * q + (8u * g + tq) * 192u + 32u * (g & 1u) + 8u * tp;
  const unsigned fb0 = ldsb + r * ROWB + ((g ^ r) * 16u);
  const unsigned stw = ldsb + r * 784u + 192u * q + 8u * g;
  const unsigned dwt = ldsb + 160256u;
  const unsigned dga = dwt + 64u * g + 16u * tp;
  const unsigned de = tid < 192u ? tid : 191u;
  const unsigned dwo = 16u * (de >> 2) + 2u * (de & 3u);
  const unsigned soff0 = blockIdx.x * TILEB;
  const unsigned grp = (blockIdx.x >> 3) & 31u;
  asm volatile(
      "s_mov_b64 s[40:41], %[srdX_lo]\n\t"
      "s_mov_b32 s42, %[nbytes]\n\t"
      "s_mov_b32 s43, 0x00020000\n\t"
      "buffer_load_ushort v224, %[dwo], %[srdD], 0 offen\n\t"
      "buffer_load_ushort v225, %[dwo], %[srdD], 0 offen offset:8\n\t"
      "buffer_load_dwordx4 a[200:203], %[stv], %[srdW], %[wpre] offen offset:0\n\t"
      "buffer_load_dwordx4 a[204:207], %[stv], %[srdW], %[wpre] offen offset:1024\n\t"
      "buffer_load_dwordx4 a[208:211], %[stv], %[srdW], %[wpre] offen offset:2048\n\t"
      "s_mov_b64 s[44:45], %[srdO_lo]\n\t"
      "s_mov_b32 s46, 0\n\t"
      "s_mov_b32 s47, 0x00020000\n\t"
      "s_getpc_b64 s[36:37]\n\t"
      ".Lpc_%=:\n\t"
      "s_mul_i32 s30, %[tn], 256\n\t"
      "s_add_u32 s31, s30, 0\n\t"
      "s_min_u32 s31, s31, .Lend_%=-.Lpc_%=-4\n\t"
      "s_load_dword s35, s[36:37], s31\n\t"
      "s_add_u32 s31, s30, 64\n\t"
      "s_min_u32 s31, s31, .Lend_%=-.Lpc_%=-4\n\t"
      "s_load_dword s35, s[36:37], s31\n\t"
      "s_add_u32 s31, s30, 128\n\t"
      "s_min_u32 s31, s31, .Lend_%=-.Lpc_%=-4\n\t"
      "s_load_dword s35, s[36:37], s31\n\t"
      "s_add_u32 s31, s30, 192\n\t"
      "s_min_u32 s31, s31, .Lend_%=-.Lpc_%=-4\n\t"
      "s_load_dword s35, s[36:37], s31\n\t"
      "s_and_b32 s31, %[tn], 63\n\t"
      "s_lshl_b32 s31, s31, 21\n\t"
      "s_sub_u32 s34, %[nbytes], 64\n\t"
      "s_min_u32 s31, s31, s34\n\t"
      "s_bitcmp1_b32 %[tn], 6\n\t"
      "s_cselect_b64 s[32:33], s[44:45], s[40:41]\n\t"
      "s_load_dword s35, s[32:33], s31\n\t"
      "s_add_u32 m0, %[m0base], 25088\n\t"
      "s_add_u32 s48, %[soff0], 0\n\t"
      "buffer_load_dwordx4 %[vx0], s[40:43], s48 offen lds\n\t"
      "buffer_load_dwordx4 %[vx1], s[40:43], s48 offen offset:1024 lds\n\t"
      "buffer_load_dwordx4 %[vx2], s[40:43], s48 offen offset:2048 lds\n\t"
      "s_add_u32 m0, %[m0w], 0\n\t"
      "s_mov_b32 s54, 0\n\t"
      "buffer_load_dwordx4 %[vw0], %[srdW], s54 offen lds\n\t"
      "buffer_load_dwordx4 %[vw1], %[srdW], s54 offen offset:1024 lds\n\t"
      "buffer_load_dwordx4 %[vw2], %[srdW], s54 offen offset:2048 lds\n\t"
      "s_add_u32 m0, %[m0w], 3072\n\t"
      "s_mov_b32 s54, 12288\n\t"
      "buffer_load_dwordx4 %[vw0], %[srdW], s54 offen lds\n\t"
      "buffer_load_dwordx4 %[vw1], %[srdW], s54 offen offset:1024 lds\n\t"
      "buffer_load_dwordx4 %[vw2], %[srdW], s54 offen offset:2048 lds\n\t"
      "v_and_b32 v226, 0xfffffff0, %[dwo]\n\t"
      "v_and_b32 v227, 6, %[dwo]\n\t"
      "v_lshlrev_b32 v226, 2, v226\n\t"
      "v_lshl_add_u32 v226, v227, 3, v226\n\t"
      "v_add_u32 v226, %[dwt], v226\n\t"
      "v_and_b32 v232, 2, %[dwo]\n\t"
      "v_lshlrev_b32 v232, 3, v232\n\t"
      "v_cmp_gt_u32 vcc, 4, v227\n\t"
      "v_mov_b32 v56, %[fb0]\n\t"
      "v_xor_b32 v57, 64, %[fb0]\n\t"
      "v_xor_b32 v58, 128, %[fb0]\n\t"
      "v_xor_b32 v59, 192, %[fb0]\n\t"
      "v_add_u32 v60, 86528, %[tb0]\n\t"
      "v_add_u32 v61, 86528, %[tb1]\n\t"
      "s_waitcnt vmcnt(12)\n\t"
      "v_lshlrev_b32 v224, v232, v224\n\t"
      "v_lshlrev_b32 v225, v232, v225\n\t"
      "v_cndmask_b32 v228, 0, v224, vcc\n\t"
      "v_cndmask_b32 v230, 0, v225, vcc\n\t"
      "v_sub_u32 v229, v224, v228\n\t"
      "v_sub_u32 v231, v225, v230\n\t"
      "ds_write_b128 v226, v[228:231]\n\t"
      "s_add_u32 m0, %[m0w], 86528\n\t"
      "s_mov_b32 s54, 24576\n\t"
      "buffer_load_dwordx4 %[vw0], %[srdW], s54 offen lds\n\t"
      "buffer_load_dwordx4 %[vw1], %[srdW], s54 offen offset:1024 lds\n\t"
      "buffer_load_dwordx4 %[vw2], %[srdW], s54 offen offset:2048 lds\n\t"
      "s_add_u32 m0, %[m0w], 89600\n\t"
      "s_mov_b32 s54, 36864\n\t"
      "buffer_load_dwordx4 %[vw0], %[srdW], s54 offen lds\n\t"
      "buffer_load_dwordx4 %[vw1], %[srdW], s54 offen offset:1024 lds\n\t"
      "buffer_load_dwordx4 %[vw2], %[srdW], s54 offen offset:2048 lds\n\t"
      "s_waitcnt lgkmcnt(0)\n\t"
      "s_waitcnt vmcnt(12)\n\t"
      "s_barrier\n\t"
      "s_waitcnt vmcnt(6)\n\t"
      "ds_read_b128 a[180:183], %[dga] offset:0\n\t"
      "ds_read_b64_tr_b16 v[64:65], %[tb0] offset:0\n\t"
      "ds_read_b64_tr_b16 v[66:67], %[tb0] offset:768\n\t"
      "ds_read_b64_tr_b16 v[68:69], %[tb1] offset:0\n\t"
      "ds_read_b64_tr_b16 v[70:71], %[tb1] offset:768\n\t"
      "ds_read_b64_tr_b16 v[72:73], %[tb0] offset:64\n\t"
      "ds_read_b64_tr_b16 v[74:75], %[tb0] offset:832\n\t"
      "ds_read_b64_tr_b16 v[76:77], %[tb1] offset:64\n\t"
      "ds_read_b64_tr_b16 v[78:79], %[tb1] offset:832\n\t"
      "ds_read_b64_tr_b16 a[60:61], %[tb0] offset:128\n\t"
      "ds_read_b64_tr_b16 a[62:63], %[tb0] offset:896\n\t"
      "ds_read_b64_tr_b16 a[64:65], %[tb1] offset:128\n\t"
      "ds_read_b64_tr_b16 a[66:67], %[tb1] offset:896\n\t"
      "s_add_u32 m0, %[m0w], 111104\n\t"
      "s_mov_b32 s54, 49152\n\t"
      "buffer_load_dwordx4 %[vw0], %[srdW], s54 offen lds\n\t"
      "buffer_load_dwordx4 %[vw1], %[srdW], s54 offen offset:1024 lds\n\t"
      "buffer_load_dwordx4 %[vw2], %[srdW], s54 offen offset:2048 lds\n\t"
      "s_add_u32 m0, %[m0w], 114176\n\t"
      "s_mov_b32 s54, 61440\n\t"
      "buffer_load_dwordx4 %[vw0], %[srdW], s54 offen lds\n\t"
      "buffer_load_dwordx4 %[vw1], %[srdW], s54 offen offset:1024 lds\n\t"
      "buffer_load_dwordx4 %[vw2], %[srdW], s54 offen offset:2048 lds\n\t"
      "s_waitcnt vmcnt(6)\n\t"
      "s_waitcnt lgkmcnt(0)\n\t"
      "; step\n\t"
      "v_mfma_f32_4x4x4_16b_bf16 v[40:43], a[180:181], v[64:65], 0\n\t"
      "ds_read_b128 a[184:187], %[dga] offset:256\n\t"
      "v_mfma_f32_4x4x4_16b_bf16 v[44:47], a[182:183], v[66:67], 0\n\t"
      "ds_read_b64_tr_b16 v[80:81], v60 offset:0\n\t"
      "v_mfma_f32_4x4x4_16b_bf16 v[48:51], a[180:181], v[68:69], 0\n\t"
      "ds_read_b64_tr_b16 v[82:83], v60 offset:768\n\t"
      "v_mfma_f32_4x4x4_16b_bf16 v[52:55], a[182:183], v[70:71], 0\n\t"
      "ds_read_b64_tr_b16 v[84:85], v61 offset:0\n\t"
      "ds_read_b64_tr_b16 v[86:87], v61 offset:768\n\t"
      "s_add_u32 m0, %[m0w], 135680\n\t"
      "s_mov_b32 s54, 73728\n\t"
      "buffer_load_dwordx4 %[vw0], %[srdW], s54 offen lds\n\t"
      "buffer_load_dwordx4 %[vw1], %[srdW], s54 offen offset:1024 lds\n\t"
      "v_cvt_pk_bf16_f32 v64, v40, v41\n\t"
      "v_cvt_pk_bf16_f32 v65, v42, v43\n\t"
      "v_cvt_pk_bf16_f32 v66, v44, v45\n\t"
      "v_cvt_pk_bf16_f32 v67, v46, v47\n\t"
      "v_cvt_pk_bf16_f32 v68, v48, v49\n\t"
      "v_cvt_pk_bf16_f32 v69, v50, v51\n\t"
      "v_cvt_pk_bf16_f32 v70, v52, v53\n\t"
      "v_cvt_pk_bf16_f32 v71, v54, v55\n\t"
      "v_mfma_f32_4x4x4_16b_bf16 v[40:43], a[180:181], v[72:73], 0\n\t"
      "ds_read_b64_tr_b16 v[88:89], v60 offset:64\n\t"
      "v_mfma_f32_4x4x4_16b_bf16 v[44:47], a[182:183], v[74:75], 0\n\t"
      "ds_read_b64_tr_b16 v[90:91], v60 offset:832\n\t"
      "v_mfma_f32_4x4x4_16b_bf16 v[48:51], a[180:181], v[76:77], 0\n\t"
      "ds_read_b64_tr_b16 v[92:93], v61 offset:64\n\t"
      "v_mfma_f32_4x4x4_16b_bf16 v[52:55], a[182:183], v[78:79], 0\n\t"
      "ds_read_b64_tr_b16 v[94:95], v61 offset:832\n\t"
      "buffer_load_dwordx4 %[vw2], %[srdW], s54 offen offset:2048 lds\n\t"
      "s_add_u32 m0, %[m0w], 138752\n\t"
      "s_mov_b32 s54, 86016\n\t"
      "buffer_load_dwordx4 %[vw0], %[srdW], s54 offen lds\n\t"
      "v_cvt_pk_bf16_f32 v72, v40, v41\n\t"
      "v_cvt_pk_bf16_f32 v73, v42, v43\n\t"
      "v_cvt_pk_bf16_f32 v74, v44, v45\n\t"
      "v_cvt_pk_bf16_f32 v75, v46, v47\n\t"
      "v_cvt_pk_bf16_f32 v76, v48, v49\n\t"
      "v_cvt_pk_bf16_f32 v77, v50, v51\n\t"
      "v_cvt_pk_bf16_f32 v78, v52, v53\n\t"
      "v_cvt_pk_bf16_f32 v79, v54, v55\n\t"
      "ds_read_b128 a[24:27], v56 offset:25088\n\t"
      "v_mfma_f32_4x4x4_16b_bf16 v[40:43], a[180:181], a[60:61], 0\n\t"
      "ds_read_b64_tr_b16 a[68:69], v60 offset:128\n\t"
      "v_mfma_f32_4x4x4_16b_bf16 v[44:47], a[182:183], a[62:63], 0\n\t"
      "ds_read_b64_tr_b16 a[70:71], v60 offset:896\n\t"
      "v_mfma_f32_4x4x4_16b_bf16 v[48:51], a[180:181], a[64:65], 0\n\t"
      "ds_read_b64_tr_b16 a[72:73], v61 offset:128\n\t"
      "v_mfma_f32_4x4x4_16b_bf16 v[52:55], a[182:183], a[66:67], 0\n\t"
      "ds_read_b64_tr_b16 a[74:75], v61 offset:896\n\t"
      "buffer_load_dwordx4 %[vw1], %[srdW], s54 offen offset:1024 lds\n\t"
      "buffer_load_dwordx4 %[vw2], %[srdW], s54 offen offset:2048 lds\n\t"
      "v_cvt_pk_bf16_f32 v40, v40, v41\n\t"
      "v_cvt_pk_bf16_f32 v41, v42, v43\n\t"
      "v_cvt_pk_bf16_f32 v44, v44, v45\n\t"
      "v_cvt_pk_bf16_f32 v45, v46, v47\n\t"
      "v_cvt_pk_bf16_f32 v48, v48, v49\n\t"
      "v_cvt_pk_bf16_f32 v49, v50, v51\n\t"
      "v_cvt_pk_bf16_f32 v52, v52, v53\n\t"
      "v_cvt_pk_bf16_f32 v53, v54, v55\n\t"
      "v_accvgpr_write_b32 a60, v40\n\t"
      "v_accvgpr_write_b32 a61, v41\n\t"
      "v_accvgpr_write_b32 a62, v44\n\t"
      "v_accvgpr_write_b32 a63, v45\n\t"
      "v_accvgpr_write_b32 a64, v48\n\t"
      "v_accvgpr_write_b32 a65, v49\n\t"
      "v_accvgpr_write_b32 a66, v52\n\t"
      "v_accvgpr_write_b32 a67, v53\n\t"
      "s_waitcnt vmcnt(6)\n\t"
      "s_waitcnt lgkmcnt(0)\n\t"
      "; step\n\t"
      "v_mfma_f32_4x4x4_16b_bf16 v[40:43], a[184:185], v[80:81], 0\n\t"
      "ds_read_b128 a[180:183], %[dga] offset:512\n\t"
      "v_mfma_f32_4x4x4_16b_bf16 v[44:47], a[186:187], v[82:83], 0\n\t"
      "ds_read_b64_tr_b16 v[96:97], v60 offset:24576\n\t"
      "v_mfma_f32_4x4x4_16b_bf16 v[48:51], a[184:185], v[84:85], 0\n\t"
      "ds_read_b64_tr_b16 v[98:99], v60 offset:25344\n\t"
      "v_mfma_f32_4x4x4_16b_bf16 v[52:55], a[186:187], v[86:87], 0\n\t"
      "ds_read_b64_tr_b16 v[100:101], v61 offset:24576\n\t"
      "ds_read_b64_tr_b16 v[102:103], v61 offset:25344\n\t"
      "s_add_u32 m0, %[m0w], 0\n\t"
      "s_mov_b32 s54, 98304\n\t"
      "buffer_load_dwordx4 %[vw0], %[srdW], s54 offen lds\n\t"
      "buffer_load_dwordx4 %[vw1], %[srdW], s54 offen offset:1024 lds\n\t"
      "v_mfma_f32_16x16x32_bf16 v[16:19], v[64:67], a[24:27], 0\n\t"
      "v_cvt_pk_bf16_f32 v80, v40, v41\n\t"
      "v_cvt_pk_bf16_f32 v81, v42, v43\n\t"
      "v_mfma_f32_16x16x32_bf16 v[20:23], v[68:71], a[24:27], 0\n\t"
      "v_cvt_pk_bf16_f32 v82, v44, v45\n\t"
      "v_cvt_pk_bf16_f32 v83, v46, v47\n\t"
      "v_cvt_pk_bf16_f32 v84, v48, v49\n\t"
      "v_cvt_pk_bf16_f32 v85, v50, v51\n\t"
      "v_cvt_pk_bf16_f32 v86, v52, v53\n\t"
      "v_cvt_pk_bf16_f32 v87, v54, v55\n\t"
      "v_mfma_f32_4x4x4_16b_bf16 v[40:43], a[184:185], v[88:89], 0\n\t"
      "ds_read_b64_tr_b16 v[104:105], v60 offset:24640\n\t"
      "v_mfma_f32_4x4x4_16b_bf16 v[44:47], a[186:187], v[90:91], 0\n\t"
      "ds_read_b64_tr_b16 v[106:107], v60 offset:25408\n\t"
      "v_mfma_f32_4x4x4_16b_bf16 v[48:51], a[184:185], v[92:93], 0\n\t"
      "ds_read_b64_tr_b16 v[108:109], v61 offset:24640\n\t"
      "v_mfma_f32_4x4x4_16b_bf16 v[52:55], a[186:187], v[94:95], 0\n\t"
      "ds_read_b64_tr_b16 v[110:111], v61 offset:25408\n\t"
      "buffer_load_dwordx4 %[vw2], %[srdW], s54 offen offset:2048 lds\n\t"
      "s_add_u32 m0, %[m0w], 3072\n\t"
      "s_mov_b32 s54, 110592\n\t"
      "buffer_load_dwordx4 %[vw0], %[srdW], s54 offen lds\n\t"
      "v_mfma_f32_16x16x32_bf16 v[24:27], v[72:75], a[24:27], 0\n\t"
      "v_cvt_pk_bf16_f32 v88, v40, v41\n\t"
      "v_cvt_pk_bf16_f32 v89, v42, v43\n\t"
      "v_mfma_f32_16x16x32_bf16 v[28:31], v[76:79], a[24:27], 0\n\t"
      "v_cvt_pk_bf16_f32 v90, v44, v45\n\t"
      "v_cvt_pk_bf16_f32 v91, v46, v47\n\t"
      "v_cvt_pk_bf16_f32 v92, v48, v49\n\t"
      "v_cvt_pk_bf16_f32 v93, v50, v51\n\t"
      "v_cvt_pk_bf16_f32 v94, v52, v53\n\t"
      "v_cvt_pk_bf16_f32 v95, v54, v55\n\t"
      "ds_read_b128 a[28:31], v57 offset:25088\n\t"
      "v_mfma_f32_4x4x4_16b_bf16 v[40:43], a[184:185], a[68:69], 0\n\t"
      "ds_read_b64_tr_b16 a[76:77], v60 offset:24704\n\t"
      "v_mfma_f32_4x4x4_16b_bf16 v[44:47], a[186:187], a[70:71], 0\n\t"
      "ds_read_b64_tr_b16 a[78:79], v60 offset:25472\n\t"
      "v_mfma_f32_4x4x4_16b_bf16 v[48:51], a[184:185], a[72:73], 0\n\t"
      "ds_read_b64_tr_b16 a[80:81], v61 offset:24704\n\t"
      "v_mfma_f32_4x4x4_16b_bf16 v[52:55], a[186:187], a[74:75], 0\n\t"
      "ds_read_b64_tr_b16 a[82:83], v61 offset:25472\n\t"
      "buffer_load_dwordx4 %[vw1], %[srdW], s54 offen offset:1024 lds\n\t"
      "buffer_load_dwordx4 %[vw2], %[srdW], s54 offen offset:2048 lds\n\t"
      "v_mfma_f32_16x16x32_bf16 v[32:35], a[60:63], a[24:27], 0\n\t"
      "v_cvt_pk_bf16_f32 v40, v40, v41\n\t"
      "v_cvt_pk_bf16_f32 v41, v42, v43\n\t"
      "v_mfma_f32_16x16x32_bf16 v[36:39], a[64:67], a[24:27], 0\n\t"
      "v_cvt_pk_bf16_f32 v44, v44, v45\n\t"
      "v_cvt_pk_bf16_f32 v45, v46, v47\n\t"
      "v_cvt_pk_bf16_f32 v48, v48, v49\n\t"
      "v_cvt_pk_bf16_f32 v49, v50, v51\n\t"
      "v_cvt_pk_bf16_f32 v52, v52, v53\n\t"
      "v_cvt_pk_bf16_f32 v53, v54, v55\n\t"
      "v_accvgpr_write_b32 a68, v40\n\t"
      "v_accvgpr_write_b32 a69, v41\n\t"
      "v_accvgpr_write_b32 a70, v44\n\t"
      "v_accvgpr_write_b32 a71, v45\n\t"
      "v_accvgpr_write_b32 a72, v48\n\t"
      "v_accvgpr_write_b32 a73, v49\n\t"
      "v_accvgpr_write_b32 a74, v52\n\t"
      "v_accvgpr_write_b32 a75, v53\n\t"
      "s_waitcnt vmcnt(6)\n\t"
      "s_waitcnt lgkmcnt(0)\n\t"
      "; step\n\t"
      "v_mfma_f32_4x4x4_16b_bf16 v[40:43], a[180:181], v[96:97], 0\n\t"
      "ds_read_b128 a[184:187], %[dga] offset:768\n\t"
      "v_mfma_f32_4x4x4_16b_bf16 v[44:47], a[182:183], v[98:99], 0\n\t"
      "ds_read_b64_tr_b16 v[112:113], v60 offset:49152\n\t"
      "v_mfma_f32_4x4x4_16b_bf16 v[48:51], a[180:181], v[100:101], 0\n\t"
      "ds_read_b64_tr_b16 v[114:115], v60 offset:49920\n\t"
      "v_mfma_f32_4x4x4_16b_bf16 v[52:55], a[182:183], v[102:103], 0\n\t"
      "ds_read_b64_tr_b16 v[116:117], v61 offset:49152\n\t"
      "ds_read_b64_tr_b16 v[118:119], v61 offset:49920\n\t"
      "s_add_u32 m0, %[m0w], 86528\n\t"
      "s_mov_b32 s54, 122880\n\t"
      "buffer_load_dwordx4 %[vw0], %[srdW], s54 offen lds\n\t"
      "buffer_load_dwordx4 %[vw1], %[srdW], s54 offen offset:1024 lds\n\t"
      "v_mfma_f32_16x16x32_bf16 v[16:19], v[80:83], a[28:31], v[16:19]\n\t"
      "v_cvt_pk_bf16_f32 v96, v40, v41\n\t"
      "v_cvt_pk_bf16_f32 v97, v42, v43\n\t"
      "v_mfma_f32_16x16x32_bf16 v[20:23], v[84:87], a[28:31], v[20:23]\n\t"
      "v_cvt_pk_bf16_f32 v98, v44, v45\n\t"
      "v_cvt_pk_bf16_f32 v99, v46, v47\n\t"
      "v_cvt_pk_bf16_f32 v100, v48, v49\n\t"
      "v_cvt_pk_bf16_f32 v101, v50, v51\n\t"
      "v_cvt_pk_bf16_f32 v102, v52, v53\n\t"
      "v_cvt_pk_bf16_f32 v103, v54, v55\n\t"
      "v_mfma_f32_4x4x4_16b_bf16 v[40:43], a[180:181], v[104:105], 0\n\t"
      "ds_read_b64_tr_b16 v[120:121], v60 offset:49216\n\t"
      "v_mfma_f32_4x4x4_16b_bf16 v[44:47], a[182:183], v[106:107], 0\n\t"
      "ds_read_b64_tr_b16 v[122:123], v60 offset:49984\n\t"
      "v_mfma_f32_4x4x4_16b_bf16 v[48:51], a[180:181], v[108:109], 0\n\t"
      "ds_read_b64_tr_b16 v[124:125], v61 offset:49216\n\t"
      "v_mfma_f32_4x4x4_16b_bf16 v[52:55], a[182:183], v[110:111], 0\n\t"
      "ds_read_b64_tr_b16 v[126:127], v61 offset:49984\n\t"
      "buffer_load_dwordx4 %[vw2], %[srdW], s54 offen offset:2048 lds\n\t"
      "s_add_u32 m0, %[m0w], 89600\n\t"
      "s_mov_b32 s54, 135168\n\t"
      "buffer_load_dwordx4 %[vw0], %[srdW], s54 offen lds\n\t"
      "v_mfma_f32_16x16x32_bf16 v[24:27], v[88:91], a[28:31], v[24:27]\n\t"
      "v_cvt_pk_bf16_f32 v104, v40, v41\n\t"
      "v_cvt_pk_bf16_f32 v105, v42, v43\n\t"
      "v_mfma_f32_16x16x32_bf16 v[28:31], v[92:95], a[28:31], v[28:31]\n\t"
      "v_cvt_pk_bf16_f32 v106, v44, v45\n\t"
      "v_cvt_pk_bf16_f32 v107, v46, v47\n\t"
      "v_cvt_pk_bf16_f32 v108, v48, v49\n\t"
      "v_cvt_pk_bf16_f32 v109, v50, v51\n\t"
      "v_cvt_pk_bf16_f32 v110, v52, v53\n\t"
      "v_cvt_pk_bf16_f32 v111, v54, v55\n\t"
      "ds_read_b128 a[32:35], v58 offset:25088\n\t"
      "v_mfma_f32_4x4x4_16b_bf16 v[40:43], a[180:181], a[76:77], 0\n\t"
      "ds_read_b64_tr_b16 a[84:85], v60 offset:49280\n\t"
      "v_mfma_f32_4x4x4_16b_bf16 v[44:47], a[182:183], a[78:79], 0\n\t"
      "ds_read_b64_tr_b16 a[86:87], v60 offset:50048\n\t"
      "v_mfma_f32_4x4x4_16b_bf16 v[48:51], a[180:181], a[80:81], 0\n\t"
      "ds_read_b64_tr_b16 a[88:89], v61 offset:49280\n\t"
      "v_mfma_f32_4x4x4_16b_bf16 v[52:55], a[182:183], a[82:83], 0\n\t"
      "ds_read_b64_tr_b16 a[90:91], v61 offset:50048\n\t"
      "buffer_load_dwordx4 %[vw1], %[srdW], s54 offen offset:1024 lds\n\t"
      "buffer_load_dwordx4 %[vw2], %[srdW], s54 offen offset:2048 lds\n\t"
      "v_mfma_f32_16x16x32_bf16 v[32:35], a[68:71], a[28:31], v[32:35]\n\t"
      "v_cvt_pk_bf16_f32 v40, v40, v41\n\t"
      "v_cvt_pk_bf16_f32 v41, v42, v43\n\t"
      "v_mfma_f32_16x16x32_bf16 v[36:39], a[72:75], a[28:31], v[36:39]\n\t"
      "v_cvt_pk_bf16_f32 v44, v44, v45\n\t"
      "v_cvt_pk_bf16_f32 v45, v46, v47\n\t"
      "v_cvt_pk_bf16_f32 v48, v48, v49\n\t"
      "v_cvt_pk_bf16_f32 v49, v50, v51\n\t"
      "v_cvt_pk_bf16_f32 v52, v52, v53\n\t"
      "v_cvt_pk_bf16_f32 v53, v54, v55\n\t"
      "v_accvgpr_write_b32 a76, v40\n\t"
      "v_accvgpr_write_b32 a77, v41\n\t"
      "v_accvgpr_write_b32 a78, v44\n\t"
      "v_accvgpr_write_b32 a79, v45\n\t"
      "v_accvgpr_write_b32 a80, v48\n\t"
      "v_accvgpr_write_b32 a81, v49\n\t"
      "v_accvgpr_write_b32 a82, v52\n\t"
      "v_accvgpr_write_b32 a83, v53\n\t"
      "s_waitcnt vmcnt(6)\n\t"
      "s_waitcnt lgkmcnt(0)\n\t"
      "; step\n\t"
      "v_mfma_f32_4x4x4_16b_bf16 v[40:43], a[184:185], v[112:113], 0\n\t"
      "ds_read_b128 a[180:183], %[dga] offset:1024\n\t"
      "v_mfma_f32_4x4x4_16b_bf16 v[44:47], a[186:187], v[114:115], 0\n\t"
      "ds_read_b64_tr_b16 v[128:129], %[tb0] offset:0\n\t"
      "v_mfma_f32_4x4x4_16b_bf16 v[48:51], a[184:185], v[116:117], 0\n\t"
      "ds_read_b64_tr_b16 v[130:131], %[tb0] offset:768\n\t"
      "v_mfma_f32_4x4x4_16b_bf16 v[52:55], a[186:187], v[118:119], 0\n\t"
      "ds_read_b64_tr_b16 v[132:133], %[tb1] offset:0\n\t"
      "ds_read_b64_tr_b16 v[134:135], %[tb1] offset:768\n\t"
      "s_add_u32 m0, %[m0w], 111104\n\t"
      "s_mov_b32 s54, 147456\n\t"
      "buffer_load_dwordx4 %[vw0], %[srdW], s54 offen lds\n\t"
      "buffer_load_dwordx4 %[vw1], %[srdW], s54 offen offset:1024 lds\n\t"
      "v_mfma_f32_16x16x32_bf16 v[16:19], v[96:99], a[32:35], v[16:19]\n\t"
      "v_cvt_pk_bf16_f32 v112, v40, v41\n\t"
      "v_cvt_pk_bf16_f32 v113, v42, v43\n\t"
      "v_mfma_f32_16x16x32_bf16 v[20:23], v[100:103], a[32:35], v[20:23]\n\t"
      "v_cvt_pk_bf16_f32 v114, v44, v45\n\t"
      "v_cvt_pk_bf16_f32 v115, v46, v47\n\t"
      "v_cvt_pk_bf16_f32 v116, v48, v49\n\t"
      "v_cvt_pk_bf16_f32 v117, v50, v51\n\t"
      "v_cvt_pk_bf16_f32 v118, v52, v53\n\t"
      "v_cvt_pk_bf16_f32 v119, v54, v55\n\t"
      "v_mfma_f32_4x4x4_16b_bf16 v[40:43], a[184:185], v[120:121], 0\n\t"
      "ds_read_b64_tr_b16 v[136:137], %[tb0] offset:64\n\t"
      "v_mfma_f32_4x4x4_16b_bf16 v[44:47], a[186:187], v[122:123], 0\n\t"
      "ds_read_b64_tr_b16 v[138:139], %[tb0] offset:832\n\t"
      "v_mfma_f32_4x4x4_16b_bf16 v[48:51], a[184:185], v[124:125], 0\n\t"
      "ds_read_b64_tr_b16 v[140:141], %[tb1] offset:64\n\t"
      "v_mfma_f32_4x4x4_16b_bf16 v[52:55], a[186:187], v[126:127], 0\n\t"
      "ds_read_b64_tr_b16 v[142:143], %[tb1] offset:832\n\t"
      "buffer_load_dwordx4 %[vw2], %[srdW], s54 offen offset:2048 lds\n\t"
      "s_add_u32 m0, %[m0w], 114176\n\t"
      "s_mov_b32 s54, 159744\n\t"
      "buffer_load_dwordx4 %[vw0], %[srdW], s54 offen lds\n\t"
      "v_mfma_f32_16x16x32_bf16 v[24:27], v[104:107], a[32:35], v[24:27]\n\t"
      "v_cvt_pk_bf16_f32 v120, v40, v41\n\t"
      "v_cvt_pk_bf16_f32 v121, v42, v43\n\t"
      "v_mfma_f32_16x16x32_bf16 v[28:31], v[108:111], a[32:35], v[28:31]\n\t"
      "v_cvt_pk_bf16_f32 v122, v44, v45\n\t"
      "v_cvt_pk_bf16_f32 v123, v46, v47\n\t"
      "v_cvt_pk_bf16_f32 v124, v48, v49\n\t"
      "v_cvt_pk_bf16_f32 v125, v50, v51\n\t"
      "v_cvt_pk_bf16_f32 v126, v52, v53\n\t"
      "v_cvt_pk_bf16_f32 v127, v54, v55\n\t"
      "ds_read_b128 a[36:39], v59 offset:25088\n\t"
      "v_mfma_f32_4x4x4_16b_bf16 v[40:43], a[184:185], a[84:85], 0\n\t"
      "ds_read_b64_tr_b16 a[92:93], %[tb0] offset:128\n\t"
      "v_mfma_f32_4x4x4_16b_bf16 v[44:47], a[186:187], a[86:87], 0\n\t"
      "ds_read_b64_tr_b16 a[94:95], %[tb0] offset:896\n\t"
      "v_mfma_f32_4x4x4_16b_bf16 v[48:51], a[184:185], a[88:89], 0\n\t"
      "ds_read_b64_tr_b16 a[96:97], %[tb1] offset:128\n\t"
      "v_mfma_f32_4x4x4_16b_bf16 v[52:55], a[186:187], a[90:91], 0\n\t"
      "ds_read_b64_tr_b16 a[98:99], %[tb1] offset:896\n\t"
      "buffer_load_dwordx4 %[vw1], %[srdW], s54 offen offset:1024 lds\n\t"
      "buffer_load_dwordx4 %[vw2], %[srdW], s54 offen offset:2048 lds\n\t"
      "v_mfma_f32_16x16x32_bf16 v[32:35], a[76:79], a[32:35], v[32:35]\n\t"
      "v_cvt_pk_bf16_f32 v40, v40, v41\n\t"
      "v_cvt_pk_bf16_f32 v41, v42, v43\n\t"
      "v_mfma_f32_16x16x32_bf16 v[36:39], a[80:83], a[32:35], v[36:39]\n\t"
      "v_cvt_pk_bf16_f32 v44, v44, v45\n\t"
      "v_cvt_pk_bf16_f32 v45, v46, v47\n\t"
      "v_cvt_pk_bf16_f32 v48, v48, v49\n\t"
      "v_cvt_pk_bf16_f32 v49, v50, v51\n\t"
      "v_cvt_pk_bf16_f32 v52, v52, v53\n\t"
      "v_cvt_pk_bf16_f32 v53, v54, v55\n\t"
      "v_accvgpr_write_b32 a84, v40\n\t"
      "v_accvgpr_write_b32 a85, v41\n\t"
      "v_accvgpr_write_b32 a86, v44\n\t"
      "v_accvgpr_write_b32 a87, v45\n\t"
      "v_accvgpr_write_b32 a88, v48\n\t"
      "v_accvgpr_write_b32 a89, v49\n\t"
      "v_accvgpr_write_b32 a90, v52\n\t"
      "v_accvgpr_write_b32 a91, v53\n\t"
      "s_waitcnt vmcnt(6)\n\t"
      "s_waitcnt lgkmcnt(0)\n\t"
      "; step\n\t"
      "v_mfma_f32_4x4x4_16b_bf16 v[40:43], a[180:181], v[128:129], 0\n\t"
      "ds_read_b128 a[184:187], %[dga] offset:1280\n\t"
      "v_mfma_f32_4x4x4_16b_bf16 v[44:47], a[182:183], v[130:131], 0\n\t"
      "ds_read_b64_tr_b16 v[144:145], v60 offset:0\n\t"
      "v_mfma_f32_4x4x4_16b_bf16 v[48:51], a[180:181], v[132:133], 0\n\t"
      "ds_read_b64_tr_b16 v[146:147], v60 offset:768\n\t"
      "v_mfma_f32_4x4x4_16b_bf16 v[52:55], a[182:183], v[134:135], 0\n\t"
      "ds_read_b64_tr_b16 v[148:149], v61 offset:0\n\t"
      "ds_read_b64_tr_b16 v[150:151], v61 offset:768\n\t"
      "s_add_u32 m0, %[m0w], 135680\n\t"
      "s_mov_b32 s54, 172032\n\t"
      "buffer_load_dwordx4 %[vw0], %[srdW], s54 offen lds\n\t"
      "buffer_load_dwordx4 %[vw1], %[srdW], s54 offen offset:1024 lds\n\t"
      "v_mfma_f32_16x16x32_bf16 v[16:19], v[112:115], a[36:39], v[16:19]\n\t"
      "v_cvt_pk_bf16_f32 v128, v40, v41\n\t"
      "v_cvt_pk_bf16_f32 v129, v42, v43\n\t"
      "v_mfma_f32_16x16x32_bf16 v[20:23], v[116:119], a[36:39], v[20:23]\n\t"
      "v_cvt_pk_bf16_f32 v130, v44, v45\n\t"
      "v_cvt_pk_bf16_f32 v131, v46, v47\n\t"
      "v_cvt_pk_bf16_f32 v132, v48, v49\n\t"
      "v_cvt_pk_bf16_f32 v133, v50, v51\n\t"
      "v_cvt_pk_bf16_f32 v134, v52, v53\n\t"
      "v_cvt_pk_bf16_f32 v135, v54, v55\n\t"
      "v_mfma_f32_4x4x4_16b_bf16 v[40:43], a[180:181], v[136:137], 0\n\t"
      "ds_read_b64_tr_b16 v[152:153], v60 offset:64\n\t"
      "v_mfma_f32_4x4x4_16b_bf16 v[44:47], a[182:183], v[138:139], 0\n\t"
      "ds_read_b64_tr_b16 v[154:155], v60 offset:832\n\t"
      "v_mfma_f32_4x4x4_16b_bf16 v[48:51], a[180:181], v[140:141], 0\n\t"
      "ds_read_b64_tr_b16 v[156:157], v61 offset:64\n\t"
      "v_mfma_f32_4x4x4_16b_bf16 v[52:55], a[182:183], v[142:143], 0\n\t"
      "ds_read_b64_tr_b16 v[158:159], v61 offset:832\n\t"
      "buffer_load_dwordx4 %[vw2], %[srdW], s54 offen offset:2048 lds\n\t"
      "s_add_u32 m0, %[m0w], 138752\n\t"
      "s_mov_b32 s54, 184320\n\t"
      "buffer_load_dwordx4 %[vw0], %[srdW], s54 offen lds\n\t"
      "v_mfma_f32_16x16x32_bf16 v[24:27], v[120:123], a[36:39], v[24:27]\n\t"
      "v_cvt_pk_bf16_f32 v136, v40, v41\n\t"
      "v_cvt_pk_bf16_f32 v137, v42, v43\n\t"
      "v_mfma_f32_16x16x32_bf16 v[28:31], v[124:127], a[36:39], v[28:31]\n\t"
      "v_cvt_pk_bf16_f32 v138, v44, v45\n\t"
      "v_cvt_pk_bf16_f32 v139, v46, v47\n\t"
      "v_cvt_pk_bf16_f32 v140, v48, v49\n\t"
      "v_cvt_pk_bf16_f32 v141, v50, v51\n\t"
      "v_cvt_pk_bf16_f32 v142, v52, v53\n\t"
      "v_cvt_pk_bf16_f32 v143, v54, v55\n\t"
      "ds_read_b128 a[40:43], v56 offset:25344\n\t"
      "v_mfma_f32_4x4x4_16b_bf16 v[40:43], a[180:181], a[92:93], 0\n\t"
      "ds_read_b64_tr_b16 a[100:101], v60 offset:128\n\t"
      "v_mfma_f32_4x4x4_16b_bf16 v[44:47], a[182:183], a[94:95], 0\n\t"
      "ds_read_b64_tr_b16 a[102:103], v60 offset:896\n\t"
      "v_mfma_f32_4x4x4_16b_bf16 v[48:51], a[180:181], a[96:97], 0\n\t"
      "ds_read_b64_tr_b16 a[104:105], v61 offset:128\n\t"
      "v_mfma_f32_4x4x4_16b_bf16 v[52:55], a[182:183], a[98:99], 0\n\t"
      "ds_read_b64_tr_b16 a[106:107], v61 offset:896\n\t"
      "buffer_load_dwordx4 %[vw1], %[srdW], s54 offen offset:1024 lds\n\t"
      "buffer_load_dwordx4 %[vw2], %[srdW], s54 offen offset:2048 lds\n\t"
      "v_mfma_f32_16x16x32_bf16 v[32:35], a[84:87], a[36:39], v[32:35]\n\t"
      "v_cvt_pk_bf16_f32 v40, v40, v41\n\t"
      "v_cvt_pk_bf16_f32 v41, v42, v43\n\t"
      "v_mfma_f32_16x16x32_bf16 v[36:39], a[88:91], a[36:39], v[36:39]\n\t"
      "v_cvt_pk_bf16_f32 v44, v44, v45\n\t"
      "v_cvt_pk_bf16_f32 v45, v46, v47\n\t"
      "v_cvt_pk_bf16_f32 v48, v48, v49\n\t"
      "v_cvt_pk_bf16_f32 v49, v50, v51\n\t"
      "v_cvt_pk_bf16_f32 v52, v52, v53\n\t"
      "v_cvt_pk_bf16_f32 v53, v54, v55\n\t"
      "v_accvgpr_write_b32 a92, v40\n\t"
      "v_accvgpr_write_b32 a93, v41\n\t"
      "v_accvgpr_write_b32 a94, v44\n\t"
      "v_accvgpr_write_b32 a95, v45\n\t"
      "v_accvgpr_write_b32 a96, v48\n\t"
      "v_accvgpr_write_b32 a97, v49\n\t"
      "v_accvgpr_write_b32 a98, v52\n\t"
      "v_accvgpr_write_b32 a99, v53\n\t"
      "s_waitcnt vmcnt(6)\n\t"
      "s_waitcnt lgkmcnt(0)\n\t"
      "; step\n\t"
      "v_mfma_f32_4x4x4_16b_bf16 v[40:43], a[184:185], v[144:145], 0\n\t"
      "ds_read_b128 a[180:183], %[dga] offset:1536\n\t"
      "v_mfma_f32_4x4x4_16b_bf16 v[44:47], a[186:187], v[146:147], 0\n\t"
      "ds_read_b64_tr_b16 v[160:161], v60 offset:24576\n\t"
      "v_mfma_f32_4x4x4_16b_bf16 v[48:51], a[184:185], v[148:149], 0\n\t"
      "ds_read_b64_tr_b16 v[162:163], v60 offset:25344\n\t"
      "v_mfma_f32_4x4x4_16b_bf16 v[52:55], a[186:187], v[150:151], 0\n\t"
      "ds_read_b64_tr_b16 v[164:165], v61 offset:24576\n\t"
      "ds_read_b64_tr_b16 v[166:167], v61 offset:25344\n\t"
      "s_add_u32 m0, %[m0w], 0\n\t"
      "s_mov_b32 s54, 196608\n\t"
      "buffer_load_dwordx4 %[vw0], %[srdW], s54 offen lds\n\t"
      "buffer_load_dwordx4 %[vw1], %[srdW], s54 offen offset:1024 lds\n\t"
      "v_mfma_f32_16x16x32_bf16 v[16:19], v[128:131], a[40:43], v[16:19]\n\t"
      "v_cvt_pk_bf16_f32 v144, v40, v41\n\t"
      "v_cvt_pk_bf16_f32 v145, v42, v43\n\t"
      "v_mfma_f32_16x16x32_bf16 v[20:23], v[132:135], a[40:43], v[20:23]\n\t"
      "v_cvt_pk_bf16_f32 v146, v44, v45\n\t"
      "v_cvt_pk_bf16_f32 v147, v46, v47\n\t"
      "v_cvt_pk_bf16_f32 v148, v48, v49\n\t"
      "v_cvt_pk_bf16_f32 v149, v50, v51\n\t"
      "v_cvt_pk_bf16_f32 v150, v52, v53\n\t"
      "v_cvt_pk_bf16_f32 v151, v54, v55\n\t"
      "v_mfma_f32_4x4x4_16b_bf16 v[40:43], a[184:185], v[152:153], 0\n\t"
      "ds_read_b64_tr_b16 v[168:169], v60 offset:24640\n\t"
      "v_mfma_f32_4x4x4_16b_bf16 v[44:47], a[186:187], v[154:155], 0\n\t"
      "ds_read_b64_tr_b16 v[170:171], v60 offset:25408\n\t"
      "v_mfma_f32_4x4x4_16b_bf16 v[48:51], a[184:185], v[156:157], 0\n\t"
      "ds_read_b64_tr_b16 v[172:173], v61 offset:24640\n\t"
      "v_mfma_f32_4x4x4_16b_bf16 v[52:55], a[186:187], v[158:159], 0\n\t"
      "ds_read_b64_tr_b16 v[174:175], v61 offset:25408\n\t"
      "buffer_load_dwordx4 %[vw2], %[srdW], s54 offen offset:2048 lds\n\t"
      "s_add_u32 m0, %[m0w], 3072\n\t"
      "s_mov_b32 s54, 208896\n\t"
      "buffer_load_dwordx4 %[vw0], %[srdW], s54 offen lds\n\t"
      "v_mfma_f32_16x16x32_bf16 v[24:27], v[136:139], a[40:43], v[24:27]\n\t"
      "v_cvt_pk_bf16_f32 v152, v40, v41\n\t"
      "v_cvt_pk_bf16_f32 v153, v42, v43\n\t"
      "v_mfma_f32_16x16x32_bf16 v[28:31], v[140:143], a[40:43], v[28:31]\n\t"
      "v_cvt_pk_bf16_f32 v154, v44, v45\n\t"
      "v_cvt_pk_bf16_f32 v155, v46, v47\n\t"
      "v_cvt_pk_bf16_f32 v156, v48, v49\n\t"
      "v_cvt_pk_bf16_f32 v157, v50, v51\n\t"
      "v_cvt_pk_bf16_f32 v158, v52, v53\n\t"
      "v_cvt_pk_bf16_f32 v159, v54, v55\n\t"
      "ds_read_b128 a[44:47], v57 offset:25344\n\t"
      "v_mfma_f32_4x4x4_16b_bf16 v[40:43], a[184:185], a[100:101], 0\n\t"
      "ds_read_b64_tr_b16 a[108:109], v60 offset:24704\n\t"
      "v_mfma_f32_4x4x4_16b_bf16 v[44:47], a[186:187], a[102:103], 0\n\t"
      "ds_read_b64_tr_b16 a[110:111], v60 offset:25472\n\t"
      "v_mfma_f32_4x4x4_16b_bf16 v[48:51], a[184:185], a[104:105], 0\n\t"
      "ds_read_b64_tr_b16 a[112:113], v61 offset:24704\n\t"
      "v_mfma_f32_4x4x4_16b_bf16 v[52:55], a[186:187], a[106:107], 0\n\t"
      "ds_read_b64_tr_b16 a[114:115], v61 offset:25472\n\t"
      "buffer_load_dwordx4 %[vw1], %[srdW], s54 offen offset:1024 lds\n\t"
      "buffer_load_dwordx4 %[vw2], %[srdW], s54 offen offset:2048 lds\n\t"
      "v_mfma_f32_16x16x32_bf16 v[32:35], a[92:95], a[40:43], v[32:35]\n\t"
      "v_cvt_pk_bf16_f32 v40, v40, v41\n\t"
      "v_cvt_pk_bf16_f32 v41, v42, v43\n\t"
      "v_mfma_f32_16x16x32_bf16 v[36:39], a[96:99], a[40:43], v[36:39]\n\t"
      "v_cvt_pk_bf16_f32 v44, v44, v45\n\t"
      "v_cvt_pk_bf16_f32 v45, v46, v47\n\t"
      "v_cvt_pk_bf16_f32 v48, v48, v49\n\t"
      "v_cvt_pk_bf16_f32 v49, v50, v51\n\t"
      "v_cvt_pk_bf16_f32 v52, v52, v53\n\t"
      "v_cvt_pk_bf16_f32 v53, v54, v55\n\t"
      "v_accvgpr_write_b32 a100, v40\n\t"
      "v_accvgpr_write_b32 a101, v41\n\t"
      "v_accvgpr_write_b32 a102, v44\n\t"
      "v_accvgpr_write_b32 a103, v45\n\t"
      "v_accvgpr_write_b32 a104, v48\n\t"
      "v_accvgpr_write_b32 a105, v49\n\t"
      "v_accvgpr_write_b32 a106, v52\n\t"
      "v_accvgpr_write_b32 a107, v53\n\t"
      "s_waitcnt vmcnt(6)\n\t"
      "s_waitcnt lgkmcnt(0)\n\t"
      "; step\n\t"
      "v_mfma_f32_4x4x4_16b_bf16 v[40:43], a[180:181], v[160:161], 0\n\t"
      "ds_read_b128 a[184:187], %[dga] offset:1792\n\t"
      "v_mfma_f32_4x4x4_16b_bf16 v[44:47], a[182:183], v[162:163], 0\n\t"
      "ds_read_b64_tr_b16 v[176:177], v60 offset:49152\n\t"
      "v_mfma_f32_4x4x4_16b_bf16 v[48:51], a[180:181], v[164:165], 0\n\t"
      "ds_read_b64_tr_b16 v[178:179], v60 offset:49920\n\t"
      "v_mfma_f32_4x4x4_16b_bf16 v[52:55], a[182:183], v[166:167], 0\n\t"
      "ds_read_b64_tr_b16 v[180:181], v61 offset:49152\n\t"
      "ds_read_b64_tr_b16 v[182:183], v61 offset:49920\n\t"
      "s_add_u32 m0, %[m0w], 86528\n\t"
      "s_mov_b32 s54, 221184\n\t"
      "buffer_load_dwordx4 %[vw0], %[srdW], s54 offen lds\n\t"
      "buffer_load_dwordx4 %[vw1], %[srdW], s54 offen offset:1024 lds\n\t"
      "v_mfma_f32_16x16x32_bf16 v[16:19], v[144:147], a[44:47], v[16:19]\n\t"
      "v_cvt_pk_bf16_f32 v160, v40, v41\n\t"
      "v_cvt_pk_bf16_f32 v161, v42, v43\n\t"
      "v_mfma_f32_16x16x32_bf16 v[20:23], v[148:151], a[44:47], v[20:23]\n\t"
      "v_cvt_pk_bf16_f32 v162, v44, v45\n\t"
      "v_cvt_pk_bf16_f32 v163, v46, v47\n\t"
      "v_cvt_pk_bf16_f32 v164, v48, v49\n\t"
      "v_cvt_pk_bf16_f32 v165, v50, v51\n\t"
      "v_cvt_pk_bf16_f32 v166, v52, v53\n\t"
      "v_cvt_pk_bf16_f32 v167, v54, v55\n\t"
      "v_mfma_f32_4x4x4_16b_bf16 v[40:43], a[180:181], v[168:169], 0\n\t"
      "ds_read_b64_tr_b16 v[184:185], v60 offset:49216\n\t"
      "v_mfma_f32_4x4x4_16b_bf16 v[44:47], a[182:183], v[170:171], 0\n\t"
      "ds_read_b64_tr_b16 v[186:187], v60 offset:49984\n\t"
      "v_mfma_f32_4x4x4_16b_bf16 v[48:51], a[180:181], v[172:173], 0\n\t"
      "ds_read_b64_tr_b16 v[188:189], v61 offset:49216\n\t"
      "v_mfma_f32_4x4x4_16b_bf16 v[52:55], a[182:183], v[174:175], 0\n\t"
      "ds_read_b64_tr_b16 v[190:191], v61 offset:49984\n\t"
      "buffer_load_dwordx4 %[vw2], %[srdW], s54 offen offset:2048 lds\n\t"
      "s_add_u32 m0, %[m0w], 89600\n\t"
      "s_mov_b32 s54, 233472\n\t"
      "buffer_load_dwordx4 %[vw0], %[srdW], s54 offen lds\n\t"
      "v_mfma_f32_16x16x32_bf16 v[24:27], v[152:155], a[44:47], v[24:27]\n\t"
      "v_cvt_pk_bf16_f32 v168, v40, v41\n\t"
      "v_cvt_pk_bf16_f32 v169, v42, v43\n\t"
      "v_mfma_f32_16x16x32_bf16 v[28:31], v[156:159], a[44:47], v[28:31]\n\t"
      "v_cvt_pk_bf16_f32 v170, v44, v45\n\t"
      "v_cvt_pk_bf16_f32 v171, v46, v47\n\t"
      "v_cvt_pk_bf16_f32 v172, v48, v49\n\t"
      "v_cvt_pk_bf16_f32 v173, v50, v51\n\t"
      "v_cvt_pk_bf16_f32 v174, v52, v53\n\t"
      "v_cvt_pk_bf16_f32 v175, v54, v55\n\t"
      "ds_read_b128 a[24:27], v58 offset:25344\n\t"
      "v_mfma_f32_4x4x4_16b_bf16 v[40:43], a[180:181], a[108:109], 0\n\t"
      "ds_read_b64_tr_b16 a[116:117], v60 offset:49280\n\t"
      "v_mfma_f32_4x4x4_16b_bf16 v[44:47], a[182:183], a[110:111], 0\n\t"
      "ds_read_b64_tr_b16 a[118:119], v60 offset:50048\n\t"
      "v_mfma_f32_4x4x4_16b_bf16 v[48:51], a[180:181], a[112:113], 0\n\t"
      "ds_read_b64_tr_b16 a[120:121], v61 offset:49280\n\t"
      "v_mfma_f32_4x4x4_16b_bf16 v[52:55], a[182:183], a[114:115], 0\n\t"
      "ds_read_b64_tr_b16 a[122:123], v61 offset:50048\n\t"
      "buffer_load_dwordx4 %[vw1], %[srdW], s54 offen offset:1024 lds\n\t"
      "buffer_load_dwordx4 %[vw2], %[srdW], s54 offen offset:2048 lds\n\t"
      "v_mfma_f32_16x16x32_bf16 v[32:35], a[100:103], a[44:47], v[32:35]\n\t"
      "v_cvt_pk_bf16_f32 v40, v40, v41\n\t"
      "v_cvt_pk_bf16_f32 v41, v42, v43\n\t"
      "v_mfma_f32_16x16x32_bf16 v[36:39], a[104:107], a[44:47], v[36:39]\n\t"
      "v_cvt_pk_bf16_f32 v44, v44, v45\n\t"
      "v_cvt_pk_bf16_f32 v45, v46, v47\n\t"
      "v_cvt_pk_bf16_f32 v48, v48, v49\n\t"
      "v_cvt_pk_bf16_f32 v49, v50, v51\n\t"
      "v_cvt_pk_bf16_f32 v52, v52, v53\n\t"
      "v_cvt_pk_bf16_f32 v53, v54, v55\n\t"
      "v_accvgpr_write_b32 a108, v40\n\t"
      "v_accvgpr_write_b32 a109, v41\n\t"
      "v_accvgpr_write_b32 a110, v44\n\t"
      "v_accvgpr_write_b32 a111, v45\n\t"
      "v_accvgpr_write_b32 a112, v48\n\t"
      "v_accvgpr_write_b32 a113, v49\n\t"
      "v_accvgpr_write_b32 a114, v52\n\t"
      "v_accvgpr_write_b32 a115, v53\n\t"
      "s_waitcnt vmcnt(6)\n\t"
      "s_waitcnt lgkmcnt(0)\n\t"
      "; step\n\t"
      "v_mfma_f32_4x4x4_16b_bf16 v[40:43], a[184:185], v[176:177], 0\n\t"
      "ds_read_b128 a[180:183], %[dga] offset:2048\n\t"
      "v_mfma_f32_4x4x4_16b_bf16 v[44:47], a[186:187], v[178:179], 0\n\t"
      "ds_read_b64_tr_b16 v[192:193], %[tb0] offset:0\n\t"
      "v_mfma_f32_4x4x4_16b_bf16 v[48:51], a[184:185], v[180:181], 0\n\t"
      "ds_read_b64_tr_b16 v[194:195], %[tb0] offset:768\n\t"
      "v_mfma_f32_4x4x4_16b_bf16 v[52:55], a[186:187], v[182:183], 0\n\t"
      "ds_read_b64_tr_b16 v[196:197], %[tb1] offset:0\n\t"
      "ds_read_b64_tr_b16 v[198:199], %[tb1] offset:768\n\t"
      "s_add_u32 m0, %[m0w], 111104\n\t"
      "s_mov_b32 s54, 245760\n\t"
      "buffer_load_dwordx4 %[vw0], %[srdW], s54 offen lds\n\t"
      "buffer_load_dwordx4 %[vw1], %[srdW], s54 offen offset:1024 lds\n\t"
      "v_mfma_f32_16x16x32_bf16 v[16:19], v[160:163], a[24:27], v[16:19]\n\t"
      "v_cvt_pk_bf16_f32 v176, v40, v41\n\t"
      "v_cvt_pk_bf16_f32 v177, v42, v43\n\t"
      "v_mfma_f32_16x16x32_bf16 v[20:23], v[164:167], a[24:27], v[20:23]\n\t"
      "v_cvt_pk_bf16_f32 v178, v44, v45\n\t"
      "v_cvt_pk_bf16_f32 v179, v46, v47\n\t"
      "v_cvt_pk_bf16_f32 v180, v48, v49\n\t"
      "v_cvt_pk_bf16_f32 v181, v50, v51\n\t"
      "v_cvt_pk_bf16_f32 v182, v52, v53\n\t"
      "v_cvt_pk_bf16_f32 v183, v54, v55\n\t"
      "v_mfma_f32_4x4x4_16b_bf16 v[40:43], a[184:185], v[184:185], 0\n\t"
      "ds_read_b64_tr_b16 v[200:201], %[tb0] offset:64\n\t"
      "v_mfma_f32_4x4x4_16b_bf16 v[44:47], a[186:187], v[186:187], 0\n\t"
      "ds_read_b64_tr_b16 v[202:203], %[tb0] offset:832\n\t"
      "v_mfma_f32_4x4x4_16b_bf16 v[48:51], a[184:185], v[188:189], 0\n\t"
      "ds_read_b64_tr_b16 v[204:205], %[tb1] offset:64\n\t"
      "v_mfma_f32_4x4x4_16b_bf16 v[52:55], a[186:187], v[190:191], 0\n\t"
      "ds_read_b64_tr_b16 v[206:207], %[tb1] offset:832\n\t"
      "buffer_load_dwordx4 %[vw2], %[srdW], s54 offen offset:2048 lds\n\t"
      "s_add_u32 m0, %[m0w], 114176\n\t"
      "s_mov_b32 s54, 258048\n\t"
      "buffer_load_dwordx4 %[vw0], %[srdW], s54 offen lds\n\t"
      "v_mfma_f32_16x16x32_bf16 v[24:27], v[168:171], a[24:27], v[24:27]\n\t"
      "v_cvt_pk_bf16_f32 v184, v40, v41\n\t"
      "v_cvt_pk_bf16_f32 v185, v42, v43\n\t"
      "v_mfma_f32_16x16x32_bf16 v[28:31], v[172:175], a[24:27], v[28:31]\n\t"
      "v_cvt_pk_bf16_f32 v186, v44, v45\n\t"
      "v_cvt_pk_bf16_f32 v187, v46, v47\n\t"
      "v_cvt_pk_bf16_f32 v188, v48, v49\n\t"
      "v_cvt_pk_bf16_f32 v189, v50, v51\n\t"
      "v_cvt_pk_bf16_f32 v190, v52, v53\n\t"
      "v_cvt_pk_bf16_f32 v191, v54, v55\n\t"
      "ds_read_b128 a[28:31], v59 offset:25344\n\t"
      "v_mfma_f32_4x4x4_16b_bf16 v[40:43], a[184:185], a[116:117], 0\n\t"
      "ds_read_b64_tr_b16 a[124:125], %[tb0] offset:128\n\t"
      "v_mfma_f32_4x4x4_16b_bf16 v[44:47], a[186:187], a[118:119], 0\n\t"
      "ds_read_b64_tr_b16 a[126:127], %[tb0] offset:896\n\t"
      "v_mfma_f32_4x4x4_16b_bf16 v[48:51], a[184:185], a[120:121], 0\n\t"
      "ds_read_b64_tr_b16 a[128:129], %[tb1] offset:128\n\t"
      "v_mfma_f32_4x4x4_16b_bf16 v[52:55], a[186:187], a[122:123], 0\n\t"
      "ds_read_b64_tr_b16 a[130:131], %[tb1] offset:896\n\t"
      "buffer_load_dwordx4 %[vw1], %[srdW], s54 offen offset:1024 lds\n\t"
      "buffer_load_dwordx4 %[vw2], %[srdW], s54 offen offset:2048 lds\n\t"
      "v_mfma_f32_16x16x32_bf16 v[32:35], a[108:111], a[24:27], v[32:35]\n\t"
      "v_cvt_pk_bf16_f32 v40, v40, v41\n\t"
      "v_cvt_pk_bf16_f32 v41, v42, v43\n\t"
      "v_mfma_f32_16x16x32_bf16 v[36:39], a[112:115], a[24:27], v[36:39]\n\t"
      "v_cvt_pk_bf16_f32 v44, v44, v45\n\t"
      "v_cvt_pk_bf16_f32 v45, v46, v47\n\t"
      "v_cvt_pk_bf16_f32 v48, v48, v49\n\t"
      "v_cvt_pk_bf16_f32 v49, v50, v51\n\t"
      "v_cvt_pk_bf16_f32 v52, v52, v53\n\t"
      "v_cvt_pk_bf16_f32 v53, v54, v55\n\t"
      "v_accvgpr_write_b32 a116, v40\n\t"
      "v_accvgpr_write_b32 a117, v41\n\t"
      "v_accvgpr_write_b32 a118, v44\n\t"
      "v_accvgpr_write_b32 a119, v45\n\t"
      "v_accvgpr_write_b32 a120, v48\n\t"
      "v_accvgpr_write_b32 a121, v49\n\t"
      "v_accvgpr_write_b32 a122, v52\n\t"
      "v_accvgpr_write_b32 a123, v53\n\t"
      "s_waitcnt vmcnt(6)\n\t"
      "s_waitcnt lgkmcnt(0)\n\t"
      "; step\n\t"
      "v_mfma_f32_4x4x4_16b_bf16 v[40:43], a[180:181], v[192:193], 0\n\t"
      "ds_read_b128 a[184:187], %[dga] offset:2304\n\t"
      "v_mfma_f32_4x4x4_16b_bf16 v[44:47], a[182:183], v[194:195], 0\n\t"
      "ds_read_b64_tr_b16 v[208:209], v60 offset:0\n\t"
      "v_mfma_f32_4x4x4_16b_bf16 v[48:51], a[180:181], v[196:197], 0\n\t"
      "ds_read_b64_tr_b16 v[210:211], v60 offset:768\n\t"
      "v_mfma_f32_4x4x4_16b_bf16 v[52:55], a[182:183], v[198:199], 0\n\t"
      "ds_read_b64_tr_b16 v[212:213], v61 offset:0\n\t"
      "ds_read_b64_tr_b16 v[214:215], v61 offset:768\n\t"
      "s_add_u32 m0, %[m0w], 135680\n\t"
      "s_mov_b32 s54, 270336\n\t"
      "buffer_load_dwordx4 %[vw0], %[srdW], s54 offen lds\n\t"
      "buffer_load_dwordx4 %[vw1], %[srdW], s54 offen offset:1024 lds\n\t"
      "v_mfma_f32_16x16x32_bf16 v[16:19], v[176:179], a[28:31], v[16:19]\n\t"
      "v_cvt_pk_bf16_f32 v192, v40, v41\n\t"
      "v_cvt_pk_bf16_f32 v193, v42, v43\n\t"
      "v_mfma_f32_16x16x32_bf16 v[20:23], v[180:183], a[28:31], v[20:23]\n\t"
      "v_cvt_pk_bf16_f32 v194, v44, v45\n\t"
      "v_cvt_pk_bf16_f32 v195, v46, v47\n\t"
      "v_cvt_pk_bf16_f32 v196, v48, v49\n\t"
      "v_cvt_pk_bf16_f32 v197, v50, v51\n\t"
      "v_cvt_pk_bf16_f32 v198, v52, v53\n\t"
      "v_cvt_pk_bf16_f32 v199, v54, v55\n\t"
      "v_mfma_f32_4x4x4_16b_bf16 v[40:43], a[180:181], v[200:201], 0\n\t"
      "ds_read_b64_tr_b16 v[216:217], v60 offset:64\n\t"
      "v_mfma_f32_4x4x4_16b_bf16 v[44:47], a[182:183], v[202:203], 0\n\t"
      "ds_read_b64_tr_b16 v[218:219], v60 offset:832\n\t"
      "v_mfma_f32_4x4x4_16b_bf16 v[48:51], a[180:181], v[204:205], 0\n\t"
      "ds_read_b64_tr_b16 v[220:221], v61 offset:64\n\t"
      "v_mfma_f32_4x4x4_16b_bf16 v[52:55], a[182:183], v[206:207], 0\n\t"
      "ds_read_b64_tr_b16 v[222:223], v61 offset:832\n\t"
      "buffer_load_dwordx4 %[vw2], %[srdW], s54 offen offset:2048 lds\n\t"
      "s_add_u32 m0, %[m0w], 138752\n\t"
      "s_mov_b32 s54, 282624\n\t"
      "buffer_load_dwordx4 %[vw0], %[srdW], s54 offen lds\n\t"
      "v_mfma_f32_16x16x32_bf16 v[24:27], v[184:187], a[28:31], v[24:27]\n\t"
      "v_cvt_pk_bf16_f32 v200, v40, v41\n\t"
      "v_cvt_pk_bf16_f32 v201, v42, v43\n\t"
      "v_mfma_f32_16x16x32_bf16 v[28:31], v[188:191], a[28:31], v[28:31]\n\t"
      "v_cvt_pk_bf16_f32 v202, v44, v45\n\t"
      "v_cvt_pk_bf16_f32 v203, v46, v47\n\t"
      "v_cvt_pk_bf16_f32 v204, v48, v49\n\t"
      "v_cvt_pk_bf16_f32 v205, v50, v51\n\t"
      "v_cvt_pk_bf16_f32 v206, v52, v53\n\t"
      "v_cvt_pk_bf16_f32 v207, v54, v55\n\t"
      "ds_read_b128 a[32:35], v56 offset:25600\n\t"
      "v_mfma_f32_4x4x4_16b_bf16 v[40:43], a[180:181], a[124:125], 0\n\t"
      "ds_read_b64_tr_b16 a[132:133], v60 offset:128\n\t"
      "v_mfma_f32_4x4x4_16b_bf16 v[44:47], a[182:183], a[126:127], 0\n\t"
      "ds_read_b64_tr_b16 a[134:135], v60 offset:896\n\t"
      "v_mfma_f32_4x4x4_16b_bf16 v[48:51], a[180:181], a[128:129], 0\n\t"
      "ds_read_b64_tr_b16 a[136:137], v61 offset:128\n\t"
      "v_mfma_f32_4x4x4_16b_bf16 v[52:55], a[182:183], a[130:131], 0\n\t"
      "ds_read_b64_tr_b16 a[138:139], v61 offset:896\n\t"
      "buffer_load_dwordx4 %[vw1], %[srdW], s54 offen offset:1024 lds\n\t"
      "buffer_load_dwordx4 %[vw2], %[srdW], s54 offen offset:2048 lds\n\t"
      "s_add_u32 m0, %[m0base], 37376\n\t"
      "s_add_u32 s48, %[soff0], 3145728\n\t"
      "buffer_load_dwordx4 %[vx0], s[40:43], s48 offen lds\n\t"
      "buffer_load_dwordx4 %[vx1], s[40:43], s48 offen offset:1024 lds\n\t"
      "buffer_load_dwordx4 %[vx2], s[40:43], s48 offen offset:2048 lds\n\t"
      "v_mfma_f32_16x16x32_bf16 v[32:35], a[116:119], a[28:31], v[32:35]\n\t"
      "v_cvt_pk_bf16_f32 v40, v40, v41\n\t"
      "v_cvt_pk_bf16_f32 v41, v42, v43\n\t"
      "v_mfma_f32_16x16x32_bf16 v[36:39], a[120:123], a[28:31], v[36:39]\n\t"
      "v_cvt_pk_bf16_f32 v44, v44, v45\n\t"
      "v_cvt_pk_bf16_f32 v45, v46, v47\n\t"
      "v_cvt_pk_bf16_f32 v48, v48, v49\n\t"
      "v_cvt_pk_bf16_f32 v49, v50, v51\n\t"
      "v_cvt_pk_bf16_f32 v52, v52, v53\n\t"
      "v_cvt_pk_bf16_f32 v53, v54, v55\n\t"
      "v_accvgpr_write_b32 a124, v40\n\t"
      "v_accvgpr_write_b32 a125, v41\n\t"
      "v_accvgpr_write_b32 a126, v44\n\t"
      "v_accvgpr_write_b32 a127, v45\n\t"
      "v_accvgpr_write_b32 a128, v48\n\t"
      "v_accvgpr_write_b32 a129, v49\n\t"
      "v_accvgpr_write_b32 a130, v52\n\t"
      "v_accvgpr_write_b32 a131, v53\n\t"
      "s_waitcnt vmcnt(9)\n\t"
      "s_waitcnt lgkmcnt(0)\n\t"
      "; step\n\t"
      "v_mfma_f32_4x4x4_16b_bf16 v[40:43], a[184:185], v[208:209], 0\n\t"
      "ds_read_b128 a[180:183], %[dga] offset:2560\n\t"
      "v_mfma_f32_4x4x4_16b_bf16 v[44:47], a[186:187], v[210:211], 0\n\t"
      "ds_read_b64_tr_b16 v[224:225], v60 offset:24576\n\t"
      "v_mfma_f32_4x4x4_16b_bf16 v[48:51], a[184:185], v[212:213], 0\n\t"
      "ds_read_b64_tr_b16 v[226:227], v60 offset:25344\n\t"
      "v_mfma_f32_4x4x4_16b_bf16 v[52:55], a[186:187], v[214:215], 0\n\t"
      "ds_read_b64_tr_b16 v[228:229], v61 offset:24576\n\t"
      "ds_read_b64_tr_b16 v[230:231], v61 offset:25344\n\t"
      "v_mfma_f32_16x16x32_bf16 v[16:19], v[192:195], a[32:35], v[16:19]\n\t"
      "v_cvt_pk_bf16_f32 v208, v40, v41\n\t"
      "v_cvt_pk_bf16_f32 v209, v42, v43\n\t"
      "v_mfma_f32_16x16x32_bf16 v[20:23], v[196:199], a[32:35], v[20:23]\n\t"
      "v_cvt_pk_bf16_f32 v210, v44, v45\n\t"
      "v_cvt_pk_bf16_f32 v211, v46, v47\n\t"
      "v_cvt_pk_bf16_f32 v212, v48, v49\n\t"
      "v_cvt_pk_bf16_f32 v213, v50, v51\n\t"
      "v_cvt_pk_bf16_f32 v214, v52, v53\n\t"
      "v_cvt_pk_bf16_f32 v215, v54, v55\n\t"
      "v_mfma_f32_4x4x4_16b_bf16 v[40:43], a[184:185], v[216:217], 0\n\t"
      "ds_read_b64_tr_b16 v[232:233], v60 offset:24640\n\t"
      "v_mfma_f32_4x4x4_16b_bf16 v[44:47], a[186:187], v[218:219], 0\n\t"
      "ds_read_b64_tr_b16 v[234:235], v60 offset:25408\n\t"
      "v_mfma_f32_4x4x4_16b_bf16 v[48:51], a[184:185], v[220:221], 0\n\t"
      "ds_read_b64_tr_b16 v[236:237], v61 offset:24640\n\t"
      "v_mfma_f32_4x4x4_16b_bf16 v[52:55], a[186:187], v[222:223], 0\n\t"
      "ds_read_b64_tr_b16 v[238:239], v61 offset:25408\n\t"
      "v_mfma_f32_16x16x32_bf16 v[24:27], v[200:203], a[32:35], v[24:27]\n\t"
      "v_cvt_pk_bf16_f32 v216, v40, v41\n\t"
      "v_cvt_pk_bf16_f32 v217, v42, v43\n\t"
      "v_mfma_f32_16x16x32_bf16 v[28:31], v[204:207], a[32:35], v[28:31]\n\t"
      "v_cvt_pk_bf16_f32 v218, v44, v45\n\t"
      "v_cvt_pk_bf16_f32 v219, v46, v47\n\t"
      "v_cvt_pk_bf16_f32 v220, v48, v49\n\t"
      "v_cvt_pk_bf16_f32 v221, v50, v51\n\t"
      "v_cvt_pk_bf16_f32 v222, v52, v53\n\t"
      "v_cvt_pk_bf16_f32 v223, v54, v55\n\t"
      "ds_read_b128 a[36:39], v57 offset:25600\n\t"
      "v_mfma_f32_4x4x4_16b_bf16 v[40:43], a[184:185], a[132:133], 0\n\t"
      "ds_read_b64_tr_b16 a[140:141], v60 offset:24704\n\t"
      "v_mfma_f32_4x4x4_16b_bf16 v[44:47], a[186:187], a[134:135], 0\n\t"
      "ds_read_b64_tr_b16 a[142:143], v60 offset:25472\n\t"
      "v_mfma_f32_4x4x4_16b_bf16 v[48:51], a[184:185], a[136:137], 0\n\t"
      "ds_read_b64_tr_b16 a[144:145], v61 offset:24704\n\t"
      "v_mfma_f32_4x4x4_16b_bf16 v[52:55], a[186:187], a[138:139], 0\n\t"
      "ds_read_b64_tr_b16 a[146:147], v61 offset:25472\n\t"
      "v_mfma_f32_16x16x32_bf16 v[32:35], a[124:127], a[32:35], v[32:35]\n\t"
      "v_cvt_pk_bf16_f32 v40, v40, v41\n\t"
      "v_cvt_pk_bf16_f32 v41, v42, v43\n\t"
      "v_mfma_f32_16x16x32_bf16 v[36:39], a[128:131], a[32:35], v[36:39]\n\t"
      "v_cvt_pk_bf16_f32 v44, v44, v45\n\t"
      "v_cvt_pk_bf16_f32 v45, v46, v47\n\t"
      "v_cvt_pk_bf16_f32 v48, v48, v49\n\t"
      "v_cvt_pk_bf16_f32 v49, v50, v51\n\t"
      "v_cvt_pk_bf16_f32 v52, v52, v53\n\t"
      "v_cvt_pk_bf16_f32 v53, v54, v55\n\t"
      "v_accvgpr_write_b32 a132, v40\n\t"
      "v_accvgpr_write_b32 a133, v41\n\t"
      "v_accvgpr_write_b32 a134, v44\n\t"
      "v_accvgpr_write_b32 a135, v45\n\t"
      "v_accvgpr_write_b32 a136, v48\n\t"
      "v_accvgpr_write_b32 a137, v49\n\t"
      "v_accvgpr_write_b32 a138, v52\n\t"
      "v_accvgpr_write_b32 a139, v53\n\t"
      "s_waitcnt vmcnt(3)\n\t"
      "s_waitcnt lgkmcnt(0)\n\t"
      "; step\n\t"
      "v_mfma_f32_4x4x4_16b_bf16 v[40:43], a[180:181], v[224:225], 0\n\t"
      "ds_read_b128 a[184:187], %[dga] offset:2816\n\t"
      "v_mfma_f32_4x4x4_16b_bf16 v[44:47], a[182:183], v[226:227], 0\n\t"
      "ds_read_b64_tr_b16 v[240:241], v60 offset:49152\n\t"
      "v_mfma_f32_4x4x4_16b_bf16 v[48:51], a[180:181], v[228:229], 0\n\t"
      "ds_read_b64_tr_b16 v[242:243], v60 offset:49920\n\t"
      "v_mfma_f32_4x4x4_16b_bf16 v[52:55], a[182:183], v[230:231], 0\n\t"
      "ds_read_b64_tr_b16 v[244:245], v61 offset:49152\n\t"
      "ds_read_b64_tr_b16 v[246:247], v61 offset:49920\n\t"
      "s_add_u32 m0, %[m0base], 49664\n\t"
      "s_add_u32 s48, %[soff0], 6291456\n\t"
      "buffer_load_dwordx4 %[vx0], s[40:43], s48 offen lds\n\t"
      "buffer_load_dwordx4 %[vx1], s[40:43], s48 offen offset:1024 lds\n\t"
      "v_mfma_f32_16x16x32_bf16 v[16:19], v[208:211], a[36:39], v[16:19]\n\t"
      "v_cvt_pk_bf16_f32 v224, v40, v41\n\t"
      "v_cvt_pk_bf16_f32 v225, v42, v43\n\t"
      "v_mfma_f32_16x16x32_bf16 v[20:23], v[212:215], a[36:39], v[20:23]\n\t"
      "v_cvt_pk_bf16_f32 v226, v44, v45\n\t"
      "v_cvt_pk_bf16_f32 v227, v46, v47\n\t"
      "v_cvt_pk_bf16_f32 v228, v48, v49\n\t"
      "v_cvt_pk_bf16_f32 v229, v50, v51\n\t"
      "v_cvt_pk_bf16_f32 v230, v52, v53\n\t"
      "v_cvt_pk_bf16_f32 v231, v54, v55\n\t"
      "v_mfma_f32_4x4x4_16b_bf16 v[40:43], a[180:181], v[232:233], 0\n\t"
      "ds_read_b64_tr_b16 v[248:249], v60 offset:49216\n\t"
      "v_mfma_f32_4x4x4_16b_bf16 v[44:47], a[182:183], v[234:235], 0\n\t"
      "ds_read_b64_tr_b16 v[250:251], v60 offset:49984\n\t"
      "v_mfma_f32_4x4x4_16b_bf16 v[48:51], a[180:181], v[236:237], 0\n\t"
      "ds_read_b64_tr_b16 v[252:253], v61 offset:49216\n\t"
      "v_mfma_f32_4x4x4_16b_bf16 v[52:55], a[182:183], v[238:239], 0\n\t"
      "ds_read_b64_tr_b16 v[254:255], v61 offset:49984\n\t"
      "buffer_load_dwordx4 %[vx2], s[40:43], s48 offen offset:2048 lds\n\t"
      "buffer_load_dword a200, %[stv], s[44:47], 0 offen\n\t"
      "v_mfma_f32_16x16x32_bf16 v[24:27], v[216:219], a[36:39], v[24:27]\n\t"
      "v_cvt_pk_bf16_f32 v232, v40, v41\n\t"
      "v_cvt_pk_bf16_f32 v233, v42, v43\n\t"
      "v_mfma_f32_16x16x32_bf16 v[28:31], v[220:223], a[36:39], v[28:31]\n\t"
      "v_cvt_pk_bf16_f32 v234, v44, v45\n\t"
      "v_cvt_pk_bf16_f32 v235, v46, v47\n\t"
      "v_cvt_pk_bf16_f32 v236, v48, v49\n\t"
      "v_cvt_pk_bf16_f32 v237, v50, v51\n\t"
      "v_cvt_pk_bf16_f32 v238, v52, v53\n\t"
      "v_cvt_pk_bf16_f32 v239, v54, v55\n\t"
      "ds_read_b128 a[40:43], v58 offset:25600\n\t"
      "v_mfma_f32_4x4x4_16b_bf16 v[40:43], a[180:181], a[140:141], 0\n\t"
      "ds_read_b64_tr_b16 a[148:149], v60 offset:49280\n\t"
      "v_mfma_f32_4x4x4_16b_bf16 v[44:47], a[182:183], a[142:143], 0\n\t"
      "ds_read_b64_tr_b16 a[150:151], v60 offset:50048\n\t"
      "v_mfma_f32_4x4x4_16b_bf16 v[48:51], a[180:181], a[144:145], 0\n\t"
      "ds_read_b64_tr_b16 a[152:153], v61 offset:49280\n\t"
      "v_mfma_f32_4x4x4_16b_bf16 v[52:55], a[182:183], a[146:147], 0\n\t"
      "ds_read_b64_tr_b16 a[154:155], v61 offset:50048\n\t"
      "buffer_load_dword a200, %[stv], s[44:47], 0 offen\n\t"
      "buffer_load_dword a200, %[stv], s[44:47], 0 offen\n\t"
      "v_mfma_f32_16x16x32_bf16 v[32:35], a[132:135], a[36:39], v[32:35]\n\t"
      "v_cvt_pk_bf16_f32 v40, v40, v41\n\t"
      "v_cvt_pk_bf16_f32 v41, v42, v43\n\t"
      "v_mfma_f32_16x16x32_bf16 v[36:39], a[136:139], a[36:39], v[36:39]\n\t"
      "v_cvt_pk_bf16_f32 v44, v44, v45\n\t"
      "v_cvt_pk_bf16_f32 v45, v46, v47\n\t"
      "v_cvt_pk_bf16_f32 v48, v48, v49\n\t"
      "v_cvt_pk_bf16_f32 v49, v50, v51\n\t"
      "v_cvt_pk_bf16_f32 v52, v52, v53\n\t"
      "v_cvt_pk_bf16_f32 v53, v54, v55\n\t"
      "v_accvgpr_write_b32 a140, v40\n\t"
      "v_accvgpr_write_b32 a141, v41\n\t"
      "v_accvgpr_write_b32 a142, v44\n\t"
      "v_accvgpr_write_b32 a143, v45\n\t"
      "v_accvgpr_write_b32 a144, v48\n\t"
      "v_accvgpr_write_b32 a145, v49\n\t"
      "v_accvgpr_write_b32 a146, v52\n\t"
      "v_accvgpr_write_b32 a147, v53\n\t"
      "s_waitcnt lgkmcnt(0)\n\t"
      "; step\n\t"
      "v_mfma_f32_4x4x4_16b_bf16 v[40:43], a[184:185], v[240:241], 0\n\t"
      "v_mfma_f32_4x4x4_16b_bf16 v[44:47], a[186:187], v[242:243], 0\n\t"
      "v_mfma_f32_4x4x4_16b_bf16 v[48:51], a[184:185], v[244:245], 0\n\t"
      "v_mfma_f32_4x4x4_16b_bf16 v[52:55], a[186:187], v[246:247], 0\n\t"
      "v_mfma_f32_16x16x32_bf16 v[16:19], v[224:227], a[40:43], v[16:19]\n\t"
      "s_nop 0\n\t"
      "v_cvt_pk_bf16_f32 v240, v40, v41\n\t"
      "v_cvt_pk_bf16_f32 v241, v42, v43\n\t"
      "v_mfma_f32_16x16x32_bf16 v[20:23], v[228:231], a[40:43], v[20:23]\n\t"
      "v_cvt_pk_bf16_f32 v242, v44, v45\n\t"
      "v_cvt_pk_bf16_f32 v243, v46, v47\n\t"
      "v_cvt_pk_bf16_f32 v244, v48, v49\n\t"
      "v_cvt_pk_bf16_f32 v245, v50, v51\n\t"
      "v_cvt_pk_bf16_f32 v246, v52, v53\n\t"
      "v_cvt_pk_bf16_f32 v247, v54, v55\n\t"
      "v_mfma_f32_4x4x4_16b_bf16 v[40:43], a[184:185], v[248:249], 0\n\t"
      "v_mfma_f32_4x4x4_16b_bf16 v[44:47], a[186:187], v[250:251], 0\n\t"
      "v_mfma_f32_4x4x4_16b_bf16 v[48:51], a[184:185], v[252:253], 0\n\t"
      "v_mfma_f32_4x4x4_16b_bf16 v[52:55], a[186:187], v[254:255], 0\n\t"
      "v_mfma_f32_16x16x32_bf16 v[24:27], v[232:235], a[40:43], v[24:27]\n\t"
      "s_nop 0\n\t"
      "v_cvt_pk_bf16_f32 v248, v40, v41\n\t"
      "v_cvt_pk_bf16_f32 v249, v42, v43\n\t"
      "v_mfma_f32_16x16x32_bf16 v[28:31], v[236:239], a[40:43], v[28:31]\n\t"
      "v_cvt_pk_bf16_f32 v250, v44, v45\n\t"
      "v_cvt_pk_bf16_f32 v251, v46, v47\n\t"
      "v_cvt_pk_bf16_f32 v252, v48, v49\n\t"
      "v_cvt_pk_bf16_f32 v253, v50, v51\n\t"
      "v_cvt_pk_bf16_f32 v254, v52, v53\n\t"
      "v_cvt_pk_bf16_f32 v255, v54, v55\n\t"
      "ds_read_b128 a[44:47], v59 offset:25600\n\t"
      "v_mfma_f32_4x4x4_16b_bf16 v[40:43], a[184:185], a[148:149], 0\n\t"
      "v_mfma_f32_4x4x4_16b_bf16 v[44:47], a[186:187], a[150:151], 0\n\t"
      "v_mfma_f32_4x4x4_16b_bf16 v[48:51], a[184:185], a[152:153], 0\n\t"
      "v_mfma_f32_4x4x4_16b_bf16 v[52:55], a[186:187], a[154:155], 0\n\t"
      "v_mfma_f32_16x16x32_bf16 v[32:35], a[140:143], a[40:43], v[32:35]\n\t"
      "s_nop 0\n\t"
      "v_cvt_pk_bf16_f32 v40, v40, v41\n\t"
      "v_cvt_pk_bf16_f32 v41, v42, v43\n\t"
      "v_mfma_f32_16x16x32_bf16 v[36:39], a[144:147], a[40:43], v[36:39]\n\t"
      "v_cvt_pk_bf16_f32 v44, v44, v45\n\t"
      "v_cvt_pk_bf16_f32 v45, v46, v47\n\t"
      "v_cvt_pk_bf16_f32 v48, v48, v49\n\t"
      "v_cvt_pk_bf16_f32 v49, v50, v51\n\t"
      "v_cvt_pk_bf16_f32 v52, v52, v53\n\t"
      "v_cvt_pk_bf16_f32 v53, v54, v55\n\t"
      "v_accvgpr_write_b32 a148, v40\n\t"
      "v_accvgpr_write_b32 a149, v41\n\t"
      "v_accvgpr_write_b32 a150, v44\n\t"
      "v_accvgpr_write_b32 a151, v45\n\t"
      "v_accvgpr_write_b32 a152, v48\n\t"
      "v_accvgpr_write_b32 a153, v49\n\t"
      "v_accvgpr_write_b32 a154, v52\n\t"
      "v_accvgpr_write_b32 a155, v53\n\t"
      "s_add_u32 m0, %[m0base], 61952\n\t"
      "s_add_u32 s48, %[soff0], 9437184\n\t"
      "buffer_load_dwordx4 %[vx0], s[40:43], s48 offen lds\n\t"
      "buffer_load_dwordx4 %[vx1], s[40:43], s48 offen offset:1024 lds\n\t"
      "buffer_load_dwordx4 %[vx2], s[40:43], s48 offen offset:2048 lds\n\t"
      "buffer_load_dword a200, %[stv], s[44:47], 0 offen\n\t"
      "buffer_load_dword a200, %[stv], s[44:47], 0 offen\n\t"
      "buffer_load_dword a200, %[stv], s[44:47], 0 offen\n\t"
      "s_waitcnt lgkmcnt(0)\n\t"
      "v_mfma_f32_16x16x32_bf16 v[16:19], v[240:243], a[44:47], v[16:19]\n\t"
      "s_sub_u32 s49, %[soff0], 0\n\t"
      "s_mov_b32 s50, 86528\n\t"
      "v_mfma_f32_16x16x32_bf16 v[20:23], v[244:247], a[44:47], v[20:23]\n\t"
      "s_mov_b32 s51, 49664\n\t"
      "v_mfma_f32_16x16x32_bf16 v[24:27], v[248:251], a[44:47], v[24:27]\n\t"
      "s_cmp_eq_u32 %[q6144], 0\n\t"
      "s_cselect_b32 s39, 90, 0\n\t"
      "s_cselect_b32 s32, 286331156, 0\n\t"
      "v_mfma_f32_16x16x32_bf16 v[28:31], v[252:255], a[44:47], v[28:31]\n\t"
      "s_cselect_b32 s33, 125129521, 0\n\t"
      "s_cselect_b32 s34, 286331159, 0\n\t"
      "s_cselect_b32 s35, 123024145, 0\n\t"
      "v_mfma_f32_16x16x32_bf16 v[32:35], a[148:151], a[44:47], v[32:35]\n\t"
      "v_mfma_f32_16x16x32_bf16 v[36:39], a[152:155], a[44:47], v[36:39]\n\t"
      "v_add_u32 v52, 37376, v56\n\t"
      "v_add_u32 v53, 37376, v57\n\t"
      "v_add_u32 v54, 37376, v58\n\t"
      "v_add_u32 v55, 37376, v59\n\t"
      "v_cvt_pk_bf16_f32 v40, v16, v17\n\t"
      "v_cvt_pk_bf16_f32 v41, v18, v19\n\t"
      "v_cvt_pk_bf16_f32 v42, v20, v21\n\t"
      "v_cvt_pk_bf16_f32 v43, v22, v23\n\t"
      "v_cvt_pk_bf16_f32 v44, v24, v25\n\t"
      "v_cvt_pk_bf16_f32 v45, v26, v27\n\t"
      "v_cvt_pk_bf16_f32 v46, v28, v29\n\t"
      "v_cvt_pk_bf16_f32 v47, v30, v31\n\t"
      "v_cvt_pk_bf16_f32 v48, v32, v33\n\t"
      "v_cvt_pk_bf16_f32 v49, v34, v35\n\t"
      "s_waitcnt vmcnt(12)\n\t"
      "s_barrier\n\t"
      "ds_read_b128 a[0:3], v52 offset:0\n\t"
      "ds_read_b128 a[4:7], v53 offset:0\n\t"
      "ds_read_b128 a[8:11], v54 offset:0\n\t"
      "ds_read_b128 a[12:15], v55 offset:0\n\t"
      "ds_read_b128 a[16:19], v52 offset:256\n\t"
      "ds_read_b128 a[20:23], v53 offset:256\n\t"
      "s_memrealtime s[36:37]\n\t"
      "s_add_u32 m0, %[m0base], 74240\n\t"
      "s_add_u32 s48, %[soff0], 12582912\n\t"
      "buffer_load_dwordx4 %[vx0], s[40:43], s48 offen lds\n\t"
      "buffer_load_dwordx4 %[vx1], s[40:43], s48 offen offset:1024 lds\n\t"
      "buffer_load_dwordx4 %[vx2], s[40:43], s48 offen offset:2048 lds\n\t"
      "ds_write_b64 %[stw], v[40:41] offset:0\n\t"
      "ds_write_b64 %[stw], v[42:43] offset:32\n\t"
      "ds_write_b64 %[stw], v[44:45] offset:64\n\t"
      "ds_write_b64 %[stw], v[46:47] offset:96\n\t"
      "ds_write_b64 %[stw], v[48:49] offset:128\n\t"
      "s_mov_b32 s46, %[nbytes]\n\t"
      "s_add_u32 s48, %[soff0], 15728640\n\t"
      "s_mov_b32 s52, %[ntrip]\n\t"
      "s_waitcnt lgkmcnt(0)\n\t"
      "s_add_u32 s38, s36, s39\n\t"
      "s_cmp_eq_u32 %[q6144], 6144\n\t"
      "s_cbranch_scc1 .Lmid1_%=\n\t"
      "s_cmp_eq_u32 %[q6144], 12288\n\t"
      "s_cbranch_scc1 .Lmid2_%=\n\t"
      "s_cmp_eq_u32 %[q6144], 18432\n\t"
      "s_cbranch_scc1 .Lmid3_%=\n\t"
      "s_branch .Lmid0_%=\n\t"
      ".Lloop0_%=:\n\t"
      "v_mfma_f32_16x16x32_bf16 v[16:19], v[64:67], a[0:3], 0\n\t"
      "ds_read_b128 a[24:27], v58 offset:256\n\t"
      "v_mfma_f32_16x16x32_bf16 v[16:19], v[80:83], a[4:7], v[16:19]\n\t"
      "ds_read_b128 a[28:31], v59 offset:256\n\t"
      "v_mfma_f32_16x16x32_bf16 v[16:19], v[96:99], a[8:11], v[16:19]\n\t"
      "ds_read_b128 a[32:35], v56 offset:512\n\t"
      "v_mfma_f32_16x16x32_bf16 v[16:19], v[112:115], a[12:15], v[16:19]\n\t"
      "ds_read_b128 a[36:39], v57 offset:512\n\t"
      "v_mfma_f32_16x16x32_bf16 v[16:19], v[128:131], a[16:19], v[16:19]\n\t"
      "ds_read_b128 a[40:43], v58 offset:512\n\t"
      "v_mfma_f32_16x16x32_bf16 v[16:19], v[144:147], a[20:23], v[16:19]\n\t"
      "ds_read_b128 a[44:47], v59 offset:512\n\t"
      "v_mfma_f32_16x16x32_bf16 v[20:23], v[68:71], a[0:3], 0\n\t"
      "v_cvt_pk_bf16_f32 v50, v36, v37\n\t"
      "v_cvt_pk_bf16_f32 v51, v38, v39\n\t"
      "v_mfma_f32_16x16x32_bf16 v[20:23], v[84:87], a[4:7], v[20:23]\n\t"
      "ds_write_b64 %[stw], v[50:51] offset:12704\n\t"
      "v_mfma_f32_16x16x32_bf16 v[20:23], v[100:103], a[8:11], v[20:23]\n\t"
      "v_add_u32 v52, s51, %[fb0]\n\t"
      "v_xor_b32 v53, 64, v52\n\t"
      "v_mfma_f32_16x16x32_bf16 v[20:23], v[116:119], a[12:15], v[20:23]\n\t"
      "v_xor_b32 v54, 128, v52\n\t"
      "v_xor_b32 v55, 192, v52\n\t"
      "v_mfma_f32_16x16x32_bf16 v[20:23], v[132:135], a[16:19], v[20:23]\n\t"
      "v_mfma_f32_16x16x32_bf16 v[20:23], v[148:151], a[20:23], v[20:23]\n\t"
      "v_mfma_f32_16x16x32_bf16 v[24:27], v[72:75], a[0:3], 0\n\t"
      "v_mfma_f32_16x16x32_bf16 v[24:27], v[88:91], a[4:7], v[24:27]\n\t"
      "v_mfma_f32_16x16x32_bf16 v[24:27], v[104:107], a[8:11], v[24:27]\n\t"
      "s_waitcnt vmcnt(12)\n\t"
      "s_waitcnt lgkmcnt(0)\n\t"
      "s_barrier\n\t"
      "v_mfma_f32_16x16x32_bf16 v[24:27], v[120:123], a[12:15], v[24:27]\n\t"
      "v_mfma_f32_16x16x32_bf16 v[24:27], v[136:139], a[16:19], v[24:27]\n\t"
      "v_mfma_f32_16x16x32_bf16 v[24:27], v[152:155], a[20:23], v[24:27]\n\t"
      "v_mfma_f32_16x16x32_bf16 v[28:31], v[76:79], a[0:3], 0\n\t"
      "ds_read_b128 a[48:51], %[str0] offset:12544\n\t"
      "v_mfma_f32_16x16x32_bf16 v[28:31], v[92:95], a[4:7], v[28:31]\n\t"
      "ds_read_b128 a[52:55], %[str1] offset:13568\n\t"
      "v_mfma_f32_16x16x32_bf16 v[28:31], v[108:111], a[8:11], v[28:31]\n\t"
      "ds_read_b128 a[56:59], %[str2] offset:14592\n\t"
      "v_mfma_f32_16x16x32_bf16 v[28:31], v[124:127], a[12:15], v[28:31]\n\t"
      "s_and_b32 s39, s32, 15\n\t"
      "s_cselect_b32 s53, 79, 0\n\t"
      "v_mfma_f32_16x16x32_bf16 v[28:31], v[140:143], a[16:19], v[28:31]\n\t"
      "s_add_u32 s39, s39, s53\n\t"
      "s_lshr_b64 s[32:33], s[32:33], 4\n\t"
      "v_mfma_f32_16x16x32_bf16 v[28:31], v[156:159], a[20:23], v[28:31]\n\t"
      "v_mfma_f32_16x16x32_bf16 v[32:35], a[60:63], a[0:3], 0\n\t"
      "v_mfma_f32_16x16x32_bf16 v[32:35], a[68:71], a[4:7], v[32:35]\n\t"
      "s_waitcnt lgkmcnt(2)\n\t"
      "v_mfma_f32_16x16x32_bf16 v[32:35], a[76:79], a[8:11], v[32:35]\n\t"
      "buffer_store_dwordx4 a[48:51], %[stv], s[44:47], s49 offen sc1\n\t"
      "v_mfma_f32_16x16x32_bf16 v[32:35], a[84:87], a[12:15], v[32:35]\n\t"
      "v_mfma_f32_16x16x32_bf16 v[32:35], a[92:95], a[16:19], v[32:35]\n\t"
      "v_mfma_f32_16x16x32_bf16 v[32:35], a[100:103], a[20:23], v[32:35]\n\t"
      "s_add_u32 m0, %[m0base], s50\n\t"
      "v_mfma_f32_16x16x32_bf16 v[36:39], a[64:67], a[0:3], 0\n\t"
      "buffer_load_dwordx4 %[vx0], s[40:43], s48 offen lds\n\t"
      "v_mfma_f32_16x16x32_bf16 v[36:39], a[72:75], a[4:7], v[36:39]\n\t"
      "s_memrealtime s[36:37]\n\t"
      "v_mfma_f32_16x16x32_bf16 v[36:39], a[80:83], a[8:11], v[36:39]\n\t"
      "v_mfma_f32_16x16x32_bf16 v[36:39], a[88:91], a[12:15], v[36:39]\n\t"
      "s_waitcnt lgkmcnt(1)\n\t"
      "v_mfma_f32_16x16x32_bf16 v[36:39], a[96:99], a[16:19], v[36:39]\n\t"
      "buffer_store_dwordx4 a[52:55], %[stv], s[44:47], s49 offen offset:1024 sc1\n\t"
      "v_mfma_f32_16x16x32_bf16 v[36:39], a[104:107], a[20:23], v[36:39]\n\t"
      "v_mfma_f32_16x16x32_bf16 v[16:19], v[160:163], a[24:27], v[16:19]\n\t"
      "ds_read_b128 a[0:3], v52 offset:0\n\t"
      "v_mfma_f32_16x16x32_bf16 v[16:19], v[176:179], a[28:31], v[16:19]\n\t"
      "ds_read_b128 a[4:7], v53 offset:0\n\t"
      "v_mfma_f32_16x16x32_bf16 v[16:19], v[192:195], a[32:35], v[16:19]\n\t"
      "ds_read_b128 a[8:11], v54 offset:0\n\t"
      "v_mfma_f32_16x16x32_bf16 v[16:19], v[208:211], a[36:39], v[16:19]\n\t"
      "ds_read_b128 a[12:15], v55 offset:0\n\t"
      "v_mfma_f32_16x16x32_bf16 v[16:19], v[224:227], a[40:43], v[16:19]\n\t"
      "ds_read_b128 a[16:19], v52 offset:256\n\t"
      "v_mfma_f32_16x16x32_bf16 v[16:19], v[240:243], a[44:47], v[16:19]\n\t"
      "ds_read_b128 a[20:23], v53 offset:256\n\t"
      "v_mfma_f32_16x16x32_bf16 v[20:23], v[164:167], a[24:27], v[20:23]\n\t"
      "s_add_u32 s51, s51, 12288\n\t"
      "s_cmp_eq_u32 s51, 160256\n\t"
      "s_cselect_b32 s51, 25088, s51\n\t"
      "v_mfma_f32_16x16x32_bf16 v[20:23], v[180:183], a[28:31], v[20:23]\n\t"
      "s_waitcnt lgkmcnt(6)\n\t"
      "v_mfma_f32_16x16x32_bf16 v[20:23], v[196:199], a[32:35], v[20:23]\n\t"
      "buffer_store_dwordx4 a[56:59], %[stv], s[44:47], s49 offen offset:2048 sc1\n\t"
      "v_mfma_f32_16x16x32_bf16 v[20:23], v[212:215], a[36:39], v[20:23]\n\t"
      "s_add_u32 s49, s49, 3145728\n\t"
      "v_mfma_f32_16x16x32_bf16 v[20:23], v[228:231], a[40:43], v[20:23]\n\t"
      "s_waitcnt lgkmcnt(0)\n\t"
      "s_sub_u32 s53, s36, s38\n\t"
      "s_cmp_lt_i32 s53, 0\n\t"
      "v_mfma_f32_16x16x32_bf16 v[20:23], v[244:247], a[44:47], v[20:23]\n\t"
      "s_cbranch_scc1 .Lspin0_%=\n\t"
      ".Lspun0_%=:\n\t"
      "s_sub_i32 s53, s53, 60\n\t"
      "s_max_i32 s53, s53, 0\n\t"
      "v_mfma_f32_16x16x32_bf16 v[24:27], v[168:171], a[24:27], v[24:27]\n\t"
      "s_add_u32 s38, s38, s53\n\t"
      "s_add_u32 s38, s38, s39\n\t"
      "v_mfma_f32_16x16x32_bf16 v[24:27], v[184:187], a[28:31], v[24:27]\n\t"
      "v_cvt_pk_bf16_f32 v40, v16, v17\n\t"
      "v_cvt_pk_bf16_f32 v41, v18, v19\n\t"
      "v_mfma_f32_16x16x32_bf16 v[24:27], v[200:203], a[32:35], v[24:27]\n\t"
      "ds_write_b64 %[stw], v[40:41] offset:0\n\t"
      "v_mfma_f32_16x16x32_bf16 v[24:27], v[216:219], a[36:39], v[24:27]\n\t"
      "buffer_load_dwordx4 %[vx1], s[40:43], s48 offen offset:1024 lds\n\t"
      "v_mfma_f32_16x16x32_bf16 v[24:27], v[232:235], a[40:43], v[24:27]\n\t"
      "v_mfma_f32_16x16x32_bf16 v[24:27], v[248:251], a[44:47], v[24:27]\n\t"
      "v_mfma_f32_16x16x32_bf16 v[28:31], v[172:175], a[24:27], v[28:31]\n\t"
      "v_mfma_f32_16x16x32_bf16 v[28:31], v[188:191], a[28:31], v[28:31]\n\t"
      "v_cvt_pk_bf16_f32 v42, v20, v21\n\t"
      "v_cvt_pk_bf16_f32 v43, v22, v23\n\t"
      "v_mfma_f32_16x16x32_bf16 v[28:31], v[204:207], a[32:35], v[28:31]\n\t"
      "ds_write_b64 %[stw], v[42:43] offset:32\n\t"
      "v_mfma_f32_16x16x32_bf16 v[28:31], v[220:223], a[36:39], v[28:31]\n\t"
      "v_mfma_f32_16x16x32_bf16 v[28:31], v[236:239], a[40:43], v[28:31]\n\t"
      "v_mfma_f32_16x16x32_bf16 v[28:31], v[252:255], a[44:47], v[28:31]\n\t"
      "v_mfma_f32_16x16x32_bf16 v[32:35], a[108:111], a[24:27], v[32:35]\n\t"
      "v_mfma_f32_16x16x32_bf16 v[32:35], a[116:119], a[28:31], v[32:35]\n\t"
      "v_cvt_pk_bf16_f32 v44, v24, v25\n\t"
      "v_cvt_pk_bf16_f32 v45, v26, v27\n\t"
      "v_mfma_f32_16x16x32_bf16 v[32:35], a[124:127], a[32:35], v[32:35]\n\t"
      "ds_write_b64 %[stw], v[44:45] offset:64\n\t"
      "v_mfma_f32_16x16x32_bf16 v[32:35], a[132:135], a[36:39], v[32:35]\n\t"
      "buffer_load_dwordx4 %[vx2], s[40:43], s48 offen offset:2048 lds\n\t"
      "v_mfma_f32_16x16x32_bf16 v[32:35], a[140:143], a[40:43], v[32:35]\n\t"
      "s_add_u32 s48, s48, 3145728\n\t"
      "s_add_u32 s50, s50, 12288\n\t"
      "v_mfma_f32_16x16x32_bf16 v[32:35], a[148:151], a[44:47], v[32:35]\n\t"
      "v_cvt_pk_bf16_f32 v46, v28, v29\n\t"
      "v_cvt_pk_bf16_f32 v47, v30, v31\n\t"
      "v_mfma_f32_16x16x32_bf16 v[36:39], a[112:115], a[24:27], v[36:39]\n\t"
      "ds_write_b64 %[stw], v[46:47] offset:96\n\t"
      "v_mfma_f32_16x16x32_bf16 v[36:39], a[120:123], a[28:31], v[36:39]\n\t"
      "s_cmp_eq_u32 s50, 160256\n\t"
      "s_cselect_b32 s50, 25088, s50\n\t"
      "v_mfma_f32_16x16x32_bf16 v[36:39], a[128:131], a[32:35], v[36:39]\n\t"
      "v_mfma_f32_16x16x32_bf16 v[36:39], a[136:139], a[36:39], v[36:39]\n\t"
      "v_mfma_f32_16x16x32_bf16 v[36:39], a[144:147], a[40:43], v[36:39]\n\t"
      "v_cvt_pk_bf16_f32 v48, v32, v33\n\t"
      "v_cvt_pk_bf16_f32 v49, v34, v35\n\t"
      "v_mfma_f32_16x16x32_bf16 v[36:39], a[152:155], a[44:47], v[36:39]\n\t"
      "ds_write_b64 %[stw], v[48:49] offset:128\n\t"
      ".Lmid0_%=:\n\t"
      "v_mfma_f32_16x16x32_bf16 v[16:19], v[64:67], a[0:3], 0\n\t"
      "ds_read_b128 a[24:27], v54 offset:256\n\t"
      "v_mfma_f32_16x16x32_bf16 v[16:19], v[80:83], a[4:7], v[16:19]\n\t"
      "ds_read_b128 a[28:31], v55 offset:256\n\t"
      "v_mfma_f32_16x16x32_bf16 v[16:19], v[96:99], a[8:11], v[16:19]\n\t"
      "ds_read_b128 a[32:35], v52 offset:512\n\t"
      "v_mfma_f32_16x16x32_bf16 v[16:19], v[112:115], a[12:15], v[16:19]\n\t"
      "ds_read_b128 a[36:39], v53 offset:512\n\t"
      "v_mfma_f32_16x16x32_bf16 v[16:19], v[128:131], a[16:19], v[16:19]\n\t"
      "ds_read_b128 a[40:43], v54 offset:512\n\t"
      "v_mfma_f32_16x16x32_bf16 v[16:19], v[144:147], a[20:23], v[16:19]\n\t"
      "ds_read_b128 a[44:47], v55 offset:512\n\t"
      "v_mfma_f32_16x16x32_bf16 v[20:23], v[68:71], a[0:3], 0\n\t"
      "v_cvt_pk_bf16_f32 v50, v36, v37\n\t"
      "v_cvt_pk_bf16_f32 v51, v38, v39\n\t"
      "v_mfma_f32_16x16x32_bf16 v[20:23], v[84:87], a[4:7], v[20:23]\n\t"
      "ds_write_b64 %[stw], v[50:51] offset:160\n\t"
      "v_mfma_f32_16x16x32_bf16 v[20:23], v[100:103], a[8:11], v[20:23]\n\t"
      "v_add_u32 v56, s51, %[fb0]\n\t"
      "v_xor_b32 v57, 64, v56\n\t"
      "v_mfma_f32_16x16x32_bf16 v[20:23], v[116:119], a[12:15], v[20:23]\n\t"
      "v_xor_b32 v58, 128, v56\n\t"
      "v_xor_b32 v59, 192, v56\n\t"
      "v_mfma_f32_16x16x32_bf16 v[20:23], v[132:135], a[16:19], v[20:23]\n\t"
      "v_mfma_f32_16x16x32_bf16 v[20:23], v[148:151], a[20:23], v[20:23]\n\t"
      "v_mfma_f32_16x16x32_bf16 v[24:27], v[72:75], a[0:3], 0\n\t"
      "v_mfma_f32_16x16x32_bf16 v[24:27], v[88:91], a[4:7], v[24:27]\n\t"
      "v_mfma_f32_16x16x32_bf16 v[24:27], v[104:107], a[8:11], v[24:27]\n\t"
      "s_waitcnt vmcnt(12)\n\t"
      "s_waitcnt lgkmcnt(0)\n\t"
      "s_barrier\n\t"
      "v_mfma_f32_16x16x32_bf16 v[24:27], v[120:123], a[12:15], v[24:27]\n\t"
      "v_mfma_f32_16x16x32_bf16 v[24:27], v[136:139], a[16:19], v[24:27]\n\t"
      "v_mfma_f32_16x16x32_bf16 v[24:27], v[152:155], a[20:23], v[24:27]\n\t"
      "v_mfma_f32_16x16x32_bf16 v[28:31], v[76:79], a[0:3], 0\n\t"
      "ds_read_b128 a[48:51], %[str0] offset:0\n\t"
      "v_mfma_f32_16x16x32_bf16 v[28:31], v[92:95], a[4:7], v[28:31]\n\t"
      "ds_read_b128 a[52:55], %[str1] offset:1024\n\t"
      "v_mfma_f32_16x16x32_bf16 v[28:31], v[108:111], a[8:11], v[28:31]\n\t"
      "ds_read_b128 a[56:59], %[str2] offset:2048\n\t"
      "v_mfma_f32_16x16x32_bf16 v[28:31], v[124:127], a[12:15], v[28:31]\n\t"
      "s_and_b32 s39, s34, 15\n\t"
      "s_cselect_b32 s53, 79, 0\n\t"
      "v_mfma_f32_16x16x32_bf16 v[28:31], v[140:143], a[16:19], v[28:31]\n\t"
      "s_add_u32 s39, s39, s53\n\t"
      "s_lshr_b64 s[34:35], s[34:35], 4\n\t"
      "v_mfma_f32_16x16x32_bf16 v[28:31], v[156:159], a[20:23], v[28:31]\n\t"
      "v_mfma_f32_16x16x32_bf16 v[32:35], a[60:63], a[0:3], 0\n\t"
      "v_mfma_f32_16x16x32_bf16 v[32:35], a[68:71], a[4:7], v[32:35]\n\t"
      "s_waitcnt lgkmcnt(2)\n\t"
      "v_mfma_f32_16x16x32_bf16 v[32:35], a[76:79], a[8:11], v[32:35]\n\t"
      "buffer_store_dwordx4 a[48:51], %[stv], s[44:47], s49 offen sc1\n\t"
      "v_mfma_f32_16x16x32_bf16 v[32:35], a[84:87], a[12:15], v[32:35]\n\t"
      "v_mfma_f32_16x16x32_bf16 v[32:35], a[92:95], a[16:19], v[32:35]\n\t"
      "v_mfma_f32_16x16x32_bf16 v[32:35], a[100:103], a[20:23], v[32:35]\n\t"
      "s_add_u32 m0, %[m0base], s50\n\t"
      "v_mfma_f32_16x16x32_bf16 v[36:39], a[64:67], a[0:3], 0\n\t"
      "buffer_load_dwordx4 %[vx0], s[40:43], s48 offen lds\n\t"
      "v_mfma_f32_16x16x32_bf16 v[36:39], a[72:75], a[4:7], v[36:39]\n\t"
      "s_memrealtime s[36:37]\n\t"
      "v_mfma_f32_16x16x32_bf16 v[36:39], a[80:83], a[8:11], v[36:39]\n\t"
      "v_mfma_f32_16x16x32_bf16 v[36:39], a[88:91], a[12:15], v[36:39]\n\t"
      "s_waitcnt lgkmcnt(1)\n\t"
      "v_mfma_f32_16x16x32_bf16 v[36:39], a[96:99], a[16:19], v[36:39]\n\t"
      "buffer_store_dwordx4 a[52:55], %[stv], s[44:47], s49 offen offset:1024 sc1\n\t"
      "v_mfma_f32_16x16x32_bf16 v[36:39], a[104:107], a[20:23], v[36:39]\n\t"
      "v_mfma_f32_16x16x32_bf16 v[16:19], v[160:163], a[24:27], v[16:19]\n\t"
      "ds_read_b128 a[0:3], v56 offset:0\n\t"
      "v_mfma_f32_16x16x32_bf16 v[16:19], v[176:179], a[28:31], v[16:19]\n\t"
      "ds_read_b128 a[4:7], v57 offset:0\n\t"
      "v_mfma_f32_16x16x32_bf16 v[16:19], v[192:195], a[32:35], v[16:19]\n\t"
      "ds_read_b128 a[8:11], v58 offset:0\n\t"
      "v_mfma_f32_16x16x32_bf16 v[16:19], v[208:211], a[36:39], v[16:19]\n\t"
      "ds_read_b128 a[12:15], v59 offset:0\n\t"
      "v_mfma_f32_16x16x32_bf16 v[16:19], v[224:227], a[40:43], v[16:19]\n\t"
      "ds_read_b128 a[16:19], v56 offset:256\n\t"
      "v_mfma_f32_16x16x32_bf16 v[16:19], v[240:243], a[44:47], v[16:19]\n\t"
      "ds_read_b128 a[20:23], v57 offset:256\n\t"
      "v_mfma_f32_16x16x32_bf16 v[20:23], v[164:167], a[24:27], v[20:23]\n\t"
      "s_add_u32 s51, s51, 12288\n\t"
      "s_cmp_eq_u32 s51, 160256\n\t"
      "s_cselect_b32 s51, 25088, s51\n\t"
      "v_mfma_f32_16x16x32_bf16 v[20:23], v[180:183], a[28:31], v[20:23]\n\t"
      "s_waitcnt lgkmcnt(6)\n\t"
      "v_mfma_f32_16x16x32_bf16 v[20:23], v[196:199], a[32:35], v[20:23]\n\t"
      "buffer_store_dwordx4 a[56:59], %[stv], s[44:47], s49 offen offset:2048 sc1\n\t"
      "v_mfma_f32_16x16x32_bf16 v[20:23], v[212:215], a[36:39], v[20:23]\n\t"
      "s_add_u32 s49, s49, 3145728\n\t"
      "v_mfma_f32_16x16x32_bf16 v[20:23], v[228:231], a[40:43], v[20:23]\n\t"
      "s_waitcnt lgkmcnt(0)\n\t"
      "s_sub_u32 s53, s36, s38\n\t"
      "s_cmp_lt_i32 s53, 0\n\t"
      "v_mfma_f32_16x16x32_bf16 v[20:23], v[244:247], a[44:47], v[20:23]\n\t"
      "s_cbranch_scc1 .Lspin1_%=\n\t"
      ".Lspun1_%=:\n\t"
      "s_sub_i32 s53, s53, 60\n\t"
      "s_max_i32 s53, s53, 0\n\t"
      "v_mfma_f32_16x16x32_bf16 v[24:27], v[168:171], a[24:27], v[24:27]\n\t"
      "s_add_u32 s38, s38, s53\n\t"
      "s_add_u32 s38, s38, s39\n\t"
      "v_mfma_f32_16x16x32_bf16 v[24:27], v[184:187], a[28:31], v[24:27]\n\t"
      "v_cvt_pk_bf16_f32 v40, v16, v17\n\t"
      "v_cvt_pk_bf16_f32 v41, v18, v19\n\t"
      "v_mfma_f32_16x16x32_bf16 v[24:27], v[200:203], a[32:35], v[24:27]\n\t"
      "ds_write_b64 %[stw], v[40:41] offset:12544\n\t"
      "v_mfma_f32_16x16x32_bf16 v[24:27], v[216:219], a[36:39], v[24:27]\n\t"
      "buffer_load_dwordx4 %[vx1], s[40:43], s48 offen offset:1024 lds\n\t"
      "v_mfma_f32_16x16x32_bf16 v[24:27], v[232:235], a[40:43], v[24:27]\n\t"
      "v_mfma_f32_16x16x32_bf16 v[24:27], v[248:251], a[44:47], v[24:27]\n\t"
      "v_mfma_f32_16x16x32_bf16 v[28:31], v[172:175], a[24:27], v[28:31]\n\t"
      "v_mfma_f32_16x16x32_bf16 v[28:31], v[188:191], a[28:31], v[28:31]\n\t"
      "v_cvt_pk_bf16_f32 v42, v20, v21\n\t"
      "v_cvt_pk_bf16_f32 v43, v22, v23\n\t"
      "v_mfma_f32_16x16x32_bf16 v[28:31], v[204:207], a[32:35], v[28:31]\n\t"
      "ds_write_b64 %[stw], v[42:43] offset:12576\n\t"
      "v_mfma_f32_16x16x32_bf16 v[28:31], v[220:223], a[36:39], v[28:31]\n\t"
      "v_mfma_f32_16x16x32_bf16 v[28:31], v[236:239], a[40:43], v[28:31]\n\t"
      "v_mfma_f32_16x16x32_bf16 v[28:31], v[252:255], a[44:47], v[28:31]\n\t"
      "v_mfma_f32_16x16x32_bf16 v[32:35], a[108:111], a[24:27], v[32:35]\n\t"
      "v_mfma_f32_16x16x32_bf16 v[32:35], a[116:119], a[28:31], v[32:35]\n\t"
      "v_cvt_pk_bf16_f32 v44, v24, v25\n\t"
      "v_cvt_pk_bf16_f32 v45, v26, v27\n\t"
      "v_mfma_f32_16x16x32_bf16 v[32:35], a[124:127], a[32:35], v[32:35]\n\t"
      "ds_write_b64 %[stw], v[44:45] offset:12608\n\t"
      "v_mfma_f32_16x16x32_bf16 v[32:35], a[132:135], a[36:39], v[32:35]\n\t"
      "buffer_load_dwordx4 %[vx2], s[40:43], s48 offen offset:2048 lds\n\t"
      "v_mfma_f32_16x16x32_bf16 v[32:35], a[140:143], a[40:43], v[32:35]\n\t"
      "s_add_u32 s48, s48, 3145728\n\t"
      "s_add_u32 s50, s50, 12288\n\t"
      "v_mfma_f32_16x16x32_bf16 v[32:35], a[148:151], a[44:47], v[32:35]\n\t"
      "v_cvt_pk_bf16_f32 v46, v28, v29\n\t"
      "v_cvt_pk_bf16_f32 v47, v30, v31\n\t"
      "v_mfma_f32_16x16x32_bf16 v[36:39], a[112:115], a[24:27], v[36:39]\n\t"
      "ds_write_b64 %[stw], v[46:47] offset:12640\n\t"
      "v_mfma_f32_16x16x32_bf16 v[36:39], a[120:123], a[28:31], v[36:39]\n\t"
      "s_cmp_eq_u32 s50, 160256\n\t"
      "s_cselect_b32 s50, 25088, s50\n\t"
      "v_mfma_f32_16x16x32_bf16 v[36:39], a[128:131], a[32:35], v[36:39]\n\t"
      "s_sub_u32 s52, s52, 1\n\t"
      "s_cmp_lg_u32 s52, 0\n\t"
      "v_mfma_f32_16x16x32_bf16 v[36:39], a[136:139], a[36:39], v[36:39]\n\t"
      "v_mfma_f32_16x16x32_bf16 v[36:39], a[144:147], a[40:43], v[36:39]\n\t"
      "v_cvt_pk_bf16_f32 v48, v32, v33\n\t"
      "v_cvt_pk_bf16_f32 v49, v34, v35\n\t"
      "v_mfma_f32_16x16x32_bf16 v[36:39], a[152:155], a[44:47], v[36:39]\n\t"
      "ds_write_b64 %[stw], v[48:49] offset:12672\n\t"
      "s_cbranch_scc1 .Lloop0_%=\n\t"
      "s_mov_b32 s52, 2\n\t"
      ".Ldrain0_%=:\n\t"
      "v_mfma_f32_16x16x32_bf16 v[16:19], v[64:67], a[0:3], 0\n\t"
      "ds_read_b128 a[24:27], v58 offset:256\n\t"
      "v_mfma_f32_16x16x32_bf16 v[16:19], v[80:83], a[4:7], v[16:19]\n\t"
      "ds_read_b128 a[28:31], v59 offset:256\n\t"
      "v_mfma_f32_16x16x32_bf16 v[16:19], v[96:99], a[8:11], v[16:19]\n\t"
      "ds_read_b128 a[32:35], v56 offset:512\n\t"
      "v_mfma_f32_16x16x32_bf16 v[16:19], v[112:115], a[12:15], v[16:19]\n\t"
      "ds_read_b128 a[36:39], v57 offset:512\n\t"
      "v_mfma_f32_16x16x32_bf16 v[16:19], v[128:131], a[16:19], v[16:19]\n\t"
      "ds_read_b128 a[40:43], v58 offset:512\n\t"
      "v_mfma_f32_16x16x32_bf16 v[16:19], v[144:147], a[20:23], v[16:19]\n\t"
      "ds_read_b128 a[44:47], v59 offset:512\n\t"
      "v_mfma_f32_16x16x32_bf16 v[20:23], v[68:71], a[0:3], 0\n\t"
      "v_cvt_pk_bf16_f32 v50, v36, v37\n\t"
      "v_cvt_pk_bf16_f32 v51, v38, v39\n\t"
      "v_mfma_f32_16x16x32_bf16 v[20:23], v[84:87], a[4:7], v[20:23]\n\t"
      "ds_write_b64 %[stw], v[50:51] offset:12704\n\t"
      "v_mfma_f32_16x16x32_bf16 v[20:23], v[100:103], a[8:11], v[20:23]\n\t"
      "v_add_u32 v52, s51, %[fb0]\n\t"
      "v_xor_b32 v53, 64, v52\n\t"
      "v_mfma_f32_16x16x32_bf16 v[20:23], v[116:119], a[12:15], v[20:23]\n\t"
      "v_xor_b32 v54, 128, v52\n\t"
      "v_xor_b32 v55, 192, v52\n\t"
      "v_mfma_f32_16x16x32_bf16 v[20:23], v[132:135], a[16:19], v[20:23]\n\t"
      "v_mfma_f32_16x16x32_bf16 v[20:23], v[148:151], a[20:23], v[20:23]\n\t"
      "v_mfma_f32_16x16x32_bf16 v[24:27], v[72:75], a[0:3], 0\n\t"
      "v_mfma_f32_16x16x32_bf16 v[24:27], v[88:91], a[4:7], v[24:27]\n\t"
      "v_mfma_f32_16x16x32_bf16 v[24:27], v[104:107], a[8:11], v[24:27]\n\t"
      "s_waitcnt vmcnt(6)\n\t"
      "s_waitcnt lgkmcnt(0)\n\t"
      "s_barrier\n\t"
      "v_mfma_f32_16x16x32_bf16 v[24:27], v[120:123], a[12:15], v[24:27]\n\t"
      "v_mfma_f32_16x16x32_bf16 v[24:27], v[136:139], a[16:19], v[24:27]\n\t"
      "v_mfma_f32_16x16x32_bf16 v[24:27], v[152:155], a[20:23], v[24:27]\n\t"
      "v_mfma_f32_16x16x32_bf16 v[28:31], v[76:79], a[0:3], 0\n\t"
      "ds_read_b128 a[48:51], %[str0] offset:12544\n\t"
      "v_mfma_f32_16x16x32_bf16 v[28:31], v[92:95], a[4:7], v[28:31]\n\t"
      "ds_read_b128 a[52:55], %[str1] offset:13568\n\t"
      "v_mfma_f32_16x16x32_bf16 v[28:31], v[108:111], a[8:11], v[28:31]\n\t"
      "ds_read_b128 a[56:59], %[str2] offset:14592\n\t"
      "v_mfma_f32_16x16x32_bf16 v[28:31], v[124:127], a[12:15], v[28:31]\n\t"
      "v_mfma_f32_16x16x32_bf16 v[28:31], v[140:143], a[16:19], v[28:31]\n\t"
      "v_mfma_f32_16x16x32_bf16 v[28:31], v[156:159], a[20:23], v[28:31]\n\t"
      "v_mfma_f32_16x16x32_bf16 v[32:35], a[60:63], a[0:3], 0\n\t"
      "v_mfma_f32_16x16x32_bf16 v[32:35], a[68:71], a[4:7], v[32:35]\n\t"
      "s_waitcnt lgkmcnt(2)\n\t"
      "v_mfma_f32_16x16x32_bf16 v[32:35], a[76:79], a[8:11], v[32:35]\n\t"
      "buffer_store_dwordx4 a[48:51], %[stv], s[44:47], s49 offen sc1\n\t"
      "v_mfma_f32_16x16x32_bf16 v[32:35], a[84:87], a[12:15], v[32:35]\n\t"
      "v_mfma_f32_16x16x32_bf16 v[32:35], a[92:95], a[16:19], v[32:35]\n\t"
      "v_mfma_f32_16x16x32_bf16 v[32:35], a[100:103], a[20:23], v[32:35]\n\t"
      "v_mfma_f32_16x16x32_bf16 v[36:39], a[64:67], a[0:3], 0\n\t"
      "v_mfma_f32_16x16x32_bf16 v[36:39], a[72:75], a[4:7], v[36:39]\n\t"
      "v_mfma_f32_16x16x32_bf16 v[36:39], a[80:83], a[8:11], v[36:39]\n\t"
      "v_mfma_f32_16x16x32_bf16 v[36:39], a[88:91], a[12:15], v[36:39]\n\t"
      "s_waitcnt lgkmcnt(1)\n\t"
      "v_mfma_f32_16x16x32_bf16 v[36:39], a[96:99], a[16:19], v[36:39]\n\t"
      "buffer_store_dwordx4 a[52:55], %[stv], s[44:47], s49 offen offset:1024 sc1\n\t"
      "v_mfma_f32_16x16x32_bf16 v[36:39], a[104:107], a[20:23], v[36:39]\n\t"
      "v_mfma_f32_16x16x32_bf16 v[16:19], v[160:163], a[24:27], v[16:19]\n\t"
      "ds_read_b128 a[0:3], v52 offset:0\n\t"
      "v_mfma_f32_16x16x32_bf16 v[16:19], v[176:179], a[28:31], v[16:19]\n\t"
      "ds_read_b128 a[4:7], v53 offset:0\n\t"
      "v_mfma_f32_16x16x32_bf16 v[16:19], v[192:195], a[32:35], v[16:19]\n\t"
      "ds_read_b128 a[8:11], v54 offset:0\n\t"
      "v_mfma_f32_16x16x32_bf16 v[16:19], v[208:211], a[36:39], v[16:19]\n\t"
      "ds_read_b128 a[12:15], v55 offset:0\n\t"
      "v_mfma_f32_16x16x32_bf16 v[16:19], v[224:227], a[40:43], v[16:19]\n\t"
      "ds_read_b128 a[16:19], v52 offset:256\n\t"
      "v_mfma_f32_16x16x32_bf16 v[16:19], v[240:243], a[44:47], v[16:19]\n\t"
      "ds_read_b128 a[20:23], v53 offset:256\n\t"
      "v_mfma_f32_16x16x32_bf16 v[20:23], v[164:167], a[24:27], v[20:23]\n\t"
      "s_add_u32 s51, s51, 12288\n\t"
      "s_cmp_eq_u32 s51, 160256\n\t"
      "s_cselect_b32 s51, 25088, s51\n\t"
      "v_mfma_f32_16x16x32_bf16 v[20:23], v[180:183], a[28:31], v[20:23]\n\t"
      "s_waitcnt lgkmcnt(6)\n\t"
      "v_mfma_f32_16x16x32_bf16 v[20:23], v[196:199], a[32:35], v[20:23]\n\t"
      "buffer_store_dwordx4 a[56:59], %[stv], s[44:47], s49 offen offset:2048 sc1\n\t"
      "v_mfma_f32_16x16x32_bf16 v[20:23], v[212:215], a[36:39], v[20:23]\n\t"
      "s_add_u32 s49, s49, 3145728\n\t"
      "v_mfma_f32_16x16x32_bf16 v[20:23], v[228:231], a[40:43], v[20:23]\n\t"
      "s_waitcnt lgkmcnt(0)\n\t"
      "v_mfma_f32_16x16x32_bf16 v[20:23], v[244:247], a[44:47], v[20:23]\n\t"
      "v_mfma_f32_16x16x32_bf16 v[24:27], v[168:171], a[24:27], v[24:27]\n\t"
      "v_mfma_f32_16x16x32_bf16 v[24:27], v[184:187], a[28:31], v[24:27]\n\t"
      "v_cvt_pk_bf16_f32 v40, v16, v17\n\t"
      "v_cvt_pk_bf16_f32 v41, v18, v19\n\t"
      "v_mfma_f32_16x16x32_bf16 v[24:27], v[200:203], a[32:35], v[24:27]\n\t"
      "ds_write_b64 %[stw], v[40:41] offset:0\n\t"
      "v_mfma_f32_16x16x32_bf16 v[24:27], v[216:219], a[36:39], v[24:27]\n\t"
      "v_mfma_f32_16x16x32_bf16 v[24:27], v[232:235], a[40:43], v[24:27]\n\t"
      "v_mfma_f32_16x16x32_bf16 v[24:27], v[248:251], a[44:47], v[24:27]\n\t"
      "v_mfma_f32_16x16x32_bf16 v[28:31], v[172:175], a[24:27], v[28:31]\n\t"
      "v_mfma_f32_16x16x32_bf16 v[28:31], v[188:191], a[28:31], v[28:31]\n\t"
      "v_cvt_pk_bf16_f32 v42, v20, v21\n\t"
      "v_cvt_pk_bf16_f32 v43, v22, v23\n\t"
      "v_mfma_f32_16x16x32_bf16 v[28:31], v[204:207], a[32:35], v[28:31]\n\t"
      "ds_write_b64 %[stw], v[42:43] offset:32\n\t"
      "v_mfma_f32_16x16x32_bf16 v[28:31], v[220:223], a[36:39], v[28:31]\n\t"
      "v_mfma_f32_16x16x32_bf16 v[28:31], v[236:239], a[40:43], v[28:31]\n\t"
      "v_mfma_f32_16x16x32_bf16 v[28:31], v[252:255], a[44:47], v[28:31]\n\t"
      "v_mfma_f32_16x16x32_bf16 v[32:35], a[108:111], a[24:27], v[32:35]\n\t"
      "v_mfma_f32_16x16x32_bf16 v[32:35], a[116:119], a[28:31], v[32:35]\n\t"
      "v_cvt_pk_bf16_f32 v44, v24, v25\n\t"
      "v_cvt_pk_bf16_f32 v45, v26, v27\n\t"
      "v_mfma_f32_16x16x32_bf16 v[32:35], a[124:127], a[32:35], v[32:35]\n\t"
      "ds_write_b64 %[stw], v[44:45] offset:64\n\t"
      "v_mfma_f32_16x16x32_bf16 v[32:35], a[132:135], a[36:39], v[32:35]\n\t"
      "v_mfma_f32_16x16x32_bf16 v[32:35], a[140:143], a[40:43], v[32:35]\n\t"
      "v_mfma_f32_16x16x32_bf16 v[32:35], a[148:151], a[44:47], v[32:35]\n\t"
      "v_cvt_pk_bf16_f32 v46, v28, v29\n\t"
      "v_cvt_pk_bf16_f32 v47, v30, v31\n\t"
      "v_mfma_f32_16x16x32_bf16 v[36:39], a[112:115], a[24:27], v[36:39]\n\t"
      "ds_write_b64 %[stw], v[46:47] offset:96\n\t"
      "v_mfma_f32_16x16x32_bf16 v[36:39], a[120:123], a[28:31], v[36:39]\n\t"
      "v_mfma_f32_16x16x32_bf16 v[36:39], a[128:131], a[32:35], v[36:39]\n\t"
      "v_mfma_f32_16x16x32_bf16 v[36:39], a[136:139], a[36:39], v[36:39]\n\t"
      "v_mfma_f32_16x16x32_bf16 v[36:39], a[144:147], a[40:43], v[36:39]\n\t"
      "v_cvt_pk_bf16_f32 v48, v32, v33\n\t"
      "v_cvt_pk_bf16_f32 v49, v34, v35\n\t"
      "v_mfma_f32_16x16x32_bf16 v[36:39], a[152:155], a[44:47], v[36:39]\n\t"
      "ds_write_b64 %[stw], v[48:49] offset:128\n\t"
      "v_mfma_f32_16x16x32_bf16 v[16:19], v[64:67], a[0:3], 0\n\t"
      "ds_read_b128 a[24:27], v54 offset:256\n\t"
      "v_mfma_f32_16x16x32_bf16 v[16:19], v[80:83], a[4:7], v[16:19]\n\t"
      "ds_read_b128 a[28:31], v55 offset:256\n\t"
      "v_mfma_f32_16x16x32_bf16 v[16:19], v[96:99], a[8:11], v[16:19]\n\t"
      "ds_read_b128 a[32:35], v52 offset:512\n\t"
      "v_mfma_f32_16x16x32_bf16 v[16:19], v[112:115], a[12:15], v[16:19]\n\t"
      "ds_read_b128 a[36:39], v53 offset:512\n\t"
      "v_mfma_f32_16x16x32_bf16 v[16:19], v[128:131], a[16:19], v[16:19]\n\t"
      "ds_read_b128 a[40:43], v54 offset:512\n\t"
      "v_mfma_f32_16x16x32_bf16 v[16:19], v[144:147], a[20:23], v[16:19]\n\t"
      "ds_read_b128 a[44:47], v55 offset:512\n\t"
      "v_mfma_f32_16x16x32_bf16 v[20:23], v[68:71], a[0:3], 0\n\t"
      "v_cvt_pk_bf16_f32 v50, v36, v37\n\t"
      "v_cvt_pk_bf16_f32 v51, v38, v39\n\t"
      "v_mfma_f32_16x16x32_bf16 v[20:23], v[84:87], a[4:7], v[20:23]\n\t"
      "ds_write_b64 %[stw], v[50:51] offset:160\n\t"
      "v_mfma_f32_16x16x32_bf16 v[20:23], v[100:103], a[8:11], v[20:23]\n\t"
      "v_add_u32 v56, s51, %[fb0]\n\t"
      "v_xor_b32 v57, 64, v56\n\t"
      "v_mfma_f32_16x16x32_bf16 v[20:23], v[116:119], a[12:15], v[20:23]\n\t"
      "v_xor_b32 v58, 128, v56\n\t"
      "v_xor_b32 v59, 192, v56\n\t"
      "v_mfma_f32_16x16x32_bf16 v[20:23], v[132:135], a[16:19], v[20:23]\n\t"
      "v_mfma_f32_16x16x32_bf16 v[20:23], v[148:151], a[20:23], v[20:23]\n\t"
      "v_mfma_f32_16x16x32_bf16 v[24:27], v[72:75], a[0:3], 0\n\t"
      "v_mfma_f32_16x16x32_bf16 v[24:27], v[88:91], a[4:7], v[24:27]\n\t"
      "v_mfma_f32_16x16x32_bf16 v[24:27], v[104:107], a[8:11], v[24:27]\n\t"
      "s_waitcnt vmcnt(6)\n\t"
      "s_waitcnt lgkmcnt(0)\n\t"
      "s_barrier\n\t"
      "v_mfma_f32_16x16x32_bf16 v[24:27], v[120:123], a[12:15], v[24:27]\n\t"
      "v_mfma_f32_16x16x32_bf16 v[24:27], v[136:139], a[16:19], v[24:27]\n\t"
      "v_mfma_f32_16x16x32_bf16 v[24:27], v[152:155], a[20:23], v[24:27]\n\t"
      "v_mfma_f32_16x16x32_bf16 v[28:31], v[76:79], a[0:3], 0\n\t"
      "ds_read_b128 a[48:51], %[str0] offset:0\n\t"
      "v_mfma_f32_16x16x32_bf16 v[28:31], v[92:95], a[4:7], v[28:31]\n\t"
      "ds_read_b128 a[52:55], %[str1] offset:1024\n\t"
      "v_mfma_f32_16x16x32_bf16 v[28:31], v[108:111], a[8:11], v[28:31]\n\t"
      "ds_read_b128 a[56:59], %[str2] offset:2048\n\t"
      "v_mfma_f32_16x16x32_bf16 v[28:31], v[124:127], a[12:15], v[28:31]\n\t"
      "v_mfma_f32_16x16x32_bf16 v[28:31], v[140:143], a[16:19], v[28:31]\n\t"
      "v_mfma_f32_16x16x32_bf16 v[28:31], v[156:159], a[20:23], v[28:31]\n\t"
      "v_mfma_f32_16x16x32_bf16 v[32:35], a[60:63], a[0:3], 0\n\t"
      "v_mfma_f32_16x16x32_bf16 v[32:35], a[68:71], a[4:7], v[32:35]\n\t"
      "s_waitcnt lgkmcnt(2)\n\t"
      "v_mfma_f32_16x16x32_bf16 v[32:35], a[76:79], a[8:11], v[32:35]\n\t"
      "buffer_store_dwordx4 a[48:51], %[stv], s[44:47], s49 offen sc1\n\t"
      "v_mfma_f32_16x16x32_bf16 v[32:35], a[84:87], a[12:15], v[32:35]\n\t"
      "v_mfma_f32_16x16x32_bf16 v[32:35], a[92:95], a[16:19], v[32:35]\n\t"
      "v_mfma_f32_16x16x32_bf16 v[32:35], a[100:103], a[20:23], v[32:35]\n\t"
      "v_mfma_f32_16x16x32_bf16 v[36:39], a[64:67], a[0:3], 0\n\t"
      "v_mfma_f32_16x16x32_bf16 v[36:39], a[72:75], a[4:7], v[36:39]\n\t"
      "v_mfma_f32_16x16x32_bf16 v[36:39], a[80:83], a[8:11], v[36:39]\n\t"
      "v_mfma_f32_16x16x32_bf16 v[36:39], a[88:91], a[12:15], v[36:39]\n\t"
      "s_waitcnt lgkmcnt(1)\n\t"
      "v_mfma_f32_16x16x32_bf16 v[36:39], a[96:99], a[16:19], v[36:39]\n\t"
      "buffer_store_dwordx4 a[52:55], %[stv], s[44:47], s49 offen offset:1024 sc1\n\t"
      "v_mfma_f32_16x16x32_bf16 v[36:39], a[104:107], a[20:23], v[36:39]\n\t"
      "v_mfma_f32_16x16x32_bf16 v[16:19], v[160:163], a[24:27], v[16:19]\n\t"
      "ds_read_b128 a[0:3], v56 offset:0\n\t"
      "v_mfma_f32_16x16x32_bf16 v[16:19], v[176:179], a[28:31], v[16:19]\n\t"
      "ds_read_b128 a[4:7], v57 offset:0\n\t"
      "v_mfma_f32_16x16x32_bf16 v[16:19], v[192:195], a[32:35], v[16:19]\n\t"
      "ds_read_b128 a[8:11], v58 offset:0\n\t"
      "v_mfma_f32_16x16x32_bf16 v[16:19], v[208:211], a[36:39], v[16:19]\n\t"
      "ds_read_b128 a[12:15], v59 offset:0\n\t"
      "v_mfma_f32_16x16x32_bf16 v[16:19], v[224:227], a[40:43], v[16:19]\n\t"
      "ds_read_b128 a[16:19], v56 offset:256\n\t"
      "v_mfma_f32_16x16x32_bf16 v[16:19], v[240:243], a[44:47], v[16:19]\n\t"
      "ds_read_b128 a[20:23], v57 offset:256\n\t"
      "v_mfma_f32_16x16x32_bf16 v[20:23], v[164:167], a[24:27], v[20:23]\n\t"
      "s_add_u32 s51, s51, 12288\n\t"
      "s_cmp_eq_u32 s51, 160256\n\t"
      "s_cselect_b32 s51, 25088, s51\n\t"
      "v_mfma_f32_16x16x32_bf16 v[20:23], v[180:183], a[28:31], v[20:23]\n\t"
      "s_waitcnt lgkmcnt(6)\n\t"
      "v_mfma_f32_16x16x32_bf16 v[20:23], v[196:199], a[32:35], v[20:23]\n\t"
      "buffer_store_dwordx4 a[56:59], %[stv], s[44:47], s49 offen offset:2048 sc1\n\t"
      "v_mfma_f32_16x16x32_bf16 v[20:23], v[212:215], a[36:39], v[20:23]\n\t"
      "s_add_u32 s49, s49, 3145728\n\t"
      "v_mfma_f32_16x16x32_bf16 v[20:23], v[228:231], a[40:43], v[20:23]\n\t"
      "s_waitcnt lgkmcnt(0)\n\t"
      "v_mfma_f32_16x16x32_bf16 v[20:23], v[244:247], a[44:47], v[20:23]\n\t"
      "v_mfma_f32_16x16x32_bf16 v[24:27], v[168:171], a[24:27], v[24:27]\n\t"
      "v_mfma_f32_16x16x32_bf16 v[24:27], v[184:187], a[28:31], v[24:27]\n\t"
      "v_cvt_pk_bf16_f32 v40, v16, v17\n\t"
      "v_cvt_pk_bf16_f32 v41, v18, v19\n\t"
      "v_mfma_f32_16x16x32_bf16 v[24:27], v[200:203], a[32:35], v[24:27]\n\t"
      "ds_write_b64 %[stw], v[40:41] offset:12544\n\t"
      "v_mfma_f32_16x16x32_bf16 v[24:27], v[216:219], a[36:39], v[24:27]\n\t"
      "v_mfma_f32_16x16x32_bf16 v[24:27], v[232:235], a[40:43], v[24:27]\n\t"
      "v_mfma_f32_16x16x32_bf16 v[24:27], v[248:251], a[44:47], v[24:27]\n\t"
      "v_mfma_f32_16x16x32_bf16 v[28:31], v[172:175], a[24:27], v[28:31]\n\t"
      "v_mfma_f32_16x16x32_bf16 v[28:31], v[188:191], a[28:31], v[28:31]\n\t"
      "v_cvt_pk_bf16_f32 v42, v20, v21\n\t"
      "v_cvt_pk_bf16_f32 v43, v22, v23\n\t"
      "v_mfma_f32_16x16x32_bf16 v[28:31], v[204:207], a[32:35], v[28:31]\n\t"
      "ds_write_b64 %[stw], v[42:43] offset:12576\n\t"
      "v_mfma_f32_16x16x32_bf16 v[28:31], v[220:223], a[36:39], v[28:31]\n\t"
      "v_mfma_f32_16x16x32_bf16 v[28:31], v[236:239], a[40:43], v[28:31]\n\t"
      "v_mfma_f32_16x16x32_bf16 v[28:31], v[252:255], a[44:47], v[28:31]\n\t"
      "v_mfma_f32_16x16x32_bf16 v[32:35], a[108:111], a[24:27], v[32:35]\n\t"
      "v_mfma_f32_16x16x32_bf16 v[32:35], a[116:119], a[28:31], v[32:35]\n\t"
      "v_cvt_pk_bf16_f32 v44, v24, v25\n\t"
      "v_cvt_pk_bf16_f32 v45, v26, v27\n\t"
      "v_mfma_f32_16x16x32_bf16 v[32:35], a[124:127], a[32:35], v[32:35]\n\t"
      "ds_write_b64 %[stw], v[44:45] offset:12608\n\t"
      "v_mfma_f32_16x16x32_bf16 v[32:35], a[132:135], a[36:39], v[32:35]\n\t"
      "v_mfma_f32_16x16x32_bf16 v[32:35], a[140:143], a[40:43], v[32:35]\n\t"
      "v_mfma_f32_16x16x32_bf16 v[32:35], a[148:151], a[44:47], v[32:35]\n\t"
      "v_cvt_pk_bf16_f32 v46, v28, v29\n\t"
      "v_cvt_pk_bf16_f32 v47, v30, v31\n\t"
      "v_mfma_f32_16x16x32_bf16 v[36:39], a[112:115], a[24:27], v[36:39]\n\t"
      "ds_write_b64 %[stw], v[46:47] offset:12640\n\t"
      "v_mfma_f32_16x16x32_bf16 v[36:39], a[120:123], a[28:31], v[36:39]\n\t"
      "v_mfma_f32_16x16x32_bf16 v[36:39], a[128:131], a[32:35], v[36:39]\n\t"
      "s_sub_u32 s52, s52, 1\n\t"
      "s_cmp_lg_u32 s52, 0\n\t"
      "v_mfma_f32_16x16x32_bf16 v[36:39], a[136:139], a[36:39], v[36:39]\n\t"
      "v_mfma_f32_16x16x32_bf16 v[36:39], a[144:147], a[40:43], v[36:39]\n\t"
      "v_cvt_pk_bf16_f32 v48, v32, v33\n\t"
      "v_cvt_pk_bf16_f32 v49, v34, v35\n\t"
      "v_mfma_f32_16x16x32_bf16 v[36:39], a[152:155], a[44:47], v[36:39]\n\t"
      "ds_write_b64 %[stw], v[48:49] offset:12672\n\t"
      "s_cbranch_scc1 .Ldrain0_%=\n\t"
      "s_branch .Ltail_%=\n\t"
      ".Lloop1_%=:\n\t"
      "v_mfma_f32_16x16x32_bf16 v[16:19], v[64:67], a[0:3], 0\n\t"
      "ds_read_b128 a[24:27], v58 offset:256\n\t"
      "v_mfma_f32_16x16x32_bf16 v[16:19], v[80:83], a[4:7], v[16:19]\n\t"
      "ds_read_b128 a[28:31], v59 offset:256\n\t"
      "v_mfma_f32_16x16x32_bf16 v[16:19], v[96:99], a[8:11], v[16:19]\n\t"
      "ds_read_b128 a[32:35], v56 offset:512\n\t"
      "v_mfma_f32_16x16x32_bf16 v[16:19], v[112:115], a[12:15], v[16:19]\n\t"
      "ds_read_b128 a[36:39], v57 offset:512\n\t"
      "v_mfma_f32_16x16x32_bf16 v[16:19], v[128:131], a[16:19], v[16:19]\n\t"
      "ds_read_b128 a[40:43], v58 offset:512\n\t"
      "v_mfma_f32_16x16x32_bf16 v[16:19], v[144:147], a[20:23], v[16:19]\n\t"
      "ds_read_b128 a[44:47], v59 offset:512\n\t"
      "v_mfma_f32_16x16x32_bf16 v[20:23], v[68:71], a[0:3], 0\n\t"
      "v_cvt_pk_bf16_f32 v50, v36, v37\n\t"
      "v_cvt_pk_bf16_f32 v51, v38, v39\n\t"
      "v_mfma_f32_16x16x32_bf16 v[20:23], v[84:87], a[4:7], v[20:23]\n\t"
      "ds_write_b64 %[stw], v[50:51] offset:12704\n\t"
      "v_mfma_f32_16x16x32_bf16 v[20:23], v[100:103], a[8:11], v[20:23]\n\t"
      "v_add_u32 v52, s51, %[fb0]\n\t"
      "v_xor_b32 v53, 64, v52\n\t"
      "v_mfma_f32_16x16x32_bf16 v[20:23], v[116:119], a[12:15], v[20:23]\n\t"
      "v_xor_b32 v54, 128, v52\n\t"
      "v_xor_b32 v55, 192, v52\n\t"
      "v_mfma_f32_16x16x32_bf16 v[20:23], v[132:135], a[16:19], v[20:23]\n\t"
      "v_mfma_f32_16x16x32_bf16 v[20:23], v[148:151], a[20:23], v[20:23]\n\t"
      "v_mfma_f32_16x16x32_bf16 v[24:27], v[72:75], a[0:3], 0\n\t"
      "v_mfma_f32_16x16x32_bf16 v[24:27], v[88:91], a[4:7], v[24:27]\n\t"
      "v_mfma_f32_16x16x32_bf16 v[24:27], v[104:107], a[8:11], v[24:27]\n\t"
      "v_mfma_f32_16x16x32_bf16 v[24:27], v[120:123], a[12:15], v[24:27]\n\t"
      "s_waitcnt vmcnt(12)\n\t"
      "s_waitcnt lgkmcnt(0)\n\t"
      "s_barrier\n\t"
      "v_mfma_f32_16x16x32_bf16 v[24:27], v[136:139], a[16:19], v[24:27]\n\t"
      "v_mfma_f32_16x16x32_bf16 v[24:27], v[152:155], a[20:23], v[24:27]\n\t"
      "v_mfma_f32_16x16x32_bf16 v[28:31], v[76:79], a[0:3], 0\n\t"
      "ds_read_b128 a[48:51], %[str0] offset:12544\n\t"
      "v_mfma_f32_16x16x32_bf16 v[28:31], v[92:95], a[4:7], v[28:31]\n\t"
      "ds_read_b128 a[52:55], %[str1] offset:13568\n\t"
      "v_mfma_f32_16x16x32_bf16 v[28:31], v[108:111], a[8:11], v[28:31]\n\t"
      "ds_read_b128 a[56:59], %[str2] offset:14592\n\t"
      "v_mfma_f32_16x16x32_bf16 v[28:31], v[124:127], a[12:15], v[28:31]\n\t"
      "v_mfma_f32_16x16x32_bf16 v[28:31], v[140:143], a[16:19], v[28:31]\n\t"
      "v_mfma_f32_16x16x32_bf16 v[28:31], v[156:159], a[20:23], v[28:31]\n\t"
      "v_mfma_f32_16x16x32_bf16 v[32:35], a[60:63], a[0:3], 0\n\t"
      "v_mfma_f32_16x16x32_bf16 v[32:35], a[68:71], a[4:7], v[32:35]\n\t"
      "s_waitcnt lgkmcnt(2)\n\t"
      "v_mfma_f32_16x16x32_bf16 v[32:35], a[76:79], a[8:11], v[32:35]\n\t"
      "buffer_store_dwordx4 a[48:51], %[stv], s[44:47], s49 offen sc1\n\t"
      "v_mfma_f32_16x16x32_bf16 v[32:35], a[84:87], a[12:15], v[32:35]\n\t"
      "v_mfma_f32_16x16x32_bf16 v[32:35], a[92:95], a[16:19], v[32:35]\n\t"
      "v_mfma_f32_16x16x32_bf16 v[32:35], a[100:103], a[20:23], v[32:35]\n\t"
      "s_add_u32 m0, %[m0base], s50\n\t"
      "v_mfma_f32_16x16x32_bf16 v[36:39], a[64:67], a[0:3], 0\n\t"
      "buffer_load_dwordx4 %[vx0], s[40:43], s48 offen lds\n\t"
      "v_mfma_f32_16x16x32_bf16 v[36:39], a[72:75], a[4:7], v[36:39]\n\t"
      "v_mfma_f32_16x16x32_bf16 v[36:39], a[80:83], a[8:11], v[36:39]\n\t"
      "v_mfma_f32_16x16x32_bf16 v[36:39], a[88:91], a[12:15], v[36:39]\n\t"
      "s_waitcnt lgkmcnt(1)\n\t"
      "v_mfma_f32_16x16x32_bf16 v[36:39], a[96:99], a[16:19], v[36:39]\n\t"
      "buffer_store_dwordx4 a[52:55], %[stv], s[44:47], s49 offen offset:1024 sc1\n\t"
      "v_mfma_f32_16x16x32_bf16 v[36:39], a[104:107], a[20:23], v[36:39]\n\t"
      "v_mfma_f32_16x16x32_bf16 v[16:19], v[160:163], a[24:27], v[16:19]\n\t"
      "ds_read_b128 a[0:3], v52 offset:0\n\t"
      "v_mfma_f32_16x16x32_bf16 v[16:19], v[176:179], a[28:31], v[16:19]\n\t"
      "ds_read_b128 a[4:7], v53 offset:0\n\t"
      "v_mfma_f32_16x16x32_bf16 v[16:19], v[192:195], a[32:35], v[16:19]\n\t"
      "ds_read_b128 a[8:11], v54 offset:0\n\t"
      "v_mfma_f32_16x16x32_bf16 v[16:19], v[208:211], a[36:39], v[16:19]\n\t"
      "ds_read_b128 a[12:15], v55 offset:0\n\t"
      "v_mfma_f32_16x16x32_bf16 v[16:19], v[224:227], a[40:43], v[16:19]\n\t"
      "ds_read_b128 a[16:19], v52 offset:256\n\t"
      "v_mfma_f32_16x16x32_bf16 v[16:19], v[240:243], a[44:47], v[16:19]\n\t"
      "ds_read_b128 a[20:23], v53 offset:256\n\t"
      "v_mfma_f32_16x16x32_bf16 v[20:23], v[164:167], a[24:27], v[20:23]\n\t"
      "s_add_u32 s51, s51, 12288\n\t"
      "s_cmp_eq_u32 s51, 160256\n\t"
      "s_cselect_b32 s51, 25088, s51\n\t"
      "v_mfma_f32_16x16x32_bf16 v[20:23], v[180:183], a[28:31], v[20:23]\n\t"
      "s_waitcnt lgkmcnt(6)\n\t"
      "v_mfma_f32_16x16x32_bf16 v[20:23], v[196:199], a[32:35], v[20:23]\n\t"
      "buffer_store_dwordx4 a[56:59], %[stv], s[44:47], s49 offen offset:2048 sc1\n\t"
      "v_mfma_f32_16x16x32_bf16 v[20:23], v[212:215], a[36:39], v[20:23]\n\t"
      "s_add_u32 s49, s49, 3145728\n\t"
      "v_mfma_f32_16x16x32_bf16 v[20:23], v[228:231], a[40:43], v[20:23]\n\t"
      "s_waitcnt lgkmcnt(0)\n\t"
      "v_mfma_f32_16x16x32_bf16 v[20:23], v[244:247], a[44:47], v[20:23]\n\t"
      "v_mfma_f32_16x16x32_bf16 v[24:27], v[168:171], a[24:27], v[24:27]\n\t"
      "v_mfma_f32_16x16x32_bf16 v[24:27], v[184:187], a[28:31], v[24:27]\n\t"
      "v_cvt_pk_bf16_f32 v40, v16, v17\n\t"
      "v_cvt_pk_bf16_f32 v41, v18, v19\n\t"
      "v_mfma_f32_16x16x32_bf16 v[24:27], v[200:203], a[32:35], v[24:27]\n\t"
      "ds_write_b64 %[stw], v[40:41] offset:0\n\t"
      "v_mfma_f32_16x16x32_bf16 v[24:27], v[216:219], a[36:39], v[24:27]\n\t"
      "buffer_load_dwordx4 %[vx1], s[40:43], s48 offen offset:1024 lds\n\t"
      "v_mfma_f32_16x16x32_bf16 v[24:27], v[232:235], a[40:43], v[24:27]\n\t"
      "v_mfma_f32_16x16x32_bf16 v[24:27], v[248:251], a[44:47], v[24:27]\n\t"
      "v_mfma_f32_16x16x32_bf16 v[28:31], v[172:175], a[24:27], v[28:31]\n\t"
      "v_mfma_f32_16x16x32_bf16 v[28:31], v[188:191], a[28:31], v[28:31]\n\t"
      "v_cvt_pk_bf16_f32 v42, v20, v21\n\t"
      "v_cvt_pk_bf16_f32 v43, v22, v23\n\t"
      "v_mfma_f32_16x16x32_bf16 v[28:31], v[204:207], a[32:35], v[28:31]\n\t"
      "ds_write_b64 %[stw], v[42:43] offset:32\n\t"
      "v_mfma_f32_16x16x32_bf16 v[28:31], v[220:223], a[36:39], v[28:31]\n\t"
      "v_mfma_f32_16x16x32_bf16 v[28:31], v[236:239], a[40:43], v[28:31]\n\t"
      "v_mfma_f32_16x16x32_bf16 v[28:31], v[252:255], a[44:47], v[28:31]\n\t"
      "v_mfma_f32_16x16x32_bf16 v[32:35], a[108:111], a[24:27], v[32:35]\n\t"
      "v_mfma_f32_16x16x32_bf16 v[32:35], a[116:119], a[28:31], v[32:35]\n\t"
      "v_cvt_pk_bf16_f32 v44, v24, v25\n\t"
      "v_cvt_pk_bf16_f32 v45, v26, v27\n\t"
      "v_mfma_f32_16x16x32_bf16 v[32:35], a[124:127], a[32:35], v[32:35]\n\t"
      "ds_write_b64 %[stw], v[44:45] offset:64\n\t"
      "v_mfma_f32_16x16x32_bf16 v[32:35], a[132:135], a[36:39], v[32:35]\n\t"
      "buffer_load_dwordx4 %[vx2], s[40:43], s48 offen offset:2048 lds\n\t"
      "v_mfma_f32_16x16x32_bf16 v[32:35], a[140:143], a[40:43], v[32:35]\n\t"
      "s_add_u32 s48, s48, 3145728\n\t"
      "s_add_u32 s50, s50, 12288\n\t"
      "v_mfma_f32_16x16x32_bf16 v[32:35], a[148:151], a[44:47], v[32:35]\n\t"
      "v_cvt_pk_bf16_f32 v46, v28, v29\n\t"
      "v_cvt_pk_bf16_f32 v47, v30, v31\n\t"
      "v_mfma_f32_16x16x32_bf16 v[36:39], a[112:115], a[24:27], v[36:39]\n\t"
      "ds_write_b64 %[stw], v[46:47] offset:96\n\t"
      "v_mfma_f32_16x16x32_bf16 v[36:39], a[120:123], a[28:31], v[36:39]\n\t"
      "s_cmp_eq_u32 s50, 160256\n\t"
      "s_cselect_b32 s50, 25088, s50\n\t"
      "v_mfma_f32_16x16x32_bf16 v[36:39], a[128:131], a[32:35], v[36:39]\n\t"
      "v_mfma_f32_16x16x32_bf16 v[36:39], a[136:139], a[36:39], v[36:39]\n\t"
      "v_mfma_f32_16x16x32_bf16 v[36:39], a[144:147], a[40:43], v[36:39]\n\t"
      "v_cvt_pk_bf16_f32 v48, v32, v33\n\t"
      "v_cvt_pk_bf16_f32 v49, v34, v35\n\t"
      "v_mfma_f32_16x16x32_bf16 v[36:39], a[152:155], a[44:47], v[36:39]\n\t"
      "ds_write_b64 %[stw], v[48:49] offset:128\n\t"
      ".Lmid1_%=:\n\t"
      "v_mfma_f32_16x16x32_bf16 v[16:19], v[64:67], a[0:3], 0\n\t"
      "ds_read_b128 a[24:27], v54 offset:256\n\t"
      "v_mfma_f32_16x16x32_bf16 v[16:19], v[80:83], a[4:7], v[16:19]\n\t"
      "ds_read_b128 a[28:31], v55 offset:256\n\t"
      "v_mfma_f32_16x16x32_bf16 v[16:19], v[96:99], a[8:11], v[16:19]\n\t"
      "ds_read_b128 a[32:35], v52 offset:512\n\t"
      "v_mfma_f32_16x16x32_bf16 v[16:19], v[112:115], a[12:15], v[16:19]\n\t"
      "ds_read_b128 a[36:39], v53 offset:512\n\t"
      "v_mfma_f32_16x16x32_bf16 v[16:19], v[128:131], a[16:19], v[16:19]\n\t"
      "ds_read_b128 a[40:43], v54 offset:512\n\t"
      "v_mfma_f32_16x16x32_bf16 v[16:19], v[144:147], a[20:23], v[16:19]\n\t"
      "ds_read_b128 a[44:47], v55 offset:512\n\t"
      "v_mfma_f32_16x16x32_bf16 v[20:23], v[68:71], a[0:3], 0\n\t"
      "v_cvt_pk_bf16_f32 v50, v36, v37\n\t"
      "v_cvt_pk_bf16_f32 v51, v38, v39\n\t"
      "v_mfma_f32_16x16x32_bf16 v[20:23], v[84:87], a[4:7], v[20:23]\n\t"
      "ds_write_b64 %[stw], v[50:51] offset:160\n\t"
      "v_mfma_f32_16x16x32_bf16 v[20:23], v[100:103], a[8:11], v[20:23]\n\t"
      "v_add_u32 v56, s51, %[fb0]\n\t"
      "v_xor_b32 v57, 64, v56\n\t"
      "v_mfma_f32_16x16x32_bf16 v[20:23], v[116:119], a[12:15], v[20:23]\n\t"
      "v_xor_b32 v58, 128, v56\n\t"
      "v_xor_b32 v59, 192, v56\n\t"
      "v_mfma_f32_16x16x32_bf16 v[20:23], v[132:135], a[16:19], v[20:23]\n\t"
      "v_mfma_f32_16x16x32_bf16 v[20:23], v[148:151], a[20:23], v[20:23]\n\t"
      "v_mfma_f32_16x16x32_bf16 v[24:27], v[72:75], a[0:3], 0\n\t"
      "v_mfma_f32_16x16x32_bf16 v[24:27], v[88:91], a[4:7], v[24:27]\n\t"
      "v_mfma_f32_16x16x32_bf16 v[24:27], v[104:107], a[8:11], v[24:27]\n\t"
      "v_mfma_f32_16x16x32_bf16 v[24:27], v[120:123], a[12:15], v[24:27]\n\t"
      "s_waitcnt vmcnt(12)\n\t"
      "s_waitcnt lgkmcnt(0)\n\t"
      "s_barrier\n\t"
      "v_mfma_f32_16x16x32_bf16 v[24:27], v[136:139], a[16:19], v[24:27]\n\t"
      "v_mfma_f32_16x16x32_bf16 v[24:27], v[152:155], a[20:23], v[24:27]\n\t"
      "v_mfma_f32_16x16x32_bf16 v[28:31], v[76:79], a[0:3], 0\n\t"
      "ds_read_b128 a[48:51], %[str0] offset:0\n\t"
      "v_mfma_f32_16x16x32_bf16 v[28:31], v[92:95], a[4:7], v[28:31]\n\t"
      "ds_read_b128 a[52:55], %[str1] offset:1024\n\t"
      "v_mfma_f32_16x16x32_bf16 v[28:31], v[108:111], a[8:11], v[28:31]\n\t"
      "ds_read_b128 a[56:59], %[str2] offset:2048\n\t"
      "v_mfma_f32_16x16x32_bf16 v[28:31], v[124:127], a[12:15], v[28:31]\n\t"
      "v_mfma_f32_16x16x32_bf16 v[28:31], v[140:143], a[16:19], v[28:31]\n\t"
      "v_mfma_f32_16x16x32_bf16 v[28:31], v[156:159], a[20:23], v[28:31]\n\t"
      "v_mfma_f32_16x16x32_bf16 v[32:35], a[60:63], a[0:3], 0\n\t"
      "v_mfma_f32_16x16x32_bf16 v[32:35], a[68:71], a[4:7], v[32:35]\n\t"
      "s_waitcnt lgkmcnt(2)\n\t"
      "v_mfma_f32_16x16x32_bf16 v[32:35], a[76:79], a[8:11], v[32:35]\n\t"
      "buffer_store_dwordx4 a[48:51], %[stv], s[44:47], s49 offen sc1\n\t"
      "v_mfma_f32_16x16x32_bf16 v[32:35], a[84:87], a[12:15], v[32:35]\n\t"
      "v_mfma_f32_16x16x32_bf16 v[32:35], a[92:95], a[16:19], v[32:35]\n\t"
      "v_mfma_f32_16x16x32_bf16 v[32:35], a[100:103], a[20:23], v[32:35]\n\t"
      "s_add_u32 m0, %[m0base], s50\n\t"
      "v_mfma_f32_16x16x32_bf16 v[36:39], a[64:67], a[0:3], 0\n\t"
      "buffer_load_dwordx4 %[vx0], s[40:43], s48 offen lds\n\t"
      "v_mfma_f32_16x16x32_bf16 v[36:39], a[72:75], a[4:7], v[36:39]\n\t"
      "v_mfma_f32_16x16x32_bf16 v[36:39], a[80:83], a[8:11], v[36:39]\n\t"
      "v_mfma_f32_16x16x32_bf16 v[36:39], a[88:91], a[12:15], v[36:39]\n\t"
      "s_waitcnt lgkmcnt(1)\n\t"
      "v_mfma_f32_16x16x32_bf16 v[36:39], a[96:99], a[16:19], v[36:39]\n\t"
      "buffer_store_dwordx4 a[52:55], %[stv], s[44:47], s49 offen offset:1024 sc1\n\t"
      "v_mfma_f32_16x16x32_bf16 v[36:39], a[104:107], a[20:23], v[36:39]\n\t"
      "v_mfma_f32_16x16x32_bf16 v[16:19], v[160:163], a[24:27], v[16:19]\n\t"
      "ds_read_b128 a[0:3], v56 offset:0\n\t"
      "v_mfma_f32_16x16x32_bf16 v[16:19], v[176:179], a[28:31], v[16:19]\n\t"
      "ds_read_b128 a[4:7], v57 offset:0\n\t"
      "v_mfma_f32_16x16x32_bf16 v[16:19], v[192:195], a[32:35], v[16:19]\n\t"
      "ds_read_b128 a[8:11], v58 offset:0\n\t"
      "v_mfma_f32_16x16x32_bf16 v[16:19], v[208:211], a[36:39], v[16:19]\n\t"
      "ds_read_b128 a[12:15], v59 offset:0\n\t"
      "v_mfma_f32_16x16x32_bf16 v[16:19], v[224:227], a[40:43], v[16:19]\n\t"
      "ds_read_b128 a[16:19], v56 offset:256\n\t"
      "v_mfma_f32_16x16x32_bf16 v[16:19], v[240:243], a[44:47], v[16:19]\n\t"
      "ds_read_b128 a[20:23], v57 offset:256\n\t"
      "v_mfma_f32_16x16x32_bf16 v[20:23], v[164:167], a[24:27], v[20:23]\n\t"
      "s_add_u32 s51, s51, 12288\n\t"
      "s_cmp_eq_u32 s51, 160256\n\t"
      "s_cselect_b32 s51, 25088, s51\n\t"
      "v_mfma_f32_16x16x32_bf16 v[20:23], v[180:183], a[28:31], v[20:23]\n\t"
      "s_waitcnt lgkmcnt(6)\n\t"
      "v_mfma_f32_16x16x32_bf16 v[20:23], v[196:199], a[32:35], v[20:23]\n\t"
      "buffer_store_dwordx4 a[56:59], %[stv], s[44:47], s49 offen offset:2048 sc1\n\t"
      "v_mfma_f32_16x16x32_bf16 v[20:23], v[212:215], a[36:39], v[20:23]\n\t"
      "s_add_u32 s49, s49, 3145728\n\t"
      "v_mfma_f32_16x16x32_bf16 v[20:23], v[228:231], a[40:43], v[20:23]\n\t"
      "s_waitcnt lgkmcnt(0)\n\t"
      "v_mfma_f32_16x16x32_bf16 v[20:23], v[244:247], a[44:47], v[20:23]\n\t"
      "v_mfma_f32_16x16x32_bf16 v[24:27], v[168:171], a[24:27], v[24:27]\n\t"
      "v_mfma_f32_16x16x32_bf16 v[24:27], v[184:187], a[28:31], v[24:27]\n\t"
      "v_cvt_pk_bf16_f32 v40, v16, v17\n\t"
      "v_cvt_pk_bf16_f32 v41, v18, v19\n\t"
      "v_mfma_f32_16x16x32_bf16 v[24:27], v[200:203], a[32:35], v[24:27]\n\t"
      "ds_write_b64 %[stw], v[40:41] offset:12544\n\t"
      "v_mfma_f32_16x16x32_bf16 v[24:27], v[216:219], a[36:39], v[24:27]\n\t"
      "buffer_load_dwordx4 %[vx1], s[40:43], s48 offen offset:1024 lds\n\t"
      "v_mfma_f32_16x16x32_bf16 v[24:27], v[232:235], a[40:43], v[24:27]\n\t"
      "v_mfma_f32_16x16x32_bf16 v[24:27], v[248:251], a[44:47], v[24:27]\n\t"
      "v_mfma_f32_16x16x32_bf16 v[28:31], v[172:175], a[24:27], v[28:31]\n\t"
      "v_mfma_f32_16x16x32_bf16 v[28:31], v[188:191], a[28:31], v[28:31]\n\t"
      "v_cvt_pk_bf16_f32 v42, v20, v21\n\t"
      "v_cvt_pk_bf16_f32 v43, v22, v23\n\t"
      "v_mfma_f32_16x16x32_bf16 v[28:31], v[204:207], a[32:35], v[28:31]\n\t"
      "ds_write_b64 %[stw], v[42:43] offset:12576\n\t"
      "v_mfma_f32_16x16x32_bf16 v[28:31], v[220:223], a[36:39], v[28:31]\n\t"
      "v_mfma_f32_16x16x32_bf16 v[28:31], v[236:239], a[40:43], v[28:31]\n\t"
      "v_mfma_f32_16x16x32_bf16 v[28:31], v[252:255], a[44:47], v[28:31]\n\t"
      "v_mfma_f32_16x16x32_bf16 v[32:35], a[108:111], a[24:27], v[32:35]\n\t"
      "v_mfma_f32_16x16x32_bf16 v[32:35], a[116:119], a[28:31], v[32:35]\n\t"
      "v_cvt_pk_bf16_f32 v44, v24, v25\n\t"
      "v_cvt_pk_bf16_f32 v45, v26, v27\n\t"
      "v_mfma_f32_16x16x32_bf16 v[32:35], a[124:127], a[32:35], v[32:35]\n\t"
      "ds_write_b64 %[stw], v[44:45] offset:12608\n\t"
      "v_mfma_f32_16x16x32_bf16 v[32:35], a[132:135], a[36:39], v[32:35]\n\t"
      "buffer_load_dwordx4 %[vx2], s[40:43], s48 offen offset:2048 lds\n\t"
      "v_mfma_f32_16x16x32_bf16 v[32:35], a[140:143], a[40:43], v[32:35]\n\t"
      "s_add_u32 s48, s48, 3145728\n\t"
      "s_add_u32 s50, s50, 12288\n\t"
      "v_mfma_f32_16x16x32_bf16 v[32:35], a[148:151], a[44:47], v[32:35]\n\t"
      "v_cvt_pk_bf16_f32 v46, v28, v29\n\t"
      "v_cvt_pk_bf16_f32 v47, v30, v31\n\t"
      "v_mfma_f32_16x16x32_bf16 v[36:39], a[112:115], a[24:27], v[36:39]\n\t"
      "ds_write_b64 %[stw], v[46:47] offset:12640\n\t"
      "v_mfma_f32_16x16x32_bf16 v[36:39], a[120:123], a[28:31], v[36:39]\n\t"
      "s_cmp_eq_u32 s50, 160256\n\t"
      "s_cselect_b32 s50, 25088, s50\n\t"
      "v_mfma_f32_16x16x32_bf16 v[36:39], a[128:131], a[32:35], v[36:39]\n\t"
      "s_sub_u32 s52, s52, 1\n\t"
      "s_cmp_lg_u32 s52, 0\n\t"
      "v_mfma_f32_16x16x32_bf16 v[36:39], a[136:139], a[36:39], v[36:39]\n\t"
      "v_mfma_f32_16x16x32_bf16 v[36:39], a[144:147], a[40:43], v[36:39]\n\t"
      "v_cvt_pk_bf16_f32 v48, v32, v33\n\t"
      "v_cvt_pk_bf16_f32 v49, v34, v35\n\t"
      "v_mfma_f32_16x16x32_bf16 v[36:39], a[152:155], a[44:47], v[36:39]\n\t"
      "ds_write_b64 %[stw], v[48:49] offset:12672\n\t"
      "s_cbranch_scc1 .Lloop1_%=\n\t"
      "s_mov_b32 s52, 2\n\t"
      ".Ldrain1_%=:\n\t"
      "v_mfma_f32_16x16x32_bf16 v[16:19], v[64:67], a[0:3], 0\n\t"
      "ds_read_b128 a[24:27], v58 offset:256\n\t"
      "v_mfma_f32_16x16x32_bf16 v[16:19], v[80:83], a[4:7], v[16:19]\n\t"
      "ds_read_b128 a[28:31], v59 offset:256\n\t"
      "v_mfma_f32_16x16x32_bf16 v[16:19], v[96:99], a[8:11], v[16:19]\n\t"
      "ds_read_b128 a[32:35], v56 offset:512\n\t"
      "v_mfma_f32_16x16x32_bf16 v[16:19], v[112:115], a[12:15], v[16:19]\n\t"
      "ds_read_b128 a[36:39], v57 offset:512\n\t"
      "v_mfma_f32_16x16x32_bf16 v[16:19], v[128:131], a[16:19], v[16:19]\n\t"
      "ds_read_b128 a[40:43], v58 offset:512\n\t"
      "v_mfma_f32_16x16x32_bf16 v[16:19], v[144:147], a[20:23], v[16:19]\n\t"
      "ds_read_b128 a[44:47], v59 offset:512\n\t"
      "v_mfma_f32_16x16x32_bf16 v[20:23], v[68:71], a[0:3], 0\n\t"
      "v_cvt_pk_bf16_f32 v50, v36, v37\n\t"
      "v_cvt_pk_bf16_f32 v51, v38, v39\n\t"
      "v_mfma_f32_16x16x32_bf16 v[20:23], v[84:87], a[4:7], v[20:23]\n\t"
      "ds_write_b64 %[stw], v[50:51] offset:12704\n\t"
      "v_mfma_f32_16x16x32_bf16 v[20:23], v[100:103], a[8:11], v[20:23]\n\t"
      "v_add_u32 v52, s51, %[fb0]\n\t"
      "v_xor_b32 v53, 64, v52\n\t"
      "v_mfma_f32_16x16x32_bf16 v[20:23], v[116:119], a[12:15], v[20:23]\n\t"
      "v_xor_b32 v54, 128, v52\n\t"
      "v_xor_b32 v55, 192, v52\n\t"
      "v_mfma_f32_16x16x32_bf16 v[20:23], v[132:135], a[16:19], v[20:23]\n\t"
      "v_mfma_f32_16x16x32_bf16 v[20:23], v[148:151], a[20:23], v[20:23]\n\t"
      "v_mfma_f32_16x16x32_bf16 v[24:27], v[72:75], a[0:3], 0\n\t"
      "v_mfma_f32_16x16x32_bf16 v[24:27], v[88:91], a[4:7], v[24:27]\n\t"
      "v_mfma_f32_16x16x32_bf16 v[24:27], v[104:107], a[8:11], v[24:27]\n\t"
      "v_mfma_f32_16x16x32_bf16 v[24:27], v[120:123], a[12:15], v[24:27]\n\t"
      "s_waitcnt vmcnt(6)\n\t"
      "s_waitcnt lgkmcnt(0)\n\t"
      "s_barrier\n\t"
      "v_mfma_f32_16x16x32_bf16 v[24:27], v[136:139], a[16:19], v[24:27]\n\t"
      "v_mfma_f32_16x16x32_bf16 v[24:27], v[152:155], a[20:23], v[24:27]\n\t"
      "v_mfma_f32_16x16x32_bf16 v[28:31], v[76:79], a[0:3], 0\n\t"
      "ds_read_b128 a[48:51], %[str0] offset:12544\n\t"
      "v_mfma_f32_16x16x32_bf16 v[28:31], v[92:95], a[4:7], v[28:31]\n\t"
      "ds_read_b128 a[52:55], %[str1] offset:13568\n\t"
      "v_mfma_f32_16x16x32_bf16 v[28:31], v[108:111], a[8:11], v[28:31]\n\t"
      "ds_read_b128 a[56:59], %[str2] offset:14592\n\t"
      "v_mfma_f32_16x16x32_bf16 v[28:31], v[124:127], a[12:15], v[28:31]\n\t"
      "v_mfma_f32_16x16x32_bf16 v[28:31], v[140:143], a[16:19], v[28:31]\n\t"
      "v_mfma_f32_16x16x32_bf16 v[28:31], v[156:159], a[20:23], v[28:31]\n\t"
      "v_mfma_f32_16x16x32_bf16 v[32:35], a[60:63], a[0:3], 0\n\t"
      "v_mfma_f32_16x16x32_bf16 v[32:35], a[68:71], a[4:7], v[32:35]\n\t"
      "s_waitcnt lgkmcnt(2)\n\t"
      "v_mfma_f32_16x16x32_bf16 v[32:35], a[76:79], a[8:11], v[32:35]\n\t"
      "buffer_store_dwordx4 a[48:51], %[stv], s[44:47], s49 offen sc1\n\t"
      "v_mfma_f32_16x16x32_bf16 v[32:35], a[84:87], a[12:15], v[32:35]\n\t"
      "v_mfma_f32_16x16x32_bf16 v[32:35], a[92:95], a[16:19], v[32:35]\n\t"
      "v_mfma_f32_16x16x32_bf16 v[32:35], a[100:103], a[20:23], v[32:35]\n\t"
      "v_mfma_f32_16x16x32_bf16 v[36:39], a[64:67], a[0:3], 0\n\t"
      "v_mfma_f32_16x16x32_bf16 v[36:39], a[72:75], a[4:7], v[36:39]\n\t"
      "v_mfma_f32_16x16x32_bf16 v[36:39], a[80:83], a[8:11], v[36:39]\n\t"
      "v_mfma_f32_16x16x32_bf16 v[36:39], a[88:91], a[12:15], v[36:39]\n\t"
      "s_waitcnt lgkmcnt(1)\n\t"
      "v_mfma_f32_16x16x32_bf16 v[36:39], a[96:99], a[16:19], v[36:39]\n\t"
      "buffer_store_dwordx4 a[52:55], %[stv], s[44:47], s49 offen offset:1024 sc1\n\t"
      "v_mfma_f32_16x16x32_bf16 v[36:39], a[104:107], a[20:23], v[36:39]\n\t"
      "v_mfma_f32_16x16x32_bf16 v[16:19], v[160:163], a[24:27], v[16:19]\n\t"
      "ds_read_b128 a[0:3], v52 offset:0\n\t"
      "v_mfma_f32_16x16x32_bf16 v[16:19], v[176:179], a[28:31], v[16:19]\n\t"
      "ds_read_b128 a[4:7], v53 offset:0\n\t"
      "v_mfma_f32_16x16x32_bf16 v[16:19], v[192:195], a[32:35], v[16:19]\n\t"
      "ds_read_b128 a[8:11], v54 offset:0\n\t"
      "v_mfma_f32_16x16x32_bf16 v[16:19], v[208:211], a[36:39], v[16:19]\n\t"
      "ds_read_b128 a[12:15], v55 offset:0\n\t"
      "v_mfma_f32_16x16x32_bf16 v[16:19], v[224:227], a[40:43], v[16:19]\n\t"
      "ds_read_b128 a[16:19], v52 offset:256\n\t"
      "v_mfma_f32_16x16x32_bf16 v[16:19], v[240:243], a[44:47], v[16:19]\n\t"
      "ds_read_b128 a[20:23], v53 offset:256\n\t"
      "v_mfma_f32_16x16x32_bf16 v[20:23], v[164:167], a[24:27], v[20:23]\n\t"
      "s_add_u32 s51, s51, 12288\n\t"
      "s_cmp_eq_u32 s51, 160256\n\t"
      "s_cselect_b32 s51, 25088, s51\n\t"
      "v_mfma_f32_16x16x32_bf16 v[20:23], v[180:183], a[28:31], v[20:23]\n\t"
      "s_waitcnt lgkmcnt(6)\n\t"
      "v_mfma_f32_16x16x32_bf16 v[20:23], v[196:199], a[32:35], v[20:23]\n\t"
      "buffer_store_dwordx4 a[56:59], %[stv], s[44:47], s49 offen offset:2048 sc1\n\t"
      "v_mfma_f32_16x16x32_bf16 v[20:23], v[212:215], a[36:39], v[20:23]\n\t"
      "s_add_u32 s49, s49, 3145728\n\t"
      "v_mfma_f32_16x16x32_bf16 v[20:23], v[228:231], a[40:43], v[20:23]\n\t"
      "s_waitcnt lgkmcnt(0)\n\t"
      "v_mfma_f32_16x16x32_bf16 v[20:23], v[244:247], a[44:47], v[20:23]\n\t"
      "v_mfma_f32_16x16x32_bf16 v[24:27], v[168:171], a[24:27], v[24:27]\n\t"
      "v_mfma_f32_16x16x32_bf16 v[24:27], v[184:187], a[28:31], v[24:27]\n\t"
      "v_cvt_pk_bf16_f32 v40, v16, v17\n\t"
      "v_cvt_pk_bf16_f32 v41, v18, v19\n\t"
      "v_mfma_f32_16x16x32_bf16 v[24:27], v[200:203], a[32:35], v[24:27]\n\t"
      "ds_write_b64 %[stw], v[40:41] offset:0\n\t"
      "v_mfma_f32_16x16x32_bf16 v[24:27], v[216:219], a[36:39], v[24:27]\n\t"
      "v_mfma_f32_16x16x32_bf16 v[24:27], v[232:235], a[40:43], v[24:27]\n\t"
      "v_mfma_f32_16x16x32_bf16 v[24:27], v[248:251], a[44:47], v[24:27]\n\t"
      "v_mfma_f32_16x16x32_bf16 v[28:31], v[172:175], a[24:27], v[28:31]\n\t"
      "v_mfma_f32_16x16x32_bf16 v[28:31], v[188:191], a[28:31], v[28:31]\n\t"
      "v_cvt_pk_bf16_f32 v42, v20, v21\n\t"
      "v_cvt_pk_bf16_f32 v43, v22, v23\n\t"
      "v_mfma_f32_16x16x32_bf16 v[28:31], v[204:207], a[32:35], v[28:31]\n\t"
      "ds_write_b64 %[stw], v[42:43] offset:32\n\t"
      "v_mfma_f32_16x16x32_bf16 v[28:31], v[220:223], a[36:39], v[28:31]\n\t"
      "v_mfma_f32_16x16x32_bf16 v[28:31], v[236:239], a[40:43], v[28:31]\n\t"
      "v_mfma_f32_16x16x32_bf16 v[28:31], v[252:255], a[44:47], v[28:31]\n\t"
      "v_mfma_f32_16x16x32_bf16 v[32:35], a[108:111], a[24:27], v[32:35]\n\t"
      "v_mfma_f32_16x16x32_bf16 v[32:35], a[116:119], a[28:31], v[32:35]\n\t"
      "v_cvt_pk_bf16_f32 v44, v24, v25\n\t"
      "v_cvt_pk_bf16_f32 v45, v26, v27\n\t"
      "v_mfma_f32_16x16x32_bf16 v[32:35], a[124:127], a[32:35], v[32:35]\n\t"
      "ds_write_b64 %[stw], v[44:45] offset:64\n\t"
      "v_mfma_f32_16x16x32_bf16 v[32:35], a[132:135], a[36:39], v[32:35]\n\t"
      "v_mfma_f32_16x16x32_bf16 v[32:35], a[140:143], a[40:43], v[32:35]\n\t"
      "v_mfma_f32_16x16x32_bf16 v[32:35], a[148:151], a[44:47], v[32:35]\n\t"
      "v_cvt_pk_bf16_f32 v46, v28, v29\n\t"
      "v_cvt_pk_bf16_f32 v47, v30, v31\n\t"
      "v_mfma_f32_16x16x32_bf16 v[36:39], a[112:115], a[24:27], v[36:39]\n\t"
      "ds_write_b64 %[stw], v[46:47] offset:96\n\t"
      "v_mfma_f32_16x16x32_bf16 v[36:39], a[120:123], a[28:31], v[36:39]\n\t"
      "v_mfma_f32_16x16x32_bf16 v[36:39], a[128:131], a[32:35], v[36:39]\n\t"
      "v_mfma_f32_16x16x32_bf16 v[36:39], a[136:139], a[36:39], v[36:39]\n\t"
      "v_mfma_f32_16x16x32_bf16 v[36:39], a[144:147], a[40:43], v[36:39]\n\t"
      "v_cvt_pk_bf16_f32 v48, v32, v33\n\t"
      "v_cvt_pk_bf16_f32 v49, v34, v35\n\t"
      "v_mfma_f32_16x16x32_bf16 v[36:39], a[152:155], a[44:47], v[36:39]\n\t"
      "ds_write_b64 %[stw], v[48:49] offset:128\n\t"
      "v_mfma_f32_16x16x32_bf16 v[16:19], v[64:67], a[0:3], 0\n\t"
      "ds_read_b128 a[24:27], v54 offset:256\n\t"
      "v_mfma_f32_16x16x32_bf16 v[16:19], v[80:83], a[4:7], v[16:19]\n\t"
      "ds_read_b128 a[28:31], v55 offset:256\n\t"
      "v_mfma_f32_16x16x32_bf16 v[16:19], v[96:99], a[8:11], v[16:19]\n\t"
      "ds_read_b128 a[32:35], v52 offset:512\n\t"
      "v_mfma_f32_16x16x32_bf16 v[16:19], v[112:115], a[12:15], v[16:19]\n\t"
      "ds_read_b128 a[36:39], v53 offset:512\n\t"
      "v_mfma_f32_16x16x32_bf16 v[16:19], v[128:131], a[16:19], v[16:19]\n\t"
      "ds_read_b128 a[40:43], v54 offset:512\n\t"
      "v_mfma_f32_16x16x32_bf16 v[16:19], v[144:147], a[20:23], v[16:19]\n\t"
      "ds_read_b128 a[44:47], v55 offset:512\n\t"
      "v_mfma_f32_16x16x32_bf16 v[20:23], v[68:71], a[0:3], 0\n\t"
      "v_cvt_pk_bf16_f32 v50, v36, v37\n\t"
      "v_cvt_pk_bf16_f32 v51, v38, v39\n\t"
      "v_mfma_f32_16x16x32_bf16 v[20:23], v[84:87], a[4:7], v[20:23]\n\t"
      "ds_write_b64 %[stw], v[50:51] offset:160\n\t"
      "v_mfma_f32_16x16x32_bf16 v[20:23], v[100:103], a[8:11], v[20:23]\n\t"
      "v_add_u32 v56, s51, %[fb0]\n\t"
      "v_xor_b32 v57, 64, v56\n\t"
      "v_mfma_f32_16x16x32_bf16 v[20:23], v[116:119], a[12:15], v[20:23]\n\t"
      "v_xor_b32 v58, 128, v56\n\t"
      "v_xor_b32 v59, 192, v56\n\t"
      "v_mfma_f32_16x16x32_bf16 v[20:23], v[132:135], a[16:19], v[20:23]\n\t"
      "v_mfma_f32_16x16x32_bf16 v[20:23], v[148:151], a[20:23], v[20:23]\n\t"
      "v_mfma_f32_16x16x32_bf16 v[24:27], v[72:75], a[0:3], 0\n\t"
      "v_mfma_f32_16x16x32_bf16 v[24:27], v[88:91], a[4:7], v[24:27]\n\t"
      "v_mfma_f32_16x16x32_bf16 v[24:27], v[104:107], a[8:11], v[24:27]\n\t"
      "v_mfma_f32_16x16x32_bf16 v[24:27], v[120:123], a[12:15], v[24:27]\n\t"
      "s_waitcnt vmcnt(6)\n\t"
      "s_waitcnt lgkmcnt(0)\n\t"
      "s_barrier\n\t"
      "v_mfma_f32_16x16x32_bf16 v[24:27], v[136:139], a[16:19], v[24:27]\n\t"
      "v_mfma_f32_16x16x32_bf16 v[24:27], v[152:155], a[20:23], v[24:27]\n\t"
      "v_mfma_f32_16x16x32_bf16 v[28:31], v[76:79], a[0:3], 0\n\t"
      "ds_read_b128 a[48:51], %[str0] offset:0\n\t"
      "v_mfma_f32_16x16x32_bf16 v[28:31], v[92:95], a[4:7], v[28:31]\n\t"
      "ds_read_b128 a[52:55], %[str1] offset:1024\n\t"
      "v_mfma_f32_16x16x32_bf16 v[28:31], v[108:111], a[8:11], v[28:31]\n\t"
      "ds_read_b128 a[56:59], %[str2] offset:2048\n\t"
      "v_mfma_f32_16x16x32_bf16 v[28:31], v[124:127], a[12:15], v[28:31]\n\t"
      "v_mfma_f32_16x16x32_bf16 v[28:31], v[140:143], a[16:19], v[28:31]\n\t"
      "v_mfma_f32_16x16x32_bf16 v[28:31], v[156:159], a[20:23], v[28:31]\n\t"
      "v_mfma_f32_16x16x32_bf16 v[32:35], a[60:63], a[0:3], 0\n\t"
      "v_mfma_f32_16x16x32_bf16 v[32:35], a[68:71], a[4:7], v[32:35]\n\t"
      "s_waitcnt lgkmcnt(2)\n\t"
      "v_mfma_f32_16x16x32_bf16 v[32:35], a[76:79], a[8:11], v[32:35]\n\t"
      "buffer_store_dwordx4 a[48:51], %[stv], s[44:47], s49 offen sc1\n\t"
      "v_mfma_f32_16x16x32_bf16 v[32:35], a[84:87], a[12:15], v[32:35]\n\t"
      "v_mfma_f32_16x16x32_bf16 v[32:35], a[92:95], a[16:19], v[32:35]\n\t"
      "v_mfma_f32_16x16x32_bf16 v[32:35], a[100:103], a[20:23], v[32:35]\n\t"
      "v_mfma_f32_16x16x32_bf16 v[36:39], a[64:67], a[0:3], 0\n\t"
      "v_mfma_f32_16x16x32_bf16 v[36:39], a[72:75], a[4:7], v[36:39]\n\t"
      "v_mfma_f32_16x16x32_bf16 v[36:39], a[80:83], a[8:11], v[36:39]\n\t"
      "v_mfma_f32_16x16x32_bf16 v[36:39], a[88:91], a[12:15], v[36:39]\n\t"
      "s_waitcnt lgkmcnt(1)\n\t"
      "v_mfma_f32_16x16x32_bf16 v[36:39], a[96:99], a[16:19], v[36:39]\n\t"
      "buffer_store_dwordx4 a[52:55], %[stv], s[44:47], s49 offen offset:1024 sc1\n\t"
      "v_mfma_f32_16x16x32_bf16 v[36:39], a[104:107], a[20:23], v[36:39]\n\t"
      "v_mfma_f32_16x16x32_bf16 v[16:19], v[160:163], a[24:27], v[16:19]\n\t"
      "ds_read_b128 a[0:3], v56 offset:0\n\t"
      "v_mfma_f32_16x16x32_bf16 v[16:19], v[176:179], a[28:31], v[16:19]\n\t"
      "ds_read_b128 a[4:7], v57 offset:0\n\t"
      "v_mfma_f32_16x16x32_bf16 v[16:19], v[192:195], a[32:35], v[16:19]\n\t"
      "ds_read_b128 a[8:11], v58 offset:0\n\t"
      "v_mfma_f32_16x16x32_bf16 v[16:19], v[208:211], a[36:39], v[16:19]\n\t"
      "ds_read_b128 a[12:15], v59 offset:0\n\t"
      "v_mfma_f32_16x16x32_bf16 v[16:19], v[224:227], a[40:43], v[16:19]\n\t"
      "ds_read_b128 a[16:19], v56 offset:256\n\t"
      "v_mfma_f32_16x16x32_bf16 v[16:19], v[240:243], a[44:47], v[16:19]\n\t"
      "ds_read_b128 a[20:23], v57 offset:256\n\t"
      "v_mfma_f32_16x16x32_bf16 v[20:23], v[164:167], a[24:27], v[20:23]\n\t"
      "s_add_u32 s51, s51, 12288\n\t"
      "s_cmp_eq_u32 s51, 160256\n\t"
      "s_cselect_b32 s51, 25088, s51\n\t"
      "v_mfma_f32_16x16x32_bf16 v[20:23], v[180:183], a[28:31], v[20:23]\n\t"
      "s_waitcnt lgkmcnt(6)\n\t"
      "v_mfma_f32_16x16x32_bf16 v[20:23], v[196:199], a[32:35], v[20:23]\n\t"
      "buffer_store_dwordx4 a[56:59], %[stv], s[44:47], s49 offen offset:2048 sc1\n\t"
      "v_mfma_f32_16x16x32_bf16 v[20:23], v[212:215], a[36:39], v[20:23]\n\t"
      "s_add_u32 s49, s49, 3145728\n\t"
      "v_mfma_f32_16x16x32_bf16 v[20:23], v[228:231], a[40:43], v[20:23]\n\t"
      "s_waitcnt lgkmcnt(0)\n\t"
      "v_mfma_f32_16x16x32_bf16 v[20:23], v[244:247], a[44:47], v[20:23]\n\t"
      "v_mfma_f32_16x16x32_bf16 v[24:27], v[168:171], a[24:27], v[24:27]\n\t"
      "v_mfma_f32_16x16x32_bf16 v[24:27], v[184:187], a[28:31], v[24:27]\n\t"
      "v_cvt_pk_bf16_f32 v40, v16, v17\n\t"
      "v_cvt_pk_bf16_f32 v41, v18, v19\n\t"
      "v_mfma_f32_16x16x32_bf16 v[24:27], v[200:203], a[32:35], v[24:27]\n\t"
      "ds_write_b64 %[stw], v[40:41] offset:12544\n\t"
      "v_mfma_f32_16x16x32_bf16 v[24:27], v[216:219], a[36:39], v[24:27]\n\t"
      "v_mfma_f32_16x16x32_bf16 v[24:27], v[232:235], a[40:43], v[24:27]\n\t"
      "v_mfma_f32_16x16x32_bf16 v[24:27], v[248:251], a[44:47], v[24:27]\n\t"
      "v_mfma_f32_16x16x32_bf16 v[28:31], v[172:175], a[24:27], v[28:31]\n\t"
      "v_mfma_f32_16x16x32_bf16 v[28:31], v[188:191], a[28:31], v[28:31]\n\t"
      "v_cvt_pk_bf16_f32 v42, v20, v21\n\t"
      "v_cvt_pk_bf16_f32 v43, v22, v23\n\t"
      "v_mfma_f32_16x16x32_bf16 v[28:31], v[204:207], a[32:35], v[28:31]\n\t"
      "ds_write_b64 %[stw], v[42:43] offset:12576\n\t"
      "v_mfma_f32_16x16x32_bf16 v[28:31], v[220:223], a[36:39], v[28:31]\n\t"
      "v_mfma_f32_16x16x32_bf16 v[28:31], v[236:239], a[40:43], v[28:31]\n\t"
      "v_mfma_f32_16x16x32_bf16 v[28:31], v[252:255], a[44:47], v[28:31]\n\t"
      "v_mfma_f32_16x16x32_bf16 v[32:35], a[108:111], a[24:27], v[32:35]\n\t"
      "v_mfma_f32_16x16x32_bf16 v[32:35], a[116:119], a[28:31], v[32:35]\n\t"
      "v_cvt_pk_bf16_f32 v44, v24, v25\n\t"
      "v_cvt_pk_bf16_f32 v45, v26, v27\n\t"
      "v_mfma_f32_16x16x32_bf16 v[32:35], a[124:127], a[32:35], v[32:35]\n\t"
      "ds_write_b64 %[stw], v[44:45] offset:12608\n\t"
      "v_mfma_f32_16x16x32_bf16 v[32:35], a[132:135], a[36:39], v[32:35]\n\t"
      "v_mfma_f32_16x16x32_bf16 v[32:35], a[140:143], a[40:43], v[32:35]\n\t"
      "v_mfma_f32_16x16x32_bf16 v[32:35], a[148:151], a[44:47], v[32:35]\n\t"
      "v_cvt_pk_bf16_f32 v46, v28, v29\n\t"
      "v_cvt_pk_bf16_f32 v47, v30, v31\n\t"
      "v_mfma_f32_16x16x32_bf16 v[36:39], a[112:115], a[24:27], v[36:39]\n\t"
      "ds_write_b64 %[stw], v[46:47] offset:12640\n\t"
      "v_mfma_f32_16x16x32_bf16 v[36:39], a[120:123], a[28:31], v[36:39]\n\t"
      "v_mfma_f32_16x16x32_bf16 v[36:39], a[128:131], a[32:35], v[36:39]\n\t"
      "s_sub_u32 s52, s52, 1\n\t"
      "s_cmp_lg_u32 s52, 0\n\t"
      "v_mfma_f32_16x16x32_bf16 v[36:39], a[136:139], a[36:39], v[36:39]\n\t"
      "v_mfma_f32_16x16x32_bf16 v[36:39], a[144:147], a[40:43], v[36:39]\n\t"
      "v_cvt_pk_bf16_f32 v48, v32, v33\n\t"
      "v_cvt_pk_bf16_f32 v49, v34, v35\n\t"
      "v_mfma_f32_16x16x32_bf16 v[36:39], a[152:155], a[44:47], v[36:39]\n\t"
      "ds_write_b64 %[stw], v[48:49] offset:12672\n\t"
      "s_cbranch_scc1 .Ldrain1_%=\n\t"
      "s_branch .Ltail_%=\n\t"
      ".Lloop2_%=:\n\t"
      "v_mfma_f32_16x16x32_bf16 v[16:19], v[64:67], a[0:3], 0\n\t"
      "ds_read_b128 a[24:27], v58 offset:256\n\t"
      "v_mfma_f32_16x16x32_bf16 v[16:19], v[80:83], a[4:7], v[16:19]\n\t"
      "ds_read_b128 a[28:31], v59 offset:256\n\t"
      "v_mfma_f32_16x16x32_bf16 v[16:19], v[96:99], a[8:11], v[16:19]\n\t"
      "ds_read_b128 a[32:35], v56 offset:512\n\t"
      "v_mfma_f32_16x16x32_bf16 v[16:19], v[112:115], a[12:15], v[16:19]\n\t"
      "ds_read_b128 a[36:39], v57 offset:512\n\t"
      "v_mfma_f32_16x16x32_bf16 v[16:19], v[128:131], a[16:19], v[16:19]\n\t"
      "ds_read_b128 a[40:43], v58 offset:512\n\t"
      "v_mfma_f32_16x16x32_bf16 v[16:19], v[144:147], a[20:23], v[16:19]\n\t"
      "ds_read_b128 a[44:47], v59 offset:512\n\t"
      "v_mfma_f32_16x16x32_bf16 v[20:23], v[68:71], a[0:3], 0\n\t"
      "v_cvt_pk_bf16_f32 v50, v36, v37\n\t"
      "v_cvt_pk_bf16_f32 v51, v38, v39\n\t"
      "v_mfma_f32_16x16x32_bf16 v[20:23], v[84:87], a[4:7], v[20:23]\n\t"
      "ds_write_b64 %[stw], v[50:51] offset:12704\n\t"
      "v_mfma_f32_16x16x32_bf16 v[20:23], v[100:103], a[8:11], v[20:23]\n\t"
      "v_add_u32 v52, s51, %[fb0]\n\t"
      "v_xor_b32 v53, 64, v52\n\t"
      "v_mfma_f32_16x16x32_bf16 v[20:23], v[116:119], a[12:15], v[20:23]\n\t"
      "v_xor_b32 v54, 128, v52\n\t"
      "v_xor_b32 v55, 192, v52\n\t"
      "v_mfma_f32_16x16x32_bf16 v[20:23], v[132:135], a[16:19], v[20:23]\n\t"
      "v_mfma_f32_16x16x32_bf16 v[20:23], v[148:151], a[20:23], v[20:23]\n\t"
      "v_mfma_f32_16x16x32_bf16 v[24:27], v[72:75], a[0:3], 0\n\t"
      "v_mfma_f32_16x16x32_bf16 v[24:27], v[88:91], a[4:7], v[24:27]\n\t"
      "v_mfma_f32_16x16x32_bf16 v[24:27], v[104:107], a[8:11], v[24:27]\n\t"
      "v_mfma_f32_16x16x32_bf16 v[24:27], v[120:123], a[12:15], v[24:27]\n\t"
      "v_mfma_f32_16x16x32_bf16 v[24:27], v[136:139], a[16:19], v[24:27]\n\t"
      "s_waitcnt vmcnt(12)\n\t"
      "s_waitcnt lgkmcnt(0)\n\t"
      "s_barrier\n\t"
      "v_mfma_f32_16x16x32_bf16 v[24:27], v[152:155], a[20:23], v[24:27]\n\t"
      "v_mfma_f32_16x16x32_bf16 v[28:31], v[76:79], a[0:3], 0\n\t"
      "ds_read_b128 a[48:51], %[str0] offset:12544\n\t"
      "v_mfma_f32_16x16x32_bf16 v[28:31], v[92:95], a[4:7], v[28:31]\n\t"
      "ds_read_b128 a[52:55], %[str1] offset:13568\n\t"
      "v_mfma_f32_16x16x32_bf16 v[28:31], v[108:111], a[8:11], v[28:31]\n\t"
      "ds_read_b128 a[56:59], %[str2] offset:14592\n\t"
      "v_mfma_f32_16x16x32_bf16 v[28:31], v[124:127], a[12:15], v[28:31]\n\t"
      "v_mfma_f32_16x16x32_bf16 v[28:31], v[140:143], a[16:19], v[28:31]\n\t"
      "v_mfma_f32_16x16x32_bf16 v[28:31], v[156:159], a[20:23], v[28:31]\n\t"
      "v_mfma_f32_16x16x32_bf16 v[32:35], a[60:63], a[0:3], 0\n\t"
      "v_mfma_f32_16x16x32_bf16 v[32:35], a[68:71], a[4:7], v[32:35]\n\t"
      "s_waitcnt lgkmcnt(2)\n\t"
      "v_mfma_f32_16x16x32_bf16 v[32:35], a[76:79], a[8:11], v[32:35]\n\t"
      "buffer_store_dwordx4 a[48:51], %[stv], s[44:47], s49 offen sc1\n\t"
      "v_mfma_f32_16x16x32_bf16 v[32:35], a[84:87], a[12:15], v[32:35]\n\t"
      "v_mfma_f32_16x16x32_bf16 v[32:35], a[92:95], a[16:19], v[32:35]\n\t"
      "v_mfma_f32_16x16x32_bf16 v[32:35], a[100:103], a[20:23], v[32:35]\n\t"
      "s_add_u32 m0, %[m0base], s50\n\t"
      "v_mfma_f32_16x16x32_bf16 v[36:39], a[64:67], a[0:3], 0\n\t"
      "buffer_load_dwordx4 %[vx0], s[40:43], s48 offen lds\n\t"
      "v_mfma_f32_16x16x32_bf16 v[36:39], a[72:75], a[4:7], v[36:39]\n\t"
      "v_mfma_f32_16x16x32_bf16 v[36:39], a[80:83], a[8:11], v[36:39]\n\t"
      "v_mfma_f32_16x16x32_bf16 v[36:39], a[88:91], a[12:15], v[36:39]\n\t"
      "s_waitcnt lgkmcnt(1)\n\t"
      "v_mfma_f32_16x16x32_bf16 v[36:39], a[96:99], a[16:19], v[36:39]\n\t"
      "buffer_store_dwordx4 a[52:55], %[stv], s[44:47], s49 offen offset:1024 sc1\n\t"
      "v_mfma_f32_16x16x32_bf16 v[36:39], a[104:107], a[20:23], v[36:39]\n\t"
      "v_mfma_f32_16x16x32_bf16 v[16:19], v[160:163], a[24:27], v[16:19]\n\t"
      "ds_read_b128 a[0:3], v52 offset:0\n\t"
      "v_mfma_f32_16x16x32_bf16 v[16:19], v[176:179], a[28:31], v[16:19]\n\t"
      "ds_read_b128 a[4:7], v53 offset:0\n\t"
      "v_mfma_f32_16x16x32_bf16 v[16:19], v[192:195], a[32:35], v[16:19]\n\t"
      "ds_read_b128 a[8:11], v54 offset:0\n\t"
      "v_mfma_f32_16x16x32_bf16 v[16:19], v[208:211], a[36:39], v[16:19]\n\t"
      "ds_read_b128 a[12:15], v55 offset:0\n\t"
      "v_mfma_f32_16x16x32_bf16 v[16:19], v[224:227], a[40:43], v[16:19]\n\t"
      "ds_read_b128 a[16:19], v52 offset:256\n\t"
      "v_mfma_f32_16x16x32_bf16 v[16:19], v[240:243], a[44:47], v[16:19]\n\t"
      "ds_read_b128 a[20:23], v53 offset:256\n\t"
      "v_mfma_f32_16x16x32_bf16 v[20:23], v[164:167], a[24:27], v[20:23]\n\t"
      "s_add_u32 s51, s51, 12288\n\t"
      "s_cmp_eq_u32 s51, 160256\n\t"
      "s_cselect_b32 s51, 25088, s51\n\t"
      "v_mfma_f32_16x16x32_bf16 v[20:23], v[180:183], a[28:31], v[20:23]\n\t"
      "s_waitcnt lgkmcnt(6)\n\t"
      "v_mfma_f32_16x16x32_bf16 v[20:23], v[196:199], a[32:35], v[20:23]\n\t"
      "buffer_store_dwordx4 a[56:59], %[stv], s[44:47], s49 offen offset:2048 sc1\n\t"
      "v_mfma_f32_16x16x32_bf16 v[20:23], v[212:215], a[36:39], v[20:23]\n\t"
      "s_add_u32 s49, s49, 3145728\n\t"
      "v_mfma_f32_16x16x32_bf16 v[20:23], v[228:231], a[40:43], v[20:23]\n\t"
      "s_waitcnt lgkmcnt(0)\n\t"
      "v_mfma_f32_16x16x32_bf16 v[20:23], v[244:247], a[44:47], v[20:23]\n\t"
      "v_mfma_f32_16x16x32_bf16 v[24:27], v[168:171], a[24:27], v[24:27]\n\t"
      "v_mfma_f32_16x16x32_bf16 v[24:27], v[184:187], a[28:31], v[24:27]\n\t"
      "v_cvt_pk_bf16_f32 v40, v16, v17\n\t"
      "v_cvt_pk_bf16_f32 v41, v18, v19\n\t"
      "v_mfma_f32_16x16x32_bf16 v[24:27], v[200:203], a[32:35], v[24:27]\n\t"
      "ds_write_b64 %[stw], v[40:41] offset:0\n\t"
      "v_mfma_f32_16x16x32_bf16 v[24:27], v[216:219], a[36:39], v[24:27]\n\t"
      "buffer_load_dwordx4 %[vx1], s[40:43], s48 offen offset:1024 lds\n\t"
      "v_mfma_f32_16x16x32_bf16 v[24:27], v[232:235], a[40:43], v[24:27]\n\t"
      "v_mfma_f32_16x16x32_bf16 v[24:27], v[248:251], a[44:47], v[24:27]\n\t"
      "v_mfma_f32_16x16x32_bf16 v[28:31], v[172:175], a[24:27], v[28:31]\n\t"
      "v_mfma_f32_16x16x32_bf16 v[28:31], v[188:191], a[28:31], v[28:31]\n\t"
      "v_cvt_pk_bf16_f32 v42, v20, v21\n\t"
      "v_cvt_pk_bf16_f32 v43, v22, v23\n\t"
      "v_mfma_f32_16x16x32_bf16 v[28:31], v[204:207], a[32:35], v[28:31]\n\t"
      "ds_write_b64 %[stw], v[42:43] offset:32\n\t"
      "v_mfma_f32_16x16x32_bf16 v[28:31], v[220:223], a[36:39], v[28:31]\n\t"
      "v_mfma_f32_16x16x32_bf16 v[28:31], v[236:239], a[40:43], v[28:31]\n\t"
      "v_mfma_f32_16x16x32_bf16 v[28:31], v[252:255], a[44:47], v[28:31]\n\t"
      "v_mfma_f32_16x16x32_bf16 v[32:35], a[108:111], a[24:27], v[32:35]\n\t"
      "v_mfma_f32_16x16x32_bf16 v[32:35], a[116:119], a[28:31], v[32:35]\n\t"
      "v_cvt_pk_bf16_f32 v44, v24, v25\n\t"
      "v_cvt_pk_bf16_f32 v45, v26, v27\n\t"
      "v_mfma_f32_16x16x32_bf16 v[32:35], a[124:127], a[32:35], v[32:35]\n\t"
      "ds_write_b64 %[stw], v[44:45] offset:64\n\t"
      "v_mfma_f32_16x16x32_bf16 v[32:35], a[132:135], a[36:39], v[32:35]\n\t"
      "buffer_load_dwordx4 %[vx2], s[40:43], s48 offen offset:2048 lds\n\t"
      "v_mfma_f32_16x16x32_bf16 v[32:35], a[140:143], a[40:43], v[32:35]\n\t"
      "s_add_u32 s48, s48, 3145728\n\t"
      "s_add_u32 s50, s50, 12288\n\t"
      "v_mfma_f32_16x16x32_bf16 v[32:35], a[148:151], a[44:47], v[32:35]\n\t"
      "v_cvt_pk_bf16_f32 v46, v28, v29\n\t"
      "v_cvt_pk_bf16_f32 v47, v30, v31\n\t"
      "v_mfma_f32_16x16x32_bf16 v[36:39], a[112:115], a[24:27], v[36:39]\n\t"
      "ds_write_b64 %[stw], v[46:47] offset:96\n\t"
      "v_mfma_f32_16x16x32_bf16 v[36:39], a[120:123], a[28:31], v[36:39]\n\t"
      "s_cmp_eq_u32 s50, 160256\n\t"
      "s_cselect_b32 s50, 25088, s50\n\t"
      "v_mfma_f32_16x16x32_bf16 v[36:39], a[128:131], a[32:35], v[36:39]\n\t"
      "v_mfma_f32_16x16x32_bf16 v[36:39], a[136:139], a[36:39], v[36:39]\n\t"
      "v_mfma_f32_16x16x32_bf16 v[36:39], a[144:147], a[40:43], v[36:39]\n\t"
      "v_cvt_pk_bf16_f32 v48, v32, v33\n\t"
      "v_cvt_pk_bf16_f32 v49, v34, v35\n\t"
      "v_mfma_f32_16x16x32_bf16 v[36:39], a[152:155], a[44:47], v[36:39]\n\t"
      "ds_write_b64 %[stw], v[48:49] offset:128\n\t"
      ".Lmid2_%=:\n\t"
      "v_mfma_f32_16x16x32_bf16 v[16:19], v[64:67], a[0:3], 0\n\t"
      "ds_read_b128 a[24:27], v54 offset:256\n\t"
      "v_mfma_f32_16x16x32_bf16 v[16:19], v[80:83], a[4:7], v[16:19]\n\t"
      "ds_read_b128 a[28:31], v55 offset:256\n\t"
      "v_mfma_f32_16x16x32_bf16 v[16:19], v[96:99], a[8:11], v[16:19]\n\t"
      "ds_read_b128 a[32:35], v52 offset:512\n\t"
      "v_mfma_f32_16x16x32_bf16 v[16:19], v[112:115], a[12:15], v[16:19]\n\t"
      "ds_read_b128 a[36:39], v53 offset:512\n\t"
      "v_mfma_f32_16x16x32_bf16 v[16:19], v[128:131], a[16:19], v[16:19]\n\t"
      "ds_read_b128 a[40:43], v54 offset:512\n\t"
      "v_mfma_f32_16x16x32_bf16 v[16:19], v[144:147], a[20:23], v[16:19]\n\t"
      "ds_read_b128 a[44:47], v55 offset:512\n\t"
      "v_mfma_f32_16x16x32_bf16 v[20:23], v[68:71], a[0:3], 0\n\t"
      "v_cvt_pk_bf16_f32 v50, v36, v37\n\t"
      "v_cvt_pk_bf16_f32 v51, v38, v39\n\t"
      "v_mfma_f32_16x16x32_bf16 v[20:23], v[84:87], a[4:7], v[20:23]\n\t"
      "ds_write_b64 %[stw], v[50:51] offset:160\n\t"
      "v_mfma_f32_16x16x32_bf16 v[20:23], v[100:103], a[8:11], v[20:23]\n\t"
      "v_add_u32 v56, s51, %[fb0]\n\t"
      "v_xor_b32 v57, 64, v56\n\t"
      "v_mfma_f32_16x16x32_bf16 v[20:23], v[116:119], a[12:15], v[20:23]\n\t"
      "v_xor_b32 v58, 128, v56\n\t"
      "v_xor_b32 v59, 192, v56\n\t"
      "v_mfma_f32_16x16x32_bf16 v[20:23], v[132:135], a[16:19], v[20:23]\n\t"
      "v_mfma_f32_16x16x32_bf16 v[20:23], v[148:151], a[20:23], v[20:23]\n\t"
      "v_mfma_f32_16x16x32_bf16 v[24:27], v[72:75], a[0:3], 0\n\t"
      "v_mfma_f32_16x16x32_bf16 v[24:27], v[88:91], a[4:7], v[24:27]\n\t"
      "v_mfma_f32_16x16x32_bf16 v[24:27], v[104:107], a[8:11], v[24:27]\n\t"
      "v_mfma_f32_16x16x32_bf16 v[24:27], v[120:123], a[12:15], v[24:27]\n\t"
      "v_mfma_f32_16x16x32_bf16 v[24:27], v[136:139], a[16:19], v[24:27]\n\t"
      "s_waitcnt vmcnt(12)\n\t"
      "s_waitcnt lgkmcnt(0)\n\t"
      "s_barrier\n\t"
      "v_mfma_f32_16x16x32_bf16 v[24:27], v[152:155], a[20:23], v[24:27]\n\t"
      "v_mfma_f32_16x16x32_bf16 v[28:31], v[76:79], a[0:3], 0\n\t"
      "ds_read_b128 a[48:51], %[str0] offset:0\n\t"
      "v_mfma_f32_16x16x32_bf16 v[28:31], v[92:95], a[4:7], v[28:31]\n\t"
      "ds_read_b128 a[52:55], %[str1] offset:1024\n\t"
      "v_mfma_f32_16x16x32_bf16 v[28:31], v[108:111], a[8:11], v[28:31]\n\t"
      "ds_read_b128 a[56:59], %[str2] offset:2048\n\t"
      "v_mfma_f32_16x16x32_bf16 v[28:31], v[124:127], a[12:15], v[28:31]\n\t"
      "v_mfma_f32_16x16x32_bf16 v[28:31], v[140:143], a[16:19], v[28:31]\n\t"
      "v_mfma_f32_16x16x32_bf16 v[28:31], v[156:159], a[20:23], v[28:31]\n\t"
      "v_mfma_f32_16x16x32_bf16 v[32:35], a[60:63], a[0:3], 0\n\t"
      "v_mfma_f32_16x16x32_bf16 v[32:35], a[68:71], a[4:7], v[32:35]\n\t"
      "s_waitcnt lgkmcnt(2)\n\t"
      "v_mfma_f32_16x16x32_bf16 v[32:35], a[76:79], a[8:11], v[32:35]\n\t"
      "buffer_store_dwordx4 a[48:51], %[stv], s[44:47], s49 offen sc1\n\t"
      "v_mfma_f32_16x16x32_bf16 v[32:35], a[84:87], a[12:15], v[32:35]\n\t"
      "v_mfma_f32_16x16x32_bf16 v[32:35], a[92:95], a[16:19], v[32:35]\n\t"
      "v_mfma_f32_16x16x32_bf16 v[32:35], a[100:103], a[20:23], v[32:35]\n\t"
      "s_add_u32 m0, %[m0base], s50\n\t"
      "v_mfma_f32_16x16x32_bf16 v[36:39], a[64:67], a[0:3], 0\n\t"
      "buffer_load_dwordx4 %[vx0], s[40:43], s48 offen lds\n\t"
      "v_mfma_f32_16x16x32_bf16 v[36:39], a[72:75], a[4:7], v[36:39]\n\t"
      "v_mfma_f32_16x16x32_bf16 v[36:39], a[80:83], a[8:11], v[36:39]\n\t"
      "v_mfma_f32_16x16x32_bf16 v[36:39], a[88:91], a[12:15], v[36:39]\n\t"
      "s_waitcnt lgkmcnt(1)\n\t"
      "v_mfma_f32_16x16x32_bf16 v[36:39], a[96:99], a[16:19], v[36:39]\n\t"
      "buffer_store_dwordx4 a[52:55], %[stv], s[44:47], s49 offen offset:1024 sc1\n\t"
      "v_mfma_f32_16x16x32_bf16 v[36:39], a[104:107], a[20:23], v[36:39]\n\t"
      "v_mfma_f32_16x16x32_bf16 v[16:19], v[160:163], a[24:27], v[16:19]\n\t"
      "ds_read_b128 a[0:3], v56 offset:0\n\t"
      "v_mfma_f32_16x16x32_bf16 v[16:19], v[176:179], a[28:31], v[16:19]\n\t"
      "ds_read_b128 a[4:7], v57 offset:0\n\t"
      "v_mfma_f32_16x16x32_bf16 v[16:19], v[192:195], a[32:35], v[16:19]\n\t"
      "ds_read_b128 a[8:11], v58 offset:0\n\t"
      "v_mfma_f32_16x16x32_bf16 v[16:19], v[208:211], a[36:39], v[16:19]\n\t"
      "ds_read_b128 a[12:15], v59 offset:0\n\t"
      "v_mfma_f32_16x16x32_bf16 v[16:19], v[224:227], a[40:43], v[16:19]\n\t"
      "ds_read_b128 a[16:19], v56 offset:256\n\t"
      "v_mfma_f32_16x16x32_bf16 v[16:19], v[240:243], a[44:47], v[16:19]\n\t"
      "ds_read_b128 a[20:23], v57 offset:256\n\t"
      "v_mfma_f32_16x16x32_bf16 v[20:23], v[164:167], a[24:27], v[20:23]\n\t"
      "s_add_u32 s51, s51, 12288\n\t"
      "s_cmp_eq_u32 s51, 160256\n\t"
      "s_cselect_b32 s51, 25088, s51\n\t"
      "v_mfma_f32_16x16x32_bf16 v[20:23], v[180:183], a[28:31], v[20:23]\n\t"
      "s_waitcnt lgkmcnt(6)\n\t"
      "v_mfma_f32_16x16x32_bf16 v[20:23], v[196:199], a[32:35], v[20:23]\n\t"
      "buffer_store_dwordx4 a[56:59], %[stv], s[44:47], s49 offen offset:2048 sc1\n\t"
      "v_mfma_f32_16x16x32_bf16 v[20:23], v[212:215], a[36:39], v[20:23]\n\t"
      "s_add_u32 s49, s49, 3145728\n\t"
      "v_mfma_f32_16x16x32_bf16 v[20:23], v[228:231], a[40:43], v[20:23]\n\t"
      "s_waitcnt lgkmcnt(0)\n\t"
      "v_mfma_f32_16x16x32_bf16 v[20:23], v[244:247], a[44:47], v[20:23]\n\t"
      "v_mfma_f32_16x16x32_bf16 v[24:27], v[168:171], a[24:27], v[24:27]\n\t"
      "v_mfma_f32_16x16x32_bf16 v[24:27], v[184:187], a[28:31], v[24:27]\n\t"
      "v_cvt_pk_bf16_f32 v40, v16, v17\n\t"
      "v_cvt_pk_bf16_f32 v41, v18, v19\n\t"
      "v_mfma_f32_16x16x32_bf16 v[24:27], v[200:203], a[32:35], v[24:27]\n\t"
      "ds_write_b64 %[stw], v[40:41] offset:12544\n\t"
      "v_mfma_f32_16x16x32_bf16 v[24:27], v[216:219], a[36:39], v[24:27]\n\t"
      "buffer_load_dwordx4 %[vx1], s[40:43], s48 offen offset:1024 lds\n\t"
      "v_mfma_f32_16x16x32_bf16 v[24:27], v[232:235], a[40:43], v[24:27]\n\t"
      "v_mfma_f32_16x16x32_bf16 v[24:27], v[248:251], a[44:47], v[24:27]\n\t"
      "v_mfma_f32_16x16x32_bf16 v[28:31], v[172:175], a[24:27], v[28:31]\n\t"
      "v_mfma_f32_16x16x32_bf16 v[28:31], v[188:191], a[28:31], v[28:31]\n\t"
      "v_cvt_pk_bf16_f32 v42, v20, v21\n\t"
      "v_cvt_pk_bf16_f32 v43, v22, v23\n\t"
      "v_mfma_f32_16x16x32_bf16 v[28:31], v[204:207], a[32:35], v[28:31]\n\t"
      "ds_write_b64 %[stw], v[42:43] offset:12576\n\t"
      "v_mfma_f32_16x16x32_bf16 v[28:31], v[220:223], a[36:39], v[28:31]\n\t"
      "v_mfma_f32_16x16x32_bf16 v[28:31], v[236:239], a[40:43], v[28:31]\n\t"
      "v_mfma_f32_16x16x32_bf16 v[28:31], v[252:255], a[44:47], v[28:31]\n\t"
      "v_mfma_f32_16x16x32_bf16 v[32:35], a[108:111], a[24:27], v[32:35]\n\t"
      "v_mfma_f32_16x16x32_bf16 v[32:35], a[116:119], a[28:31], v[32:35]\n\t"
      "v_cvt_pk_bf16_f32 v44, v24, v25\n\t"
      "v_cvt_pk_bf16_f32 v45, v26, v27\n\t"
      "v_mfma_f32_16x16x32_bf16 v[32:35], a[124:127], a[32:35], v[32:35]\n\t"
      "ds_write_b64 %[stw], v[44:45] offset:12608\n\t"
      "v_mfma_f32_16x16x32_bf16 v[32:35], a[132:135], a[36:39], v[32:35]\n\t"
      "buffer_load_dwordx4 %[vx2], s[40:43], s48 offen offset:2048 lds\n\t"
      "v_mfma_f32_16x16x32_bf16 v[32:35], a[140:143], a[40:43], v[32:35]\n\t"
      "s_add_u32 s48, s48, 3145728\n\t"
      "s_add_u32 s50, s50, 12288\n\t"
      "v_mfma_f32_16x16x32_bf16 v[32:35], a[148:151], a[44:47], v[32:35]\n\t"
      "v_cvt_pk_bf16_f32 v46, v28, v29\n\t"
      "v_cvt_pk_bf16_f32 v47, v30, v31\n\t"
      "v_mfma_f32_16x16x32_bf16 v[36:39], a[112:115], a[24:27], v[36:39]\n\t"
      "ds_write_b64 %[stw], v[46:47] offset:12640\n\t"
      "v_mfma_f32_16x16x32_bf16 v[36:39], a[120:123], a[28:31], v[36:39]\n\t"
      "s_cmp_eq_u32 s50, 160256\n\t"
      "s_cselect_b32 s50, 25088, s50\n\t"
      "v_mfma_f32_16x16x32_bf16 v[36:39], a[128:131], a[32:35], v[36:39]\n\t"
      "s_sub_u32 s52, s52, 1\n\t"
      "s_cmp_lg_u32 s52, 0\n\t"
      "v_mfma_f32_16x16x32_bf16 v[36:39], a[136:139], a[36:39], v[36:39]\n\t"
      "v_mfma_f32_16x16x32_bf16 v[36:39], a[144:147], a[40:43], v[36:39]\n\t"
      "v_cvt_pk_bf16_f32 v48, v32, v33\n\t"
      "v_cvt_pk_bf16_f32 v49, v34, v35\n\t"
      "v_mfma_f32_16x16x32_bf16 v[36:39], a[152:155], a[44:47], v[36:39]\n\t"
      "ds_write_b64 %[stw], v[48:49] offset:12672\n\t"
      "s_cbranch_scc1 .Lloop2_%=\n\t"
      "s_mov_b32 s52, 2\n\t"
      ".Ldrain2_%=:\n\t"
      "v_mfma_f32_16x16x32_bf16 v[16:19], v[64:67], a[0:3], 0\n\t"
      "ds_read_b128 a[24:27], v58 offset:256\n\t"
      "v_mfma_f32_16x16x32_bf16 v[16:19], v[80:83], a[4:7], v[16:19]\n\t"
      "ds_read_b128 a[28:31], v59 offset:256\n\t"
      "v_mfma_f32_16x16x32_bf16 v[16:19], v[96:99], a[8:11], v[16:19]\n\t"
      "ds_read_b128 a[32:35], v56 offset:512\n\t"
      "v_mfma_f32_16x16x32_bf16 v[16:19], v[112:115], a[12:15], v[16:19]\n\t"
      "ds_read_b128 a[36:39], v57 offset:512\n\t"
      "v_mfma_f32_16x16x32_bf16 v[16:19], v[128:131], a[16:19], v[16:19]\n\t"
      "ds_read_b128 a[40:43], v58 offset:512\n\t"
      "v_mfma_f32_16x16x32_bf16 v[16:19], v[144:147], a[20:23], v[16:19]\n\t"
      "ds_read_b128 a[44:47], v59 offset:512\n\t"
      "v_mfma_f32_16x16x32_bf16 v[20:23], v[68:71], a[0:3], 0\n\t"
      "v_cvt_pk_bf16_f32 v50, v36, v37\n\t"
      "v_cvt_pk_bf16_f32 v51, v38, v39\n\t"
      "v_mfma_f32_16x16x32_bf16 v[20:23], v[84:87], a[4:7], v[20:23]\n\t"
      "ds_write_b64 %[stw], v[50:51] offset:12704\n\t"
      "v_mfma_f32_16x16x32_bf16 v[20:23], v[100:103], a[8:11], v[20:23]\n\t"
      "v_add_u32 v52, s51, %[fb0]\n\t"
      "v_xor_b32 v53, 64, v52\n\t"
      "v_mfma_f32_16x16x32_bf16 v[20:23], v[116:119], a[12:15], v[20:23]\n\t"
      "v_xor_b32 v54, 128, v52\n\t"
      "v_xor_b32 v55, 192, v52\n\t"
      "v_mfma_f32_16x16x32_bf16 v[20:23], v[132:135], a[16:19], v[20:23]\n\t"
      "v_mfma_f32_16x16x32_bf16 v[20:23], v[148:151], a[20:23], v[20:23]\n\t"
      "v_mfma_f32_16x16x32_bf16 v[24:27], v[72:75], a[0:3], 0\n\t"
      "v_mfma_f32_16x16x32_bf16 v[24:27], v[88:91], a[4:7], v[24:27]\n\t"
      "v_mfma_f32_16x16x32_bf16 v[24:27], v[104:107], a[8:11], v[24:27]\n\t"
      "v_mfma_f32_16x16x32_bf16 v[24:27], v[120:123], a[12:15], v[24:27]\n\t"
      "v_mfma_f32_16x16x32_bf16 v[24:27], v[136:139], a[16:19], v[24:27]\n\t"
      "s_waitcnt vmcnt(6)\n\t"
      "s_waitcnt lgkmcnt(0)\n\t"
      "s_barrier\n\t"
      "v_mfma_f32_16x16x32_bf16 v[24:27], v[152:155], a[20:23], v[24:27]\n\t"
      "v_mfma_f32_16x16x32_bf16 v[28:31], v[76:79], a[0:3], 0\n\t"
      "ds_read_b128 a[48:51], %[str0] offset:12544\n\t"
      "v_mfma_f32_16x16x32_bf16 v[28:31], v[92:95], a[4:7], v[28:31]\n\t"
      "ds_read_b128 a[52:55], %[str1] offset:13568\n\t"
      "v_mfma_f32_16x16x32_bf16 v[28:31], v[108:111], a[8:11], v[28:31]\n\t"
      "ds_read_b128 a[56:59], %[str2] offset:14592\n\t"
      "v_mfma_f32_16x16x32_bf16 v[28:31], v[124:127], a[12:15], v[28:31]\n\t"
      "v_mfma_f32_16x16x32_bf16 v[28:31], v[140:143], a[16:19], v[28:31]\n\t"
      "v_mfma_f32_16x16x32_bf16 v[28:31], v[156:159], a[20:23], v[28:31]\n\t"
      "v_mfma_f32_16x16x32_bf16 v[32:35], a[60:63], a[0:3], 0\n\t"
      "v_mfma_f32_16x16x32_bf16 v[32:35], a[68:71], a[4:7], v[32:35]\n\t"
      "s_waitcnt lgkmcnt(2)\n\t"
      "v_mfma_f32_16x16x32_bf16 v[32:35], a[76:79], a[8:11], v[32:35]\n\t"
      "buffer_store_dwordx4 a[48:51], %[stv], s[44:47], s49 offen sc1\n\t"
      "v_mfma_f32_16x16x32_bf16 v[32:35], a[84:87], a[12:15], v[32:35]\n\t"
      "v_mfma_f32_16x16x32_bf16 v[32:35], a[92:95], a[16:19], v[32:35]\n\t"
      "v_mfma_f32_16x16x32_bf16 v[32:35], a[100:103], a[20:23], v[32:35]\n\t"
      "v_mfma_f32_16x16x32_bf16 v[36:39], a[64:67], a[0:3], 0\n\t"
      "v_mfma_f32_16x16x32_bf16 v[36:39], a[72:75], a[4:7], v[36:39]\n\t"
      "v_mfma_f32_16x16x32_bf16 v[36:39], a[80:83], a[8:11], v[36:39]\n\t"
      "v_mfma_f32_16x16x32_bf16 v[36:39], a[88:91], a[12:15], v[36:39]\n\t"
      "s_waitcnt lgkmcnt(1)\n\t"
      "v_mfma_f32_16x16x32_bf16 v[36:39], a[96:99], a[16:19], v[36:39]\n\t"
      "buffer_store_dwordx4 a[52:55], %[stv], s[44:47], s49 offen offset:1024 sc1\n\t"
      "v_mfma_f32_16x16x32_bf16 v[36:39], a[104:107], a[20:23], v[36:39]\n\t"
      "v_mfma_f32_16x16x32_bf16 v[16:19], v[160:163], a[24:27], v[16:19]\n\t"
      "ds_read_b128 a[0:3], v52 offset:0\n\t"
      "v_mfma_f32_16x16x32_bf16 v[16:19], v[176:179], a[28:31], v[16:19]\n\t"
      "ds_read_b128 a[4:7], v53 offset:0\n\t"
      "v_mfma_f32_16x16x32_bf16 v[16:19], v[192:195], a[32:35], v[16:19]\n\t"
      "ds_read_b128 a[8:11], v54 offset:0\n\t"
      "v_mfma_f32_16x16x32_bf16 v[16:19], v[208:211], a[36:39], v[16:19]\n\t"
      "ds_read_b128 a[12:15], v55 offset:0\n\t"
      "v_mfma_f32_16x16x32_bf16 v[16:19], v[224:227], a[40:43], v[16:19]\n\t"
      "ds_read_b128 a[16:19], v52 offset:256\n\t"
      "v_mfma_f32_16x16x32_bf16 v[16:19], v[240:243], a[44:47], v[16:19]\n\t"
      "ds_read_b128 a[20:23], v53 offset:256\n\t"
      "v_mfma_f32_16x16x32_bf16 v[20:23], v[164:167], a[24:27], v[20:23]\n\t"
      "s_add_u32 s51, s51, 12288\n\t"
      "s_cmp_eq_u32 s51, 160256\n\t"
      "s_cselect_b32 s51, 25088, s51\n\t"
      "v_mfma_f32_16x16x32_bf16 v[20:23], v[180:183], a[28:31], v[20:23]\n\t"
      "s_waitcnt lgkmcnt(6)\n\t"
      "v_mfma_f32_16x16x32_bf16 v[20:23], v[196:199], a[32:35], v[20:23]\n\t"
      "buffer_store_dwordx4 a[56:59], %[stv], s[44:47], s49 offen offset:2048 sc1\n\t"
      "v_mfma_f32_16x16x32_bf16 v[20:23], v[212:215], a[36:39], v[20:23]\n\t"
      "s_add_u32 s49, s49, 3145728\n\t"
      "v_mfma_f32_16x16x32_bf16 v[20:23], v[228:231], a[40:43], v[20:23]\n\t"
      "s_waitcnt lgkmcnt(0)\n\t"
      "v_mfma_f32_16x16x32_bf16 v[20:23], v[244:247], a[44:47], v[20:23]\n\t"
      "v_mfma_f32_16x16x32_bf16 v[24:27], v[168:171], a[24:27], v[24:27]\n\t"
      "v_mfma_f32_16x16x32_bf16 v[24:27], v[184:187], a[28:31], v[24:27]\n\t"
      "v_cvt_pk_bf16_f32 v40, v16, v17\n\t"
      "v_cvt_pk_bf16_f32 v41, v18, v19\n\t"
      "v_mfma_f32_16x16x32_bf16 v[24:27], v[200:203], a[32:35], v[24:27]\n\t"
      "ds_write_b64 %[stw], v[40:41] offset:0\n\t"
      "v_mfma_f32_16x16x32_bf16 v[24:27], v[216:219], a[36:39], v[24:27]\n\t"
      "v_mfma_f32_16x16x32_bf16 v[24:27], v[232:235], a[40:43], v[24:27]\n\t"
      "v_mfma_f32_16x16x32_bf16 v[24:27], v[248:251], a[44:47], v[24:27]\n\t"
      "v_mfma_f32_16x16x32_bf16 v[28:31], v[172:175], a[24:27], v[28:31]\n\t"
      "v_mfma_f32_16x16x32_bf16 v[28:31], v[188:191], a[28:31], v[28:31]\n\t"
      "v_cvt_pk_bf16_f32 v42, v20, v21\n\t"
      "v_cvt_pk_bf16_f32 v43, v22, v23\n\t"
      "v_mfma_f32_16x16x32_bf16 v[28:31], v[204:207], a[32:35], v[28:31]\n\t"
      "ds_write_b64 %[stw], v[42:43] offset:32\n\t"
      "v_mfma_f32_16x16x32_bf16 v[28:31], v[220:223], a[36:39], v[28:31]\n\t"
      "v_mfma_f32_16x16x32_bf16 v[28:31], v[236:239], a[40:43], v[28:31]\n\t"
      "v_mfma_f32_16x16x32_bf16 v[28:31], v[252:255], a[44:47], v[28:31]\n\t"
      "v_mfma_f32_16x16x32_bf16 v[32:35], a[108:111], a[24:27], v[32:35]\n\t"
      "v_mfma_f32_16x16x32_bf16 v[32:35], a[116:119], a[28:31], v[32:35]\n\t"
      "v_cvt_pk_bf16_f32 v44, v24, v25\n\t"
      "v_cvt_pk_bf16_f32 v45, v26, v27\n\t"
      "v_mfma_f32_16x16x32_bf16 v[32:35], a[124:127], a[32:35], v[32:35]\n\t"
      "ds_write_b64 %[stw], v[44:45] offset:64\n\t"
      "v_mfma_f32_16x16x32_bf16 v[32:35], a[132:135], a[36:39], v[32:35]\n\t"
      "v_mfma_f32_16x16x32_bf16 v[32:35], a[140:143], a[40:43], v[32:35]\n\t"
      "v_mfma_f32_16x16x32_bf16 v[32:35], a[148:151], a[44:47], v[32:35]\n\t"
      "v_cvt_pk_bf16_f32 v46, v28, v29\n\t"
      "v_cvt_pk_bf16_f32 v47, v30, v31\n\t"
      "v_mfma_f32_16x16x32_bf16 v[36:39], a[112:115], a[24:27], v[36:39]\n\t"
      "ds_write_b64 %[stw], v[46:47] offset:96\n\t"
      "v_mfma_f32_16x16x32_bf16 v[36:39], a[120:123], a[28:31], v[36:39]\n\t"
      "v_mfma_f32_16x16x32_bf16 v[36:39], a[128:131], a[32:35], v[36:39]\n\t"
      "v_mfma_f32_16x16x32_bf16 v[36:39], a[136:139], a[36:39], v[36:39]\n\t"
      "v_mfma_f32_16x16x32_bf16 v[36:39], a[144:147], a[40:43], v[36:39]\n\t"
      "v_cvt_pk_bf16_f32 v48, v32, v33\n\t"
      "v_cvt_pk_bf16_f32 v49, v34, v35\n\t"
      "v_mfma_f32_16x16x32_bf16 v[36:39], a[152:155], a[44:47], v[36:39]\n\t"
      "ds_write_b64 %[stw], v[48:49] offset:128\n\t"
      "v_mfma_f32_16x16x32_bf16 v[16:19], v[64:67], a[0:3], 0\n\t"
      "ds_read_b128 a[24:27], v54 offset:256\n\t"
      "v_mfma_f32_16x16x32_bf16 v[16:19], v[80:83], a[4:7], v[16:19]\n\t"
      "ds_read_b128 a[28:31], v55 offset:256\n\t"
      "v_mfma_f32_16x16x32_bf16 v[16:19], v[96:99], a[8:11], v[16:19]\n\t"
      "ds_read_b128 a[32:35], v52 offset:512\n\t"
      "v_mfma_f32_16x16x32_bf16 v[16:19], v[112:115], a[12:15], v[16:19]\n\t"
      "ds_read_b128 a[36:39], v53 offset:512\n\t"
      "v_mfma_f32_16x16x32_bf16 v[16:19], v[128:131], a[16:19], v[16:19]\n\t"
      "ds_read_b128 a[40:43], v54 offset:512\n\t"
      "v_mfma_f32_16x16x32_bf16 v[16:19], v[144:147], a[20:23], v[16:19]\n\t"
      "ds_read_b128 a[44:47], v55 offset:512\n\t"
      "v_mfma_f32_16x16x32_bf16 v[20:23], v[68:71], a[0:3], 0\n\t"
      "v_cvt_pk_bf16_f32 v50, v36, v37\n\t"
      "v_cvt_pk_bf16_f32 v51, v38, v39\n\t"
      "v_mfma_f32_16x16x32_bf16 v[20:23], v[84:87], a[4:7], v[20:23]\n\t"
      "ds_write_b64 %[stw], v[50:51] offset:160\n\t"
      "v_mfma_f32_16x16x32_bf16 v[20:23], v[100:103], a[8:11], v[20:23]\n\t"
      "v_add_u32 v56, s51, %[fb0]\n\t"
      "v_xor_b32 v57, 64, v56\n\t"
      "v_mfma_f32_16x16x32_bf16 v[20:23], v[116:119], a[12:15], v[20:23]\n\t"
      "v_xor_b32 v58, 128, v56\n\t"
      "v_xor_b32 v59, 192, v56\n\t"
      "v_mfma_f32_16x16x32_bf16 v[20:23], v[132:135], a[16:19], v[20:23]\n\t"
      "v_mfma_f32_16x16x32_bf16 v[20:23], v[148:151], a[20:23], v[20:23]\n\t"
      "v_mfma_f32_16x16x32_bf16 v[24:27], v[72:75], a[0:3], 0\n\t"
      "v_mfma_f32_16x16x32_bf16 v[24:27], v[88:91], a[4:7], v[24:27]\n\t"
      "v_mfma_f32_16x16x32_bf16 v[24:27], v[104:107], a[8:11], v[24:27]\n\t"
      "v_mfma_f32_16x16x32_bf16 v[24:27], v[120:123], a[12:15], v[24:27]\n\t"
      "v_mfma_f32_16x16x32_bf16 v[24:27], v[136:139], a[16:19], v[24:27]\n\t"
      "s_waitcnt vmcnt(6)\n\t"
      "s_waitcnt lgkmcnt(0)\n\t"
      "s_barrier\n\t"
      "v_mfma_f32_16x16x32_bf16 v[24:27], v[152:155], a[20:23], v[24:27]\n\t"
      "v_mfma_f32_16x16x32_bf16 v[28:31], v[76:79], a[0:3], 0\n\t"
      "ds_read_b128 a[48:51], %[str0] offset:0\n\t"
      "v_mfma_f32_16x16x32_bf16 v[28:31], v[92:95], a[4:7], v[28:31]\n\t"
      "ds_read_b128 a[52:55], %[str1] offset:1024\n\t"
      "v_mfma_f32_16x16x32_bf16 v[28:31], v[108:111], a[8:11], v[28:31]\n\t"
      "ds_read_b128 a[56:59], %[str2] offset:2048\n\t"
      "v_mfma_f32_16x16x32_bf16 v[28:31], v[124:127], a[12:15], v[28:31]\n\t"
      "v_mfma_f32_16x16x32_bf16 v[28:31], v[140:143], a[16:19], v[28:31]\n\t"
      "v_mfma_f32_16x16x32_bf16 v[28:31], v[156:159], a[20:23], v[28:31]\n\t"
      "v_mfma_f32_16x16x32_bf16 v[32:35], a[60:63], a[0:3], 0\n\t"
      "v_mfma_f32_16x16x32_bf16 v[32:35], a[68:71], a[4:7], v[32:35]\n\t"
      "s_waitcnt lgkmcnt(2)\n\t"
      "v_mfma_f32_16x16x32_bf16 v[32:35], a[76:79], a[8:11], v[32:35]\n\t"
      "buffer_store_dwordx4 a[48:51], %[stv], s[44:47], s49 offen sc1\n\t"
      "v_mfma_f32_16x16x32_bf16 v[32:35], a[84:87], a[12:15], v[32:35]\n\t"
      "v_mfma_f32_16x16x32_bf16 v[32:35], a[92:95], a[16:19], v[32:35]\n\t"
      "v_mfma_f32_16x16x32_bf16 v[32:35], a[100:103], a[20:23], v[32:35]\n\t"
      "v_mfma_f32_16x16x32_bf16 v[36:39], a[64:67], a[0:3], 0\n\t"
      "v_mfma_f32_16x16x32_bf16 v[36:39], a[72:75], a[4:7], v[36:39]\n\t"
      "v_mfma_f32_16x16x32_bf16 v[36:39], a[80:83], a[8:11], v[36:39]\n\t"
      "v_mfma_f32_16x16x32_bf16 v[36:39], a[88:91], a[12:15], v[36:39]\n\t"
      "s_waitcnt lgkmcnt(1)\n\t"
      "v_mfma_f32_16x16x32_bf16 v[36:39], a[96:99], a[16:19], v[36:39]\n\t"
      "buffer_store_dwordx4 a[52:55], %[stv], s[44:47], s49 offen offset:1024 sc1\n\t"
      "v_mfma_f32_16x16x32_bf16 v[36:39], a[104:107], a[20:23], v[36:39]\n\t"
      "v_mfma_f32_16x16x32_bf16 v[16:19], v[160:163], a[24:27], v[16:19]\n\t"
      "ds_read_b128 a[0:3], v56 offset:0\n\t"
      "v_mfma_f32_16x16x32_bf16 v[16:19], v[176:179], a[28:31], v[16:19]\n\t"
      "ds_read_b128 a[4:7], v57 offset:0\n\t"
      "v_mfma_f32_16x16x32_bf16 v[16:19], v[192:195], a[32:35], v[16:19]\n\t"
      "ds_read_b128 a[8:11], v58 offset:0\n\t"
      "v_mfma_f32_16x16x32_bf16 v[16:19], v[208:211], a[36:39], v[16:19]\n\t"
      "ds_read_b128 a[12:15], v59 offset:0\n\t"
      "v_mfma_f32_16x16x32_bf16 v[16:19], v[224:227], a[40:43], v[16:19]\n\t"
      "ds_read_b128 a[16:19], v56 offset:256\n\t"
      "v_mfma_f32_16x16x32_bf16 v[16:19], v[240:243], a[44:47], v[16:19]\n\t"
      "ds_read_b128 a[20:23], v57 offset:256\n\t"
      "v_mfma_f32_16x16x32_bf16 v[20:23], v[164:167], a[24:27], v[20:23]\n\t"
      "s_add_u32 s51, s51, 12288\n\t"
      "s_cmp_eq_u32 s51, 160256\n\t"
      "s_cselect_b32 s51, 25088, s51\n\t"
      "v_mfma_f32_16x16x32_bf16 v[20:23], v[180:183], a[28:31], v[20:23]\n\t"
      "s_waitcnt lgkmcnt(6)\n\t"
      "v_mfma_f32_16x16x32_bf16 v[20:23], v[196:199], a[32:35], v[20:23]\n\t"
      "buffer_store_dwordx4 a[56:59], %[stv], s[44:47], s49 offen offset:2048 sc1\n\t"
      "v_mfma_f32_16x16x32_bf16 v[20:23], v[212:215], a[36:39], v[20:23]\n\t"
      "s_add_u32 s49, s49, 3145728\n\t"
      "v_mfma_f32_16x16x32_bf16 v[20:23], v[228:231], a[40:43], v[20:23]\n\t"
      "s_waitcnt lgkmcnt(0)\n\t"
      "v_mfma_f32_16x16x32_bf16 v[20:23], v[244:247], a[44:47], v[20:23]\n\t"
      "v_mfma_f32_16x16x32_bf16 v[24:27], v[168:171], a[24:27], v[24:27]\n\t"
      "v_mfma_f32_16x16x32_bf16 v[24:27], v[184:187], a[28:31], v[24:27]\n\t"
      "v_cvt_pk_bf16_f32 v40, v16, v17\n\t"
      "v_cvt_pk_bf16_f32 v41, v18, v19\n\t"
      "v_mfma_f32_16x16x32_bf16 v[24:27], v[200:203], a[32:35], v[24:27]\n\t"
      "ds_write_b64 %[stw], v[40:41] offset:12544\n\t"
      "v_mfma_f32_16x16x32_bf16 v[24:27], v[216:219], a[36:39], v[24:27]\n\t"
      "v_mfma_f32_16x16x32_bf16 v[24:27], v[232:235], a[40:43], v[24:27]\n\t"
      "v_mfma_f32_16x16x32_bf16 v[24:27], v[248:251], a[44:47], v[24:27]\n\t"
      "v_mfma_f32_16x16x32_bf16 v[28:31], v[172:175], a[24:27], v[28:31]\n\t"
      "v_mfma_f32_16x16x32_bf16 v[28:31], v[188:191], a[28:31], v[28:31]\n\t"
      "v_cvt_pk_bf16_f32 v42, v20, v21\n\t"
      "v_cvt_pk_bf16_f32 v43, v22, v23\n\t"
      "v_mfma_f32_16x16x32_bf16 v[28:31], v[204:207], a[32:35], v[28:31]\n\t"
      "ds_write_b64 %[stw], v[42:43] offset:12576\n\t"
      "v_mfma_f32_16x16x32_bf16 v[28:31], v[220:223], a[36:39], v[28:31]\n\t"
      "v_mfma_f32_16x16x32_bf16 v[28:31], v[236:239], a[40:43], v[28:31]\n\t"
      "v_mfma_f32_16x16x32_bf16 v[28:31], v[252:255], a[44:47], v[28:31]\n\t"
      "v_mfma_f32_16x16x32_bf16 v[32:35], a[108:111], a[24:27], v[32:35]\n\t"
      "v_mfma_f32_16x16x32_bf16 v[32:35], a[116:119], a[28:31], v[32:35]\n\t"
      "v_cvt_pk_bf16_f32 v44, v24, v25\n\t"
      "v_cvt_pk_bf16_f32 v45, v26, v27\n\t"
      "v_mfma_f32_16x16x32_bf16 v[32:35], a[124:127], a[32:35], v[32:35]\n\t"
      "ds_write_b64 %[stw], v[44:45] offset:12608\n\t"
      "v_mfma_f32_16x16x32_bf16 v[32:35], a[132:135], a[36:39], v[32:35]\n\t"
      "v_mfma_f32_16x16x32_bf16 v[32:35], a[140:143], a[40:43], v[32:35]\n\t"
      "v_mfma_f32_16x16x32_bf16 v[32:35], a[148:151], a[44:47], v[32:35]\n\t"
      "v_cvt_pk_bf16_f32 v46, v28, v29\n\t"
      "v_cvt_pk_bf16_f32 v47, v30, v31\n\t"
      "v_mfma_f32_16x16x32_bf16 v[36:39], a[112:115], a[24:27], v[36:39]\n\t"
      "ds_write_b64 %[stw], v[46:47] offset:12640\n\t"
      "v_mfma_f32_16x16x32_bf16 v[36:39], a[120:123], a[28:31], v[36:39]\n\t"
      "v_mfma_f32_16x16x32_bf16 v[36:39], a[128:131], a[32:35], v[36:39]\n\t"
      "s_sub_u32 s52, s52, 1\n\t"
      "s_cmp_lg_u32 s52, 0\n\t"
      "v_mfma_f32_16x16x32_bf16 v[36:39], a[136:139], a[36:39], v[36:39]\n\t"
      "v_mfma_f32_16x16x32_bf16 v[36:39], a[144:147], a[40:43], v[36:39]\n\t"
      "v_cvt_pk_bf16_f32 v48, v32, v33\n\t"
      "v_cvt_pk_bf16_f32 v49, v34, v35\n\t"
      "v_mfma_f32_16x16x32_bf16 v[36:39], a[152:155], a[44:47], v[36:39]\n\t"
      "ds_write_b64 %[stw], v[48:49] offset:12672\n\t"
      "s_cbranch_scc1 .Ldrain2_%=\n\t"
      "s_branch .Ltail_%=\n\t"
      ".Lloop3_%=:\n\t"
      "v_mfma_f32_16x16x32_bf16 v[16:19], v[64:67], a[0:3], 0\n\t"
      "ds_read_b128 a[24:27], v58 offset:256\n\t"
      "v_mfma_f32_16x16x32_bf16 v[16:19], v[80:83], a[4:7], v[16:19]\n\t"
      "ds_read_b128 a[28:31], v59 offset:256\n\t"
      "v_mfma_f32_16x16x32_bf16 v[16:19], v[96:99], a[8:11], v[16:19]\n\t"
      "ds_read_b128 a[32:35], v56 offset:512\n\t"
      "v_mfma_f32_16x16x32_bf16 v[16:19], v[112:115], a[12:15], v[16:19]\n\t"
      "ds_read_b128 a[36:39], v57 offset:512\n\t"
      "v_mfma_f32_16x16x32_bf16 v[16:19], v[128:131], a[16:19], v[16:19]\n\t"
      "ds_read_b128 a[40:43], v58 offset:512\n\t"
      "v_mfma_f32_16x16x32_bf16 v[16:19], v[144:147], a[20:23], v[16:19]\n\t"
      "ds_read_b128 a[44:47], v59 offset:512\n\t"
      "v_mfma_f32_16x16x32_bf16 v[20:23], v[68:71], a[0:3], 0\n\t"
      "v_cvt_pk_bf16_f32 v50, v36, v37\n\t"
      "v_cvt_pk_bf16_f32 v51, v38, v39\n\t"
      "v_mfma_f32_16x16x32_bf16 v[20:23], v[84:87], a[4:7], v[20:23]\n\t"
      "ds_write_b64 %[stw], v[50:51] offset:12704\n\t"
      "v_mfma_f32_16x16x32_bf16 v[20:23], v[100:103], a[8:11], v[20:23]\n\t"
      "v_add_u32 v52, s51, %[fb0]\n\t"
      "v_xor_b32 v53, 64, v52\n\t"
      "v_mfma_f32_16x16x32_bf16 v[20:23], v[116:119], a[12:15], v[20:23]\n\t"
      "v_xor_b32 v54, 128, v52\n\t"
      "v_xor_b32 v55, 192, v52\n\t"
      "v_mfma_f32_16x16x32_bf16 v[20:23], v[132:135], a[16:19], v[20:23]\n\t"
      "v_mfma_f32_16x16x32_bf16 v[20:23], v[148:151], a[20:23], v[20:23]\n\t"
      "v_mfma_f32_16x16x32_bf16 v[24:27], v[72:75], a[0:3], 0\n\t"
      "v_mfma_f32_16x16x32_bf16 v[24:27], v[88:91], a[4:7], v[24:27]\n\t"
      "v_mfma_f32_16x16x32_bf16 v[24:27], v[104:107], a[8:11], v[24:27]\n\t"
      "v_mfma_f32_16x16x32_bf16 v[24:27], v[120:123], a[12:15], v[24:27]\n\t"
      "v_mfma_f32_16x16x32_bf16 v[24:27], v[136:139], a[16:19], v[24:27]\n\t"
      "v_mfma_f32_16x16x32_bf16 v[24:27], v[152:155], a[20:23], v[24:27]\n\t"
      "s_waitcnt vmcnt(12)\n\t"
      "s_waitcnt lgkmcnt(0)\n\t"
      "s_barrier\n\t"
      "v_mfma_f32_16x16x32_bf16 v[28:31], v[76:79], a[0:3], 0\n\t"
      "ds_read_b128 a[48:51], %[str0] offset:12544\n\t"
      "v_mfma_f32_16x16x32_bf16 v[28:31], v[92:95], a[4:7], v[28:31]\n\t"
      "ds_read_b128 a[52:55], %[str1] offset:13568\n\t"
      "v_mfma_f32_16x16x32_bf16 v[28:31], v[108:111], a[8:11], v[28:31]\n\t"
      "ds_read_b128 a[56:59], %[str2] offset:14592\n\t"
      "v_mfma_f32_16x16x32_bf16 v[28:31], v[124:127], a[12:15], v[28:31]\n\t"
      "v_mfma_f32_16x16x32_bf16 v[28:31], v[140:143], a[16:19], v[28:31]\n\t"
      "v_mfma_f32_16x16x32_bf16 v[28:31], v[156:159], a[20:23], v[28:31]\n\t"
      "v_mfma_f32_16x16x32_bf16 v[32:35], a[60:63], a[0:3], 0\n\t"
      "v_mfma_f32_16x16x32_bf16 v[32:35], a[68:71], a[4:7], v[32:35]\n\t"
      "s_waitcnt lgkmcnt(2)\n\t"
      "v_mfma_f32_16x16x32_bf16 v[32:35], a[76:79], a[8:11], v[32:35]\n\t"
      "buffer_store_dwordx4 a[48:51], %[stv], s[44:47], s49 offen sc1\n\t"
      "v_mfma_f32_16x16x32_bf16 v[32:35], a[84:87], a[12:15], v[32:35]\n\t"
      "v_mfma_f32_16x16x32_bf16 v[32:35], a[92:95], a[16:19], v[32:35]\n\t"
      "v_mfma_f32_16x16x32_bf16 v[32:35], a[100:103], a[20:23], v[32:35]\n\t"
      "s_add_u32 m0, %[m0base], s50\n\t"
      "v_mfma_f32_16x16x32_bf16 v[36:39], a[64:67], a[0:3], 0\n\t"
      "buffer_load_dwordx4 %[vx0], s[40:43], s48 offen lds\n\t"
      "v_mfma_f32_16x16x32_bf16 v[36:39], a[72:75], a[4:7], v[36:39]\n\t"
      "v_mfma_f32_16x16x32_bf16 v[36:39], a[80:83], a[8:11], v[36:39]\n\t"
      "v_mfma_f32_16x16x32_bf16 v[36:39], a[88:91], a[12:15], v[36:39]\n\t"
      "s_waitcnt lgkmcnt(1)\n\t"
      "v_mfma_f32_16x16x32_bf16 v[36:39], a[96:99], a[16:19], v[36:39]\n\t"
      "buffer_store_dwordx4 a[52:55], %[stv], s[44:47], s49 offen offset:1024 sc1\n\t"
      "v_mfma_f32_16x16x32_bf16 v[36:39], a[104:107], a[20:23], v[36:39]\n\t"
      "v_mfma_f32_16x16x32_bf16 v[16:19], v[160:163], a[24:27], v[16:19]\n\t"
      "ds_read_b128 a[0:3], v52 offset:0\n\t"
      "v_mfma_f32_16x16x32_bf16 v[16:19], v[176:179], a[28:31], v[16:19]\n\t"
      "ds_read_b128 a[4:7], v53 offset:0\n\t"
      "v_mfma_f32_16x16x32_bf16 v[16:19], v[192:195], a[32:35], v[16:19]\n\t"
      "ds_read_b128 a[8:11], v54 offset:0\n\t"
      "v_mfma_f32_16x16x32_bf16 v[16:19], v[208:211], a[36:39], v[16:19]\n\t"
      "ds_read_b128 a[12:15], v55 offset:0\n\t"
      "v_mfma_f32_16x16x32_bf16 v[16:19], v[224:227], a[40:43], v[16:19]\n\t"
      "ds_read_b128 a[16:19], v52 offset:256\n\t"
      "v_mfma_f32_16x16x32_bf16 v[16:19], v[240:243], a[44:47], v[16:19]\n\t"
      "ds_read_b128 a[20:23], v53 offset:256\n\t"
      "v_mfma_f32_16x16x32_bf16 v[20:23], v[164:167], a[24:27], v[20:23]\n\t"
      "s_add_u32 s51, s51, 12288\n\t"
      "s_cmp_eq_u32 s51, 160256\n\t"
      "s_cselect_b32 s51, 25088, s51\n\t"
      "v_mfma_f32_16x16x32_bf16 v[20:23], v[180:183], a[28:31], v[20:23]\n\t"
      "s_waitcnt lgkmcnt(6)\n\t"
      "v_mfma_f32_16x16x32_bf16 v[20:23], v[196:199], a[32:35], v[20:23]\n\t"
      "buffer_store_dwordx4 a[56:59], %[stv], s[44:47], s49 offen offset:2048 sc1\n\t"
      "v_mfma_f32_16x16x32_bf16 v[20:23], v[212:215], a[36:39], v[20:23]\n\t"
      "s_add_u32 s49, s49, 3145728\n\t"
      "v_mfma_f32_16x16x32_bf16 v[20:23], v[228:231], a[40:43], v[20:23]\n\t"
      "s_waitcnt lgkmcnt(0)\n\t"
      "v_mfma_f32_16x16x32_bf16 v[20:23], v[244:247], a[44:47], v[20:23]\n\t"
      "v_mfma_f32_16x16x32_bf16 v[24:27], v[168:171], a[24:27], v[24:27]\n\t"
      "v_mfma_f32_16x16x32_bf16 v[24:27], v[184:187], a[28:31], v[24:27]\n\t"
      "v_cvt_pk_bf16_f32 v40, v16, v17\n\t"
      "v_cvt_pk_bf16_f32 v41, v18, v19\n\t"
      "v_mfma_f32_16x16x32_bf16 v[24:27], v[200:203], a[32:35], v[24:27]\n\t"
      "ds_write_b64 %[stw], v[40:41] offset:0\n\t"
      "v_mfma_f32_16x16x32_bf16 v[24:27], v[216:219], a[36:39], v[24:27]\n\t"
      "buffer_load_dwordx4 %[vx1], s[40:43], s48 offen offset:1024 lds\n\t"
      "v_mfma_f32_16x16x32_bf16 v[24:27], v[232:235], a[40:43], v[24:27]\n\t"
      "v_mfma_f32_16x16x32_bf16 v[24:27], v[248:251], a[44:47], v[24:27]\n\t"
      "v_mfma_f32_16x16x32_bf16 v[28:31], v[172:175], a[24:27], v[28:31]\n\t"
      "v_mfma_f32_16x16x32_bf16 v[28:31], v[188:191], a[28:31], v[28:31]\n\t"
      "v_cvt_pk_bf16_f32 v42, v20, v21\n\t"
      "v_cvt_pk_bf16_f32 v43, v22, v23\n\t"
      "v_mfma_f32_16x16x32_bf16 v[28:31], v[204:207], a[32:35], v[28:31]\n\t"
      "ds_write_b64 %[stw], v[42:43] offset:32\n\t"
      "v_mfma_f32_16x16x32_bf16 v[28:31], v[220:223], a[36:39], v[28:31]\n\t"
      "v_mfma_f32_16x16x32_bf16 v[28:31], v[236:239], a[40:43], v[28:31]\n\t"
      "v_mfma_f32_16x16x32_bf16 v[28:31], v[252:255], a[44:47], v[28:31]\n\t"
      "v_mfma_f32_16x16x32_bf16 v[32:35], a[108:111], a[24:27], v[32:35]\n\t"
      "v_mfma_f32_16x16x32_bf16 v[32:35], a[116:119], a[28:31], v[32:35]\n\t"
      "v_cvt_pk_bf16_f32 v44, v24, v25\n\t"
      "v_cvt_pk_bf16_f32 v45, v26, v27\n\t"
      "v_mfma_f32_16x16x32_bf16 v[32:35], a[124:127], a[32:35], v[32:35]\n\t"
      "ds_write_b64 %[stw], v[44:45] offset:64\n\t"
      "v_mfma_f32_16x16x32_bf16 v[32:35], a[132:135], a[36:39], v[32:35]\n\t"
      "buffer_load_dwordx4 %[vx2], s[40:43], s48 offen offset:2048 lds\n\t"
      "v_mfma_f32_16x16x32_bf16 v[32:35], a[140:143], a[40:43], v[32:35]\n\t"
      "s_add_u32 s48, s48, 3145728\n\t"
      "s_add_u32 s50, s50, 12288\n\t"
      "v_mfma_f32_16x16x32_bf16 v[32:35], a[148:151], a[44:47], v[32:35]\n\t"
      "v_cvt_pk_bf16_f32 v46, v28, v29\n\t"
      "v_cvt_pk_bf16_f32 v47, v30, v31\n\t"
      "v_mfma_f32_16x16x32_bf16 v[36:39], a[112:115], a[24:27], v[36:39]\n\t"
      "ds_write_b64 %[stw], v[46:47] offset:96\n\t"
      "v_mfma_f32_16x16x32_bf16 v[36:39], a[120:123], a[28:31], v[36:39]\n\t"
      "s_cmp_eq_u32 s50, 160256\n\t"
      "s_cselect_b32 s50, 25088, s50\n\t"
      "v_mfma_f32_16x16x32_bf16 v[36:39], a[128:131], a[32:35], v[36:39]\n\t"
      "v_mfma_f32_16x16x32_bf16 v[36:39], a[136:139], a[36:39], v[36:39]\n\t"
      "v_mfma_f32_16x16x32_bf16 v[36:39], a[144:147], a[40:43], v[36:39]\n\t"
      "v_cvt_pk_bf16_f32 v48, v32, v33\n\t"
      "v_cvt_pk_bf16_f32 v49, v34, v35\n\t"
      "v_mfma_f32_16x16x32_bf16 v[36:39], a[152:155], a[44:47], v[36:39]\n\t"
      "ds_write_b64 %[stw], v[48:49] offset:128\n\t"
      ".Lmid3_%=:\n\t"
      "v_mfma_f32_16x16x32_bf16 v[16:19], v[64:67], a[0:3], 0\n\t"
      "ds_read_b128 a[24:27], v54 offset:256\n\t"
      "v_mfma_f32_16x16x32_bf16 v[16:19], v[80:83], a[4:7], v[16:19]\n\t"
      "ds_read_b128 a[28:31], v55 offset:256\n\t"
      "v_mfma_f32_16x16x32_bf16 v[16:19], v[96:99], a[8:11], v[16:19]\n\t"
      "ds_read_b128 a[32:35], v52 offset:512\n\t"
      "v_mfma_f32_16x16x32_bf16 v[16:19], v[112:115], a[12:15], v[16:19]\n\t"
      "ds_read_b128 a[36:39], v53 offset:512\n\t"
      "v_mfma_f32_16x16x32_bf16 v[16:19], v[128:131], a[16:19], v[16:19]\n\t"
      "ds_read_b128 a[40:43], v54 offset:512\n\t"
      "v_mfma_f32_16x16x32_bf16 v[16:19], v[144:147], a[20:23], v[16:19]\n\t"
      "ds_read_b128 a[44:47], v55 offset:512\n\t"
      "v_mfma_f32_16x16x32_bf16 v[20:23], v[68:71], a[0:3], 0\n\t"
      "v_cvt_pk_bf16_f32 v50, v36, v37\n\t"
      "v_cvt_pk_bf16_f32 v51, v38, v39\n\t"
      "v_mfma_f32_16x16x32_bf16 v[20:23], v[84:87], a[4:7], v[20:23]\n\t"
      "ds_write_b64 %[stw], v[50:51] offset:160\n\t"
      "v_mfma_f32_16x16x32_bf16 v[20:23], v[100:103], a[8:11], v[20:23]\n\t"
      "v_add_u32 v56, s51, %[fb0]\n\t"
      "v_xor_b32 v57, 64, v56\n\t"
      "v_mfma_f32_16x16x32_bf16 v[20:23], v[116:119], a[12:15], v[20:23]\n\t"
      "v_xor_b32 v58, 128, v56\n\t"
      "v_xor_b32 v59, 192, v56\n\t"
      "v_mfma_f32_16x16x32_bf16 v[20:23], v[132:135], a[16:19], v[20:23]\n\t"
      "v_mfma_f32_16x16x32_bf16 v[20:23], v[148:151], a[20:23], v[20:23]\n\t"
      "v_mfma_f32_16x16x32_bf16 v[24:27], v[72:75], a[0:3], 0\n\t"
      "v_mfma_f32_16x16x32_bf16 v[24:27], v[88:91], a[4:7], v[24:27]\n\t"
      "v_mfma_f32_16x16x32_bf16 v[24:27], v[104:107], a[8:11], v[24:27]\n\t"
      "v_mfma_f32_16x16x32_bf16 v[24:27], v[120:123], a[12:15], v[24:27]\n\t"
      "v_mfma_f32_16x16x32_bf16 v[24:27], v[136:139], a[16:19], v[24:27]\n\t"
      "v_mfma_f32_16x16x32_bf16 v[24:27], v[152:155], a[20:23], v[24:27]\n\t"
      "s_waitcnt vmcnt(12)\n\t"
      "s_waitcnt lgkmcnt(0)\n\t"
      "s_barrier\n\t"
      "v_mfma_f32_16x16x32_bf16 v[28:31], v[76:79], a[0:3], 0\n\t"
      "ds_read_b128 a[48:51], %[str0] offset:0\n\t"
      "v_mfma_f32_16x16x32_bf16 v[28:31], v[92:95], a[4:7], v[28:31]\n\t"
      "ds_read_b128 a[52:55], %[str1] offset:1024\n\t"
      "v_mfma_f32_16x16x32_bf16 v[28:31], v[108:111], a[8:11], v[28:31]\n\t"
      "ds_read_b128 a[56:59], %[str2] offset:2048\n\t"
      "v_mfma_f32_16x16x32_bf16 v[28:31], v[124:127], a[12:15], v[28:31]\n\t"
      "v_mfma_f32_16x16x32_bf16 v[28:31], v[140:143], a[16:19], v[28:31]\n\t"
      "v_mfma_f32_16x16x32_bf16 v[28:31], v[156:159], a[20:23], v[28:31]\n\t"
      "v_mfma_f32_16x16x32_bf16 v[32:35], a[60:63], a[0:3], 0\n\t"
      "v_mfma_f32_16x16x32_bf16 v[32:35], a[68:71], a[4:7], v[32:35]\n\t"
      "s_waitcnt lgkmcnt(2)\n\t"
      "v_mfma_f32_16x16x32_bf16 v[32:35], a[76:79], a[8:11], v[32:35]\n\t"
      "buffer_store_dwordx4 a[48:51], %[stv], s[44:47], s49 offen sc1\n\t"
      "v_mfma_f32_16x16x32_bf16 v[32:35], a[84:87], a[12:15], v[32:35]\n\t"
      "v_mfma_f32_16x16x32_bf16 v[32:35], a[92:95], a[16:19], v[32:35]\n\t"
      "v_mfma_f32_16x16x32_bf16 v[32:35], a[100:103], a[20:23], v[32:35]\n\t"
      "s_add_u32 m0, %[m0base], s50\n\t"
      "v_mfma_f32_16x16x32_bf16 v[36:39], a[64:67], a[0:3], 0\n\t"
      "buffer_load_dwordx4 %[vx0], s[40:43], s48 offen lds\n\t"
      "v_mfma_f32_16x16x32_bf16 v[36:39], a[72:75], a[4:7], v[36:39]\n\t"
      "v_mfma_f32_16x16x32_bf16 v[36:39], a[80:83], a[8:11], v[36:39]\n\t"
      "v_mfma_f32_16x16x32_bf16 v[36:39], a[88:91], a[12:15], v[36:39]\n\t"
      "s_waitcnt lgkmcnt(1)\n\t"
      "v_mfma_f32_16x16x32_bf16 v[36:39], a[96:99], a[16:19], v[36:39]\n\t"
      "buffer_store_dwordx4 a[52:55], %[stv], s[44:47], s49 offen offset:1024 sc1\n\t"
      "v_mfma_f32_16x16x32_bf16 v[36:39], a[104:107], a[20:23], v[36:39]\n\t"
      "v_mfma_f32_16x16x32_bf16 v[16:19], v[160:163], a[24:27], v[16:19]\n\t"
      "ds_read_b128 a[0:3], v56 offset:0\n\t"
      "v_mfma_f32_16x16x32_bf16 v[16:19], v[176:179], a[28:31], v[16:19]\n\t"
      "ds_read_b128 a[4:7], v57 offset:0\n\t"
      "v_mfma_f32_16x16x32_bf16 v[16:19], v[192:195], a[32:35], v[16:19]\n\t"
      "ds_read_b128 a[8:11], v58 offset:0\n\t"
      "v_mfma_f32_16x16x32_bf16 v[16:19], v[208:211], a[36:39], v[16:19]\n\t"
      "ds_read_b128 a[12:15], v59 offset:0\n\t"
      "v_mfma_f32_16x16x32_bf16 v[16:19], v[224:227], a[40:43], v[16:19]\n\t"
      "ds_read_b128 a[16:19], v56 offset:256\n\t"
      "v_mfma_f32_16x16x32_bf16 v[16:19], v[240:243], a[44:47], v[16:19]\n\t"
      "ds_read_b128 a[20:23], v57 offset:256\n\t"
      "v_mfma_f32_16x16x32_bf16 v[20:23], v[164:167], a[24:27], v[20:23]\n\t"
      "s_add_u32 s51, s51, 12288\n\t"
      "s_cmp_eq_u32 s51, 160256\n\t"
      "s_cselect_b32 s51, 25088, s51\n\t"
      "v_mfma_f32_16x16x32_bf16 v[20:23], v[180:183], a[28:31], v[20:23]\n\t"
      "s_waitcnt lgkmcnt(6)\n\t"
      "v_mfma_f32_16x16x32_bf16 v[20:23], v[196:199], a[32:35], v[20:23]\n\t"
      "buffer_store_dwordx4 a[56:59], %[stv], s[44:47], s49 offen offset:2048 sc1\n\t"
      "v_mfma_f32_16x16x32_bf16 v[20:23], v[212:215], a[36:39], v[20:23]\n\t"
      "s_add_u32 s49, s49, 3145728\n\t"
      "v_mfma_f32_16x16x32_bf16 v[20:23], v[228:231], a[40:43], v[20:23]\n\t"
      "s_waitcnt lgkmcnt(0)\n\t"
      "v_mfma_f32_16x16x32_bf16 v[20:23], v[244:247], a[44:47], v[20:23]\n\t"
      "v_mfma_f32_16x16x32_bf16 v[24:27], v[168:171], a[24:27], v[24:27]\n\t"
      "v_mfma_f32_16x16x32_bf16 v[24:27], v[184:187], a[28:31], v[24:27]\n\t"
      "v_cvt_pk_bf16_f32 v40, v16, v17\n\t"
      "v_cvt_pk_bf16_f32 v41, v18, v19\n\t"
      "v_mfma_f32_16x16x32_bf16 v[24:27], v[200:203], a[32:35], v[24:27]\n\t"
      "ds_write_b64 %[stw], v[40:41] offset:12544\n\t"
      "v_mfma_f32_16x16x32_bf16 v[24:27], v[216:219], a[36:39], v[24:27]\n\t"
      "buffer_load_dwordx4 %[vx1], s[40:43], s48 offen offset:1024 lds\n\t"
      "v_mfma_f32_16x16x32_bf16 v[24:27], v[232:235], a[40:43], v[24:27]\n\t"
      "v_mfma_f32_16x16x32_bf16 v[24:27], v[248:251], a[44:47], v[24:27]\n\t"
      "v_mfma_f32_16x16x32_bf16 v[28:31], v[172:175], a[24:27], v[28:31]\n\t"
      "v_mfma_f32_16x16x32_bf16 v[28:31], v[188:191], a[28:31], v[28:31]\n\t"
      "v_cvt_pk_bf16_f32 v42, v20, v21\n\t"
      "v_cvt_pk_bf16_f32 v43, v22, v23\n\t"
      "v_mfma_f32_16x16x32_bf16 v[28:31], v[204:207], a[32:35], v[28:31]\n\t"
      "ds_write_b64 %[stw], v[42:43] offset:12576\n\t"
      "v_mfma_f32_16x16x32_bf16 v[28:31], v[220:223], a[36:39], v[28:31]\n\t"
      "v_mfma_f32_16x16x32_bf16 v[28:31], v[236:239], a[40:43], v[28:31]\n\t"
      "v_mfma_f32_16x16x32_bf16 v[28:31], v[252:255], a[44:47], v[28:31]\n\t"
      "v_mfma_f32_16x16x32_bf16 v[32:35], a[108:111], a[24:27], v[32:35]\n\t"
      "v_mfma_f32_16x16x32_bf16 v[32:35], a[116:119], a[28:31], v[32:35]\n\t"
      "v_cvt_pk_bf16_f32 v44, v24, v25\n\t"
      "v_cvt_pk_bf16_f32 v45, v26, v27\n\t"
      "v_mfma_f32_16x16x32_bf16 v[32:35], a[124:127], a[32:35], v[32:35]\n\t"
      "ds_write_b64 %[stw], v[44:45] offset:12608\n\t"
      "v_mfma_f32_16x16x32_bf16 v[32:35], a[132:135], a[36:39], v[32:35]\n\t"
      "buffer_load_dwordx4 %[vx2], s[40:43], s48 offen offset:2048 lds\n\t"
      "v_mfma_f32_16x16x32_bf16 v[32:35], a[140:143], a[40:43], v[32:35]\n\t"
      "s_add_u32 s48, s48, 3145728\n\t"
      "s_add_u32 s50, s50, 12288\n\t"
      "v_mfma_f32_16x16x32_bf16 v[32:35], a[148:151], a[44:47], v[32:35]\n\t"
      "v_cvt_pk_bf16_f32 v46, v28, v29\n\t"
      "v_cvt_pk_bf16_f32 v47, v30, v31\n\t"
      "v_mfma_f32_16x16x32_bf16 v[36:39], a[112:115], a[24:27], v[36:39]\n\t"
      "ds_write_b64 %[stw], v[46:47] offset:12640\n\t"
      "v_mfma_f32_16x16x32_bf16 v[36:39], a[120:123], a[28:31], v[36:39]\n\t"
      "s_cmp_eq_u32 s50, 160256\n\t"
      "s_cselect_b32 s50, 25088, s50\n\t"
      "v_mfma_f32_16x16x32_bf16 v[36:39], a[128:131], a[32:35], v[36:39]\n\t"
      "s_sub_u32 s52, s52, 1\n\t"
      "s_cmp_lg_u32 s52, 0\n\t"
      "v_mfma_f32_16x16x32_bf16 v[36:39], a[136:139], a[36:39], v[36:39]\n\t"
      "v_mfma_f32_16x16x32_bf16 v[36:39], a[144:147], a[40:43], v[36:39]\n\t"
      "v_cvt_pk_bf16_f32 v48, v32, v33\n\t"
      "v_cvt_pk_bf16_f32 v49, v34, v35\n\t"
      "v_mfma_f32_16x16x32_bf16 v[36:39], a[152:155], a[44:47], v[36:39]\n\t"
      "ds_write_b64 %[stw], v[48:49] offset:12672\n\t"
      "s_cbranch_scc1 .Lloop3_%=\n\t"
      "s_mov_b32 s52, 2\n\t"
      ".Ldrain3_%=:\n\t"
      "v_mfma_f32_16x16x32_bf16 v[16:19], v[64:67], a[0:3], 0\n\t"
      "ds_read_b128 a[24:27], v58 offset:256\n\t"
      "v_mfma_f32_16x16x32_bf16 v[16:19], v[80:83], a[4:7], v[16:19]\n\t"
      "ds_read_b128 a[28:31], v59 offset:256\n\t"
      "v_mfma_f32_16x16x32_bf16 v[16:19], v[96:99], a[8:11], v[16:19]\n\t"
      "ds_read_b128 a[32:35], v56 offset:512\n\t"
      "v_mfma_f32_16x16x32_bf16 v[16:19], v[112:115], a[12:15], v[16:19]\n\t"
      "ds_read_b128 a[36:39], v57 offset:512\n\t"
      "v_mfma_f32_16x16x32_bf16 v[16:19], v[128:131], a[16:19], v[16:19]\n\t"
      "ds_read_b128 a[40:43], v58 offset:512\n\t"
      "v_mfma_f32_16x16x32_bf16 v[16:19], v[144:147], a[20:23], v[16:19]\n\t"
      "ds_read_b128 a[44:47], v59 offset:512\n\t"
      "v_mfma_f32_16x16x32_bf16 v[20:23], v[68:71], a[0:3], 0\n\t"
      "v_cvt_pk_bf16_f32 v50, v36, v37\n\t"
      "v_cvt_pk_bf16_f32 v51, v38, v39\n\t"
      "v_mfma_f32_16x16x32_bf16 v[20:23], v[84:87], a[4:7], v[20:23]\n\t"
      "ds_write_b64 %[stw], v[50:51] offset:12704\n\t"
      "v_mfma_f32_16x16x32_bf16 v[20:23], v[100:103], a[8:11], v[20:23]\n\t"
      "v_add_u32 v52, s51, %[fb0]\n\t"
      "v_xor_b32 v53, 64, v52\n\t"
      "v_mfma_f32_16x16x32_bf16 v[20:23], v[116:119], a[12:15], v[20:23]\n\t"
      "v_xor_b32 v54, 128, v52\n\t"
      "v_xor_b32 v55, 192, v52\n\t"
      "v_mfma_f32_16x16x32_bf16 v[20:23], v[132:135], a[16:19], v[20:23]\n\t"
      "v_mfma_f32_16x16x32_bf16 v[20:23], v[148:151], a[20:23], v[20:23]\n\t"
      "v_mfma_f32_16x16x32_bf16 v[24:27], v[72:75], a[0:3], 0\n\t"
      "v_mfma_f32_16x16x32_bf16 v[24:27], v[88:91], a[4:7], v[24:27]\n\t"
      "v_mfma_f32_16x16x32_bf16 v[24:27], v[104:107], a[8:11], v[24:27]\n\t"
      "v_mfma_f32_16x16x32_bf16 v[24:27], v[120:123], a[12:15], v[24:27]\n\t"
      "v_mfma_f32_16x16x32_bf16 v[24:27], v[136:139], a[16:19], v[24:27]\n\t"
      "v_mfma_f32_16x16x32_bf16 v[24:27], v[152:155], a[20:23], v[24:27]\n\t"
      "s_waitcnt vmcnt(6)\n\t"
      "s_waitcnt lgkmcnt(0)\n\t"
      "s_barrier\n\t"
      "v_mfma_f32_16x16x32_bf16 v[28:31], v[76:79], a[0:3], 0\n\t"
      "ds_read_b128 a[48:51], %[str0] offset:12544\n\t"
      "v_mfma_f32_16x16x32_bf16 v[28:31], v[92:95], a[4:7], v[28:31]\n\t"
      "ds_read_b128 a[52:55], %[str1] offset:13568\n\t"
      "v_mfma_f32_16x16x32_bf16 v[28:31], v[108:111], a[8:11], v[28:31]\n\t"
      "ds_read_b128 a[56:59], %[str2] offset:14592\n\t"
      "v_mfma_f32_16x16x32_bf16 v[28:31], v[124:127], a[12:15], v[28:31]\n\t"
      "v_mfma_f32_16x16x32_bf16 v[28:31], v[140:143], a[16:19], v[28:31]\n\t"
      "v_mfma_f32_16x16x32_bf16 v[28:31], v[156:159], a[20:23], v[28:31]\n\t"
      "v_mfma_f32_16x16x32_bf16 v[32:35], a[60:63], a[0:3], 0\n\t"
      "v_mfma_f32_16x16x32_bf16 v[32:35], a[68:71], a[4:7], v[32:35]\n\t"
      "s_waitcnt lgkmcnt(2)\n\t"
      "v_mfma_f32_16x16x32_bf16 v[32:35], a[76:79], a[8:11], v[32:35]\n\t"
      "buffer_store_dwordx4 a[48:51], %[stv], s[44:47], s49 offen sc1\n\t"
      "v_mfma_f32_16x16x32_bf16 v[32:35], a[84:87], a[12:15], v[32:35]\n\t"
      "v_mfma_f32_16x16x32_bf16 v[32:35], a[92:95], a[16:19], v[32:35]\n\t"
      "v_mfma_f32_16x16x32_bf16 v[32:35], a[100:103], a[20:23], v[32:35]\n\t"
      "v_mfma_f32_16x16x32_bf16 v[36:39], a[64:67], a[0:3], 0\n\t"
      "v_mfma_f32_16x16x32_bf16 v[36:39], a[72:75], a[4:7], v[36:39]\n\t"
      "v_mfma_f32_16x16x32_bf16 v[36:39], a[80:83], a[8:11], v[36:39]\n\t"
      "v_mfma_f32_16x16x32_bf16 v[36:39], a[88:91], a[12:15], v[36:39]\n\t"
      "s_waitcnt lgkmcnt(1)\n\t"
      "v_mfma_f32_16x16x32_bf16 v[36:39], a[96:99], a[16:19], v[36:39]\n\t"
      "buffer_store_dwordx4 a[52:55], %[stv], s[44:47], s49 offen offset:1024 sc1\n\t"
      "v_mfma_f32_16x16x32_bf16 v[36:39], a[104:107], a[20:23], v[36:39]\n\t"
      "v_mfma_f32_16x16x32_bf16 v[16:19], v[160:163], a[24:27], v[16:19]\n\t"
      "ds_read_b128 a[0:3], v52 offset:0\n\t"
      "v_mfma_f32_16x16x32_bf16 v[16:19], v[176:179], a[28:31], v[16:19]\n\t"
      "ds_read_b128 a[4:7], v53 offset:0\n\t"
      "v_mfma_f32_16x16x32_bf16 v[16:19], v[192:195], a[32:35], v[16:19]\n\t"
      "ds_read_b128 a[8:11], v54 offset:0\n\t"
      "v_mfma_f32_16x16x32_bf16 v[16:19], v[208:211], a[36:39], v[16:19]\n\t"
      "ds_read_b128 a[12:15], v55 offset:0\n\t"
      "v_mfma_f32_16x16x32_bf16 v[16:19], v[224:227], a[40:43], v[16:19]\n\t"
      "ds_read_b128 a[16:19], v52 offset:256\n\t"
      "v_mfma_f32_16x16x32_bf16 v[16:19], v[240:243], a[44:47], v[16:19]\n\t"
      "ds_read_b128 a[20:23], v53 offset:256\n\t"
      "v_mfma_f32_16x16x32_bf16 v[20:23], v[164:167], a[24:27], v[20:23]\n\t"
      "s_add_u32 s51, s51, 12288\n\t"
      "s_cmp_eq_u32 s51, 160256\n\t"
      "s_cselect_b32 s51, 25088, s51\n\t"
      "v_mfma_f32_16x16x32_bf16 v[20:23], v[180:183], a[28:31], v[20:23]\n\t"
      "s_waitcnt lgkmcnt(6)\n\t"
      "v_mfma_f32_16x16x32_bf16 v[20:23], v[196:199], a[32:35], v[20:23]\n\t"
      "buffer_store_dwordx4 a[56:59], %[stv], s[44:47], s49 offen offset:2048 sc1\n\t"
      "v_mfma_f32_16x16x32_bf16 v[20:23], v[212:215], a[36:39], v[20:23]\n\t"
      "s_add_u32 s49, s49, 3145728\n\t"
      "v_mfma_f32_16x16x32_bf16 v[20:23], v[228:231], a[40:43], v[20:23]\n\t"
      "s_waitcnt lgkmcnt(0)\n\t"
      "v_mfma_f32_16x16x32_bf16 v[20:23], v[244:247], a[44:47], v[20:23]\n\t"
      "v_mfma_f32_16x16x32_bf16 v[24:27], v[168:171], a[24:27], v[24:27]\n\t"
      "v_mfma_f32_16x16x32_bf16 v[24:27], v[184:187], a[28:31], v[24:27]\n\t"
      "v_cvt_pk_bf16_f32 v40, v16, v17\n\t"
      "v_cvt_pk_bf16_f32 v41, v18, v19\n\t"
      "v_mfma_f32_16x16x32_bf16 v[24:27], v[200:203], a[32:35], v[24:27]\n\t"
      "ds_write_b64 %[stw], v[40:41] offset:0\n\t"
      "v_mfma_f32_16x16x32_bf16 v[24:27], v[216:219], a[36:39], v[24:27]\n\t"
      "v_mfma_f32_16x16x32_bf16 v[24:27], v[232:235], a[40:43], v[24:27]\n\t"
      "v_mfma_f32_16x16x32_bf16 v[24:27], v[248:251], a[44:47], v[24:27]\n\t"
      "v_mfma_f32_16x16x32_bf16 v[28:31], v[172:175], a[24:27], v[28:31]\n\t"
      "v_mfma_f32_16x16x32_bf16 v[28:31], v[188:191], a[28:31], v[28:31]\n\t"
      "v_cvt_pk_bf16_f32 v42, v20, v21\n\t"
      "v_cvt_pk_bf16_f32 v43, v22, v23\n\t"
      "v_mfma_f32_16x16x32_bf16 v[28:31], v[204:207], a[32:35], v[28:31]\n\t"
      "ds_write_b64 %[stw], v[42:43] offset:32\n\t"
      "v_mfma_f32_16x16x32_bf16 v[28:31], v[220:223], a[36:39], v[28:31]\n\t"
      "v_mfma_f32_16x16x32_bf16 v[28:31], v[236:239], a[40:43], v[28:31]\n\t"
      "v_mfma_f32_16x16x32_bf16 v[28:31], v[252:255], a[44:47], v[28:31]\n\t"
      "v_mfma_f32_16x16x32_bf16 v[32:35], a[108:111], a[24:27], v[32:35]\n\t"
      "v_mfma_f32_16x16x32_bf16 v[32:35], a[116:119], a[28:31], v[32:35]\n\t"
      "v_cvt_pk_bf16_f32 v44, v24, v25\n\t"
      "v_cvt_pk_bf16_f32 v45, v26, v27\n\t"
      "v_mfma_f32_16x16x32_bf16 v[32:35], a[124:127], a[32:35], v[32:35]\n\t"
      "ds_write_b64 %[stw], v[44:45] offset:64\n\t"
      "v_mfma_f32_16x16x32_bf16 v[32:35], a[132:135], a[36:39], v[32:35]\n\t"
      "v_mfma_f32_16x16x32_bf16 v[32:35], a[140:143], a[40:43], v[32:35]\n\t"
      "v_mfma_f32_16x16x32_bf16 v[32:35], a[148:151], a[44:47], v[32:35]\n\t"
      "v_cvt_pk_bf16_f32 v46, v28, v29\n\t"
      "v_cvt_pk_bf16_f32 v47, v30, v31\n\t"
      "v_mfma_f32_16x16x32_bf16 v[36:39], a[112:115], a[24:27], v[36:39]\n\t"
      "ds_write_b64 %[stw], v[46:47] offset:96\n\t"
      "v_mfma_f32_16x16x32_bf16 v[36:39], a[120:123], a[28:31], v[36:39]\n\t"
      "v_mfma_f32_16x16x32_bf16 v[36:39], a[128:131], a[32:35], v[36:39]\n\t"
      "v_mfma_f32_16x16x32_bf16 v[36:39], a[136:139], a[36:39], v[36:39]\n\t"
      "v_mfma_f32_16x16x32_bf16 v[36:39], a[144:147], a[40:43], v[36:39]\n\t"
      "v_cvt_pk_bf16_f32 v48, v32, v33\n\t"
      "v_cvt_pk_bf16_f32 v49, v34, v35\n\t"
      "v_mfma_f32_16x16x32_bf16 v[36:39], a[152:155], a[44:47], v[36:39]\n\t"
      "ds_write_b64 %[stw], v[48:49] offset:128\n\t"
      "v_mfma_f32_16x16x32_bf16 v[16:19], v[64:67], a[0:3], 0\n\t"
      "ds_read_b128 a[24:27], v54 offset:256\n\t"
      "v_mfma_f32_16x16x32_bf16 v[16:19], v[80:83], a[4:7], v[16:19]\n\t"
      "ds_read_b128 a[28:31], v55 offset:256\n\t"
      "v_mfma_f32_16x16x32_bf16 v[16:19], v[96:99], a[8:11], v[16:19]\n\t"
      "ds_read_b128 a[32:35], v52 offset:512\n\t"
      "v_mfma_f32_16x16x32_bf16 v[16:19], v[112:115], a[12:15], v[16:19]\n\t"
      "ds_read_b128 a[36:39], v53 offset:512\n\t"
      "v_mfma_f32_16x16x32_bf16 v[16:19], v[128:131], a[16:19], v[16:19]\n\t"
      "ds_read_b128 a[40:43], v54 offset:512\n\t"
      "v_mfma_f32_16x16x32_bf16 v[16:19], v[144:147], a[20:23], v[16:19]\n\t"
      "ds_read_b128 a[44:47], v55 offset:512\n\t"
      "v_mfma_f32_16x16x32_bf16 v[20:23], v[68:71], a[0:3], 0\n\t"
      "v_cvt_pk_bf16_f32 v50, v36, v37\n\t"
      "v_cvt_pk_bf16_f32 v51, v38, v39\n\t"
      "v_mfma_f32_16x16x32_bf16 v[20:23], v[84:87], a[4:7], v[20:23]\n\t"
      "ds_write_b64 %[stw], v[50:51] offset:160\n\t"
      "v_mfma_f32_16x16x32_bf16 v[20:23], v[100:103], a[8:11], v[20:23]\n\t"
      "v_add_u32 v56, s51, %[fb0]\n\t"
      "v_xor_b32 v57, 64, v56\n\t"
      "v_mfma_f32_16x16x32_bf16 v[20:23], v[116:119], a[12:15], v[20:23]\n\t"
      "v_xor_b32 v58, 128, v56\n\t"
      "v_xor_b32 v59, 192, v56\n\t"
      "v_mfma_f32_16x16x32_bf16 v[20:23], v[132:135], a[16:19], v[20:23]\n\t"
      "v_mfma_f32_16x16x32_bf16 v[20:23], v[148:151], a[20:23], v[20:23]\n\t"
      "v_mfma_f32_16x16x32_bf16 v[24:27], v[72:75], a[0:3], 0\n\t"
      "v_mfma_f32_16x16x32_bf16 v[24:27], v[88:91], a[4:7], v[24:27]\n\t"
      "v_mfma_f32_16x16x32_bf16 v[24:27], v[104:107], a[8:11], v[24:27]\n\t"
      "v_mfma_f32_16x16x32_bf16 v[24:27], v[120:123], a[12:15], v[24:27]\n\t"
      "v_mfma_f32_16x16x32_bf16 v[24:27], v[136:139], a[16:19], v[24:27]\n\t"
      "v_mfma_f32_16x16x32_bf16 v[24:27], v[152:155], a[20:23], v[24:27]\n\t"
      "s_waitcnt vmcnt(6)\n\t"
      "s_waitcnt lgkmcnt(0)\n\t"
      "s_barrier\n\t"
      "v_mfma_f32_16x16x32_bf16 v[28:31], v[76:79], a[0:3], 0\n\t"
      "ds_read_b128 a[48:51], %[str0] offset:0\n\t"
      "v_mfma_f32_16x16x32_bf16 v[28:31], v[92:95], a[4:7], v[28:31]\n\t"
      "ds_read_b128 a[52:55], %[str1] offset:1024\n\t"
      "v_mfma_f32_16x16x32_bf16 v[28:31], v[108:111], a[8:11], v[28:31]\n\t"
      "ds_read_b128 a[56:59], %[str2] offset:2048\n\t"
      "v_mfma_f32_16x16x32_bf16 v[28:31], v[124:127], a[12:15], v[28:31]\n\t"
      "v_mfma_f32_16x16x32_bf16 v[28:31], v[140:143], a[16:19], v[28:31]\n\t"
      "v_mfma_f32_16x16x32_bf16 v[28:31], v[156:159], a[20:23], v[28:31]\n\t"
      "v_mfma_f32_16x16x32_bf16 v[32:35], a[60:63], a[0:3], 0\n\t"
      "v_mfma_f32_16x16x32_bf16 v[32:35], a[68:71], a[4:7], v[32:35]\n\t"
      "s_waitcnt lgkmcnt(2)\n\t"
      "v_mfma_f32_16x16x32_bf16 v[32:35], a[76:79], a[8:11], v[32:35]\n\t"
      "buffer_store_dwordx4 a[48:51], %[stv], s[44:47], s49 offen sc1\n\t"
      "v_mfma_f32_16x16x32_bf16 v[32:35], a[84:87], a[12:15], v[32:35]\n\t"
      "v_mfma_f32_16x16x32_bf16 v[32:35], a[92:95], a[16:19], v[32:35]\n\t"
      "v_mfma_f32_16x16x32_bf16 v[32:35], a[100:103], a[20:23], v[32:35]\n\t"
      "v_mfma_f32_16x16x32_bf16 v[36:39], a[64:67], a[0:3], 0\n\t"
      "v_mfma_f32_16x16x32_bf16 v[36:39], a[72:75], a[4:7], v[36:39]\n\t"
      "v_mfma_f32_16x16x32_bf16 v[36:39], a[80:83], a[8:11], v[36:39]\n\t"
      "v_mfma_f32_16x16x32_bf16 v[36:39], a[88:91], a[12:15], v[36:39]\n\t"
      "s_waitcnt lgkmcnt(1)\n\t"
      "v_mfma_f32_16x16x32_bf16 v[36:39], a[96:99], a[16:19], v[36:39]\n\t"
      "buffer_store_dwordx4 a[52:55], %[stv], s[44:47], s49 offen offset:1024 sc1\n\t"
      "v_mfma_f32_16x16x32_bf16 v[36:39], a[104:107], a[20:23], v[36:39]\n\t"
      "v_mfma_f32_16x16x32_bf16 v[16:19], v[160:163], a[24:27], v[16:19]\n\t"
      "ds_read_b128 a[0:3], v56 offset:0\n\t"
      "v_mfma_f32_16x16x32_bf16 v[16:19], v[176:179], a[28:31], v[16:19]\n\t"
      "ds_read_b128 a[4:7], v57 offset:0\n\t"
      "v_mfma_f32_16x16x32_bf16 v[16:19], v[192:195], a[32:35], v[16:19]\n\t"
      "ds_read_b128 a[8:11], v58 offset:0\n\t"
      "v_mfma_f32_16x16x32_bf16 v[16:19], v[208:211], a[36:39], v[16:19]\n\t"
      "ds_read_b128 a[12:15], v59 offset:0\n\t"
      "v_mfma_f32_16x16x32_bf16 v[16:19], v[224:227], a[40:43], v[16:19]\n\t"
      "ds_read_b128 a[16:19], v56 offset:256\n\t"
      "v_mfma_f32_16x16x32_bf16 v[16:19], v[240:243], a[44:47], v[16:19]\n\t"
      "ds_read_b128 a[20:23], v57 offset:256\n\t"
      "v_mfma_f32_16x16x32_bf16 v[20:23], v[164:167], a[24:27], v[20:23]\n\t"
      "s_add_u32 s51, s51, 12288\n\t"
      "s_cmp_eq_u32 s51, 160256\n\t"
      "s_cselect_b32 s51, 25088, s51\n\t"
      "v_mfma_f32_16x16x32_bf16 v[20:23], v[180:183], a[28:31], v[20:23]\n\t"
      "s_waitcnt lgkmcnt(6)\n\t"
      "v_mfma_f32_16x16x32_bf16 v[20:23], v[196:199], a[32:35], v[20:23]\n\t"
      "buffer_store_dwordx4 a[56:59], %[stv], s[44:47], s49 offen offset:2048 sc1\n\t"
      "v_mfma_f32_16x16x32_bf16 v[20:23], v[212:215], a[36:39], v[20:23]\n\t"
      "s_add_u32 s49, s49, 3145728\n\t"
      "v_mfma_f32_16x16x32_bf16 v[20:23], v[228:231], a[40:43], v[20:23]\n\t"
      "s_waitcnt lgkmcnt(0)\n\t"
      "v_mfma_f32_16x16x32_bf16 v[20:23], v[244:247], a[44:47], v[20:23]\n\t"
      "v_mfma_f32_16x16x32_bf16 v[24:27], v[168:171], a[24:27], v[24:27]\n\t"
      "v_mfma_f32_16x16x32_bf16 v[24:27], v[184:187], a[28:31], v[24:27]\n\t"
      "v_cvt_pk_bf16_f32 v40, v16, v17\n\t"
      "v_cvt_pk_bf16_f32 v41, v18, v19\n\t"
      "v_mfma_f32_16x16x32_bf16 v[24:27], v[200:203], a[32:35], v[24:27]\n\t"
      "ds_write_b64 %[stw], v[40:41] offset:12544\n\t"
      "v_mfma_f32_16x16x32_bf16 v[24:27], v[216:219], a[36:39], v[24:27]\n\t"
      "v_mfma_f32_16x16x32_bf16 v[24:27], v[232:235], a[40:43], v[24:27]\n\t"
      "v_mfma_f32_16x16x32_bf16 v[24:27], v[248:251], a[44:47], v[24:27]\n\t"
      "v_mfma_f32_16x16x32_bf16 v[28:31], v[172:175], a[24:27], v[28:31]\n\t"
      "v_mfma_f32_16x16x32_bf16 v[28:31], v[188:191], a[28:31], v[28:31]\n\t"
      "v_cvt_pk_bf16_f32 v42, v20, v21\n\t"
      "v_cvt_pk_bf16_f32 v43, v22, v23\n\t"
      "v_mfma_f32_16x16x32_bf16 v[28:31], v[204:207], a[32:35], v[28:31]\n\t"
      "ds_write_b64 %[stw], v[42:43] offset:12576\n\t"
      "v_mfma_f32_16x16x32_bf16 v[28:31], v[220:223], a[36:39], v[28:31]\n\t"
      "v_mfma_f32_16x16x32_bf16 v[28:31], v[236:239], a[40:43], v[28:31]\n\t"
      "v_mfma_f32_16x16x32_bf16 v[28:31], v[252:255], a[44:47], v[28:31]\n\t"
      "v_mfma_f32_16x16x32_bf16 v[32:35], a[108:111], a[24:27], v[32:35]\n\t"
      "v_mfma_f32_16x16x32_bf16 v[32:35], a[116:119], a[28:31], v[32:35]\n\t"
      "v_cvt_pk_bf16_f32 v44, v24, v25\n\t"
      "v_cvt_pk_bf16_f32 v45, v26, v27\n\t"
      "v_mfma_f32_16x16x32_bf16 v[32:35], a[124:127], a[32:35], v[32:35]\n\t"
      "ds_write_b64 %[stw], v[44:45] offset:12608\n\t"
      "v_mfma_f32_16x16x32_bf16 v[32:35], a[132:135], a[36:39], v[32:35]\n\t"
      "v_mfma_f32_16x16x32_bf16 v[32:35], a[140:143], a[40:43], v[32:35]\n\t"
      "v_mfma_f32_16x16x32_bf16 v[32:35], a[148:151], a[44:47], v[32:35]\n\t"
      "v_cvt_pk_bf16_f32 v46, v28, v29\n\t"
      "v_cvt_pk_bf16_f32 v47, v30, v31\n\t"
      "v_mfma_f32_16x16x32_bf16 v[36:39], a[112:115], a[24:27], v[36:39]\n\t"
      "ds_write_b64 %[stw], v[46:47] offset:12640\n\t"
      "v_mfma_f32_16x16x32_bf16 v[36:39], a[120:123], a[28:31], v[36:39]\n\t"
      "v_mfma_f32_16x16x32_bf16 v[36:39], a[128:131], a[32:35], v[36:39]\n\t"
      "s_sub_u32 s52, s52, 1\n\t"
      "s_cmp_lg_u32 s52, 0\n\t"
      "v_mfma_f32_16x16x32_bf16 v[36:39], a[136:139], a[36:39], v[36:39]\n\t"
      "v_mfma_f32_16x16x32_bf16 v[36:39], a[144:147], a[40:43], v[36:39]\n\t"
      "v_cvt_pk_bf16_f32 v48, v32, v33\n\t"
      "v_cvt_pk_bf16_f32 v49, v34, v35\n\t"
      "v_mfma_f32_16x16x32_bf16 v[36:39], a[152:155], a[44:47], v[36:39]\n\t"
      "ds_write_b64 %[stw], v[48:49] offset:12672\n\t"
      "s_cbranch_scc1 .Ldrain3_%=\n\t"
      ".Ltail_%=:\n\t"
      "s_nop 6\n\t"
      "v_cvt_pk_bf16_f32 v50, v36, v37\n\t"
      "v_cvt_pk_bf16_f32 v51, v38, v39\n\t"
      "ds_write_b64 %[stw], v[50:51] offset:12704\n\t"
      "s_waitcnt lgkmcnt(0)\n\t"
      "s_barrier\n\t"
      "ds_read_b128 a[48:51], %[str0] offset:12544\n\t"
      "ds_read_b128 a[52:55], %[str1] offset:13568\n\t"
      "ds_read_b128 a[56:59], %[str2] offset:14592\n\t"
      "s_waitcnt lgkmcnt(2)\n\t"
      "buffer_store_dwordx4 a[48:51], %[stv], s[44:47], s49 offen sc1\n\t"
      "s_waitcnt lgkmcnt(1)\n\t"
      "buffer_store_dwordx4 a[52:55], %[stv], s[44:47], s49 offen offset:1024 sc1\n\t"
      "s_waitcnt lgkmcnt(0)\n\t"
      "buffer_store_dwordx4 a[56:59], %[stv], s[44:47], s49 offen offset:2048 sc1\n\t"
      "s_branch .Lend_%=\n\t"
      ".Lspin0_%=:\n\t"
      "s_memrealtime s[36:37]\n\t"
      "s_waitcnt lgkmcnt(0)\n\t"
      "s_sub_u32 s53, s36, s38\n\t"
      "s_cmp_lt_i32 s53, 0\n\t"
      "s_cbranch_scc1 .Lspin0_%=\n\t"
      "s_branch .Lspun0_%=\n\t"
      ".Lspin1_%=:\n\t"
      "s_memrealtime s[36:37]\n\t"
      "s_waitcnt lgkmcnt(0)\n\t"
      "s_sub_u32 s53, s36, s38\n\t"
      "s_cmp_lt_i32 s53, 0\n\t"
      "s_cbranch_scc1 .Lspin1_%=\n\t"
      "s_branch .Lspun1_%=\n\t"
      ".Lend_%=:\n\t"
      :
      : [vx0] "v"(vx[0]), [vx1] "v"(vx[1]), [vx2] "v"(vx[2]), [vw0] "v"(vw[0]), [vw1] "v"(vw[1]), [vw2] "v"(vw[2]),
        [str0] "v"(str[0]), [str1] "v"(str[1]), [str2] "v"(str[2]), [stv] "v"(stv), [stw] "v"(stw), [fb0] "v"(fb0),
        [tb0] "v"(tb0), [tb1] "v"(tb0 ^ 32u), [dga] "v"(dga), [dwo] "v"(dwo),
        [srdX_lo] "s"(x), [srdO_lo] "s"(out), [srdW] "s"(srdW), [srdD] "s"(srdD), [soff0] "s"(soff0),
        [m0base] "s"(ldsb + q * 3072u), [m0w] "s"(ldsb + q * 6144u), [q6144] "s"(q * 6144u), [dwt] "s"(dwt),
        [wpre] "s"(grp * 9216u), [tn] "s"(grp * 4u + q), [ntrip] "s"(ntrip), [nbytes] "s"(nbytes)
      : "v16", "v17", "v18", "v19", "v20", "v21", "v22", "v23", "v24", "v25", "v26", "v27", "v28", "v29", "v30", "v31", "v32", "v33", "v34", "v35", "v36", "v37", "v38", "v39", "v40", "v41", "v42", "v43", "v44", "v45", "v46", "v47", "v48", "v49", "v50", "v51", "v52", "v53", "v54", "v55", "v56", "v57", "v58", "v59", "v60", "v61", "v62", "v63", "v64", "v65", "v66", "v67", "v68", "v69", "v70", "v71", "v72", "v73", "v74", "v75", "v76", "v77", "v78", "v79", "v80", "v81", "v82", "v83", "v84", "v85", "v86", "v87", "v88", "v89", "v90", "v91", "v92", "v93", "v94", "v95", "v96", "v97", "v98", "v99", "v100", "v101", "v102", "v103", "v104", "v105", "v106", "v107", "v108", "v109", "v110", "v111", "v112", "v113", "v114", "v115", "v116", "v117", "v118", "v119", "v120", "v121", "v122", "v123", "v124", "v125", "v126", "v127", "v128", "v129", "v130", "v131", "v132", "v133", "v134", "v135", "v136", "v137", "v138", "v139", "v140", "v141", "v142", "v143", "v144", "v145", "v146", "v147", "v148", "v149", "v150", "v151", "v152", "v153", "v154", "v155", "v156", "v157", "v158", "v159", "v160", "v161", "v162", "v163", "v164", "v165", "v166", "v167", "v168", "v169", "v170", "v171", "v172", "v173", "v174", "v175", "v176", "v177", "v178", "v179", "v180", "v181", "v182", "v183", "v184", "v185", "v186", "v187", "v188", "v189", "v190", "v191", "v192", "v193", "v194", "v195", "v196", "v197", "v198", "v199", "v200", "v201", "v202", "v203", "v204", "v205", "v206", "v207", "v208", "v209", "v210", "v211", "v212", "v213", "v214", "v215", "v216", "v217", "v218", "v219", "v220", "v221", "v222", "v223", "v224", "v225", "v226", "v227", "v228", "v229", "v230", "v231", "v232", "v233", "v234", "v235", "v236", "v237", "v238", "v239", "v240", "v241", "v242", "v243", "v244", "v245", "v246", "v247", "v248", "v249", "v250", "v251", "v252", "v253", "v254", "v255", "a0", "a1", "a2", "a3", "a4", "a5", "a6", "a7", "a8", "a9", "a10", "a11", "a12", "a13", "a14", "a15", "a16", "a17", "a18", "a19", "a20", "a21", "a22", "a23", "a24", "a25", "a26", "a27", "a28", "a29", "a30", "a31", "a32", "a33", "a34", "a35", "a36", "a37", "a38", "a39", "a40", "a41", "a42", "a43", "a44", "a45", "a46", "a47", "a48", "a49", "a50", "a51", "a52", "a53", "a54", "a55", "a56", "a57", "a58", "a59", "a60", "a61", "a62", "a63", "a64", "a65", "a66", "a67", "a68", "a69", "a70", "a71", "a72", "a73", "a74", "a75", "a76", "a77", "a78", "a79", "a80", "a81", "a82", "a83", "a84", "a85", "a86", "a87", "a88", "a89", "a90", "a91", "a92", "a93", "a94", "a95", "a96", "a97", "a98", "a99", "a100", "a101", "a102", "a103", "a104", "a105", "a106", "a107", "a108", "a109", "a110", "a111", "a112", "a113", "a114", "a115", "a116", "a117", "a118", "a119", "a120", "a121", "a122", "a123", "a124", "a125", "a126", "a127", "a128", "a129", "a130", "a131", "a132", "a133", "a134", "a135", "a136", "a137", "a138", "a139", "a140", "a141", "a142", "a143", "a144", "a145", "a146", "a147", "a148", "a149", "a150", "a151", "a152", "a153", "a154", "a155", "a156", "a157", "a158", "a159", "a160", "a161", "a162", "a163", "a164", "a165", "a166", "a167", "a168", "a169", "a170", "a171", "a172", "a173", "a174", "a175", "a176", "a177", "a178", "a179", "a180", "a181", "a182", "a183", "a184", "a185", "a186", "a187", "a188", "a189", "a190", "a191", "a192", "a193", "a194", "a195", "a196", "a197", "a198", "a199", "a200", "a201", "a202", "a203", "a204", "a205", "a206", "a207", "a208", "a209", "a210", "a211", "a212", "a213", "a214", "a215", "a216", "a217", "a218", "a219", "a220", "a221", "a222", "a223", "a224", "a225", "a226", "a227", "a228", "a229", "a230", "a231", "a232", "a233", "a234", "a235", "a236", "a237", "a238", "a239", "a240", "a241", "a242", "a243", "a244", "a245", "a246", "a247", "a248", "a249", "a250", "a251", "a252", "a253", "a254", "a255", "s30", "s31", "s32", "s33", "s34", "s35", "s36", "s37", "s38", "s39", "s40", "s41", "s42", "s43", "s44", "s45", "s46", "s47", "s48", "s49", "s50", "s51", "s52", "s53", "s54", "s55", "vcc", "scc", "memory");
}

extern "C" void kernel_launch(void* const* d_in, const int* in_sizes, int n_in, void* d_out, int out_size,
                              void* d_ws, size_t ws_size, hipStream_t stream) {
  const unsigned short* x = (const unsigned short*)d_in[0];
  const unsigned short* dw = (const unsigned short*)d_in[1];
  const unsigned short* pw = (const unsigned short*)d_in[2];
  unsigned short* out = (unsigned short*)d_out;
  const unsigned nbytes = (unsigned)in_sizes[0] * 2u;
  const unsigned ntiles = (nbytes + TILEB - 1) / TILEB;
  const unsigned nit = (ntiles + NWG - 1) / NWG;
  const unsigned ntrip = nit > 6 ? (nit + 1) / 2 - 2 : 1;
  dwsep_kernel<<<NWG, 256, 0, stream>>>(x, dw, pw, out, nbytes, ntrip);
}
```

```cpp
#include <hip/hip_runtime.h>

constexpr unsigned ROWB = 768, TILEB = 12288, NWG = 256;

__global__ __launch_bounds__(256, 1) void dwsep_kernel(const unsigned short* __restrict__ x,
                                                       const unsigned short* __restrict__ dw,
                                                       const unsigned short* __restrict__ pw,
                                                       unsigned short* __restrict__ out, unsigned* ws) {
  __shared__ __attribute__((aligned(256))) char lds[163392];
  unsigned tid = threadIdx.x;
  asm volatile(
      "s_memtime s[32:33]\n\t"
      "s_load_dwordx8 s[4:11], %[karg], 0\n\t"
      "s_load_dwordx2 s[12:13], %[karg], 0x20\n\t"
      "v_readfirstlane_b32 s30, %[tid]\n\t"
      "s_lshr_b32 s31, %[blk], 1\n\t"
      "s_and_b32 s31, s31, 12\n\t"
      "s_and_b32 s34, %[blk], 3\n\t"
      "s_or_b32 s31, s31, s34\n\t"
      "s_lshr_b32 s30, s30, 6\n\t"
      "s_lshr_b32 s34, %[blk], 5\n\t"
      "s_bfe_u32 s35, %[blk], 0x10002\n\t"
      "s_mul_i32 s17, s30, 6144\n\t"
      "s_mul_i32 s18, s30, 3072\n\t"
      "s_mul_i32 s14, s34, 14155776\n\t"
      "s_mul_i32 s36, s31, 49152\n\t"
      "s_add_u32 s14, s14, s36\n\t"
      "s_mul_i32 s36, s35, 24576\n\t"
      "s_add_u32 s15, s14, s36\n\t"
      "s_mov_b32 s16, 0\n\t"
      "s_lshr_b32 s36, %[blk], 3\n\t"
      "s_lshl2_add_u32 s24, s36, s30\n\t"
      "s_sub_u32 s36, s24, s36\n\t"
      "s_sub_u32 s36, s36, 1\n\t"
      "s_mul_i32 s23, s36, 3072\n\t"
      "s_lshl_b32 s20, s34, 3\n\t"
      "s_mul_i32 s36, s34, 27\n\t"
      "s_lshr_b32 s36, s36, 2\n\t"
      "s_lshr_b32 s37, s31, 1\n\t"
      "s_add_u32 s19, s37, s20\n\t"
      "s_add_u32 s37, s37, s36\n\t"
      "s_min_u32 s37, s37, 53\n\t"
      "s_lshl_b32 s22, s37, 21\n\t"
      "s_lshl_b32 s36, s36, 21\n\t"
      "s_sub_u32 s21, s14, s36\n\t"
      "s_add_u32 s21, s21, 24576\n\t"
      "s_lshl_b32 s19, s19, 8\n\t"
      "s_and_b32 s31, s31, 1\n\t"
      "s_lshl1_add_u32 s31, s31, s35\n\t"
      "s_lshl2_add_u32 s19, s31, s19\n\t"
      ".Lstart_%=:\n\t"
      "s_memrealtime s[28:29]\n\t"
      "v_mbcnt_lo_u32_b32 v233, -1, 0\n\t"
      "v_mbcnt_hi_u32_b32 v233, -1, v233\n\t"
      "s_mul_i32 s30, s17, 683\n\t"
      "s_lshr_b32 s30, s30, 16\n\t"
      "s_and_b32 s30, s30, 0xc0\n\t"
      "s_brev_b32 s34, 8\n\t"
      "s_mov_b32 s35, 0x20201010\n\t"
      "s_mov_b32 s36, 0x30303020\n\t"
      "v_lshl_add_u32 v1, v233, 4, s18\n\t"
      "v_lshrrev_b32 v16, 1, v233\n\t"
      "v_and_b32 v16, 24, v16\n\t"
      "v_bfe_u32 v17, s34, v16, 8\n\t"
      "v_or_b32 v17, s30, v17\n\t"
      "v_xor_b32 v18, v17, v1\n\t"
      "v_add_u32 v19, 80, v17\n\t"
      "v_add_u32 v17, 160, v17\n\t"
      "v_and_b32 v19, 256, v19\n\t"
      "v_and_b32 v17, 256, v17\n\t"
      "v_mad_u32_u24 v2, v19, 30, v18\n\t"
      "v_mad_u32_u24 v5, v17, 33, v18\n\t"
      "v_add_u32 v5, 19968, v5\n\t"
      "v_bfe_u32 v17, s35, v16, 8\n\t"
      "v_or_b32 v17, s30, v17\n\t"
      "v_xor_b32 v18, v17, v1\n\t"
      "v_add_u32 v19, 80, v17\n\t"
      "v_add_u32 v17, 160, v17\n\t"
      "v_and_b32 v19, 256, v19\n\t"
      "v_and_b32 v17, 256, v17\n\t"
      "v_mad_u32_u24 v3, v19, 30, v18\n\t"
      "v_mad_u32_u24 v6, v17, 33, v18\n\t"
      "v_add_u32 v6, 19968, v6\n\t"
      "v_bfe_u32 v17, s36, v16, 8\n\t"
      "v_or_b32 v17, s30, v17\n\t"
      "v_xor_b32 v18, v17, v1\n\t"
      "v_add_u32 v19, 80, v17\n\t"
      "v_add_u32 v17, 160, v17\n\t"
      "v_and_b32 v19, 256, v19\n\t"
      "v_and_b32 v17, 256, v17\n\t"
      "v_mad_u32_u24 v4, v19, 30, v18\n\t"
      "v_mad_u32_u24 v7, v17, 33, v18\n\t"
      "v_add_u32 v7, 19968, v7\n\t"
      "s_sub_u32 s31, s30, 64\n\t"
      "v_add_u32 v17, s31, v233\n\t"
      "v_min_u32 v17, 193, v17\n\t"
      "v_lshlrev_b32 v18, 2, v17\n\t"
      "v_lshlrev_b32 v17, 1, v17\n\t"
      "v_and_b32 v18, -16, v18\n\t"
      "v_and_or_b32 v8, v17, 6, v18\n\t"
      "s_mov_b32 s86, 294912\n\t"
      "s_mov_b32 s87, 0x00020000\n\t"
      "s_mov_b32 s42, 113246208\n\t"
      "s_mov_b32 s43, 0x00020000\n\t"
      "s_cmp_lg_u32 s17, 0\n\t"
      "s_cbranch_scc1 .Lno0_%=\n\t"
      "s_bfe_u32 s31, s19, 0x30008\n\t"
      "s_sub_u32 s31, s31, 64\n\t"
      "v_lshrrev_b32 v16, 2, v233\n\t"
      "v_add_u32 v17, s20, v16\n\t"
      "v_lshl_add_u32 v18, v16, 3, s31\n\t"
      "v_cmp_gt_u32 vcc, 32, v233\n\t"
      "v_and_b32 v19, 3, v233\n\t"
      "v_lshlrev_b32 v19, 2, v19\n\t"
      "v_cndmask_b32 v17, v18, v17, vcc\n\t"
      "v_lshl_add_u32 v62, v17, 8, v19\n\t"
      "s_waitcnt lgkmcnt(2)\n\t"
      "s_add_u32 s32, s32, %[blk]\n\t"
      "s_mul_i32 s32, s32, 0x9e3779b1\n\t"
      "s_lshr_b32 s32, s32, 9\n\t"
      "s_and_b32 s32, s32, 0x1fdf80\n\t"
      "s_or_b32 s22, s22, s32\n\t"
      "v_mov_b32 v234, s22\n\t"
      "v_add_u32 v235, 8192, v234\n\t"
      "s_movk_i32 s64, 120\n\t"
      "s_mov_b64 vcc, 0\n\t"
      ".Lsweep_%=:\n\t"
      "s_getreg_b32 s30, hwreg(HW_REG_IB_STS, 0, 12)\n\t"
      "s_and_b32 s31, s30, 15\n\t"
      "s_lshr_b32 s30, s30, 8\n\t"
      "s_cmp_eq_u32 s31, 8\n\t"
      "s_addc_u32 s62, s62, 0\n\t"
      "s_cmp_eq_u32 s30, 1\n\t"
      "s_addc_u32 s63, s63, 0\n\t"
      "s_cmp_gt_u32 s31, 7\n\t"
      "s_addc_u32 s30, s30, 0\n\t"
      "s_sub_u32 s64, s64, 1\n\t"
      "s_min_u32 s30, s30, s64\n\t"
      "s_cmp_lg_u32 s30, 0\n\t"
      "s_cbranch_scc1 .Lsweep_%=\n\t"
      "s_cbranch_vccnz .Lswept_%=\n\t"
      "s_waitcnt lgkmcnt(0)\n\t"
      "global_load_dword v236, v234, s[4:5]\n\t"
      "global_load_dword v237, v235, s[4:5]\n\t"
      "s_load_dword s60, s[10:11], s22\n\t"
      "s_load_dword s61, s[10:11], s22 offset:8192\n\t"
      "global_load_dword v63, v62, s[12:13] sc1\n\t"
      "s_mov_b64 s[40:41], s[4:5]\n\t"
      "s_add_u32 m0, s18, 25088\n\t"
      "s_add_u32 s48, s15, 0\n\t"
      "buffer_load_dwordx4 v2, s[40:43], s48 offen nt lds\n\t"
      "buffer_load_dwordx4 v3, s[40:43], s48 offen offset:1024 nt lds\n\t"
      "buffer_load_dwordx4 v4, s[40:43], s48 offen offset:2048 nt lds\n\t"
      "s_add_u32 m0, s18, 37376\n\t"
      "s_add_u32 s48, s15, 0\n\t"
      "buffer_load_dwordx4 v5, s[40:43], s48 offen nt lds\n\t"
      "buffer_load_dwordx4 v6, s[40:43], s48 offen offset:1024 nt lds\n\t"
      "buffer_load_dwordx4 v7, s[40:43], s48 offen offset:2048 nt lds\n\t"
      "s_barrier\n\t"
      "s_mov_b64 s[62:63], 0\n\t"
      "s_movk_i32 s64, 120\n\t"
      "s_mov_b64 vcc, -1\n\t"
      "s_branch .Lsweep_%=\n\t"
      ".Lswept_%=:\n\t"
      "s_min_u32 s62, s62, 7\n\t"
      "s_min_u32 s63, s63, 7\n\t"
      "s_lshl_b32 s63, s63, 8\n\t"
      "s_or_b32 s30, s62, s63\n\t"
      "s_sub_u32 s31, 0x808, s30\n\t"
      "s_add_u32 s30, s30, 0x10808\n\t"
      "s_getreg_b32 s62, hwreg(HW_REG_XCC_ID, 2, 1)\n\t"
      "s_cmp_lg_u32 s62, 0\n\t"
      "s_cselect_b32 s91, s30, s31\n\t"
      "s_mov_b64 s[84:85], s[8:9]\n\t"
      "v_mul_u32_u24 v16, 49152, v1\n\t"
      "v_add_u32 v16, s21, v16\n\t"
      "v_lshrrev_b32 v16, 21, v16\n\t"
      "v_lshlrev_b32 v16, 2, v16\n\t"
      "v_accvgpr_write_b32 a248, v16\n\t"
      "s_branch .Lgo0_%=\n\t"
      ".Lno0_%=:\n\t"
      "s_movk_i32 s82, 768\n\t"
      "s_mov_b32 s83, 0x00020000\n\t"
      "v_and_b32 v234, 0x3f0, v1\n\t"
      "s_waitcnt lgkmcnt(0)\n\t"
      "s_mov_b64 s[84:85], s[8:9]\n\t"
      "s_mov_b64 s[80:81], s[6:7]\n\t"
      "s_mov_b64 s[40:41], s[4:5]\n\t"
      "s_barrier\n\t"
      "buffer_load_dwordx4 a[200:203], v234, s[84:87], s23 offen offset:0\n\t"
      "buffer_load_dwordx4 a[204:207], v234, s[84:87], s23 offen offset:1024\n\t"
      "buffer_load_dwordx4 a[208:211], v234, s[84:87], s23 offen offset:2048\n\t"
      "buffer_load_ushort v224, v8, s[80:83], 0 offen\n\t"
      "buffer_load_ushort v225, v8, s[80:83], 0 offen offset:8\n\t"
      "s_add_u32 m0, s18, 25088\n\t"
      "s_add_u32 s48, s15, 0\n\t"
      "buffer_load_dwordx4 v2, s[40:43], s48 offen nt lds\n\t"
      "buffer_load_dwordx4 v3, s[40:43], s48 offen offset:1024 nt lds\n\t"
      "buffer_load_dwordx4 v4, s[40:43], s48 offen offset:2048 nt lds\n\t"
      "s_add_u32 m0, s18, 37376\n\t"
      "s_add_u32 s48, s15, 0\n\t"
      "buffer_load_dwordx4 v5, s[40:43], s48 offen nt lds\n\t"
      "buffer_load_dwordx4 v6, s[40:43], s48 offen offset:1024 nt lds\n\t"
      "buffer_load_dwordx4 v7, s[40:43], s48 offen offset:2048 nt lds\n\t"
      ".Lgo0_%=:\n\t"
      "s_getpc_b64 s[36:37]\n\t"
      ".Lpc_%=:\n\t"
      "s_lshr_b32 s30, s17, 5\n\t"
      "v_lshlrev_b32 v16, 4, v233\n\t"
      "v_add_u32 v17, 0, v233\n\t"
      "v_mul_u32_u24 v17, 171, v17\n\t"
      "v_lshrrev_b32 v17, 11, v17\n\t"
      "v_lshlrev_b32 v18, 2, v17\n\t"
      "v_and_b32 v18, 32, v18\n\t"
      "v_xor_b32 v18, v18, v16\n\t"
      "v_mul_u32_u24 v17, 576, v17\n\t"
      "v_add3_u32 v9, v17, s30, v18\n\t"
      "v_add_u32 v17, 64, v233\n\t"
      "v_mul_u32_u24 v17, 171, v17\n\t"
      "v_lshrrev_b32 v17, 11, v17\n\t"
      "v_lshlrev_b32 v18, 2, v17\n\t"
      "v_and_b32 v18, 32, v18\n\t"
      "v_xor_b32 v18, v18, v16\n\t"
      "v_mul_u32_u24 v17, 576, v17\n\t"
      "v_add3_u32 v10, v17, s30, v18\n\t"
      "v_add_u32 v17, 128, v233\n\t"
      "v_mul_u32_u24 v17, 171, v17\n\t"
      "v_lshrrev_b32 v17, 11, v17\n\t"
      "v_lshlrev_b32 v18, 2, v17\n\t"
      "v_and_b32 v18, 32, v18\n\t"
      "v_xor_b32 v18, v18, v16\n\t"
      "v_mul_u32_u24 v17, 576, v17\n\t"
      "v_add3_u32 v11, v17, s30, v18\n\t"
      "s_mov_b64 s[44:45], s[10:11]\n\t"
      "s_mov_b32 s46, 0\n\t"
      "s_mov_b32 s47, 0x00020000\n\t"
      "s_lshl_b32 s30, s24, 6\n\t"
      "s_add_u32 s31, s30, 0\n\t"
      "s_min_u32 s31, s31, .Lend_%=-.Lpc_%=-4\n\t"
      "s_load_dword s35, s[36:37], s31\n\t"
      "s_add_u32 s31, s30, 8192\n\t"
      "s_min_u32 s31, s31, .Lend_%=-.Lpc_%=-4\n\t"
      "s_load_dword s35, s[36:37], s31\n\t"
      "s_add_u32 s31, s30, 16384\n\t"
      "s_min_u32 s31, s31, .Lend_%=-.Lpc_%=-4\n\t"
      "s_load_dword s35, s[36:37], s31\n\t"
      "s_add_u32 s31, s30, 24576\n\t"
      "s_min_u32 s31, s31, .Lend_%=-.Lpc_%=-4\n\t"
      "s_load_dword s35, s[36:37], s31\n\t"
      "s_add_u32 s31, s30, 32768\n\t"
      "s_min_u32 s31, s31, .Lend_%=-.Lpc_%=-4\n\t"
      "s_load_dword s35, s[36:37], s31\n\t"
      "s_and_b32 s31, s24, 63\n\t"
      "s_lshl_b32 s31, s31, 21\n\t"
      "s_min_u32 s31, s31, 113246144\n\t"
      "s_bitcmp1_b32 s24, 6\n\t"
      "s_cselect_b64 s[32:33], s[44:45], s[40:41]\n\t"
      "s_load_dword s35, s[32:33], s31\n\t"
      "s_lshr_b32 s30, s17, 5\n\t"
      "v_lshrrev_b32 v16, 4, v233\n\t"
      "v_and_b32 v17, 15, v233\n\t"
      "v_and_b32 v18, 3, v233\n\t"
      "v_bfe_u32 v19, v233, 2, 2\n\t"
      "v_lshl_add_u32 v19, v16, 3, v19\n\t"
      "v_mul_u32_u24 v19, 192, v19\n\t"
      "v_lshl_add_u32 v19, v18, 3, v19\n\t"
      "v_lshlrev_b32 v20, 1, v233\n\t"
      "v_and_b32 v20, 32, v20\n\t"
      "v_add3_u32 v12, v19, v20, s17\n\t"
      "v_xor_b32 v13, 32, v12\n\t"
      "v_xor_b32 v19, v16, v17\n\t"
      "v_lshlrev_b32 v19, 4, v19\n\t"
      "v_mul_u32_u24 v20, 768, v17\n\t"
      "v_add_u32 v15, v19, v20\n\t"
      "v_mul_u32_u24 v20, 784, v17\n\t"
      "v_lshl_add_u32 v20, v16, 3, v20\n\t"
      "v_add_u32 v0, s30, v20\n\t"
      "v_lshlrev_b32 v19, 6, v16\n\t"
      "v_lshl_add_u32 v19, v18, 4, v19\n\t"
      "v_add_u32 v14, 160256, v19\n\t"
      "v_and_b32 v226, 0xfffffff0, v8\n\t"
      "v_and_b32 v227, 6, v8\n\t"
      "v_lshlrev_b32 v226, 2, v226\n\t"
      "v_lshl_add_u32 v226, v227, 3, v226\n\t"
      "v_add_u32 v226, 160256, v226\n\t"
      "v_and_b32 v232, 2, v8\n\t"
      "v_lshlrev_b32 v232, 3, v232\n\t"
      "v_cmp_gt_u32 vcc, 4, v227\n\t"
      "v_mov_b32 v56, v15\n\t"
      "v_xor_b32 v57, 64, v15\n\t"
      "v_xor_b32 v58, 128, v15\n\t"
      "v_xor_b32 v59, 192, v15\n\t"
      "v_add_u32 v60, 86528, v12\n\t"
      "v_add_u32 v61, 86528, v13\n\t"
      "s_waitcnt vmcnt(6)\n\t"
      "s_bfe_u32 s30, s19, 0x30008\n\t"
      "s_bfe_u32 s31, s19, 0x20002\n\t"
      "s_lshl2_add_u32 s30, s30, s31\n\t"
      "v_readlane_b32 s90, v63, s30\n\t"
      "v_lshlrev_b32 v224, v232, v224\n\t"
      "v_lshlrev_b32 v225, v232, v225\n\t"
      "v_cndmask_b32 v228, 0, v224, vcc\n\t"
      "v_cndmask_b32 v230, 0, v225, vcc\n\t"
      "v_sub_u32 v229, v224, v228\n\t"
      "v_sub_u32 v231, v225, v230\n\t"
      "ds_write_b128 v226, v[228:231]\n\t"
      "s_add_u32 s90, s90, 0x20000\n\t"
      "s_andn2_b32 s90, s90, 0x1ffff\n\t"
      "s_or_b32 s90, s90, s91\n\t"
      "s_or_b32 s30, s17, s16\n\t"
      "s_cbranch_scc1 .Lno2_%=\n\t"
      "v_mov_b32 v234, s90\n\t"
      "v_mov_b32 v235, s19\n\t"
      "global_store_dword v235, v234, s[12:13] sc1\n\t"
      ".Lno2_%=:\n\t"
      "s_add_u32 m0, s17, 0\n\t"
      "s_mov_b32 s54, 0\n\t"
      "buffer_load_dwordx4 v9, s[84:87], s54 offen lds\n\t"
      "buffer_load_dwordx4 v10, s[84:87], s54 offen offset:1024 lds\n\t"
      "buffer_load_dwordx4 v11, s[84:87], s54 offen offset:2048 lds\n\t"
      "s_add_u32 m0, s17, 3072\n\t"
      "s_mov_b32 s54, 12288\n\t"
      "buffer_load_dwordx4 v9, s[84:87], s54 offen lds\n\t"
      "buffer_load_dwordx4 v10, s[84:87], s54 offen offset:1024 lds\n\t"
      "buffer_load_dwordx4 v11, s[84:87], s54 offen offset:2048 lds\n\t"
      "s_add_u32 m0, s17, 86528\n\t"
      "s_mov_b32 s54, 24576\n\t"
      "buffer_load_dwordx4 v9, s[84:87], s54 offen lds\n\t"
      "buffer_load_dwordx4 v10, s[84:87], s54 offen offset:1024 lds\n\t"
      "buffer_load_dwordx4 v11, s[84:87], s54 offen offset:2048 lds\n\t"
      "s_add_u32 m0, s17, 89600\n\t"
      "s_mov_b32 s54, 36864\n\t"
      "buffer_load_dwordx4 v9, s[84:87], s54 offen lds\n\t"
      "buffer_load_dwordx4 v10, s[84:87], s54 offen offset:1024 lds\n\t"
      "buffer_load_dwordx4 v11, s[84:87], s54 offen offset:2048 lds\n\t"
      "s_waitcnt lgkmcnt(0)\n\t"
      "s_waitcnt vmcnt(12)\n\t"
      "s_barrier\n\t"
      "s_waitcnt vmcnt(6)\n\t"
      "ds_read_b128 a[180:183], v14 offset:0\n\t"
      "ds_read_b64_tr_b16 v[64:65], v12 offset:0\n\t"
      "ds_read_b64_tr_b16 v[66:67], v12 offset:768\n\t"
      "ds_read_b64_tr_b16 v[68:69], v13 offset:0\n\t"
      "ds_read_b64_tr_b16 v[70:71], v13 offset:768\n\t"
      "ds_read_b64_tr_b16 v[72:73], v12 offset:64\n\t"
      "ds_read_b64_tr_b16 v[74:75], v12 offset:832\n\t"
      "ds_read_b64_tr_b16 v[76:77], v13 offset:64\n\t"
      "ds_read_b64_tr_b16 v[78:79], v13 offset:832\n\t"
      "ds_read_b64_tr_b16 a[60:61], v12 offset:128\n\t"
      "ds_read_b64_tr_b16 a[62:63], v12 offset:896\n\t"
      "ds_read_b64_tr_b16 a[64:65], v13 offset:128\n\t"
      "ds_read_b64_tr_b16 a[66:67], v13 offset:896\n\t"
      "s_add_u32 m0, s17, 111104\n\t"
      "s_mov_b32 s54, 49152\n\t"
      "buffer_load_dwordx4 v9, s[84:87], s54 offen lds\n\t"
      "buffer_load_dwordx4 v10, s[84:87], s54 offen offset:1024 lds\n\t"
      "buffer_load_dwordx4 v11, s[84:87], s54 offen offset:2048 lds\n\t"
      "s_add_u32 m0, s17, 114176\n\t"
      "s_mov_b32 s54, 61440\n\t"
      "buffer_load_dwordx4 v9, s[84:87], s54 offen lds\n\t"
      "buffer_load_dwordx4 v10, s[84:87], s54 offen offset:1024 lds\n\t"
      "buffer_load_dwordx4 v11, s[84:87], s54 offen offset:2048 lds\n\t"
      "s_waitcnt vmcnt(6)\n\t"
      "s_waitcnt lgkmcnt(0)\n\t"
      "; step\n\t"
      "v_mfma_f32_4x4x4_16b_bf16 v[40:43], a[180:181], v[64:65], 0\n\t"
      "ds_read_b128 a[184:187], v14 offset:256\n\t"
      "v_mfma_f32_4x4x4_16b_bf16 v[44:47], a[182:183], v[66:67], 0\n\t"
      "ds_read_b64_tr_b16 v[80:81], v60 offset:0\n\t"
      "v_mfma_f32_4x4x4_16b_bf16 v[48:51], a[180:181], v[68:69], 0\n\t"
      "ds_read_b64_tr_b16 v[82:83], v60 offset:768\n\t"
      "v_mfma_f32_4x4x4_16b_bf16 v[52:55], a[182:183], v[70:71], 0\n\t"
      "ds_read_b64_tr_b16 v[84:85], v61 offset:0\n\t"
      "ds_read_b64_tr_b16 v[86:87], v61 offset:768\n\t"
      "s_add_u32 m0, s17, 135680\n\t"
      "s_mov_b32 s54, 73728\n\t"
      "buffer_load_dwordx4 v9, s[84:87], s54 offen lds\n\t"
      "buffer_load_dwordx4 v10, s[84:87], s54 offen offset:1024 lds\n\t"
      "v_cvt_pk_bf16_f32 v64, v40, v41\n\t"
      "v_cvt_pk_bf16_f32 v65, v42, v43\n\t"
      "v_cvt_pk_bf16_f32 v66, v44, v45\n\t"
      "v_cvt_pk_bf16_f32 v67, v46, v47\n\t"
      "v_cvt_pk_bf16_f32 v68, v48, v49\n\t"
      "v_cvt_pk_bf16_f32 v69, v50, v51\n\t"
      "v_cvt_pk_bf16_f32 v70, v52, v53\n\t"
      "v_cvt_pk_bf16_f32 v71, v54, v55\n\t"
      "v_mfma_f32_4x4x4_16b_bf16 v[40:43], a[180:181], v[72:73], 0\n\t"
      "ds_read_b64_tr_b16 v[88:89], v60 offset:64\n\t"
      "v_mfma_f32_4x4x4_16b_bf16 v[44:47], a[182:183], v[74:75], 0\n\t"
      "ds_read_b64_tr_b16 v[90:91], v60 offset:832\n\t"
      "v_mfma_f32_4x4x4_16b_bf16 v[48:51], a[180:181], v[76:77], 0\n\t"
      "ds_read_b64_tr_b16 v[92:93], v61 offset:64\n\t"
      "v_mfma_f32_4x4x4_16b_bf16 v[52:55], a[182:183], v[78:79], 0\n\t"
      "ds_read_b64_tr_b16 v[94:95], v61 offset:832\n\t"
      "buffer_load_dwordx4 v11, s[84:87], s54 offen offset:2048 lds\n\t"
      "s_add_u32 m0, s17, 138752\n\t"
      "s_mov_b32 s54, 86016\n\t"
      "buffer_load_dwordx4 v9, s[84:87], s54 offen lds\n\t"
      "v_cvt_pk_bf16_f32 v72, v40, v41\n\t"
      "v_cvt_pk_bf16_f32 v73, v42, v43\n\t"
      "v_cvt_pk_bf16_f32 v74, v44, v45\n\t"
      "v_cvt_pk_bf16_f32 v75, v46, v47\n\t"
      "v_cvt_pk_bf16_f32 v76, v48, v49\n\t"
      "v_cvt_pk_bf16_f32 v77, v50, v51\n\t"
      "v_cvt_pk_bf16_f32 v78, v52, v53\n\t"
      "v_cvt_pk_bf16_f32 v79, v54, v55\n\t"
      "ds_read_b128 a[224:227], v56 offset:25088\n\t"
      "v_mfma_f32_4x4x4_16b_bf16 v[40:43], a[180:181], a[60:61], 0\n\t"
      "ds_read_b64_tr_b16 a[68:69], v60 offset:128\n\t"
      "v_mfma_f32_4x4x4_16b_bf16 v[44:47], a[182:183], a[62:63], 0\n\t"
      "ds_read_b64_tr_b16 a[70:71], v60 offset:896\n\t"
      "v_mfma_f32_4x4x4_16b_bf16 v[48:51], a[180:181], a[64:65], 0\n\t"
      "ds_read_b64_tr_b16 a[72:73], v61 offset:128\n\t"
      "v_mfma_f32_4x4x4_16b_bf16 v[52:55], a[182:183], a[66:67], 0\n\t"
      "ds_read_b64_tr_b16 a[74:75], v61 offset:896\n\t"
      "buffer_load_dwordx4 v10, s[84:87], s54 offen offset:1024 lds\n\t"
      "buffer_load_dwordx4 v11, s[84:87], s54 offen offset:2048 lds\n\t"
      "ds_read_b128 a[228:231], v56 offset:37376\n\t"
      "v_cvt_pk_bf16_f32 v40, v40, v41\n\t"
      "v_cvt_pk_bf16_f32 v41, v42, v43\n\t"
      "v_cvt_pk_bf16_f32 v44, v44, v45\n\t"
      "v_cvt_pk_bf16_f32 v45, v46, v47\n\t"
      "v_cvt_pk_bf16_f32 v48, v48, v49\n\t"
      "v_cvt_pk_bf16_f32 v49, v50, v51\n\t"
      "v_cvt_pk_bf16_f32 v52, v52, v53\n\t"
      "v_cvt_pk_bf16_f32 v53, v54, v55\n\t"
      "v_accvgpr_write_b32 a60, v40\n\t"
      "v_accvgpr_write_b32 a61, v41\n\t"
      "v_accvgpr_write_b32 a62, v44\n\t"
      "v_accvgpr_write_b32 a63, v45\n\t"
      "v_accvgpr_write_b32 a64, v48\n\t"
      "v_accvgpr_write_b32 a65, v49\n\t"
      "v_accvgpr_write_b32 a66, v52\n\t"
      "v_accvgpr_write_b32 a67, v53\n\t"
      "s_waitcnt vmcnt(6)\n\t"
      "s_waitcnt lgkmcnt(1)\n\t"
      "; step\n\t"
      "v_mfma_f32_4x4x4_16b_bf16 v[40:43], a[184:185], v[80:81], 0\n\t"
      "ds_read_b128 a[180:183], v14 offset:512\n\t"
      "v_mfma_f32_4x4x4_16b_bf16 v[44:47], a[186:187], v[82:83], 0\n\t"
      "ds_read_b64_tr_b16 v[96:97], v60 offset:24576\n\t"
      "v_mfma_f32_4x4x4_16b_bf16 v[48:51], a[184:185], v[84:85], 0\n\t"
      "ds_read_b64_tr_b16 v[98:99], v60 offset:25344\n\t"
      "v_mfma_f32_4x4x4_16b_bf16 v[52:55], a[186:187], v[86:87], 0\n\t"
      "ds_read_b64_tr_b16 v[100:101], v61 offset:24576\n\t"
      "ds_read_b64_tr_b16 v[102:103], v61 offset:25344\n\t"
      "s_add_u32 m0, s17, 0\n\t"
      "s_mov_b32 s54, 98304\n\t"
      "buffer_load_dwordx4 v9, s[84:87], s54 offen lds\n\t"
      "buffer_load_dwordx4 v10, s[84:87], s54 offen offset:1024 lds\n\t"
      "v_mfma_f32_16x16x32_bf16 v[16:19], v[64:67], a[224:227], 0\n\t"
      "v_cvt_pk_bf16_f32 v80, v40, v41\n\t"
      "v_cvt_pk_bf16_f32 v81, v42, v43\n\t"
      "v_mfma_f32_16x16x32_bf16 v[20:23], v[68:71], a[224:227], 0\n\t"
      "v_cvt_pk_bf16_f32 v82, v44, v45\n\t"
      "v_cvt_pk_bf16_f32 v83, v46, v47\n\t"
      "v_mfma_f32_16x16x32_bf16 v[24:27], v[72:75], a[224:227], 0\n\t"
      "v_cvt_pk_bf16_f32 v84, v48, v49\n\t"
      "v_cvt_pk_bf16_f32 v85, v50, v51\n\t"
      "v_mfma_f32_16x16x32_bf16 v[28:31], v[76:79], a[224:227], 0\n\t"
      "v_cvt_pk_bf16_f32 v86, v52, v53\n\t"
      "v_cvt_pk_bf16_f32 v87, v54, v55\n\t"
      "v_mfma_f32_4x4x4_16b_bf16 v[40:43], a[184:185], v[88:89], 0\n\t"
      "ds_read_b64_tr_b16 v[104:105], v60 offset:24640\n\t"
      "v_mfma_f32_4x4x4_16b_bf16 v[44:47], a[186:187], v[90:91], 0\n\t"
      "ds_read_b64_tr_b16 v[106:107], v60 offset:25408\n\t"
      "v_mfma_f32_4x4x4_16b_bf16 v[48:51], a[184:185], v[92:93], 0\n\t"
      "ds_read_b64_tr_b16 v[108:109], v61 offset:24640\n\t"
      "v_mfma_f32_4x4x4_16b_bf16 v[52:55], a[186:187], v[94:95], 0\n\t"
      "ds_read_b64_tr_b16 v[110:111], v61 offset:25408\n\t"
      "buffer_load_dwordx4 v11, s[84:87], s54 offen offset:2048 lds\n\t"
      "s_add_u32 m0, s17, 3072\n\t"
      "s_mov_b32 s54, 110592\n\t"
      "buffer_load_dwordx4 v9, s[84:87], s54 offen lds\n\t"
      "v_mfma_f32_16x16x32_bf16 v[32:35], a[60:63], a[224:227], 0\n\t"
      "v_cvt_pk_bf16_f32 v88, v40, v41\n\t"
      "v_cvt_pk_bf16_f32 v89, v42, v43\n\t"
      "v_mfma_f32_16x16x32_bf16 v[36:39], a[64:67], a[224:227], 0\n\t"
      "v_cvt_pk_bf16_f32 v90, v44, v45\n\t"
      "v_cvt_pk_bf16_f32 v91, v46, v47\n\t"
      "s_waitcnt lgkmcnt(9)\n\t"
      "v_mfma_f32_16x16x32_bf16 a[156:159], v[64:67], a[228:231], 0\n\t"
      "v_cvt_pk_bf16_f32 v92, v48, v49\n\t"
      "v_cvt_pk_bf16_f32 v93, v50, v51\n\t"
      "v_mfma_f32_16x16x32_bf16 a[160:163], v[68:71], a[228:231], 0\n\t"
      "v_cvt_pk_bf16_f32 v94, v52, v53\n\t"
      "v_cvt_pk_bf16_f32 v95, v54, v55\n\t"
      "ds_read_b128 a[232:235], v57 offset:25088\n\t"
      "v_mfma_f32_4x4x4_16b_bf16 v[40:43], a[184:185], a[68:69], 0\n\t"
      "ds_read_b64_tr_b16 a[76:77], v60 offset:24704\n\t"
      "v_mfma_f32_4x4x4_16b_bf16 v[44:47], a[186:187], a[70:71], 0\n\t"
      "ds_read_b64_tr_b16 a[78:79], v60 offset:25472\n\t"
      "v_mfma_f32_4x4x4_16b_bf16 v[48:51], a[184:185], a[72:73], 0\n\t"
      "ds_read_b64_tr_b16 a[80:81], v61 offset:24704\n\t"
      "v_mfma_f32_4x4x4_16b_bf16 v[52:55], a[186:187], a[74:75], 0\n\t"
      "ds_read_b64_tr_b16 a[82:83], v61 offset:25472\n\t"
      "buffer_load_dwordx4 v10, s[84:87], s54 offen offset:1024 lds\n\t"
      "buffer_load_dwordx4 v11, s[84:87], s54 offen offset:2048 lds\n\t"
      "ds_read_b128 a[236:239], v57 offset:37376\n\t"
      "v_mfma_f32_16x16x32_bf16 a[164:167], v[72:75], a[228:231], 0\n\t"
      "v_cvt_pk_bf16_f32 v40, v40, v41\n\t"
      "v_cvt_pk_bf16_f32 v41, v42, v43\n\t"
      "v_mfma_f32_16x16x32_bf16 a[168:171], v[76:79], a[228:231], 0\n\t"
      "v_cvt_pk_bf16_f32 v44, v44, v45\n\t"
      "v_cvt_pk_bf16_f32 v45, v46, v47\n\t"
      "v_mfma_f32_16x16x32_bf16 a[172:175], a[60:63], a[228:231], 0\n\t"
      "v_cvt_pk_bf16_f32 v48, v48, v49\n\t"
      "v_cvt_pk_bf16_f32 v49, v50, v51\n\t"
      "v_mfma_f32_16x16x32_bf16 a[176:179], a[64:67], a[228:231], 0\n\t"
      "v_cvt_pk_bf16_f32 v52, v52, v53\n\t"
      "v_cvt_pk_bf16_f32 v53, v54, v55\n\t"
      "v_accvgpr_write_b32 a68, v40\n\t"
      "v_accvgpr_write_b32 a69, v41\n\t"
      "v_accvgpr_write_b32 a70, v44\n\t"
      "v_accvgpr_write_b32 a71, v45\n\t"
      "v_accvgpr_write_b32 a72, v48\n\t"
      "v_accvgpr_write_b32 a73, v49\n\t"
      "v_accvgpr_write_b32 a74, v52\n\t"
      "v_accvgpr_write_b32 a75, v53\n\t"
      "s_waitcnt vmcnt(6)\n\t"
      "s_waitcnt lgkmcnt(1)\n\t"
      "; step\n\t"
      "v_mfma_f32_4x4x4_16b_bf16 v[40:43], a[180:181], v[96:97], 0\n\t"
      "ds_read_b128 a[184:187], v14 offset:768\n\t"
      "v_mfma_f32_4x4x4_16b_bf16 v[44:47], a[182:183], v[98:99], 0\n\t"
      "ds_read_b64_tr_b16 v[112:113], v60 offset:49152\n\t"
      "v_mfma_f32_4x4x4_16b_bf16 v[48:51], a[180:181], v[100:101], 0\n\t"
      "ds_read_b64_tr_b16 v[114:115], v60 offset:49920\n\t"
      "v_mfma_f32_4x4x4_16b_bf16 v[52:55], a[182:183], v[102:103], 0\n\t"
      "ds_read_b64_tr_b16 v[116:117], v61 offset:49152\n\t"
      "ds_read_b64_tr_b16 v[118:119], v61 offset:49920\n\t"
      "s_add_u32 m0, s17, 86528\n\t"
      "s_mov_b32 s54, 122880\n\t"
      "buffer_load_dwordx4 v9, s[84:87], s54 offen lds\n\t"
      "buffer_load_dwordx4 v10, s[84:87], s54 offen offset:1024 lds\n\t"
      "v_mfma_f32_16x16x32_bf16 v[16:19], v[80:83], a[232:235], v[16:19]\n\t"
      "v_cvt_pk_bf16_f32 v96, v40, v41\n\t"
      "v_cvt_pk_bf16_f32 v97, v42, v43\n\t"
      "v_mfma_f32_16x16x32_bf16 v[20:23], v[84:87], a[232:235], v[20:23]\n\t"
      "v_cvt_pk_bf16_f32 v98, v44, v45\n\t"
      "v_cvt_pk_bf16_f32 v99, v46, v47\n\t"
      "v_mfma_f32_16x16x32_bf16 v[24:27], v[88:91], a[232:235], v[24:27]\n\t"
      "v_cvt_pk_bf16_f32 v100, v48, v49\n\t"
      "v_cvt_pk_bf16_f32 v101, v50, v51\n\t"
      "v_mfma_f32_16x16x32_bf16 v[28:31], v[92:95], a[232:235], v[28:31]\n\t"
      "v_cvt_pk_bf16_f32 v102, v52, v53\n\t"
      "v_cvt_pk_bf16_f32 v103, v54, v55\n\t"
      "v_mfma_f32_4x4x4_16b_bf16 v[40:43], a[180:181], v[104:105], 0\n\t"
      "ds_read_b64_tr_b16 v[120:121], v60 offset:49216\n\t"
      "v_mfma_f32_4x4x4_16b_bf16 v[44:47], a[182:183], v[106:107], 0\n\t"
      "ds_read_b64_tr_b16 v[122:123], v60 offset:49984\n\t"
      "v_mfma_f32_4x4x4_16b_bf16 v[48:51], a[180:181], v[108:109], 0\n\t"
      "ds_read_b64_tr_b16 v[124:125], v61 offset:49216\n\t"
      "v_mfma_f32_4x4x4_16b_bf16 v[52:55], a[182:183], v[110:111], 0\n\t"
      "ds_read_b64_tr_b16 v[126:127], v61 offset:49984\n\t"
      "buffer_load_dwordx4 v11, s[84:87], s54 offen offset:2048 lds\n\t"
      "s_add_u32 m0, s17, 89600\n\t"
      "s_mov_b32 s54, 135168\n\t"
      "buffer_load_dwordx4 v9, s[84:87], s54 offen lds\n\t"
      "v_mfma_f32_16x16x32_bf16 v[32:35], a[68:71], a[232:235], v[32:35]\n\t"
      "v_cvt_pk_bf16_f32 v104, v40, v41\n\t"
      "v_cvt_pk_bf16_f32 v105, v42, v43\n\t"
      "v_mfma_f32_16x16x32_bf16 v[36:39], a[72:75], a[232:235], v[36:39]\n\t"
      "v_cvt_pk_bf16_f32 v106, v44, v45\n\t"
      "v_cvt_pk_bf16_f32 v107, v46, v47\n\t"
      "s_waitcnt lgkmcnt(9)\n\t"
      "v_mfma_f32_16x16x32_bf16 a[156:159], v[80:83], a[236:239], a[156:159]\n\t"
      "v_cvt_pk_bf16_f32 v108, v48, v49\n\t"
      "v_cvt_pk_bf16_f32 v109, v50, v51\n\t"
      "v_mfma_f32_16x16x32_bf16 a[160:163], v[84:87], a[236:239], a[160:163]\n\t"
      "v_cvt_pk_bf16_f32 v110, v52, v53\n\t"
      "v_cvt_pk_bf16_f32 v111, v54, v55\n\t"
      "ds_read_b128 a[240:243], v58 offset:25088\n\t"
      "v_mfma_f32_4x4x4_16b_bf16 v[40:43], a[180:181], a[76:77], 0\n\t"
      "ds_read_b64_tr_b16 a[84:85], v60 offset:49280\n\t"
      "v_mfma_f32_4x4x4_16b_bf16 v[44:47], a[182:183], a[78:79], 0\n\t"
      "ds_read_b64_tr_b16 a[86:87], v60 offset:50048\n\t"
      "v_mfma_f32_4x4x4_16b_bf16 v[48:51], a[180:181], a[80:81], 0\n\t"
      "ds_read_b64_tr_b16 a[88:89], v61 offset:49280\n\t"
      "v_mfma_f32_4x4x4_16b_bf16 v[52:55], a[182:183], a[82:83], 0\n\t"
      "ds_read_b64_tr_b16 a[90:91], v61 offset:50048\n\t"
      "buffer_load_dwordx4 v10, s[84:87], s54 offen offset:1024 lds\n\t"
      "buffer_load_dwordx4 v11, s[84:87], s54 offen offset:2048 lds\n\t"
      "ds_read_b128 a[244:247], v58 offset:37376\n\t"
      "v_mfma_f32_16x16x32_bf16 a[164:167], v[88:91], a[236:239], a[164:167]\n\t"
      "v_cvt_pk_bf16_f32 v40, v40, v41\n\t"
      "v_cvt_pk_bf16_f32 v41, v42, v43\n\t"
      "v_mfma_f32_16x16x32_bf16 a[168:171], v[92:95], a[236:239], a[168:171]\n\t"
      "v_cvt_pk_bf16_f32 v44, v44, v45\n\t"
      "v_cvt_pk_bf16_f32 v45, v46, v47\n\t"
      "v_mfma_f32_16x16x32_bf16 a[172:175], a[68:71], a[236:239], a[172:175]\n\t"
      "v_cvt_pk_bf16_f32 v48, v48, v49\n\t"
      "v_cvt_pk_bf16_f32 v49, v50, v51\n\t"
      "v_mfma_f32_16x16x32_bf16 a[176:179], a[72:75], a[236:239], a[176:179]\n\t"
      "v_cvt_pk_bf16_f32 v52, v52, v53\n\t"
      "v_cvt_pk_bf16_f32 v53, v54, v55\n\t"
      "v_accvgpr_write_b32 a76, v40\n\t"
      "v_accvgpr_write_b32 a77, v41\n\t"
      "v_accvgpr_write_b32 a78, v44\n\t"
      "v_accvgpr_write_b32 a79, v45\n\t"
      "v_accvgpr_write_b32 a80, v48\n\t"
      "v_accvgpr_write_b32 a81, v49\n\t"
      "v_accvgpr_write_b32 a82, v52\n\t"
      "v_accvgpr_write_b32 a83, v53\n\t"
      "s_waitcnt vmcnt(6)\n\t"
      "s_waitcnt lgkmcnt(1)\n\t"
      "; step\n\t"
      "v_mfma_f32_4x4x4_16b_bf16 v[40:43], a[184:185], v[112:113], 0\n\t"
      "ds_read_b128 a[180:183], v14 offset:1024\n\t"
      "v_mfma_f32_4x4x4_16b_bf16 v[44:47], a[186:187], v[114:115], 0\n\t"
      "ds_read_b64_tr_b16 v[128:129], v12 offset:0\n\t"
      "v_mfma_f32_4x4x4_16b_bf16 v[48:51], a[184:185], v[116:117], 0\n\t"
      "ds_read_b64_tr_b16 v[130:131], v12 offset:768\n\t"
      "v_mfma_f32_4x4x4_16b_bf16 v[52:55], a[186:187], v[118:119], 0\n\t"
      "ds_read_b64_tr_b16 v[132:133], v13 offset:0\n\t"
      "ds_read_b64_tr_b16 v[134:135], v13 offset:768\n\t"
      "s_add_u32 m0, s17, 111104\n\t"
      "s_mov_b32 s54, 147456\n\t"
      "buffer_load_dwordx4 v9, s[84:87], s54 offen lds\n\t"
      "buffer_load_dwordx4 v10, s[84:87], s54 offen offset:1024 lds\n\t"
      "v_mfma_f32_16x16x32_bf16 v[16:19], v[96:99], a[240:243], v[16:19]\n\t"
      "v_cvt_pk_bf16_f32 v112, v40, v41\n\t"
      "v_cvt_pk_bf16_f32 v113, v42, v43\n\t"
      "v_mfma_f32_16x16x32_bf16 v[20:23], v[100:103], a[240:243], v[20:23]\n\t"
      "v_cvt_pk_bf16_f32 v114, v44, v45\n\t"
      "v_cvt_pk_bf16_f32 v115, v46, v47\n\t"
      "v_mfma_f32_16x16x32_bf16 v[24:27], v[104:107], a[240:243], v[24:27]\n\t"
      "v_cvt_pk_bf16_f32 v116, v48, v49\n\t"
      "v_cvt_pk_bf16_f32 v117, v50, v51\n\t"
      "v_mfma_f32_16x16x32_bf16 v[28:31], v[108:111], a[240:243], v[28:31]\n\t"
      "v_cvt_pk_bf16_f32 v118, v52, v53\n\t"
      "v_cvt_pk_bf16_f32 v119, v54, v55\n\t"
      "v_mfma_f32_4x4x4_16b_bf16 v[40:43], a[184:185], v[120:121], 0\n\t"
      "ds_read_b64_tr_b16 v[136:137], v12 offset:64\n\t"
      "v_mfma_f32_4x4x4_16b_bf16 v[44:47], a[186:187], v[122:123], 0\n\t"
      "ds_read_b64_tr_b16 v[138:139], v12 offset:832\n\t"
      "v_mfma_f32_4x4x4_16b_bf16 v[48:51], a[184:185], v[124:125], 0\n\t"
      "ds_read_b64_tr_b16 v[140:141], v13 offset:64\n\t"
      "v_mfma_f32_4x4x4_16b_bf16 v[52:55], a[186:187], v[126:127], 0\n\t"
      "ds_read_b64_tr_b16 v[142:143], v13 offset:832\n\t"
      "buffer_load_dwordx4 v11, s[84:87], s54 offen offset:2048 lds\n\t"
      "s_add_u32 m0, s17, 114176\n\t"
      "s_mov_b32 s54, 159744\n\t"
      "buffer_load_dwordx4 v9, s[84:87], s54 offen lds\n\t"
      "v_mfma_f32_16x16x32_bf16 v[32:35], a[76:79], a[240:243], v[32:35]\n\t"
      "v_cvt_pk_bf16_f32 v120, v40, v41\n\t"
      "v_cvt_pk_bf16_f32 v121, v42, v43\n\t"
      "v_mfma_f32_16x16x32_bf16 v[36:39], a[80:83], a[240:243], v[36:39]\n\t"
      "v_cvt_pk_bf16_f32 v122, v44, v45\n\t"
      "v_cvt_pk_bf16_f32 v123, v46, v47\n\t"
      "s_waitcnt lgkmcnt(9)\n\t"
      "v_mfma_f32_16x16x32_bf16 a[156:159], v[96:99], a[244:247], a[156:159]\n\t"
      "v_cvt_pk_bf16_f32 v124, v48, v49\n\t"
      "v_cvt_pk_bf16_f32 v125, v50, v51\n\t"
      "v_mfma_f32_16x16x32_bf16 a[160:163], v[100:103], a[244:247], a[160:163]\n\t"
      "v_cvt_pk_bf16_f32 v126, v52, v53\n\t"
      "v_cvt_pk_bf16_f32 v127, v54, v55\n\t"
      "ds_read_b128 a[224:227], v59 offset:25088\n\t"
      "v_mfma_f32_4x4x4_16b_bf16 v[40:43], a[184:185], a[84:85], 0\n\t"
      "ds_read_b64_tr_b16 a[92:93], v12 offset:128\n\t"
      "v_mfma_f32_4x4x4_16b_bf16 v[44:47], a[186:187], a[86:87], 0\n\t"
      "ds_read_b64_tr_b16 a[94:95], v12 offset:896\n\t"
      "v_mfma_f32_4x4x4_16b_bf16 v[48:51], a[184:185], a[88:89], 0\n\t"
      "ds_read_b64_tr_b16 a[96:97], v13 offset:128\n\t"
      "v_mfma_f32_4x4x4_16b_bf16 v[52:55], a[186:187], a[90:91], 0\n\t"
      "ds_read_b64_tr_b16 a[98:99], v13 offset:896\n\t"
      "buffer_load_dwordx4 v10, s[84:87], s54 offen offset:1024 lds\n\t"
      "buffer_load_dwordx4 v11, s[84:87], s54 offen offset:2048 lds\n\t"
      "ds_read_b128 a[228:231], v59 offset:37376\n\t"
      "s_add_u32 m0, s17, 135680\n\t"
      "s_mov_b32 s54, 172032\n\t"
      "buffer_load_dwordx4 v9, s[84:87], s54 offen lds\n\t"
      "buffer_load_dwordx4 v10, s[84:87], s54 offen offset:1024 lds\n\t"
      "buffer_load_dwordx4 v11, s[84:87], s54 offen offset:2048 lds\n\t"
      "s_add_u32 m0, s17, 138752\n\t"
      "s_mov_b32 s54, 184320\n\t"
      "buffer_load_dwordx4 v9, s[84:87], s54 offen lds\n\t"
      "buffer_load_dwordx4 v10, s[84:87], s54 offen offset:1024 lds\n\t"
      "buffer_load_dwordx4 v11, s[84:87], s54 offen offset:2048 lds\n\t"
      "v_mfma_f32_16x16x32_bf16 a[164:167], v[104:107], a[244:247], a[164:167]\n\t"
      "v_cvt_pk_bf16_f32 v40, v40, v41\n\t"
      "v_cvt_pk_bf16_f32 v41, v42, v43\n\t"
      "v_mfma_f32_16x16x32_bf16 a[168:171], v[108:111], a[244:247], a[168:171]\n\t"
      "v_cvt_pk_bf16_f32 v44, v44, v45\n\t"
      "v_cvt_pk_bf16_f32 v45, v46, v47\n\t"
      "v_mfma_f32_16x16x32_bf16 a[172:175], a[76:79], a[244:247], a[172:175]\n\t"
      "v_cvt_pk_bf16_f32 v48, v48, v49\n\t"
      "v_cvt_pk_bf16_f32 v49, v50, v51\n\t"
      "v_mfma_f32_16x16x32_bf16 a[176:179], a[80:83], a[244:247], a[176:179]\n\t"
      "v_cvt_pk_bf16_f32 v52, v52, v53\n\t"
      "v_cvt_pk_bf16_f32 v53, v54, v55\n\t"
      "v_accvgpr_write_b32 a84, v40\n\t"
      "v_accvgpr_write_b32 a85, v41\n\t"
      "v_accvgpr_write_b32 a86, v44\n\t"
      "v_accvgpr_write_b32 a87, v45\n\t"
      "v_accvgpr_write_b32 a88, v48\n\t"
      "v_accvgpr_write_b32 a89, v49\n\t"
      "v_accvgpr_write_b32 a90, v52\n\t"
      "v_accvgpr_write_b32 a91, v53\n\t"
      "s_waitcnt vmcnt(12)\n\t"
      "s_waitcnt lgkmcnt(1)\n\t"
      "; step\n\t"
      "v_mfma_f32_4x4x4_16b_bf16 v[40:43], a[180:181], v[128:129], 0\n\t"
      "ds_read_b128 a[184:187], v14 offset:1280\n\t"
      "v_mfma_f32_4x4x4_16b_bf16 v[44:47], a[182:183], v[130:131], 0\n\t"
      "ds_read_b64_tr_b16 v[144:145], v60 offset:0\n\t"
      "v_mfma_f32_4x4x4_16b_bf16 v[48:51], a[180:181], v[132:133], 0\n\t"
      "ds_read_b64_tr_b16 v[146:147], v60 offset:768\n\t"
      "v_mfma_f32_4x4x4_16b_bf16 v[52:55], a[182:183], v[134:135], 0\n\t"
      "ds_read_b64_tr_b16 v[148:149], v61 offset:0\n\t"
      "ds_read_b64_tr_b16 v[150:151], v61 offset:768\n\t"
      "s_add_u32 m0, s17, 0\n\t"
      "s_mov_b32 s54, 196608\n\t"
      "buffer_load_dwordx4 v9, s[84:87], s54 offen lds\n\t"
      "buffer_load_dwordx4 v10, s[84:87], s54 offen offset:1024 lds\n\t"
      "v_mfma_f32_16x16x32_bf16 v[16:19], v[112:115], a[224:227], v[16:19]\n\t"
      "v_cvt_pk_bf16_f32 v128, v40, v41\n\t"
      "v_cvt_pk_bf16_f32 v129, v42, v43\n\t"
      "v_mfma_f32_16x16x32_bf16 v[20:23], v[116:119], a[224:227], v[20:23]\n\t"
      "v_cvt_pk_bf16_f32 v130, v44, v45\n\t"
      "v_cvt_pk_bf16_f32 v131, v46, v47\n\t"
      "v_mfma_f32_16x16x32_bf16 v[24:27], v[120:123], a[224:227], v[24:27]\n\t"
      "v_cvt_pk_bf16_f32 v132, v48, v49\n\t"
      "v_cvt_pk_bf16_f32 v133, v50, v51\n\t"
      "v_mfma_f32_16x16x32_bf16 v[28:31], v[124:127], a[224:227], v[28:31]\n\t"
      "v_cvt_pk_bf16_f32 v134, v52, v53\n\t"
      "v_cvt_pk_bf16_f32 v135, v54, v55\n\t"
      "v_mfma_f32_4x4x4_16b_bf16 v[40:43], a[180:181], v[136:137], 0\n\t"
      "ds_read_b64_tr_b16 v[152:153], v60 offset:64\n\t"
      "v_mfma_f32_4x4x4_16b_bf16 v[44:47], a[182:183], v[138:139], 0\n\t"
      "ds_read_b64_tr_b16 v[154:155], v60 offset:832\n\t"
      "v_mfma_f32_4x4x4_16b_bf16 v[48:51], a[180:181], v[140:141], 0\n\t"
      "ds_read_b64_tr_b16 v[156:157], v61 offset:64\n\t"
      "v_mfma_f32_4x4x4_16b_bf16 v[52:55], a[182:183], v[142:143], 0\n\t"
      "ds_read_b64_tr_b16 v[158:159], v61 offset:832\n\t"
      "buffer_load_dwordx4 v11, s[84:87], s54 offen offset:2048 lds\n\t"
      "s_add_u32 m0, s17, 3072\n\t"
      "s_mov_b32 s54, 208896\n\t"
      "buffer_load_dwordx4 v9, s[84:87], s54 offen lds\n\t"
      "v_mfma_f32_16x16x32_bf16 v[32:35], a[84:87], a[224:227], v[32:35]\n\t"
      "v_cvt_pk_bf16_f32 v136, v40, v41\n\t"
      "v_cvt_pk_bf16_f32 v137, v42, v43\n\t"
      "v_mfma_f32_16x16x32_bf16 v[36:39], a[88:91], a[224:227], v[36:39]\n\t"
      "v_cvt_pk_bf16_f32 v138, v44, v45\n\t"
      "v_cvt_pk_bf16_f32 v139, v46, v47\n\t"
      "s_waitcnt lgkmcnt(9)\n\t"
      "v_mfma_f32_16x16x32_bf16 a[156:159], v[112:115], a[228:231], a[156:159]\n\t"
      "v_cvt_pk_bf16_f32 v140, v48, v49\n\t"
      "v_cvt_pk_bf16_f32 v141, v50, v51\n\t"
      "v_mfma_f32_16x16x32_bf16 a[160:163], v[116:119], a[228:231], a[160:163]\n\t"
      "v_cvt_pk_bf16_f32 v142, v52, v53\n\t"
      "v_cvt_pk_bf16_f32 v143, v54, v55\n\t"
      "ds_read_b128 a[232:235], v56 offset:25344\n\t"
      "v_mfma_f32_4x4x4_16b_bf16 v[40:43], a[180:181], a[92:93], 0\n\t"
      "ds_read_b64_tr_b16 a[100:101], v60 offset:128\n\t"
      "v_mfma_f32_4x4x4_16b_bf16 v[44:47], a[182:183], a[94:95], 0\n\t"
      "ds_read_b64_tr_b16 a[102:103], v60 offset:896\n\t"
      "v_mfma_f32_4x4x4_16b_bf16 v[48:51], a[180:181], a[96:97], 0\n\t"
      "ds_read_b64_tr_b16 a[104:105], v61 offset:128\n\t"
      "v_mfma_f32_4x4x4_16b_bf16 v[52:55], a[182:183], a[98:99], 0\n\t"
      "ds_read_b64_tr_b16 a[106:107], v61 offset:896\n\t"
      "buffer_load_dwordx4 v10, s[84:87], s54 offen offset:1024 lds\n\t"
      "buffer_load_dwordx4 v11, s[84:87], s54 offen offset:2048 lds\n\t"
      "ds_read_b128 a[236:239], v56 offset:37632\n\t"
      "v_mfma_f32_16x16x32_bf16 a[164:167], v[120:123], a[228:231], a[164:167]\n\t"
      "v_cvt_pk_bf16_f32 v40, v40, v41\n\t"
      "v_cvt_pk_bf16_f32 v41, v42, v43\n\t"
      "v_mfma_f32_16x16x32_bf16 a[168:171], v[124:127], a[228:231], a[168:171]\n\t"
      "v_cvt_pk_bf16_f32 v44, v44, v45\n\t"
      "v_cvt_pk_bf16_f32 v45, v46, v47\n\t"
      "v_mfma_f32_16x16x32_bf16 a[172:175], a[84:87], a[228:231], a[172:175]\n\t"
      "v_cvt_pk_bf16_f32 v48, v48, v49\n\t"
      "v_cvt_pk_bf16_f32 v49, v50, v51\n\t"
      "v_mfma_f32_16x16x32_bf16 a[176:179], a[88:91], a[228:231], a[176:179]\n\t"
      "v_cvt_pk_bf16_f32 v52, v52, v53\n\t"
      "v_cvt_pk_bf16_f32 v53, v54, v55\n\t"
      "v_accvgpr_write_b32 a92, v40\n\t"
      "v_accvgpr_write_b32 a93, v41\n\t"
      "v_accvgpr_write_b32 a94, v44\n\t"
      "v_accvgpr_write_b32 a95, v45\n\t"
      "v_accvgpr_write_b32 a96, v48\n\t"
      "v_accvgpr_write_b32 a97, v49\n\t"
      "v_accvgpr_write_b32 a98, v52\n\t"
      "v_accvgpr_write_b32 a99, v53\n\t"
      "s_waitcnt vmcnt(12)\n\t"
      "s_waitcnt lgkmcnt(1)\n\t"
      "; step\n\t"
      "v_mfma_f32_4x4x4_16b_bf16 v[40:43], a[184:185], v[144:145], 0\n\t"
      "ds_read_b128 a[180:183], v14 offset:1536\n\t"
      "v_mfma_f32_4x4x4_16b_bf16 v[44:47], a[186:187], v[146:147], 0\n\t"
      "ds_read_b64_tr_b16 v[160:161], v60 offset:24576\n\t"
      "v_mfma_f32_4x4x4_16b_bf16 v[48:51], a[184:185], v[148:149], 0\n\t"
      "ds_read_b64_tr_b16 v[162:163], v60 offset:25344\n\t"
      "v_mfma_f32_4x4x4_16b_bf16 v[52:55], a[186:187], v[150:151], 0\n\t"
      "ds_read_b64_tr_b16 v[164:165], v61 offset:24576\n\t"
      "ds_read_b64_tr_b16 v[166:167], v61 offset:25344\n\t"
      "s_add_u32 m0, s17, 86528\n\t"
      "s_mov_b32 s54, 221184\n\t"
      "buffer_load_dwordx4 v9, s[84:87], s54 offen lds\n\t"
      "buffer_load_dwordx4 v10, s[84:87], s54 offen offset:1024 lds\n\t"
      "v_mfma_f32_16x16x32_bf16 v[16:19], v[128:131], a[232:235], v[16:19]\n\t"
      "v_cvt_pk_bf16_f32 v144, v40, v41\n\t"
      "v_cvt_pk_bf16_f32 v145, v42, v43\n\t"
      "v_mfma_f32_16x16x32_bf16 v[20:23], v[132:135], a[232:235], v[20:23]\n\t"
      "v_cvt_pk_bf16_f32 v146, v44, v45\n\t"
      "v_cvt_pk_bf16_f32 v147, v46, v47\n\t"
      "v_mfma_f32_16x16x32_bf16 v[24:27], v[136:139], a[232:235], v[24:27]\n\t"
      "v_cvt_pk_bf16_f32 v148, v48, v49\n\t"
      "v_cvt_pk_bf16_f32 v149, v50, v51\n\t"
      "v_mfma_f32_16x16x32_bf16 v[28:31], v[140:143], a[232:235], v[28:31]\n\t"
      "v_cvt_pk_bf16_f32 v150, v52, v53\n\t"
      "v_cvt_pk_bf16_f32 v151, v54, v55\n\t"
      "v_mfma_f32_4x4x4_16b_bf16 v[40:43], a[184:185], v[152:153], 0\n\t"
      "ds_read_b64_tr_b16 v[168:169], v60 offset:24640\n\t"
      "v_mfma_f32_4x4x4_16b_bf16 v[44:47], a[186:187], v[154:155], 0\n\t"
      "ds_read_b64_tr_b16 v[170:171], v60 offset:25408\n\t"
      "v_mfma_f32_4x4x4_16b_bf16 v[48:51], a[184:185], v[156:157], 0\n\t"
      "ds_read_b64_tr_b16 v[172:173], v61 offset:24640\n\t"
      "v_mfma_f32_4x4x4_16b_bf16 v[52:55], a[186:187], v[158:159], 0\n\t"
      "ds_read_b64_tr_b16 v[174:175], v61 offset:25408\n\t"
      "buffer_load_dwordx4 v11, s[84:87], s54 offen offset:2048 lds\n\t"
      "s_add_u32 m0, s17, 89600\n\t"
      "s_mov_b32 s54, 233472\n\t"
      "buffer_load_dwordx4 v9, s[84:87], s54 offen lds\n\t"
      "v_mfma_f32_16x16x32_bf16 v[32:35], a[92:95], a[232:235], v[32:35]\n\t"
      "v_cvt_pk_bf16_f32 v152, v40, v41\n\t"
      "v_cvt_pk_bf16_f32 v153, v42, v43\n\t"
      "v_mfma_f32_16x16x32_bf16 v[36:39], a[96:99], a[232:235], v[36:39]\n\t"
      "v_cvt_pk_bf16_f32 v154, v44, v45\n\t"
      "v_cvt_pk_bf16_f32 v155, v46, v47\n\t"
      "s_waitcnt lgkmcnt(9)\n\t"
      "v_mfma_f32_16x16x32_bf16 a[156:159], v[128:131], a[236:239], a[156:159]\n\t"
      "v_cvt_pk_bf16_f32 v156, v48, v49\n\t"
      "v_cvt_pk_bf16_f32 v157, v50, v51\n\t"
      "v_mfma_f32_16x16x32_bf16 a[160:163], v[132:135], a[236:239], a[160:163]\n\t"
      "v_cvt_pk_bf16_f32 v158, v52, v53\n\t"
      "v_cvt_pk_bf16_f32 v159, v54, v55\n\t"
      "ds_read_b128 a[240:243], v57 offset:25344\n\t"
      "v_mfma_f32_4x4x4_16b_bf16 v[40:43], a[184:185], a[100:101], 0\n\t"
      "ds_read_b64_tr_b16 a[108:109], v60 offset:24704\n\t"
      "v_mfma_f32_4x4x4_16b_bf16 v[44:47], a[186:187], a[102:103], 0\n\t"
      "ds_read_b64_tr_b16 a[110:111], v60 offset:25472\n\t"
      "v_mfma_f32_4x4x4_16b_bf16 v[48:51], a[184:185], a[104:105], 0\n\t"
      "ds_read_b64_tr_b16 a[112:113], v61 offset:24704\n\t"
      "v_mfma_f32_4x4x4_16b_bf16 v[52:55], a[186:187], a[106:107], 0\n\t"
      "ds_read_b64_tr_b16 a[114:115], v61 offset:25472\n\t"
      "buffer_load_dwordx4 v10, s[84:87], s54 offen offset:1024 lds\n\t"
      "buffer_load_dwordx4 v11, s[84:87], s54 offen offset:2048 lds\n\t"
      "ds_read_b128 a[244:247], v57 offset:37632\n\t"
      "v_mfma_f32_16x16x32_bf16 a[164:167], v[136:139], a[236:239], a[164:167]\n\t"
      "v_cvt_pk_bf16_f32 v40, v40, v41\n\t"
      "v_cvt_pk_bf16_f32 v41, v42, v43\n\t"
      "v_mfma_f32_16x16x32_bf16 a[168:171], v[140:143], a[236:239], a[168:171]\n\t"
      "v_cvt_pk_bf16_f32 v44, v44, v45\n\t"
      "v_cvt_pk_bf16_f32 v45, v46, v47\n\t"
      "v_mfma_f32_16x16x32_bf16 a[172:175], a[92:95], a[236:239], a[172:175]\n\t"
      "v_cvt_pk_bf16_f32 v48, v48, v49\n\t"
      "v_cvt_pk_bf16_f32 v49, v50, v51\n\t"
      "v_mfma_f32_16x16x32_bf16 a[176:179], a[96:99], a[236:239], a[176:179]\n\t"
      "v_cvt_pk_bf16_f32 v52, v52, v53\n\t"
      "v_cvt_pk_bf16_f32 v53, v54, v55\n\t"
      "v_accvgpr_write_b32 a100, v40\n\t"
      "v_accvgpr_write_b32 a101, v41\n\t"
      "v_accvgpr_write_b32 a102, v44\n\t"
      "v_accvgpr_write_b32 a103, v45\n\t"
      "v_accvgpr_write_b32 a104, v48\n\t"
      "v_accvgpr_write_b32 a105, v49\n\t"
      "v_accvgpr_write_b32 a106, v52\n\t"
      "v_accvgpr_write_b32 a107, v53\n\t"
      "s_waitcnt vmcnt(12)\n\t"
      "s_waitcnt lgkmcnt(1)\n\t"
      "; step\n\t"
      "v_mfma_f32_4x4x4_16b_bf16 v[40:43], a[180:181], v[160:161], 0\n\t"
      "ds_read_b128 a[184:187], v14 offset:1792\n\t"
      "v_mfma_f32_4x4x4_16b_bf16 v[44:47], a[182:183], v[162:163], 0\n\t"
      "ds_read_b64_tr_b16 v[176:177], v60 offset:49152\n\t"
      "v_mfma_f32_4x4x4_16b_bf16 v[48:51], a[180:181], v[164:165], 0\n\t"
      "ds_read_b64_tr_b16 v[178:179], v60 offset:49920\n\t"
      "v_mfma_f32_4x4x4_16b_bf16 v[52:55], a[182:183], v[166:167], 0\n\t"
      "ds_read_b64_tr_b16 v[180:181], v61 offset:49152\n\t"
      "ds_read_b64_tr_b16 v[182:183], v61 offset:49920\n\t"
      "s_add_u32 m0, s17, 111104\n\t"
      "s_mov_b32 s54, 245760\n\t"
      "buffer_load_dwordx4 v9, s[84:87], s54 offen lds\n\t"
      "buffer_load_dwordx4 v10, s[84:87], s54 offen offset:1024 lds\n\t"
      "v_mfma_f32_16x16x32_bf16 v[16:19], v[144:147], a[240:243], v[16:19]\n\t"
      "v_cvt_pk_bf16_f32 v160, v40, v41\n\t"
      "v_cvt_pk_bf16_f32 v161, v42, v43\n\t"
      "v_mfma_f32_16x16x32_bf16 v[20:23], v[148:151], a[240:243], v[20:23]\n\t"
      "v_cvt_pk_bf16_f32 v162, v44, v45\n\t"
      "v_cvt_pk_bf16_f32 v163, v46, v47\n\t"
      "v_mfma_f32_16x16x32_bf16 v[24:27], v[152:155], a[240:243], v[24:27]\n\t"
      "v_cvt_pk_bf16_f32 v164, v48, v49\n\t"
      "v_cvt_pk_bf16_f32 v165, v50, v51\n\t"
      "v_mfma_f32_16x16x32_bf16 v[28:31], v[156:159], a[240:243], v[28:31]\n\t"
      "v_cvt_pk_bf16_f32 v166, v52, v53\n\t"
      "v_cvt_pk_bf16_f32 v167, v54, v55\n\t"
      "v_mfma_f32_4x4x4_16b_bf16 v[40:43], a[180:181], v[168:169], 0\n\t"
      "ds_read_b64_tr_b16 v[184:185], v60 offset:49216\n\t"
      "v_mfma_f32_4x4x4_16b_bf16 v[44:47], a[182:183], v[170:171], 0\n\t"
      "ds_read_b64_tr_b16 v[186:187], v60 offset:49984\n\t"
      "v_mfma_f32_4x4x4_16b_bf16 v[48:51], a[180:181], v[172:173], 0\n\t"
      "ds_read_b64_tr_b16 v[188:189], v61 offset:49216\n\t"
      "v_mfma_f32_4x4x4_16b_bf16 v[52:55], a[182:183], v[174:175], 0\n\t"
      "ds_read_b64_tr_b16 v[190:191], v61 offset:49984\n\t"
      "buffer_load_dwordx4 v11, s[84:87], s54 offen offset:2048 lds\n\t"
      "s_add_u32 m0, s17, 114176\n\t"
      "s_mov_b32 s54, 258048\n\t"
      "buffer_load_dwordx4 v9, s[84:87], s54 offen lds\n\t"
      "v_mfma_f32_16x16x32_bf16 v[32:35], a[100:103], a[240:243], v[32:35]\n\t"
      "v_cvt_pk_bf16_f32 v168, v40, v41\n\t"
      "v_cvt_pk_bf16_f32 v169, v42, v43\n\t"
      "v_mfma_f32_16x16x32_bf16 v[36:39], a[104:107], a[240:243], v[36:39]\n\t"
      "v_cvt_pk_bf16_f32 v170, v44, v45\n\t"
      "v_cvt_pk_bf16_f32 v171, v46, v47\n\t"
      "s_waitcnt lgkmcnt(9)\n\t"
      "v_mfma_f32_16x16x32_bf16 a[156:159], v[144:147], a[244:247], a[156:159]\n\t"
      "v_cvt_pk_bf16_f32 v172, v48, v49\n\t"
      "v_cvt_pk_bf16_f32 v173, v50, v51\n\t"
      "v_mfma_f32_16x16x32_bf16 a[160:163], v[148:151], a[244:247], a[160:163]\n\t"
      "v_cvt_pk_bf16_f32 v174, v52, v53\n\t"
      "v_cvt_pk_bf16_f32 v175, v54, v55\n\t"
      "ds_read_b128 a[224:227], v58 offset:25344\n\t"
      "v_mfma_f32_4x4x4_16b_bf16 v[40:43], a[180:181], a[108:109], 0\n\t"
      "ds_read_b64_tr_b16 a[116:117], v60 offset:49280\n\t"
      "v_mfma_f32_4x4x4_16b_bf16 v[44:47], a[182:183], a[110:111], 0\n\t"
      "ds_read_b64_tr_b16 a[118:119], v60 offset:50048\n\t"
      "v_mfma_f32_4x4x4_16b_bf16 v[48:51], a[180:181], a[112:113], 0\n\t"
      "ds_read_b64_tr_b16 a[120:121], v61 offset:49280\n\t"
      "v_mfma_f32_4x4x4_16b_bf16 v[52:55], a[182:183], a[114:115], 0\n\t"
      "ds_read_b64_tr_b16 a[122:123], v61 offset:50048\n\t"
      "buffer_load_dwordx4 v10, s[84:87], s54 offen offset:1024 lds\n\t"
      "buffer_load_dwordx4 v11, s[84:87], s54 offen offset:2048 lds\n\t"
      "ds_read_b128 a[228:231], v58 offset:37632\n\t"
      "v_mfma_f32_16x16x32_bf16 a[164:167], v[152:155], a[244:247], a[164:167]\n\t"
      "v_cvt_pk_bf16_f32 v40, v40, v41\n\t"
      "v_cvt_pk_bf16_f32 v41, v42, v43\n\t"
      "v_mfma_f32_16x16x32_bf16 a[168:171], v[156:159], a[244:247], a[168:171]\n\t"
      "v_cvt_pk_bf16_f32 v44, v44, v45\n\t"
      "v_cvt_pk_bf16_f32 v45, v46, v47\n\t"
      "v_mfma_f32_16x16x32_bf16 a[172:175], a[100:103], a[244:247], a[172:175]\n\t"
      "v_cvt_pk_bf16_f32 v48, v48, v49\n\t"
      "v_cvt_pk_bf16_f32 v49, v50, v51\n\t"
      "v_mfma_f32_16x16x32_bf16 a[176:179], a[104:107], a[244:247], a[176:179]\n\t"
      "v_cvt_pk_bf16_f32 v52, v52, v53\n\t"
      "v_cvt_pk_bf16_f32 v53, v54, v55\n\t"
      "v_accvgpr_write_b32 a108, v40\n\t"
      "v_accvgpr_write_b32 a109, v41\n\t"
      "v_accvgpr_write_b32 a110, v44\n\t"
      "v_accvgpr_write_b32 a111, v45\n\t"
      "v_accvgpr_write_b32 a112, v48\n\t"
      "v_accvgpr_write_b32 a113, v49\n\t"
      "v_accvgpr_write_b32 a114, v52\n\t"
      "v_accvgpr_write_b32 a115, v53\n\t"
      "s_waitcnt vmcnt(12)\n\t"
      "s_waitcnt lgkmcnt(1)\n\t"
      "; step\n\t"
      "v_mfma_f32_4x4x4_16b_bf16 v[40:43], a[184:185], v[176:177], 0\n\t"
      "ds_read_b128 a[180:183], v14 offset:2048\n\t"
      "v_mfma_f32_4x4x4_16b_bf16 v[44:47], a[186:187], v[178:179], 0\n\t"
      "ds_read_b64_tr_b16 v[192:193], v12 offset:0\n\t"
      "v_mfma_f32_4x4x4_16b_bf16 v[48:51], a[184:185], v[180:181], 0\n\t"
      "ds_read_b64_tr_b16 v[194:195], v12 offset:768\n\t"
      "v_mfma_f32_4x4x4_16b_bf16 v[52:55], a[186:187], v[182:183], 0\n\t"
      "ds_read_b64_tr_b16 v[196:197], v13 offset:0\n\t"
      "ds_read_b64_tr_b16 v[198:199], v13 offset:768\n\t"
      "s_add_u32 m0, s17, 135680\n\t"
      "s_mov_b32 s54, 270336\n\t"
      "buffer_load_dwordx4 v9, s[84:87], s54 offen lds\n\t"
      "buffer_load_dwordx4 v10, s[84:87], s54 offen offset:1024 lds\n\t"
      "v_mfma_f32_16x16x32_bf16 v[16:19], v[160:163], a[224:227], v[16:19]\n\t"
      "v_cvt_pk_bf16_f32 v176, v40, v41\n\t"
      "v_cvt_pk_bf16_f32 v177, v42, v43\n\t"
      "v_mfma_f32_16x16x32_bf16 v[20:23], v[164:167], a[224:227], v[20:23]\n\t"
      "v_cvt_pk_bf16_f32 v178, v44, v45\n\t"
      "v_cvt_pk_bf16_f32 v179, v46, v47\n\t"
      "v_mfma_f32_16x16x32_bf16 v[24:27], v[168:171], a[224:227], v[24:27]\n\t"
      "v_cvt_pk_bf16_f32 v180, v48, v49\n\t"
      "v_cvt_pk_bf16_f32 v181, v50, v51\n\t"
      "v_mfma_f32_16x16x32_bf16 v[28:31], v[172:175], a[224:227], v[28:31]\n\t"
      "v_cvt_pk_bf16_f32 v182, v52, v53\n\t"
      "v_cvt_pk_bf16_f32 v183, v54, v55\n\t"
      "v_mfma_f32_4x4x4_16b_bf16 v[40:43], a[184:185], v[184:185], 0\n\t"
      "ds_read_b64_tr_b16 v[200:201], v12 offset:64\n\t"
      "v_mfma_f32_4x4x4_16b_bf16 v[44:47], a[186:187], v[186:187], 0\n\t"
      "ds_read_b64_tr_b16 v[202:203], v12 offset:832\n\t"
      "v_mfma_f32_4x4x4_16b_bf16 v[48:51], a[184:185], v[188:189], 0\n\t"
      "ds_read_b64_tr_b16 v[204:205], v13 offset:64\n\t"
      "v_mfma_f32_4x4x4_16b_bf16 v[52:55], a[186:187], v[190:191], 0\n\t"
      "ds_read_b64_tr_b16 v[206:207], v13 offset:832\n\t"
      "buffer_load_dwordx4 v11, s[84:87], s54 offen offset:2048 lds\n\t"
      "s_add_u32 m0, s17, 138752\n\t"
      "s_mov_b32 s54, 282624\n\t"
      "buffer_load_dwordx4 v9, s[84:87], s54 offen lds\n\t"
      "v_mfma_f32_16x16x32_bf16 v[32:35], a[108:111], a[224:227], v[32:35]\n\t"
      "v_cvt_pk_bf16_f32 v184, v40, v41\n\t"
      "v_cvt_pk_bf16_f32 v185, v42, v43\n\t"
      "v_mfma_f32_16x16x32_bf16 v[36:39], a[112:115], a[224:227], v[36:39]\n\t"
      "v_cvt_pk_bf16_f32 v186, v44, v45\n\t"
      "v_cvt_pk_bf16_f32 v187, v46, v47\n\t"
      "s_waitcnt lgkmcnt(9)\n\t"
      "v_mfma_f32_16x16x32_bf16 a[156:159], v[160:163], a[228:231], a[156:159]\n\t"
      "v_cvt_pk_bf16_f32 v188, v48, v49\n\t"
      "v_cvt_pk_bf16_f32 v189, v50, v51\n\t"
      "v_mfma_f32_16x16x32_bf16 a[160:163], v[164:167], a[228:231], a[160:163]\n\t"
      "v_cvt_pk_bf16_f32 v190, v52, v53\n\t"
      "v_cvt_pk_bf16_f32 v191, v54, v55\n\t"
      "ds_read_b128 a[232:235], v59 offset:25344\n\t"
      "v_mfma_f32_4x4x4_16b_bf16 v[40:43], a[184:185], a[116:117], 0\n\t"
      "ds_read_b64_tr_b16 a[124:125], v12 offset:128\n\t"
      "v_mfma_f32_4x4x4_16b_bf16 v[44:47], a[186:187], a[118:119], 0\n\t"
      "ds_read_b64_tr_b16 a[126:127], v12 offset:896\n\t"
      "v_mfma_f32_4x4x4_16b_bf16 v[48:51], a[184:185], a[120:121], 0\n\t"
      "ds_read_b64_tr_b16 a[128:129], v13 offset:128\n\t"
      "v_mfma_f32_4x4x4_16b_bf16 v[52:55], a[186:187], a[122:123], 0\n\t"
      "ds_read_b64_tr_b16 a[130:131], v13 offset:896\n\t"
      "buffer_load_dwordx4 v10, s[84:87], s54 offen offset:1024 lds\n\t"
      "buffer_load_dwordx4 v11, s[84:87], s54 offen offset:2048 lds\n\t"
      "ds_read_b128 a[236:239], v59 offset:37632\n\t"
      "s_or_b32 s30, s17, s16\n\t"
      "s_cbranch_scc1 .Lno3_%=\n\t"
      "global_load_dword a252, v62, s[12:13] sc1\n\t"
      ".Lno3_%=:\n\t"
      "s_add_u32 m0, s18, 49664\n\t"
      "s_add_u32 s48, s15, 786432\n\t"
      "buffer_load_dwordx4 v2, s[40:43], s48 offen nt lds\n\t"
      "buffer_load_dwordx4 v3, s[40:43], s48 offen offset:1024 nt lds\n\t"
      "buffer_load_dwordx4 v4, s[40:43], s48 offen offset:2048 nt lds\n\t"
      "v_mfma_f32_16x16x32_bf16 a[164:167], v[168:171], a[228:231], a[164:167]\n\t"
      "v_cvt_pk_bf16_f32 v40, v40, v41\n\t"
      "v_cvt_pk_bf16_f32 v41, v42, v43\n\t"
      "v_mfma_f32_16x16x32_bf16 a[168:171], v[172:175], a[228:231], a[168:171]\n\t"
      "v_cvt_pk_bf16_f32 v44, v44, v45\n\t"
      "v_cvt_pk_bf16_f32 v45, v46, v47\n\t"
      "v_mfma_f32_16x16x32_bf16 a[172:175], a[108:111], a[228:231], a[172:175]\n\t"
      "v_cvt_pk_bf16_f32 v48, v48, v49\n\t"
      "v_cvt_pk_bf16_f32 v49, v50, v51\n\t"
      "v_mfma_f32_16x16x32_bf16 a[176:179], a[112:115], a[228:231], a[176:179]\n\t"
      "v_cvt_pk_bf16_f32 v52, v52, v53\n\t"
      "v_cvt_pk_bf16_f32 v53, v54, v55\n\t"
      "v_accvgpr_write_b32 a116, v40\n\t"
      "v_accvgpr_write_b32 a117, v41\n\t"
      "v_accvgpr_write_b32 a118, v44\n\t"
      "v_accvgpr_write_b32 a119, v45\n\t"
      "v_accvgpr_write_b32 a120, v48\n\t"
      "v_accvgpr_write_b32 a121, v49\n\t"
      "v_accvgpr_write_b32 a122, v52\n\t"
      "v_accvgpr_write_b32 a123, v53\n\t"
      "s_waitcnt vmcnt(15)\n\t"
      "s_waitcnt lgkmcnt(1)\n\t"
      "; step\n\t"
      "v_mfma_f32_4x4x4_16b_bf16 v[40:43], a[180:181], v[192:193], 0\n\t"
      "ds_read_b128 a[184:187], v14 offset:2304\n\t"
      "v_mfma_f32_4x4x4_16b_bf16 v[44:47], a[182:183], v[194:195], 0\n\t"
      "ds_read_b64_tr_b16 v[208:209], v60 offset:0\n\t"
      "v_mfma_f32_4x4x4_16b_bf16 v[48:51], a[180:181], v[196:197], 0\n\t"
      "ds_read_b64_tr_b16 v[210:211], v60 offset:768\n\t"
      "v_mfma_f32_4x4x4_16b_bf16 v[52:55], a[182:183], v[198:199], 0\n\t"
      "ds_read_b64_tr_b16 v[212:213], v61 offset:0\n\t"
      "ds_read_b64_tr_b16 v[214:215], v61 offset:768\n\t"
      "v_mfma_f32_16x16x32_bf16 v[16:19], v[176:179], a[232:235], v[16:19]\n\t"
      "v_cvt_pk_bf16_f32 v192, v40, v41\n\t"
      "v_cvt_pk_bf16_f32 v193, v42, v43\n\t"
      "v_mfma_f32_16x16x32_bf16 v[20:23], v[180:183], a[232:235], v[20:23]\n\t"
      "v_cvt_pk_bf16_f32 v194, v44, v45\n\t"
      "v_cvt_pk_bf16_f32 v195, v46, v47\n\t"
      "v_mfma_f32_16x16x32_bf16 v[24:27], v[184:187], a[232:235], v[24:27]\n\t"
      "v_cvt_pk_bf16_f32 v196, v48, v49\n\t"
      "v_cvt_pk_bf16_f32 v197, v50, v51\n\t"
      "v_mfma_f32_16x16x32_bf16 v[28:31], v[188:191], a[232:235], v[28:31]\n\t"
      "v_cvt_pk_bf16_f32 v198, v52, v53\n\t"
      "v_cvt_pk_bf16_f32 v199, v54, v55\n\t"
      "v_mfma_f32_4x4x4_16b_bf16 v[40:43], a[180:181], v[200:201], 0\n\t"
      "ds_read_b64_tr_b16 v[216:217], v60 offset:64\n\t"
      "v_mfma_f32_4x4x4_16b_bf16 v[44:47], a[182:183], v[202:203], 0\n\t"
      "ds_read_b64_tr_b16 v[218:219], v60 offset:832\n\t"
      "v_mfma_f32_4x4x4_16b_bf16 v[48:51], a[180:181], v[204:205], 0\n\t"
      "ds_read_b64_tr_b16 v[220:221], v61 offset:64\n\t"
      "v_mfma_f32_4x4x4_16b_bf16 v[52:55], a[182:183], v[206:207], 0\n\t"
      "ds_read_b64_tr_b16 v[222:223], v61 offset:832\n\t"
      "v_mfma_f32_16x16x32_bf16 v[32:35], a[116:119], a[232:235], v[32:35]\n\t"
      "v_cvt_pk_bf16_f32 v200, v40, v41\n\t"
      "v_cvt_pk_bf16_f32 v201, v42, v43\n\t"
      "v_mfma_f32_16x16x32_bf16 v[36:39], a[120:123], a[232:235], v[36:39]\n\t"
      "v_cvt_pk_bf16_f32 v202, v44, v45\n\t"
      "v_cvt_pk_bf16_f32 v203, v46, v47\n\t"
      "s_waitcnt lgkmcnt(9)\n\t"
      "v_mfma_f32_16x16x32_bf16 a[156:159], v[176:179], a[236:239], a[156:159]\n\t"
      "v_cvt_pk_bf16_f32 v204, v48, v49\n\t"
      "v_cvt_pk_bf16_f32 v205, v50, v51\n\t"
      "v_mfma_f32_16x16x32_bf16 a[160:163], v[180:183], a[236:239], a[160:163]\n\t"
      "v_cvt_pk_bf16_f32 v206, v52, v53\n\t"
      "v_cvt_pk_bf16_f32 v207, v54, v55\n\t"
      "ds_read_b128 a[240:243], v56 offset:25600\n\t"
      "v_mfma_f32_4x4x4_16b_bf16 v[40:43], a[180:181], a[124:125], 0\n\t"
      "ds_read_b64_tr_b16 a[132:133], v60 offset:128\n\t"
      "v_mfma_f32_4x4x4_16b_bf16 v[44:47], a[182:183], a[126:127], 0\n\t"
      "ds_read_b64_tr_b16 a[134:135], v60 offset:896\n\t"
      "v_mfma_f32_4x4x4_16b_bf16 v[48:51], a[180:181], a[128:129], 0\n\t"
      "ds_read_b64_tr_b16 a[136:137], v61 offset:128\n\t"
      "v_mfma_f32_4x4x4_16b_bf16 v[52:55], a[182:183], a[130:131], 0\n\t"
      "ds_read_b64_tr_b16 a[138:139], v61 offset:896\n\t"
      "ds_read_b128 a[244:247], v56 offset:37888\n\t"
      "v_mfma_f32_16x16x32_bf16 a[164:167], v[184:187], a[236:239], a[164:167]\n\t"
      "v_cvt_pk_bf16_f32 v40, v40, v41\n\t"
      "v_cvt_pk_bf16_f32 v41, v42, v43\n\t"
      "v_mfma_f32_16x16x32_bf16 a[168:171], v[188:191], a[236:239], a[168:171]\n\t"
      "v_cvt_pk_bf16_f32 v44, v44, v45\n\t"
      "v_cvt_pk_bf16_f32 v45, v46, v47\n\t"
      "v_mfma_f32_16x16x32_bf16 a[172:175], a[116:119], a[236:239], a[172:175]\n\t"
      "v_cvt_pk_bf16_f32 v48, v48, v49\n\t"
      "v_cvt_pk_bf16_f32 v49, v50, v51\n\t"
      "v_mfma_f32_16x16x32_bf16 a[176:179], a[120:123], a[236:239], a[176:179]\n\t"
      "v_cvt_pk_bf16_f32 v52, v52, v53\n\t"
      "v_cvt_pk_bf16_f32 v53, v54, v55\n\t"
      "v_accvgpr_write_b32 a124, v40\n\t"
      "v_accvgpr_write_b32 a125, v41\n\t"
      "v_accvgpr_write_b32 a126, v44\n\t"
      "v_accvgpr_write_b32 a127, v45\n\t"
      "v_accvgpr_write_b32 a128, v48\n\t"
      "v_accvgpr_write_b32 a129, v49\n\t"
      "v_accvgpr_write_b32 a130, v52\n\t"
      "v_accvgpr_write_b32 a131, v53\n\t"
      "s_waitcnt vmcnt(9)\n\t"
      "s_waitcnt lgkmcnt(1)\n\t"
      "; step\n\t"
      "v_mfma_f32_4x4x4_16b_bf16 v[40:43], a[184:185], v[208:209], 0\n\t"
      "ds_read_b128 a[180:183], v14 offset:2560\n\t"
      "v_mfma_f32_4x4x4_16b_bf16 v[44:47], a[186:187], v[210:211], 0\n\t"
      "ds_read_b64_tr_b16 v[224:225], v60 offset:24576\n\t"
      "v_mfma_f32_4x4x4_16b_bf16 v[48:51], a[184:185], v[212:213], 0\n\t"
      "ds_read_b64_tr_b16 v[226:227], v60 offset:25344\n\t"
      "v_mfma_f32_4x4x4_16b_bf16 v[52:55], a[186:187], v[214:215], 0\n\t"
      "ds_read_b64_tr_b16 v[228:229], v61 offset:24576\n\t"
      "ds_read_b64_tr_b16 v[230:231], v61 offset:25344\n\t"
      "s_add_u32 m0, s18, 61952\n\t"
      "s_add_u32 s48, s15, 786432\n\t"
      "buffer_load_dwordx4 v5, s[40:43], s48 offen nt lds\n\t"
      "buffer_load_dwordx4 v6, s[40:43], s48 offen offset:1024 nt lds\n\t"
      "v_mfma_f32_16x16x32_bf16 v[16:19], v[192:195], a[240:243], v[16:19]\n\t"
      "v_cvt_pk_bf16_f32 v208, v40, v41\n\t"
      "v_cvt_pk_bf16_f32 v209, v42, v43\n\t"
      "v_mfma_f32_16x16x32_bf16 v[20:23], v[196:199], a[240:243], v[20:23]\n\t"
      "v_cvt_pk_bf16_f32 v210, v44, v45\n\t"
      "v_cvt_pk_bf16_f32 v211, v46, v47\n\t"
      "v_mfma_f32_16x16x32_bf16 v[24:27], v[200:203], a[240:243], v[24:27]\n\t"
      "v_cvt_pk_bf16_f32 v212, v48, v49\n\t"
      "v_cvt_pk_bf16_f32 v213, v50, v51\n\t"
      "v_mfma_f32_16x16x32_bf16 v[28:31], v[204:207], a[240:243], v[28:31]\n\t"
      "v_cvt_pk_bf16_f32 v214, v52, v53\n\t"
      "v_cvt_pk_bf16_f32 v215, v54, v55\n\t"
      "v_mfma_f32_4x4x4_16b_bf16 v[40:43], a[184:185], v[216:217], 0\n\t"
      "ds_read_b64_tr_b16 v[232:233], v60 offset:24640\n\t"
      "v_mfma_f32_4x4x4_16b_bf16 v[44:47], a[186:187], v[218:219], 0\n\t"
      "ds_read_b64_tr_b16 v[234:235], v60 offset:25408\n\t"
      "v_mfma_f32_4x4x4_16b_bf16 v[48:51], a[184:185], v[220:221], 0\n\t"
      "ds_read_b64_tr_b16 v[236:237], v61 offset:24640\n\t"
      "v_mfma_f32_4x4x4_16b_bf16 v[52:55], a[186:187], v[222:223], 0\n\t"
      "ds_read_b64_tr_b16 v[238:239], v61 offset:25408\n\t"
      "buffer_load_dwordx4 v7, s[40:43], s48 offen offset:2048 nt lds\n\t"
      "v_mfma_f32_16x16x32_bf16 v[32:35], a[124:127], a[240:243], v[32:35]\n\t"
      "v_cvt_pk_bf16_f32 v216, v40, v41\n\t"
      "v_cvt_pk_bf16_f32 v217, v42, v43\n\t"
      "v_mfma_f32_16x16x32_bf16 v[36:39], a[128:131], a[240:243], v[36:39]\n\t"
      "v_cvt_pk_bf16_f32 v218, v44, v45\n\t"
      "v_cvt_pk_bf16_f32 v219, v46, v47\n\t"
      "s_waitcnt lgkmcnt(9)\n\t"
      "v_mfma_f32_16x16x32_bf16 a[156:159], v[192:195], a[244:247], a[156:159]\n\t"
      "v_cvt_pk_bf16_f32 v220, v48, v49\n\t"
      "v_cvt_pk_bf16_f32 v221, v50, v51\n\t"
      "v_mfma_f32_16x16x32_bf16 a[160:163], v[196:199], a[244:247], a[160:163]\n\t"
      "v_cvt_pk_bf16_f32 v222, v52, v53\n\t"
      "v_cvt_pk_bf16_f32 v223, v54, v55\n\t"
      "ds_read_b128 a[224:227], v57 offset:25600\n\t"
      "v_mfma_f32_4x4x4_16b_bf16 v[40:43], a[184:185], a[132:133], 0\n\t"
      "ds_read_b64_tr_b16 a[140:141], v60 offset:24704\n\t"
      "v_mfma_f32_4x4x4_16b_bf16 v[44:47], a[186:187], a[134:135], 0\n\t"
      "ds_read_b64_tr_b16 a[142:143], v60 offset:25472\n\t"
      "v_mfma_f32_4x4x4_16b_bf16 v[48:51], a[184:185], a[136:137], 0\n\t"
      "ds_read_b64_tr_b16 a[144:145], v61 offset:24704\n\t"
      "v_mfma_f32_4x4x4_16b_bf16 v[52:55], a[186:187], a[138:139], 0\n\t"
      "ds_read_b64_tr_b16 a[146:147], v61 offset:25472\n\t"
      "ds_read_b128 a[228:231], v57 offset:37888\n\t"
      "v_mfma_f32_16x16x32_bf16 a[164:167], v[200:203], a[244:247], a[164:167]\n\t"
      "v_cvt_pk_bf16_f32 v40, v40, v41\n\t"
      "v_cvt_pk_bf16_f32 v41, v42, v43\n\t"
      "v_mfma_f32_16x16x32_bf16 a[168:171], v[204:207], a[244:247], a[168:171]\n\t"
      "v_cvt_pk_bf16_f32 v44, v44, v45\n\t"
      "v_cvt_pk_bf16_f32 v45, v46, v47\n\t"
      "v_mfma_f32_16x16x32_bf16 a[172:175], a[124:127], a[244:247], a[172:175]\n\t"
      "v_cvt_pk_bf16_f32 v48, v48, v49\n\t"
      "v_cvt_pk_bf16_f32 v49, v50, v51\n\t"
      "v_mfma_f32_16x16x32_bf16 a[176:179], a[128:131], a[244:247], a[176:179]\n\t"
      "v_cvt_pk_bf16_f32 v52, v52, v53\n\t"
      "v_cvt_pk_bf16_f32 v53, v54, v55\n\t"
      "v_accvgpr_write_b32 a132, v40\n\t"
      "v_accvgpr_write_b32 a133, v41\n\t"
      "v_accvgpr_write_b32 a134, v44\n\t"
      "v_accvgpr_write_b32 a135, v45\n\t"
      "v_accvgpr_write_b32 a136, v48\n\t"
      "v_accvgpr_write_b32 a137, v49\n\t"
      "v_accvgpr_write_b32 a138, v52\n\t"
      "v_accvgpr_write_b32 a139, v53\n\t"
      "s_waitcnt vmcnt(6)\n\t"
      "s_waitcnt lgkmcnt(1)\n\t"
      "; step\n\t"
      "v_mfma_f32_4x4x4_16b_bf16 v[40:43], a[180:181], v[224:225], 0\n\t"
      "ds_read_b128 a[184:187], v14 offset:2816\n\t"
      "v_mfma_f32_4x4x4_16b_bf16 v[44:47], a[182:183], v[226:227], 0\n\t"
      "ds_read_b64_tr_b16 v[240:241], v60 offset:49152\n\t"
      "v_mfma_f32_4x4x4_16b_bf16 v[48:51], a[180:181], v[228:229], 0\n\t"
      "ds_read_b64_tr_b16 v[242:243], v60 offset:49920\n\t"
      "v_mfma_f32_4x4x4_16b_bf16 v[52:55], a[182:183], v[230:231], 0\n\t"
      "ds_read_b64_tr_b16 v[244:245], v61 offset:49152\n\t"
      "ds_read_b64_tr_b16 v[246:247], v61 offset:49920\n\t"
      "v_mfma_f32_16x16x32_bf16 v[16:19], v[208:211], a[224:227], v[16:19]\n\t"
      "v_cvt_pk_bf16_f32 v224, v40, v41\n\t"
      "v_cvt_pk_bf16_f32 v225, v42, v43\n\t"
      "v_mfma_f32_16x16x32_bf16 v[20:23], v[212:215], a[224:227], v[20:23]\n\t"
      "v_cvt_pk_bf16_f32 v226, v44, v45\n\t"
      "v_cvt_pk_bf16_f32 v227, v46, v47\n\t"
      "v_mfma_f32_16x16x32_bf16 v[24:27], v[216:219], a[224:227], v[24:27]\n\t"
      "v_cvt_pk_bf16_f32 v228, v48, v49\n\t"
      "v_cvt_pk_bf16_f32 v229, v50, v51\n\t"
      "v_mfma_f32_16x16x32_bf16 v[28:31], v[220:223], a[224:227], v[28:31]\n\t"
      "v_cvt_pk_bf16_f32 v230, v52, v53\n\t"
      "v_cvt_pk_bf16_f32 v231, v54, v55\n\t"
      "v_mfma_f32_4x4x4_16b_bf16 v[40:43], a[180:181], v[232:233], 0\n\t"
      "ds_read_b64_tr_b16 v[248:249], v60 offset:49216\n\t"
      "v_mfma_f32_4x4x4_16b_bf16 v[44:47], a[182:183], v[234:235], 0\n\t"
      "ds_read_b64_tr_b16 v[250:251], v60 offset:49984\n\t"
      "v_mfma_f32_4x4x4_16b_bf16 v[48:51], a[180:181], v[236:237], 0\n\t"
      "ds_read_b64_tr_b16 v[252:253], v61 offset:49216\n\t"
      "v_mfma_f32_4x4x4_16b_bf16 v[52:55], a[182:183], v[238:239], 0\n\t"
      "ds_read_b64_tr_b16 v[254:255], v61 offset:49984\n\t"
      "v_mfma_f32_16x16x32_bf16 v[32:35], a[132:135], a[224:227], v[32:35]\n\t"
      "v_cvt_pk_bf16_f32 v232, v40, v41\n\t"
      "v_cvt_pk_bf16_f32 v233, v42, v43\n\t"
      "v_mfma_f32_16x16x32_bf16 v[36:39], a[136:139], a[224:227], v[36:39]\n\t"
      "v_cvt_pk_bf16_f32 v234, v44, v45\n\t"
      "v_cvt_pk_bf16_f32 v235, v46, v47\n\t"
      "s_waitcnt lgkmcnt(9)\n\t"
      "v_mfma_f32_16x16x32_bf16 a[156:159], v[208:211], a[228:231], a[156:159]\n\t"
      "v_cvt_pk_bf16_f32 v236, v48, v49\n\t"
      "v_cvt_pk_bf16_f32 v237, v50, v51\n\t"
      "v_mfma_f32_16x16x32_bf16 a[160:163], v[212:215], a[228:231], a[160:163]\n\t"
      "v_cvt_pk_bf16_f32 v238, v52, v53\n\t"
      "v_cvt_pk_bf16_f32 v239, v54, v55\n\t"
      "ds_read_b128 a[232:235], v58 offset:25600\n\t"
      "v_mfma_f32_4x4x4_16b_bf16 v[40:43], a[180:181], a[140:141], 0\n\t"
      "ds_read_b64_tr_b16 a[148:149], v60 offset:49280\n\t"
      "v_mfma_f32_4x4x4_16b_bf16 v[44:47], a[182:183], a[142:143], 0\n\t"
      "ds_read_b64_tr_b16 a[150:151], v60 offset:50048\n\t"
      "v_mfma_f32_4x4x4_16b_bf16 v[48:51], a[180:181], a[144:145], 0\n\t"
      "ds_read_b64_tr_b16 a[152:153], v61 offset:49280\n\t"
      "v_mfma_f32_4x4x4_16b_bf16 v[52:55], a[182:183], a[146:147], 0\n\t"
      "ds_read_b64_tr_b16 a[154:155], v61 offset:50048\n\t"
      "v_mfma_f32_16x16x32_bf16 a[164:167], v[216:219], a[228:231], a[164:167]\n\t"
      "v_cvt_pk_bf16_f32 v40, v40, v41\n\t"
      "v_cvt_pk_bf16_f32 v41, v42, v43\n\t"
      "v_mfma_f32_16x16x32_bf16 a[168:171], v[220:223], a[228:231], a[168:171]\n\t"
      "v_cvt_pk_bf16_f32 v44, v44, v45\n\t"
      "v_cvt_pk_bf16_f32 v45, v46, v47\n\t"
      "v_mfma_f32_16x16x32_bf16 a[172:175], a[132:135], a[228:231], a[172:175]\n\t"
      "v_cvt_pk_bf16_f32 v48, v48, v49\n\t"
      "v_cvt_pk_bf16_f32 v49, v50, v51\n\t"
      "v_mfma_f32_16x16x32_bf16 a[176:179], a[136:139], a[228:231], a[176:179]\n\t"
      "v_cvt_pk_bf16_f32 v52, v52, v53\n\t"
      "v_cvt_pk_bf16_f32 v53, v54, v55\n\t"
      "v_accvgpr_write_b32 a140, v40\n\t"
      "v_accvgpr_write_b32 a141, v41\n\t"
      "v_accvgpr_write_b32 a142, v44\n\t"
      "v_accvgpr_write_b32 a143, v45\n\t"
      "v_accvgpr_write_b32 a144, v48\n\t"
      "v_accvgpr_write_b32 a145, v49\n\t"
      "v_accvgpr_write_b32 a146, v52\n\t"
      "v_accvgpr_write_b32 a147, v53\n\t"
      "s_waitcnt lgkmcnt(0)\n\t"
      "; step\n\t"
      "v_mfma_f32_4x4x4_16b_bf16 v[40:43], a[184:185], v[240:241], 0\n\t"
      "v_mfma_f32_4x4x4_16b_bf16 v[44:47], a[186:187], v[242:243], 0\n\t"
      "v_mfma_f32_4x4x4_16b_bf16 v[48:51], a[184:185], v[244:245], 0\n\t"
      "v_mfma_f32_4x4x4_16b_bf16 v[52:55], a[186:187], v[246:247], 0\n\t"
      "v_mfma_f32_16x16x32_bf16 v[16:19], v[224:227], a[232:235], v[16:19]\n\t"
      "s_nop 0\n\t"
      "v_cvt_pk_bf16_f32 v240, v40, v41\n\t"
      "v_cvt_pk_bf16_f32 v241, v42, v43\n\t"
      "v_mfma_f32_16x16x32_bf16 v[20:23], v[228:231], a[232:235], v[20:23]\n\t"
      "v_cvt_pk_bf16_f32 v242, v44, v45\n\t"
      "v_cvt_pk_bf16_f32 v243, v46, v47\n\t"
      "v_cvt_pk_bf16_f32 v244, v48, v49\n\t"
      "v_cvt_pk_bf16_f32 v245, v50, v51\n\t"
      "v_cvt_pk_bf16_f32 v246, v52, v53\n\t"
      "v_cvt_pk_bf16_f32 v247, v54, v55\n\t"
      "v_mfma_f32_4x4x4_16b_bf16 v[40:43], a[184:185], v[248:249], 0\n\t"
      "v_mfma_f32_4x4x4_16b_bf16 v[44:47], a[186:187], v[250:251], 0\n\t"
      "v_mfma_f32_4x4x4_16b_bf16 v[48:51], a[184:185], v[252:253], 0\n\t"
      "v_mfma_f32_4x4x4_16b_bf16 v[52:55], a[186:187], v[254:255], 0\n\t"
      "v_mfma_f32_16x16x32_bf16 v[24:27], v[232:235], a[232:235], v[24:27]\n\t"
      "s_nop 0\n\t"
      "v_cvt_pk_bf16_f32 v248, v40, v41\n\t"
      "v_cvt_pk_bf16_f32 v249, v42, v43\n\t"
      "v_mfma_f32_16x16x32_bf16 v[28:31], v[236:239], a[232:235], v[28:31]\n\t"
      "v_cvt_pk_bf16_f32 v250, v44, v45\n\t"
      "v_cvt_pk_bf16_f32 v251, v46, v47\n\t"
      "v_cvt_pk_bf16_f32 v252, v48, v49\n\t"
      "v_cvt_pk_bf16_f32 v253, v50, v51\n\t"
      "v_cvt_pk_bf16_f32 v254, v52, v53\n\t"
      "v_cvt_pk_bf16_f32 v255, v54, v55\n\t"
      "ds_read_b128 a[236:239], v59 offset:25600\n\t"
      "v_mfma_f32_4x4x4_16b_bf16 v[40:43], a[184:185], a[148:149], 0\n\t"
      "v_mfma_f32_4x4x4_16b_bf16 v[44:47], a[186:187], a[150:151], 0\n\t"
      "v_mfma_f32_4x4x4_16b_bf16 v[48:51], a[184:185], a[152:153], 0\n\t"
      "v_mfma_f32_4x4x4_16b_bf16 v[52:55], a[186:187], a[154:155], 0\n\t"
      "ds_read_b128 a[240:243], v58 offset:37888\n\t"
      "ds_read_b128 a[244:247], v59 offset:37888\n\t"
      "v_mfma_f32_16x16x32_bf16 v[32:35], a[140:143], a[232:235], v[32:35]\n\t"
      "v_cvt_pk_bf16_f32 v40, v40, v41\n\t"
      "v_cvt_pk_bf16_f32 v41, v42, v43\n\t"
      "v_mfma_f32_16x16x32_bf16 v[36:39], a[144:147], a[232:235], v[36:39]\n\t"
      "v_cvt_pk_bf16_f32 v44, v44, v45\n\t"
      "v_cvt_pk_bf16_f32 v45, v46, v47\n\t"
      "v_cvt_pk_bf16_f32 v48, v48, v49\n\t"
      "v_cvt_pk_bf16_f32 v49, v50, v51\n\t"
      "v_cvt_pk_bf16_f32 v52, v52, v53\n\t"
      "v_cvt_pk_bf16_f32 v53, v54, v55\n\t"
      "v_accvgpr_write_b32 a148, v40\n\t"
      "v_accvgpr_write_b32 a149, v41\n\t"
      "v_accvgpr_write_b32 a150, v44\n\t"
      "v_accvgpr_write_b32 a151, v45\n\t"
      "v_accvgpr_write_b32 a152, v48\n\t"
      "v_accvgpr_write_b32 a153, v49\n\t"
      "v_accvgpr_write_b32 a154, v52\n\t"
      "v_accvgpr_write_b32 a155, v53\n\t"
      "s_waitcnt lgkmcnt(2)\n\t"
      "v_mfma_f32_16x16x32_bf16 v[16:19], v[240:243], a[236:239], v[16:19]\n\t"
      "s_add_u32 s49, s15, 0\n\t"
      "s_mov_b32 s50, 98816\n\t"
      "s_mov_b32 s51, 61952\n\t"
      "v_mfma_f32_16x16x32_bf16 v[20:23], v[244:247], a[236:239], v[20:23]\n\t"
      "s_mov_b32 s58, s14\n\t"
      "s_add_u32 s56, s14, 2359296\n\t"
      "v_mfma_f32_16x16x32_bf16 v[24:27], v[248:251], a[236:239], v[24:27]\n\t"
      "s_movk_i32 s39, 84\n\t"
      "s_movk_i32 s29, 74\n\t"
      "s_mov_b32 s32, 286331262\n\t"
      "v_mfma_f32_16x16x32_bf16 v[28:31], v[252:255], a[236:239], v[28:31]\n\t"
      "s_mov_b32 s33, 89477937\n\t"
      "s_mov_b32 s34, 286331226\n\t"
      "s_mov_b32 s35, 5591857\n\t"
      "v_mfma_f32_16x16x32_bf16 v[32:35], a[148:151], a[236:239], v[32:35]\n\t"
      "v_mov_b32 v52, 163328\n\t"
      "v_mfma_f32_16x16x32_bf16 v[36:39], a[152:155], a[236:239], v[36:39]\n\t"
      "s_cmp_lg_u32 s17, 0\n\t"
      "s_cselect_b32 s39, 0, s39\n\t"
      "s_cbranch_scc1 .Lno4_%=\n\t"
      "v_accvgpr_read_b32 v41, a248\n\t"
      "v_mov_b32 v51, 0x4040\n\t"
      "s_movk_i32 s52, 2000\n\t"
      "s_mov_b32 s91, 0\n\t"
      "s_waitcnt vmcnt(6) \n\t"
      "v_accvgpr_read_b32 v40, a252\n\t"
      ".Lcheck_%=:\n\t"
      "v_cmp_ne_u32 s[54:55], v40, v63\n\t"
      "s_cmp_lg_u32 s16, 0\n\t"
      "v_add_u32_dpp v42, v40, v40 quad_perm:[2,3,0,1] row_mask:0xf bank_mask:0xf\n\t"
      "s_cselect_b32 s30, -1, s54\n\t"
      "s_cmp_lg_u32 s30, -1\n\t"
      "s_cbranch_scc1 .Lwait_%=\n\t"
      ".Lwaited_%=:\n\t"
      "v_add_u32_dpp v42, v42, v42 quad_perm:[1,0,3,2] row_mask:0xf bank_mask:0xf\n\t"
      "s_bfe_u32 s30, s19, 0x30008\n\t"
      "s_bfe_u32 s31, s19, 0x10003\n\t"
      "s_lshl1_add_u32 s30, s30, s31\n\t"
      "s_lshl_b32 s30, s30, 1\n\t"
      "v_mov_b32 v43, v42\n\t"
      "v_subrev_u32_dpp v43, v42, v51 row_half_mirror row_mask:0x3 bank_mask:0xf\n\t"
      "v_lshl_add_u32 v42, v42, 1, v43\n\t"
      "v_readlane_b32 s31, v40, s30\n\t"
      "v_add_u32 v42, 0x1f1f, v42\n\t"
      "v_and_b32 v43, 128, v42\n\t"
      "v_and_b32 v44, 32768, v42\n\t"
      "v_cmp_ne_u32 s[86:87], 0, v43\n\t"
      "v_cmp_ne_u32 s[36:37], 0, v44\n\t"
      "s_bitcmp1_b32 s19, 2\n\t"
      "s_cselect_b32 s30, 0x3ffff, 0\n\t"
      "s_bitcmp1_b32 s31, 16\n\t"
      "s_cselect_b32 s31, 262140, 0\n\t"
      "s_xor_b32 s30, s30, s31\n\t"
      "v_lshrrev_b32 v41, v41, s86\n\t"
      "v_and_b32 v41, 1, v41\n\t"
      "v_cmp_ne_u32 vcc, 0, v41\n\t"
      "s_xor_b32 s36, s37, s87\n\t"
      "s_and_b32 s36, s36, 0x11111111\n\t"
      "s_bcnt1_i32_b32 s36, s36\n\t"
      "s_and_b32 s31, vcc_lo, 262140\n\t"
      "s_xor_b32 s31, s31, s30\n\t"
      "s_or_b32 s31, s31, s91\n\t"
      "s_and_b32 s30, s16, 0x3ffff\n\t"
      "s_cmp_lg_u32 s16, 0\n\t"
      "s_cselect_b32 s31, s30, s31\n\t"
      "v_mov_b32 v53, s31\n\t"
      "ds_write_b32 v52, v53\n\t"
      "s_lshl_b32 s30, s36, 2\n\t"
      "s_mov_b32 s54, 1394671616\n\t"
      "s_mov_b32 s55, 6\n\t"
      "s_lshr_b64 s[54:55], s[54:55], s30\n\t"
      "s_and_b32 s30, s54, 15\n\t"
      "s_add_u32 s29, s29, s30\n\t"
      ".Lno4_%=:\n\t"
      "s_waitcnt vmcnt(3)\n\t"
      "s_waitcnt lgkmcnt(0)\n\t"
      "s_barrier\n\t"
      "ds_read_b32 v63, v52\n\t"
      "s_add_u32 s90, s90, 0x20000\n\t"
      "s_or_b32 s30, s17, s16\n\t"
      "s_cbranch_scc1 .Lno5_%=\n\t"
      "v_mov_b32 v54, s90\n\t"
      "v_mov_b32 v55, s19\n\t"
      "global_store_dword v55, v54, s[12:13] sc1\n\t"
      ".Lno5_%=:\n\t"
      "s_memrealtime s[36:37]\n\t"
      "v_mfma_f32_16x16x32_bf16 a[156:159], v[224:227], a[240:243], a[156:159]\n\t"
      "ds_read_b128 a[0:3], v56 offset:49664\n\t"
      "ds_read_b128 a[4:7], v57 offset:49664\n\t"
      "v_mfma_f32_16x16x32_bf16 a[160:163], v[228:231], a[240:243], a[160:163]\n\t"
      "ds_read_b128 a[8:11], v58 offset:49664\n\t"
      "ds_read_b128 a[12:15], v59 offset:49664\n\t"
      "v_mfma_f32_16x16x32_bf16 a[164:167], v[232:235], a[240:243], a[164:167]\n\t"
      "ds_read_b128 a[16:19], v56 offset:49920\n\t"
      "ds_read_b128 a[20:23], v57 offset:49920\n\t"
      "v_mfma_f32_16x16x32_bf16 a[168:171], v[236:239], a[240:243], a[168:171]\n\t"
      "v_add_u32 v56, 49664, v56\n\t"
      "v_add_u32 v57, 49664, v57\n\t"
      "v_mfma_f32_16x16x32_bf16 a[172:175], a[140:143], a[240:243], a[172:175]\n\t"
      "v_add_u32 v58, 49664, v58\n\t"
      "v_add_u32 v59, 49664, v59\n\t"
      "v_mfma_f32_16x16x32_bf16 a[176:179], a[144:147], a[240:243], a[176:179]\n\t"
      "v_cvt_pk_bf16_f32 v40, v16, v17\n\t"
      "v_cvt_pk_bf16_f32 v41, v18, v19\n\t"
      "v_mfma_f32_16x16x32_bf16 a[156:159], v[240:243], a[244:247], a[156:159]\n\t"
      "v_cvt_pk_bf16_f32 v42, v20, v21\n\t"
      "v_cvt_pk_bf16_f32 v43, v22, v23\n\t"
      "v_mfma_f32_16x16x32_bf16 a[160:163], v[244:247], a[244:247], a[160:163]\n\t"
      "ds_write_b64 v0, v[40:41] offset:0\n\t"
      "ds_write_b64 v0, v[42:43] offset:32\n\t"
      "v_mfma_f32_16x16x32_bf16 a[164:167], v[248:251], a[244:247], a[164:167]\n\t"
      "v_cvt_pk_bf16_f32 v44, v24, v25\n\t"
      "v_cvt_pk_bf16_f32 v45, v26, v27\n\t"
      "v_mfma_f32_16x16x32_bf16 a[168:171], v[252:255], a[244:247], a[168:171]\n\t"
      "v_cvt_pk_bf16_f32 v46, v28, v29\n\t"
      "v_cvt_pk_bf16_f32 v47, v30, v31\n\t"
      "v_mfma_f32_16x16x32_bf16 a[172:175], a[148:151], a[244:247], a[172:175]\n\t"
      "ds_write_b64 v0, v[44:45] offset:64\n\t"
      "ds_write_b64 v0, v[46:47] offset:96\n\t"
      "v_mfma_f32_16x16x32_bf16 a[176:179], a[152:155], a[244:247], a[176:179]\n\t"
      "v_cvt_pk_bf16_f32 v48, v32, v33\n\t"
      "v_cvt_pk_bf16_f32 v49, v34, v35\n\t"
      "v_cvt_pk_bf16_f32 v50, v36, v37\n\t"
      "v_cvt_pk_bf16_f32 v51, v38, v39\n\t"
      "ds_write_b64 v0, v[48:49] offset:128\n\t"
      "ds_write_b64 v0, v[50:51] offset:160\n\t"
      "v_xor_b32 v62, v2, v1\n\t"
      "v_and_b32 v62, 0xf0, v62\n\t"
      "v_xor_b32 v9, v62, v2\n\t"
      "v_xor_b32 v12, v62, v5\n\t"
      "v_add_u32 v8, v62, v1\n\t"
      "v_xor_b32 v62, v3, v1\n\t"
      "v_and_b32 v62, 0xf0, v62\n\t"
      "v_xor_b32 v10, v62, v3\n\t"
      "v_xor_b32 v13, v62, v6\n\t"
      "v_add_u32 v60, v62, v1\n\t"
      "v_xor_b32 v62, v4, v1\n\t"
      "v_and_b32 v62, 0xf0, v62\n\t"
      "v_xor_b32 v11, v62, v4\n\t"
      "v_xor_b32 v14, v62, v7\n\t"
      "v_add_u32 v61, v62, v1\n\t"
      "s_waitcnt lgkmcnt(12)\n\t"
      "v_readfirstlane_b32 s91, v63\n\t"
      "s_nop 3\n\t"
      "s_mov_b32 s59, s91\n\t"
      "s_lshr_b32 s57, s91, 3\n\t"
      "s_add_u32 m0, s18, 74240\n\t"
      "s_bitcmp1_b32 s91, 2\n\t"
      "s_cselect_b32 s48, 24576, 0\n\t"
      "s_add_u32 s48, s48, s14\n\t"
      "s_add_u32 s48, s48, 1572864\n\t"
      "buffer_load_dwordx4 v2, s[40:43], s48 offen nt lds\n\t"
      "buffer_load_dwordx4 v3, s[40:43], s48 offen offset:1024 nt lds\n\t"
      "buffer_load_dwordx4 v4, s[40:43], s48 offen offset:2048 nt lds\n\t"
      "s_mov_b32 s46, 113246208\n\t"
      "s_bitcmp1_b32 s91, 3\n\t"
      "s_cselect_b32 s48, 24576, 0\n\t"
      "s_add_u32 s48, s48, s14\n\t"
      "s_add_u32 s48, s48, 2359296\n\t"
      "s_mov_b32 s52, 15\n\t"
      "s_waitcnt lgkmcnt(11)\n\t"
      "v_mfma_f32_16x16x32_bf16 v[16:19], v[64:67], a[0:3], 0\n\t"
      "ds_read_b128 a[24:27], v58 offset:256\n\t"
      "ds_read_b128 a[28:31], v59 offset:256\n\t"
      "s_waitcnt lgkmcnt(12)\n\t"
      "v_mfma_f32_16x16x32_bf16 v[16:19], v[80:83], a[4:7], v[16:19]\n\t"
      "ds_read_b128 a[32:35], v56 offset:512\n\t"
      "ds_read_b128 a[36:39], v57 offset:512\n\t"
      "s_waitcnt lgkmcnt(13)\n\t"
      "v_mfma_f32_16x16x32_bf16 v[16:19], v[96:99], a[8:11], v[16:19]\n\t"
      "ds_read_b128 a[40:43], v58 offset:512\n\t"
      "ds_read_b128 a[44:47], v59 offset:512\n\t"
      "s_waitcnt lgkmcnt(14)\n\t"
      "v_mfma_f32_16x16x32_bf16 v[16:19], v[112:115], a[12:15], v[16:19]\n\t"
      "v_accvgpr_read_b32 v52, a156\n\t"
      "v_accvgpr_read_b32 v53, a157\n\t"
      "s_waitcnt lgkmcnt(13)\n\t"
      "v_mfma_f32_16x16x32_bf16 v[16:19], v[128:131], a[16:19], v[16:19]\n\t"
      "v_accvgpr_read_b32 v54, a158\n\t"
      "v_accvgpr_read_b32 v55, a159\n\t"
      "s_waitcnt lgkmcnt(12)\n\t"
      "v_mfma_f32_16x16x32_bf16 v[16:19], v[144:147], a[20:23], v[16:19]\n\t"
      "v_cvt_pk_bf16_f32 v40, v52, v53\n\t"
      "v_cvt_pk_bf16_f32 v41, v54, v55\n\t"
      "v_mfma_f32_16x16x32_bf16 v[20:23], v[68:71], a[0:3], 0\n\t"
      "ds_write_b64 v0, v[40:41] offset:12544\n\t"
      "v_mfma_f32_16x16x32_bf16 v[20:23], v[84:87], a[4:7], v[20:23]\n\t"
      "s_waitcnt lgkmcnt(7)\n\t"
      "s_barrier\n\t"
      "v_mfma_f32_16x16x32_bf16 v[20:23], v[100:103], a[8:11], v[20:23]\n\t"
      "ds_read_b128 a[48:51], v8 offset:0\n\t"
      "ds_read_b128 a[52:55], v60 offset:1024\n\t"
      "ds_read_b128 a[56:59], v61 offset:2048\n\t"
      "v_mfma_f32_16x16x32_bf16 v[20:23], v[116:119], a[12:15], v[20:23]\n\t"
      "v_accvgpr_read_b32 v52, a160\n\t"
      "v_accvgpr_read_b32 v53, a161\n\t"
      "v_mfma_f32_16x16x32_bf16 v[20:23], v[132:135], a[16:19], v[20:23]\n\t"
      "v_accvgpr_read_b32 v54, a162\n\t"
      "v_accvgpr_read_b32 v55, a163\n\t"
      "v_mfma_f32_16x16x32_bf16 v[20:23], v[148:151], a[20:23], v[20:23]\n\t"
      "v_cvt_pk_bf16_f32 v42, v52, v53\n\t"
      "v_cvt_pk_bf16_f32 v43, v54, v55\n\t"
      "v_mfma_f32_16x16x32_bf16 v[24:27], v[72:75], a[0:3], 0\n\t"
      "ds_write_b64 v0, v[42:43] offset:12576\n\t"
      "s_waitcnt lgkmcnt(3)\n\t"
      "v_mfma_f32_16x16x32_bf16 v[24:27], v[88:91], a[4:7], v[24:27]\n\t"
      "buffer_store_dwordx4 a[48:51], v9, s[44:47], s49 offen sc1\n\t"
      "v_mfma_f32_16x16x32_bf16 v[24:27], v[104:107], a[8:11], v[24:27]\n\t"
      "v_accvgpr_read_b32 v52, a164\n\t"
      "v_accvgpr_read_b32 v53, a165\n\t"
      "v_mfma_f32_16x16x32_bf16 v[24:27], v[120:123], a[12:15], v[24:27]\n\t"
      "v_accvgpr_read_b32 v54, a166\n\t"
      "v_accvgpr_read_b32 v55, a167\n\t"
      "v_mfma_f32_16x16x32_bf16 v[24:27], v[136:139], a[16:19], v[24:27]\n\t"
      "v_cvt_pk_bf16_f32 v44, v52, v53\n\t"
      "v_cvt_pk_bf16_f32 v45, v54, v55\n\t"
      "v_mfma_f32_16x16x32_bf16 v[24:27], v[152:155], a[20:23], v[24:27]\n\t"
      "ds_write_b64 v0, v[44:45] offset:12608\n\t"
      "s_waitcnt lgkmcnt(3)\n\t"
      "v_mfma_f32_16x16x32_bf16 v[28:31], v[76:79], a[0:3], 0\n\t"
      "buffer_store_dwordx4 a[52:55], v10, s[44:47], s49 offen offset:1024 sc1\n\t"
      "v_mfma_f32_16x16x32_bf16 v[28:31], v[92:95], a[4:7], v[28:31]\n\t"
      "v_accvgpr_read_b32 v52, a168\n\t"
      "v_accvgpr_read_b32 v53, a169\n\t"
      "v_mfma_f32_16x16x32_bf16 v[28:31], v[108:111], a[8:11], v[28:31]\n\t"
      "v_accvgpr_read_b32 v54, a170\n\t"
      "v_accvgpr_read_b32 v55, a171\n\t"
      "v_mfma_f32_16x16x32_bf16 v[28:31], v[124:127], a[12:15], v[28:31]\n\t"
      "v_cvt_pk_bf16_f32 v46, v52, v53\n\t"
      "v_cvt_pk_bf16_f32 v47, v54, v55\n\t"
      "v_mfma_f32_16x16x32_bf16 v[28:31], v[140:143], a[16:19], v[28:31]\n\t"
      "ds_write_b64 v0, v[46:47] offset:12640\n\t"
      "s_waitcnt lgkmcnt(3)\n\t"
      "v_mfma_f32_16x16x32_bf16 v[28:31], v[156:159], a[20:23], v[28:31]\n\t"
      "buffer_store_dwordx4 a[56:59], v11, s[44:47], s49 offen offset:2048 sc1\n\t"
      "v_mfma_f32_16x16x32_bf16 v[32:35], a[60:63], a[0:3], 0\n\t"
      "v_accvgpr_read_b32 v52, a172\n\t"
      "v_accvgpr_read_b32 v53, a173\n\t"
      "v_mfma_f32_16x16x32_bf16 v[32:35], a[68:71], a[4:7], v[32:35]\n\t"
      "v_accvgpr_read_b32 v54, a174\n\t"
      "v_accvgpr_read_b32 v55, a175\n\t"
      "v_mfma_f32_16x16x32_bf16 v[32:35], a[76:79], a[8:11], v[32:35]\n\t"
      "v_cvt_pk_bf16_f32 v48, v52, v53\n\t"
      "v_cvt_pk_bf16_f32 v49, v54, v55\n\t"
      "v_mfma_f32_16x16x32_bf16 v[32:35], a[84:87], a[12:15], v[32:35]\n\t"
      "ds_write_b64 v0, v[48:49] offset:12672\n\t"
      "v_mfma_f32_16x16x32_bf16 v[32:35], a[92:95], a[16:19], v[32:35]\n\t"
      "v_accvgpr_read_b32 v52, a176\n\t"
      "v_accvgpr_read_b32 v53, a177\n\t"
      "v_mfma_f32_16x16x32_bf16 v[32:35], a[100:103], a[20:23], v[32:35]\n\t"
      "v_accvgpr_read_b32 v54, a178\n\t"
      "v_accvgpr_read_b32 v55, a179\n\t"
      "v_mfma_f32_16x16x32_bf16 v[36:39], a[64:67], a[0:3], 0\n\t"
      "v_cvt_pk_bf16_f32 v50, v52, v53\n\t"
      "v_cvt_pk_bf16_f32 v51, v54, v55\n\t"
      "v_mfma_f32_16x16x32_bf16 v[36:39], a[72:75], a[4:7], v[36:39]\n\t"
      "ds_write_b64 v0, v[50:51] offset:12704\n\t"
      "v_mfma_f32_16x16x32_bf16 v[36:39], a[80:83], a[8:11], v[36:39]\n\t"
      "v_add_u32 v52, s51, v15\n\t"
      "v_xor_b32 v53, 64, v52\n\t"
      "v_mfma_f32_16x16x32_bf16 v[36:39], a[88:91], a[12:15], v[36:39]\n\t"
      "v_xor_b32 v54, 128, v52\n\t"
      "v_xor_b32 v55, 192, v52\n\t"
      "v_mfma_f32_16x16x32_bf16 v[36:39], a[96:99], a[16:19], v[36:39]\n\t"
      "s_waitcnt lgkmcnt(0)\n\t"
      "s_add_u32 s38, s36, s39\n\t"
      "s_sub_u32 s28, s36, s28\n\t"
      "s_sub_i32 s28, s28, 530\n\t"
      "s_max_i32 s28, s28, 0\n\t"
      "s_min_i32 s28, s28, 90\n\t"
      "s_and_b32 s39, s32, 15\n\t"
      "s_cselect_b32 s53, s29, 0\n\t"
      "s_memrealtime s[36:37]\n\t"
      "v_mfma_f32_16x16x32_bf16 v[36:39], a[104:107], a[20:23], v[36:39]\n\t"
      "s_add_u32 s39, s39, s53\n\t"
      "s_lshr_b64 s[32:33], s[32:33], 4\n\t"
      "s_add_u32 m0, s18, 86528\n\t"
      "v_mfma_f32_16x16x32_bf16 v[16:19], v[160:163], a[24:27], v[16:19]\n\t"
      "s_bitcmp1_b32 s91, 2\n\t"
      "s_cselect_b32 s54, 24576, 0\n\t"
      "s_add_u32 s54, s54, s14\n\t"
      "s_add_u32 s54, s54, 1572864\n\t"
      "v_mfma_f32_16x16x32_bf16 v[16:19], v[176:179], a[28:31], v[16:19]\n\t"
      "s_waitcnt vmcnt(6)\n\t"
      "s_barrier\n\t"
      "v_mfma_f32_16x16x32_bf16 v[16:19], v[192:195], a[32:35], v[16:19]\n\t"
      "ds_read_b128 a[0:3], v52 offset:0\n\t"
      "ds_read_b128 a[188:191], v8 offset:12544\n\t"
      "buffer_load_dwordx4 v5, s[40:43], s54 offen nt lds\n\t"
      "v_mfma_f32_16x16x32_bf16 v[16:19], v[208:211], a[36:39], v[16:19]\n\t"
      "ds_read_b128 a[4:7], v53 offset:0\n\t"
      "ds_read_b128 a[192:195], v60 offset:13568\n\t"
      "v_mfma_f32_16x16x32_bf16 v[16:19], v[224:227], a[40:43], v[16:19]\n\t"
      "ds_read_b128 a[8:11], v54 offset:0\n\t"
      "ds_read_b128 a[196:199], v61 offset:14592\n\t"
      "buffer_load_dwordx4 v6, s[40:43], s54 offen offset:1024 nt lds\n\t"
      "v_mfma_f32_16x16x32_bf16 v[16:19], v[240:243], a[44:47], v[16:19]\n\t"
      "ds_read_b128 a[12:15], v55 offset:0\n\t"
      "v_mfma_f32_16x16x32_bf16 v[20:23], v[164:167], a[24:27], v[20:23]\n\t"
      "ds_read_b128 a[16:19], v52 offset:256\n\t"
      "buffer_load_dwordx4 v7, s[40:43], s54 offen offset:2048 nt lds\n\t"
      "v_mfma_f32_16x16x32_bf16 v[20:23], v[180:183], a[28:31], v[20:23]\n\t"
      "ds_read_b128 a[20:23], v53 offset:256\n\t"
      "s_add_u32 m0, s18, s50\n\t"
      "v_mfma_f32_16x16x32_bf16 v[20:23], v[196:199], a[32:35], v[20:23]\n\t"
      "s_add_u32 s51, s51, 12288\n\t"
      "s_cmp_eq_u32 s51, 160256\n\t"
      "s_cselect_b32 s51, 25088, s51\n\t"
      "s_waitcnt lgkmcnt(7)\n\t"
      "v_mfma_f32_16x16x32_bf16 v[20:23], v[212:215], a[36:39], v[20:23]\n\t"
      "buffer_store_dwordx4 a[188:191], v12, s[44:47], s49 offen sc1\n\t"
      "v_mfma_f32_16x16x32_bf16 v[20:23], v[228:231], a[40:43], v[20:23]\n\t"
      "v_mfma_f32_16x16x32_bf16 v[20:23], v[244:247], a[44:47], v[20:23]\n\t"
      "s_waitcnt lgkmcnt(0)\n\t"
      "s_sub_u32 s53, s36, s38\n\t"
      "s_cmp_lt_i32 s53, 0\n\t"
      "v_mfma_f32_16x16x32_bf16 v[24:27], v[168:171], a[24:27], v[24:27]\n\t"
      "s_cbranch_scc1 .Lspinf_%=\n\t"
      ".Lspunf_%=:\n\t"
      "s_sub_i32 s53, s53, 30\n\t"
      "s_max_i32 s53, s53, 0\n\t"
      "v_mfma_f32_16x16x32_bf16 v[24:27], v[184:187], a[28:31], v[24:27]\n\t"
      "s_add_u32 s38, s38, s53\n\t"
      "s_add_u32 s38, s38, s39\n\t"
      "v_mfma_f32_16x16x32_bf16 v[24:27], v[200:203], a[32:35], v[24:27]\n\t"
      "v_cvt_pk_bf16_f32 v40, v16, v17\n\t"
      "v_cvt_pk_bf16_f32 v41, v18, v19\n\t"
      "v_mfma_f32_16x16x32_bf16 v[24:27], v[216:219], a[36:39], v[24:27]\n\t"
      "ds_write_b64 v0, v[40:41] offset:0\n\t"
      "v_mfma_f32_16x16x32_bf16 v[24:27], v[232:235], a[40:43], v[24:27]\n\t"
      "buffer_load_dwordx4 v2, s[40:43], s48 offen nt lds\n\t"
      "v_mfma_f32_16x16x32_bf16 v[24:27], v[248:251], a[44:47], v[24:27]\n\t"
      "buffer_store_dwordx4 a[192:195], v13, s[44:47], s49 offen offset:1024 sc1\n\t"
      "v_mfma_f32_16x16x32_bf16 v[28:31], v[172:175], a[24:27], v[28:31]\n\t"
      "v_mfma_f32_16x16x32_bf16 v[28:31], v[188:191], a[28:31], v[28:31]\n\t"
      "v_cvt_pk_bf16_f32 v42, v20, v21\n\t"
      "v_cvt_pk_bf16_f32 v43, v22, v23\n\t"
      "v_mfma_f32_16x16x32_bf16 v[28:31], v[204:207], a[32:35], v[28:31]\n\t"
      "ds_write_b64 v0, v[42:43] offset:32\n\t"
      "v_mfma_f32_16x16x32_bf16 v[28:31], v[220:223], a[36:39], v[28:31]\n\t"
      "buffer_load_dwordx4 v3, s[40:43], s48 offen offset:1024 nt lds\n\t"
      "v_mfma_f32_16x16x32_bf16 v[28:31], v[236:239], a[40:43], v[28:31]\n\t"
      "buffer_store_dwordx4 a[196:199], v14, s[44:47], s49 offen offset:2048 sc1\n\t"
      "v_mfma_f32_16x16x32_bf16 v[28:31], v[252:255], a[44:47], v[28:31]\n\t"
      "s_add_u32 s58, s58, 786432\n\t"
      "s_lshr_b32 s59, s59, 1\n\t"
      "v_mfma_f32_16x16x32_bf16 v[32:35], a[108:111], a[24:27], v[32:35]\n\t"
      "s_bitcmp1_b32 s59, 0\n\t"
      "s_cselect_b32 s49, 24576, 0\n\t"
      "s_add_u32 s49, s49, s58\n\t"
      "v_mfma_f32_16x16x32_bf16 v[32:35], a[116:119], a[28:31], v[32:35]\n\t"
      "v_cvt_pk_bf16_f32 v44, v24, v25\n\t"
      "v_cvt_pk_bf16_f32 v45, v26, v27\n\t"
      "v_mfma_f32_16x16x32_bf16 v[32:35], a[124:127], a[32:35], v[32:35]\n\t"
      "ds_write_b64 v0, v[44:45] offset:64\n\t"
      "v_mfma_f32_16x16x32_bf16 v[32:35], a[132:135], a[36:39], v[32:35]\n\t"
      "buffer_load_dwordx4 v4, s[40:43], s48 offen offset:2048 nt lds\n\t"
      "v_mfma_f32_16x16x32_bf16 v[32:35], a[140:143], a[40:43], v[32:35]\n\t"
      "s_add_u32 s50, s50, 12288\n\t"
      "v_mfma_f32_16x16x32_bf16 v[32:35], a[148:151], a[44:47], v[32:35]\n\t"
      "v_cvt_pk_bf16_f32 v46, v28, v29\n\t"
      "v_cvt_pk_bf16_f32 v47, v30, v31\n\t"
      "v_mfma_f32_16x16x32_bf16 v[36:39], a[112:115], a[24:27], v[36:39]\n\t"
      "ds_write_b64 v0, v[46:47] offset:96\n\t"
      "v_mfma_f32_16x16x32_bf16 v[36:39], a[120:123], a[28:31], v[36:39]\n\t"
      "s_cmp_eq_u32 s50, 160256\n\t"
      "s_cselect_b32 s50, 25088, s50\n\t"
      "v_mfma_f32_16x16x32_bf16 v[36:39], a[128:131], a[32:35], v[36:39]\n\t"
      "v_mfma_f32_16x16x32_bf16 v[36:39], a[136:139], a[36:39], v[36:39]\n\t"
      "v_mfma_f32_16x16x32_bf16 v[36:39], a[144:147], a[40:43], v[36:39]\n\t"
      "v_cvt_pk_bf16_f32 v48, v32, v33\n\t"
      "v_cvt_pk_bf16_f32 v49, v34, v35\n\t"
      "v_mfma_f32_16x16x32_bf16 v[36:39], a[152:155], a[44:47], v[36:39]\n\t"
      "ds_write_b64 v0, v[48:49] offset:128\n\t"
      "s_cmp_eq_u32 s17, 6144\n\t"
      "s_cbranch_scc1 .Lmid1_%=\n\t"
      "s_cmp_eq_u32 s17, 12288\n\t"
      "s_cbranch_scc1 .Lmid2_%=\n\t"
      "s_cmp_eq_u32 s17, 18432\n\t"
      "s_cbranch_scc1 .Lmid3_%=\n\t"
      "s_branch .Lmid0_%=\n\t"
      ".Lloop0_%=:\n\t"
      "v_mfma_f32_16x16x32_bf16 v[16:19], v[64:67], a[0:3], 0\n\t"
      "ds_read_b128 a[24:27], v58 offset:256\n\t"
      "v_mfma_f32_16x16x32_bf16 v[16:19], v[80:83], a[4:7], v[16:19]\n\t"
      "ds_read_b128 a[28:31], v59 offset:256\n\t"
      "v_mfma_f32_16x16x32_bf16 v[16:19], v[96:99], a[8:11], v[16:19]\n\t"
      "ds_read_b128 a[32:35], v56 offset:512\n\t"
      "v_mfma_f32_16x16x32_bf16 v[16:19], v[112:115], a[12:15], v[16:19]\n\t"
      "ds_read_b128 a[36:39], v57 offset:512\n\t"
      "v_mfma_f32_16x16x32_bf16 v[16:19], v[128:131], a[16:19], v[16:19]\n\t"
      "ds_read_b128 a[40:43], v58 offset:512\n\t"
      "v_mfma_f32_16x16x32_bf16 v[16:19], v[144:147], a[20:23], v[16:19]\n\t"
      "ds_read_b128 a[44:47], v59 offset:512\n\t"
      "v_mfma_f32_16x16x32_bf16 v[20:23], v[68:71], a[0:3], 0\n\t"
      "v_cvt_pk_bf16_f32 v50, v36, v37\n\t"
      "v_cvt_pk_bf16_f32 v51, v38, v39\n\t"
      "v_mfma_f32_16x16x32_bf16 v[20:23], v[84:87], a[4:7], v[20:23]\n\t"
      "ds_write_b64 v0, v[50:51] offset:12704\n\t"
      "v_mfma_f32_16x16x32_bf16 v[20:23], v[100:103], a[8:11], v[20:23]\n\t"
      "v_add_u32 v52, s51, v15\n\t"
      "v_xor_b32 v53, 64, v52\n\t"
      "v_mfma_f32_16x16x32_bf16 v[20:23], v[116:119], a[12:15], v[20:23]\n\t"
      "v_xor_b32 v54, 128, v52\n\t"
      "v_xor_b32 v55, 192, v52\n\t"
      "v_mfma_f32_16x16x32_bf16 v[20:23], v[132:135], a[16:19], v[20:23]\n\t"
      "s_add_u32 s56, s56, 786432\n\t"
      "s_lshr_b32 s57, s57, 1\n\t"
      "v_mfma_f32_16x16x32_bf16 v[20:23], v[148:151], a[20:23], v[20:23]\n\t"
      "s_bitcmp1_b32 s57, 0\n\t"
      "s_cselect_b32 s48, 24576, 0\n\t"
      "s_add_u32 s48, s48, s56\n\t"
      "v_mfma_f32_16x16x32_bf16 v[24:27], v[72:75], a[0:3], 0\n\t"
      "v_mfma_f32_16x16x32_bf16 v[24:27], v[88:91], a[4:7], v[24:27]\n\t"
      "v_mfma_f32_16x16x32_bf16 v[24:27], v[104:107], a[8:11], v[24:27]\n\t"
      "s_waitcnt vmcnt(12)\n\t"
      "s_waitcnt lgkmcnt(0)\n\t"
      "s_barrier\n\t"
      "v_mfma_f32_16x16x32_bf16 v[24:27], v[120:123], a[12:15], v[24:27]\n\t"
      "v_mfma_f32_16x16x32_bf16 v[24:27], v[136:139], a[16:19], v[24:27]\n\t"
      "v_mfma_f32_16x16x32_bf16 v[24:27], v[152:155], a[20:23], v[24:27]\n\t"
      "v_mfma_f32_16x16x32_bf16 v[28:31], v[76:79], a[0:3], 0\n\t"
      "ds_read_b128 a[48:51], v8 offset:12544\n\t"
      "v_mfma_f32_16x16x32_bf16 v[28:31], v[92:95], a[4:7], v[28:31]\n\t"
      "ds_read_b128 a[52:55], v60 offset:13568\n\t"
      "v_mfma_f32_16x16x32_bf16 v[28:31], v[108:111], a[8:11], v[28:31]\n\t"
      "ds_read_b128 a[56:59], v61 offset:14592\n\t"
      "v_mfma_f32_16x16x32_bf16 v[28:31], v[124:127], a[12:15], v[28:31]\n\t"
      "s_and_b32 s39, s32, 15\n\t"
      "s_cselect_b32 s53, s29, 0\n\t"
      "v_mfma_f32_16x16x32_bf16 v[28:31], v[140:143], a[16:19], v[28:31]\n\t"
      "s_add_u32 s39, s39, s53\n\t"
      "s_lshr_b64 s[32:33], s[32:33], 4\n\t"
      "v_mfma_f32_16x16x32_bf16 v[28:31], v[156:159], a[20:23], v[28:31]\n\t"
      "s_cmp_le_u32 s52, 11\n\t"
      "s_cselect_b32 s53, 4, 0\n\t"
      "s_min_u32 s53, s28, s53\n\t"
      "v_mfma_f32_16x16x32_bf16 v[32:35], a[60:63], a[0:3], 0\n\t"
      "s_sub_u32 s28, s28, s53\n\t"
      "s_sub_u32 s39, s39, s53\n\t"
      "v_mfma_f32_16x16x32_bf16 v[32:35], a[68:71], a[4:7], v[32:35]\n\t"
      "s_waitcnt lgkmcnt(2)\n\t"
      "v_mfma_f32_16x16x32_bf16 v[32:35], a[76:79], a[8:11], v[32:35]\n\t"
      "buffer_store_dwordx4 a[48:51], v12, s[44:47], s49 offen sc1\n\t"
      "v_mfma_f32_16x16x32_bf16 v[32:35], a[84:87], a[12:15], v[32:35]\n\t"
      "v_mfma_f32_16x16x32_bf16 v[32:35], a[92:95], a[16:19], v[32:35]\n\t"
      "v_mfma_f32_16x16x32_bf16 v[32:35], a[100:103], a[20:23], v[32:35]\n\t"
      "s_add_u32 m0, s18, s50\n\t"
      "v_mfma_f32_16x16x32_bf16 v[36:39], a[64:67], a[0:3], 0\n\t"
      "buffer_load_dwordx4 v2, s[40:43], s48 offen nt lds\n\t"
      "v_mfma_f32_16x16x32_bf16 v[36:39], a[72:75], a[4:7], v[36:39]\n\t"
      "s_memrealtime s[36:37]\n\t"
      "v_mfma_f32_16x16x32_bf16 v[36:39], a[80:83], a[8:11], v[36:39]\n\t"
      "v_mfma_f32_16x16x32_bf16 v[36:39], a[88:91], a[12:15], v[36:39]\n\t"
      "s_waitcnt lgkmcnt(1)\n\t"
      "v_mfma_f32_16x16x32_bf16 v[36:39], a[96:99], a[16:19], v[36:39]\n\t"
      "buffer_store_dwordx4 a[52:55], v13, s[44:47], s49 offen offset:1024 sc1\n\t"
      "v_mfma_f32_16x16x32_bf16 v[36:39], a[104:107], a[20:23], v[36:39]\n\t"
      "v_mfma_f32_16x16x32_bf16 v[16:19], v[160:163], a[24:27], v[16:19]\n\t"
      "ds_read_b128 a[0:3], v52 offset:0\n\t"
      "v_mfma_f32_16x16x32_bf16 v[16:19], v[176:179], a[28:31], v[16:19]\n\t"
      "ds_read_b128 a[4:7], v53 offset:0\n\t"
      "v_mfma_f32_16x16x32_bf16 v[16:19], v[192:195], a[32:35], v[16:19]\n\t"
      "ds_read_b128 a[8:11], v54 offset:0\n\t"
      "v_mfma_f32_16x16x32_bf16 v[16:19], v[208:211], a[36:39], v[16:19]\n\t"
      "ds_read_b128 a[12:15], v55 offset:0\n\t"
      "v_mfma_f32_16x16x32_bf16 v[16:19], v[224:227], a[40:43], v[16:19]\n\t"
      "ds_read_b128 a[16:19], v52 offset:256\n\t"
      "v_mfma_f32_16x16x32_bf16 v[16:19], v[240:243], a[44:47], v[16:19]\n\t"
      "ds_read_b128 a[20:23], v53 offset:256\n\t"
      "v_mfma_f32_16x16x32_bf16 v[20:23], v[164:167], a[24:27], v[20:23]\n\t"
      "s_add_u32 s51, s51, 12288\n\t"
      "s_cmp_eq_u32 s51, 160256\n\t"
      "s_cselect_b32 s51, 25088, s51\n\t"
      "v_mfma_f32_16x16x32_bf16 v[20:23], v[180:183], a[28:31], v[20:23]\n\t"
      "s_waitcnt lgkmcnt(6)\n\t"
      "v_mfma_f32_16x16x32_bf16 v[20:23], v[196:199], a[32:35], v[20:23]\n\t"
      "buffer_store_dwordx4 a[56:59], v14, s[44:47], s49 offen offset:2048 sc1\n\t"
      "v_mfma_f32_16x16x32_bf16 v[20:23], v[212:215], a[36:39], v[20:23]\n\t"
      "v_mfma_f32_16x16x32_bf16 v[20:23], v[228:231], a[40:43], v[20:23]\n\t"
      "s_waitcnt lgkmcnt(0)\n\t"
      "s_sub_u32 s53, s36, s38\n\t"
      "s_cmp_lt_i32 s53, 0\n\t"
      "v_mfma_f32_16x16x32_bf16 v[20:23], v[244:247], a[44:47], v[20:23]\n\t"
      "s_cbranch_scc1 .Lspin0_%=\n\t"
      ".Lspun0_%=:\n\t"
      "s_sub_i32 s53, s53, 30\n\t"
      "s_max_i32 s53, s53, 0\n\t"
      "v_mfma_f32_16x16x32_bf16 v[24:27], v[168:171], a[24:27], v[24:27]\n\t"
      "s_add_u32 s38, s38, s53\n\t"
      "s_add_u32 s38, s38, s39\n\t"
      "v_mfma_f32_16x16x32_bf16 v[24:27], v[184:187], a[28:31], v[24:27]\n\t"
      "v_cvt_pk_bf16_f32 v40, v16, v17\n\t"
      "v_cvt_pk_bf16_f32 v41, v18, v19\n\t"
      "v_mfma_f32_16x16x32_bf16 v[24:27], v[200:203], a[32:35], v[24:27]\n\t"
      "ds_write_b64 v0, v[40:41] offset:0\n\t"
      "v_mfma_f32_16x16x32_bf16 v[24:27], v[216:219], a[36:39], v[24:27]\n\t"
      "buffer_load_dwordx4 v3, s[40:43], s48 offen offset:1024 nt lds\n\t"
      "v_mfma_f32_16x16x32_bf16 v[24:27], v[232:235], a[40:43], v[24:27]\n\t"
      "s_add_u32 s58, s58, 786432\n\t"
      "s_lshr_b32 s59, s59, 1\n\t"
      "v_mfma_f32_16x16x32_bf16 v[24:27], v[248:251], a[44:47], v[24:27]\n\t"
      "s_bitcmp1_b32 s59, 0\n\t"
      "s_cselect_b32 s49, 24576, 0\n\t"
      "s_add_u32 s49, s49, s58\n\t"
      "v_mfma_f32_16x16x32_bf16 v[28:31], v[172:175], a[24:27], v[28:31]\n\t"
      "v_mfma_f32_16x16x32_bf16 v[28:31], v[188:191], a[28:31], v[28:31]\n\t"
      "v_cvt_pk_bf16_f32 v42, v20, v21\n\t"
      "v_cvt_pk_bf16_f32 v43, v22, v23\n\t"
      "v_mfma_f32_16x16x32_bf16 v[28:31], v[204:207], a[32:35], v[28:31]\n\t"
      "ds_write_b64 v0, v[42:43] offset:32\n\t"
      "v_mfma_f32_16x16x32_bf16 v[28:31], v[220:223], a[36:39], v[28:31]\n\t"
      "v_mfma_f32_16x16x32_bf16 v[28:31], v[236:239], a[40:43], v[28:31]\n\t"
      "v_mfma_f32_16x16x32_bf16 v[28:31], v[252:255], a[44:47], v[28:31]\n\t"
      "v_mfma_f32_16x16x32_bf16 v[32:35], a[108:111], a[24:27], v[32:35]\n\t"
      "v_mfma_f32_16x16x32_bf16 v[32:35], a[116:119], a[28:31], v[32:35]\n\t"
      "v_cvt_pk_bf16_f32 v44, v24, v25\n\t"
      "v_cvt_pk_bf16_f32 v45, v26, v27\n\t"
      "v_mfma_f32_16x16x32_bf16 v[32:35], a[124:127], a[32:35], v[32:35]\n\t"
      "ds_write_b64 v0, v[44:45] offset:64\n\t"
      "v_mfma_f32_16x16x32_bf16 v[32:35], a[132:135], a[36:39], v[32:35]\n\t"
      "buffer_load_dwordx4 v4, s[40:43], s48 offen offset:2048 nt lds\n\t"
      "v_mfma_f32_16x16x32_bf16 v[32:35], a[140:143], a[40:43], v[32:35]\n\t"
      "s_add_u32 s50, s50, 12288\n\t"
      "v_mfma_f32_16x16x32_bf16 v[32:35], a[148:151], a[44:47], v[32:35]\n\t"
      "v_cvt_pk_bf16_f32 v46, v28, v29\n\t"
      "v_cvt_pk_bf16_f32 v47, v30, v31\n\t"
      "v_mfma_f32_16x16x32_bf16 v[36:39], a[112:115], a[24:27], v[36:39]\n\t"
      "ds_write_b64 v0, v[46:47] offset:96\n\t"
      "v_mfma_f32_16x16x32_bf16 v[36:39], a[120:123], a[28:31], v[36:39]\n\t"
      "s_cmp_eq_u32 s50, 160256\n\t"
      "s_cselect_b32 s50, 25088, s50\n\t"
      "v_mfma_f32_16x16x32_bf16 v[36:39], a[128:131], a[32:35], v[36:39]\n\t"
      "v_mfma_f32_16x16x32_bf16 v[36:39], a[136:139], a[36:39], v[36:39]\n\t"
      "v_mfma_f32_16x16x32_bf16 v[36:39], a[144:147], a[40:43], v[36:39]\n\t"
      "v_cvt_pk_bf16_f32 v48, v32, v33\n\t"
      "v_cvt_pk_bf16_f32 v49, v34, v35\n\t"
      "v_mfma_f32_16x16x32_bf16 v[36:39], a[152:155], a[44:47], v[36:39]\n\t"
      "ds_write_b64 v0, v[48:49] offset:128\n\t"
      ".Lmid0_%=:\n\t"
      "v_mfma_f32_16x16x32_bf16 v[16:19], v[64:67], a[0:3], 0\n\t"
      "ds_read_b128 a[24:27], v54 offset:256\n\t"
      "v_mfma_f32_16x16x32_bf16 v[16:19], v[80:83], a[4:7], v[16:19]\n\t"
      "ds_read_b128 a[28:31], v55 offset:256\n\t"
      "v_mfma_f32_16x16x32_bf16 v[16:19], v[96:99], a[8:11], v[16:19]\n\t"
      "ds_read_b128 a[32:35], v52 offset:512\n\t"
      "v_mfma_f32_16x16x32_bf16 v[16:19], v[112:115], a[12:15], v[16:19]\n\t"
      "ds_read_b128 a[36:39], v53 offset:512\n\t"
      "v_mfma_f32_16x16x32_bf16 v[16:19], v[128:131], a[16:19], v[16:19]\n\t"
      "ds_read_b128 a[40:43], v54 offset:512\n\t"
      "v_mfma_f32_16x16x32_bf16 v[16:19], v[144:147], a[20:23], v[16:19]\n\t"
      "ds_read_b128 a[44:47], v55 offset:512\n\t"
      "v_mfma_f32_16x16x32_bf16 v[20:23], v[68:71], a[0:3], 0\n\t"
      "v_cvt_pk_bf16_f32 v50, v36, v37\n\t"
      "v_cvt_pk_bf16_f32 v51, v38, v39\n\t"
      "v_mfma_f32_16x16x32_bf16 v[20:23], v[84:87], a[4:7], v[20:23]\n\t"
      "ds_write_b64 v0, v[50:51] offset:160\n\t"
      "v_mfma_f32_16x16x32_bf16 v[20:23], v[100:103], a[8:11], v[20:23]\n\t"
      "v_add_u32 v56, s51, v15\n\t"
      "v_xor_b32 v57, 64, v56\n\t"
      "v_mfma_f32_16x16x32_bf16 v[20:23], v[116:119], a[12:15], v[20:23]\n\t"
      "v_xor_b32 v58, 128, v56\n\t"
      "v_xor_b32 v59, 192, v56\n\t"
      "v_mfma_f32_16x16x32_bf16 v[20:23], v[132:135], a[16:19], v[20:23]\n\t"
      "v_mfma_f32_16x16x32_bf16 v[20:23], v[148:151], a[20:23], v[20:23]\n\t"
      "v_mfma_f32_16x16x32_bf16 v[24:27], v[72:75], a[0:3], 0\n\t"
      "v_mfma_f32_16x16x32_bf16 v[24:27], v[88:91], a[4:7], v[24:27]\n\t"
      "v_mfma_f32_16x16x32_bf16 v[24:27], v[104:107], a[8:11], v[24:27]\n\t"
      "s_waitcnt vmcnt(12)\n\t"
      "s_waitcnt lgkmcnt(0)\n\t"
      "s_barrier\n\t"
      "v_mfma_f32_16x16x32_bf16 v[24:27], v[120:123], a[12:15], v[24:27]\n\t"
      "v_mfma_f32_16x16x32_bf16 v[24:27], v[136:139], a[16:19], v[24:27]\n\t"
      "v_mfma_f32_16x16x32_bf16 v[24:27], v[152:155], a[20:23], v[24:27]\n\t"
      "v_mfma_f32_16x16x32_bf16 v[28:31], v[76:79], a[0:3], 0\n\t"
      "ds_read_b128 a[48:51], v8 offset:0\n\t"
      "v_mfma_f32_16x16x32_bf16 v[28:31], v[92:95], a[4:7], v[28:31]\n\t"
      "ds_read_b128 a[52:55], v60 offset:1024\n\t"
      "v_mfma_f32_16x16x32_bf16 v[28:31], v[108:111], a[8:11], v[28:31]\n\t"
      "ds_read_b128 a[56:59], v61 offset:2048\n\t"
      "v_mfma_f32_16x16x32_bf16 v[28:31], v[124:127], a[12:15], v[28:31]\n\t"
      "s_and_b32 s39, s34, 15\n\t"
      "s_cselect_b32 s53, s29, 0\n\t"
      "v_mfma_f32_16x16x32_bf16 v[28:31], v[140:143], a[16:19], v[28:31]\n\t"
      "s_add_u32 s39, s39, s53\n\t"
      "s_lshr_b64 s[34:35], s[34:35], 4\n\t"
      "v_mfma_f32_16x16x32_bf16 v[28:31], v[156:159], a[20:23], v[28:31]\n\t"
      "s_cmp_le_u32 s52, 11\n\t"
      "s_cselect_b32 s53, 4, 0\n\t"
      "s_min_u32 s53, s28, s53\n\t"
      "v_mfma_f32_16x16x32_bf16 v[32:35], a[60:63], a[0:3], 0\n\t"
      "s_sub_u32 s28, s28, s53\n\t"
      "s_sub_u32 s39, s39, s53\n\t"
      "v_mfma_f32_16x16x32_bf16 v[32:35], a[68:71], a[4:7], v[32:35]\n\t"
      "s_waitcnt lgkmcnt(2)\n\t"
      "v_mfma_f32_16x16x32_bf16 v[32:35], a[76:79], a[8:11], v[32:35]\n\t"
      "buffer_store_dwordx4 a[48:51], v9, s[44:47], s49 offen sc1\n\t"
      "v_mfma_f32_16x16x32_bf16 v[32:35], a[84:87], a[12:15], v[32:35]\n\t"
      "v_mfma_f32_16x16x32_bf16 v[32:35], a[92:95], a[16:19], v[32:35]\n\t"
      "v_mfma_f32_16x16x32_bf16 v[32:35], a[100:103], a[20:23], v[32:35]\n\t"
      "s_add_u32 m0, s18, s50\n\t"
      "v_mfma_f32_16x16x32_bf16 v[36:39], a[64:67], a[0:3], 0\n\t"
      "buffer_load_dwordx4 v5, s[40:43], s48 offen nt lds\n\t"
      "v_mfma_f32_16x16x32_bf16 v[36:39], a[72:75], a[4:7], v[36:39]\n\t"
      "s_memrealtime s[36:37]\n\t"
      "v_mfma_f32_16x16x32_bf16 v[36:39], a[80:83], a[8:11], v[36:39]\n\t"
      "v_mfma_f32_16x16x32_bf16 v[36:39], a[88:91], a[12:15], v[36:39]\n\t"
      "s_waitcnt lgkmcnt(1)\n\t"
      "v_mfma_f32_16x16x32_bf16 v[36:39], a[96:99], a[16:19], v[36:39]\n\t"
      "buffer_store_dwordx4 a[52:55], v10, s[44:47], s49 offen offset:1024 sc1\n\t"
      "v_mfma_f32_16x16x32_bf16 v[36:39], a[104:107], a[20:23], v[36:39]\n\t"
      "v_mfma_f32_16x16x32_bf16 v[16:19], v[160:163], a[24:27], v[16:19]\n\t"
      "ds_read_b128 a[0:3], v56 offset:0\n\t"
      "v_mfma_f32_16x16x32_bf16 v[16:19], v[176:179], a[28:31], v[16:19]\n\t"
      "ds_read_b128 a[4:7], v57 offset:0\n\t"
      "v_mfma_f32_16x16x32_bf16 v[16:19], v[192:195], a[32:35], v[16:19]\n\t"
      "ds_read_b128 a[8:11], v58 offset:0\n\t"
      "v_mfma_f32_16x16x32_bf16 v[16:19], v[208:211], a[36:39], v[16:19]\n\t"
      "ds_read_b128 a[12:15], v59 offset:0\n\t"
      "v_mfma_f32_16x16x32_bf16 v[16:19], v[224:227], a[40:43], v[16:19]\n\t"
      "ds_read_b128 a[16:19], v56 offset:256\n\t"
      "v_mfma_f32_16x16x32_bf16 v[16:19], v[240:243], a[44:47], v[16:19]\n\t"
      "ds_read_b128 a[20:23], v57 offset:256\n\t"
      "v_mfma_f32_16x16x32_bf16 v[20:23], v[164:167], a[24:27], v[20:23]\n\t"
      "s_add_u32 s51, s51, 12288\n\t"
      "s_cmp_eq_u32 s51, 160256\n\t"
      "s_cselect_b32 s51, 25088, s51\n\t"
      "v_mfma_f32_16x16x32_bf16 v[20:23], v[180:183], a[28:31], v[20:23]\n\t"
      "s_waitcnt lgkmcnt(6)\n\t"
      "v_mfma_f32_16x16x32_bf16 v[20:23], v[196:199], a[32:35], v[20:23]\n\t"
      "buffer_store_dwordx4 a[56:59], v11, s[44:47], s49 offen offset:2048 sc1\n\t"
      "v_mfma_f32_16x16x32_bf16 v[20:23], v[212:215], a[36:39], v[20:23]\n\t"
      "v_mfma_f32_16x16x32_bf16 v[20:23], v[228:231], a[40:43], v[20:23]\n\t"
      "s_waitcnt lgkmcnt(0)\n\t"
      "s_sub_u32 s53, s36, s38\n\t"
      "s_cmp_lt_i32 s53, 0\n\t"
      "v_mfma_f32_16x16x32_bf16 v[20:23], v[244:247], a[44:47], v[20:23]\n\t"
      "s_cbranch_scc1 .Lspin1_%=\n\t"
      ".Lspun1_%=:\n\t"
      "s_sub_i32 s53, s53, 30\n\t"
      "s_max_i32 s53, s53, 0\n\t"
      "v_mfma_f32_16x16x32_bf16 v[24:27], v[168:171], a[24:27], v[24:27]\n\t"
      "s_add_u32 s38, s38, s53\n\t"
      "s_add_u32 s38, s38, s39\n\t"
      "v_mfma_f32_16x16x32_bf16 v[24:27], v[184:187], a[28:31], v[24:27]\n\t"
      "v_cvt_pk_bf16_f32 v40, v16, v17\n\t"
      "v_cvt_pk_bf16_f32 v41, v18, v19\n\t"
      "v_mfma_f32_16x16x32_bf16 v[24:27], v[200:203], a[32:35], v[24:27]\n\t"
      "ds_write_b64 v0, v[40:41] offset:12544\n\t"
      "v_mfma_f32_16x16x32_bf16 v[24:27], v[216:219], a[36:39], v[24:27]\n\t"
      "buffer_load_dwordx4 v6, s[40:43], s48 offen offset:1024 nt lds\n\t"
      "v_mfma_f32_16x16x32_bf16 v[24:27], v[232:235], a[40:43], v[24:27]\n\t"
      "v_mfma_f32_16x16x32_bf16 v[24:27], v[248:251], a[44:47], v[24:27]\n\t"
      "v_mfma_f32_16x16x32_bf16 v[28:31], v[172:175], a[24:27], v[28:31]\n\t"
      "v_mfma_f32_16x16x32_bf16 v[28:31], v[188:191], a[28:31], v[28:31]\n\t"
      "v_cvt_pk_bf16_f32 v42, v20, v21\n\t"
      "v_cvt_pk_bf16_f32 v43, v22, v23\n\t"
      "v_mfma_f32_16x16x32_bf16 v[28:31], v[204:207], a[32:35], v[28:31]\n\t"
      "ds_write_b64 v0, v[42:43] offset:12576\n\t"
      "v_mfma_f32_16x16x32_bf16 v[28:31], v[220:223], a[36:39], v[28:31]\n\t"
      "v_mfma_f32_16x16x32_bf16 v[28:31], v[236:239], a[40:43], v[28:31]\n\t"
      "v_mfma_f32_16x16x32_bf16 v[28:31], v[252:255], a[44:47], v[28:31]\n\t"
      "v_mfma_f32_16x16x32_bf16 v[32:35], a[108:111], a[24:27], v[32:35]\n\t"
      "v_mfma_f32_16x16x32_bf16 v[32:35], a[116:119], a[28:31], v[32:35]\n\t"
      "v_cvt_pk_bf16_f32 v44, v24, v25\n\t"
      "v_cvt_pk_bf16_f32 v45, v26, v27\n\t"
      "v_mfma_f32_16x16x32_bf16 v[32:35], a[124:127], a[32:35], v[32:35]\n\t"
      "ds_write_b64 v0, v[44:45] offset:12608\n\t"
      "v_mfma_f32_16x16x32_bf16 v[32:35], a[132:135], a[36:39], v[32:35]\n\t"
      "buffer_load_dwordx4 v7, s[40:43], s48 offen offset:2048 nt lds\n\t"
      "v_mfma_f32_16x16x32_bf16 v[32:35], a[140:143], a[40:43], v[32:35]\n\t"
      "s_add_u32 s50, s50, 12288\n\t"
      "v_mfma_f32_16x16x32_bf16 v[32:35], a[148:151], a[44:47], v[32:35]\n\t"
      "v_cvt_pk_bf16_f32 v46, v28, v29\n\t"
      "v_cvt_pk_bf16_f32 v47, v30, v31\n\t"
      "v_mfma_f32_16x16x32_bf16 v[36:39], a[112:115], a[24:27], v[36:39]\n\t"
      "ds_write_b64 v0, v[46:47] offset:12640\n\t"
      "v_mfma_f32_16x16x32_bf16 v[36:39], a[120:123], a[28:31], v[36:39]\n\t"
      "s_cmp_eq_u32 s50, 160256\n\t"
      "s_cselect_b32 s50, 25088, s50\n\t"
      "v_mfma_f32_16x16x32_bf16 v[36:39], a[128:131], a[32:35], v[36:39]\n\t"
      "s_sub_u32 s52, s52, 1\n\t"
      "s_cmp_lg_u32 s52, 0\n\t"
      "v_mfma_f32_16x16x32_bf16 v[36:39], a[136:139], a[36:39], v[36:39]\n\t"
      "v_mfma_f32_16x16x32_bf16 v[36:39], a[144:147], a[40:43], v[36:39]\n\t"
      "v_cvt_pk_bf16_f32 v48, v32, v33\n\t"
      "v_cvt_pk_bf16_f32 v49, v34, v35\n\t"
      "v_mfma_f32_16x16x32_bf16 v[36:39], a[152:155], a[44:47], v[36:39]\n\t"
      "ds_write_b64 v0, v[48:49] offset:12672\n\t"
      "s_cbranch_scc1 .Lloop0_%=\n\t"
      "s_cmp_eq_u32 s48, 113221632\n\t"
      "s_cbranch_scc1 .Lwrap0_%=\n\t"
      ".Lwrapped0_%=:\n\t"
      "s_mov_b32 s52, 2\n\t"
      ".Ldrain0_%=:\n\t"
      "v_mfma_f32_16x16x32_bf16 v[16:19], v[64:67], a[0:3], 0\n\t"
      "ds_read_b128 a[24:27], v58 offset:256\n\t"
      "v_mfma_f32_16x16x32_bf16 v[16:19], v[80:83], a[4:7], v[16:19]\n\t"
      "ds_read_b128 a[28:31], v59 offset:256\n\t"
      "v_mfma_f32_16x16x32_bf16 v[16:19], v[96:99], a[8:11], v[16:19]\n\t"
      "ds_read_b128 a[32:35], v56 offset:512\n\t"
      "v_mfma_f32_16x16x32_bf16 v[16:19], v[112:115], a[12:15], v[16:19]\n\t"
      "ds_read_b128 a[36:39], v57 offset:512\n\t"
      "v_mfma_f32_16x16x32_bf16 v[16:19], v[128:131], a[16:19], v[16:19]\n\t"
      "ds_read_b128 a[40:43], v58 offset:512\n\t"
      "v_mfma_f32_16x16x32_bf16 v[16:19], v[144:147], a[20:23], v[16:19]\n\t"
      "ds_read_b128 a[44:47], v59 offset:512\n\t"
      "v_mfma_f32_16x16x32_bf16 v[20:23], v[68:71], a[0:3], 0\n\t"
      "v_cvt_pk_bf16_f32 v50, v36, v37\n\t"
      "v_cvt_pk_bf16_f32 v51, v38, v39\n\t"
      "v_mfma_f32_16x16x32_bf16 v[20:23], v[84:87], a[4:7], v[20:23]\n\t"
      "ds_write_b64 v0, v[50:51] offset:12704\n\t"
      "v_mfma_f32_16x16x32_bf16 v[20:23], v[100:103], a[8:11], v[20:23]\n\t"
      "v_add_u32 v52, s51, v15\n\t"
      "v_xor_b32 v53, 64, v52\n\t"
      "v_mfma_f32_16x16x32_bf16 v[20:23], v[116:119], a[12:15], v[20:23]\n\t"
      "v_xor_b32 v54, 128, v52\n\t"
      "v_xor_b32 v55, 192, v52\n\t"
      "v_mfma_f32_16x16x32_bf16 v[20:23], v[132:135], a[16:19], v[20:23]\n\t"
      "v_mfma_f32_16x16x32_bf16 v[20:23], v[148:151], a[20:23], v[20:23]\n\t"
      "v_mfma_f32_16x16x32_bf16 v[24:27], v[72:75], a[0:3], 0\n\t"
      "v_mfma_f32_16x16x32_bf16 v[24:27], v[88:91], a[4:7], v[24:27]\n\t"
      "v_mfma_f32_16x16x32_bf16 v[24:27], v[104:107], a[8:11], v[24:27]\n\t"
      "s_waitcnt vmcnt(6)\n\t"
      "s_waitcnt lgkmcnt(0)\n\t"
      "s_barrier\n\t"
      "v_mfma_f32_16x16x32_bf16 v[24:27], v[120:123], a[12:15], v[24:27]\n\t"
      "v_mfma_f32_16x16x32_bf16 v[24:27], v[136:139], a[16:19], v[24:27]\n\t"
      "v_mfma_f32_16x16x32_bf16 v[24:27], v[152:155], a[20:23], v[24:27]\n\t"
      "v_mfma_f32_16x16x32_bf16 v[28:31], v[76:79], a[0:3], 0\n\t"
      "ds_read_b128 a[48:51], v8 offset:12544\n\t"
      "v_mfma_f32_16x16x32_bf16 v[28:31], v[92:95], a[4:7], v[28:31]\n\t"
      "ds_read_b128 a[52:55], v60 offset:13568\n\t"
      "v_mfma_f32_16x16x32_bf16 v[28:31], v[108:111], a[8:11], v[28:31]\n\t"
      "ds_read_b128 a[56:59], v61 offset:14592\n\t"
      "v_mfma_f32_16x16x32_bf16 v[28:31], v[124:127], a[12:15], v[28:31]\n\t"
      "v_mfma_f32_16x16x32_bf16 v[28:31], v[140:143], a[16:19], v[28:31]\n\t"
      "v_mfma_f32_16x16x32_bf16 v[28:31], v[156:159], a[20:23], v[28:31]\n\t"
      "v_mfma_f32_16x16x32_bf16 v[32:35], a[60:63], a[0:3], 0\n\t"
      "v_mfma_f32_16x16x32_bf16 v[32:35], a[68:71], a[4:7], v[32:35]\n\t"
      "s_waitcnt lgkmcnt(2)\n\t"
      "v_mfma_f32_16x16x32_bf16 v[32:35], a[76:79], a[8:11], v[32:35]\n\t"
      "buffer_store_dwordx4 a[48:51], v12, s[44:47], s49 offen sc1\n\t"
      "v_mfma_f32_16x16x32_bf16 v[32:35], a[84:87], a[12:15], v[32:35]\n\t"
      "v_mfma_f32_16x16x32_bf16 v[32:35], a[92:95], a[16:19], v[32:35]\n\t"
      "v_mfma_f32_16x16x32_bf16 v[32:35], a[100:103], a[20:23], v[32:35]\n\t"
      "v_mfma_f32_16x16x32_bf16 v[36:39], a[64:67], a[0:3], 0\n\t"
      "v_mfma_f32_16x16x32_bf16 v[36:39], a[72:75], a[4:7], v[36:39]\n\t"
      "v_mfma_f32_16x16x32_bf16 v[36:39], a[80:83], a[8:11], v[36:39]\n\t"
      "v_mfma_f32_16x16x32_bf16 v[36:39], a[88:91], a[12:15], v[36:39]\n\t"
      "s_waitcnt lgkmcnt(1)\n\t"
      "v_mfma_f32_16x16x32_bf16 v[36:39], a[96:99], a[16:19], v[36:39]\n\t"
      "buffer_store_dwordx4 a[52:55], v13, s[44:47], s49 offen offset:1024 sc1\n\t"
      "v_mfma_f32_16x16x32_bf16 v[36:39], a[104:107], a[20:23], v[36:39]\n\t"
      "v_mfma_f32_16x16x32_bf16 v[16:19], v[160:163], a[24:27], v[16:19]\n\t"
      "ds_read_b128 a[0:3], v52 offset:0\n\t"
      "v_mfma_f32_16x16x32_bf16 v[16:19], v[176:179], a[28:31], v[16:19]\n\t"
      "ds_read_b128 a[4:7], v53 offset:0\n\t"
      "v_mfma_f32_16x16x32_bf16 v[16:19], v[192:195], a[32:35], v[16:19]\n\t"
      "ds_read_b128 a[8:11], v54 offset:0\n\t"
      "v_mfma_f32_16x16x32_bf16 v[16:19], v[208:211], a[36:39], v[16:19]\n\t"
      "ds_read_b128 a[12:15], v55 offset:0\n\t"
      "v_mfma_f32_16x16x32_bf16 v[16:19], v[224:227], a[40:43], v[16:19]\n\t"
      "ds_read_b128 a[16:19], v52 offset:256\n\t"
      "v_mfma_f32_16x16x32_bf16 v[16:19], v[240:243], a[44:47], v[16:19]\n\t"
      "ds_read_b128 a[20:23], v53 offset:256\n\t"
      "v_mfma_f32_16x16x32_bf16 v[20:23], v[164:167], a[24:27], v[20:23]\n\t"
      "s_add_u32 s51, s51, 12288\n\t"
      "s_cmp_eq_u32 s51, 160256\n\t"
      "s_cselect_b32 s51, 25088, s51\n\t"
      "v_mfma_f32_16x16x32_bf16 v[20:23], v[180:183], a[28:31], v[20:23]\n\t"
      "s_waitcnt lgkmcnt(6)\n\t"
      "v_mfma_f32_16x16x32_bf16 v[20:23], v[196:199], a[32:35], v[20:23]\n\t"
      "buffer_store_dwordx4 a[56:59], v14, s[44:47], s49 offen offset:2048 sc1\n\t"
      "v_mfma_f32_16x16x32_bf16 v[20:23], v[212:215], a[36:39], v[20:23]\n\t"
      "v_mfma_f32_16x16x32_bf16 v[20:23], v[228:231], a[40:43], v[20:23]\n\t"
      "s_waitcnt lgkmcnt(0)\n\t"
      "v_mfma_f32_16x16x32_bf16 v[20:23], v[244:247], a[44:47], v[20:23]\n\t"
      "v_mfma_f32_16x16x32_bf16 v[24:27], v[168:171], a[24:27], v[24:27]\n\t"
      "v_mfma_f32_16x16x32_bf16 v[24:27], v[184:187], a[28:31], v[24:27]\n\t"
      "v_cvt_pk_bf16_f32 v40, v16, v17\n\t"
      "v_cvt_pk_bf16_f32 v41, v18, v19\n\t"
      "v_mfma_f32_16x16x32_bf16 v[24:27], v[200:203], a[32:35], v[24:27]\n\t"
      "ds_write_b64 v0, v[40:41] offset:0\n\t"
      "v_mfma_f32_16x16x32_bf16 v[24:27], v[216:219], a[36:39], v[24:27]\n\t"
      "v_mfma_f32_16x16x32_bf16 v[24:27], v[232:235], a[40:43], v[24:27]\n\t"
      "s_add_u32 s58, s58, 786432\n\t"
      "s_lshr_b32 s59, s59, 1\n\t"
      "v_mfma_f32_16x16x32_bf16 v[24:27], v[248:251], a[44:47], v[24:27]\n\t"
      "s_bitcmp1_b32 s59, 0\n\t"
      "s_cselect_b32 s49, 24576, 0\n\t"
      "s_add_u32 s49, s49, s58\n\t"
      "v_mfma_f32_16x16x32_bf16 v[28:31], v[172:175], a[24:27], v[28:31]\n\t"
      "v_mfma_f32_16x16x32_bf16 v[28:31], v[188:191], a[28:31], v[28:31]\n\t"
      "v_cvt_pk_bf16_f32 v42, v20, v21\n\t"
      "v_cvt_pk_bf16_f32 v43, v22, v23\n\t"
      "v_mfma_f32_16x16x32_bf16 v[28:31], v[204:207], a[32:35], v[28:31]\n\t"
      "ds_write_b64 v0, v[42:43] offset:32\n\t"
      "v_mfma_f32_16x16x32_bf16 v[28:31], v[220:223], a[36:39], v[28:31]\n\t"
      "v_mfma_f32_16x16x32_bf16 v[28:31], v[236:239], a[40:43], v[28:31]\n\t"
      "v_mfma_f32_16x16x32_bf16 v[28:31], v[252:255], a[44:47], v[28:31]\n\t"
      "v_mfma_f32_16x16x32_bf16 v[32:35], a[108:111], a[24:27], v[32:35]\n\t"
      "v_mfma_f32_16x16x32_bf16 v[32:35], a[116:119], a[28:31], v[32:35]\n\t"
      "v_cvt_pk_bf16_f32 v44, v24, v25\n\t"
      "v_cvt_pk_bf16_f32 v45, v26, v27\n\t"
      "v_mfma_f32_16x16x32_bf16 v[32:35], a[124:127], a[32:35], v[32:35]\n\t"
      "ds_write_b64 v0, v[44:45] offset:64\n\t"
      "v_mfma_f32_16x16x32_bf16 v[32:35], a[132:135], a[36:39], v[32:35]\n\t"
      "v_mfma_f32_16x16x32_bf16 v[32:35], a[140:143], a[40:43], v[32:35]\n\t"
      "v_mfma_f32_16x16x32_bf16 v[32:35], a[148:151], a[44:47], v[32:35]\n\t"
      "v_cvt_pk_bf16_f32 v46, v28, v29\n\t"
      "v_cvt_pk_bf16_f32 v47, v30, v31\n\t"
      "v_mfma_f32_16x16x32_bf16 v[36:39], a[112:115], a[24:27], v[36:39]\n\t"
      "ds_write_b64 v0, v[46:47] offset:96\n\t"
      "v_mfma_f32_16x16x32_bf16 v[36:39], a[120:123], a[28:31], v[36:39]\n\t"
      "v_mfma_f32_16x16x32_bf16 v[36:39], a[128:131], a[32:35], v[36:39]\n\t"
      "v_mfma_f32_16x16x32_bf16 v[36:39], a[136:139], a[36:39], v[36:39]\n\t"
      "v_mfma_f32_16x16x32_bf16 v[36:39], a[144:147], a[40:43], v[36:39]\n\t"
      "v_cvt_pk_bf16_f32 v48, v32, v33\n\t"
      "v_cvt_pk_bf16_f32 v49, v34, v35\n\t"
      "v_mfma_f32_16x16x32_bf16 v[36:39], a[152:155], a[44:47], v[36:39]\n\t"
      "ds_write_b64 v0, v[48:49] offset:128\n\t"
      "v_mfma_f32_16x16x32_bf16 v[16:19], v[64:67], a[0:3], 0\n\t"
      "ds_read_b128 a[24:27], v54 offset:256\n\t"
      "v_mfma_f32_16x16x32_bf16 v[16:19], v[80:83], a[4:7], v[16:19]\n\t"
      "ds_read_b128 a[28:31], v55 offset:256\n\t"
      "v_mfma_f32_16x16x32_bf16 v[16:19], v[96:99], a[8:11], v[16:19]\n\t"
      "ds_read_b128 a[32:35], v52 offset:512\n\t"
      "v_mfma_f32_16x16x32_bf16 v[16:19], v[112:115], a[12:15], v[16:19]\n\t"
      "ds_read_b128 a[36:39], v53 offset:512\n\t"
      "v_mfma_f32_16x16x32_bf16 v[16:19], v[128:131], a[16:19], v[16:19]\n\t"
      "ds_read_b128 a[40:43], v54 offset:512\n\t"
      "v_mfma_f32_16x16x32_bf16 v[16:19], v[144:147], a[20:23], v[16:19]\n\t"
      "ds_read_b128 a[44:47], v55 offset:512\n\t"
      "v_mfma_f32_16x16x32_bf16 v[20:23], v[68:71], a[0:3], 0\n\t"
      "v_cvt_pk_bf16_f32 v50, v36, v37\n\t"
      "v_cvt_pk_bf16_f32 v51, v38, v39\n\t"
      "v_mfma_f32_16x16x32_bf16 v[20:23], v[84:87], a[4:7], v[20:23]\n\t"
      "ds_write_b64 v0, v[50:51] offset:160\n\t"
      "v_mfma_f32_16x16x32_bf16 v[20:23], v[100:103], a[8:11], v[20:23]\n\t"
      "v_add_u32 v56, s51, v15\n\t"
      "v_xor_b32 v57, 64, v56\n\t"
      "v_mfma_f32_16x16x32_bf16 v[20:23], v[116:119], a[12:15], v[20:23]\n\t"
      "v_xor_b32 v58, 128, v56\n\t"
      "v_xor_b32 v59, 192, v56\n\t"
      "v_mfma_f32_16x16x32_bf16 v[20:23], v[132:135], a[16:19], v[20:23]\n\t"
      "v_mfma_f32_16x16x32_bf16 v[20:23], v[148:151], a[20:23], v[20:23]\n\t"
      "v_mfma_f32_16x16x32_bf16 v[24:27], v[72:75], a[0:3], 0\n\t"
      "v_mfma_f32_16x16x32_bf16 v[24:27], v[88:91], a[4:7], v[24:27]\n\t"
      "v_mfma_f32_16x16x32_bf16 v[24:27], v[104:107], a[8:11], v[24:27]\n\t"
      "s_waitcnt vmcnt(6)\n\t"
      "s_waitcnt lgkmcnt(0)\n\t"
      "s_barrier\n\t"
      "v_mfma_f32_16x16x32_bf16 v[24:27], v[120:123], a[12:15], v[24:27]\n\t"
      "v_mfma_f32_16x16x32_bf16 v[24:27], v[136:139], a[16:19], v[24:27]\n\t"
      "v_mfma_f32_16x16x32_bf16 v[24:27], v[152:155], a[20:23], v[24:27]\n\t"
      "v_mfma_f32_16x16x32_bf16 v[28:31], v[76:79], a[0:3], 0\n\t"
      "ds_read_b128 a[48:51], v8 offset:0\n\t"
      "v_mfma_f32_16x16x32_bf16 v[28:31], v[92:95], a[4:7], v[28:31]\n\t"
      "ds_read_b128 a[52:55], v60 offset:1024\n\t"
      "v_mfma_f32_16x16x32_bf16 v[28:31], v[108:111], a[8:11], v[28:31]\n\t"
      "ds_read_b128 a[56:59], v61 offset:2048\n\t"
      "v_mfma_f32_16x16x32_bf16 v[28:31], v[124:127], a[12:15], v[28:31]\n\t"
      "v_mfma_f32_16x16x32_bf16 v[28:31], v[140:143], a[16:19], v[28:31]\n\t"
      "v_mfma_f32_16x16x32_bf16 v[28:31], v[156:159], a[20:23], v[28:31]\n\t"
      "v_mfma_f32_16x16x32_bf16 v[32:35], a[60:63], a[0:3], 0\n\t"
      "v_mfma_f32_16x16x32_bf16 v[32:35], a[68:71], a[4:7], v[32:35]\n\t"
      "s_waitcnt lgkmcnt(2)\n\t"
      "v_mfma_f32_16x16x32_bf16 v[32:35], a[76:79], a[8:11], v[32:35]\n\t"
      "buffer_store_dwordx4 a[48:51], v9, s[44:47], s49 offen sc1\n\t"
      "v_mfma_f32_16x16x32_bf16 v[32:35], a[84:87], a[12:15], v[32:35]\n\t"
      "v_mfma_f32_16x16x32_bf16 v[32:35], a[92:95], a[16:19], v[32:35]\n\t"
      "v_mfma_f32_16x16x32_bf16 v[32:35], a[100:103], a[20:23], v[32:35]\n\t"
      "v_mfma_f32_16x16x32_bf16 v[36:39], a[64:67], a[0:3], 0\n\t"
      "v_mfma_f32_16x16x32_bf16 v[36:39], a[72:75], a[4:7], v[36:39]\n\t"
      "v_mfma_f32_16x16x32_bf16 v[36:39], a[80:83], a[8:11], v[36:39]\n\t"
      "v_mfma_f32_16x16x32_bf16 v[36:39], a[88:91], a[12:15], v[36:39]\n\t"
      "s_waitcnt lgkmcnt(1)\n\t"
      "v_mfma_f32_16x16x32_bf16 v[36:39], a[96:99], a[16:19], v[36:39]\n\t"
      "buffer_store_dwordx4 a[52:55], v10, s[44:47], s49 offen offset:1024 sc1\n\t"
      "v_mfma_f32_16x16x32_bf16 v[36:39], a[104:107], a[20:23], v[36:39]\n\t"
      "v_mfma_f32_16x16x32_bf16 v[16:19], v[160:163], a[24:27], v[16:19]\n\t"
      "ds_read_b128 a[0:3], v56 offset:0\n\t"
      "v_mfma_f32_16x16x32_bf16 v[16:19], v[176:179], a[28:31], v[16:19]\n\t"
      "ds_read_b128 a[4:7], v57 offset:0\n\t"
      "v_mfma_f32_16x16x32_bf16 v[16:19], v[192:195], a[32:35], v[16:19]\n\t"
      "ds_read_b128 a[8:11], v58 offset:0\n\t"
      "v_mfma_f32_16x16x32_bf16 v[16:19], v[208:211], a[36:39], v[16:19]\n\t"
      "ds_read_b128 a[12:15], v59 offset:0\n\t"
      "v_mfma_f32_16x16x32_bf16 v[16:19], v[224:227], a[40:43], v[16:19]\n\t"
      "ds_read_b128 a[16:19], v56 offset:256\n\t"
      "v_mfma_f32_16x16x32_bf16 v[16:19], v[240:243], a[44:47], v[16:19]\n\t"
      "ds_read_b128 a[20:23], v57 offset:256\n\t"
      "v_mfma_f32_16x16x32_bf16 v[20:23], v[164:167], a[24:27], v[20:23]\n\t"
      "s_add_u32 s51, s51, 12288\n\t"
      "s_cmp_eq_u32 s51, 160256\n\t"
      "s_cselect_b32 s51, 25088, s51\n\t"
      "v_mfma_f32_16x16x32_bf16 v[20:23], v[180:183], a[28:31], v[20:23]\n\t"
      "s_waitcnt lgkmcnt(6)\n\t"
      "v_mfma_f32_16x16x32_bf16 v[20:23], v[196:199], a[32:35], v[20:23]\n\t"
      "buffer_store_dwordx4 a[56:59], v11, s[44:47], s49 offen offset:2048 sc1\n\t"
      "v_mfma_f32_16x16x32_bf16 v[20:23], v[212:215], a[36:39], v[20:23]\n\t"
      "v_mfma_f32_16x16x32_bf16 v[20:23], v[228:231], a[40:43], v[20:23]\n\t"
      "s_waitcnt lgkmcnt(0)\n\t"
      "v_mfma_f32_16x16x32_bf16 v[20:23], v[244:247], a[44:47], v[20:23]\n\t"
      "v_mfma_f32_16x16x32_bf16 v[24:27], v[168:171], a[24:27], v[24:27]\n\t"
      "v_mfma_f32_16x16x32_bf16 v[24:27], v[184:187], a[28:31], v[24:27]\n\t"
      "v_cvt_pk_bf16_f32 v40, v16, v17\n\t"
      "v_cvt_pk_bf16_f32 v41, v18, v19\n\t"
      "v_mfma_f32_16x16x32_bf16 v[24:27], v[200:203], a[32:35], v[24:27]\n\t"
      "ds_write_b64 v0, v[40:41] offset:12544\n\t"
      "v_mfma_f32_16x16x32_bf16 v[24:27], v[216:219], a[36:39], v[24:27]\n\t"
      "v_mfma_f32_16x16x32_bf16 v[24:27], v[232:235], a[40:43], v[24:27]\n\t"
      "v_mfma_f32_16x16x32_bf16 v[24:27], v[248:251], a[44:47], v[24:27]\n\t"
      "v_mfma_f32_16x16x32_bf16 v[28:31], v[172:175], a[24:27], v[28:31]\n\t"
      "v_mfma_f32_16x16x32_bf16 v[28:31], v[188:191], a[28:31], v[28:31]\n\t"
      "v_cvt_pk_bf16_f32 v42, v20, v21\n\t"
      "v_cvt_pk_bf16_f32 v43, v22, v23\n\t"
      "v_mfma_f32_16x16x32_bf16 v[28:31], v[204:207], a[32:35], v[28:31]\n\t"
      "ds_write_b64 v0, v[42:43] offset:12576\n\t"
      "v_mfma_f32_16x16x32_bf16 v[28:31], v[220:223], a[36:39], v[28:31]\n\t"
      "v_mfma_f32_16x16x32_bf16 v[28:31], v[236:239], a[40:43], v[28:31]\n\t"
      "v_mfma_f32_16x16x32_bf16 v[28:31], v[252:255], a[44:47], v[28:31]\n\t"
      "v_mfma_f32_16x16x32_bf16 v[32:35], a[108:111], a[24:27], v[32:35]\n\t"
      "v_mfma_f32_16x16x32_bf16 v[32:35], a[116:119], a[28:31], v[32:35]\n\t"
      "v_cvt_pk_bf16_f32 v44, v24, v25\n\t"
      "v_cvt_pk_bf16_f32 v45, v26, v27\n\t"
      "v_mfma_f32_16x16x32_bf16 v[32:35], a[124:127], a[32:35], v[32:35]\n\t"
      "ds_write_b64 v0, v[44:45] offset:12608\n\t"
      "v_mfma_f32_16x16x32_bf16 v[32:35], a[132:135], a[36:39], v[32:35]\n\t"
      "v_mfma_f32_16x16x32_bf16 v[32:35], a[140:143], a[40:43], v[32:35]\n\t"
      "v_mfma_f32_16x16x32_bf16 v[32:35], a[148:151], a[44:47], v[32:35]\n\t"
      "v_cvt_pk_bf16_f32 v46, v28, v29\n\t"
      "v_cvt_pk_bf16_f32 v47, v30, v31\n\t"
      "v_mfma_f32_16x16x32_bf16 v[36:39], a[112:115], a[24:27], v[36:39]\n\t"
      "ds_write_b64 v0, v[46:47] offset:12640\n\t"
      "v_mfma_f32_16x16x32_bf16 v[36:39], a[120:123], a[28:31], v[36:39]\n\t"
      "v_mfma_f32_16x16x32_bf16 v[36:39], a[128:131], a[32:35], v[36:39]\n\t"
      "s_sub_u32 s52, s52, 1\n\t"
      "s_cmp_lg_u32 s52, 0\n\t"
      "v_mfma_f32_16x16x32_bf16 v[36:39], a[136:139], a[36:39], v[36:39]\n\t"
      "v_mfma_f32_16x16x32_bf16 v[36:39], a[144:147], a[40:43], v[36:39]\n\t"
      "v_cvt_pk_bf16_f32 v48, v32, v33\n\t"
      "v_cvt_pk_bf16_f32 v49, v34, v35\n\t"
      "v_mfma_f32_16x16x32_bf16 v[36:39], a[152:155], a[44:47], v[36:39]\n\t"
      "ds_write_b64 v0, v[48:49] offset:12672\n\t"
      "s_cbranch_scc1 .Ldrain0_%=\n\t"
      "s_branch .Ltail_%=\n\t"
      ".Lloop1_%=:\n\t"
      "v_mfma_f32_16x16x32_bf16 v[16:19], v[64:67], a[0:3], 0\n\t"
      "ds_read_b128 a[24:27], v58 offset:256\n\t"
      "v_mfma_f32_16x16x32_bf16 v[16:19], v[80:83], a[4:7], v[16:19]\n\t"
      "ds_read_b128 a[28:31], v59 offset:256\n\t"
      "v_mfma_f32_16x16x32_bf16 v[16:19], v[96:99], a[8:11], v[16:19]\n\t"
      "ds_read_b128 a[32:35], v56 offset:512\n\t"
      "v_mfma_f32_16x16x32_bf16 v[16:19], v[112:115], a[12:15], v[16:19]\n\t"
      "ds_read_b128 a[36:39], v57 offset:512\n\t"
      "v_mfma_f32_16x16x32_bf16 v[16:19], v[128:131], a[16:19], v[16:19]\n\t"
      "ds_read_b128 a[40:43], v58 offset:512\n\t"
      "v_mfma_f32_16x16x32_bf16 v[16:19], v[144:147], a[20:23], v[16:19]\n\t"
      "ds_read_b128 a[44:47], v59 offset:512\n\t"
      "v_mfma_f32_16x16x32_bf16 v[20:23], v[68:71], a[0:3], 0\n\t"
      "v_cvt_pk_bf16_f32 v50, v36, v37\n\t"
      "v_cvt_pk_bf16_f32 v51, v38, v39\n\t"
      "v_mfma_f32_16x16x32_bf16 v[20:23], v[84:87], a[4:7], v[20:23]\n\t"
      "ds_write_b64 v0, v[50:51] offset:12704\n\t"
      "v_mfma_f32_16x16x32_bf16 v[20:23], v[100:103], a[8:11], v[20:23]\n\t"
      "v_add_u32 v52, s51, v15\n\t"
      "v_xor_b32 v53, 64, v52\n\t"
      "v_mfma_f32_16x16x32_bf16 v[20:23], v[116:119], a[12:15], v[20:23]\n\t"
      "v_xor_b32 v54, 128, v52\n\t"
      "v_xor_b32 v55, 192, v52\n\t"
      "v_mfma_f32_16x16x32_bf16 v[20:23], v[132:135], a[16:19], v[20:23]\n\t"
      "s_add_u32 s56, s56, 786432\n\t"
      "s_lshr_b32 s57, s57, 1\n\t"
      "v_mfma_f32_16x16x32_bf16 v[20:23], v[148:151], a[20:23], v[20:23]\n\t"
      "s_bitcmp1_b32 s57, 0\n\t"
      "s_cselect_b32 s48, 24576, 0\n\t"
      "s_add_u32 s48, s48, s56\n\t"
      "v_mfma_f32_16x16x32_bf16 v[24:27], v[72:75], a[0:3], 0\n\t"
      "v_mfma_f32_16x16x32_bf16 v[24:27], v[88:91], a[4:7], v[24:27]\n\t"
      "v_mfma_f32_16x16x32_bf16 v[24:27], v[104:107], a[8:11], v[24:27]\n\t"
      "v_mfma_f32_16x16x32_bf16 v[24:27], v[120:123], a[12:15], v[24:27]\n\t"
      "s_waitcnt vmcnt(12)\n\t"
      "s_waitcnt lgkmcnt(0)\n\t"
      "s_barrier\n\t"
      "v_mfma_f32_16x16x32_bf16 v[24:27], v[136:139], a[16:19], v[24:27]\n\t"
      "v_mfma_f32_16x16x32_bf16 v[24:27], v[152:155], a[20:23], v[24:27]\n\t"
      "v_mfma_f32_16x16x32_bf16 v[28:31], v[76:79], a[0:3], 0\n\t"
      "ds_read_b128 a[48:51], v8 offset:12544\n\t"
      "v_mfma_f32_16x16x32_bf16 v[28:31], v[92:95], a[4:7], v[28:31]\n\t"
      "ds_read_b128 a[52:55], v60 offset:13568\n\t"
      "v_mfma_f32_16x16x32_bf16 v[28:31], v[108:111], a[8:11], v[28:31]\n\t"
      "ds_read_b128 a[56:59], v61 offset:14592\n\t"
      "v_mfma_f32_16x16x32_bf16 v[28:31], v[124:127], a[12:15], v[28:31]\n\t"
      "v_mfma_f32_16x16x32_bf16 v[28:31], v[140:143], a[16:19], v[28:31]\n\t"
      "v_mfma_f32_16x16x32_bf16 v[28:31], v[156:159], a[20:23], v[28:31]\n\t"
      "v_mfma_f32_16x16x32_bf16 v[32:35], a[60:63], a[0:3], 0\n\t"
      "v_mfma_f32_16x16x32_bf16 v[32:35], a[68:71], a[4:7], v[32:35]\n\t"
      "s_waitcnt lgkmcnt(2)\n\t"
      "v_mfma_f32_16x16x32_bf16 v[32:35], a[76:79], a[8:11], v[32:35]\n\t"
      "buffer_store_dwordx4 a[48:51], v12, s[44:47], s49 offen sc1\n\t"
      "v_mfma_f32_16x16x32_bf16 v[32:35], a[84:87], a[12:15], v[32:35]\n\t"
      "v_mfma_f32_16x16x32_bf16 v[32:35], a[92:95], a[16:19], v[32:35]\n\t"
      "v_mfma_f32_16x16x32_bf16 v[32:35], a[100:103], a[20:23], v[32:35]\n\t"
      "s_add_u32 m0, s18, s50\n\t"
      "v_mfma_f32_16x16x32_bf16 v[36:39], a[64:67], a[0:3], 0\n\t"
      "buffer_load_dwordx4 v2, s[40:43], s48 offen nt lds\n\t"
      "v_mfma_f32_16x16x32_bf16 v[36:39], a[72:75], a[4:7], v[36:39]\n\t"
      "v_mfma_f32_16x16x32_bf16 v[36:39], a[80:83], a[8:11], v[36:39]\n\t"
      "v_mfma_f32_16x16x32_bf16 v[36:39], a[88:91], a[12:15], v[36:39]\n\t"
      "s_waitcnt lgkmcnt(1)\n\t"
      "v_mfma_f32_16x16x32_bf16 v[36:39], a[96:99], a[16:19], v[36:39]\n\t"
      "buffer_store_dwordx4 a[52:55], v13, s[44:47], s49 offen offset:1024 sc1\n\t"
      "v_mfma_f32_16x16x32_bf16 v[36:39], a[104:107], a[20:23], v[36:39]\n\t"
      "v_mfma_f32_16x16x32_bf16 v[16:19], v[160:163], a[24:27], v[16:19]\n\t"
      "ds_read_b128 a[0:3], v52 offset:0\n\t"
      "v_mfma_f32_16x16x32_bf16 v[16:19], v[176:179], a[28:31], v[16:19]\n\t"
      "ds_read_b128 a[4:7], v53 offset:0\n\t"
      "v_mfma_f32_16x16x32_bf16 v[16:19], v[192:195], a[32:35], v[16:19]\n\t"
      "ds_read_b128 a[8:11], v54 offset:0\n\t"
      "v_mfma_f32_16x16x32_bf16 v[16:19], v[208:211], a[36:39], v[16:19]\n\t"
      "ds_read_b128 a[12:15], v55 offset:0\n\t"
      "v_mfma_f32_16x16x32_bf16 v[16:19], v[224:227], a[40:43], v[16:19]\n\t"
      "ds_read_b128 a[16:19], v52 offset:256\n\t"
      "v_mfma_f32_16x16x32_bf16 v[16:19], v[240:243], a[44:47], v[16:19]\n\t"
      "ds_read_b128 a[20:23], v53 offset:256\n\t"
      "v_mfma_f32_16x16x32_bf16 v[20:23], v[164:167], a[24:27], v[20:23]\n\t"
      "s_add_u32 s51, s51, 12288\n\t"
      "s_cmp_eq_u32 s51, 160256\n\t"
      "s_cselect_b32 s51, 25088, s51\n\t"
      "v_mfma_f32_16x16x32_bf16 v[20:23], v[180:183], a[28:31], v[20:23]\n\t"
      "s_waitcnt lgkmcnt(6)\n\t"
      "v_mfma_f32_16x16x32_bf16 v[20:23], v[196:199], a[32:35], v[20:23]\n\t"
      "buffer_store_dwordx4 a[56:59], v14, s[44:47], s49 offen offset:2048 sc1\n\t"
      "v_mfma_f32_16x16x32_bf16 v[20:23], v[212:215], a[36:39], v[20:23]\n\t"
      "v_mfma_f32_16x16x32_bf16 v[20:23], v[228:231], a[40:43], v[20:23]\n\t"
      "s_waitcnt lgkmcnt(0)\n\t"
      "v_mfma_f32_16x16x32_bf16 v[20:23], v[244:247], a[44:47], v[20:23]\n\t"
      "v_mfma_f32_16x16x32_bf16 v[24:27], v[168:171], a[24:27], v[24:27]\n\t"
      "v_mfma_f32_16x16x32_bf16 v[24:27], v[184:187], a[28:31], v[24:27]\n\t"
      "v_cvt_pk_bf16_f32 v40, v16, v17\n\t"
      "v_cvt_pk_bf16_f32 v41, v18, v19\n\t"
      "v_mfma_f32_16x16x32_bf16 v[24:27], v[200:203], a[32:35], v[24:27]\n\t"
      "ds_write_b64 v0, v[40:41] offset:0\n\t"
      "v_mfma_f32_16x16x32_bf16 v[24:27], v[216:219], a[36:39], v[24:27]\n\t"
      "buffer_load_dwordx4 v3, s[40:43], s48 offen offset:1024 nt lds\n\t"
      "v_mfma_f32_16x16x32_bf16 v[24:27], v[232:235], a[40:43], v[24:27]\n\t"
      "s_add_u32 s58, s58, 786432\n\t"
      "s_lshr_b32 s59, s59, 1\n\t"
      "v_mfma_f32_16x16x32_bf16 v[24:27], v[248:251], a[44:47], v[24:27]\n\t"
      "s_bitcmp1_b32 s59, 0\n\t"
      "s_cselect_b32 s49, 24576, 0\n\t"
      "s_add_u32 s49, s49, s58\n\t"
      "v_mfma_f32_16x16x32_bf16 v[28:31], v[172:175], a[24:27], v[28:31]\n\t"
      "v_mfma_f32_16x16x32_bf16 v[28:31], v[188:191], a[28:31], v[28:31]\n\t"
      "v_cvt_pk_bf16_f32 v42, v20, v21\n\t"
      "v_cvt_pk_bf16_f32 v43, v22, v23\n\t"
      "v_mfma_f32_16x16x32_bf16 v[28:31], v[204:207], a[32:35], v[28:31]\n\t"
      "ds_write_b64 v0, v[42:43] offset:32\n\t"
      "v_mfma_f32_16x16x32_bf16 v[28:31], v[220:223], a[36:39], v[28:31]\n\t"
      "v_mfma_f32_16x16x32_bf16 v[28:31], v[236:239], a[40:43], v[28:31]\n\t"
      "v_mfma_f32_16x16x32_bf16 v[28:31], v[252:255], a[44:47], v[28:31]\n\t"
      "v_mfma_f32_16x16x32_bf16 v[32:35], a[108:111], a[24:27], v[32:35]\n\t"
      "v_mfma_f32_16x16x32_bf16 v[32:35], a[116:119], a[28:31], v[32:35]\n\t"
      "v_cvt_pk_bf16_f32 v44, v24, v25\n\t"
      "v_cvt_pk_bf16_f32 v45, v26, v27\n\t"
      "v_mfma_f32_16x16x32_bf16 v[32:35], a[124:127], a[32:35], v[32:35]\n\t"
      "ds_write_b64 v0, v[44:45] offset:64\n\t"
      "v_mfma_f32_16x16x32_bf16 v[32:35], a[132:135], a[36:39], v[32:35]\n\t"
      "buffer_load_dwordx4 v4, s[40:43], s48 offen offset:2048 nt lds\n\t"
      "v_mfma_f32_16x16x32_bf16 v[32:35], a[140:143], a[40:43], v[32:35]\n\t"
      "s_add_u32 s50, s50, 12288\n\t"
      "v_mfma_f32_16x16x32_bf16 v[32:35], a[148:151], a[44:47], v[32:35]\n\t"
      "v_cvt_pk_bf16_f32 v46, v28, v29\n\t"
      "v_cvt_pk_bf16_f32 v47, v30, v31\n\t"
      "v_mfma_f32_16x16x32_bf16 v[36:39], a[112:115], a[24:27], v[36:39]\n\t"
      "ds_write_b64 v0, v[46:47] offset:96\n\t"
      "v_mfma_f32_16x16x32_bf16 v[36:39], a[120:123], a[28:31], v[36:39]\n\t"
      "s_cmp_eq_u32 s50, 160256\n\t"
      "s_cselect_b32 s50, 25088, s50\n\t"
      "v_mfma_f32_16x16x32_bf16 v[36:39], a[128:131], a[32:35], v[36:39]\n\t"
      "v_mfma_f32_16x16x32_bf16 v[36:39], a[136:139], a[36:39], v[36:39]\n\t"
      "v_mfma_f32_16x16x32_bf16 v[36:39], a[144:147], a[40:43], v[36:39]\n\t"
      "v_cvt_pk_bf16_f32 v48, v32, v33\n\t"
      "v_cvt_pk_bf16_f32 v49, v34, v35\n\t"
      "v_mfma_f32_16x16x32_bf16 v[36:39], a[152:155], a[44:47], v[36:39]\n\t"
      "ds_write_b64 v0, v[48:49] offset:128\n\t"
      ".Lmid1_%=:\n\t"
      "v_mfma_f32_16x16x32_bf16 v[16:19], v[64:67], a[0:3], 0\n\t"
      "ds_read_b128 a[24:27], v54 offset:256\n\t"
      "v_mfma_f32_16x16x32_bf16 v[16:19], v[80:83], a[4:7], v[16:19]\n\t"
      "ds_read_b128 a[28:31], v55 offset:256\n\t"
      "v_mfma_f32_16x16x32_bf16 v[16:19], v[96:99], a[8:11], v[16:19]\n\t"
      "ds_read_b128 a[32:35], v52 offset:512\n\t"
      "v_mfma_f32_16x16x32_bf16 v[16:19], v[112:115], a[12:15], v[16:19]\n\t"
      "ds_read_b128 a[36:39], v53 offset:512\n\t"
      "v_mfma_f32_16x16x32_bf16 v[16:19], v[128:131], a[16:19], v[16:19]\n\t"
      "ds_read_b128 a[40:43], v54 offset:512\n\t"
      "v_mfma_f32_16x16x32_bf16 v[16:19], v[144:147], a[20:23], v[16:19]\n\t"
      "ds_read_b128 a[44:47], v55 offset:512\n\t"
      "v_mfma_f32_16x16x32_bf16 v[20:23], v[68:71], a[0:3], 0\n\t"
      "v_cvt_pk_bf16_f32 v50, v36, v37\n\t"
      "v_cvt_pk_bf16_f32 v51, v38, v39\n\t"
      "v_mfma_f32_16x16x32_bf16 v[20:23], v[84:87], a[4:7], v[20:23]\n\t"
      "ds_write_b64 v0, v[50:51] offset:160\n\t"
      "v_mfma_f32_16x16x32_bf16 v[20:23], v[100:103], a[8:11], v[20:23]\n\t"
      "v_add_u32 v56, s51, v15\n\t"
      "v_xor_b32 v57, 64, v56\n\t"
      "v_mfma_f32_16x16x32_bf16 v[20:23], v[116:119], a[12:15], v[20:23]\n\t"
      "v_xor_b32 v58, 128, v56\n\t"
      "v_xor_b32 v59, 192, v56\n\t"
      "v_mfma_f32_16x16x32_bf16 v[20:23], v[132:135], a[16:19], v[20:23]\n\t"
      "v_mfma_f32_16x16x32_bf16 v[20:23], v[148:151], a[20:23], v[20:23]\n\t"
      "v_mfma_f32_16x16x32_bf16 v[24:27], v[72:75], a[0:3], 0\n\t"
      "v_mfma_f32_16x16x32_bf16 v[24:27], v[88:91], a[4:7], v[24:27]\n\t"
      "v_mfma_f32_16x16x32_bf16 v[24:27], v[104:107], a[8:11], v[24:27]\n\t"
      "v_mfma_f32_16x16x32_bf16 v[24:27], v[120:123], a[12:15], v[24:27]\n\t"
      "s_waitcnt vmcnt(12)\n\t"
      "s_waitcnt lgkmcnt(0)\n\t"
      "s_barrier\n\t"
      "v_mfma_f32_16x16x32_bf16 v[24:27], v[136:139], a[16:19], v[24:27]\n\t"
      "v_mfma_f32_16x16x32_bf16 v[24:27], v[152:155], a[20:23], v[24:27]\n\t"
      "v_mfma_f32_16x16x32_bf16 v[28:31], v[76:79], a[0:3], 0\n\t"
      "ds_read_b128 a[48:51], v8 offset:0\n\t"
      "v_mfma_f32_16x16x32_bf16 v[28:31], v[92:95], a[4:7], v[28:31]\n\t"
      "ds_read_b128 a[52:55], v60 offset:1024\n\t"
      "v_mfma_f32_16x16x32_bf16 v[28:31], v[108:111], a[8:11], v[28:31]\n\t"
      "ds_read_b128 a[56:59], v61 offset:2048\n\t"
      "v_mfma_f32_16x16x32_bf16 v[28:31], v[124:127], a[12:15], v[28:31]\n\t"
      "v_mfma_f32_16x16x32_bf16 v[28:31], v[140:143], a[16:19], v[28:31]\n\t"
      "v_mfma_f32_16x16x32_bf16 v[28:31], v[156:159], a[20:23], v[28:31]\n\t"
      "v_mfma_f32_16x16x32_bf16 v[32:35], a[60:63], a[0:3], 0\n\t"
      "v_mfma_f32_16x16x32_bf16 v[32:35], a[68:71], a[4:7], v[32:35]\n\t"
      "s_waitcnt lgkmcnt(2)\n\t"
      "v_mfma_f32_16x16x32_bf16 v[32:35], a[76:79], a[8:11], v[32:35]\n\t"
      "buffer_store_dwordx4 a[48:51], v9, s[44:47], s49 offen sc1\n\t"
      "v_mfma_f32_16x16x32_bf16 v[32:35], a[84:87], a[12:15], v[32:35]\n\t"
      "v_mfma_f32_16x16x32_bf16 v[32:35], a[92:95], a[16:19], v[32:35]\n\t"
      "v_mfma_f32_16x16x32_bf16 v[32:35], a[100:103], a[20:23], v[32:35]\n\t"
      "s_add_u32 m0, s18, s50\n\t"
      "v_mfma_f32_16x16x32_bf16 v[36:39], a[64:67], a[0:3], 0\n\t"
      "buffer_load_dwordx4 v5, s[40:43], s48 offen nt lds\n\t"
      "v_mfma_f32_16x16x32_bf16 v[36:39], a[72:75], a[4:7], v[36:39]\n\t"
      "v_mfma_f32_16x16x32_bf16 v[36:39], a[80:83], a[8:11], v[36:39]\n\t"
      "v_mfma_f32_16x16x32_bf16 v[36:39], a[88:91], a[12:15], v[36:39]\n\t"
      "s_waitcnt lgkmcnt(1)\n\t"
      "v_mfma_f32_16x16x32_bf16 v[36:39], a[96:99], a[16:19], v[36:39]\n\t"
      "buffer_store_dwordx4 a[52:55], v10, s[44:47], s49 offen offset:1024 sc1\n\t"
      "v_mfma_f32_16x16x32_bf16 v[36:39], a[104:107], a[20:23], v[36:39]\n\t"
      "v_mfma_f32_16x16x32_bf16 v[16:19], v[160:163], a[24:27], v[16:19]\n\t"
      "ds_read_b128 a[0:3], v56 offset:0\n\t"
      "v_mfma_f32_16x16x32_bf16 v[16:19], v[176:179], a[28:31], v[16:19]\n\t"
      "ds_read_b128 a[4:7], v57 offset:0\n\t"
      "v_mfma_f32_16x16x32_bf16 v[16:19], v[192:195], a[32:35], v[16:19]\n\t"
      "ds_read_b128 a[8:11], v58 offset:0\n\t"
      "v_mfma_f32_16x16x32_bf16 v[16:19], v[208:211], a[36:39], v[16:19]\n\t"
      "ds_read_b128 a[12:15], v59 offset:0\n\t"
      "v_mfma_f32_16x16x32_bf16 v[16:19], v[224:227], a[40:43], v[16:19]\n\t"
      "ds_read_b128 a[16:19], v56 offset:256\n\t"
      "v_mfma_f32_16x16x32_bf16 v[16:19], v[240:243], a[44:47], v[16:19]\n\t"
      "ds_read_b128 a[20:23], v57 offset:256\n\t"
      "v_mfma_f32_16x16x32_bf16 v[20:23], v[164:167], a[24:27], v[20:23]\n\t"
      "s_add_u32 s51, s51, 12288\n\t"
      "s_cmp_eq_u32 s51, 160256\n\t"
      "s_cselect_b32 s51, 25088, s51\n\t"
      "v_mfma_f32_16x16x32_bf16 v[20:23], v[180:183], a[28:31], v[20:23]\n\t"
      "s_waitcnt lgkmcnt(6)\n\t"
      "v_mfma_f32_16x16x32_bf16 v[20:23], v[196:199], a[32:35], v[20:23]\n\t"
      "buffer_store_dwordx4 a[56:59], v11, s[44:47], s49 offen offset:2048 sc1\n\t"
      "v_mfma_f32_16x16x32_bf16 v[20:23], v[212:215], a[36:39], v[20:23]\n\t"
      "v_mfma_f32_16x16x32_bf16 v[20:23], v[228:231], a[40:43], v[20:23]\n\t"
      "s_waitcnt lgkmcnt(0)\n\t"
      "v_mfma_f32_16x16x32_bf16 v[20:23], v[244:247], a[44:47], v[20:23]\n\t"
      "v_mfma_f32_16x16x32_bf16 v[24:27], v[168:171], a[24:27], v[24:27]\n\t"
      "v_mfma_f32_16x16x32_bf16 v[24:27], v[184:187], a[28:31], v[24:27]\n\t"
      "v_cvt_pk_bf16_f32 v40, v16, v17\n\t"
      "v_cvt_pk_bf16_f32 v41, v18, v19\n\t"
      "v_mfma_f32_16x16x32_bf16 v[24:27], v[200:203], a[32:35], v[24:27]\n\t"
      "ds_write_b64 v0, v[40:41] offset:12544\n\t"
      "v_mfma_f32_16x16x32_bf16 v[24:27], v[216:219], a[36:39], v[24:27]\n\t"
      "buffer_load_dwordx4 v6, s[40:43], s48 offen offset:1024 nt lds\n\t"
      "v_mfma_f32_16x16x32_bf16 v[24:27], v[232:235], a[40:43], v[24:27]\n\t"
      "v_mfma_f32_16x16x32_bf16 v[24:27], v[248:251], a[44:47], v[24:27]\n\t"
      "v_mfma_f32_16x16x32_bf16 v[28:31], v[172:175], a[24:27], v[28:31]\n\t"
      "v_mfma_f32_16x16x32_bf16 v[28:31], v[188:191], a[28:31], v[28:31]\n\t"
      "v_cvt_pk_bf16_f32 v42, v20, v21\n\t"
      "v_cvt_pk_bf16_f32 v43, v22, v23\n\t"
      "v_mfma_f32_16x16x32_bf16 v[28:31], v[204:207], a[32:35], v[28:31]\n\t"
      "ds_write_b64 v0, v[42:43] offset:12576\n\t"
      "v_mfma_f32_16x16x32_bf16 v[28:31], v[220:223], a[36:39], v[28:31]\n\t"
      "v_mfma_f32_16x16x32_bf16 v[28:31], v[236:239], a[40:43], v[28:31]\n\t"
      "v_mfma_f32_16x16x32_bf16 v[28:31], v[252:255], a[44:47], v[28:31]\n\t"
      "v_mfma_f32_16x16x32_bf16 v[32:35], a[108:111], a[24:27], v[32:35]\n\t"
      "v_mfma_f32_16x16x32_bf16 v[32:35], a[116:119], a[28:31], v[32:35]\n\t"
      "v_cvt_pk_bf16_f32 v44, v24, v25\n\t"
      "v_cvt_pk_bf16_f32 v45, v26, v27\n\t"
      "v_mfma_f32_16x16x32_bf16 v[32:35], a[124:127], a[32:35], v[32:35]\n\t"
      "ds_write_b64 v0, v[44:45] offset:12608\n\t"
      "v_mfma_f32_16x16x32_bf16 v[32:35], a[132:135], a[36:39], v[32:35]\n\t"
      "buffer_load_dwordx4 v7, s[40:43], s48 offen offset:2048 nt lds\n\t"
      "v_mfma_f32_16x16x32_bf16 v[32:35], a[140:143], a[40:43], v[32:35]\n\t"
      "s_add_u32 s50, s50, 12288\n\t"
      "v_mfma_f32_16x16x32_bf16 v[32:35], a[148:151], a[44:47], v[32:35]\n\t"
      "v_cvt_pk_bf16_f32 v46, v28, v29\n\t"
      "v_cvt_pk_bf16_f32 v47, v30, v31\n\t"
      "v_mfma_f32_16x16x32_bf16 v[36:39], a[112:115], a[24:27], v[36:39]\n\t"
      "ds_write_b64 v0, v[46:47] offset:12640\n\t"
      "v_mfma_f32_16x16x32_bf16 v[36:39], a[120:123], a[28:31], v[36:39]\n\t"
      "s_cmp_eq_u32 s50, 160256\n\t"
      "s_cselect_b32 s50, 25088, s50\n\t"
      "v_mfma_f32_16x16x32_bf16 v[36:39], a[128:131], a[32:35], v[36:39]\n\t"
      "s_sub_u32 s52, s52, 1\n\t"
      "s_cmp_lg_u32 s52, 0\n\t"
      "v_mfma_f32_16x16x32_bf16 v[36:39], a[136:139], a[36:39], v[36:39]\n\t"
      "v_mfma_f32_16x16x32_bf16 v[36:39], a[144:147], a[40:43], v[36:39]\n\t"
      "v_cvt_pk_bf16_f32 v48, v32, v33\n\t"
      "v_cvt_pk_bf16_f32 v49, v34, v35\n\t"
      "v_mfma_f32_16x16x32_bf16 v[36:39], a[152:155], a[44:47], v[36:39]\n\t"
      "ds_write_b64 v0, v[48:49] offset:12672\n\t"
      "s_cbranch_scc1 .Lloop1_%=\n\t"
      "s_cmp_eq_u32 s48, 113221632\n\t"
      "s_cbranch_scc1 .Lwrap1_%=\n\t"
      ".Lwrapped1_%=:\n\t"
      "s_mov_b32 s52, 2\n\t"
      ".Ldrain1_%=:\n\t"
      "v_mfma_f32_16x16x32_bf16 v[16:19], v[64:67], a[0:3], 0\n\t"
      "ds_read_b128 a[24:27], v58 offset:256\n\t"
      "v_mfma_f32_16x16x32_bf16 v[16:19], v[80:83], a[4:7], v[16:19]\n\t"
      "ds_read_b128 a[28:31], v59 offset:256\n\t"
      "v_mfma_f32_16x16x32_bf16 v[16:19], v[96:99], a[8:11], v[16:19]\n\t"
      "ds_read_b128 a[32:35], v56 offset:512\n\t"
      "v_mfma_f32_16x16x32_bf16 v[16:19], v[112:115], a[12:15], v[16:19]\n\t"
      "ds_read_b128 a[36:39], v57 offset:512\n\t"
      "v_mfma_f32_16x16x32_bf16 v[16:19], v[128:131], a[16:19], v[16:19]\n\t"
      "ds_read_b128 a[40:43], v58 offset:512\n\t"
      "v_mfma_f32_16x16x32_bf16 v[16:19], v[144:147], a[20:23], v[16:19]\n\t"
      "ds_read_b128 a[44:47], v59 offset:512\n\t"
      "v_mfma_f32_16x16x32_bf16 v[20:23], v[68:71], a[0:3], 0\n\t"
      "v_cvt_pk_bf16_f32 v50, v36, v37\n\t"
      "v_cvt_pk_bf16_f32 v51, v38, v39\n\t"
      "v_mfma_f32_16x16x32_bf16 v[20:23], v[84:87], a[4:7], v[20:23]\n\t"
      "ds_write_b64 v0, v[50:51] offset:12704\n\t"
      "v_mfma_f32_16x16x32_bf16 v[20:23], v[100:103], a[8:11], v[20:23]\n\t"
      "v_add_u32 v52, s51, v15\n\t"
      "v_xor_b32 v53, 64, v52\n\t"
      "v_mfma_f32_16x16x32_bf16 v[20:23], v[116:119], a[12:15], v[20:23]\n\t"
      "v_xor_b32 v54, 128, v52\n\t"
      "v_xor_b32 v55, 192, v52\n\t"
      "v_mfma_f32_16x16x32_bf16 v[20:23], v[132:135], a[16:19], v[20:23]\n\t"
      "v_mfma_f32_16x16x32_bf16 v[20:23], v[148:151], a[20:23], v[20:23]\n\t"
      "v_mfma_f32_16x16x32_bf16 v[24:27], v[72:75], a[0:3], 0\n\t"
      "v_mfma_f32_16x16x32_bf16 v[24:27], v[88:91], a[4:7], v[24:27]\n\t"
      "v_mfma_f32_16x16x32_bf16 v[24:27], v[104:107], a[8:11], v[24:27]\n\t"
      "v_mfma_f32_16x16x32_bf16 v[24:27], v[120:123], a[12:15], v[24:27]\n\t"
      "s_waitcnt vmcnt(6)\n\t"
      "s_waitcnt lgkmcnt(0)\n\t"
      "s_barrier\n\t"
      "v_mfma_f32_16x16x32_bf16 v[24:27], v[136:139], a[16:19], v[24:27]\n\t"
      "v_mfma_f32_16x16x32_bf16 v[24:27], v[152:155], a[20:23], v[24:27]\n\t"
      "v_mfma_f32_16x16x32_bf16 v[28:31], v[76:79], a[0:3], 0\n\t"
      "ds_read_b128 a[48:51], v8 offset:12544\n\t"
      "v_mfma_f32_16x16x32_bf16 v[28:31], v[92:95], a[4:7], v[28:31]\n\t"
      "ds_read_b128 a[52:55], v60 offset:13568\n\t"
      "v_mfma_f32_16x16x32_bf16 v[28:31], v[108:111], a[8:11], v[28:31]\n\t"
      "ds_read_b128 a[56:59], v61 offset:14592\n\t"
      "v_mfma_f32_16x16x32_bf16 v[28:31], v[124:127], a[12:15], v[28:31]\n\t"
      "v_mfma_f32_16x16x32_bf16 v[28:31], v[140:143], a[16:19], v[28:31]\n\t"
      "v_mfma_f32_16x16x32_bf16 v[28:31], v[156:159], a[20:23], v[28:31]\n\t"
      "v_mfma_f32_16x16x32_bf16 v[32:35], a[60:63], a[0:3], 0\n\t"
      "v_mfma_f32_16x16x32_bf16 v[32:35], a[68:71], a[4:7], v[32:35]\n\t"
      "s_waitcnt lgkmcnt(2)\n\t"
      "v_mfma_f32_16x16x32_bf16 v[32:35], a[76:79], a[8:11], v[32:35]\n\t"
      "buffer_store_dwordx4 a[48:51], v12, s[44:47], s49 offen sc1\n\t"
      "v_mfma_f32_16x16x32_bf16 v[32:35], a[84:87], a[12:15], v[32:35]\n\t"
      "v_mfma_f32_16x16x32_bf16 v[32:35], a[92:95], a[16:19], v[32:35]\n\t"
      "v_mfma_f32_16x16x32_bf16 v[32:35], a[100:103], a[20:23], v[32:35]\n\t"
      "v_mfma_f32_16x16x32_bf16 v[36:39], a[64:67], a[0:3], 0\n\t"
      "v_mfma_f32_16x16x32_bf16 v[36:39], a[72:75], a[4:7], v[36:39]\n\t"
      "v_mfma_f32_16x16x32_bf16 v[36:39], a[80:83], a[8:11], v[36:39]\n\t"
      "v_mfma_f32_16x16x32_bf16 v[36:39], a[88:91], a[12:15], v[36:39]\n\t"
      "s_waitcnt lgkmcnt(1)\n\t"
      "v_mfma_f32_16x16x32_bf16 v[36:39], a[96:99], a[16:19], v[36:39]\n\t"
      "buffer_store_dwordx4 a[52:55], v13, s[44:47], s49 offen offset:1024 sc1\n\t"
      "v_mfma_f32_16x16x32_bf16 v[36:39], a[104:107], a[20:23], v[36:39]\n\t"
      "v_mfma_f32_16x16x32_bf16 v[16:19], v[160:163], a[24:27], v[16:19]\n\t"
      "ds_read_b128 a[0:3], v52 offset:0\n\t"
      "v_mfma_f32_16x16x32_bf16 v[16:19], v[176:179], a[28:31], v[16:19]\n\t"
      "ds_read_b128 a[4:7], v53 offset:0\n\t"
      "v_mfma_f32_16x16x32_bf16 v[16:19], v[192:195], a[32:35], v[16:19]\n\t"
      "ds_read_b128 a[8:11], v54 offset:0\n\t"
      "v_mfma_f32_16x16x32_bf16 v[16:19], v[208:211], a[36:39], v[16:19]\n\t"
      "ds_read_b128 a[12:15], v55 offset:0\n\t"
      "v_mfma_f32_16x16x32_bf16 v[16:19], v[224:227], a[40:43], v[16:19]\n\t"
      "ds_read_b128 a[16:19], v52 offset:256\n\t"
      "v_mfma_f32_16x16x32_bf16 v[16:19], v[240:243], a[44:47], v[16:19]\n\t"
      "ds_read_b128 a[20:23], v53 offset:256\n\t"
      "v_mfma_f32_16x16x32_bf16 v[20:23], v[164:167], a[24:27], v[20:23]\n\t"
      "s_add_u32 s51, s51, 12288\n\t"
      "s_cmp_eq_u32 s51, 160256\n\t"
      "s_cselect_b32 s51, 25088, s51\n\t"
      "v_mfma_f32_16x16x32_bf16 v[20:23], v[180:183], a[28:31], v[20:23]\n\t"
      "s_waitcnt lgkmcnt(6)\n\t"
      "v_mfma_f32_16x16x32_bf16 v[20:23], v[196:199], a[32:35], v[20:23]\n\t"
      "buffer_store_dwordx4 a[56:59], v14, s[44:47], s49 offen offset:2048 sc1\n\t"
      "v_mfma_f32_16x16x32_bf16 v[20:23], v[212:215], a[36:39], v[20:23]\n\t"
      "v_mfma_f32_16x16x32_bf16 v[20:23], v[228:231], a[40:43], v[20:23]\n\t"
      "s_waitcnt lgkmcnt(0)\n\t"
      "v_mfma_f32_16x16x32_bf16 v[20:23], v[244:247], a[44:47], v[20:23]\n\t"
      "v_mfma_f32_16x16x32_bf16 v[24:27], v[168:171], a[24:27], v[24:27]\n\t"
      "v_mfma_f32_16x16x32_bf16 v[24:27], v[184:187], a[28:31], v[24:27]\n\t"
      "v_cvt_pk_bf16_f32 v40, v16, v17\n\t"
      "v_cvt_pk_bf16_f32 v41, v18, v19\n\t"
      "v_mfma_f32_16x16x32_bf16 v[24:27], v[200:203], a[32:35], v[24:27]\n\t"
      "ds_write_b64 v0, v[40:41] offset:0\n\t"
      "v_mfma_f32_16x16x32_bf16 v[24:27], v[216:219], a[36:39], v[24:27]\n\t"
      "v_mfma_f32_16x16x32_bf16 v[24:27], v[232:235], a[40:43], v[24:27]\n\t"
      "s_add_u32 s58, s58, 786432\n\t"
      "s_lshr_b32 s59, s59, 1\n\t"
      "v_mfma_f32_16x16x32_bf16 v[24:27], v[248:251], a[44:47], v[24:27]\n\t"
      "s_bitcmp1_b32 s59, 0\n\t"
      "s_cselect_b32 s49, 24576, 0\n\t"
      "s_add_u32 s49, s49, s58\n\t"
      "v_mfma_f32_16x16x32_bf16 v[28:31], v[172:175], a[24:27], v[28:31]\n\t"
      "v_mfma_f32_16x16x32_bf16 v[28:31], v[188:191], a[28:31], v[28:31]\n\t"
      "v_cvt_pk_bf16_f32 v42, v20, v21\n\t"
      "v_cvt_pk_bf16_f32 v43, v22, v23\n\t"
      "v_mfma_f32_16x16x32_bf16 v[28:31], v[204:207], a[32:35], v[28:31]\n\t"
      "ds_write_b64 v0, v[42:43] offset:32\n\t"
      "v_mfma_f32_16x16x32_bf16 v[28:31], v[220:223], a[36:39], v[28:31]\n\t"
      "v_mfma_f32_16x16x32_bf16 v[28:31], v[236:239], a[40:43], v[28:31]\n\t"
      "v_mfma_f32_16x16x32_bf16 v[28:31], v[252:255], a[44:47], v[28:31]\n\t"
      "v_mfma_f32_16x16x32_bf16 v[32:35], a[108:111], a[24:27], v[32:35]\n\t"
      "v_mfma_f32_16x16x32_bf16 v[32:35], a[116:119], a[28:31], v[32:35]\n\t"
      "v_cvt_pk_bf16_f32 v44, v24, v25\n\t"
      "v_cvt_pk_bf16_f32 v45, v26, v27\n\t"
      "v_mfma_f32_16x16x32_bf16 v[32:35], a[124:127], a[32:35], v[32:35]\n\t"
      "ds_write_b64 v0, v[44:45] offset:64\n\t"
      "v_mfma_f32_16x16x32_bf16 v[32:35], a[132:135], a[36:39], v[32:35]\n\t"
      "v_mfma_f32_16x16x32_bf16 v[32:35], a[140:143], a[40:43], v[32:35]\n\t"
      "v_mfma_f32_16x16x32_bf16 v[32:35], a[148:151], a[44:47], v[32:35]\n\t"
      "v_cvt_pk_bf16_f32 v46, v28, v29\n\t"
      "v_cvt_pk_bf16_f32 v47, v30, v31\n\t"
      "v_mfma_f32_16x16x32_bf16 v[36:39], a[112:115], a[24:27], v[36:39]\n\t"
      "ds_write_b64 v0, v[46:47] offset:96\n\t"
      "v_mfma_f32_16x16x32_bf16 v[36:39], a[120:123], a[28:31], v[36:39]\n\t"
      "v_mfma_f32_16x16x32_bf16 v[36:39], a[128:131], a[32:35], v[36:39]\n\t"
      "v_mfma_f32_16x16x32_bf16 v[36:39], a[136:139], a[36:39], v[36:39]\n\t"
      "v_mfma_f32_16x16x32_bf16 v[36:39], a[144:147], a[40:43], v[36:39]\n\t"
      "v_cvt_pk_bf16_f32 v48, v32, v33\n\t"
      "v_cvt_pk_bf16_f32 v49, v34, v35\n\t"
      "v_mfma_f32_16x16x32_bf16 v[36:39], a[152:155], a[44:47], v[36:39]\n\t"
      "ds_write_b64 v0, v[48:49] offset:128\n\t"
      "v_mfma_f32_16x16x32_bf16 v[16:19], v[64:67], a[0:3], 0\n\t"
      "ds_read_b128 a[24:27], v54 offset:256\n\t"
      "v_mfma_f32_16x16x32_bf16 v[16:19], v[80:83], a[4:7], v[16:19]\n\t"
      "ds_read_b128 a[28:31], v55 offset:256\n\t"
      "v_mfma_f32_16x16x32_bf16 v[16:19], v[96:99], a[8:11], v[16:19]\n\t"
      "ds_read_b128 a[32:35], v52 offset:512\n\t"
      "v_mfma_f32_16x16x32_bf16 v[16:19], v[112:115], a[12:15], v[16:19]\n\t"
      "ds_read_b128 a[36:39], v53 offset:512\n\t"
      "v_mfma_f32_16x16x32_bf16 v[16:19], v[128:131], a[16:19], v[16:19]\n\t"
      "ds_read_b128 a[40:43], v54 offset:512\n\t"
      "v_mfma_f32_16x16x32_bf16 v[16:19], v[144:147], a[20:23], v[16:19]\n\t"
      "ds_read_b128 a[44:47], v55 offset:512\n\t"
      "v_mfma_f32_16x16x32_bf16 v[20:23], v[68:71], a[0:3], 0\n\t"
      "v_cvt_pk_bf16_f32 v50, v36, v37\n\t"
      "v_cvt_pk_bf16_f32 v51, v38, v39\n\t"
      "v_mfma_f32_16x16x32_bf16 v[20:23], v[84:87], a[4:7], v[20:23]\n\t"
      "ds_write_b64 v0, v[50:51] offset:160\n\t"
      "v_mfma_f32_16x16x32_bf16 v[20:23], v[100:103], a[8:11], v[20:23]\n\t"
      "v_add_u32 v56, s51, v15\n\t"
      "v_xor_b32 v57, 64, v56\n\t"
      "v_mfma_f32_16x16x32_bf16 v[20:23], v[116:119], a[12:15], v[20:23]\n\t"
      "v_xor_b32 v58, 128, v56\n\t"
      "v_xor_b32 v59, 192, v56\n\t"
      "v_mfma_f32_16x16x32_bf16 v[20:23], v[132:135], a[16:19], v[20:23]\n\t"
      "v_mfma_f32_16x16x32_bf16 v[20:23], v[148:151], a[20:23], v[20:23]\n\t"
      "v_mfma_f32_16x16x32_bf16 v[24:27], v[72:75], a[0:3], 0\n\t"
      "v_mfma_f32_16x16x32_bf16 v[24:27], v[88:91], a[4:7], v[24:27]\n\t"
      "v_mfma_f32_16x16x32_bf16 v[24:27], v[104:107], a[8:11], v[24:27]\n\t"
      "v_mfma_f32_16x16x32_bf16 v[24:27], v[120:123], a[12:15], v[24:27]\n\t"
      "s_waitcnt vmcnt(6)\n\t"
      "s_waitcnt lgkmcnt(0)\n\t"
      "s_barrier\n\t"
      "v_mfma_f32_16x16x32_bf16 v[24:27], v[136:139], a[16:19], v[24:27]\n\t"
      "v_mfma_f32_16x16x32_bf16 v[24:27], v[152:155], a[20:23], v[24:27]\n\t"
      "v_mfma_f32_16x16x32_bf16 v[28:31], v[76:79], a[0:3], 0\n\t"
      "ds_read_b128 a[48:51], v8 offset:0\n\t"
      "v_mfma_f32_16x16x32_bf16 v[28:31], v[92:95], a[4:7], v[28:31]\n\t"
      "ds_read_b128 a[52:55], v60 offset:1024\n\t"
      "v_mfma_f32_16x16x32_bf16 v[28:31], v[108:111], a[8:11], v[28:31]\n\t"
      "ds_read_b128 a[56:59], v61 offset:2048\n\t"
      "v_mfma_f32_16x16x32_bf16 v[28:31], v[124:127], a[12:15], v[28:31]\n\t"
      "v_mfma_f32_16x16x32_bf16 v[28:31], v[140:143], a[16:19], v[28:31]\n\t"
      "v_mfma_f32_16x16x32_bf16 v[28:31], v[156:159], a[20:23], v[28:31]\n\t"
      "v_mfma_f32_16x16x32_bf16 v[32:35], a[60:63], a[0:3], 0\n\t"
      "v_mfma_f32_16x16x32_bf16 v[32:35], a[68:71], a[4:7], v[32:35]\n\t"
      "s_waitcnt lgkmcnt(2)\n\t"
      "v_mfma_f32_16x16x32_bf16 v[32:35], a[76:79], a[8:11], v[32:35]\n\t"
      "buffer_store_dwordx4 a[48:51], v9, s[44:47], s49 offen sc1\n\t"
      "v_mfma_f32_16x16x32_bf16 v[32:35], a[84:87], a[12:15], v[32:35]\n\t"
      "v_mfma_f32_16x16x32_bf16 v[32:35], a[92:95], a[16:19], v[32:35]\n\t"
      "v_mfma_f32_16x16x32_bf16 v[32:35], a[100:103], a[20:23], v[32:35]\n\t"
      "v_mfma_f32_16x16x32_bf16 v[36:39], a[64:67], a[0:3], 0\n\t"
      "v_mfma_f32_16x16x32_bf16 v[36:39], a[72:75], a[4:7], v[36:39]\n\t"
      "v_mfma_f32_16x16x32_bf16 v[36:39], a[80:83], a[8:11], v[36:39]\n\t"
      "v_mfma_f32_16x16x32_bf16 v[36:39], a[88:91], a[12:15], v[36:39]\n\t"
      "s_waitcnt lgkmcnt(1)\n\t"
      "v_mfma_f32_16x16x32_bf16 v[36:39], a[96:99], a[16:19], v[36:39]\n\t"
      "buffer_store_dwordx4 a[52:55], v10, s[44:47], s49 offen offset:1024 sc1\n\t"
      "v_mfma_f32_16x16x32_bf16 v[36:39], a[104:107], a[20:23], v[36:39]\n\t"
      "v_mfma_f32_16x16x32_bf16 v[16:19], v[160:163], a[24:27], v[16:19]\n\t"
      "ds_read_b128 a[0:3], v56 offset:0\n\t"
      "v_mfma_f32_16x16x32_bf16 v[16:19], v[176:179], a[28:31], v[16:19]\n\t"
      "ds_read_b128 a[4:7], v57 offset:0\n\t"
      "v_mfma_f32_16x16x32_bf16 v[16:19], v[192:195], a[32:35], v[16:19]\n\t"
      "ds_read_b128 a[8:11], v58 offset:0\n\t"
      "v_mfma_f32_16x16x32_bf16 v[16:19], v[208:211], a[36:39], v[16:19]\n\t"
      "ds_read_b128 a[12:15], v59 offset:0\n\t"
      "v_mfma_f32_16x16x32_bf16 v[16:19], v[224:227], a[40:43], v[16:19]\n\t"
      "ds_read_b128 a[16:19], v56 offset:256\n\t"
      "v_mfma_f32_16x16x32_bf16 v[16:19], v[240:243], a[44:47], v[16:19]\n\t"
      "ds_read_b128 a[20:23], v57 offset:256\n\t"
      "v_mfma_f32_16x16x32_bf16 v[20:23], v[164:167], a[24:27], v[20:23]\n\t"
      "s_add_u32 s51, s51, 12288\n\t"
      "s_cmp_eq_u32 s51, 160256\n\t"
      "s_cselect_b32 s51, 25088, s51\n\t"
      "v_mfma_f32_16x16x32_bf16 v[20:23], v[180:183], a[28:31], v[20:23]\n\t"
      "s_waitcnt lgkmcnt(6)\n\t"
      "v_mfma_f32_16x16x32_bf16 v[20:23], v[196:199], a[32:35], v[20:23]\n\t"
      "buffer_store_dwordx4 a[56:59], v11, s[44:47], s49 offen offset:2048 sc1\n\t"
      "v_mfma_f32_16x16x32_bf16 v[20:23], v[212:215], a[36:39], v[20:23]\n\t"
      "v_mfma_f32_16x16x32_bf16 v[20:23], v[228:231], a[40:43], v[20:23]\n\t"
      "s_waitcnt lgkmcnt(0)\n\t"
      "v_mfma_f32_16x16x32_bf16 v[20:23], v[244:247], a[44:47], v[20:23]\n\t"
      "v_mfma_f32_16x16x32_bf16 v[24:27], v[168:171], a[24:27], v[24:27]\n\t"
      "v_mfma_f32_16x16x32_bf16 v[24:27], v[184:187], a[28:31], v[24:27]\n\t"
      "v_cvt_pk_bf16_f32 v40, v16, v17\n\t"
      "v_cvt_pk_bf16_f32 v41, v18, v19\n\t"
      "v_mfma_f32_16x16x32_bf16 v[24:27], v[200:203], a[32:35], v[24:27]\n\t"
      "ds_write_b64 v0, v[40:41] offset:12544\n\t"
      "v_mfma_f32_16x16x32_bf16 v[24:27], v[216:219], a[36:39], v[24:27]\n\t"
      "v_mfma_f32_16x16x32_bf16 v[24:27], v[232:235], a[40:43], v[24:27]\n\t"
      "v_mfma_f32_16x16x32_bf16 v[24:27], v[248:251], a[44:47], v[24:27]\n\t"
      "v_mfma_f32_16x16x32_bf16 v[28:31], v[172:175], a[24:27], v[28:31]\n\t"
      "v_mfma_f32_16x16x32_bf16 v[28:31], v[188:191], a[28:31], v[28:31]\n\t"
      "v_cvt_pk_bf16_f32 v42, v20, v21\n\t"
      "v_cvt_pk_bf16_f32 v43, v22, v23\n\t"
      "v_mfma_f32_16x16x32_bf16 v[28:31], v[204:207], a[32:35], v[28:31]\n\t"
      "ds_write_b64 v0, v[42:43] offset:12576\n\t"
      "v_mfma_f32_16x16x32_bf16 v[28:31], v[220:223], a[36:39], v[28:31]\n\t"
      "v_mfma_f32_16x16x32_bf16 v[28:31], v[236:239], a[40:43], v[28:31]\n\t"
      "v_mfma_f32_16x16x32_bf16 v[28:31], v[252:255], a[44:47], v[28:31]\n\t"
      "v_mfma_f32_16x16x32_bf16 v[32:35], a[108:111], a[24:27], v[32:35]\n\t"
      "v_mfma_f32_16x16x32_bf16 v[32:35], a[116:119], a[28:31], v[32:35]\n\t"
      "v_cvt_pk_bf16_f32 v44, v24, v25\n\t"
      "v_cvt_pk_bf16_f32 v45, v26, v27\n\t"
      "v_mfma_f32_16x16x32_bf16 v[32:35], a[124:127], a[32:35], v[32:35]\n\t"
      "ds_write_b64 v0, v[44:45] offset:12608\n\t"
      "v_mfma_f32_16x16x32_bf16 v[32:35], a[132:135], a[36:39], v[32:35]\n\t"
      "v_mfma_f32_16x16x32_bf16 v[32:35], a[140:143], a[40:43], v[32:35]\n\t"
      "v_mfma_f32_16x16x32_bf16 v[32:35], a[148:151], a[44:47], v[32:35]\n\t"
      "v_cvt_pk_bf16_f32 v46, v28, v29\n\t"
      "v_cvt_pk_bf16_f32 v47, v30, v31\n\t"
      "v_mfma_f32_16x16x32_bf16 v[36:39], a[112:115], a[24:27], v[36:39]\n\t"
      "ds_write_b64 v0, v[46:47] offset:12640\n\t"
      "v_mfma_f32_16x16x32_bf16 v[36:39], a[120:123], a[28:31], v[36:39]\n\t"
      "v_mfma_f32_16x16x32_bf16 v[36:39], a[128:131], a[32:35], v[36:39]\n\t"
      "s_sub_u32 s52, s52, 1\n\t"
      "s_cmp_lg_u32 s52, 0\n\t"
      "v_mfma_f32_16x16x32_bf16 v[36:39], a[136:139], a[36:39], v[36:39]\n\t"
      "v_mfma_f32_16x16x32_bf16 v[36:39], a[144:147], a[40:43], v[36:39]\n\t"
      "v_cvt_pk_bf16_f32 v48, v32, v33\n\t"
      "v_cvt_pk_bf16_f32 v49, v34, v35\n\t"
      "v_mfma_f32_16x16x32_bf16 v[36:39], a[152:155], a[44:47], v[36:39]\n\t"
      "ds_write_b64 v0, v[48:49] offset:12672\n\t"
      "s_cbranch_scc1 .Ldrain1_%=\n\t"
      "s_branch .Ltail_%=\n\t"
      ".Lloop2_%=:\n\t"
      "v_mfma_f32_16x16x32_bf16 v[16:19], v[64:67], a[0:3], 0\n\t"
      "ds_read_b128 a[24:27], v58 offset:256\n\t"
      "v_mfma_f32_16x16x32_bf16 v[16:19], v[80:83], a[4:7], v[16:19]\n\t"
      "ds_read_b128 a[28:31], v59 offset:256\n\t"
      "v_mfma_f32_16x16x32_bf16 v[16:19], v[96:99], a[8:11], v[16:19]\n\t"
      "ds_read_b128 a[32:35], v56 offset:512\n\t"
      "v_mfma_f32_16x16x32_bf16 v[16:19], v[112:115], a[12:15], v[16:19]\n\t"
      "ds_read_b128 a[36:39], v57 offset:512\n\t"
      "v_mfma_f32_16x16x32_bf16 v[16:19], v[128:131], a[16:19], v[16:19]\n\t"
      "ds_read_b128 a[40:43], v58 offset:512\n\t"
      "v_mfma_f32_16x16x32_bf16 v[16:19], v[144:147], a[20:23], v[16:19]\n\t"
      "ds_read_b128 a[44:47], v59 offset:512\n\t"
      "v_mfma_f32_16x16x32_bf16 v[20:23], v[68:71], a[0:3], 0\n\t"
      "v_cvt_pk_bf16_f32 v50, v36, v37\n\t"
      "v_cvt_pk_bf16_f32 v51, v38, v39\n\t"
      "v_mfma_f32_16x16x32_bf16 v[20:23], v[84:87], a[4:7], v[20:23]\n\t"
      "ds_write_b64 v0, v[50:51] offset:12704\n\t"
      "v_mfma_f32_16x16x32_bf16 v[20:23], v[100:103], a[8:11], v[20:23]\n\t"
      "v_add_u32 v52, s51, v15\n\t"
      "v_xor_b32 v53, 64, v52\n\t"
      "v_mfma_f32_16x16x32_bf16 v[20:23], v[116:119], a[12:15], v[20:23]\n\t"
      "v_xor_b32 v54, 128, v52\n\t"
      "v_xor_b32 v55, 192, v52\n\t"
      "v_mfma_f32_16x16x32_bf16 v[20:23], v[132:135], a[16:19], v[20:23]\n\t"
      "s_add_u32 s56, s56, 786432\n\t"
      "s_lshr_b32 s57, s57, 1\n\t"
      "v_mfma_f32_16x16x32_bf16 v[20:23], v[148:151], a[20:23], v[20:23]\n\t"
      "s_bitcmp1_b32 s57, 0\n\t"
      "s_cselect_b32 s48, 24576, 0\n\t"
      "s_add_u32 s48, s48, s56\n\t"
      "v_mfma_f32_16x16x32_bf16 v[24:27], v[72:75], a[0:3], 0\n\t"
      "v_mfma_f32_16x16x32_bf16 v[24:27], v[88:91], a[4:7], v[24:27]\n\t"
      "v_mfma_f32_16x16x32_bf16 v[24:27], v[104:107], a[8:11], v[24:27]\n\t"
      "v_mfma_f32_16x16x32_bf16 v[24:27], v[120:123], a[12:15], v[24:27]\n\t"
      "v_mfma_f32_16x16x32_bf16 v[24:27], v[136:139], a[16:19], v[24:27]\n\t"
      "s_waitcnt vmcnt(12)\n\t"
      "s_waitcnt lgkmcnt(0)\n\t"
      "s_barrier\n\t"
      "v_mfma_f32_16x16x32_bf16 v[24:27], v[152:155], a[20:23], v[24:27]\n\t"
      "v_mfma_f32_16x16x32_bf16 v[28:31], v[76:79], a[0:3], 0\n\t"
      "ds_read_b128 a[48:51], v8 offset:12544\n\t"
      "v_mfma_f32_16x16x32_bf16 v[28:31], v[92:95], a[4:7], v[28:31]\n\t"
      "ds_read_b128 a[52:55], v60 offset:13568\n\t"
      "v_mfma_f32_16x16x32_bf16 v[28:31], v[108:111], a[8:11], v[28:31]\n\t"
      "ds_read_b128 a[56:59], v61 offset:14592\n\t"
      "v_mfma_f32_16x16x32_bf16 v[28:31], v[124:127], a[12:15], v[28:31]\n\t"
      "v_mfma_f32_16x16x32_bf16 v[28:31], v[140:143], a[16:19], v[28:31]\n\t"
      "v_mfma_f32_16x16x32_bf16 v[28:31], v[156:159], a[20:23], v[28:31]\n\t"
      "v_mfma_f32_16x16x32_bf16 v[32:35], a[60:63], a[0:3], 0\n\t"
      "v_mfma_f32_16x16x32_bf16 v[32:35], a[68:71], a[4:7], v[32:35]\n\t"
      "s_waitcnt lgkmcnt(2)\n\t"
      "v_mfma_f32_16x16x32_bf16 v[32:35], a[76:79], a[8:11], v[32:35]\n\t"
      "buffer_store_dwordx4 a[48:51], v12, s[44:47], s49 offen sc1\n\t"
      "v_mfma_f32_16x16x32_bf16 v[32:35], a[84:87], a[12:15], v[32:35]\n\t"
      "v_mfma_f32_16x16x32_bf16 v[32:35], a[92:95], a[16:19], v[32:35]\n\t"
      "v_mfma_f32_16x16x32_bf16 v[32:35], a[100:103], a[20:23], v[32:35]\n\t"
      "s_add_u32 m0, s18, s50\n\t"
      "v_mfma_f32_16x16x32_bf16 v[36:39], a[64:67], a[0:3], 0\n\t"
      "buffer_load_dwordx4 v2, s[40:43], s48 offen nt lds\n\t"
      "v_mfma_f32_16x16x32_bf16 v[36:39], a[72:75], a[4:7], v[36:39]\n\t"
      "v_mfma_f32_16x16x32_bf16 v[36:39], a[80:83], a[8:11], v[36:39]\n\t"
      "v_mfma_f32_16x16x32_bf16 v[36:39], a[88:91], a[12:15], v[36:39]\n\t"
      "s_waitcnt lgkmcnt(1)\n\t"
      "v_mfma_f32_16x16x32_bf16 v[36:39], a[96:99], a[16:19], v[36:39]\n\t"
      "buffer_store_dwordx4 a[52:55], v13, s[44:47], s49 offen offset:1024 sc1\n\t"
      "v_mfma_f32_16x16x32_bf16 v[36:39], a[104:107], a[20:23], v[36:39]\n\t"
      "v_mfma_f32_16x16x32_bf16 v[16:19], v[160:163], a[24:27], v[16:19]\n\t"
      "ds_read_b128 a[0:3], v52 offset:0\n\t"
      "v_mfma_f32_16x16x32_bf16 v[16:19], v[176:179], a[28:31], v[16:19]\n\t"
      "ds_read_b128 a[4:7], v53 offset:0\n\t"
      "v_mfma_f32_16x16x32_bf16 v[16:19], v[192:195], a[32:35], v[16:19]\n\t"
      "ds_read_b128 a[8:11], v54 offset:0\n\t"
      "v_mfma_f32_16x16x32_bf16 v[16:19], v[208:211], a[36:39], v[16:19]\n\t"
      "ds_read_b128 a[12:15], v55 offset:0\n\t"
      "v_mfma_f32_16x16x32_bf16 v[16:19], v[224:227], a[40:43], v[16:19]\n\t"
      "ds_read_b128 a[16:19], v52 offset:256\n\t"
      "v_mfma_f32_16x16x32_bf16 v[16:19], v[240:243], a[44:47], v[16:19]\n\t"
      "ds_read_b128 a[20:23], v53 offset:256\n\t"
      "v_mfma_f32_16x16x32_bf16 v[20:23], v[164:167], a[24:27], v[20:23]\n\t"
      "s_add_u32 s51, s51, 12288\n\t"
      "s_cmp_eq_u32 s51, 160256\n\t"
      "s_cselect_b32 s51, 25088, s51\n\t"
      "v_mfma_f32_16x16x32_bf16 v[20:23], v[180:183], a[28:31], v[20:23]\n\t"
      "s_waitcnt lgkmcnt(6)\n\t"
      "v_mfma_f32_16x16x32_bf16 v[20:23], v[196:199], a[32:35], v[20:23]\n\t"
      "buffer_store_dwordx4 a[56:59], v14, s[44:47], s49 offen offset:2048 sc1\n\t"
      "v_mfma_f32_16x16x32_bf16 v[20:23], v[212:215], a[36:39], v[20:23]\n\t"
      "v_mfma_f32_16x16x32_bf16 v[20:23], v[228:231], a[40:43], v[20:23]\n\t"
      "s_waitcnt lgkmcnt(0)\n\t"
      "v_mfma_f32_16x16x32_bf16 v[20:23], v[244:247], a[44:47], v[20:23]\n\t"
      "v_mfma_f32_16x16x32_bf16 v[24:27], v[168:171], a[24:27], v[24:27]\n\t"
      "v_mfma_f32_16x16x32_bf16 v[24:27], v[184:187], a[28:31], v[24:27]\n\t"
      "v_cvt_pk_bf16_f32 v40, v16, v17\n\t"
      "v_cvt_pk_bf16_f32 v41, v18, v19\n\t"
      "v_mfma_f32_16x16x32_bf16 v[24:27], v[200:203], a[32:35], v[24:27]\n\t"
      "ds_write_b64 v0, v[40:41] offset:0\n\t"
      "v_mfma_f32_16x16x32_bf16 v[24:27], v[216:219], a[36:39], v[24:27]\n\t"
      "buffer_load_dwordx4 v3, s[40:43], s48 offen offset:1024 nt lds\n\t"
      "v_mfma_f32_16x16x32_bf16 v[24:27], v[232:235], a[40:43], v[24:27]\n\t"
      "s_add_u32 s58, s58, 786432\n\t"
      "s_lshr_b32 s59, s59, 1\n\t"
      "v_mfma_f32_16x16x32_bf16 v[24:27], v[248:251], a[44:47], v[24:27]\n\t"
      "s_bitcmp1_b32 s59, 0\n\t"
      "s_cselect_b32 s49, 24576, 0\n\t"
      "s_add_u32 s49, s49, s58\n\t"
      "v_mfma_f32_16x16x32_bf16 v[28:31], v[172:175], a[24:27], v[28:31]\n\t"
      "v_mfma_f32_16x16x32_bf16 v[28:31], v[188:191], a[28:31], v[28:31]\n\t"
      "v_cvt_pk_bf16_f32 v42, v20, v21\n\t"
      "v_cvt_pk_bf16_f32 v43, v22, v23\n\t"
      "v_mfma_f32_16x16x32_bf16 v[28:31], v[204:207], a[32:35], v[28:31]\n\t"
      "ds_write_b64 v0, v[42:43] offset:32\n\t"
      "v_mfma_f32_16x16x32_bf16 v[28:31], v[220:223], a[36:39], v[28:31]\n\t"
      "v_mfma_f32_16x16x32_bf16 v[28:31], v[236:239], a[40:43], v[28:31]\n\t"
      "v_mfma_f32_16x16x32_bf16 v[28:31], v[252:255], a[44:47], v[28:31]\n\t"
      "v_mfma_f32_16x16x32_bf16 v[32:35], a[108:111], a[24:27], v[32:35]\n\t"
      "v_mfma_f32_16x16x32_bf16 v[32:35], a[116:119], a[28:31], v[32:35]\n\t"
      "v_cvt_pk_bf16_f32 v44, v24, v25\n\t"
      "v_cvt_pk_bf16_f32 v45, v26, v27\n\t"
      "v_mfma_f32_16x16x32_bf16 v[32:35], a[124:127], a[32:35], v[32:35]\n\t"
      "ds_write_b64 v0, v[44:45] offset:64\n\t"
      "v_mfma_f32_16x16x32_bf16 v[32:35], a[132:135], a[36:39], v[32:35]\n\t"
      "buffer_load_dwordx4 v4, s[40:43], s48 offen offset:2048 nt lds\n\t"
      "v_mfma_f32_16x16x32_bf16 v[32:35], a[140:143], a[40:43], v[32:35]\n\t"
      "s_add_u32 s50, s50, 12288\n\t"
      "v_mfma_f32_16x16x32_bf16 v[32:35], a[148:151], a[44:47], v[32:35]\n\t"
      "v_cvt_pk_bf16_f32 v46, v28, v29\n\t"
      "v_cvt_pk_bf16_f32 v47, v30, v31\n\t"
      "v_mfma_f32_16x16x32_bf16 v[36:39], a[112:115], a[24:27], v[36:39]\n\t"
      "ds_write_b64 v0, v[46:47] offset:96\n\t"
      "v_mfma_f32_16x16x32_bf16 v[36:39], a[120:123], a[28:31], v[36:39]\n\t"
      "s_cmp_eq_u32 s50, 160256\n\t"
      "s_cselect_b32 s50, 25088, s50\n\t"
      "v_mfma_f32_16x16x32_bf16 v[36:39], a[128:131], a[32:35], v[36:39]\n\t"
      "v_mfma_f32_16x16x32_bf16 v[36:39], a[136:139], a[36:39], v[36:39]\n\t"
      "v_mfma_f32_16x16x32_bf16 v[36:39], a[144:147], a[40:43], v[36:39]\n\t"
      "v_cvt_pk_bf16_f32 v48, v32, v33\n\t"
      "v_cvt_pk_bf16_f32 v49, v34, v35\n\t"
      "v_mfma_f32_16x16x32_bf16 v[36:39], a[152:155], a[44:47], v[36:39]\n\t"
      "ds_write_b64 v0, v[48:49] offset:128\n\t"
      ".Lmid2_%=:\n\t"
      "v_mfma_f32_16x16x32_bf16 v[16:19], v[64:67], a[0:3], 0\n\t"
      "ds_read_b128 a[24:27], v54 offset:256\n\t"
      "v_mfma_f32_16x16x32_bf16 v[16:19], v[80:83], a[4:7], v[16:19]\n\t"
      "ds_read_b128 a[28:31], v55 offset:256\n\t"
      "v_mfma_f32_16x16x32_bf16 v[16:19], v[96:99], a[8:11], v[16:19]\n\t"
      "ds_read_b128 a[32:35], v52 offset:512\n\t"
      "v_mfma_f32_16x16x32_bf16 v[16:19], v[112:115], a[12:15], v[16:19]\n\t"
      "ds_read_b128 a[36:39], v53 offset:512\n\t"
      "v_mfma_f32_16x16x32_bf16 v[16:19], v[128:131], a[16:19], v[16:19]\n\t"
      "ds_read_b128 a[40:43], v54 offset:512\n\t"
      "v_mfma_f32_16x16x32_bf16 v[16:19], v[144:147], a[20:23], v[16:19]\n\t"
      "ds_read_b128 a[44:47], v55 offset:512\n\t"
      "v_mfma_f32_16x16x32_bf16 v[20:23], v[68:71], a[0:3], 0\n\t"
      "v_cvt_pk_bf16_f32 v50, v36, v37\n\t"
      "v_cvt_pk_bf16_f32 v51, v38, v39\n\t"
      "v_mfma_f32_16x16x32_bf16 v[20:23], v[84:87], a[4:7], v[20:23]\n\t"
      "ds_write_b64 v0, v[50:51] offset:160\n\t"
      "v_mfma_f32_16x16x32_bf16 v[20:23], v[100:103], a[8:11], v[20:23]\n\t"
      "v_add_u32 v56, s51, v15\n\t"
      "v_xor_b32 v57, 64, v56\n\t"
      "v_mfma_f32_16x16x32_bf16 v[20:23], v[116:119], a[12:15], v[20:23]\n\t"
      "v_xor_b32 v58, 128, v56\n\t"
      "v_xor_b32 v59, 192, v56\n\t"
      "v_mfma_f32_16x16x32_bf16 v[20:23], v[132:135], a[16:19], v[20:23]\n\t"
      "v_mfma_f32_16x16x32_bf16 v[20:23], v[148:151], a[20:23], v[20:23]\n\t"
      "v_mfma_f32_16x16x32_bf16 v[24:27], v[72:75], a[0:3], 0\n\t"
      "v_mfma_f32_16x16x32_bf16 v[24:27], v[88:91], a[4:7], v[24:27]\n\t"
      "v_mfma_f32_16x16x32_bf16 v[24:27], v[104:107], a[8:11], v[24:27]\n\t"
      "v_mfma_f32_16x16x32_bf16 v[24:27], v[120:123], a[12:15], v[24:27]\n\t"
      "v_mfma_f32_16x16x32_bf16 v[24:27], v[136:139], a[16:19], v[24:27]\n\t"
      "s_waitcnt vmcnt(12)\n\t"
      "s_waitcnt lgkmcnt(0)\n\t"
      "s_barrier\n\t"
      "v_mfma_f32_16x16x32_bf16 v[24:27], v[152:155], a[20:23], v[24:27]\n\t"
      "v_mfma_f32_16x16x32_bf16 v[28:31], v[76:79], a[0:3], 0\n\t"
      "ds_read_b128 a[48:51], v8 offset:0\n\t"
      "v_mfma_f32_16x16x32_bf16 v[28:31], v[92:95], a[4:7], v[28:31]\n\t"
      "ds_read_b128 a[52:55], v60 offset:1024\n\t"
      "v_mfma_f32_16x16x32_bf16 v[28:31], v[108:111], a[8:11], v[28:31]\n\t"
      "ds_read_b128 a[56:59], v61 offset:2048\n\t"
      "v_mfma_f32_16x16x32_bf16 v[28:31], v[124:127], a[12:15], v[28:31]\n\t"
      "v_mfma_f32_16x16x32_bf16 v[28:31], v[140:143], a[16:19], v[28:31]\n\t"
      "v_mfma_f32_16x16x32_bf16 v[28:31], v[156:159], a[20:23], v[28:31]\n\t"
      "v_mfma_f32_16x16x32_bf16 v[32:35], a[60:63], a[0:3], 0\n\t"
      "v_mfma_f32_16x16x32_bf16 v[32:35], a[68:71], a[4:7], v[32:35]\n\t"
      "s_waitcnt lgkmcnt(2)\n\t"
      "v_mfma_f32_16x16x32_bf16 v[32:35], a[76:79], a[8:11], v[32:35]\n\t"
      "buffer_store_dwordx4 a[48:51], v9, s[44:47], s49 offen sc1\n\t"
      "v_mfma_f32_16x16x32_bf16 v[32:35], a[84:87], a[12:15], v[32:35]\n\t"
      "v_mfma_f32_16x16x32_bf16 v[32:35], a[92:95], a[16:19], v[32:35]\n\t"
      "v_mfma_f32_16x16x32_bf16 v[32:35], a[100:103], a[20:23], v[32:35]\n\t"
      "s_add_u32 m0, s18, s50\n\t"
      "v_mfma_f32_16x16x32_bf16 v[36:39], a[64:67], a[0:3], 0\n\t"
      "buffer_load_dwordx4 v5, s[40:43], s48 offen nt lds\n\t"
      "v_mfma_f32_16x16x32_bf16 v[36:39], a[72:75], a[4:7], v[36:39]\n\t"
      "v_mfma_f32_16x16x32_bf16 v[36:39], a[80:83], a[8:11], v[36:39]\n\t"
      "v_mfma_f32_16x16x32_bf16 v[36:39], a[88:91], a[12:15], v[36:39]\n\t"
      "s_waitcnt lgkmcnt(1)\n\t"
      "v_mfma_f32_16x16x32_bf16 v[36:39], a[96:99], a[16:19], v[36:39]\n\t"
      "buffer_store_dwordx4 a[52:55], v10, s[44:47], s49 offen offset:1024 sc1\n\t"
      "v_mfma_f32_16x16x32_bf16 v[36:39], a[104:107], a[20:23], v[36:39]\n\t"
      "v_mfma_f32_16x16x32_bf16 v[16:19], v[160:163], a[24:27], v[16:19]\n\t"
      "ds_read_b128 a[0:3], v56 offset:0\n\t"
      "v_mfma_f32_16x16x32_bf16 v[16:19], v[176:179], a[28:31], v[16:19]\n\t"
      "ds_read_b128 a[4:7], v57 offset:0\n\t"
      "v_mfma_f32_16x16x32_bf16 v[16:19], v[192:195], a[32:35], v[16:19]\n\t"
      "ds_read_b128 a[8:11], v58 offset:0\n\t"
      "v_mfma_f32_16x16x32_bf16 v[16:19], v[208:211], a[36:39], v[16:19]\n\t"
      "ds_read_b128 a[12:15], v59 offset:0\n\t"
      "v_mfma_f32_16x16x32_bf16 v[16:19], v[224:227], a[40:43], v[16:19]\n\t"
      "ds_read_b128 a[16:19], v56 offset:256\n\t"
      "v_mfma_f32_16x16x32_bf16 v[16:19], v[240:243], a[44:47], v[16:19]\n\t"
      "ds_read_b128 a[20:23], v57 offset:256\n\t"
      "v_mfma_f32_16x16x32_bf16 v[20:23], v[164:167], a[24:27], v[20:23]\n\t"
      "s_add_u32 s51, s51, 12288\n\t"
      "s_cmp_eq_u32 s51, 160256\n\t"
      "s_cselect_b32 s51, 25088, s51\n\t"
      "v_mfma_f32_16x16x32_bf16 v[20:23], v[180:183], a[28:31], v[20:23]\n\t"
      "s_waitcnt lgkmcnt(6)\n\t"
      "v_mfma_f32_16x16x32_bf16 v[20:23], v[196:199], a[32:35], v[20:23]\n\t"
      "buffer_store_dwordx4 a[56:59], v11, s[44:47], s49 offen offset:2048 sc1\n\t"
      "v_mfma_f32_16x16x32_bf16 v[20:23], v[212:215], a[36:39], v[20:23]\n\t"
      "v_mfma_f32_16x16x32_bf16 v[20:23], v[228:231], a[40:43], v[20:23]\n\t"
      "s_waitcnt lgkmcnt(0)\n\t"
      "v_mfma_f32_16x16x32_bf16 v[20:23], v[244:247], a[44:47], v[20:23]\n\t"
      "v_mfma_f32_16x16x32_bf16 v[24:27], v[168:171], a[24:27], v[24:27]\n\t"
      "v_mfma_f32_16x16x32_bf16 v[24:27], v[184:187], a[28:31], v[24:27]\n\t"
      "v_cvt_pk_bf16_f32 v40, v16, v17\n\t"
      "v_cvt_pk_bf16_f32 v41, v18, v19\n\t"
      "v_mfma_f32_16x16x32_bf16 v[24:27], v[200:203], a[32:35], v[24:27]\n\t"
      "ds_write_b64 v0, v[40:41] offset:12544\n\t"
      "v_mfma_f32_16x16x32_bf16 v[24:27], v[216:219], a[36:39], v[24:27]\n\t"
      "buffer_load_dwordx4 v6, s[40:43], s48 offen offset:1024 nt lds\n\t"
      "v_mfma_f32_16x16x32_bf16 v[24:27], v[232:235], a[40:43], v[24:27]\n\t"
      "v_mfma_f32_16x16x32_bf16 v[24:27], v[248:251], a[44:47], v[24:27]\n\t"
      "v_mfma_f32_16x16x32_bf16 v[28:31], v[172:175], a[24:27], v[28:31]\n\t"
      "v_mfma_f32_16x16x32_bf16 v[28:31], v[188:191], a[28:31], v[28:31]\n\t"
      "v_cvt_pk_bf16_f32 v42, v20, v21\n\t"
      "v_cvt_pk_bf16_f32 v43, v22, v23\n\t"
      "v_mfma_f32_16x16x32_bf16 v[28:31], v[204:207], a[32:35], v[28:31]\n\t"
      "ds_write_b64 v0, v[42:43] offset:12576\n\t"
      "v_mfma_f32_16x16x32_bf16 v[28:31], v[220:223], a[36:39], v[28:31]\n\t"
      "v_mfma_f32_16x16x32_bf16 v[28:31], v[236:239], a[40:43], v[28:31]\n\t"
      "v_mfma_f32_16x16x32_bf16 v[28:31], v[252:255], a[44:47], v[28:31]\n\t"
      "v_mfma_f32_16x16x32_bf16 v[32:35], a[108:111], a[24:27], v[32:35]\n\t"
      "v_mfma_f32_16x16x32_bf16 v[32:35], a[116:119], a[28:31], v[32:35]\n\t"
      "v_cvt_pk_bf16_f32 v44, v24, v25\n\t"
      "v_cvt_pk_bf16_f32 v45, v26, v27\n\t"
      "v_mfma_f32_16x16x32_bf16 v[32:35], a[124:127], a[32:35], v[32:35]\n\t"
      "ds_write_b64 v0, v[44:45] offset:12608\n\t"
      "v_mfma_f32_16x16x32_bf16 v[32:35], a[132:135], a[36:39], v[32:35]\n\t"
      "buffer_load_dwordx4 v7, s[40:43], s48 offen offset:2048 nt lds\n\t"
      "v_mfma_f32_16x16x32_bf16 v[32:35], a[140:143], a[40:43], v[32:35]\n\t"
      "s_add_u32 s50, s50, 12288\n\t"
      "v_mfma_f32_16x16x32_bf16 v[32:35], a[148:151], a[44:47], v[32:35]\n\t"
      "v_cvt_pk_bf16_f32 v46, v28, v29\n\t"
      "v_cvt_pk_bf16_f32 v47, v30, v31\n\t"
      "v_mfma_f32_16x16x32_bf16 v[36:39], a[112:115], a[24:27], v[36:39]\n\t"
      "ds_write_b64 v0, v[46:47] offset:12640\n\t"
      "v_mfma_f32_16x16x32_bf16 v[36:39], a[120:123], a[28:31], v[36:39]\n\t"
      "s_cmp_eq_u32 s50, 160256\n\t"
      "s_cselect_b32 s50, 25088, s50\n\t"
      "v_mfma_f32_16x16x32_bf16 v[36:39], a[128:131], a[32:35], v[36:39]\n\t"
      "s_sub_u32 s52, s52, 1\n\t"
      "s_cmp_lg_u32 s52, 0\n\t"
      "v_mfma_f32_16x16x32_bf16 v[36:39], a[136:139], a[36:39], v[36:39]\n\t"
      "v_mfma_f32_16x16x32_bf16 v[36:39], a[144:147], a[40:43], v[36:39]\n\t"
      "v_cvt_pk_bf16_f32 v48, v32, v33\n\t"
      "v_cvt_pk_bf16_f32 v49, v34, v35\n\t"
      "v_mfma_f32_16x16x32_bf16 v[36:39], a[152:155], a[44:47], v[36:39]\n\t"
      "ds_write_b64 v0, v[48:49] offset:12672\n\t"
      "s_cbranch_scc1 .Lloop2_%=\n\t"
      "s_cmp_eq_u32 s48, 113221632\n\t"
      "s_cbranch_scc1 .Lwrap2_%=\n\t"
      ".Lwrapped2_%=:\n\t"
      "s_mov_b32 s52, 2\n\t"
      ".Ldrain2_%=:\n\t"
      "v_mfma_f32_16x16x32_bf16 v[16:19], v[64:67], a[0:3], 0\n\t"
      "ds_read_b128 a[24:27], v58 offset:256\n\t"
      "v_mfma_f32_16x16x32_bf16 v[16:19], v[80:83], a[4:7], v[16:19]\n\t"
      "ds_read_b128 a[28:31], v59 offset:256\n\t"
      "v_mfma_f32_16x16x32_bf16 v[16:19], v[96:99], a[8:11], v[16:19]\n\t"
      "ds_read_b128 a[32:35], v56 offset:512\n\t"
      "v_mfma_f32_16x16x32_bf16 v[16:19], v[112:115], a[12:15], v[16:19]\n\t"
      "ds_read_b128 a[36:39], v57 offset:512\n\t"
      "v_mfma_f32_16x16x32_bf16 v[16:19], v[128:131], a[16:19], v[16:19]\n\t"
      "ds_read_b128 a[40:43], v58 offset:512\n\t"
      "v_mfma_f32_16x16x32_bf16 v[16:19], v[144:147], a[20:23], v[16:19]\n\t"
      "ds_read_b128 a[44:47], v59 offset:512\n\t"
      "v_mfma_f32_16x16x32_bf16 v[20:23], v[68:71], a[0:3], 0\n\t"
      "v_cvt_pk_bf16_f32 v50, v36, v37\n\t"
      "v_cvt_pk_bf16_f32 v51, v38, v39\n\t"
      "v_mfma_f32_16x16x32_bf16 v[20:23], v[84:87], a[4:7], v[20:23]\n\t"
      "ds_write_b64 v0, v[50:51] offset:12704\n\t"
      "v_mfma_f32_16x16x32_bf16 v[20:23], v[100:103], a[8:11], v[20:23]\n\t"
      "v_add_u32 v52, s51, v15\n\t"
      "v_xor_b32 v53, 64, v52\n\t"
      "v_mfma_f32_16x16x32_bf16 v[20:23], v[116:119], a[12:15], v[20:23]\n\t"
      "v_xor_b32 v54, 128, v52\n\t"
      "v_xor_b32 v55, 192, v52\n\t"
      "v_mfma_f32_16x16x32_bf16 v[20:23], v[132:135], a[16:19], v[20:23]\n\t"
      "v_mfma_f32_16x16x32_bf16 v[20:23], v[148:151], a[20:23], v[20:23]\n\t"
      "v_mfma_f32_16x16x32_bf16 v[24:27], v[72:75], a[0:3], 0\n\t"
      "v_mfma_f32_16x16x32_bf16 v[24:27], v[88:91], a[4:7], v[24:27]\n\t"
      "v_mfma_f32_16x16x32_bf16 v[24:27], v[104:107], a[8:11], v[24:27]\n\t"
      "v_mfma_f32_16x16x32_bf16 v[24:27], v[120:123], a[12:15], v[24:27]\n\t"
      "v_mfma_f32_16x16x32_bf16 v[24:27], v[136:139], a[16:19], v[24:27]\n\t"
      "s_waitcnt vmcnt(6)\n\t"
      "s_waitcnt lgkmcnt(0)\n\t"
      "s_barrier\n\t"
      "v_mfma_f32_16x16x32_bf16 v[24:27], v[152:155], a[20:23], v[24:27]\n\t"
      "v_mfma_f32_16x16x32_bf16 v[28:31], v[76:79], a[0:3], 0\n\t"
      "ds_read_b128 a[48:51], v8 offset:12544\n\t"
      "v_mfma_f32_16x16x32_bf16 v[28:31], v[92:95], a[4:7], v[28:31]\n\t"
      "ds_read_b128 a[52:55], v60 offset:13568\n\t"
      "v_mfma_f32_16x16x32_bf16 v[28:31], v[108:111], a[8:11], v[28:31]\n\t"
      "ds_read_b128 a[56:59], v61 offset:14592\n\t"
      "v_mfma_f32_16x16x32_bf16 v[28:31], v[124:127], a[12:15], v[28:31]\n\t"
      "v_mfma_f32_16x16x32_bf16 v[28:31], v[140:143], a[16:19], v[28:31]\n\t"
      "v_mfma_f32_16x16x32_bf16 v[28:31], v[156:159], a[20:23], v[28:31]\n\t"
      "v_mfma_f32_16x16x32_bf16 v[32:35], a[60:63], a[0:3], 0\n\t"
      "v_mfma_f32_16x16x32_bf16 v[32:35], a[68:71], a[4:7], v[32:35]\n\t"
      "s_waitcnt lgkmcnt(2)\n\t"
      "v_mfma_f32_16x16x32_bf16 v[32:35], a[76:79], a[8:11], v[32:35]\n\t"
      "buffer_store_dwordx4 a[48:51], v12, s[44:47], s49 offen sc1\n\t"
      "v_mfma_f32_16x16x32_bf16 v[32:35], a[84:87], a[12:15], v[32:35]\n\t"
      "v_mfma_f32_16x16x32_bf16 v[32:35], a[92:95], a[16:19], v[32:35]\n\t"
      "v_mfma_f32_16x16x32_bf16 v[32:35], a[100:103], a[20:23], v[32:35]\n\t"
      "v_mfma_f32_16x16x32_bf16 v[36:39], a[64:67], a[0:3], 0\n\t"
      "v_mfma_f32_16x16x32_bf16 v[36:39], a[72:75], a[4:7], v[36:39]\n\t"
      "v_mfma_f32_16x16x32_bf16 v[36:39], a[80:83], a[8:11], v[36:39]\n\t"
      "v_mfma_f32_16x16x32_bf16 v[36:39], a[88:91], a[12:15], v[36:39]\n\t"
      "s_waitcnt lgkmcnt(1)\n\t"
      "v_mfma_f32_16x16x32_bf16 v[36:39], a[96:99], a[16:19], v[36:39]\n\t"
      "buffer_store_dwordx4 a[52:55], v13, s[44:47], s49 offen offset:1024 sc1\n\t"
      "v_mfma_f32_16x16x32_bf16 v[36:39], a[104:107], a[20:23], v[36:39]\n\t"
      "v_mfma_f32_16x16x32_bf16 v[16:19], v[160:163], a[24:27], v[16:19]\n\t"
      "ds_read_b128 a[0:3], v52 offset:0\n\t"
      "v_mfma_f32_16x16x32_bf16 v[16:19], v[176:179], a[28:31], v[16:19]\n\t"
      "ds_read_b128 a[4:7], v53 offset:0\n\t"
      "v_mfma_f32_16x16x32_bf16 v[16:19], v[192:195], a[32:35], v[16:19]\n\t"
      "ds_read_b128 a[8:11], v54 offset:0\n\t"
      "v_mfma_f32_16x16x32_bf16 v[16:19], v[208:211], a[36:39], v[16:19]\n\t"
      "ds_read_b128 a[12:15], v55 offset:0\n\t"
      "v_mfma_f32_16x16x32_bf16 v[16:19], v[224:227], a[40:43], v[16:19]\n\t"
      "ds_read_b128 a[16:19], v52 offset:256\n\t"
      "v_mfma_f32_16x16x32_bf16 v[16:19], v[240:243], a[44:47], v[16:19]\n\t"
      "ds_read_b128 a[20:23], v53 offset:256\n\t"
      "v_mfma_f32_16x16x32_bf16 v[20:23], v[164:167], a[24:27], v[20:23]\n\t"
      "s_add_u32 s51, s51, 12288\n\t"
      "s_cmp_eq_u32 s51, 160256\n\t"
      "s_cselect_b32 s51, 25088, s51\n\t"
      "v_mfma_f32_16x16x32_bf16 v[20:23], v[180:183], a[28:31], v[20:23]\n\t"
      "s_waitcnt lgkmcnt(6)\n\t"
      "v_mfma_f32_16x16x32_bf16 v[20:23], v[196:199], a[32:35], v[20:23]\n\t"
      "buffer_store_dwordx4 a[56:59], v14, s[44:47], s49 offen offset:2048 sc1\n\t"
      "v_mfma_f32_16x16x32_bf16 v[20:23], v[212:215], a[36:39], v[20:23]\n\t"
      "v_mfma_f32_16x16x32_bf16 v[20:23], v[228:231], a[40:43], v[20:23]\n\t"
      "s_waitcnt lgkmcnt(0)\n\t"
      "v_mfma_f32_16x16x32_bf16 v[20:23], v[244:247], a[44:47], v[20:23]\n\t"
      "v_mfma_f32_16x16x32_bf16 v[24:27], v[168:171], a[24:27], v[24:27]\n\t"
      "v_mfma_f32_16x16x32_bf16 v[24:27], v[184:187], a[28:31], v[24:27]\n\t"
      "v_cvt_pk_bf16_f32 v40, v16, v17\n\t"
      "v_cvt_pk_bf16_f32 v41, v18, v19\n\t"
      "v_mfma_f32_16x16x32_bf16 v[24:27], v[200:203], a[32:35], v[24:27]\n\t"
      "ds_write_b64 v0, v[40:41] offset:0\n\t"
      "v_mfma_f32_16x16x32_bf16 v[24:27], v[216:219], a[36:39], v[24:27]\n\t"
      "v_mfma_f32_16x16x32_bf16 v[24:27], v[232:235], a[40:43], v[24:27]\n\t"
      "s_add_u32 s58, s58, 786432\n\t"
      "s_lshr_b32 s59, s59, 1\n\t"
      "v_mfma_f32_16x16x32_bf16 v[24:27], v[248:251], a[44:47], v[24:27]\n\t"
      "s_bitcmp1_b32 s59, 0\n\t"
      "s_cselect_b32 s49, 24576, 0\n\t"
      "s_add_u32 s49, s49, s58\n\t"
      "v_mfma_f32_16x16x32_bf16 v[28:31], v[172:175], a[24:27], v[28:31]\n\t"
      "v_mfma_f32_16x16x32_bf16 v[28:31], v[188:191], a[28:31], v[28:31]\n\t"
      "v_cvt_pk_bf16_f32 v42, v20, v21\n\t"
      "v_cvt_pk_bf16_f32 v43, v22, v23\n\t"
      "v_mfma_f32_16x16x32_bf16 v[28:31], v[204:207], a[32:35], v[28:31]\n\t"
      "ds_write_b64 v0, v[42:43] offset:32\n\t"
      "v_mfma_f32_16x16x32_bf16 v[28:31], v[220:223], a[36:39], v[28:31]\n\t"
      "v_mfma_f32_16x16x32_bf16 v[28:31], v[236:239], a[40:43], v[28:31]\n\t"
      "v_mfma_f32_16x16x32_bf16 v[28:31], v[252:255], a[44:47], v[28:31]\n\t"
      "v_mfma_f32_16x16x32_bf16 v[32:35], a[108:111], a[24:27], v[32:35]\n\t"
      "v_mfma_f32_16x16x32_bf16 v[32:35], a[116:119], a[28:31], v[32:35]\n\t"
      "v_cvt_pk_bf16_f32 v44, v24, v25\n\t"
      "v_cvt_pk_bf16_f32 v45, v26, v27\n\t"
      "v_mfma_f32_16x16x32_bf16 v[32:35], a[124:127], a[32:35], v[32:35]\n\t"
      "ds_write_b64 v0, v[44:45] offset:64\n\t"
      "v_mfma_f32_16x16x32_bf16 v[32:35], a[132:135], a[36:39], v[32:35]\n\t"
      "v_mfma_f32_16x16x32_bf16 v[32:35], a[140:143], a[40:43], v[32:35]\n\t"
      "v_mfma_f32_16x16x32_bf16 v[32:35], a[148:151], a[44:47], v[32:35]\n\t"
      "v_cvt_pk_bf16_f32 v46, v28, v29\n\t"
      "v_cvt_pk_bf16_f32 v47, v30, v31\n\t"
      "v_mfma_f32_16x16x32_bf16 v[36:39], a[112:115], a[24:27], v[36:39]\n\t"
      "ds_write_b64 v0, v[46:47] offset:96\n\t"
      "v_mfma_f32_16x16x32_bf16 v[36:39], a[120:123], a[28:31], v[36:39]\n\t"
      "v_mfma_f32_16x16x32_bf16 v[36:39], a[128:131], a[32:35], v[36:39]\n\t"
      "v_mfma_f32_16x16x32_bf16 v[36:39], a[136:139], a[36:39], v[36:39]\n\t"
      "v_mfma_f32_16x16x32_bf16 v[36:39], a[144:147], a[40:43], v[36:39]\n\t"
      "v_cvt_pk_bf16_f32 v48, v32, v33\n\t"
      "v_cvt_pk_bf16_f32 v49, v34, v35\n\t"
      "v_mfma_f32_16x16x32_bf16 v[36:39], a[152:155], a[44:47], v[36:39]\n\t"
      "ds_write_b64 v0, v[48:49] offset:128\n\t"
      "v_mfma_f32_16x16x32_bf16 v[16:19], v[64:67], a[0:3], 0\n\t"
      "ds_read_b128 a[24:27], v54 offset:256\n\t"
      "v_mfma_f32_16x16x32_bf16 v[16:19], v[80:83], a[4:7], v[16:19]\n\t"
      "ds_read_b128 a[28:31], v55 offset:256\n\t"
      "v_mfma_f32_16x16x32_bf16 v[16:19], v[96:99], a[8:11], v[16:19]\n\t"
      "ds_read_b128 a[32:35], v52 offset:512\n\t"
      "v_mfma_f32_16x16x32_bf16 v[16:19], v[112:115], a[12:15], v[16:19]\n\t"
      "ds_read_b128 a[36:39], v53 offset:512\n\t"
      "v_mfma_f32_16x16x32_bf16 v[16:19], v[128:131], a[16:19], v[16:19]\n\t"
      "ds_read_b128 a[40:43], v54 offset:512\n\t"
      "v_mfma_f32_16x16x32_bf16 v[16:19], v[144:147], a[20:23], v[16:19]\n\t"
      "ds_read_b128 a[44:47], v55 offset:512\n\t"
      "v_mfma_f32_16x16x32_bf16 v[20:23], v[68:71], a[0:3], 0\n\t"
      "v_cvt_pk_bf16_f32 v50, v36, v37\n\t"
      "v_cvt_pk_bf16_f32 v51, v38, v39\n\t"
      "v_mfma_f32_16x16x32_bf16 v[20:23], v[84:87], a[4:7], v[20:23]\n\t"
      "ds_write_b64 v0, v[50:51] offset:160\n\t"
      "v_mfma_f32_16x16x32_bf16 v[20:23], v[100:103], a[8:11], v[20:23]\n\t"
      "v_add_u32 v56, s51, v15\n\t"
      "v_xor_b32 v57, 64, v56\n\t"
      "v_mfma_f32_16x16x32_bf16 v[20:23], v[116:119], a[12:15], v[20:23]\n\t"
      "v_xor_b32 v58, 128, v56\n\t"
      "v_xor_b32 v59, 192, v56\n\t"
      "v_mfma_f32_16x16x32_bf16 v[20:23], v[132:135], a[16:19], v[20:23]\n\t"
      "v_mfma_f32_16x16x32_bf16 v[20:23], v[148:151], a[20:23], v[20:23]\n\t"
      "v_mfma_f32_16x16x32_bf16 v[24:27], v[72:75], a[0:3], 0\n\t"
      "v_mfma_f32_16x16x32_bf16 v[24:27], v[88:91], a[4:7], v[24:27]\n\t"
      "v_mfma_f32_16x16x32_bf16 v[24:27], v[104:107], a[8:11], v[24:27]\n\t"
      "v_mfma_f32_16x16x32_bf16 v[24:27], v[120:123], a[12:15], v[24:27]\n\t"
      "v_mfma_f32_16x16x32_bf16 v[24:27], v[136:139], a[16:19], v[24:27]\n\t"
      "s_waitcnt vmcnt(6)\n\t"
      "s_waitcnt lgkmcnt(0)\n\t"
      "s_barrier\n\t"
      "v_mfma_f32_16x16x32_bf16 v[24:27], v[152:155], a[20:23], v[24:27]\n\t"
      "v_mfma_f32_16x16x32_bf16 v[28:31], v[76:79], a[0:3], 0\n\t"
      "ds_read_b128 a[48:51], v8 offset:0\n\t"
      "v_mfma_f32_16x16x32_bf16 v[28:31], v[92:95], a[4:7], v[28:31]\n\t"
      "ds_read_b128 a[52:55], v60 offset:1024\n\t"
      "v_mfma_f32_16x16x32_bf16 v[28:31], v[108:111], a[8:11], v[28:31]\n\t"
      "ds_read_b128 a[56:59], v61 offset:2048\n\t"
      "v_mfma_f32_16x16x32_bf16 v[28:31], v[124:127], a[12:15], v[28:31]\n\t"
      "v_mfma_f32_16x16x32_bf16 v[28:31], v[140:143], a[16:19], v[28:31]\n\t"
      "v_mfma_f32_16x16x32_bf16 v[28:31], v[156:159], a[20:23], v[28:31]\n\t"
      "v_mfma_f32_16x16x32_bf16 v[32:35], a[60:63], a[0:3], 0\n\t"
      "v_mfma_f32_16x16x32_bf16 v[32:35], a[68:71], a[4:7], v[32:35]\n\t"
      "s_waitcnt lgkmcnt(2)\n\t"
      "v_mfma_f32_16x16x32_bf16 v[32:35], a[76:79], a[8:11], v[32:35]\n\t"
      "buffer_store_dwordx4 a[48:51], v9, s[44:47], s49 offen sc1\n\t"
      "v_mfma_f32_16x16x32_bf16 v[32:35], a[84:87], a[12:15], v[32:35]\n\t"
      "v_mfma_f32_16x16x32_bf16 v[32:35], a[92:95], a[16:19], v[32:35]\n\t"
      "v_mfma_f32_16x16x32_bf16 v[32:35], a[100:103], a[20:23], v[32:35]\n\t"
      "v_mfma_f32_16x16x32_bf16 v[36:39], a[64:67], a[0:3], 0\n\t"
      "v_mfma_f32_16x16x32_bf16 v[36:39], a[72:75], a[4:7], v[36:39]\n\t"
      "v_mfma_f32_16x16x32_bf16 v[36:39], a[80:83], a[8:11], v[36:39]\n\t"
      "v_mfma_f32_16x16x32_bf16 v[36:39], a[88:91], a[12:15], v[36:39]\n\t"
      "s_waitcnt lgkmcnt(1)\n\t"
      "v_mfma_f32_16x16x32_bf16 v[36:39], a[96:99], a[16:19], v[36:39]\n\t"
      "buffer_store_dwordx4 a[52:55], v10, s[44:47], s49 offen offset:1024 sc1\n\t"
      "v_mfma_f32_16x16x32_bf16 v[36:39], a[104:107], a[20:23], v[36:39]\n\t"
      "v_mfma_f32_16x16x32_bf16 v[16:19], v[160:163], a[24:27], v[16:19]\n\t"
      "ds_read_b128 a[0:3], v56 offset:0\n\t"
      "v_mfma_f32_16x16x32_bf16 v[16:19], v[176:179], a[28:31], v[16:19]\n\t"
      "ds_read_b128 a[4:7], v57 offset:0\n\t"
      "v_mfma_f32_16x16x32_bf16 v[16:19], v[192:195], a[32:35], v[16:19]\n\t"
      "ds_read_b128 a[8:11], v58 offset:0\n\t"
      "v_mfma_f32_16x16x32_bf16 v[16:19], v[208:211], a[36:39], v[16:19]\n\t"
      "ds_read_b128 a[12:15], v59 offset:0\n\t"
      "v_mfma_f32_16x16x32_bf16 v[16:19], v[224:227], a[40:43], v[16:19]\n\t"
      "ds_read_b128 a[16:19], v56 offset:256\n\t"
      "v_mfma_f32_16x16x32_bf16 v[16:19], v[240:243], a[44:47], v[16:19]\n\t"
      "ds_read_b128 a[20:23], v57 offset:256\n\t"
      "v_mfma_f32_16x16x32_bf16 v[20:23], v[164:167], a[24:27], v[20:23]\n\t"
      "s_add_u32 s51, s51, 12288\n\t"
      "s_cmp_eq_u32 s51, 160256\n\t"
      "s_cselect_b32 s51, 25088, s51\n\t"
      "v_mfma_f32_16x16x32_bf16 v[20:23], v[180:183], a[28:31], v[20:23]\n\t"
      "s_waitcnt lgkmcnt(6)\n\t"
      "v_mfma_f32_16x16x32_bf16 v[20:23], v[196:199], a[32:35], v[20:23]\n\t"
      "buffer_store_dwordx4 a[56:59], v11, s[44:47], s49 offen offset:2048 sc1\n\t"
      "v_mfma_f32_16x16x32_bf16 v[20:23], v[212:215], a[36:39], v[20:23]\n\t"
      "v_mfma_f32_16x16x32_bf16 v[20:23], v[228:231], a[40:43], v[20:23]\n\t"
      "s_waitcnt lgkmcnt(0)\n\t"
      "v_mfma_f32_16x16x32_bf16 v[20:23], v[244:247], a[44:47], v[20:23]\n\t"
      "v_mfma_f32_16x16x32_bf16 v[24:27], v[168:171], a[24:27], v[24:27]\n\t"
      "v_mfma_f32_16x16x32_bf16 v[24:27], v[184:187], a[28:31], v[24:27]\n\t"
      "v_cvt_pk_bf16_f32 v40, v16, v17\n\t"
      "v_cvt_pk_bf16_f32 v41, v18, v19\n\t"
      "v_mfma_f32_16x16x32_bf16 v[24:27], v[200:203], a[32:35], v[24:27]\n\t"
      "ds_write_b64 v0, v[40:41] offset:12544\n\t"
      "v_mfma_f32_16x16x32_bf16 v[24:27], v[216:219], a[36:39], v[24:27]\n\t"
      "v_mfma_f32_16x16x32_bf16 v[24:27], v[232:235], a[40:43], v[24:27]\n\t"
      "v_mfma_f32_16x16x32_bf16 v[24:27], v[248:251], a[44:47], v[24:27]\n\t"
      "v_mfma_f32_16x16x32_bf16 v[28:31], v[172:175], a[24:27], v[28:31]\n\t"
      "v_mfma_f32_16x16x32_bf16 v[28:31], v[188:191], a[28:31], v[28:31]\n\t"
      "v_cvt_pk_bf16_f32 v42, v20, v21\n\t"
      "v_cvt_pk_bf16_f32 v43, v22, v23\n\t"
      "v_mfma_f32_16x16x32_bf16 v[28:31], v[204:207], a[32:35], v[28:31]\n\t"
      "ds_write_b64 v0, v[42:43] offset:12576\n\t"
      "v_mfma_f32_16x16x32_bf16 v[28:31], v[220:223], a[36:39], v[28:31]\n\t"
      "v_mfma_f32_16x16x32_bf16 v[28:31], v[236:239], a[40:43], v[28:31]\n\t"
      "v_mfma_f32_16x16x32_bf16 v[28:31], v[252:255], a[44:47], v[28:31]\n\t"
      "v_mfma_f32_16x16x32_bf16 v[32:35], a[108:111], a[24:27], v[32:35]\n\t"
      "v_mfma_f32_16x16x32_bf16 v[32:35], a[116:119], a[28:31], v[32:35]\n\t"
      "v_cvt_pk_bf16_f32 v44, v24, v25\n\t"
      "v_cvt_pk_bf16_f32 v45, v26, v27\n\t"
      "v_mfma_f32_16x16x32_bf16 v[32:35], a[124:127], a[32:35], v[32:35]\n\t"
      "ds_write_b64 v0, v[44:45] offset:12608\n\t"
      "v_mfma_f32_16x16x32_bf16 v[32:35], a[132:135], a[36:39], v[32:35]\n\t"
      "v_mfma_f32_16x16x32_bf16 v[32:35], a[140:143], a[40:43], v[32:35]\n\t"
      "v_mfma_f32_16x16x32_bf16 v[32:35], a[148:151], a[44:47], v[32:35]\n\t"
      "v_cvt_pk_bf16_f32 v46, v28, v29\n\t"
      "v_cvt_pk_bf16_f32 v47, v30, v31\n\t"
      "v_mfma_f32_16x16x32_bf16 v[36:39], a[112:115], a[24:27], v[36:39]\n\t"
      "ds_write_b64 v0, v[46:47] offset:12640\n\t"
      "v_mfma_f32_16x16x32_bf16 v[36:39], a[120:123], a[28:31], v[36:39]\n\t"
      "v_mfma_f32_16x16x32_bf16 v[36:39], a[128:131], a[32:35], v[36:39]\n\t"
      "s_sub_u32 s52, s52, 1\n\t"
      "s_cmp_lg_u32 s52, 0\n\t"
      "v_mfma_f32_16x16x32_bf16 v[36:39], a[136:139], a[36:39], v[36:39]\n\t"
      "v_mfma_f32_16x16x32_bf16 v[36:39], a[144:147], a[40:43], v[36:39]\n\t"
      "v_cvt_pk_bf16_f32 v48, v32, v33\n\t"
      "v_cvt_pk_bf16_f32 v49, v34, v35\n\t"
      "v_mfma_f32_16x16x32_bf16 v[36:39], a[152:155], a[44:47], v[36:39]\n\t"
      "ds_write_b64 v0, v[48:49] offset:12672\n\t"
      "s_cbranch_scc1 .Ldrain2_%=\n\t"
      "s_branch .Ltail_%=\n\t"
      ".Lloop3_%=:\n\t"
      "v_mfma_f32_16x16x32_bf16 v[16:19], v[64:67], a[0:3], 0\n\t"
      "ds_read_b128 a[24:27], v58 offset:256\n\t"
      "v_mfma_f32_16x16x32_bf16 v[16:19], v[80:83], a[4:7], v[16:19]\n\t"
      "ds_read_b128 a[28:31], v59 offset:256\n\t"
      "v_mfma_f32_16x16x32_bf16 v[16:19], v[96:99], a[8:11], v[16:19]\n\t"
      "ds_read_b128 a[32:35], v56 offset:512\n\t"
      "v_mfma_f32_16x16x32_bf16 v[16:19], v[112:115], a[12:15], v[16:19]\n\t"
      "ds_read_b128 a[36:39], v57 offset:512\n\t"
      "v_mfma_f32_16x16x32_bf16 v[16:19], v[128:131], a[16:19], v[16:19]\n\t"
      "ds_read_b128 a[40:43], v58 offset:512\n\t"
      "v_mfma_f32_16x16x32_bf16 v[16:19], v[144:147], a[20:23], v[16:19]\n\t"
      "ds_read_b128 a[44:47], v59 offset:512\n\t"
      "v_mfma_f32_16x16x32_bf16 v[20:23], v[68:71], a[0:3], 0\n\t"
      "v_cvt_pk_bf16_f32 v50, v36, v37\n\t"
      "v_cvt_pk_bf16_f32 v51, v38, v39\n\t"
      "v_mfma_f32_16x16x32_bf16 v[20:23], v[84:87], a[4:7], v[20:23]\n\t"
      "ds_write_b64 v0, v[50:51] offset:12704\n\t"
      "v_mfma_f32_16x16x32_bf16 v[20:23], v[100:103], a[8:11], v[20:23]\n\t"
      "v_add_u32 v52, s51, v15\n\t"
      "v_xor_b32 v53, 64, v52\n\t"
      "v_mfma_f32_16x16x32_bf16 v[20:23], v[116:119], a[12:15], v[20:23]\n\t"
      "v_xor_b32 v54, 128, v52\n\t"
      "v_xor_b32 v55, 192, v52\n\t"
      "v_mfma_f32_16x16x32_bf16 v[20:23], v[132:135], a[16:19], v[20:23]\n\t"
      "s_add_u32 s56, s56, 786432\n\t"
      "s_lshr_b32 s57, s57, 1\n\t"
      "v_mfma_f32_16x16x32_bf16 v[20:23], v[148:151], a[20:23], v[20:23]\n\t"
      "s_bitcmp1_b32 s57, 0\n\t"
      "s_cselect_b32 s48, 24576, 0\n\t"
      "s_add_u32 s48, s48, s56\n\t"
      "v_mfma_f32_16x16x32_bf16 v[24:27], v[72:75], a[0:3], 0\n\t"
      "v_mfma_f32_16x16x32_bf16 v[24:27], v[88:91], a[4:7], v[24:27]\n\t"
      "v_mfma_f32_16x16x32_bf16 v[24:27], v[104:107], a[8:11], v[24:27]\n\t"
      "v_mfma_f32_16x16x32_bf16 v[24:27], v[120:123], a[12:15], v[24:27]\n\t"
      "v_mfma_f32_16x16x32_bf16 v[24:27], v[136:139], a[16:19], v[24:27]\n\t"
      "v_mfma_f32_16x16x32_bf16 v[24:27], v[152:155], a[20:23], v[24:27]\n\t"
      "s_waitcnt vmcnt(12)\n\t"
      "s_waitcnt lgkmcnt(0)\n\t"
      "s_barrier\n\t"
      "v_mfma_f32_16x16x32_bf16 v[28:31], v[76:79], a[0:3], 0\n\t"
      "ds_read_b128 a[48:51], v8 offset:12544\n\t"
      "v_mfma_f32_16x16x32_bf16 v[28:31], v[92:95], a[4:7], v[28:31]\n\t"
      "ds_read_b128 a[52:55], v60 offset:13568\n\t"
      "v_mfma_f32_16x16x32_bf16 v[28:31], v[108:111], a[8:11], v[28:31]\n\t"
      "ds_read_b128 a[56:59], v61 offset:14592\n\t"
      "v_mfma_f32_16x16x32_bf16 v[28:31], v[124:127], a[12:15], v[28:31]\n\t"
      "v_mfma_f32_16x16x32_bf16 v[28:31], v[140:143], a[16:19], v[28:31]\n\t"
      "v_mfma_f32_16x16x32_bf16 v[28:31], v[156:159], a[20:23], v[28:31]\n\t"
      "v_mfma_f32_16x16x32_bf16 v[32:35], a[60:63], a[0:3], 0\n\t"
      "v_mfma_f32_16x16x32_bf16 v[32:35], a[68:71], a[4:7], v[32:35]\n\t"
      "s_waitcnt lgkmcnt(2)\n\t"
      "v_mfma_f32_16x16x32_bf16 v[32:35], a[76:79], a[8:11], v[32:35]\n\t"
      "buffer_store_dwordx4 a[48:51], v12, s[44:47], s49 offen sc1\n\t"
      "v_mfma_f32_16x16x32_bf16 v[32:35], a[84:87], a[12:15], v[32:35]\n\t"
      "v_mfma_f32_16x16x32_bf16 v[32:35], a[92:95], a[16:19], v[32:35]\n\t"
      "v_mfma_f32_16x16x32_bf16 v[32:35], a[100:103], a[20:23], v[32:35]\n\t"
      "s_add_u32 m0, s18, s50\n\t"
      "v_mfma_f32_16x16x32_bf16 v[36:39], a[64:67], a[0:3], 0\n\t"
      "buffer_load_dwordx4 v2, s[40:43], s48 offen nt lds\n\t"
      "v_mfma_f32_16x16x32_bf16 v[36:39], a[72:75], a[4:7], v[36:39]\n\t"
      "v_mfma_f32_16x16x32_bf16 v[36:39], a[80:83], a[8:11], v[36:39]\n\t"
      "v_mfma_f32_16x16x32_bf16 v[36:39], a[88:91], a[12:15], v[36:39]\n\t"
      "s_waitcnt lgkmcnt(1)\n\t"
      "v_mfma_f32_16x16x32_bf16 v[36:39], a[96:99], a[16:19], v[36:39]\n\t"
      "buffer_store_dwordx4 a[52:55], v13, s[44:47], s49 offen offset:1024 sc1\n\t"
      "v_mfma_f32_16x16x32_bf16 v[36:39], a[104:107], a[20:23], v[36:39]\n\t"
      "v_mfma_f32_16x16x32_bf16 v[16:19], v[160:163], a[24:27], v[16:19]\n\t"
      "ds_read_b128 a[0:3], v52 offset:0\n\t"
      "v_mfma_f32_16x16x32_bf16 v[16:19], v[176:179], a[28:31], v[16:19]\n\t"
      "ds_read_b128 a[4:7], v53 offset:0\n\t"
      "v_mfma_f32_16x16x32_bf16 v[16:19], v[192:195], a[32:35], v[16:19]\n\t"
      "ds_read_b128 a[8:11], v54 offset:0\n\t"
      "v_mfma_f32_16x16x32_bf16 v[16:19], v[208:211], a[36:39], v[16:19]\n\t"
      "ds_read_b128 a[12:15], v55 offset:0\n\t"
      "v_mfma_f32_16x16x32_bf16 v[16:19], v[224:227], a[40:43], v[16:19]\n\t"
      "ds_read_b128 a[16:19], v52 offset:256\n\t"
      "v_mfma_f32_16x16x32_bf16 v[16:19], v[240:243], a[44:47], v[16:19]\n\t"
      "ds_read_b128 a[20:23], v53 offset:256\n\t"
      "v_mfma_f32_16x16x32_bf16 v[20:23], v[164:167], a[24:27], v[20:23]\n\t"
      "s_add_u32 s51, s51, 12288\n\t"
      "s_cmp_eq_u32 s51, 160256\n\t"
      "s_cselect_b32 s51, 25088, s51\n\t"
      "v_mfma_f32_16x16x32_bf16 v[20:23], v[180:183], a[28:31], v[20:23]\n\t"
      "s_waitcnt lgkmcnt(6)\n\t"
      "v_mfma_f32_16x16x32_bf16 v[20:23], v[196:199], a[32:35], v[20:23]\n\t"
      "buffer_store_dwordx4 a[56:59], v14, s[44:47], s49 offen offset:2048 sc1\n\t"
      "v_mfma_f32_16x16x32_bf16 v[20:23], v[212:215], a[36:39], v[20:23]\n\t"
      "v_mfma_f32_16x16x32_bf16 v[20:23], v[228:231], a[40:43], v[20:23]\n\t"
      "s_waitcnt lgkmcnt(0)\n\t"
      "v_mfma_f32_16x16x32_bf16 v[20:23], v[244:247], a[44:47], v[20:23]\n\t"
      "v_mfma_f32_16x16x32_bf16 v[24:27], v[168:171], a[24:27], v[24:27]\n\t"
      "v_mfma_f32_16x16x32_bf16 v[24:27], v[184:187], a[28:31], v[24:27]\n\t"
      "v_cvt_pk_bf16_f32 v40, v16, v17\n\t"
      "v_cvt_pk_bf16_f32 v41, v18, v19\n\t"
      "v_mfma_f32_16x16x32_bf16 v[24:27], v[200:203], a[32:35], v[24:27]\n\t"
      "ds_write_b64 v0, v[40:41] offset:0\n\t"
      "v_mfma_f32_16x16x32_bf16 v[24:27], v[216:219], a[36:39], v[24:27]\n\t"
      "buffer_load_dwordx4 v3, s[40:43], s48 offen offset:1024 nt lds\n\t"
      "v_mfma_f32_16x16x32_bf16 v[24:27], v[232:235], a[40:43], v[24:27]\n\t"
      "s_add_u32 s58, s58, 786432\n\t"
      "s_lshr_b32 s59, s59, 1\n\t"
      "v_mfma_f32_16x16x32_bf16 v[24:27], v[248:251], a[44:47], v[24:27]\n\t"
      "s_bitcmp1_b32 s59, 0\n\t"
      "s_cselect_b32 s49, 24576, 0\n\t"
      "s_add_u32 s49, s49, s58\n\t"
      "v_mfma_f32_16x16x32_bf16 v[28:31], v[172:175], a[24:27], v[28:31]\n\t"
      "v_mfma_f32_16x16x32_bf16 v[28:31], v[188:191], a[28:31], v[28:31]\n\t"
      "v_cvt_pk_bf16_f32 v42, v20, v21\n\t"
      "v_cvt_pk_bf16_f32 v43, v22, v23\n\t"
      "v_mfma_f32_16x16x32_bf16 v[28:31], v[204:207], a[32:35], v[28:31]\n\t"
      "ds_write_b64 v0, v[42:43] offset:32\n\t"
      "v_mfma_f32_16x16x32_bf16 v[28:31], v[220:223], a[36:39], v[28:31]\n\t"
      "v_mfma_f32_16x16x32_bf16 v[28:31], v[236:239], a[40:43], v[28:31]\n\t"
      "v_mfma_f32_16x16x32_bf16 v[28:31], v[252:255], a[44:47], v[28:31]\n\t"
      "v_mfma_f32_16x16x32_bf16 v[32:35], a[108:111], a[24:27], v[32:35]\n\t"
      "v_mfma_f32_16x16x32_bf16 v[32:35], a[116:119], a[28:31], v[32:35]\n\t"
      "v_cvt_pk_bf16_f32 v44, v24, v25\n\t"
      "v_cvt_pk_bf16_f32 v45, v26, v27\n\t"
      "v_mfma_f32_16x16x32_bf16 v[32:35], a[124:127], a[32:35], v[32:35]\n\t"
      "ds_write_b64 v0, v[44:45] offset:64\n\t"
      "v_mfma_f32_16x16x32_bf16 v[32:35], a[132:135], a[36:39], v[32:35]\n\t"
      "buffer_load_dwordx4 v4, s[40:43], s48 offen offset:2048 nt lds\n\t"
      "v_mfma_f32_16x16x32_bf16 v[32:35], a[140:143], a[40:43], v[32:35]\n\t"
      "s_add_u32 s50, s50, 12288\n\t"
      "v_mfma_f32_16x16x32_bf16 v[32:35], a[148:151], a[44:47], v[32:35]\n\t"
      "v_cvt_pk_bf16_f32 v46, v28, v29\n\t"
      "v_cvt_pk_bf16_f32 v47, v30, v31\n\t"
      "v_mfma_f32_16x16x32_bf16 v[36:39], a[112:115], a[24:27], v[36:39]\n\t"
      "ds_write_b64 v0, v[46:47] offset:96\n\t"
      "v_mfma_f32_16x16x32_bf16 v[36:39], a[120:123], a[28:31], v[36:39]\n\t"
      "s_cmp_eq_u32 s50, 160256\n\t"
      "s_cselect_b32 s50, 25088, s50\n\t"
      "v_mfma_f32_16x16x32_bf16 v[36:39], a[128:131], a[32:35], v[36:39]\n\t"
      "v_mfma_f32_16x16x32_bf16 v[36:39], a[136:139], a[36:39], v[36:39]\n\t"
      "v_mfma_f32_16x16x32_bf16 v[36:39], a[144:147], a[40:43], v[36:39]\n\t"
      "v_cvt_pk_bf16_f32 v48, v32, v33\n\t"
      "v_cvt_pk_bf16_f32 v49, v34, v35\n\t"
      "v_mfma_f32_16x16x32_bf16 v[36:39], a[152:155], a[44:47], v[36:39]\n\t"
      "ds_write_b64 v0, v[48:49] offset:128\n\t"
      ".Lmid3_%=:\n\t"
      "v_mfma_f32_16x16x32_bf16 v[16:19], v[64:67], a[0:3], 0\n\t"
      "ds_read_b128 a[24:27], v54 offset:256\n\t"
      "v_mfma_f32_16x16x32_bf16 v[16:19], v[80:83], a[4:7], v[16:19]\n\t"
      "ds_read_b128 a[28:31], v55 offset:256\n\t"
      "v_mfma_f32_16x16x32_bf16 v[16:19], v[96:99], a[8:11], v[16:19]\n\t"
      "ds_read_b128 a[32:35], v52 offset:512\n\t"
      "v_mfma_f32_16x16x32_bf16 v[16:19], v[112:115], a[12:15], v[16:19]\n\t"
      "ds_read_b128 a[36:39], v53 offset:512\n\t"
      "v_mfma_f32_16x16x32_bf16 v[16:19], v[128:131], a[16:19], v[16:19]\n\t"
      "ds_read_b128 a[40:43], v54 offset:512\n\t"
      "v_mfma_f32_16x16x32_bf16 v[16:19], v[144:147], a[20:23], v[16:19]\n\t"
      "ds_read_b128 a[44:47], v55 offset:512\n\t"
      "v_mfma_f32_16x16x32_bf16 v[20:23], v[68:71], a[0:3], 0\n\t"
      "v_cvt_pk_bf16_f32 v50, v36, v37\n\t"
      "v_cvt_pk_bf16_f32 v51, v38, v39\n\t"
      "v_mfma_f32_16x16x32_bf16 v[20:23], v[84:87], a[4:7], v[20:23]\n\t"
      "ds_write_b64 v0, v[50:51] offset:160\n\t"
      "v_mfma_f32_16x16x32_bf16 v[20:23], v[100:103], a[8:11], v[20:23]\n\t"
      "v_add_u32 v56, s51, v15\n\t"
      "v_xor_b32 v57, 64, v56\n\t"
      "v_mfma_f32_16x16x32_bf16 v[20:23], v[116:119], a[12:15], v[20:23]\n\t"
      "v_xor_b32 v58, 128, v56\n\t"
      "v_xor_b32 v59, 192, v56\n\t"
      "v_mfma_f32_16x16x32_bf16 v[20:23], v[132:135], a[16:19], v[20:23]\n\t"
      "v_mfma_f32_16x16x32_bf16 v[20:23], v[148:151], a[20:23], v[20:23]\n\t"
      "v_mfma_f32_16x16x32_bf16 v[24:27], v[72:75], a[0:3], 0\n\t"
      "v_mfma_f32_16x16x32_bf16 v[24:27], v[88:91], a[4:7], v[24:27]\n\t"
      "v_mfma_f32_16x16x32_bf16 v[24:27], v[104:107], a[8:11], v[24:27]\n\t"
      "v_mfma_f32_16x16x32_bf16 v[24:27], v[120:123], a[12:15], v[24:27]\n\t"
      "v_mfma_f32_16x16x32_bf16 v[24:27], v[136:139], a[16:19], v[24:27]\n\t"
      "v_mfma_f32_16x16x32_bf16 v[24:27], v[152:155], a[20:23], v[24:27]\n\t"
      "s_waitcnt vmcnt(12)\n\t"
      "s_waitcnt lgkmcnt(0)\n\t"
      "s_barrier\n\t"
      "v_mfma_f32_16x16x32_bf16 v[28:31], v[76:79], a[0:3], 0\n\t"
      "ds_read_b128 a[48:51], v8 offset:0\n\t"
      "v_mfma_f32_16x16x32_bf16 v[28:31], v[92:95], a[4:7], v[28:31]\n\t"
      "ds_read_b128 a[52:55], v60 offset:1024\n\t"
      "v_mfma_f32_16x16x32_bf16 v[28:31], v[108:111], a[8:11], v[28:31]\n\t"
      "ds_read_b128 a[56:59], v61 offset:2048\n\t"
      "v_mfma_f32_16x16x32_bf16 v[28:31], v[124:127], a[12:15], v[28:31]\n\t"
      "v_mfma_f32_16x16x32_bf16 v[28:31], v[140:143], a[16:19], v[28:31]\n\t"
      "v_mfma_f32_16x16x32_bf16 v[28:31], v[156:159], a[20:23], v[28:31]\n\t"
      "v_mfma_f32_16x16x32_bf16 v[32:35], a[60:63], a[0:3], 0\n\t"
      "v_mfma_f32_16x16x32_bf16 v[32:35], a[68:71], a[4:7], v[32:35]\n\t"
      "s_waitcnt lgkmcnt(2)\n\t"
      "v_mfma_f32_16x16x32_bf16 v[32:35], a[76:79], a[8:11], v[32:35]\n\t"
      "buffer_store_dwordx4 a[48:51], v9, s[44:47], s49 offen sc1\n\t"
      "v_mfma_f32_16x16x32_bf16 v[32:35], a[84:87], a[12:15], v[32:35]\n\t"
      "v_mfma_f32_16x16x32_bf16 v[32:35], a[92:95], a[16:19], v[32:35]\n\t"
      "v_mfma_f32_16x16x32_bf16 v[32:35], a[100:103], a[20:23], v[32:35]\n\t"
      "s_add_u32 m0, s18, s50\n\t"
      "v_mfma_f32_16x16x32_bf16 v[36:39], a[64:67], a[0:3], 0\n\t"
      "buffer_load_dwordx4 v5, s[40:43], s48 offen nt lds\n\t"
      "v_mfma_f32_16x16x32_bf16 v[36:39], a[72:75], a[4:7], v[36:39]\n\t"
      "v_mfma_f32_16x16x32_bf16 v[36:39], a[80:83], a[8:11], v[36:39]\n\t"
      "v_mfma_f32_16x16x32_bf16 v[36:39], a[88:91], a[12:15], v[36:39]\n\t"
      "s_waitcnt lgkmcnt(1)\n\t"
      "v_mfma_f32_16x16x32_bf16 v[36:39], a[96:99], a[16:19], v[36:39]\n\t"
      "buffer_store_dwordx4 a[52:55], v10, s[44:47], s49 offen offset:1024 sc1\n\t"
      "v_mfma_f32_16x16x32_bf16 v[36:39], a[104:107], a[20:23], v[36:39]\n\t"
      "v_mfma_f32_16x16x32_bf16 v[16:19], v[160:163], a[24:27], v[16:19]\n\t"
      "ds_read_b128 a[0:3], v56 offset:0\n\t"
      "v_mfma_f32_16x16x32_bf16 v[16:19], v[176:179], a[28:31], v[16:19]\n\t"
      "ds_read_b128 a[4:7], v57 offset:0\n\t"
      "v_mfma_f32_16x16x32_bf16 v[16:19], v[192:195], a[32:35], v[16:19]\n\t"
      "ds_read_b128 a[8:11], v58 offset:0\n\t"
      "v_mfma_f32_16x16x32_bf16 v[16:19], v[208:211], a[36:39], v[16:19]\n\t"
      "ds_read_b128 a[12:15], v59 offset:0\n\t"
      "v_mfma_f32_16x16x32_bf16 v[16:19], v[224:227], a[40:43], v[16:19]\n\t"
      "ds_read_b128 a[16:19], v56 offset:256\n\t"
      "v_mfma_f32_16x16x32_bf16 v[16:19], v[240:243], a[44:47], v[16:19]\n\t"
      "ds_read_b128 a[20:23], v57 offset:256\n\t"
      "v_mfma_f32_16x16x32_bf16 v[20:23], v[164:167], a[24:27], v[20:23]\n\t"
      "s_add_u32 s51, s51, 12288\n\t"
      "s_cmp_eq_u32 s51, 160256\n\t"
      "s_cselect_b32 s51, 25088, s51\n\t"
      "v_mfma_f32_16x16x32_bf16 v[20:23], v[180:183], a[28:31], v[20:23]\n\t"
      "s_waitcnt lgkmcnt(6)\n\t"
      "v_mfma_f32_16x16x32_bf16 v[20:23], v[196:199], a[32:35], v[20:23]\n\t"
      "buffer_store_dwordx4 a[56:59], v11, s[44:47], s49 offen offset:2048 sc1\n\t"
      "v_mfma_f32_16x16x32_bf16 v[20:23], v[212:215], a[36:39], v[20:23]\n\t"
      "v_mfma_f32_16x16x32_bf16 v[20:23], v[228:231], a[40:43], v[20:23]\n\t"
      "s_waitcnt lgkmcnt(0)\n\t"
      "v_mfma_f32_16x16x32_bf16 v[20:23], v[244:247], a[44:47], v[20:23]\n\t"
      "v_mfma_f32_16x16x32_bf16 v[24:27], v[168:171], a[24:27], v[24:27]\n\t"
      "v_mfma_f32_16x16x32_bf16 v[24:27], v[184:187], a[28:31], v[24:27]\n\t"
      "v_cvt_pk_bf16_f32 v40, v16, v17\n\t"
      "v_cvt_pk_bf16_f32 v41, v18, v19\n\t"
      "v_mfma_f32_16x16x32_bf16 v[24:27], v[200:203], a[32:35], v[24:27]\n\t"
      "ds_write_b64 v0, v[40:41] offset:12544\n\t"
      "v_mfma_f32_16x16x32_bf16 v[24:27], v[216:219], a[36:39], v[24:27]\n\t"
      "buffer_load_dwordx4 v6, s[40:43], s48 offen offset:1024 nt lds\n\t"
      "v_mfma_f32_16x16x32_bf16 v[24:27], v[232:235], a[40:43], v[24:27]\n\t"
      "v_mfma_f32_16x16x32_bf16 v[24:27], v[248:251], a[44:47], v[24:27]\n\t"
      "v_mfma_f32_16x16x32_bf16 v[28:31], v[172:175], a[24:27], v[28:31]\n\t"
      "v_mfma_f32_16x16x32_bf16 v[28:31], v[188:191], a[28:31], v[28:31]\n\t"
      "v_cvt_pk_bf16_f32 v42, v20, v21\n\t"
      "v_cvt_pk_bf16_f32 v43, v22, v23\n\t"
      "v_mfma_f32_16x16x32_bf16 v[28:31], v[204:207], a[32:35], v[28:31]\n\t"
      "ds_write_b64 v0, v[42:43] offset:12576\n\t"
      "v_mfma_f32_16x16x32_bf16 v[28:31], v[220:223], a[36:39], v[28:31]\n\t"
      "v_mfma_f32_16x16x32_bf16 v[28:31], v[236:239], a[40:43], v[28:31]\n\t"
      "v_mfma_f32_16x16x32_bf16 v[28:31], v[252:255], a[44:47], v[28:31]\n\t"
      "v_mfma_f32_16x16x32_bf16 v[32:35], a[108:111], a[24:27], v[32:35]\n\t"
      "v_mfma_f32_16x16x32_bf16 v[32:35], a[116:119], a[28:31], v[32:35]\n\t"
      "v_cvt_pk_bf16_f32 v44, v24, v25\n\t"
      "v_cvt_pk_bf16_f32 v45, v26, v27\n\t"
      "v_mfma_f32_16x16x32_bf16 v[32:35], a[124:127], a[32:35], v[32:35]\n\t"
      "ds_write_b64 v0, v[44:45] offset:12608\n\t"
      "v_mfma_f32_16x16x32_bf16 v[32:35], a[132:135], a[36:39], v[32:35]\n\t"
      "buffer_load_dwordx4 v7, s[40:43], s48 offen offset:2048 nt lds\n\t"
      "v_mfma_f32_16x16x32_bf16 v[32:35], a[140:143], a[40:43], v[32:35]\n\t"
      "s_add_u32 s50, s50, 12288\n\t"
      "v_mfma_f32_16x16x32_bf16 v[32:35], a[148:151], a[44:47], v[32:35]\n\t"
      "v_cvt_pk_bf16_f32 v46, v28, v29\n\t"
      "v_cvt_pk_bf16_f32 v47, v30, v31\n\t"
      "v_mfma_f32_16x16x32_bf16 v[36:39], a[112:115], a[24:27], v[36:39]\n\t"
      "ds_write_b64 v0, v[46:47] offset:12640\n\t"
      "v_mfma_f32_16x16x32_bf16 v[36:39], a[120:123], a[28:31], v[36:39]\n\t"
      "s_cmp_eq_u32 s50, 160256\n\t"
      "s_cselect_b32 s50, 25088, s50\n\t"
      "v_mfma_f32_16x16x32_bf16 v[36:39], a[128:131], a[32:35], v[36:39]\n\t"
      "s_sub_u32 s52, s52, 1\n\t"
      "s_cmp_lg_u32 s52, 0\n\t"
      "v_mfma_f32_16x16x32_bf16 v[36:39], a[136:139], a[36:39], v[36:39]\n\t"
      "v_mfma_f32_16x16x32_bf16 v[36:39], a[144:147], a[40:43], v[36:39]\n\t"
      "v_cvt_pk_bf16_f32 v48, v32, v33\n\t"
      "v_cvt_pk_bf16_f32 v49, v34, v35\n\t"
      "v_mfma_f32_16x16x32_bf16 v[36:39], a[152:155], a[44:47], v[36:39]\n\t"
      "ds_write_b64 v0, v[48:49] offset:12672\n\t"
      "s_cbranch_scc1 .Lloop3_%=\n\t"
      "s_cmp_eq_u32 s48, 113221632\n\t"
      "s_cbranch_scc1 .Lwrap3_%=\n\t"
      ".Lwrapped3_%=:\n\t"
      "s_mov_b32 s52, 2\n\t"
      ".Ldrain3_%=:\n\t"
      "v_mfma_f32_16x16x32_bf16 v[16:19], v[64:67], a[0:3], 0\n\t"
      "ds_read_b128 a[24:27], v58 offset:256\n\t"
      "v_mfma_f32_16x16x32_bf16 v[16:19], v[80:83], a[4:7], v[16:19]\n\t"
      "ds_read_b128 a[28:31], v59 offset:256\n\t"
      "v_mfma_f32_16x16x32_bf16 v[16:19], v[96:99], a[8:11], v[16:19]\n\t"
      "ds_read_b128 a[32:35], v56 offset:512\n\t"
      "v_mfma_f32_16x16x32_bf16 v[16:19], v[112:115], a[12:15], v[16:19]\n\t"
      "ds_read_b128 a[36:39], v57 offset:512\n\t"
      "v_mfma_f32_16x16x32_bf16 v[16:19], v[128:131], a[16:19], v[16:19]\n\t"
      "ds_read_b128 a[40:43], v58 offset:512\n\t"
      "v_mfma_f32_16x16x32_bf16 v[16:19], v[144:147], a[20:23], v[16:19]\n\t"
      "ds_read_b128 a[44:47], v59 offset:512\n\t"
      "v_mfma_f32_16x16x32_bf16 v[20:23], v[68:71], a[0:3], 0\n\t"
      "v_cvt_pk_bf16_f32 v50, v36, v37\n\t"
      "v_cvt_pk_bf16_f32 v51, v38, v39\n\t"
      "v_mfma_f32_16x16x32_bf16 v[20:23], v[84:87], a[4:7], v[20:23]\n\t"
      "ds_write_b64 v0, v[50:51] offset:12704\n\t"
      "v_mfma_f32_16x16x32_bf16 v[20:23], v[100:103], a[8:11], v[20:23]\n\t"
      "v_add_u32 v52, s51, v15\n\t"
      "v_xor_b32 v53, 64, v52\n\t"
      "v_mfma_f32_16x16x32_bf16 v[20:23], v[116:119], a[12:15], v[20:23]\n\t"
      "v_xor_b32 v54, 128, v52\n\t"
      "v_xor_b32 v55, 192, v52\n\t"
      "v_mfma_f32_16x16x32_bf16 v[20:23], v[132:135], a[16:19], v[20:23]\n\t"
      "v_mfma_f32_16x16x32_bf16 v[20:23], v[148:151], a[20:23], v[20:23]\n\t"
      "v_mfma_f32_16x16x32_bf16 v[24:27], v[72:75], a[0:3], 0\n\t"
      "v_mfma_f32_16x16x32_bf16 v[24:27], v[88:91], a[4:7], v[24:27]\n\t"
      "v_mfma_f32_16x16x32_bf16 v[24:27], v[104:107], a[8:11], v[24:27]\n\t"
      "v_mfma_f32_16x16x32_bf16 v[24:27], v[120:123], a[12:15], v[24:27]\n\t"
      "v_mfma_f32_16x16x32_bf16 v[24:27], v[136:139], a[16:19], v[24:27]\n\t"
      "v_mfma_f32_16x16x32_bf16 v[24:27], v[152:155], a[20:23], v[24:27]\n\t"
      "s_waitcnt vmcnt(6)\n\t"
      "s_waitcnt lgkmcnt(0)\n\t"
      "s_barrier\n\t"
      "v_mfma_f32_16x16x32_bf16 v[28:31], v[76:79], a[0:3], 0\n\t"
      "ds_read_b128 a[48:51], v8 offset:12544\n\t"
      "v_mfma_f32_16x16x32_bf16 v[28:31], v[92:95], a[4:7], v[28:31]\n\t"
      "ds_read_b128 a[52:55], v60 offset:13568\n\t"
      "v_mfma_f32_16x16x32_bf16 v[28:31], v[108:111], a[8:11], v[28:31]\n\t"
      "ds_read_b128 a[56:59], v61 offset:14592\n\t"
      "v_mfma_f32_16x16x32_bf16 v[28:31], v[124:127], a[12:15], v[28:31]\n\t"
      "v_mfma_f32_16x16x32_bf16 v[28:31], v[140:143], a[16:19], v[28:31]\n\t"
      "v_mfma_f32_16x16x32_bf16 v[28:31], v[156:159], a[20:23], v[28:31]\n\t"
      "v_mfma_f32_16x16x32_bf16 v[32:35], a[60:63], a[0:3], 0\n\t"
      "v_mfma_f32_16x16x32_bf16 v[32:35], a[68:71], a[4:7], v[32:35]\n\t"
      "s_waitcnt lgkmcnt(2)\n\t"
      "v_mfma_f32_16x16x32_bf16 v[32:35], a[76:79], a[8:11], v[32:35]\n\t"
      "buffer_store_dwordx4 a[48:51], v12, s[44:47], s49 offen sc1\n\t"
      "v_mfma_f32_16x16x32_bf16 v[32:35], a[84:87], a[12:15], v[32:35]\n\t"
      "v_mfma_f32_16x16x32_bf16 v[32:35], a[92:95], a[16:19], v[32:35]\n\t"
      "v_mfma_f32_16x16x32_bf16 v[32:35], a[100:103], a[20:23], v[32:35]\n\t"
      "v_mfma_f32_16x16x32_bf16 v[36:39], a[64:67], a[0:3], 0\n\t"
      "v_mfma_f32_16x16x32_bf16 v[36:39], a[72:75], a[4:7], v[36:39]\n\t"
      "v_mfma_f32_16x16x32_bf16 v[36:39], a[80:83], a[8:11], v[36:39]\n\t"
      "v_mfma_f32_16x16x32_bf16 v[36:39], a[88:91], a[12:15], v[36:39]\n\t"
      "s_waitcnt lgkmcnt(1)\n\t"
      "v_mfma_f32_16x16x32_bf16 v[36:39], a[96:99], a[16:19], v[36:39]\n\t"
      "buffer_store_dwordx4 a[52:55], v13, s[44:47], s49 offen offset:1024 sc1\n\t"
      "v_mfma_f32_16x16x32_bf16 v[36:39], a[104:107], a[20:23], v[36:39]\n\t"
      "v_mfma_f32_16x16x32_bf16 v[16:19], v[160:163], a[24:27], v[16:19]\n\t"
      "ds_read_b128 a[0:3], v52 offset:0\n\t"
      "v_mfma_f32_16x16x32_bf16 v[16:19], v[176:179], a[28:31], v[16:19]\n\t"
      "ds_read_b128 a[4:7], v53 offset:0\n\t"
      "v_mfma_f32_16x16x32_bf16 v[16:19], v[192:195], a[32:35], v[16:19]\n\t"
      "ds_read_b128 a[8:11], v54 offset:0\n\t"
      "v_mfma_f32_16x16x32_bf16 v[16:19], v[208:211], a[36:39], v[16:19]\n\t"
      "ds_read_b128 a[12:15], v55 offset:0\n\t"
      "v_mfma_f32_16x16x32_bf16 v[16:19], v[224:227], a[40:43], v[16:19]\n\t"
      "ds_read_b128 a[16:19], v52 offset:256\n\t"
      "v_mfma_f32_16x16x32_bf16 v[16:19], v[240:243], a[44:47], v[16:19]\n\t"
      "ds_read_b128 a[20:23], v53 offset:256\n\t"
      "v_mfma_f32_16x16x32_bf16 v[20:23], v[164:167], a[24:27], v[20:23]\n\t"
      "s_add_u32 s51, s51, 12288\n\t"
      "s_cmp_eq_u32 s51, 160256\n\t"
      "s_cselect_b32 s51, 25088, s51\n\t"
      "v_mfma_f32_16x16x32_bf16 v[20:23], v[180:183], a[28:31], v[20:23]\n\t"
      "s_waitcnt lgkmcnt(6)\n\t"
      "v_mfma_f32_16x16x32_bf16 v[20:23], v[196:199], a[32:35], v[20:23]\n\t"
      "buffer_store_dwordx4 a[56:59], v14, s[44:47], s49 offen offset:2048 sc1\n\t"
      "v_mfma_f32_16x16x32_bf16 v[20:23], v[212:215], a[36:39], v[20:23]\n\t"
      "v_mfma_f32_16x16x32_bf16 v[20:23], v[228:231], a[40:43], v[20:23]\n\t"
      "s_waitcnt lgkmcnt(0)\n\t"
      "v_mfma_f32_16x16x32_bf16 v[20:23], v[244:247], a[44:47], v[20:23]\n\t"
      "v_mfma_f32_16x16x32_bf16 v[24:27], v[168:171], a[24:27], v[24:27]\n\t"
      "v_mfma_f32_16x16x32_bf16 v[24:27], v[184:187], a[28:31], v[24:27]\n\t"
      "v_cvt_pk_bf16_f32 v40, v16, v17\n\t"
      "v_cvt_pk_bf16_f32 v41, v18, v19\n\t"
      "v_mfma_f32_16x16x32_bf16 v[24:27], v[200:203], a[32:35], v[24:27]\n\t"
      "ds_write_b64 v0, v[40:41] offset:0\n\t"
      "v_mfma_f32_16x16x32_bf16 v[24:27], v[216:219], a[36:39], v[24:27]\n\t"
      "v_mfma_f32_16x16x32_bf16 v[24:27], v[232:235], a[40:43], v[24:27]\n\t"
      "s_add_u32 s58, s58, 786432\n\t"
      "s_lshr_b32 s59, s59, 1\n\t"
      "v_mfma_f32_16x16x32_bf16 v[24:27], v[248:251], a[44:47], v[24:27]\n\t"
      "s_bitcmp1_b32 s59, 0\n\t"
      "s_cselect_b32 s49, 24576, 0\n\t"
      "s_add_u32 s49, s49, s58\n\t"
      "v_mfma_f32_16x16x32_bf16 v[28:31], v[172:175], a[24:27], v[28:31]\n\t"
      "v_mfma_f32_16x16x32_bf16 v[28:31], v[188:191], a[28:31], v[28:31]\n\t"
      "v_cvt_pk_bf16_f32 v42, v20, v21\n\t"
      "v_cvt_pk_bf16_f32 v43, v22, v23\n\t"
      "v_mfma_f32_16x16x32_bf16 v[28:31], v[204:207], a[32:35], v[28:31]\n\t"
      "ds_write_b64 v0, v[42:43] offset:32\n\t"
      "v_mfma_f32_16x16x32_bf16 v[28:31], v[220:223], a[36:39], v[28:31]\n\t"
      "v_mfma_f32_16x16x32_bf16 v[28:31], v[236:239], a[40:43], v[28:31]\n\t"
      "v_mfma_f32_16x16x32_bf16 v[28:31], v[252:255], a[44:47], v[28:31]\n\t"
      "v_mfma_f32_16x16x32_bf16 v[32:35], a[108:111], a[24:27], v[32:35]\n\t"
      "v_mfma_f32_16x16x32_bf16 v[32:35], a[116:119], a[28:31], v[32:35]\n\t"
      "v_cvt_pk_bf16_f32 v44, v24, v25\n\t"
      "v_cvt_pk_bf16_f32 v45, v26, v27\n\t"
      "v_mfma_f32_16x16x32_bf16 v[32:35], a[124:127], a[32:35], v[32:35]\n\t"
      "ds_write_b64 v0, v[44:45] offset:64\n\t"
      "v_mfma_f32_16x16x32_bf16 v[32:35], a[132:135], a[36:39], v[32:35]\n\t"
      "v_mfma_f32_16x16x32_bf16 v[32:35], a[140:143], a[40:43], v[32:35]\n\t"
      "v_mfma_f32_16x16x32_bf16 v[32:35], a[148:151], a[44:47], v[32:35]\n\t"
      "v_cvt_pk_bf16_f32 v46, v28, v29\n\t"
      "v_cvt_pk_bf16_f32 v47, v30, v31\n\t"
      "v_mfma_f32_16x16x32_bf16 v[36:39], a[112:115], a[24:27], v[36:39]\n\t"
      "ds_write_b64 v0, v[46:47] offset:96\n\t"
      "v_mfma_f32_16x16x32_bf16 v[36:39], a[120:123], a[28:31], v[36:39]\n\t"
      "v_mfma_f32_16x16x32_bf16 v[36:39], a[128:131], a[32:35], v[36:39]\n\t"
      "v_mfma_f32_16x16x32_bf16 v[36:39], a[136:139], a[36:39], v[36:39]\n\t"
      "v_mfma_f32_16x16x32_bf16 v[36:39], a[144:147], a[40:43], v[36:39]\n\t"
      "v_cvt_pk_bf16_f32 v48, v32, v33\n\t"
      "v_cvt_pk_bf16_f32 v49, v34, v35\n\t"
      "v_mfma_f32_16x16x32_bf16 v[36:39], a[152:155], a[44:47], v[36:39]\n\t"
      "ds_write_b64 v0, v[48:49] offset:128\n\t"
      "v_mfma_f32_16x16x32_bf16 v[16:19], v[64:67], a[0:3], 0\n\t"
      "ds_read_b128 a[24:27], v54 offset:256\n\t"
      "v_mfma_f32_16x16x32_bf16 v[16:19], v[80:83], a[4:7], v[16:19]\n\t"
      "ds_read_b128 a[28:31], v55 offset:256\n\t"
      "v_mfma_f32_16x16x32_bf16 v[16:19], v[96:99], a[8:11], v[16:19]\n\t"
      "ds_read_b128 a[32:35], v52 offset:512\n\t"
      "v_mfma_f32_16x16x32_bf16 v[16:19], v[112:115], a[12:15], v[16:19]\n\t"
      "ds_read_b128 a[36:39], v53 offset:512\n\t"
      "v_mfma_f32_16x16x32_bf16 v[16:19], v[128:131], a[16:19], v[16:19]\n\t"
      "ds_read_b128 a[40:43], v54 offset:512\n\t"
      "v_mfma_f32_16x16x32_bf16 v[16:19], v[144:147], a[20:23], v[16:19]\n\t"
      "ds_read_b128 a[44:47], v55 offset:512\n\t"
      "v_mfma_f32_16x16x32_bf16 v[20:23], v[68:71], a[0:3], 0\n\t"
      "v_cvt_pk_bf16_f32 v50, v36, v37\n\t"
      "v_cvt_pk_bf16_f32 v51, v38, v39\n\t"
      "v_mfma_f32_16x16x32_bf16 v[20:23], v[84:87], a[4:7], v[20:23]\n\t"
      "ds_write_b64 v0, v[50:51] offset:160\n\t"
      "v_mfma_f32_16x16x32_bf16 v[20:23], v[100:103], a[8:11], v[20:23]\n\t"
      "v_add_u32 v56, s51, v15\n\t"
      "v_xor_b32 v57, 64, v56\n\t"
      "v_mfma_f32_16x16x32_bf16 v[20:23], v[116:119], a[12:15], v[20:23]\n\t"
      "v_xor_b32 v58, 128, v56\n\t"
      "v_xor_b32 v59, 192, v56\n\t"
      "v_mfma_f32_16x16x32_bf16 v[20:23], v[132:135], a[16:19], v[20:23]\n\t"
      "v_mfma_f32_16x16x32_bf16 v[20:23], v[148:151], a[20:23], v[20:23]\n\t"
      "v_mfma_f32_16x16x32_bf16 v[24:27], v[72:75], a[0:3], 0\n\t"
      "v_mfma_f32_16x16x32_bf16 v[24:27], v[88:91], a[4:7], v[24:27]\n\t"
      "v_mfma_f32_16x16x32_bf16 v[24:27], v[104:107], a[8:11], v[24:27]\n\t"
      "v_mfma_f32_16x16x32_bf16 v[24:27], v[120:123], a[12:15], v[24:27]\n\t"
      "v_mfma_f32_16x16x32_bf16 v[24:27], v[136:139], a[16:19], v[24:27]\n\t"
      "v_mfma_f32_16x16x32_bf16 v[24:27], v[152:155], a[20:23], v[24:27]\n\t"
      "s_waitcnt vmcnt(6)\n\t"
      "s_waitcnt lgkmcnt(0)\n\t"
      "s_barrier\n\t"
      "v_mfma_f32_16x16x32_bf16 v[28:31], v[76:79], a[0:3], 0\n\t"
      "ds_read_b128 a[48:51], v8 offset:0\n\t"
      "v_mfma_f32_16x16x32_bf16 v[28:31], v[92:95], a[4:7], v[28:31]\n\t"
      "ds_read_b128 a[52:55], v60 offset:1024\n\t"
      "v_mfma_f32_16x16x32_bf16 v[28:31], v[108:111], a[8:11], v[28:31]\n\t"
      "ds_read_b128 a[56:59], v61 offset:2048\n\t"
      "v_mfma_f32_16x16x32_bf16 v[28:31], v[124:127], a[12:15], v[28:31]\n\t"
      "v_mfma_f32_16x16x32_bf16 v[28:31], v[140:143], a[16:19], v[28:31]\n\t"
      "v_mfma_f32_16x16x32_bf16 v[28:31], v[156:159], a[20:23], v[28:31]\n\t"
      "v_mfma_f32_16x16x32_bf16 v[32:35], a[60:63], a[0:3], 0\n\t"
      "v_mfma_f32_16x16x32_bf16 v[32:35], a[68:71], a[4:7], v[32:35]\n\t"
      "s_waitcnt lgkmcnt(2)\n\t"
      "v_mfma_f32_16x16x32_bf16 v[32:35], a[76:79], a[8:11], v[32:35]\n\t"
      "buffer_store_dwordx4 a[48:51], v9, s[44:47], s49 offen sc1\n\t"
      "v_mfma_f32_16x16x32_bf16 v[32:35], a[84:87], a[12:15], v[32:35]\n\t"
      "v_mfma_f32_16x16x32_bf16 v[32:35], a[92:95], a[16:19], v[32:35]\n\t"
      "v_mfma_f32_16x16x32_bf16 v[32:35], a[100:103], a[20:23], v[32:35]\n\t"
      "v_mfma_f32_16x16x32_bf16 v[36:39], a[64:67], a[0:3], 0\n\t"
      "v_mfma_f32_16x16x32_bf16 v[36:39], a[72:75], a[4:7], v[36:39]\n\t"
      "v_mfma_f32_16x16x32_bf16 v[36:39], a[80:83], a[8:11], v[36:39]\n\t"
      "v_mfma_f32_16x16x32_bf16 v[36:39], a[88:91], a[12:15], v[36:39]\n\t"
      "s_waitcnt lgkmcnt(1)\n\t"
      "v_mfma_f32_16x16x32_bf16 v[36:39], a[96:99], a[16:19], v[36:39]\n\t"
      "buffer_store_dwordx4 a[52:55], v10, s[44:47], s49 offen offset:1024 sc1\n\t"
      "v_mfma_f32_16x16x32_bf16 v[36:39], a[104:107], a[20:23], v[36:39]\n\t"
      "v_mfma_f32_16x16x32_bf16 v[16:19], v[160:163], a[24:27], v[16:19]\n\t"
      "ds_read_b128 a[0:3], v56 offset:0\n\t"
      "v_mfma_f32_16x16x32_bf16 v[16:19], v[176:179], a[28:31], v[16:19]\n\t"
      "ds_read_b128 a[4:7], v57 offset:0\n\t"
      "v_mfma_f32_16x16x32_bf16 v[16:19], v[192:195], a[32:35], v[16:19]\n\t"
      "ds_read_b128 a[8:11], v58 offset:0\n\t"
      "v_mfma_f32_16x16x32_bf16 v[16:19], v[208:211], a[36:39], v[16:19]\n\t"
      "ds_read_b128 a[12:15], v59 offset:0\n\t"
      "v_mfma_f32_16x16x32_bf16 v[16:19], v[224:227], a[40:43], v[16:19]\n\t"
      "ds_read_b128 a[16:19], v56 offset:256\n\t"
      "v_mfma_f32_16x16x32_bf16 v[16:19], v[240:243], a[44:47], v[16:19]\n\t"
      "ds_read_b128 a[20:23], v57 offset:256\n\t"
      "v_mfma_f32_16x16x32_bf16 v[20:23], v[164:167], a[24:27], v[20:23]\n\t"
      "s_add_u32 s51, s51, 12288\n\t"
      "s_cmp_eq_u32 s51, 160256\n\t"
      "s_cselect_b32 s51, 25088, s51\n\t"
      "v_mfma_f32_16x16x32_bf16 v[20:23], v[180:183], a[28:31], v[20:23]\n\t"
      "s_waitcnt lgkmcnt(6)\n\t"
      "v_mfma_f32_16x16x32_bf16 v[20:23], v[196:199], a[32:35], v[20:23]\n\t"
      "buffer_store_dwordx4 a[56:59], v11, s[44:47], s49 offen offset:2048 sc1\n\t"
      "v_mfma_f32_16x16x32_bf16 v[20:23], v[212:215], a[36:39], v[20:23]\n\t"
      "v_mfma_f32_16x16x32_bf16 v[20:23], v[228:231], a[40:43], v[20:23]\n\t"
      "s_waitcnt lgkmcnt(0)\n\t"
      "v_mfma_f32_16x16x32_bf16 v[20:23], v[244:247], a[44:47], v[20:23]\n\t"
      "v_mfma_f32_16x16x32_bf16 v[24:27], v[168:171], a[24:27], v[24:27]\n\t"
      "v_mfma_f32_16x16x32_bf16 v[24:27], v[184:187], a[28:31], v[24:27]\n\t"
      "v_cvt_pk_bf16_f32 v40, v16, v17\n\t"
      "v_cvt_pk_bf16_f32 v41, v18, v19\n\t"
      "v_mfma_f32_16x16x32_bf16 v[24:27], v[200:203], a[32:35], v[24:27]\n\t"
      "ds_write_b64 v0, v[40:41] offset:12544\n\t"
      "v_mfma_f32_16x16x32_bf16 v[24:27], v[216:219], a[36:39], v[24:27]\n\t"
      "v_mfma_f32_16x16x32_bf16 v[24:27], v[232:235], a[40:43], v[24:27]\n\t"
      "v_mfma_f32_16x16x32_bf16 v[24:27], v[248:251], a[44:47], v[24:27]\n\t"
      "v_mfma_f32_16x16x32_bf16 v[28:31], v[172:175], a[24:27], v[28:31]\n\t"
      "v_mfma_f32_16x16x32_bf16 v[28:31], v[188:191], a[28:31], v[28:31]\n\t"
      "v_cvt_pk_bf16_f32 v42, v20, v21\n\t"
      "v_cvt_pk_bf16_f32 v43, v22, v23\n\t"
      "v_mfma_f32_16x16x32_bf16 v[28:31], v[204:207], a[32:35], v[28:31]\n\t"
      "ds_write_b64 v0, v[42:43] offset:12576\n\t"
      "v_mfma_f32_16x16x32_bf16 v[28:31], v[220:223], a[36:39], v[28:31]\n\t"
      "v_mfma_f32_16x16x32_bf16 v[28:31], v[236:239], a[40:43], v[28:31]\n\t"
      "v_mfma_f32_16x16x32_bf16 v[28:31], v[252:255], a[44:47], v[28:31]\n\t"
      "v_mfma_f32_16x16x32_bf16 v[32:35], a[108:111], a[24:27], v[32:35]\n\t"
      "v_mfma_f32_16x16x32_bf16 v[32:35], a[116:119], a[28:31], v[32:35]\n\t"
      "v_cvt_pk_bf16_f32 v44, v24, v25\n\t"
      "v_cvt_pk_bf16_f32 v45, v26, v27\n\t"
      "v_mfma_f32_16x16x32_bf16 v[32:35], a[124:127], a[32:35], v[32:35]\n\t"
      "ds_write_b64 v0, v[44:45] offset:12608\n\t"
      "v_mfma_f32_16x16x32_bf16 v[32:35], a[132:135], a[36:39], v[32:35]\n\t"
      "v_mfma_f32_16x16x32_bf16 v[32:35], a[140:143], a[40:43], v[32:35]\n\t"
      "v_mfma_f32_16x16x32_bf16 v[32:35], a[148:151], a[44:47], v[32:35]\n\t"
      "v_cvt_pk_bf16_f32 v46, v28, v29\n\t"
      "v_cvt_pk_bf16_f32 v47, v30, v31\n\t"
      "v_mfma_f32_16x16x32_bf16 v[36:39], a[112:115], a[24:27], v[36:39]\n\t"
      "ds_write_b64 v0, v[46:47] offset:12640\n\t"
      "v_mfma_f32_16x16x32_bf16 v[36:39], a[120:123], a[28:31], v[36:39]\n\t"
      "v_mfma_f32_16x16x32_bf16 v[36:39], a[128:131], a[32:35], v[36:39]\n\t"
      "s_sub_u32 s52, s52, 1\n\t"
      "s_cmp_lg_u32 s52, 0\n\t"
      "v_mfma_f32_16x16x32_bf16 v[36:39], a[136:139], a[36:39], v[36:39]\n\t"
      "v_mfma_f32_16x16x32_bf16 v[36:39], a[144:147], a[40:43], v[36:39]\n\t"
      "v_cvt_pk_bf16_f32 v48, v32, v33\n\t"
      "v_cvt_pk_bf16_f32 v49, v34, v35\n\t"
      "v_mfma_f32_16x16x32_bf16 v[36:39], a[152:155], a[44:47], v[36:39]\n\t"
      "ds_write_b64 v0, v[48:49] offset:12672\n\t"
      "s_cbranch_scc1 .Ldrain3_%=\n\t"
      ".Ltail_%=:\n\t"
      "s_nop 6\n\t"
      "v_cvt_pk_bf16_f32 v50, v36, v37\n\t"
      "v_cvt_pk_bf16_f32 v51, v38, v39\n\t"
      "ds_write_b64 v0, v[50:51] offset:12704\n\t"
      "s_cmp_eq_u32 s49, 113221632\n\t"
      "s_cbranch_scc1 .Lwrapo_%=\n\t"
      ".Lwrappedo_%=:\n\t"
      "s_waitcnt lgkmcnt(0)\n\t"
      "s_barrier\n\t"
      "ds_read_b128 a[48:51], v8 offset:12544\n\t"
      "ds_read_b128 a[52:55], v60 offset:13568\n\t"
      "ds_read_b128 a[56:59], v61 offset:14592\n\t"
      "s_waitcnt lgkmcnt(2)\n\t"
      "buffer_store_dwordx4 a[48:51], v12, s[44:47], s49 offen sc1\n\t"
      "s_waitcnt lgkmcnt(1)\n\t"
      "buffer_store_dwordx4 a[52:55], v13, s[44:47], s49 offen offset:1024 sc1\n\t"
      "s_waitcnt lgkmcnt(0)\n\t"
      "buffer_store_dwordx4 a[56:59], v14, s[44:47], s49 offen offset:2048 sc1\n\t"
      "s_cmp_lt_i32 s91, 0\n\t"
      "s_cselect_b32 s30, -1, 0\n\t"
      "s_xor_b32 s16, s91, 0x3ffff\n\t"
      "s_and_b32 s16, s16, s30\n\t"
      "s_cbranch_scc0 .Lend_%=\n\t"
      "s_waitcnt vmcnt(0) lgkmcnt(0)\n\t"
      "s_barrier\n\t"
      "s_lshl1_add_u32 s30, s14, 24576\n\t"
      "s_sub_u32 s15, s30, s15\n\t"
      "s_branch .Lstart_%=\n\t"
      ".Lwrap0_%=:\n\t"
      "v_add_u32 v5, s48, v5\n\t"
      "v_subrev_u32 v62, 113246208, v5\n\t"
      "v_min_u32 v5, v5, v62\n\t"
      "v_add_u32 v6, s48, v6\n\t"
      "v_subrev_u32 v62, 113246208, v6\n\t"
      "v_min_u32 v6, v6, v62\n\t"
      "v_add_u32 v7, s48, v7\n\t"
      "v_subrev_u32 v62, 113246208, v7\n\t"
      "v_min_u32 v7, v7, v62\n\t"
      "s_mov_b32 s48, 0\n\t"
      "buffer_load_dwordx4 v5, s[40:43], s48 offen nt lds\n\t"
      "buffer_load_dwordx4 v6, s[40:43], s48 offen offset:1024 nt lds\n\t"
      "buffer_load_dwordx4 v7, s[40:43], s48 offen offset:2048 nt lds\n\t"
      "s_branch .Lwrapped0_%=\n\t"
      ".Lwrap1_%=:\n\t"
      "v_add_u32 v5, s48, v5\n\t"
      "v_subrev_u32 v62, 113246208, v5\n\t"
      "v_min_u32 v5, v5, v62\n\t"
      "v_add_u32 v6, s48, v6\n\t"
      "v_subrev_u32 v62, 113246208, v6\n\t"
      "v_min_u32 v6, v6, v62\n\t"
      "v_add_u32 v7, s48, v7\n\t"
      "v_subrev_u32 v62, 113246208, v7\n\t"
      "v_min_u32 v7, v7, v62\n\t"
      "s_mov_b32 s48, 0\n\t"
      "buffer_load_dwordx4 v5, s[40:43], s48 offen nt lds\n\t"
      "buffer_load_dwordx4 v6, s[40:43], s48 offen offset:1024 nt lds\n\t"
      "buffer_load_dwordx4 v7, s[40:43], s48 offen offset:2048 nt lds\n\t"
      "s_branch .Lwrapped1_%=\n\t"
      ".Lwrap2_%=:\n\t"
      "v_add_u32 v5, s48, v5\n\t"
      "v_subrev_u32 v62, 113246208, v5\n\t"
      "v_min_u32 v5, v5, v62\n\t"
      "v_add_u32 v6, s48, v6\n\t"
      "v_subrev_u32 v62, 113246208, v6\n\t"
      "v_min_u32 v6, v6, v62\n\t"
      "v_add_u32 v7, s48, v7\n\t"
      "v_subrev_u32 v62, 113246208, v7\n\t"
      "v_min_u32 v7, v7, v62\n\t"
      "s_mov_b32 s48, 0\n\t"
      "buffer_load_dwordx4 v5, s[40:43], s48 offen nt lds\n\t"
      "buffer_load_dwordx4 v6, s[40:43], s48 offen offset:1024 nt lds\n\t"
      "buffer_load_dwordx4 v7, s[40:43], s48 offen offset:2048 nt lds\n\t"
      "s_branch .Lwrapped2_%=\n\t"
      ".Lwrap3_%=:\n\t"
      "v_add_u32 v5, s48, v5\n\t"
      "v_subrev_u32 v62, 113246208, v5\n\t"
      "v_min_u32 v5, v5, v62\n\t"
      "v_add_u32 v6, s48, v6\n\t"
      "v_subrev_u32 v62, 113246208, v6\n\t"
      "v_min_u32 v6, v6, v62\n\t"
      "v_add_u32 v7, s48, v7\n\t"
      "v_subrev_u32 v62, 113246208, v7\n\t"
      "v_min_u32 v7, v7, v62\n\t"
      "s_mov_b32 s48, 0\n\t"
      "buffer_load_dwordx4 v5, s[40:43], s48 offen nt lds\n\t"
      "buffer_load_dwordx4 v6, s[40:43], s48 offen offset:1024 nt lds\n\t"
      "buffer_load_dwordx4 v7, s[40:43], s48 offen offset:2048 nt lds\n\t"
      "s_branch .Lwrapped3_%=\n\t"
      ".Lwrapo_%=:\n\t"
      "v_add_u32 v12, s49, v12\n\t"
      "v_subrev_u32 v62, 113246208, v12\n\t"
      "v_min_u32 v12, v12, v62\n\t"
      "v_add_u32 v13, s49, v13\n\t"
      "v_subrev_u32 v62, 113246208, v13\n\t"
      "v_min_u32 v13, v13, v62\n\t"
      "v_add_u32 v14, s49, v14\n\t"
      "v_subrev_u32 v62, 113246208, v14\n\t"
      "v_min_u32 v14, v14, v62\n\t"
      "s_mov_b32 s49, 0\n\t"
      "s_branch .Lwrappedo_%=\n\t"
      ".Lwait_%=:\n\t"
      "s_sub_u32 s52, s52, 1\n\t"
      "s_cbranch_scc1 .Lwait2_%=\n\t"
      "s_add_u32 s90, s90, 0x20000\n\t"
      "v_mov_b32 v54, s90\n\t"
      "v_mov_b32 v55, s19\n\t"
      "global_store_dword v55, v54, s[12:13] sc1\n\t"
      "s_sleep 16\n\t"
      "global_load_dword v40, v62, s[12:13] sc1\n\t"
      "s_waitcnt vmcnt(0)\n\t"
      "s_branch .Lcheck_%=\n\t"
      ".Lwait2_%=:\n\t"
      "s_brev_b32 s91, 1\n\t"
      "s_branch .Lwaited_%=\n\t"
      ".Lspinf_%=:\n\t"
      "s_memrealtime s[36:37]\n\t"
      "s_waitcnt lgkmcnt(0)\n\t"
      "s_sub_u32 s53, s36, s38\n\t"
      "s_cmp_lt_i32 s53, 0\n\t"
      "s_cbranch_scc1 .Lspinf_%=\n\t"
      "s_branch .Lspunf_%=\n\t"
      ".Lspin0_%=:\n\t"
      "s_memrealtime s[36:37]\n\t"
      "s_waitcnt lgkmcnt(0)\n\t"
      "s_sub_u32 s53, s36, s38\n\t"
      "s_cmp_lt_i32 s53, 0\n\t"
      "s_cbranch_scc1 .Lspin0_%=\n\t"
      "s_branch .Lspun0_%=\n\t"
      ".Lspin1_%=:\n\t"
      "s_memrealtime s[36:37]\n\t"
      "s_waitcnt lgkmcnt(0)\n\t"
      "s_sub_u32 s53, s36, s38\n\t"
      "s_cmp_lt_i32 s53, 0\n\t"
      "s_cbranch_scc1 .Lspin1_%=\n\t"
      "s_branch .Lspun1_%=\n\t"
      ".Lend_%=:\n\t"
      : [tid] "+v"(tid)
      : [karg] "s"(__builtin_amdgcn_kernarg_segment_ptr()), [blk] "s"(blockIdx.x), [lds] "s"((unsigned)(size_t)lds)
      : "v1", "v2", "v3", "v4", "v5", "v6", "v7", "v8", "v9", "v10", "v11", "v12", "v13", "v14", "v15", "v16", "v17", "v18", "v19", "v20", "v21", "v22", "v23", "v24", "v25", "v26", "v27", "v28", "v29", "v30", "v31", "v32", "v33", "v34", "v35", "v36", "v37", "v38", "v39", "v40", "v41", "v42", "v43", "v44", "v45", "v46", "v47", "v48", "v49", "v50", "v51", "v52", "v53", "v54", "v55", "v56", "v57", "v58", "v59", "v60", "v61", "v62", "v63", "v64", "v65", "v66", "v67", "v68", "v69", "v70", "v71", "v72", "v73", "v74", "v75", "v76", "v77", "v78", "v79", "v80", "v81", "v82", "v83", "v84", "v85", "v86", "v87", "v88", "v89", "v90", "v91", "v92", "v93", "v94", "v95", "v96", "v97", "v98", "v99", "v100", "v101", "v102", "v103", "v104", "v105", "v106", "v107", "v108", "v109", "v110", "v111", "v112", "v113", "v114", "v115", "v116", "v117", "v118", "v119", "v120", "v121", "v122", "v123", "v124", "v125", "v126", "v127", "v128", "v129", "v130", "v131", "v132", "v133", "v134", "v135", "v136", "v137", "v138", "v139", "v140", "v141", "v142", "v143", "v144", "v145", "v146", "v147", "v148", "v149", "v150", "v151", "v152", "v153", "v154", "v155", "v156", "v157", "v158", "v159", "v160", "v161", "v162", "v163", "v164", "v165", "v166", "v167", "v168", "v169", "v170", "v171", "v172", "v173", "v174", "v175", "v176", "v177", "v178", "v179", "v180", "v181", "v182", "v183", "v184", "v185", "v186", "v187", "v188", "v189", "v190", "v191", "v192", "v193", "v194", "v195", "v196", "v197", "v198", "v199", "v200", "v201", "v202", "v203", "v204", "v205", "v206", "v207", "v208", "v209", "v210", "v211", "v212", "v213", "v214", "v215", "v216", "v217", "v218", "v219", "v220", "v221", "v222", "v223", "v224", "v225", "v226", "v227", "v228", "v229", "v230", "v231", "v232", "v233", "v234", "v235", "v236", "v237", "v238", "v239", "v240", "v241", "v242", "v243", "v244", "v245", "v246", "v247", "v248", "v249", "v250", "v251", "v252", "v253", "v254", "v255", "a0", "a1", "a2", "a3", "a4", "a5", "a6", "a7", "a8", "a9", "a10", "a11", "a12", "a13", "a14", "a15", "a16", "a17", "a18", "a19", "a20", "a21", "a22", "a23", "a24", "a25", "a26", "a27", "a28", "a29", "a30", "a31", "a32", "a33", "a34", "a35", "a36", "a37", "a38", "a39", "a40", "a41", "a42", "a43", "a44", "a45", "a46", "a47", "a48", "a49", "a50", "a51", "a52", "a53", "a54", "a55", "a56", "a57", "a58", "a59", "a60", "a61", "a62", "a63", "a64", "a65", "a66", "a67", "a68", "a69", "a70", "a71", "a72", "a73", "a74", "a75", "a76", "a77", "a78", "a79", "a80", "a81", "a82", "a83", "a84", "a85", "a86", "a87", "a88", "a89", "a90", "a91", "a92", "a93", "a94", "a95", "a96", "a97", "a98", "a99", "a100", "a101", "a102", "a103", "a104", "a105", "a106", "a107", "a108", "a109", "a110", "a111", "a112", "a113", "a114", "a115", "a116", "a117", "a118", "a119", "a120", "a121", "a122", "a123", "a124", "a125", "a126", "a127", "a128", "a129", "a130", "a131", "a132", "a133", "a134", "a135", "a136", "a137", "a138", "a139", "a140", "a141", "a142", "a143", "a144", "a145", "a146", "a147", "a148", "a149", "a150", "a151", "a152", "a153", "a154", "a155", "a156", "a157", "a158", "a159", "a160", "a161", "a162", "a163", "a164", "a165", "a166", "a167", "a168", "a169", "a170", "a171", "a172", "a173", "a174", "a175", "a176", "a177", "a178", "a179", "a180", "a181", "a182", "a183", "a184", "a185", "a186", "a187", "a188", "a189", "a190", "a191", "a192", "a193", "a194", "a195", "a196", "a197", "a198", "a199", "a200", "a201", "a202", "a203", "a204", "a205", "a206", "a207", "a208", "a209", "a210", "a211", "a212", "a213", "a214", "a215", "a216", "a217", "a218", "a219", "a220", "a221", "a222", "a223", "a224", "a225", "a226", "a227", "a228", "a229", "a230", "a231", "a232", "a233", "a234", "a235", "a236", "a237", "a238", "a239", "a240", "a241", "a242", "a243", "a244", "a245", "a246", "a247", "a248", "a249", "a250", "a251", "a252", "a253", "a254", "a255", "s4", "s5", "s6", "s7", "s8", "s9", "s10", "s11", "s12", "s13", "s14", "s15", "s16", "s17", "s18", "s19", "s20", "s21", "s22", "s23", "s24", "s25", "s26", "s27", "s28", "s29", "s30", "s31", "s32", "s33", "s34", "s35", "s36", "s37", "s38", "s39", "s40", "s41", "s42", "s43", "s44", "s45", "s46", "s47", "s48", "s49", "s50", "s51", "s52", "s53", "s54", "s55", "s56", "s57", "s58", "s59", "s60", "s61", "s62", "s63", "s64", "s65", "s66", "s67", "s68", "s69", "s70", "s71", "s72", "s73", "s74", "s75", "s76", "s77", "s78", "s79", "s80", "s81", "s82", "s83", "s84", "s85", "s86", "s87", "s88", "s89", "s90", "s91", "vcc", "scc", "memory");
}

__global__ __launch_bounds__(384) void dwsep_plain(const unsigned short* __restrict__ x, const unsigned short* __restrict__ dw,
                                                   const unsigned short* __restrict__ pw, unsigned short* __restrict__ out) {
  __shared__ float xs[384];
  const unsigned n = threadIdx.x;
  const size_t row = (size_t)blockIdx.x * 384u;
  xs[n] = __uint_as_float((unsigned)x[row + n] << 16) * __uint_as_float((unsigned)dw[n] << 16);
  __syncthreads();
  float acc = 0.f;
  for (int k = 0; k < 384; ++k) acc += xs[k] * __uint_as_float((unsigned)pw[k * 384 + n] << 16);
  const __bf16 o = (__bf16)acc;
  out[row + n] = __builtin_bit_cast(unsigned short, o);
}

extern "C" void kernel_launch(void* const* d_in, const int* in_sizes, int n_in, void* d_out, int out_size,
                              void* d_ws, size_t ws_size, hipStream_t stream) {
  const unsigned short* x = (const unsigned short*)d_in[0];
  const unsigned short* dw = (const unsigned short*)d_in[1];
  const unsigned short* pw = (const unsigned short*)d_in[2];
  unsigned short* out = (unsigned short*)d_out;
  if (in_sizes[0] == 56623104) dwsep_kernel<<<NWG, 256, 0, stream>>>(x, dw, pw, out, (unsigned*)d_ws);
  else dwsep_plain<<<in_sizes[0] / 384, 384, 0, stream>>>(x, dw, pw, out);
}
```

```cpp
#include <hip/hip_runtime.h>

constexpr unsigned ROWB = 768, TILEB = 12288, NWG = 256;

__global__ __launch_bounds__(256, 1) void dwsep_kernel(const unsigned short* __restrict__ x,
                                                       const unsigned short* __restrict__ dw,
                                                       const unsigned short* __restrict__ pw,
                                                       unsigned short* __restrict__ out, unsigned* ws) {
  __shared__ __attribute__((aligned(256))) char lds[163392];
  unsigned tid = threadIdx.x;
  asm volatile(
      "s_memtime s[32:33]\n\t"
      "s_load_dwordx8 s[4:11], %[karg], 0\n\t"
      "s_load_dwordx2 s[12:13], %[karg], 0x20\n\t"
      "v_readfirstlane_b32 s30, %[tid]\n\t"
      "s_lshr_b32 s31, %[blk], 1\n\t"
      "s_and_b32 s31, s31, 12\n\t"
      "s_and_b32 s34, %[blk], 3\n\t"
      "s_or_b32 s31, s31, s34\n\t"
      "s_lshr_b32 s30, s30, 6\n\t"
      "s_lshr_b32 s34, %[blk], 5\n\t"
      "s_bfe_u32 s35, %[blk], 0x10002\n\t"
      "s_mul_i32 s17, s30, 6144\n\t"
      "s_mul_i32 s18, s30, 3072\n\t"
      "s_mul_i32 s14, s34, 14155776\n\t"
      "s_mul_i32 s36, s31, 49152\n\t"
      "s_add_u32 s14, s14, s36\n\t"
      "s_mul_i32 s36, s35, 24576\n\t"
      "s_add_u32 s15, s14, s36\n\t"
      "s_mov_b32 s16, 0\n\t"
      "s_lshr_b32 s36, %[blk], 3\n\t"
      "s_lshl2_add_u32 s24, s36, s30\n\t"
      "s_sub_u32 s36, s24, s36\n\t"
      "s_sub_u32 s36, s36, 1\n\t"
      "s_mul_i32 s23, s36, 3072\n\t"
      "s_lshl_b32 s20, s34, 3\n\t"
      "s_mul_i32 s36, s34, 27\n\t"
      "s_lshr_b32 s36, s36, 2\n\t"
      "s_lshr_b32 s37, s31, 1\n\t"
      "s_add_u32 s19, s37, s20\n\t"
      "s_add_u32 s37, s37, s36\n\t"
      "s_min_u32 s37, s37, 53\n\t"
      "s_lshl_b32 s22, s37, 21\n\t"
      "s_lshl_b32 s36, s36, 21\n\t"
      "s_sub_u32 s21, s14, s36\n\t"
      "s_add_u32 s21, s21, 24576\n\t"
      "s_lshl_b32 s19, s19, 8\n\t"
      "s_and_b32 s31, s31, 1\n\t"
      "s_lshl1_add_u32 s31, s31, s35\n\t"
      "s_lshl2_add_u32 s19, s31, s19\n\t"
      ".Lstart_%=:\n\t"
      "v_mbcnt_lo_u32_b32 v233, -1, 0\n\t"
      "v_mbcnt_hi_u32_b32 v233, -1, v233\n\t"
      "s_mul_i32 s30, s17, 683\n\t"
      "s_lshr_b32 s30, s30, 16\n\t"
      "s_and_b32 s30, s30, 0xc0\n\t"
      "s_brev_b32 s34, 8\n\t"
      "s_mov_b32 s35, 0x20201010\n\t"
      "s_mov_b32 s36, 0x30303020\n\t"
      "v_lshl_add_u32 v1, v233, 4, s18\n\t"
      "v_lshrrev_b32 v16, 1, v233\n\t"
      "v_and_b32 v16, 24, v16\n\t"
      "v_bfe_u32 v17, s34, v16, 8\n\t"
      "v_or_b32 v17, s30, v17\n\t"
      "v_xor_b32 v18, v17, v1\n\t"
      "v_add_u32 v19, 80, v17\n\t"
      "v_add_u32 v17, 160, v17\n\t"
      "v_and_b32 v19, 256, v19\n\t"
      "v_and_b32 v17, 256, v17\n\t"
      "v_mad_u32_u24 v2, v19, 30, v18\n\t"
      "v_mad_u32_u24 v5, v17, 33, v18\n\t"
      "v_add_u32 v5, 19968, v5\n\t"
      "v_bfe_u32 v17, s35, v16, 8\n\t"
      "v_or_b32 v17, s30, v17\n\t"
      "v_xor_b32 v18, v17, v1\n\t"
      "v_add_u32 v19, 80, v17\n\t"
      "v_add_u32 v17, 160, v17\n\t"
      "v_and_b32 v19, 256, v19\n\t"
      "v_and_b32 v17, 256, v17\n\t"
      "v_mad_u32_u24 v3, v19, 30, v18\n\t"
      "v_mad_u32_u24 v6, v17, 33, v18\n\t"
      "v_add_u32 v6, 19968, v6\n\t"
      "v_bfe_u32 v17, s36, v16, 8\n\t"
      "v_or_b32 v17, s30, v17\n\t"
      "v_xor_b32 v18, v17, v1\n\t"
      "v_add_u32 v19, 80, v17\n\t"
      "v_add_u32 v17, 160, v17\n\t"
      "v_and_b32 v19, 256, v19\n\t"
      "v_and_b32 v17, 256, v17\n\t"
      "v_mad_u32_u24 v4, v19, 30, v18\n\t"
      "v_mad_u32_u24 v7, v17, 33, v18\n\t"
      "v_add_u32 v7, 19968, v7\n\t"
      "s_sub_u32 s31, s30, 64\n\t"
      "v_add_u32 v17, s31, v233\n\t"
      "v_min_u32 v17, 193, v17\n\t"
      "v_lshlrev_b32 v18, 2, v17\n\t"
      "v_lshlrev_b32 v17, 1, v17\n\t"
      "v_and_b32 v18, -16, v18\n\t"
      "v_and_or_b32 v8, v17, 6, v18\n\t"
      "s_mov_b32 s86, 294912\n\t"
      "s_mov_b32 s87, 0x00020000\n\t"
      "s_mov_b32 s42, 113246208\n\t"
      "s_mov_b32 s43, 0x00020000\n\t"
      "s_cmp_lg_u32 s17, 0\n\t"
      "s_cbranch_scc1 .Lno0_%=\n\t"
      "s_movk_i32 s31, -61\n\t"
      "v_lshrrev_b32 v16, 2, v233\n\t"
      "v_add_u32 v17, s20, v16\n\t"
      "v_lshl_add_u32 v18, v16, 3, s31\n\t"
      "v_cmp_gt_u32 vcc, 32, v233\n\t"
      "v_and_b32 v19, 3, v233\n\t"
      "v_lshlrev_b32 v19, 2, v19\n\t"
      "v_cndmask_b32 v17, v18, v17, vcc\n\t"
      "v_lshl_add_u32 v62, v17, 8, v19\n\t"
      "s_waitcnt lgkmcnt(2)\n\t"
      "s_add_u32 s32, s32, %[blk]\n\t"
      "s_mul_i32 s32, s32, 0x9e3779b1\n\t"
      "s_lshr_b32 s32, s32, 9\n\t"
      "s_and_b32 s32, s32, 0x1fdf80\n\t"
      "s_or_b32 s22, s22, s32\n\t"
      "v_mov_b32 v234, s22\n\t"
      "v_add_u32 v235, 8192, v234\n\t"
      "s_movk_i32 s64, 120\n\t"
      "s_mov_b64 vcc, 0\n\t"
      ".Lsweep_%=:\n\t"
      "s_getreg_b32 s30, hwreg(HW_REG_IB_STS, 0, 12)\n\t"
      "s_and_b32 s31, s30, 15\n\t"
      "s_lshr_b32 s30, s30, 8\n\t"
      "s_cmp_eq_u32 s31, 8\n\t"
      "s_addc_u32 s62, s62, 0\n\t"
      "s_cmp_eq_u32 s30, 1\n\t"
      "s_addc_u32 s63, s63, 0\n\t"
      "s_cmp_gt_u32 s31, 7\n\t"
      "s_addc_u32 s30, s30, 0\n\t"
      "s_sub_u32 s64, s64, 1\n\t"
      "s_min_u32 s30, s30, s64\n\t"
      "s_cmp_lg_u32 s30, 0\n\t"
      "s_cbranch_scc1 .Lsweep_%=\n\t"
      "s_cbranch_vccnz .Lswept_%=\n\t"
      "s_waitcnt lgkmcnt(0)\n\t"
      "global_load_dword v236, v234, s[4:5]\n\t"
      "global_load_dword v237, v235, s[4:5]\n\t"
      "s_memrealtime s[28:29]\n\t"
      "s_load_dword s60, s[10:11], s22\n\t"
      "s_load_dword s61, s[10:11], s22 offset:8192\n\t"
      "global_load_dword v63, v62, s[12:13] sc1\n\t"
      "s_mov_b64 s[40:41], s[4:5]\n\t"
      "s_add_u32 m0, s18, 25088\n\t"
      "s_add_u32 s48, s15, 0\n\t"
      "buffer_load_dwordx4 v2, s[40:43], s48 offen nt lds\n\t"
      "buffer_load_dwordx4 v3, s[40:43], s48 offen offset:1024 nt lds\n\t"
      "buffer_load_dwordx4 v4, s[40:43], s48 offen offset:2048 nt lds\n\t"
      "s_add_u32 m0, s18, 37376\n\t"
      "s_add_u32 s48, s15, 0\n\t"
      "buffer_load_dwordx4 v5, s[40:43], s48 offen nt lds\n\t"
      "buffer_load_dwordx4 v6, s[40:43], s48 offen offset:1024 nt lds\n\t"
      "buffer_load_dwordx4 v7, s[40:43], s48 offen offset:2048 nt lds\n\t"
      "s_barrier\n\t"
      "s_mov_b64 s[62:63], 0\n\t"
      "s_movk_i32 s64, 120\n\t"
      "s_mov_b64 vcc, -1\n\t"
      "s_branch .Lsweep_%=\n\t"
      ".Lswept_%=:\n\t"
      "s_min_u32 s62, s62, 7\n\t"
      "s_min_u32 s63, s63, 7\n\t"
      "s_lshl_b32 s63, s63, 8\n\t"
      "s_or_b32 s30, s62, s63\n\t"
      "s_sub_u32 s31, 0x808, s30\n\t"
      "s_add_u32 s30, s30, 0x10808\n\t"
      "s_getreg_b32 s62, hwreg(HW_REG_XCC_ID, 2, 1)\n\t"
      "s_cmp_lg_u32 s62, 0\n\t"
      "s_cselect_b32 s91, s30, s31\n\t"
      "s_mov_b64 s[84:85], s[8:9]\n\t"
      "v_mul_u32_u24 v16, 49152, v1\n\t"
      "v_add_u32 v16, s21, v16\n\t"
      "v_lshrrev_b32 v16, 21, v16\n\t"
      "v_lshlrev_b32 v16, 2, v16\n\t"
      "v_accvgpr_write_b32 a248, v16\n\t"
      "s_branch .Lgo0_%=\n\t"
      ".Lno0_%=:\n\t"
      "s_movk_i32 s82, 768\n\t"
      "s_mov_b32 s83, 0x00020000\n\t"
      "v_and_b32 v234, 0x3f0, v1\n\t"
      "s_waitcnt lgkmcnt(0)\n\t"
      "s_mov_b64 s[84:85], s[8:9]\n\t"
      "s_mov_b64 s[80:81], s[6:7]\n\t"
      "s_mov_b64 s[40:41], s[4:5]\n\t"
      "s_barrier\n\t"
      "buffer_load_dwordx4 a[200:203], v234, s[84:87], s23 offen offset:0\n\t"
      "buffer_load_dwordx4 a[204:207], v234, s[84:87], s23 offen offset:1024\n\t"
      "buffer_load_dwordx4 a[208:211], v234, s[84:87], s23 offen offset:2048\n\t"
      "buffer_load_ushort v224, v8, s[80:83], 0 offen\n\t"
      "buffer_load_ushort v225, v8, s[80:83], 0 offen offset:8\n\t"
      "s_add_u32 m0, s18, 25088\n\t"
      "s_add_u32 s48, s15, 0\n\t"
      "buffer_load_dwordx4 v2, s[40:43], s48 offen nt lds\n\t"
      "buffer_load_dwordx4 v3, s[40:43], s48 offen offset:1024 nt lds\n\t"
      "buffer_load_dwordx4 v4, s[40:43], s48 offen offset:2048 nt lds\n\t"
      "s_add_u32 m0, s18, 37376\n\t"
      "s_add_u32 s48, s15, 0\n\t"
      "buffer_load_dwordx4 v5, s[40:43], s48 offen nt lds\n\t"
      "buffer_load_dwordx4 v6, s[40:43], s48 offen offset:1024 nt lds\n\t"
      "buffer_load_dwordx4 v7, s[40:43], s48 offen offset:2048 nt lds\n\t"
      ".Lgo0_%=:\n\t"
      "s_getpc_b64 s[36:37]\n\t"
      ".Lpc_%=:\n\t"
      "s_lshr_b32 s30, s17, 5\n\t"
      "v_lshlrev_b32 v16, 4, v233\n\t"
      "v_add_u32 v17, 0, v233\n\t"
      "v_mul_u32_u24 v17, 171, v17\n\t"
      "v_lshrrev_b32 v17, 11, v17\n\t"
      "v_lshlrev_b32 v18, 2, v17\n\t"
      "v_and_b32 v18, 32, v18\n\t"
      "v_xor_b32 v18, v18, v16\n\t"
      "v_mul_u32_u24 v17, 576, v17\n\t"
      "v_add3_u32 v9, v17, s30, v18\n\t"
      "v_add_u32 v17, 64, v233\n\t"
      "v_mul_u32_u24 v17, 171, v17\n\t"
      "v_lshrrev_b32 v17, 11, v17\n\t"
      "v_lshlrev_b32 v18, 2, v17\n\t"
      "v_and_b32 v18, 32, v18\n\t"
      "v_xor_b32 v18, v18, v16\n\t"
      "v_mul_u32_u24 v17, 576, v17\n\t"
      "v_add3_u32 v10, v17, s30, v18\n\t"
      "v_add_u32 v17, 128, v233\n\t"
      "v_mul_u32_u24 v17, 171, v17\n\t"
      "v_lshrrev_b32 v17, 11, v17\n\t"
      "v_lshlrev_b32 v18, 2, v17\n\t"
      "v_and_b32 v18, 32, v18\n\t"
      "v_xor_b32 v18, v18, v16\n\t"
      "v_mul_u32_u24 v17, 576, v17\n\t"
      "v_add3_u32 v11, v17, s30, v18\n\t"
      "s_mov_b64 s[44:45], s[10:11]\n\t"
      "s_mov_b32 s46, 0\n\t"
      "s_mov_b32 s47, 0x00020000\n\t"
      "s_lshl_b32 s30, s24, 6\n\t"
      "s_add_u32 s31, s30, 0\n\t"
      "s_min_u32 s31, s31, .Lend_%=-.Lpc_%=-4\n\t"
      "s_load_dword s35, s[36:37], s31\n\t"
      "s_add_u32 s31, s30, 8192\n\t"
      "s_min_u32 s31, s31, .Lend_%=-.Lpc_%=-4\n\t"
      "s_load_dword s35, s[36:37], s31\n\t"
      "s_add_u32 s31, s30, 16384\n\t"
      "s_min_u32 s31, s31, .Lend_%=-.Lpc_%=-4\n\t"
      "s_load_dword s35, s[36:37], s31\n\t"
      "s_add_u32 s31, s30, 24576\n\t"
      "s_min_u32 s31, s31, .Lend_%=-.Lpc_%=-4\n\t"
      "s_load_dword s35, s[36:37], s31\n\t"
      "s_and_b32 s31, s24, 63\n\t"
      "s_lshl_b32 s31, s31, 21\n\t"
      "s_min_u32 s31, s31, 113246144\n\t"
      "s_bitcmp1_b32 s24, 6\n\t"
      "s_cselect_b64 s[32:33], s[44:45], s[40:41]\n\t"
      "s_load_dword s35, s[32:33], s31\n\t"
      "s_lshr_b32 s30, s17, 5\n\t"
      "v_lshrrev_b32 v16, 4, v233\n\t"
      "v_and_b32 v17, 15, v233\n\t"
      "v_and_b32 v18, 3, v233\n\t"
      "v_bfe_u32 v19, v233, 2, 2\n\t"
      "v_lshl_add_u32 v19, v16, 3, v19\n\t"
      "v_mul_u32_u24 v19, 192, v19\n\t"
      "v_lshl_add_u32 v19, v18, 3, v19\n\t"
      "v_lshlrev_b32 v20, 1, v233\n\t"
      "v_and_b32 v20, 32, v20\n\t"
      "v_add3_u32 v12, v19, v20, s17\n\t"
      "v_xor_b32 v13, 32, v12\n\t"
      "v_xor_b32 v19, v16, v17\n\t"
      "v_lshlrev_b32 v19, 4, v19\n\t"
      "v_mul_u32_u24 v20, 768, v17\n\t"
      "v_add_u32 v15, v19, v20\n\t"
      "v_mul_u32_u24 v20, 784, v17\n\t"
      "v_lshl_add_u32 v20, v16, 3, v20\n\t"
      "v_add_u32 v0, s30, v20\n\t"
      "v_lshlrev_b32 v19, 6, v16\n\t"
      "v_lshl_add_u32 v19, v18, 4, v19\n\t"
      "v_add_u32 v14, 160256, v19\n\t"
      "v_and_b32 v226, 0xfffffff0, v8\n\t"
      "v_and_b32 v227, 6, v8\n\t"
      "v_lshlrev_b32 v226, 2, v226\n\t"
      "v_lshl_add_u32 v226, v227, 3, v226\n\t"
      "v_add_u32 v226, 160256, v226\n\t"
      "v_and_b32 v232, 2, v8\n\t"
      "v_lshlrev_b32 v232, 3, v232\n\t"
      "v_cmp_gt_u32 vcc, 4, v227\n\t"
      "v_mov_b32 v56, v15\n\t"
      "v_xor_b32 v57, 64, v15\n\t"
      "v_xor_b32 v58, 128, v15\n\t"
      "v_xor_b32 v59, 192, v15\n\t"
      "v_add_u32 v60, 86528, v12\n\t"
      "v_add_u32 v61, 86528, v13\n\t"
      "s_waitcnt vmcnt(6)\n\t"
      "s_bfe_u32 s30, s19, 0x30008\n\t"
      "s_bfe_u32 s31, s19, 0x20002\n\t"
      "s_lshl2_add_u32 s30, s30, s31\n\t"
      "v_readlane_b32 s90, v63, s30\n\t"
      "v_lshlrev_b32 v224, v232, v224\n\t"
      "v_lshlrev_b32 v225, v232, v225\n\t"
      "v_cndmask_b32 v228, 0, v224, vcc\n\t"
      "v_cndmask_b32 v230, 0, v225, vcc\n\t"
      "v_sub_u32 v229, v224, v228\n\t"
      "v_sub_u32 v231, v225, v230\n\t"
      "ds_write_b128 v226, v[228:231]\n\t"
      "s_add_u32 s90, s90, 0x20000\n\t"
      "s_andn2_b32 s90, s90, 0x1ffff\n\t"
      "s_or_b32 s90, s90, s91\n\t"
      "s_or_b32 s30, s17, s16\n\t"
      "s_cbranch_scc1 .Lno2_%=\n\t"
      "v_mov_b32 v234, s90\n\t"
      "v_mov_b32 v235, s19\n\t"
      "global_store_dword v235, v234, s[12:13] sc1\n\t"
      ".Lno2_%=:\n\t"
      "s_add_u32 m0, s17, 0\n\t"
      "s_mov_b32 s54, 0\n\t"
      "buffer_load_dwordx4 v9, s[84:87], s54 offen lds\n\t"
      "buffer_load_dwordx4 v10, s[84:87], s54 offen offset:1024 lds\n\t"
      "buffer_load_dwordx4 v11, s[84:87], s54 offen offset:2048 lds\n\t"
      "s_add_u32 m0, s17, 3072\n\t"
      "s_mov_b32 s54, 12288\n\t"
      "buffer_load_dwordx4 v9, s[84:87], s54 offen lds\n\t"
      "buffer_load_dwordx4 v10, s[84:87], s54 offen offset:1024 lds\n\t"
      "buffer_load_dwordx4 v11, s[84:87], s54 offen offset:2048 lds\n\t"
      "s_add_u32 m0, s17, 86528\n\t"
      "s_mov_b32 s54, 24576\n\t"
      "buffer_load_dwordx4 v9, s[84:87], s54 offen lds\n\t"
      "buffer_load_dwordx4 v10, s[84:87], s54 offen offset:1024 lds\n\t"
      "buffer_load_dwordx4 v11, s[84:87], s54 offen offset:2048 lds\n\t"
      "s_add_u32 m0, s17, 89600\n\t"
      "s_mov_b32 s54, 36864\n\t"
      "buffer_load_dwordx4 v9, s[84:87], s54 offen lds\n\t"
      "buffer_load_dwordx4 v10, s[84:87], s54 offen offset:1024 lds\n\t"
      "buffer_load_dwordx4 v11, s[84:87], s54 offen offset:2048 lds\n\t"
      "s_waitcnt lgkmcnt(0)\n\t"
      "s_waitcnt vmcnt(12)\n\t"
      "s_barrier\n\t"
      "s_waitcnt vmcnt(6)\n\t"
      "ds_read_b128 a[180:183], v14 offset:0\n\t"
      "ds_read_b64_tr_b16 v[64:65], v12 offset:0\n\t"
      "ds_read_b64_tr_b16 v[66:67], v12 offset:768\n\t"
      "ds_read_b64_tr_b16 v[68:69], v13 offset:0\n\t"
      "ds_read_b64_tr_b16 v[70:71], v13 offset:768\n\t"
      "ds_read_b64_tr_b16 v[72:73], v12 offset:64\n\t"
      "ds_read_b64_tr_b16 v[74:75], v12 offset:832\n\t"
      "ds_read_b64_tr_b16 v[76:77], v13 offset:64\n\t"
      "ds_read_b64_tr_b16 v[78:79], v13 offset:832\n\t"
      "ds_read_b64_tr_b16 a[60:61], v12 offset:128\n\t"
      "ds_read_b64_tr_b16 a[62:63], v12 offset:896\n\t"
      "ds_read_b64_tr_b16 a[64:65], v13 offset:128\n\t"
      "ds_read_b64_tr_b16 a[66:67], v13 offset:896\n\t"
      "s_add_u32 m0, s17, 111104\n\t"
      "s_mov_b32 s54, 49152\n\t"
      "buffer_load_dwordx4 v9, s[84:87], s54 offen lds\n\t"
      "buffer_load_dwordx4 v10, s[84:87], s54 offen offset:1024 lds\n\t"
      "buffer_load_dwordx4 v11, s[84:87], s54 offen offset:2048 lds\n\t"
      "s_add_u32 m0, s17, 114176\n\t"
      "s_mov_b32 s54, 61440\n\t"
      "buffer_load_dwordx4 v9, s[84:87], s54 offen lds\n\t"
      "buffer_load_dwordx4 v10, s[84:87], s54 offen offset:1024 lds\n\t"
      "buffer_load_dwordx4 v11, s[84:87], s54 offen offset:2048 lds\n\t"
      "s_waitcnt vmcnt(6)\n\t"
      "s_waitcnt lgkmcnt(0)\n\t"
      "; step\n\t"
      "v_mfma_f32_4x4x4_16b_bf16 v[40:43], a[180:181], v[64:65], 0\n\t"
      "ds_read_b128 a[184:187], v14 offset:256\n\t"
      "v_mfma_f32_4x4x4_16b_bf16 v[44:47], a[182:183], v[66:67], 0\n\t"
      "ds_read_b64_tr_b16 v[80:81], v60 offset:0\n\t"
      "v_mfma_f32_4x4x4_16b_bf16 v[48:51], a[180:181], v[68:69], 0\n\t"
      "ds_read_b64_tr_b16 v[82:83], v60 offset:768\n\t"
      "v_mfma_f32_4x4x4_16b_bf16 v[52:55], a[182:183], v[70:71], 0\n\t"
      "ds_read_b64_tr_b16 v[84:85], v61 offset:0\n\t"
      "ds_read_b64_tr_b16 v[86:87], v61 offset:768\n\t"
      "s_add_u32 m0, s17, 135680\n\t"
      "s_mov_b32 s54, 73728\n\t"
      "buffer_load_dwordx4 v9, s[84:87], s54 offen lds\n\t"
      "buffer_load_dwordx4 v10, s[84:87], s54 offen offset:1024 lds\n\t"
      "v_cvt_pk_bf16_f32 v64, v40, v41\n\t"
      "v_cvt_pk_bf16_f32 v65, v42, v43\n\t"
      "v_cvt_pk_bf16_f32 v66, v44, v45\n\t"
      "v_cvt_pk_bf16_f32 v67, v46, v47\n\t"
      "v_cvt_pk_bf16_f32 v68, v48, v49\n\t"
      "v_cvt_pk_bf16_f32 v69, v50, v51\n\t"
      "v_cvt_pk_bf16_f32 v70, v52, v53\n\t"
      "v_cvt_pk_bf16_f32 v71, v54, v55\n\t"
      "v_mfma_f32_4x4x4_16b_bf16 v[40:43], a[180:181], v[72:73], 0\n\t"
      "ds_read_b64_tr_b16 v[88:89], v60 offset:64\n\t"
      "v_mfma_f32_4x4x4_16b_bf16 v[44:47], a[182:183], v[74:75], 0\n\t"
      "ds_read_b64_tr_b16 v[90:91], v60 offset:832\n\t"
      "v_mfma_f32_4x4x4_16b_bf16 v[48:51], a[180:181], v[76:77], 0\n\t"
      "ds_read_b64_tr_b16 v[92:93], v61 offset:64\n\t"
      "v_mfma_f32_4x4x4_16b_bf16 v[52:55], a[182:183], v[78:79], 0\n\t"
      "ds_read_b64_tr_b16 v[94:95], v61 offset:832\n\t"
      "buffer_load_dwordx4 v11, s[84:87], s54 offen offset:2048 lds\n\t"
      "s_add_u32 m0, s17, 138752\n\t"
      "s_mov_b32 s54, 86016\n\t"
      "buffer_load_dwordx4 v9, s[84:87], s54 offen lds\n\t"
      "v_cvt_pk_bf16_f32 v72, v40, v41\n\t"
      "v_cvt_pk_bf16_f32 v73, v42, v43\n\t"
      "v_cvt_pk_bf16_f32 v74, v44, v45\n\t"
      "v_cvt_pk_bf16_f32 v75, v46, v47\n\t"
      "v_cvt_pk_bf16_f32 v76, v48, v49\n\t"
      "v_cvt_pk_bf16_f32 v77, v50, v51\n\t"
      "v_cvt_pk_bf16_f32 v78, v52, v53\n\t"
      "v_cvt_pk_bf16_f32 v79, v54, v55\n\t"
      "ds_read_b128 a[224:227], v56 offset:25088\n\t"
      "v_mfma_f32_4x4x4_16b_bf16 v[40:43], a[180:181], a[60:61], 0\n\t"
      "ds_read_b64_tr_b16 a[68:69], v60 offset:128\n\t"
      "v_mfma_f32_4x4x4_16b_bf16 v[44:47], a[182:183], a[62:63], 0\n\t"
      "ds_read_b64_tr_b16 a[70:71], v60 offset:896\n\t"
      "v_mfma_f32_4x4x4_16b_bf16 v[48:51], a[180:181], a[64:65], 0\n\t"
      "ds_read_b64_tr_b16 a[72:73], v61 offset:128\n\t"
      "v_mfma_f32_4x4x4_16b_bf16 v[52:55], a[182:183], a[66:67], 0\n\t"
      "ds_read_b64_tr_b16 a[74:75], v61 offset:896\n\t"
      "buffer_load_dwordx4 v10, s[84:87], s54 offen offset:1024 lds\n\t"
      "buffer_load_dwordx4 v11, s[84:87], s54 offen offset:2048 lds\n\t"
      "ds_read_b128 a[228:231], v56 offset:37376\n\t"
      "v_cvt_pk_bf16_f32 v40, v40, v41\n\t"
      "v_cvt_pk_bf16_f32 v41, v42, v43\n\t"
      "v_cvt_pk_bf16_f32 v44, v44, v45\n\t"
      "v_cvt_pk_bf16_f32 v45, v46, v47\n\t"
      "v_cvt_pk_bf16_f32 v48, v48, v49\n\t"
      "v_cvt_pk_bf16_f32 v49, v50, v51\n\t"
      "v_cvt_pk_bf16_f32 v52, v52, v53\n\t"
      "v_cvt_pk_bf16_f32 v53, v54, v55\n\t"
      "v_accvgpr_write_b32 a60, v40\n\t"
      "v_accvgpr_write_b32 a61, v41\n\t"
      "v_accvgpr_write_b32 a62, v44\n\t"
      "v_accvgpr_write_b32 a63, v45\n\t"
      "v_accvgpr_write_b32 a64, v48\n\t"
      "v_accvgpr_write_b32 a65, v49\n\t"
      "v_accvgpr_write_b32 a66, v52\n\t"
      "v_accvgpr_write_b32 a67, v53\n\t"
      "s_waitcnt vmcnt(6)\n\t"
      "s_waitcnt lgkmcnt(1)\n\t"
      "; step\n\t"
      "v_mfma_f32_4x4x4_16b_bf16 v[40:43], a[184:185], v[80:81], 0\n\t"
      "ds_read_b128 a[180:183], v14 offset:512\n\t"
      "v_mfma_f32_4x4x4_16b_bf16 v[44:47], a[186:187], v[82:83], 0\n\t"
      "ds_read_b64_tr_b16 v[96:97], v60 offset:24576\n\t"
      "v_mfma_f32_4x4x4_16b_bf16 v[48:51], a[184:185], v[84:85], 0\n\t"
      "ds_read_b64_tr_b16 v[98:99], v60 offset:25344\n\t"
      "v_mfma_f32_4x4x4_16b_bf16 v[52:55], a[186:187], v[86:87], 0\n\t"
      "ds_read_b64_tr_b16 v[100:101], v61 offset:24576\n\t"
      "ds_read_b64_tr_b16 v[102:103], v61 offset:25344\n\t"
      "s_add_u32 m0, s17, 0\n\t"
      "s_mov_b32 s54, 98304\n\t"
      "buffer_load_dwordx4 v9, s[84:87], s54 offen lds\n\t"
      "buffer_load_dwordx4 v10, s[84:87], s54 offen offset:1024 lds\n\t"
      "v_mfma_f32_16x16x32_bf16 v[16:19], v[64:67], a[224:227], 0\n\t"
      "v_cvt_pk_bf16_f32 v80, v40, v41\n\t"
      "v_cvt_pk_bf16_f32 v81, v42, v43\n\t"
      "v_mfma_f32_16x16x32_bf16 v[20:23], v[68:71], a[224:227], 0\n\t"
      "v_cvt_pk_bf16_f32 v82, v44, v45\n\t"
      "v_cvt_pk_bf16_f32 v83, v46, v47\n\t"
      "v_mfma_f32_16x16x32_bf16 v[24:27], v[72:75], a[224:227], 0\n\t"
      "v_cvt_pk_bf16_f32 v84, v48, v49\n\t"
      "v_cvt_pk_bf16_f32 v85, v50, v51\n\t"
      "v_mfma_f32_16x16x32_bf16 v[28:31], v[76:79], a[224:227], 0\n\t"
      "v_cvt_pk_bf16_f32 v86, v52, v53\n\t"
      "v_cvt_pk_bf16_f32 v87, v54, v55\n\t"
      "v_mfma_f32_4x4x4_16b_bf16 v[40:43], a[184:185], v[88:89], 0\n\t"
      "ds_read_b64_tr_b16 v[104:105], v60 offset:24640\n\t"
      "v_mfma_f32_4x4x4_16b_bf16 v[44:47], a[186:187], v[90:91], 0\n\t"
      "ds_read_b64_tr_b16 v[106:107], v60 offset:25408\n\t"
      "v_mfma_f32_4x4x4_16b_bf16 v[48:51], a[184:185], v[92:93], 0\n\t"
      "ds_read_b64_tr_b16 v[108:109], v61 offset:24640\n\t"
      "v_mfma_f32_4x4x4_16b_bf16 v[52:55], a[186:187], v[94:95], 0\n\t"
      "ds_read_b64_tr_b16 v[110:111], v61 offset:25408\n\t"
      "buffer_load_dwordx4 v11, s[84:87], s54 offen offset:2048 lds\n\t"
      "s_add_u32 m0, s17, 3072\n\t"
      "s_mov_b32 s54, 110592\n\t"
      "buffer_load_dwordx4 v9, s[84:87], s54 offen lds\n\t"
      "v_mfma_f32_16x16x32_bf16 v[32:35], a[60:63], a[224:227], 0\n\t"
      "v_cvt_pk_bf16_f32 v88, v40, v41\n\t"
      "v_cvt_pk_bf16_f32 v89, v42, v43\n\t"
      "v_mfma_f32_16x16x32_bf16 v[36:39], a[64:67], a[224:227], 0\n\t"
      "v_cvt_pk_bf16_f32 v90, v44, v45\n\t"
      "v_cvt_pk_bf16_f32 v91, v46, v47\n\t"
      "s_waitcnt lgkmcnt(9)\n\t"
      "v_mfma_f32_16x16x32_bf16 a[156:159], v[64:67], a[228:231], 0\n\t"
      "v_cvt_pk_bf16_f32 v92, v48, v49\n\t"
      "v_cvt_pk_bf16_f32 v93, v50, v51\n\t"
      "v_mfma_f32_16x16x32_bf16 a[160:163], v[68:71], a[228:231], 0\n\t"
      "v_cvt_pk_bf16_f32 v94, v52, v53\n\t"
      "v_cvt_pk_bf16_f32 v95, v54, v55\n\t"
      "ds_read_b128 a[232:235], v57 offset:25088\n\t"
      "v_mfma_f32_4x4x4_16b_bf16 v[40:43], a[184:185], a[68:69], 0\n\t"
      "ds_read_b64_tr_b16 a[76:77], v60 offset:24704\n\t"
      "v_mfma_f32_4x4x4_16b_bf16 v[44:47], a[186:187], a[70:71], 0\n\t"
      "ds_read_b64_tr_b16 a[78:79], v60 offset:25472\n\t"
      "v_mfma_f32_4x4x4_16b_bf16 v[48:51], a[184:185], a[72:73], 0\n\t"
      "ds_read_b64_tr_b16 a[80:81], v61 offset:24704\n\t"
      "v_mfma_f32_4x4x4_16b_bf16 v[52:55], a[186:187], a[74:75], 0\n\t"
      "ds_read_b64_tr_b16 a[82:83], v61 offset:25472\n\t"
      "buffer_load_dwordx4 v10, s[84:87], s54 offen offset:1024 lds\n\t"
      "buffer_load_dwordx4 v11, s[84:87], s54 offen offset:2048 lds\n\t"
      "ds_read_b128 a[236:239], v57 offset:37376\n\t"
      "v_mfma_f32_16x16x32_bf16 a[164:167], v[72:75], a[228:231], 0\n\t"
      "v_cvt_pk_bf16_f32 v40, v40, v41\n\t"
      "v_cvt_pk_bf16_f32 v41, v42, v43\n\t"
      "v_mfma_f32_16x16x32_bf16 a[168:171], v[76:79], a[228:231], 0\n\t"
      "v_cvt_pk_bf16_f32 v44, v44, v45\n\t"
      "v_cvt_pk_bf16_f32 v45, v46, v47\n\t"
      "v_mfma_f32_16x16x32_bf16 a[172:175], a[60:63], a[228:231], 0\n\t"
      "v_cvt_pk_bf16_f32 v48, v48, v49\n\t"
      "v_cvt_pk_bf16_f32 v49, v50, v51\n\t"
      "v_mfma_f32_16x16x32_bf16 a[176:179], a[64:67], a[228:231], 0\n\t"
      "v_cvt_pk_bf16_f32 v52, v52, v53\n\t"
      "v_cvt_pk_bf16_f32 v53, v54, v55\n\t"
      "v_accvgpr_write_b32 a68, v40\n\t"
      "v_accvgpr_write_b32 a69, v41\n\t"
      "v_accvgpr_write_b32 a70, v44\n\t"
      "v_accvgpr_write_b32 a71, v45\n\t"
      "v_accvgpr_write_b32 a72, v48\n\t"
      "v_accvgpr_write_b32 a73, v49\n\t"
      "v_accvgpr_write_b32 a74, v52\n\t"
      "v_accvgpr_write_b32 a75, v53\n\t"
      "s_waitcnt vmcnt(6)\n\t"
      "s_waitcnt lgkmcnt(1)\n\t"
      "; step\n\t"
      "v_mfma_f32_4x4x4_16b_bf16 v[40:43], a[180:181], v[96:97], 0\n\t"
      "ds_read_b128 a[184:187], v14 offset:768\n\t"
      "v_mfma_f32_4x4x4_16b_bf16 v[44:47], a[182:183], v[98:99], 0\n\t"
      "ds_read_b64_tr_b16 v[112:113], v60 offset:49152\n\t"
      "v_mfma_f32_4x4x4_16b_bf16 v[48:51], a[180:181], v[100:101], 0\n\t"
      "ds_read_b64_tr_b16 v[114:115], v60 offset:49920\n\t"
      "v_mfma_f32_4x4x4_16b_bf16 v[52:55], a[182:183], v[102:103], 0\n\t"
      "ds_read_b64_tr_b16 v[116:117], v61 offset:49152\n\t"
      "ds_read_b64_tr_b16 v[118:119], v61 offset:49920\n\t"
      "s_add_u32 m0, s17, 86528\n\t"
      "s_mov_b32 s54, 122880\n\t"
      "buffer_load_dwordx4 v9, s[84:87], s54 offen lds\n\t"
      "buffer_load_dwordx4 v10, s[84:87], s54 offen offset:1024 lds\n\t"
      "v_mfma_f32_16x16x32_bf16 v[16:19], v[80:83], a[232:235], v[16:19]\n\t"
      "v_cvt_pk_bf16_f32 v96, v40, v41\n\t"
      "v_cvt_pk_bf16_f32 v97, v42, v43\n\t"
      "v_mfma_f32_16x16x32_bf16 v[20:23], v[84:87], a[232:235], v[20:23]\n\t"
      "v_cvt_pk_bf16_f32 v98, v44, v45\n\t"
      "v_cvt_pk_bf16_f32 v99, v46, v47\n\t"
      "v_mfma_f32_16x16x32_bf16 v[24:27], v[88:91], a[232:235], v[24:27]\n\t"
      "v_cvt_pk_bf16_f32 v100, v48, v49\n\t"
      "v_cvt_pk_bf16_f32 v101, v50, v51\n\t"
      "v_mfma_f32_16x16x32_bf16 v[28:31], v[92:95], a[232:235], v[28:31]\n\t"
      "v_cvt_pk_bf16_f32 v102, v52, v53\n\t"
      "v_cvt_pk_bf16_f32 v103, v54, v55\n\t"
      "v_mfma_f32_4x4x4_16b_bf16 v[40:43], a[180:181], v[104:105], 0\n\t"
      "ds_read_b64_tr_b16 v[120:121], v60 offset:49216\n\t"
      "v_mfma_f32_4x4x4_16b_bf16 v[44:47], a[182:183], v[106:107], 0\n\t"
      "ds_read_b64_tr_b16 v[122:123], v60 offset:49984\n\t"
      "v_mfma_f32_4x4x4_16b_bf16 v[48:51], a[180:181], v[108:109], 0\n\t"
      "ds_read_b64_tr_b16 v[124:125], v61 offset:49216\n\t"
      "v_mfma_f32_4x4x4_16b_bf16 v[52:55], a[182:183], v[110:111], 0\n\t"
      "ds_read_b64_tr_b16 v[126:127], v61 offset:49984\n\t"
      "buffer_load_dwordx4 v11, s[84:87], s54 offen offset:2048 lds\n\t"
      "s_add_u32 m0, s17, 89600\n\t"
      "s_mov_b32 s54, 135168\n\t"
      "buffer_load_dwordx4 v9, s[84:87], s54 offen lds\n\t"
      "v_mfma_f32_16x16x32_bf16 v[32:35], a[68:71], a[232:235], v[32:35]\n\t"
      "v_cvt_pk_bf16_f32 v104, v40, v41\n\t"
      "v_cvt_pk_bf16_f32 v105, v42, v43\n\t"
      "v_mfma_f32_16x16x32_bf16 v[36:39], a[72:75], a[232:235], v[36:39]\n\t"
      "v_cvt_pk_bf16_f32 v106, v44, v45\n\t"
      "v_cvt_pk_bf16_f32 v107, v46, v47\n\t"
      "s_waitcnt lgkmcnt(9)\n\t"
      "v_mfma_f32_16x16x32_bf16 a[156:159], v[80:83], a[236:239], a[156:159]\n\t"
      "v_cvt_pk_bf16_f32 v108, v48, v49\n\t"
      "v_cvt_pk_bf16_f32 v109, v50, v51\n\t"
      "v_mfma_f32_16x16x32_bf16 a[160:163], v[84:87], a[236:239], a[160:163]\n\t"
      "v_cvt_pk_bf16_f32 v110, v52, v53\n\t"
      "v_cvt_pk_bf16_f32 v111, v54, v55\n\t"
      "ds_read_b128 a[240:243], v58 offset:25088\n\t"
      "v_mfma_f32_4x4x4_16b_bf16 v[40:43], a[180:181], a[76:77], 0\n\t"
      "ds_read_b64_tr_b16 a[84:85], v60 offset:49280\n\t"
      "v_mfma_f32_4x4x4_16b_bf16 v[44:47], a[182:183], a[78:79], 0\n\t"
      "ds_read_b64_tr_b16 a[86:87], v60 offset:50048\n\t"
      "v_mfma_f32_4x4x4_16b_bf16 v[48:51], a[180:181], a[80:81], 0\n\t"
      "ds_read_b64_tr_b16 a[88:89], v61 offset:49280\n\t"
      "v_mfma_f32_4x4x4_16b_bf16 v[52:55], a[182:183], a[82:83], 0\n\t"
      "ds_read_b64_tr_b16 a[90:91], v61 offset:50048\n\t"
      "buffer_load_dwordx4 v10, s[84:87], s54 offen offset:1024 lds\n\t"
      "buffer_load_dwordx4 v11, s[84:87], s54 offen offset:2048 lds\n\t"
      "ds_read_b128 a[244:247], v58 offset:37376\n\t"
      "v_mfma_f32_16x16x32_bf16 a[164:167], v[88:91], a[236:239], a[164:167]\n\t"
      "v_cvt_pk_bf16_f32 v40, v40, v41\n\t"
      "v_cvt_pk_bf16_f32 v41, v42, v43\n\t"
      "v_mfma_f32_16x16x32_bf16 a[168:171], v[92:95], a[236:239], a[168:171]\n\t"
      "v_cvt_pk_bf16_f32 v44, v44, v45\n\t"
      "v_cvt_pk_bf16_f32 v45, v46, v47\n\t"
      "v_mfma_f32_16x16x32_bf16 a[172:175], a[68:71], a[236:239], a[172:175]\n\t"
      "v_cvt_pk_bf16_f32 v48, v48, v49\n\t"
      "v_cvt_pk_bf16_f32 v49, v50, v51\n\t"
      "v_mfma_f32_16x16x32_bf16 a[176:179], a[72:75], a[236:239], a[176:179]\n\t"
      "v_cvt_pk_bf16_f32 v52, v52, v53\n\t"
      "v_cvt_pk_bf16_f32 v53, v54, v55\n\t"
      "v_accvgpr_write_b32 a76, v40\n\t"
      "v_accvgpr_write_b32 a77, v41\n\t"
      "v_accvgpr_write_b32 a78, v44\n\t"
      "v_accvgpr_write_b32 a79, v45\n\t"
      "v_accvgpr_write_b32 a80, v48\n\t"
      "v_accvgpr_write_b32 a81, v49\n\t"
      "v_accvgpr_write_b32 a82, v52\n\t"
      "v_accvgpr_write_b32 a83, v53\n\t"
      "s_waitcnt vmcnt(6)\n\t"
      "s_waitcnt lgkmcnt(1)\n\t"
      "; step\n\t"
      "v_mfma_f32_4x4x4_16b_bf16 v[40:43], a[184:185], v[112:113], 0\n\t"
      "ds_read_b128 a[180:183], v14 offset:1024\n\t"
      "v_mfma_f32_4x4x4_16b_bf16 v[44:47], a[186:187], v[114:115], 0\n\t"
      "ds_read_b64_tr_b16 v[128:129], v12 offset:0\n\t"
      "v_mfma_f32_4x4x4_16b_bf16 v[48:51], a[184:185], v[116:117], 0\n\t"
      "ds_read_b64_tr_b16 v[130:131], v12 offset:768\n\t"
      "v_mfma_f32_4x4x4_16b_bf16 v[52:55], a[186:187], v[118:119], 0\n\t"
      "ds_read_b64_tr_b16 v[132:133], v13 offset:0\n\t"
      "ds_read_b64_tr_b16 v[134:135], v13 offset:768\n\t"
      "s_add_u32 m0, s17, 111104\n\t"
      "s_mov_b32 s54, 147456\n\t"
      "buffer_load_dwordx4 v9, s[84:87], s54 offen lds\n\t"
      "buffer_load_dwordx4 v10, s[84:87], s54 offen offset:1024 lds\n\t"
      "v_mfma_f32_16x16x32_bf16 v[16:19], v[96:99], a[240:243], v[16:19]\n\t"
      "v_cvt_pk_bf16_f32 v112, v40, v41\n\t"
      "v_cvt_pk_bf16_f32 v113, v42, v43\n\t"
      "v_mfma_f32_16x16x32_bf16 v[20:23], v[100:103], a[240:243], v[20:23]\n\t"
      "v_cvt_pk_bf16_f32 v114, v44, v45\n\t"
      "v_cvt_pk_bf16_f32 v115, v46, v47\n\t"
      "v_mfma_f32_16x16x32_bf16 v[24:27], v[104:107], a[240:243], v[24:27]\n\t"
      "v_cvt_pk_bf16_f32 v116, v48, v49\n\t"
      "v_cvt_pk_bf16_f32 v117, v50, v51\n\t"
      "v_mfma_f32_16x16x32_bf16 v[28:31], v[108:111], a[240:243], v[28:31]\n\t"
      "v_cvt_pk_bf16_f32 v118, v52, v53\n\t"
      "v_cvt_pk_bf16_f32 v119, v54, v55\n\t"
      "v_mfma_f32_4x4x4_16b_bf16 v[40:43], a[184:185], v[120:121], 0\n\t"
      "ds_read_b64_tr_b16 v[136:137], v12 offset:64\n\t"
      "v_mfma_f32_4x4x4_16b_bf16 v[44:47], a[186:187], v[122:123], 0\n\t"
      "ds_read_b64_tr_b16 v[138:139], v12 offset:832\n\t"
      "v_mfma_f32_4x4x4_16b_bf16 v[48:51], a[184:185], v[124:125], 0\n\t"
      "ds_read_b64_tr_b16 v[140:141], v13 offset:64\n\t"
      "v_mfma_f32_4x4x4_16b_bf16 v[52:55], a[186:187], v[126:127], 0\n\t"
      "ds_read_b64_tr_b16 v[142:143], v13 offset:832\n\t"
      "buffer_load_dwordx4 v11, s[84:87], s54 offen offset:2048 lds\n\t"
      "s_add_u32 m0, s17, 114176\n\t"
      "s_mov_b32 s54, 159744\n\t"
      "buffer_load_dwordx4 v9, s[84:87], s54 offen lds\n\t"
      "v_mfma_f32_16x16x32_bf16 v[32:35], a[76:79], a[240:243], v[32:35]\n\t"
      "v_cvt_pk_bf16_f32 v120, v40, v41\n\t"
      "v_cvt_pk_bf16_f32 v121, v42, v43\n\t"
      "v_mfma_f32_16x16x32_bf16 v[36:39], a[80:83], a[240:243], v[36:39]\n\t"
      "v_cvt_pk_bf16_f32 v122, v44, v45\n\t"
      "v_cvt_pk_bf16_f32 v123, v46, v47\n\t"
      "s_waitcnt lgkmcnt(9)\n\t"
      "v_mfma_f32_16x16x32_bf16 a[156:159], v[96:99], a[244:247], a[156:159]\n\t"
      "v_cvt_pk_bf16_f32 v124, v48, v49\n\t"
      "v_cvt_pk_bf16_f32 v125, v50, v51\n\t"
      "v_mfma_f32_16x16x32_bf16 a[160:163], v[100:103], a[244:247], a[160:163]\n\t"
      "v_cvt_pk_bf16_f32 v126, v52, v53\n\t"
      "v_cvt_pk_bf16_f32 v127, v54, v55\n\t"
      "ds_read_b128 a[224:227], v59 offset:25088\n\t"
      "v_mfma_f32_4x4x4_16b_bf16 v[40:43], a[184:185], a[84:85], 0\n\t"
      "ds_read_b64_tr_b16 a[92:93], v12 offset:128\n\t"
      "v_mfma_f32_4x4x4_16b_bf16 v[44:47], a[186:187], a[86:87], 0\n\t"
      "ds_read_b64_tr_b16 a[94:95], v12 offset:896\n\t"
      "v_mfma_f32_4x4x4_16b_bf16 v[48:51], a[184:185], a[88:89], 0\n\t"
      "ds_read_b64_tr_b16 a[96:97], v13 offset:128\n\t"
      "v_mfma_f32_4x4x4_16b_bf16 v[52:55], a[186:187], a[90:91], 0\n\t"
      "ds_read_b64_tr_b16 a[98:99], v13 offset:896\n\t"
      "buffer_load_dwordx4 v10, s[84:87], s54 offen offset:1024 lds\n\t"
      "buffer_load_dwordx4 v11, s[84:87], s54 offen offset:2048 lds\n\t"
      "ds_read_b128 a[228:231], v59 offset:37376\n\t"
      "s_add_u32 m0, s17, 135680\n\t"
      "s_mov_b32 s54, 172032\n\t"
      "buffer_load_dwordx4 v9, s[84:87], s54 offen lds\n\t"
      "buffer_load_dwordx4 v10, s[84:87], s54 offen offset:1024 lds\n\t"
      "buffer_load_dwordx4 v11, s[84:87], s54 offen offset:2048 lds\n\t"
      "s_add_u32 m0, s17, 138752\n\t"
      "s_mov_b32 s54, 184320\n\t"
      "buffer_load_dwordx4 v9, s[84:87], s54 offen lds\n\t"
      "buffer_load_dwordx4 v10, s[84:87], s54 offen offset:1024 lds\n\t"
      "buffer_load_dwordx4 v11, s[84:87], s54 offen offset:2048 lds\n\t"
      "v_mfma_f32_16x16x32_bf16 a[164:167], v[104:107], a[244:247], a[164:167]\n\t"
      "v_cvt_pk_bf16_f32 v40, v40, v41\n\t"
      "v_cvt_pk_bf16_f32 v41, v42, v43\n\t"
      "v_mfma_f32_16x16x32_bf16 a[168:171], v[108:111], a[244:247], a[168:171]\n\t"
      "v_cvt_pk_bf16_f32 v44, v44, v45\n\t"
      "v_cvt_pk_bf16_f32 v45, v46, v47\n\t"
      "v_mfma_f32_16x16x32_bf16 a[172:175], a[76:79], a[244:247], a[172:175]\n\t"
      "v_cvt_pk_bf16_f32 v48, v48, v49\n\t"
      "v_cvt_pk_bf16_f32 v49, v50, v51\n\t"
      "v_mfma_f32_16x16x32_bf16 a[176:179], a[80:83], a[244:247], a[176:179]\n\t"
      "v_cvt_pk_bf16_f32 v52, v52, v53\n\t"
      "v_cvt_pk_bf16_f32 v53, v54, v55\n\t"
      "v_accvgpr_write_b32 a84, v40\n\t"
      "v_accvgpr_write_b32 a85, v41\n\t"
      "v_accvgpr_write_b32 a86, v44\n\t"
      "v_accvgpr_write_b32 a87, v45\n\t"
      "v_accvgpr_write_b32 a88, v48\n\t"
      "v_accvgpr_write_b32 a89, v49\n\t"
      "v_accvgpr_write_b32 a90, v52\n\t"
      "v_accvgpr_write_b32 a91, v53\n\t"
      "s_waitcnt vmcnt(12)\n\t"
      "s_waitcnt lgkmcnt(1)\n\t"
      "; step\n\t"
      "v_mfma_f32_4x4x4_16b_bf16 v[40:43], a[180:181], v[128:129], 0\n\t"
      "ds_read_b128 a[184:187], v14 offset:1280\n\t"
      "v_mfma_f32_4x4x4_16b_bf16 v[44:47], a[182:183], v[130:131], 0\n\t"
      "ds_read_b64_tr_b16 v[144:145], v60 offset:0\n\t"
      "v_mfma_f32_4x4x4_16b_bf16 v[48:51], a[180:181], v[132:133], 0\n\t"
      "ds_read_b64_tr_b16 v[146:147], v60 offset:768\n\t"
      "v_mfma_f32_4x4x4_16b_bf16 v[52:55], a[182:183], v[134:135], 0\n\t"
      "ds_read_b64_tr_b16 v[148:149], v61 offset:0\n\t"
      "ds_read_b64_tr_b16 v[150:151], v61 offset:768\n\t"
      "s_add_u32 m0, s17, 0\n\t"
      "s_mov_b32 s54, 196608\n\t"
      "buffer_load_dwordx4 v9, s[84:87], s54 offen lds\n\t"
      "buffer_load_dwordx4 v10, s[84:87], s54 offen offset:1024 lds\n\t"
      "v_mfma_f32_16x16x32_bf16 v[16:19], v[112:115], a[224:227], v[16:19]\n\t"
      "v_cvt_pk_bf16_f32 v128, v40, v41\n\t"
      "v_cvt_pk_bf16_f32 v129, v42, v43\n\t"
      "v_mfma_f32_16x16x32_bf16 v[20:23], v[116:119], a[224:227], v[20:23]\n\t"
      "v_cvt_pk_bf16_f32 v130, v44, v45\n\t"
      "v_cvt_pk_bf16_f32 v131, v46, v47\n\t"
      "v_mfma_f32_16x16x32_bf16 v[24:27], v[120:123], a[224:227], v[24:27]\n\t"
      "v_cvt_pk_bf16_f32 v132, v48, v49\n\t"
      "v_cvt_pk_bf16_f32 v133, v50, v51\n\t"
      "v_mfma_f32_16x16x32_bf16 v[28:31], v[124:127], a[224:227], v[28:31]\n\t"
      "v_cvt_pk_bf16_f32 v134, v52, v53\n\t"
      "v_cvt_pk_bf16_f32 v135, v54, v55\n\t"
      "v_mfma_f32_4x4x4_16b_bf16 v[40:43], a[180:181], v[136:137], 0\n\t"
      "ds_read_b64_tr_b16 v[152:153], v60 offset:64\n\t"
      "v_mfma_f32_4x4x4_16b_bf16 v[44:47], a[182:183], v[138:139], 0\n\t"
      "ds_read_b64_tr_b16 v[154:155], v60 offset:832\n\t"
      "v_mfma_f32_4x4x4_16b_bf16 v[48:51], a[180:181], v[140:141], 0\n\t"
      "ds_read_b64_tr_b16 v[156:157], v61 offset:64\n\t"
      "v_mfma_f32_4x4x4_16b_bf16 v[52:55], a[182:183], v[142:143], 0\n\t"
      "ds_read_b64_tr_b16 v[158:159], v61 offset:832\n\t"
      "buffer_load_dwordx4 v11, s[84:87], s54 offen offset:2048 lds\n\t"
      "s_add_u32 m0, s17, 3072\n\t"
      "s_mov_b32 s54, 208896\n\t"
      "buffer_load_dwordx4 v9, s[84:87], s54 offen lds\n\t"
      "v_mfma_f32_16x16x32_bf16 v[32:35], a[84:87], a[224:227], v[32:35]\n\t"
      "v_cvt_pk_bf16_f32 v136, v40, v41\n\t"
      "v_cvt_pk_bf16_f32 v137, v42, v43\n\t"
      "v_mfma_f32_16x16x32_bf16 v[36:39], a[88:91], a[224:227], v[36:39]\n\t"
      "v_cvt_pk_bf16_f32 v138, v44, v45\n\t"
      "v_cvt_pk_bf16_f32 v139, v46, v47\n\t"
      "s_waitcnt lgkmcnt(9)\n\t"
      "v_mfma_f32_16x16x32_bf16 a[156:159], v[112:115], a[228:231], a[156:159]\n\t"
      "v_cvt_pk_bf16_f32 v140, v48, v49\n\t"
      "v_cvt_pk_bf16_f32 v141, v50, v51\n\t"
      "v_mfma_f32_16x16x32_bf16 a[160:163], v[116:119], a[228:231], a[160:163]\n\t"
      "v_cvt_pk_bf16_f32 v142, v52, v53\n\t"
      "v_cvt_pk_bf16_f32 v143, v54, v55\n\t"
      "ds_read_b128 a[232:235], v56 offset:25344\n\t"
      "v_mfma_f32_4x4x4_16b_bf16 v[40:43], a[180:181], a[92:93], 0\n\t"
      "ds_read_b64_tr_b16 a[100:101], v60 offset:128\n\t"
      "v_mfma_f32_4x4x4_16b_bf16 v[44:47], a[182:183], a[94:95], 0\n\t"
      "ds_read_b64_tr_b16 a[102:103], v60 offset:896\n\t"
      "v_mfma_f32_4x4x4_16b_bf16 v[48:51], a[180:181], a[96:97], 0\n\t"
      "ds_read_b64_tr_b16 a[104:105], v61 offset:128\n\t"
      "v_mfma_f32_4x4x4_16b_bf16 v[52:55], a[182:183], a[98:99], 0\n\t"
      "ds_read_b64_tr_b16 a[106:107], v61 offset:896\n\t"
      "buffer_load_dwordx4 v10, s[84:87], s54 offen offset:1024 lds\n\t"
      "buffer_load_dwordx4 v11, s[84:87], s54 offen offset:2048 lds\n\t"
      "ds_read_b128 a[236:239], v56 offset:37632\n\t"
      "v_mfma_f32_16x16x32_bf16 a[164:167], v[120:123], a[228:231], a[164:167]\n\t"
      "v_cvt_pk_bf16_f32 v40, v40, v41\n\t"
      "v_cvt_pk_bf16_f32 v41, v42, v43\n\t"
      "v_mfma_f32_16x16x32_bf16 a[168:171], v[124:127], a[228:231], a[168:171]\n\t"
      "v_cvt_pk_bf16_f32 v44, v44, v45\n\t"
      "v_cvt_pk_bf16_f32 v45, v46, v47\n\t"
      "v_mfma_f32_16x16x32_bf16 a[172:175], a[84:87], a[228:231], a[172:175]\n\t"
      "v_cvt_pk_bf16_f32 v48, v48, v49\n\t"
      "v_cvt_pk_bf16_f32 v49, v50, v51\n\t"
      "v_mfma_f32_16x16x32_bf16 a[176:179], a[88:91], a[228:231], a[176:179]\n\t"
      "v_cvt_pk_bf16_f32 v52, v52, v53\n\t"
      "v_cvt_pk_bf16_f32 v53, v54, v55\n\t"
      "v_accvgpr_write_b32 a92, v40\n\t"
      "v_accvgpr_write_b32 a93, v41\n\t"
      "v_accvgpr_write_b32 a94, v44\n\t"
      "v_accvgpr_write_b32 a95, v45\n\t"
      "v_accvgpr_write_b32 a96, v48\n\t"
      "v_accvgpr_write_b32 a97, v49\n\t"
      "v_accvgpr_write_b32 a98, v52\n\t"
      "v_accvgpr_write_b32 a99, v53\n\t"
      "s_waitcnt vmcnt(12)\n\t"
      "s_waitcnt lgkmcnt(1)\n\t"
      "; step\n\t"
      "v_mfma_f32_4x4x4_16b_bf16 v[40:43], a[184:185], v[144:145], 0\n\t"
      "ds_read_b128 a[180:183], v14 offset:1536\n\t"
      "v_mfma_f32_4x4x4_16b_bf16 v[44:47], a[186:187], v[146:147], 0\n\t"
      "ds_read_b64_tr_b16 v[160:161], v60 offset:24576\n\t"
      "v_mfma_f32_4x4x4_16b_bf16 v[48:51], a[184:185], v[148:149], 0\n\t"
      "ds_read_b64_tr_b16 v[162:163], v60 offset:25344\n\t"
      "v_mfma_f32_4x4x4_16b_bf16 v[52:55], a[186:187], v[150:151], 0\n\t"
      "ds_read_b64_tr_b16 v[164:165], v61 offset:24576\n\t"
      "ds_read_b64_tr_b16 v[166:167], v61 offset:25344\n\t"
      "s_add_u32 m0, s17, 86528\n\t"
      "s_mov_b32 s54, 221184\n\t"
      "buffer_load_dwordx4 v9, s[84:87], s54 offen lds\n\t"
      "buffer_load_dwordx4 v10, s[84:87], s54 offen offset:1024 lds\n\t"
      "v_mfma_f32_16x16x32_bf16 v[16:19], v[128:131], a[232:235], v[16:19]\n\t"
      "v_cvt_pk_bf16_f32 v144, v40, v41\n\t"
      "v_cvt_pk_bf16_f32 v145, v42, v43\n\t"
      "v_mfma_f32_16x16x32_bf16 v[20:23], v[132:135], a[232:235], v[20:23]\n\t"
      "v_cvt_pk_bf16_f32 v146, v44, v45\n\t"
      "v_cvt_pk_bf16_f32 v147, v46, v47\n\t"
      "v_mfma_f32_16x16x32_bf16 v[24:27], v[136:139], a[232:235], v[24:27]\n\t"
      "v_cvt_pk_bf16_f32 v148, v48, v49\n\t"
      "v_cvt_pk_bf16_f32 v149, v50, v51\n\t"
      "v_mfma_f32_16x16x32_bf16 v[28:31], v[140:143], a[232:235], v[28:31]\n\t"
      "v_cvt_pk_bf16_f32 v150, v52, v53\n\t"
      "v_cvt_pk_bf16_f32 v151, v54, v55\n\t"
      "v_mfma_f32_4x4x4_16b_bf16 v[40:43], a[184:185], v[152:153], 0\n\t"
      "ds_read_b64_tr_b16 v[168:169], v60 offset:24640\n\t"
      "v_mfma_f32_4x4x4_16b_bf16 v[44:47], a[186:187], v[154:155], 0\n\t"
      "ds_read_b64_tr_b16 v[170:171], v60 offset:25408\n\t"
      "v_mfma_f32_4x4x4_16b_bf16 v[48:51], a[184:185], v[156:157], 0\n\t"
      "ds_read_b64_tr_b16 v[172:173], v61 offset:24640\n\t"
      "v_mfma_f32_4x4x4_16b_bf16 v[52:55], a[186:187], v[158:159], 0\n\t"
      "ds_read_b64_tr_b16 v[174:175], v61 offset:25408\n\t"
      "buffer_load_dwordx4 v11, s[84:87], s54 offen offset:2048 lds\n\t"
      "s_add_u32 m0, s17, 89600\n\t"
      "s_mov_b32 s54, 233472\n\t"
      "buffer_load_dwordx4 v9, s[84:87], s54 offen lds\n\t"
      "v_mfma_f32_16x16x32_bf16 v[32:35], a[92:95], a[232:235], v[32:35]\n\t"
      "v_cvt_pk_bf16_f32 v152, v40, v41\n\t"
      "v_cvt_pk_bf16_f32 v153, v42, v43\n\t"
      "v_mfma_f32_16x16x32_bf16 v[36:39], a[96:99], a[232:235], v[36:39]\n\t"
      "v_cvt_pk_bf16_f32 v154, v44, v45\n\t"
      "v_cvt_pk_bf16_f32 v155, v46, v47\n\t"
      "s_waitcnt lgkmcnt(9)\n\t"
      "v_mfma_f32_16x16x32_bf16 a[156:159], v[128:131], a[236:239], a[156:159]\n\t"
      "v_cvt_pk_bf16_f32 v156, v48, v49\n\t"
      "v_cvt_pk_bf16_f32 v157, v50, v51\n\t"
      "v_mfma_f32_16x16x32_bf16 a[160:163], v[132:135], a[236:239], a[160:163]\n\t"
      "v_cvt_pk_bf16_f32 v158, v52, v53\n\t"
      "v_cvt_pk_bf16_f32 v159, v54, v55\n\t"
      "ds_read_b128 a[240:243], v57 offset:25344\n\t"
      "v_mfma_f32_4x4x4_16b_bf16 v[40:43], a[184:185], a[100:101], 0\n\t"
      "ds_read_b64_tr_b16 a[108:109], v60 offset:24704\n\t"
      "v_mfma_f32_4x4x4_16b_bf16 v[44:47], a[186:187], a[102:103], 0\n\t"
      "ds_read_b64_tr_b16 a[110:111], v60 offset:25472\n\t"
      "v_mfma_f32_4x4x4_16b_bf16 v[48:51], a[184:185], a[104:105], 0\n\t"
      "ds_read_b64_tr_b16 a[112:113], v61 offset:24704\n\t"
      "v_mfma_f32_4x4x4_16b_bf16 v[52:55], a[186:187], a[106:107], 0\n\t"
      "ds_read_b64_tr_b16 a[114:115], v61 offset:25472\n\t"
      "buffer_load_dwordx4 v10, s[84:87], s54 offen offset:1024 lds\n\t"
      "buffer_load_dwordx4 v11, s[84:87], s54 offen offset:2048 lds\n\t"
      "ds_read_b128 a[244:247], v57 offset:37632\n\t"
      "v_mfma_f32_16x16x32_bf16 a[164:167], v[136:139], a[236:239], a[164:167]\n\t"
      "v_cvt_pk_bf16_f32 v40, v40, v41\n\t"
      "v_cvt_pk_bf16_f32 v41, v42, v43\n\t"
      "v_mfma_f32_16x16x32_bf16 a[168:171], v[140:143], a[236:239], a[168:171]\n\t"
      "v_cvt_pk_bf16_f32 v44, v44, v45\n\t"
      "v_cvt_pk_bf16_f32 v45, v46, v47\n\t"
      "v_mfma_f32_16x16x32_bf16 a[172:175], a[92:95], a[236:239], a[172:175]\n\t"
      "v_cvt_pk_bf16_f32 v48, v48, v49\n\t"
      "v_cvt_pk_bf16_f32 v49, v50, v51\n\t"
      "v_mfma_f32_16x16x32_bf16 a[176:179], a[96:99], a[236:239], a[176:179]\n\t"
      "v_cvt_pk_bf16_f32 v52, v52, v53\n\t"
      "v_cvt_pk_bf16_f32 v53, v54, v55\n\t"
      "v_accvgpr_write_b32 a100, v40\n\t"
      "v_accvgpr_write_b32 a101, v41\n\t"
      "v_accvgpr_write_b32 a102, v44\n\t"
      "v_accvgpr_write_b32 a103, v45\n\t"
      "v_accvgpr_write_b32 a104, v48\n\t"
      "v_accvgpr_write_b32 a105, v49\n\t"
      "v_accvgpr_write_b32 a106, v52\n\t"
      "v_accvgpr_write_b32 a107, v53\n\t"
      "s_waitcnt vmcnt(12)\n\t"
      "s_waitcnt lgkmcnt(1)\n\t"
      "; step\n\t"
      "v_mfma_f32_4x4x4_16b_bf16 v[40:43], a[180:181], v[160:161], 0\n\t"
      "ds_read_b128 a[184:187], v14 offset:1792\n\t"
      "v_mfma_f32_4x4x4_16b_bf16 v[44:47], a[182:183], v[162:163], 0\n\t"
      "ds_read_b64_tr_b16 v[176:177], v60 offset:49152\n\t"
      "v_mfma_f32_4x4x4_16b_bf16 v[48:51], a[180:181], v[164:165], 0\n\t"
      "ds_read_b64_tr_b16 v[178:179], v60 offset:49920\n\t"
      "v_mfma_f32_4x4x4_16b_bf16 v[52:55], a[182:183], v[166:167], 0\n\t"
      "ds_read_b64_tr_b16 v[180:181], v61 offset:49152\n\t"
      "ds_read_b64_tr_b16 v[182:183], v61 offset:49920\n\t"
      "s_add_u32 m0, s17, 111104\n\t"
      "s_mov_b32 s54, 245760\n\t"
      "buffer_load_dwordx4 v9, s[84:87], s54 offen lds\n\t"
      "buffer_load_dwordx4 v10, s[84:87], s54 offen offset:1024 lds\n\t"
      "v_mfma_f32_16x16x32_bf16 v[16:19], v[144:147], a[240:243], v[16:19]\n\t"
      "v_cvt_pk_bf16_f32 v160, v40, v41\n\t"
      "v_cvt_pk_bf16_f32 v161, v42, v43\n\t"
      "v_mfma_f32_16x16x32_bf16 v[20:23], v[148:151], a[240:243], v[20:23]\n\t"
      "v_cvt_pk_bf16_f32 v162, v44, v45\n\t"
      "v_cvt_pk_bf16_f32 v163, v46, v47\n\t"
      "v_mfma_f32_16x16x32_bf16 v[24:27], v[152:155], a[240:243], v[24:27]\n\t"
      "v_cvt_pk_bf16_f32 v164, v48, v49\n\t"
      "v_cvt_pk_bf16_f32 v165, v50, v51\n\t"
      "v_mfma_f32_16x16x32_bf16 v[28:31], v[156:159], a[240:243], v[28:31]\n\t"
      "v_cvt_pk_bf16_f32 v166, v52, v53\n\t"
      "v_cvt_pk_bf16_f32 v167, v54, v55\n\t"
      "v_mfma_f32_4x4x4_16b_bf16 v[40:43], a[180:181], v[168:169], 0\n\t"
      "ds_read_b64_tr_b16 v[184:185], v60 offset:49216\n\t"
      "v_mfma_f32_4x4x4_16b_bf16 v[44:47], a[182:183], v[170:171], 0\n\t"
      "ds_read_b64_tr_b16 v[186:187], v60 offset:49984\n\t"
      "v_mfma_f32_4x4x4_16b_bf16 v[48:51], a[180:181], v[172:173], 0\n\t"
      "ds_read_b64_tr_b16 v[188:189], v61 offset:49216\n\t"
      "v_mfma_f32_4x4x4_16b_bf16 v[52:55], a[182:183], v[174:175], 0\n\t"
      "ds_read_b64_tr_b16 v[190:191], v61 offset:49984\n\t"
      "buffer_load_dwordx4 v11, s[84:87], s54 offen offset:2048 lds\n\t"
      "s_add_u32 m0, s17, 114176\n\t"
      "s_mov_b32 s54, 258048\n\t"
      "buffer_load_dwordx4 v9, s[84:87], s54 offen lds\n\t"
      "v_mfma_f32_16x16x32_bf16 v[32:35], a[100:103], a[240:243], v[32:35]\n\t"
      "v_cvt_pk_bf16_f32 v168, v40, v41\n\t"
      "v_cvt_pk_bf16_f32 v169, v42, v43\n\t"
      "v_mfma_f32_16x16x32_bf16 v[36:39], a[104:107], a[240:243], v[36:39]\n\t"
      "v_cvt_pk_bf16_f32 v170, v44, v45\n\t"
      "v_cvt_pk_bf16_f32 v171, v46, v47\n\t"
      "s_waitcnt lgkmcnt(9)\n\t"
      "v_mfma_f32_16x16x32_bf16 a[156:159], v[144:147], a[244:247], a[156:159]\n\t"
      "v_cvt_pk_bf16_f32 v172, v48, v49\n\t"
      "v_cvt_pk_bf16_f32 v173, v50, v51\n\t"
      "v_mfma_f32_16x16x32_bf16 a[160:163], v[148:151], a[244:247], a[160:163]\n\t"
      "v_cvt_pk_bf16_f32 v174, v52, v53\n\t"
      "v_cvt_pk_bf16_f32 v175, v54, v55\n\t"
      "ds_read_b128 a[224:227], v58 offset:25344\n\t"
      "v_mfma_f32_4x4x4_16b_bf16 v[40:43], a[180:181], a[108:109], 0\n\t"
      "ds_read_b64_tr_b16 a[116:117], v60 offset:49280\n\t"
      "v_mfma_f32_4x4x4_16b_bf16 v[44:47], a[182:183], a[110:111], 0\n\t"
      "ds_read_b64_tr_b16 a[118:119], v60 offset:50048\n\t"
      "v_mfma_f32_4x4x4_16b_bf16 v[48:51], a[180:181], a[112:113], 0\n\t"
      "ds_read_b64_tr_b16 a[120:121], v61 offset:49280\n\t"
      "v_mfma_f32_4x4x4_16b_bf16 v[52:55], a[182:183], a[114:115], 0\n\t"
      "ds_read_b64_tr_b16 a[122:123], v61 offset:50048\n\t"
      "buffer_load_dwordx4 v10, s[84:87], s54 offen offset:1024 lds\n\t"
      "buffer_load_dwordx4 v11, s[84:87], s54 offen offset:2048 lds\n\t"
      "ds_read_b128 a[228:231], v58 offset:37632\n\t"
      "v_mfma_f32_16x16x32_bf16 a[164:167], v[152:155], a[244:247], a[164:167]\n\t"
      "v_cvt_pk_bf16_f32 v40, v40, v41\n\t"
      "v_cvt_pk_bf16_f32 v41, v42, v43\n\t"
      "v_mfma_f32_16x16x32_bf16 a[168:171], v[156:159], a[244:247], a[168:171]\n\t"
      "v_cvt_pk_bf16_f32 v44, v44, v45\n\t"
      "v_cvt_pk_bf16_f32 v45, v46, v47\n\t"
      "v_mfma_f32_16x16x32_bf16 a[172:175], a[100:103], a[244:247], a[172:175]\n\t"
      "v_cvt_pk_bf16_f32 v48, v48, v49\n\t"
      "v_cvt_pk_bf16_f32 v49, v50, v51\n\t"
      "v_mfma_f32_16x16x32_bf16 a[176:179], a[104:107], a[244:247], a[176:179]\n\t"
      "v_cvt_pk_bf16_f32 v52, v52, v53\n\t"
      "v_cvt_pk_bf16_f32 v53, v54, v55\n\t"
      "v_accvgpr_write_b32 a108, v40\n\t"
      "v_accvgpr_write_b32 a109, v41\n\t"
      "v_accvgpr_write_b32 a110, v44\n\t"
      "v_accvgpr_write_b32 a111, v45\n\t"
      "v_accvgpr_write_b32 a112, v48\n\t"
      "v_accvgpr_write_b32 a113, v49\n\t"
      "v_accvgpr_write_b32 a114, v52\n\t"
      "v_accvgpr_write_b32 a115, v53\n\t"
      "s_waitcnt vmcnt(12)\n\t"
      "s_waitcnt lgkmcnt(1)\n\t"
      "; step\n\t"
      "v_mfma_f32_4x4x4_16b_bf16 v[40:43], a[184:185], v[176:177], 0\n\t"
      "ds_read_b128 a[180:183], v14 offset:2048\n\t"
      "v_mfma_f32_4x4x4_16b_bf16 v[44:47], a[186:187], v[178:179], 0\n\t"
      "ds_read_b64_tr_b16 v[192:193], v12 offset:0\n\t"
      "v_mfma_f32_4x4x4_16b_bf16 v[48:51], a[184:185], v[180:181], 0\n\t"
      "ds_read_b64_tr_b16 v[194:195], v12 offset:768\n\t"
      "v_mfma_f32_4x4x4_16b_bf16 v[52:55], a[186:187], v[182:183], 0\n\t"
      "ds_read_b64_tr_b16 v[196:197], v13 offset:0\n\t"
      "ds_read_b64_tr_b16 v[198:199], v13 offset:768\n\t"
      "s_add_u32 m0, s17, 135680\n\t"
      "s_mov_b32 s54, 270336\n\t"
      "buffer_load_dwordx4 v9, s[84:87], s54 offen lds\n\t"
      "buffer_load_dwordx4 v10, s[84:87], s54 offen offset:1024 lds\n\t"
      "v_mfma_f32_16x16x32_bf16 v[16:19], v[160:163], a[224:227], v[16:19]\n\t"
      "v_cvt_pk_bf16_f32 v176, v40, v41\n\t"
      "v_cvt_pk_bf16_f32 v177, v42, v43\n\t"
      "v_mfma_f32_16x16x32_bf16 v[20:23], v[164:167], a[224:227], v[20:23]\n\t"
      "v_cvt_pk_bf16_f32 v178, v44, v45\n\t"
      "v_cvt_pk_bf16_f32 v179, v46, v47\n\t"
      "v_mfma_f32_16x16x32_bf16 v[24:27], v[168:171], a[224:227], v[24:27]\n\t"
      "v_cvt_pk_bf16_f32 v180, v48, v49\n\t"
      "v_cvt_pk_bf16_f32 v181, v50, v51\n\t"
      "v_mfma_f32_16x16x32_bf16 v[28:31], v[172:175], a[224:227], v[28:31]\n\t"
      "v_cvt_pk_bf16_f32 v182, v52, v53\n\t"
      "v_cvt_pk_bf16_f32 v183, v54, v55\n\t"
      "v_mfma_f32_4x4x4_16b_bf16 v[40:43], a[184:185], v[184:185], 0\n\t"
      "ds_read_b64_tr_b16 v[200:201], v12 offset:64\n\t"
      "v_mfma_f32_4x4x4_16b_bf16 v[44:47], a[186:187], v[186:187], 0\n\t"
      "ds_read_b64_tr_b16 v[202:203], v12 offset:832\n\t"
      "v_mfma_f32_4x4x4_16b_bf16 v[48:51], a[184:185], v[188:189], 0\n\t"
      "ds_read_b64_tr_b16 v[204:205], v13 offset:64\n\t"
      "v_mfma_f32_4x4x4_16b_bf16 v[52:55], a[186:187], v[190:191], 0\n\t"
      "ds_read_b64_tr_b16 v[206:207], v13 offset:832\n\t"
      "buffer_load_dwordx4 v11, s[84:87], s54 offen offset:2048 lds\n\t"
      "s_add_u32 m0, s17, 138752\n\t"
      "s_mov_b32 s54, 282624\n\t"
      "buffer_load_dwordx4 v9, s[84:87], s54 offen lds\n\t"
      "v_mfma_f32_16x16x32_bf16 v[32:35], a[108:111], a[224:227], v[32:35]\n\t"
      "v_cvt_pk_bf16_f32 v184, v40, v41\n\t"
      "v_cvt_pk_bf16_f32 v185, v42, v43\n\t"
      "v_mfma_f32_16x16x32_bf16 v[36:39], a[112:115], a[224:227], v[36:39]\n\t"
      "v_cvt_pk_bf16_f32 v186, v44, v45\n\t"
      "v_cvt_pk_bf16_f32 v187, v46, v47\n\t"
      "s_waitcnt lgkmcnt(9)\n\t"
      "v_mfma_f32_16x16x32_bf16 a[156:159], v[160:163], a[228:231], a[156:159]\n\t"
      "v_cvt_pk_bf16_f32 v188, v48, v49\n\t"
      "v_cvt_pk_bf16_f32 v189, v50, v51\n\t"
      "v_mfma_f32_16x16x32_bf16 a[160:163], v[164:167], a[228:231], a[160:163]\n\t"
      "v_cvt_pk_bf16_f32 v190, v52, v53\n\t"
      "v_cvt_pk_bf16_f32 v191, v54, v55\n\t"
      "ds_read_b128 a[232:235], v59 offset:25344\n\t"
      "v_mfma_f32_4x4x4_16b_bf16 v[40:43], a[184:185], a[116:117], 0\n\t"
      "ds_read_b64_tr_b16 a[124:125], v12 offset:128\n\t"
      "v_mfma_f32_4x4x4_16b_bf16 v[44:47], a[186:187], a[118:119], 0\n\t"
      "ds_read_b64_tr_b16 a[126:127], v12 offset:896\n\t"
      "v_mfma_f32_4x4x4_16b_bf16 v[48:51], a[184:185], a[120:121], 0\n\t"
      "ds_read_b64_tr_b16 a[128:129], v13 offset:128\n\t"
      "v_mfma_f32_4x4x4_16b_bf16 v[52:55], a[186:187], a[122:123], 0\n\t"
      "ds_read_b64_tr_b16 a[130:131], v13 offset:896\n\t"
      "buffer_load_dwordx4 v10, s[84:87], s54 offen offset:1024 lds\n\t"
      "buffer_load_dwordx4 v11, s[84:87], s54 offen offset:2048 lds\n\t"
      "ds_read_b128 a[236:239], v59 offset:37632\n\t"
      "s_or_b32 s30, s17, s16\n\t"
      "s_cbranch_scc1 .Lno3_%=\n\t"
      "global_load_dword a252, v62, s[12:13] sc1\n\t"
      ".Lno3_%=:\n\t"
      "s_add_u32 m0, s18, 49664\n\t"
      "s_add_u32 s48, s15, 786432\n\t"
      "buffer_load_dwordx4 v2, s[40:43], s48 offen nt lds\n\t"
      "buffer_load_dwordx4 v3, s[40:43], s48 offen offset:1024 nt lds\n\t"
      "buffer_load_dwordx4 v4, s[40:43], s48 offen offset:2048 nt lds\n\t"
      "v_mfma_f32_16x16x32_bf16 a[164:167], v[168:171], a[228:231], a[164:167]\n\t"
      "v_cvt_pk_bf16_f32 v40, v40, v41\n\t"
      "v_cvt_pk_bf16_f32 v41, v42, v43\n\t"
      "v_mfma_f32_16x16x32_bf16 a[168:171], v[172:175], a[228:231], a[168:171]\n\t"
      "v_cvt_pk_bf16_f32 v44, v44, v45\n\t"
      "v_cvt_pk_bf16_f32 v45, v46, v47\n\t"
      "v_mfma_f32_16x16x32_bf16 a[172:175], a[108:111], a[228:231], a[172:175]\n\t"
      "v_cvt_pk_bf16_f32 v48, v48, v49\n\t"
      "v_cvt_pk_bf16_f32 v49, v50, v51\n\t"
      "v_mfma_f32_16x16x32_bf16 a[176:179], a[112:115], a[228:231], a[176:179]\n\t"
      "v_cvt_pk_bf16_f32 v52, v52, v53\n\t"
      "v_cvt_pk_bf16_f32 v53, v54, v55\n\t"
      "v_accvgpr_write_b32 a116, v40\n\t"
      "v_accvgpr_write_b32 a117, v41\n\t"
      "v_accvgpr_write_b32 a118, v44\n\t"
      "v_accvgpr_write_b32 a119, v45\n\t"
      "v_accvgpr_write_b32 a120, v48\n\t"
      "v_accvgpr_write_b32 a121, v49\n\t"
      "v_accvgpr_write_b32 a122, v52\n\t"
      "v_accvgpr_write_b32 a123, v53\n\t"
      "s_waitcnt vmcnt(15)\n\t"
      "s_waitcnt lgkmcnt(1)\n\t"
      "; step\n\t"
      "v_mfma_f32_4x4x4_16b_bf16 v[40:43], a[180:181], v[192:193], 0\n\t"
      "ds_read_b128 a[184:187], v14 offset:2304\n\t"
      "v_mfma_f32_4x4x4_16b_bf16 v[44:47], a[182:183], v[194:195], 0\n\t"
      "ds_read_b64_tr_b16 v[208:209], v60 offset:0\n\t"
      "v_mfma_f32_4x4x4_16b_bf16 v[48:51], a[180:181], v[196:197], 0\n\t"
      "ds_read_b64_tr_b16 v[210:211], v60 offset:768\n\t"
      "v_mfma_f32_4x4x4_16b_bf16 v[52:55], a[182:183], v[198:199], 0\n\t"
      "ds_read_b64_tr_b16 v[212:213], v61 offset:0\n\t"
      "ds_read_b64_tr_b16 v[214:215], v61 offset:768\n\t"
      "s_add_u32 m0, s18, 61952\n\t"
      "s_add_u32 s48, s15, 786432\n\t"
      "buffer_load_dwordx4 v5, s[40:43], s48 offen nt lds\n\t"
      "buffer_load_dwordx4 v6, s[40:43], s48 offen offset:1024 nt lds\n\t"
      "v_mfma_f32_16x16x32_bf16 v[16:19], v[176:179], a[232:235], v[16:19]\n\t"
      "v_cvt_pk_bf16_f32 v192, v40, v41\n\t"
      "v_cvt_pk_bf16_f32 v193, v42, v43\n\t"
      "v_mfma_f32_16x16x32_bf16 v[20:23], v[180:183], a[232:235], v[20:23]\n\t"
      "v_cvt_pk_bf16_f32 v194, v44, v45\n\t"
      "v_cvt_pk_bf16_f32 v195, v46, v47\n\t"
      "v_mfma_f32_16x16x32_bf16 v[24:27], v[184:187], a[232:235], v[24:27]\n\t"
      "v_cvt_pk_bf16_f32 v196, v48, v49\n\t"
      "v_cvt_pk_bf16_f32 v197, v50, v51\n\t"
      "v_mfma_f32_16x16x32_bf16 v[28:31], v[188:191], a[232:235], v[28:31]\n\t"
      "v_cvt_pk_bf16_f32 v198, v52, v53\n\t"
      "v_cvt_pk_bf16_f32 v199, v54, v55\n\t"
      "v_mfma_f32_4x4x4_16b_bf16 v[40:43], a[180:181], v[200:201], 0\n\t"
      "ds_read_b64_tr_b16 v[216:217], v60 offset:64\n\t"
      "v_mfma_f32_4x4x4_16b_bf16 v[44:47], a[182:183], v[202:203], 0\n\t"
      "ds_read_b64_tr_b16 v[218:219], v60 offset:832\n\t"
      "v_mfma_f32_4x4x4_16b_bf16 v[48:51], a[180:181], v[204:205], 0\n\t"
      "ds_read_b64_tr_b16 v[220:221], v61 offset:64\n\t"
      "v_mfma_f32_4x4x4_16b_bf16 v[52:55], a[182:183], v[206:207], 0\n\t"
      "ds_read_b64_tr_b16 v[222:223], v61 offset:832\n\t"
      "buffer_load_dwordx4 v7, s[40:43], s48 offen offset:2048 nt lds\n\t"
      "v_mfma_f32_16x16x32_bf16 v[32:35], a[116:119], a[232:235], v[32:35]\n\t"
      "v_cvt_pk_bf16_f32 v200, v40, v41\n\t"
      "v_cvt_pk_bf16_f32 v201, v42, v43\n\t"
      "v_mfma_f32_16x16x32_bf16 v[36:39], a[120:123], a[232:235], v[36:39]\n\t"
      "v_cvt_pk_bf16_f32 v202, v44, v45\n\t"
      "v_cvt_pk_bf16_f32 v203, v46, v47\n\t"
      "s_waitcnt lgkmcnt(9)\n\t"
      "v_mfma_f32_16x16x32_bf16 a[156:159], v[176:179], a[236:239], a[156:159]\n\t"
      "v_cvt_pk_bf16_f32 v204, v48, v49\n\t"
      "v_cvt_pk_bf16_f32 v205, v50, v51\n\t"
      "v_mfma_f32_16x16x32_bf16 a[160:163], v[180:183], a[236:239], a[160:163]\n\t"
      "v_cvt_pk_bf16_f32 v206, v52, v53\n\t"
      "v_cvt_pk_bf16_f32 v207, v54, v55\n\t"
      "ds_read_b128 a[240:243], v56 offset:25600\n\t"
      "v_mfma_f32_4x4x4_16b_bf16 v[40:43], a[180:181], a[124:125], 0\n\t"
      "ds_read_b64_tr_b16 a[132:133], v60 offset:128\n\t"
      "v_mfma_f32_4x4x4_16b_bf16 v[44:47], a[182:183], a[126:127], 0\n\t"
      "ds_read_b64_tr_b16 a[134:135], v60 offset:896\n\t"
      "v_mfma_f32_4x4x4_16b_bf16 v[48:51], a[180:181], a[128:129], 0\n\t"
      "ds_read_b64_tr_b16 a[136:137], v61 offset:128\n\t"
      "v_mfma_f32_4x4x4_16b_bf16 v[52:55], a[182:183], a[130:131], 0\n\t"
      "ds_read_b64_tr_b16 a[138:139], v61 offset:896\n\t"
      "ds_read_b128 a[244:247], v56 offset:37888\n\t"
      "v_mfma_f32_16x16x32_bf16 a[164:167], v[184:187], a[236:239], a[164:167]\n\t"
      "v_cvt_pk_bf16_f32 v40, v40, v41\n\t"
      "v_cvt_pk_bf16_f32 v41, v42, v43\n\t"
      "v_mfma_f32_16x16x32_bf16 a[168:171], v[188:191], a[236:239], a[168:171]\n\t"
      "v_cvt_pk_bf16_f32 v44, v44, v45\n\t"
      "v_cvt_pk_bf16_f32 v45, v46, v47\n\t"
      "v_mfma_f32_16x16x32_bf16 a[172:175], a[116:119], a[236:239], a[172:175]\n\t"
      "v_cvt_pk_bf16_f32 v48, v48, v49\n\t"
      "v_cvt_pk_bf16_f32 v49, v50, v51\n\t"
      "v_mfma_f32_16x16x32_bf16 a[176:179], a[120:123], a[236:239], a[176:179]\n\t"
      "v_cvt_pk_bf16_f32 v52, v52, v53\n\t"
      "v_cvt_pk_bf16_f32 v53, v54, v55\n\t"
      "v_accvgpr_write_b32 a124, v40\n\t"
      "v_accvgpr_write_b32 a125, v41\n\t"
      "v_accvgpr_write_b32 a126, v44\n\t"
      "v_accvgpr_write_b32 a127, v45\n\t"
      "v_accvgpr_write_b32 a128, v48\n\t"
      "v_accvgpr_write_b32 a129, v49\n\t"
      "v_accvgpr_write_b32 a130, v52\n\t"
      "v_accvgpr_write_b32 a131, v53\n\t"
      "s_waitcnt vmcnt(12)\n\t"
      "s_waitcnt lgkmcnt(1)\n\t"
      "; step\n\t"
      "v_mfma_f32_4x4x4_16b_bf16 v[40:43], a[184:185], v[208:209], 0\n\t"
      "ds_read_b128 a[180:183], v14 offset:2560\n\t"
      "v_mfma_f32_4x4x4_16b_bf16 v[44:47], a[186:187], v[210:211], 0\n\t"
      "ds_read_b64_tr_b16 v[224:225], v60 offset:24576\n\t"
      "v_mfma_f32_4x4x4_16b_bf16 v[48:51], a[184:185], v[212:213], 0\n\t"
      "ds_read_b64_tr_b16 v[226:227], v60 offset:25344\n\t"
      "v_mfma_f32_4x4x4_16b_bf16 v[52:55], a[186:187], v[214:215], 0\n\t"
      "ds_read_b64_tr_b16 v[228:229], v61 offset:24576\n\t"
      "ds_read_b64_tr_b16 v[230:231], v61 offset:25344\n\t"
      "v_mfma_f32_16x16x32_bf16 v[16:19], v[192:195], a[240:243], v[16:19]\n\t"
      "v_cvt_pk_bf16_f32 v208, v40, v41\n\t"
      "v_cvt_pk_bf16_f32 v209, v42, v43\n\t"
      "v_mfma_f32_16x16x32_bf16 v[20:23], v[196:199], a[240:243], v[20:23]\n\t"
      "v_cvt_pk_bf16_f32 v210, v44, v45\n\t"
      "v_cvt_pk_bf16_f32 v211, v46, v47\n\t"
      "v_mfma_f32_16x16x32_bf16 v[24:27], v[200:203], a[240:243], v[24:27]\n\t"
      "v_cvt_pk_bf16_f32 v212, v48, v49\n\t"
      "v_cvt_pk_bf16_f32 v213, v50, v51\n\t"
      "v_mfma_f32_16x16x32_bf16 v[28:31], v[204:207], a[240:243], v[28:31]\n\t"
      "v_cvt_pk_bf16_f32 v214, v52, v53\n\t"
      "v_cvt_pk_bf16_f32 v215, v54, v55\n\t"
      "v_mfma_f32_4x4x4_16b_bf16 v[40:43], a[184:185], v[216:217], 0\n\t"
      "ds_read_b64_tr_b16 v[232:233], v60 offset:24640\n\t"
      "v_mfma_f32_4x4x4_16b_bf16 v[44:47], a[186:187], v[218:219], 0\n\t"
      "ds_read_b64_tr_b16 v[234:235], v60 offset:25408\n\t"
      "v_mfma_f32_4x4x4_16b_bf16 v[48:51], a[184:185], v[220:221], 0\n\t"
      "ds_read_b64_tr_b16 v[236:237], v61 offset:24640\n\t"
      "v_mfma_f32_4x4x4_16b_bf16 v[52:55], a[186:187], v[222:223], 0\n\t"
      "ds_read_b64_tr_b16 v[238:239], v61 offset:25408\n\t"
      "v_mfma_f32_16x16x32_bf16 v[32:35], a[124:127], a[240:243], v[32:35]\n\t"
      "v_cvt_pk_bf16_f32 v216, v40, v41\n\t"
      "v_cvt_pk_bf16_f32 v217, v42, v43\n\t"
      "v_mfma_f32_16x16x32_bf16 v[36:39], a[128:131], a[240:243], v[36:39]\n\t"
      "v_cvt_pk_bf16_f32 v218, v44, v45\n\t"
      "v_cvt_pk_bf16_f32 v219, v46, v47\n\t"
      "s_waitcnt lgkmcnt(9)\n\t"
      "v_mfma_f32_16x16x32_bf16 a[156:159], v[192:195], a[244:247], a[156:159]\n\t"
      "v_cvt_pk_bf16_f32 v220, v48, v49\n\t"
      "v_cvt_pk_bf16_f32 v221, v50, v51\n\t"
      "v_mfma_f32_16x16x32_bf16 a[160:163], v[196:199], a[244:247], a[160:163]\n\t"
      "v_cvt_pk_bf16_f32 v222, v52, v53\n\t"
      "v_cvt_pk_bf16_f32 v223, v54, v55\n\t"
      "ds_read_b128 a[224:227], v57 offset:25600\n\t"
      "v_mfma_f32_4x4x4_16b_bf16 v[40:43], a[184:185], a[132:133], 0\n\t"
      "ds_read_b64_tr_b16 a[140:141], v60 offset:24704\n\t"
      "v_mfma_f32_4x4x4_16b_bf16 v[44:47], a[186:187], a[134:135], 0\n\t"
      "ds_read_b64_tr_b16 a[142:143], v60 offset:25472\n\t"
      "v_mfma_f32_4x4x4_16b_bf16 v[48:51], a[184:185], a[136:137], 0\n\t"
      "ds_read_b64_tr_b16 a[144:145], v61 offset:24704\n\t"
      "v_mfma_f32_4x4x4_16b_bf16 v[52:55], a[186:187], a[138:139], 0\n\t"
      "ds_read_b64_tr_b16 a[146:147], v61 offset:25472\n\t"
      "ds_read_b128 a[228:231], v57 offset:37888\n\t"
      "v_mfma_f32_16x16x32_bf16 a[164:167], v[200:203], a[244:247], a[164:167]\n\t"
      "v_cvt_pk_bf16_f32 v40, v40, v41\n\t"
      "v_cvt_pk_bf16_f32 v41, v42, v43\n\t"
      "v_mfma_f32_16x16x32_bf16 a[168:171], v[204:207], a[244:247], a[168:171]\n\t"
      "v_cvt_pk_bf16_f32 v44, v44, v45\n\t"
      "v_cvt_pk_bf16_f32 v45, v46, v47\n\t"
      "v_mfma_f32_16x16x32_bf16 a[172:175], a[124:127], a[244:247], a[172:175]\n\t"
      "v_cvt_pk_bf16_f32 v48, v48, v49\n\t"
      "v_cvt_pk_bf16_f32 v49, v50, v51\n\t"
      "v_mfma_f32_16x16x32_bf16 a[176:179], a[128:131], a[244:247], a[176:179]\n\t"
      "v_cvt_pk_bf16_f32 v52, v52, v53\n\t"
      "v_cvt_pk_bf16_f32 v53, v54, v55\n\t"
      "v_accvgpr_write_b32 a132, v40\n\t"
      "v_accvgpr_write_b32 a133, v41\n\t"
      "v_accvgpr_write_b32 a134, v44\n\t"
      "v_accvgpr_write_b32 a135, v45\n\t"
      "v_accvgpr_write_b32 a136, v48\n\t"
      "v_accvgpr_write_b32 a137, v49\n\t"
      "v_accvgpr_write_b32 a138, v52\n\t"
      "v_accvgpr_write_b32 a139, v53\n\t"
      "s_waitcnt vmcnt(6)\n\t"
      "s_waitcnt lgkmcnt(1)\n\t"
      "; step\n\t"
      "v_mfma_f32_4x4x4_16b_bf16 v[40:43], a[180:181], v[224:225], 0\n\t"
      "ds_read_b128 a[184:187], v14 offset:2816\n\t"
      "v_mfma_f32_4x4x4_16b_bf16 v[44:47], a[182:183], v[226:227], 0\n\t"
      "ds_read_b64_tr_b16 v[240:241], v60 offset:49152\n\t"
      "v_mfma_f32_4x4x4_16b_bf16 v[48:51], a[180:181], v[228:229], 0\n\t"
      "ds_read_b64_tr_b16 v[242:243], v60 offset:49920\n\t"
      "v_mfma_f32_4x4x4_16b_bf16 v[52:55], a[182:183], v[230:231], 0\n\t"
      "ds_read_b64_tr_b16 v[244:245], v61 offset:49152\n\t"
      "ds_read_b64_tr_b16 v[246:247], v61 offset:49920\n\t"
      "v_mfma_f32_16x16x32_bf16 v[16:19], v[208:211], a[224:227], v[16:19]\n\t"
      "v_cvt_pk_bf16_f32 v224, v40, v41\n\t"
      "v_cvt_pk_bf16_f32 v225, v42, v43\n\t"
      "v_mfma_f32_16x16x32_bf16 v[20:23], v[212:215], a[224:227], v[20:23]\n\t"
      "v_cvt_pk_bf16_f32 v226, v44, v45\n\t"
      "v_cvt_pk_bf16_f32 v227, v46, v47\n\t"
      "v_mfma_f32_16x16x32_bf16 v[24:27], v[216:219], a[224:227], v[24:27]\n\t"
      "v_cvt_pk_bf16_f32 v228, v48, v49\n\t"
      "v_cvt_pk_bf16_f32 v229, v50, v51\n\t"
      "v_mfma_f32_16x16x32_bf16 v[28:31], v[220:223], a[224:227], v[28:31]\n\t"
      "v_cvt_pk_bf16_f32 v230, v52, v53\n\t"
      "v_cvt_pk_bf16_f32 v231, v54, v55\n\t"
      "v_mfma_f32_4x4x4_16b_bf16 v[40:43], a[180:181], v[232:233], 0\n\t"
      "ds_read_b64_tr_b16 v[248:249], v60 offset:49216\n\t"
      "v_mfma_f32_4x4x4_16b_bf16 v[44:47], a[182:183], v[234:235], 0\n\t"
      "ds_read_b64_tr_b16 v[250:251], v60 offset:49984\n\t"
      "v_mfma_f32_4x4x4_16b_bf16 v[48:51], a[180:181], v[236:237], 0\n\t"
      "ds_read_b64_tr_b16 v[252:253], v61 offset:49216\n\t"
      "v_mfma_f32_4x4x4_16b_bf16 v[52:55], a[182:183], v[238:239], 0\n\t"
      "ds_read_b64_tr_b16 v[254:255], v61 offset:49984\n\t"
      "v_mfma_f32_16x16x32_bf16 v[32:35], a[132:135], a[224:227], v[32:35]\n\t"
      "v_cvt_pk_bf16_f32 v232, v40, v41\n\t"
      "v_cvt_pk_bf16_f32 v233, v42, v43\n\t"
      "v_mfma_f32_16x16x32_bf16 v[36:39], a[136:139], a[224:227], v[36:39]\n\t"
      "v_cvt_pk_bf16_f32 v234, v44, v45\n\t"
      "v_cvt_pk_bf16_f32 v235, v46, v47\n\t"
      "s_waitcnt lgkmcnt(9)\n\t"
      "v_mfma_f32_16x16x32_bf16 a[156:159], v[208:211], a[228:231], a[156:159]\n\t"
      "v_cvt_pk_bf16_f32 v236, v48, v49\n\t"
      "v_cvt_pk_bf16_f32 v237, v50, v51\n\t"
      "v_mfma_f32_16x16x32_bf16 a[160:163], v[212:215], a[228:231], a[160:163]\n\t"
      "v_cvt_pk_bf16_f32 v238, v52, v53\n\t"
      "v_cvt_pk_bf16_f32 v239, v54, v55\n\t"
      "ds_read_b128 a[232:235], v58 offset:25600\n\t"
      "v_mfma_f32_4x4x4_16b_bf16 v[40:43], a[180:181], a[140:141], 0\n\t"
      "ds_read_b64_tr_b16 a[148:149], v60 offset:49280\n\t"
      "v_mfma_f32_4x4x4_16b_bf16 v[44:47], a[182:183], a[142:143], 0\n\t"
      "ds_read_b64_tr_b16 a[150:151], v60 offset:50048\n\t"
      "v_mfma_f32_4x4x4_16b_bf16 v[48:51], a[180:181], a[144:145], 0\n\t"
      "ds_read_b64_tr_b16 a[152:153], v61 offset:49280\n\t"
      "v_mfma_f32_4x4x4_16b_bf16 v[52:55], a[182:183], a[146:147], 0\n\t"
      "ds_read_b64_tr_b16 a[154:155], v61 offset:50048\n\t"
      "v_mfma_f32_16x16x32_bf16 a[164:167], v[216:219], a[228:231], a[164:167]\n\t"
      "v_cvt_pk_bf16_f32 v40, v40, v41\n\t"
      "v_cvt_pk_bf16_f32 v41, v42, v43\n\t"
      "v_mfma_f32_16x16x32_bf16 a[168:171], v[220:223], a[228:231], a[168:171]\n\t"
      "v_cvt_pk_bf16_f32 v44, v44, v45\n\t"
      "v_cvt_pk_bf16_f32 v45, v46, v47\n\t"
      "v_mfma_f32_16x16x32_bf16 a[172:175], a[132:135], a[228:231], a[172:175]\n\t"
      "v_cvt_pk_bf16_f32 v48, v48, v49\n\t"
      "v_cvt_pk_bf16_f32 v49, v50, v51\n\t"
      "v_mfma_f32_16x16x32_bf16 a[176:179], a[136:139], a[228:231], a[176:179]\n\t"
      "v_cvt_pk_bf16_f32 v52, v52, v53\n\t"
      "v_cvt_pk_bf16_f32 v53, v54, v55\n\t"
      "v_accvgpr_write_b32 a140, v40\n\t"
      "v_accvgpr_write_b32 a141, v41\n\t"
      "v_accvgpr_write_b32 a142, v44\n\t"
      "v_accvgpr_write_b32 a143, v45\n\t"
      "v_accvgpr_write_b32 a144, v48\n\t"
      "v_accvgpr_write_b32 a145, v49\n\t"
      "v_accvgpr_write_b32 a146, v52\n\t"
      "v_accvgpr_write_b32 a147, v53\n\t"
      "s_waitcnt lgkmcnt(0)\n\t"
      "; step\n\t"
      "v_mfma_f32_4x4x4_16b_bf16 v[40:43], a[184:185], v[240:241], 0\n\t"
      "v_mfma_f32_4x4x4_16b_bf16 v[44:47], a[186:187], v[242:243], 0\n\t"
      "v_mfma_f32_4x4x4_16b_bf16 v[48:51], a[184:185], v[244:245], 0\n\t"
      "v_mfma_f32_4x4x4_16b_bf16 v[52:55], a[186:187], v[246:247], 0\n\t"
      "v_mfma_f32_16x16x32_bf16 v[16:19], v[224:227], a[232:235], v[16:19]\n\t"
      "s_nop 0\n\t"
      "v_cvt_pk_bf16_f32 v240, v40, v41\n\t"
      "v_cvt_pk_bf16_f32 v241, v42, v43\n\t"
      "v_mfma_f32_16x16x32_bf16 v[20:23], v[228:231], a[232:235], v[20:23]\n\t"
      "v_cvt_pk_bf16_f32 v242, v44, v45\n\t"
      "v_cvt_pk_bf16_f32 v243, v46, v47\n\t"
      "v_cvt_pk_bf16_f32 v244, v48, v49\n\t"
      "v_cvt_pk_bf16_f32 v245, v50, v51\n\t"
      "v_cvt_pk_bf16_f32 v246, v52, v53\n\t"
      "v_cvt_pk_bf16_f32 v247, v54, v55\n\t"
      "v_mfma_f32_4x4x4_16b_bf16 v[40:43], a[184:185], v[248:249], 0\n\t"
      "v_mfma_f32_4x4x4_16b_bf16 v[44:47], a[186:187], v[250:251], 0\n\t"
      "v_mfma_f32_4x4x4_16b_bf16 v[48:51], a[184:185], v[252:253], 0\n\t"
      "v_mfma_f32_4x4x4_16b_bf16 v[52:55], a[186:187], v[254:255], 0\n\t"
      "v_mfma_f32_16x16x32_bf16 v[24:27], v[232:235], a[232:235], v[24:27]\n\t"
      "s_nop 0\n\t"
      "v_cvt_pk_bf16_f32 v248, v40, v41\n\t"
      "v_cvt_pk_bf16_f32 v249, v42, v43\n\t"
      "v_mfma_f32_16x16x32_bf16 v[28:31], v[236:239], a[232:235], v[28:31]\n\t"
      "v_cvt_pk_bf16_f32 v250, v44, v45\n\t"
      "v_cvt_pk_bf16_f32 v251, v46, v47\n\t"
      "v_cvt_pk_bf16_f32 v252, v48, v49\n\t"
      "v_cvt_pk_bf16_f32 v253, v50, v51\n\t"
      "v_cvt_pk_bf16_f32 v254, v52, v53\n\t"
      "v_cvt_pk_bf16_f32 v255, v54, v55\n\t"
      "ds_read_b128 a[236:239], v59 offset:25600\n\t"
      "v_mfma_f32_4x4x4_16b_bf16 v[40:43], a[184:185], a[148:149], 0\n\t"
      "v_mfma_f32_4x4x4_16b_bf16 v[44:47], a[186:187], a[150:151], 0\n\t"
      "v_mfma_f32_4x4x4_16b_bf16 v[48:51], a[184:185], a[152:153], 0\n\t"
      "v_mfma_f32_4x4x4_16b_bf16 v[52:55], a[186:187], a[154:155], 0\n\t"
      "ds_read_b128 a[240:243], v58 offset:37888\n\t"
      "ds_read_b128 a[244:247], v59 offset:37888\n\t"
      "v_mfma_f32_16x16x32_bf16 v[32:35], a[140:143], a[232:235], v[32:35]\n\t"
      "v_cvt_pk_bf16_f32 v40, v40, v41\n\t"
      "v_cvt_pk_bf16_f32 v41, v42, v43\n\t"
      "v_mfma_f32_16x16x32_bf16 v[36:39], a[144:147], a[232:235], v[36:39]\n\t"
      "v_cvt_pk_bf16_f32 v44, v44, v45\n\t"
      "v_cvt_pk_bf16_f32 v45, v46, v47\n\t"
      "v_cvt_pk_bf16_f32 v48, v48, v49\n\t"
      "v_cvt_pk_bf16_f32 v49, v50, v51\n\t"
      "v_cvt_pk_bf16_f32 v52, v52, v53\n\t"
      "v_cvt_pk_bf16_f32 v53, v54, v55\n\t"
      "v_accvgpr_write_b32 a148, v40\n\t"
      "v_accvgpr_write_b32 a149, v41\n\t"
      "v_accvgpr_write_b32 a150, v44\n\t"
      "v_accvgpr_write_b32 a151, v45\n\t"
      "v_accvgpr_write_b32 a152, v48\n\t"
      "v_accvgpr_write_b32 a153, v49\n\t"
      "v_accvgpr_write_b32 a154, v52\n\t"
      "v_accvgpr_write_b32 a155, v53\n\t"
      "s_waitcnt lgkmcnt(2)\n\t"
      "v_mfma_f32_16x16x32_bf16 v[16:19], v[240:243], a[236:239], v[16:19]\n\t"
      "s_add_u32 s49, s15, 0\n\t"
      "s_mov_b32 s50, 98816\n\t"
      "s_mov_b32 s51, 61952\n\t"
      "v_mfma_f32_16x16x32_bf16 v[20:23], v[244:247], a[236:239], v[20:23]\n\t"
      "s_mov_b32 s58, s14\n\t"
      "s_add_u32 s56, s14, 2359296\n\t"
      "v_mfma_f32_16x16x32_bf16 v[24:27], v[248:251], a[236:239], v[24:27]\n\t"
      "s_movk_i32 s39, 100\n\t"
      "s_movk_i32 s29, 74\n\t"
      "s_mov_b32 s32, 286331245\n\t"
      "v_mfma_f32_16x16x32_bf16 v[28:31], v[252:255], a[236:239], v[28:31]\n\t"
      "s_mov_b32 s33, 89477937\n\t"
      "s_mov_b32 s34, 286331209\n\t"
      "s_mov_b32 s35, 5591857\n\t"
      "v_mfma_f32_16x16x32_bf16 v[32:35], a[148:151], a[236:239], v[32:35]\n\t"
      "v_mov_b32 v52, 163328\n\t"
      "v_mfma_f32_16x16x32_bf16 v[36:39], a[152:155], a[236:239], v[36:39]\n\t"
      "s_cmp_lg_u32 s17, 0\n\t"
      "s_cselect_b32 s39, 0, s39\n\t"
      "s_cbranch_scc1 .Lno4_%=\n\t"
      "v_accvgpr_read_b32 v41, a248\n\t"
      "v_mov_b32 v51, 0x4040\n\t"
      "s_movk_i32 s52, 2000\n\t"
      "s_mov_b32 s91, 0\n\t"
      "s_waitcnt vmcnt(6) \n\t"
      "v_accvgpr_read_b32 v40, a252\n\t"
      ".Lcheck_%=:\n\t"
      "v_cmp_ne_u32 s[54:55], v40, v63\n\t"
      "s_cmp_lg_u32 s16, 0\n\t"
      "v_add_u32_dpp v42, v40, v40 quad_perm:[2,3,0,1] row_mask:0xf bank_mask:0xf\n\t"
      "s_cselect_b32 s30, -1, s54\n\t"
      "s_cmp_lg_u32 s30, -1\n\t"
      "s_cbranch_scc1 .Lwait_%=\n\t"
      ".Lwaited_%=:\n\t"
      "v_add_u32_dpp v42, v42, v42 quad_perm:[1,0,3,2] row_mask:0xf bank_mask:0xf\n\t"
      "s_bfe_u32 s30, s19, 0x30008\n\t"
      "s_bfe_u32 s31, s19, 0x10003\n\t"
      "s_lshl1_add_u32 s30, s30, s31\n\t"
      "s_lshl_b32 s30, s30, 1\n\t"
      "v_mov_b32 v43, v42\n\t"
      "v_subrev_u32_dpp v43, v42, v51 row_half_mirror row_mask:0x3 bank_mask:0xf\n\t"
      "v_lshl_add_u32 v42, v42, 1, v43\n\t"
      "v_readlane_b32 s31, v40, s30\n\t"
      "v_add_u32 v42, 0x1f1f, v42\n\t"
      "v_and_b32 v43, 128, v42\n\t"
      "v_and_b32 v44, 32768, v42\n\t"
      "v_cmp_ne_u32 s[86:87], 0, v43\n\t"
      "v_cmp_ne_u32 s[36:37], 0, v44\n\t"
      "s_bitcmp1_b32 s19, 2\n\t"
      "s_cselect_b32 s30, 0x3ffff, 0\n\t"
      "s_bitcmp1_b32 s31, 16\n\t"
      "s_cselect_b32 s31, 262140, 0\n\t"
      "s_xor_b32 s30, s30, s31\n\t"
      "v_lshrrev_b32 v41, v41, s86\n\t"
      "v_and_b32 v41, 1, v41\n\t"
      "v_cmp_ne_u32 vcc, 0, v41\n\t"
      "s_xor_b32 s36, s37, s87\n\t"
      "s_and_b32 s36, s36, 0x11111111\n\t"
      "s_bcnt1_i32_b32 s36, s36\n\t"
      "s_and_b32 s31, vcc_lo, 262140\n\t"
      "s_xor_b32 s31, s31, s30\n\t"
      "s_or_b32 s31, s31, s91\n\t"
      "s_and_b32 s30, s16, 0x3ffff\n\t"
      "s_cmp_lg_u32 s16, 0\n\t"
      "s_cselect_b32 s31, s30, s31\n\t"
      "v_mov_b32 v53, s31\n\t"
      "ds_write_b32 v52, v53\n\t"
      "s_lshl_b32 s30, s36, 2\n\t"
      "s_mov_b32 s54, 1126306048\n\t"
      "s_mov_b32 s55, 4\n\t"
      "s_lshr_b64 s[54:55], s[54:55], s30\n\t"
      "s_and_b32 s30, s54, 15\n\t"
      "s_add_u32 s29, s29, s30\n\t"
      ".Lno4_%=:\n\t"
      "s_waitcnt vmcnt(3)\n\t"
      "s_waitcnt lgkmcnt(0)\n\t"
      "s_barrier\n\t"
      "ds_read_b32 v63, v52\n\t"
      "s_add_u32 s90, s90, 0x20000\n\t"
      "s_or_b32 s30, s17, s16\n\t"
      "s_cbranch_scc1 .Lno5_%=\n\t"
      "v_mov_b32 v54, s90\n\t"
      "v_mov_b32 v55, s19\n\t"
      "global_store_dword v55, v54, s[12:13] sc1\n\t"
      ".Lno5_%=:\n\t"
      "s_memrealtime s[36:37]\n\t"
      "v_mfma_f32_16x16x32_bf16 a[156:159], v[224:227], a[240:243], a[156:159]\n\t"
      "ds_read_b128 a[0:3], v56 offset:49664\n\t"
      "ds_read_b128 a[4:7], v57 offset:49664\n\t"
      "v_mfma_f32_16x16x32_bf16 a[160:163], v[228:231], a[240:243], a[160:163]\n\t"
      "ds_read_b128 a[8:11], v58 offset:49664\n\t"
      "ds_read_b128 a[12:15], v59 offset:49664\n\t"
      "v_mfma_f32_16x16x32_bf16 a[164:167], v[232:235], a[240:243], a[164:167]\n\t"
      "ds_read_b128 a[16:19], v56 offset:49920\n\t"
      "ds_read_b128 a[20:23], v57 offset:49920\n\t"
      "v_mfma_f32_16x16x32_bf16 a[168:171], v[236:239], a[240:243], a[168:171]\n\t"
      "v_add_u32 v56, 49664, v56\n\t"
      "v_add_u32 v57, 49664, v57\n\t"
      "v_mfma_f32_16x16x32_bf16 a[172:175], a[140:143], a[240:243], a[172:175]\n\t"
      "v_add_u32 v58, 49664, v58\n\t"
      "v_add_u32 v59, 49664, v59\n\t"
      "v_mfma_f32_16x16x32_bf16 a[176:179], a[144:147], a[240:243], a[176:179]\n\t"
      "v_cvt_pk_bf16_f32 v40, v16, v17\n\t"
      "v_cvt_pk_bf16_f32 v41, v18, v19\n\t"
      "v_mfma_f32_16x16x32_bf16 a[156:159], v[240:243], a[244:247], a[156:159]\n\t"
      "v_cvt_pk_bf16_f32 v42, v20, v21\n\t"
      "v_cvt_pk_bf16_f32 v43, v22, v23\n\t"
      "v_mfma_f32_16x16x32_bf16 a[160:163], v[244:247], a[244:247], a[160:163]\n\t"
      "ds_write_b64 v0, v[40:41] offset:0\n\t"
      "ds_write_b64 v0, v[42:43] offset:32\n\t"
      "v_mfma_f32_16x16x32_bf16 a[164:167], v[248:251], a[244:247], a[164:167]\n\t"
      "v_cvt_pk_bf16_f32 v44, v24, v25\n\t"
      "v_cvt_pk_bf16_f32 v45, v26, v27\n\t"
      "v_mfma_f32_16x16x32_bf16 a[168:171], v[252:255], a[244:247], a[168:171]\n\t"
      "v_cvt_pk_bf16_f32 v46, v28, v29\n\t"
      "v_cvt_pk_bf16_f32 v47, v30, v31\n\t"
      "v_mfma_f32_16x16x32_bf16 a[172:175], a[148:151], a[244:247], a[172:175]\n\t"
      "ds_write_b64 v0, v[44:45] offset:64\n\t"
      "ds_write_b64 v0, v[46:47] offset:96\n\t"
      "v_mfma_f32_16x16x32_bf16 a[176:179], a[152:155], a[244:247], a[176:179]\n\t"
      "v_cvt_pk_bf16_f32 v48, v32, v33\n\t"
      "v_cvt_pk_bf16_f32 v49, v34, v35\n\t"
      "v_cvt_pk_bf16_f32 v50, v36, v37\n\t"
      "v_cvt_pk_bf16_f32 v51, v38, v39\n\t"
      "ds_write_b64 v0, v[48:49] offset:128\n\t"
      "ds_write_b64 v0, v[50:51] offset:160\n\t"
      "v_xor_b32 v62, v2, v1\n\t"
      "v_and_b32 v62, 0xf0, v62\n\t"
      "v_xor_b32 v9, v62, v2\n\t"
      "v_xor_b32 v12, v62, v5\n\t"
      "v_add_u32 v8, v62, v1\n\t"
      "v_xor_b32 v62, v3, v1\n\t"
      "v_and_b32 v62, 0xf0, v62\n\t"
      "v_xor_b32 v10, v62, v3\n\t"
      "v_xor_b32 v13, v62, v6\n\t"
      "v_add_u32 v60, v62, v1\n\t"
      "v_xor_b32 v62, v4, v1\n\t"
      "v_and_b32 v62, 0xf0, v62\n\t"
      "v_xor_b32 v11, v62, v4\n\t"
      "v_xor_b32 v14, v62, v7\n\t"
      "v_add_u32 v61, v62, v1\n\t"
      "s_waitcnt lgkmcnt(12)\n\t"
      "v_readfirstlane_b32 s91, v63\n\t"
      "s_nop 3\n\t"
      "s_mov_b32 s59, s91\n\t"
      "s_lshr_b32 s57, s91, 3\n\t"
      "s_add_u32 m0, s18, 74240\n\t"
      "s_bitcmp1_b32 s91, 2\n\t"
      "s_cselect_b32 s48, 24576, 0\n\t"
      "s_add_u32 s48, s48, s14\n\t"
      "s_add_u32 s48, s48, 1572864\n\t"
      "buffer_load_dwordx4 v2, s[40:43], s48 offen nt lds\n\t"
      "buffer_load_dwordx4 v3, s[40:43], s48 offen offset:1024 nt lds\n\t"
      "buffer_load_dwordx4 v4, s[40:43], s48 offen offset:2048 nt lds\n\t"
      "s_mov_b32 s46, 113246208\n\t"
      "s_bitcmp1_b32 s91, 3\n\t"
      "s_cselect_b32 s48, 24576, 0\n\t"
      "s_add_u32 s48, s48, s14\n\t"
      "s_add_u32 s48, s48, 2359296\n\t"
      "s_mov_b32 s52, 15\n\t"
      "s_waitcnt lgkmcnt(11)\n\t"
      "v_mfma_f32_16x16x32_bf16 v[16:19], v[64:67], a[0:3], 0\n\t"
      "ds_read_b128 a[24:27], v58 offset:256\n\t"
      "ds_read_b128 a[28:31], v59 offset:256\n\t"
      "s_waitcnt lgkmcnt(12)\n\t"
      "v_mfma_f32_16x16x32_bf16 v[16:19], v[80:83], a[4:7], v[16:19]\n\t"
      "ds_read_b128 a[32:35], v56 offset:512\n\t"
      "ds_read_b128 a[36:39], v57 offset:512\n\t"
      "s_waitcnt lgkmcnt(13)\n\t"
      "v_mfma_f32_16x16x32_bf16 v[16:19], v[96:99], a[8:11], v[16:19]\n\t"
      "ds_read_b128 a[40:43], v58 offset:512\n\t"
      "ds_read_b128 a[44:47], v59 offset:512\n\t"
      "s_waitcnt lgkmcnt(14)\n\t"
      "v_mfma_f32_16x16x32_bf16 v[16:19], v[112:115], a[12:15], v[16:19]\n\t"
      "v_accvgpr_read_b32 v52, a156\n\t"
      "v_accvgpr_read_b32 v53, a157\n\t"
      "s_waitcnt lgkmcnt(13)\n\t"
      "v_mfma_f32_16x16x32_bf16 v[16:19], v[128:131], a[16:19], v[16:19]\n\t"
      "v_accvgpr_read_b32 v54, a158\n\t"
      "v_accvgpr_read_b32 v55, a159\n\t"
      "s_waitcnt lgkmcnt(12)\n\t"
      "v_mfma_f32_16x16x32_bf16 v[16:19], v[144:147], a[20:23], v[16:19]\n\t"
      "v_cvt_pk_bf16_f32 v40, v52, v53\n\t"
      "v_cvt_pk_bf16_f32 v41, v54, v55\n\t"
      "v_mfma_f32_16x16x32_bf16 v[20:23], v[68:71], a[0:3], 0\n\t"
      "ds_write_b64 v0, v[40:41] offset:12544\n\t"
      "v_mfma_f32_16x16x32_bf16 v[20:23], v[84:87], a[4:7], v[20:23]\n\t"
      "s_waitcnt lgkmcnt(7)\n\t"
      "s_barrier\n\t"
      "v_mfma_f32_16x16x32_bf16 v[20:23], v[100:103], a[8:11], v[20:23]\n\t"
      "ds_read_b128 a[48:51], v8 offset:0\n\t"
      "ds_read_b128 a[52:55], v60 offset:1024\n\t"
      "ds_read_b128 a[56:59], v61 offset:2048\n\t"
      "v_mfma_f32_16x16x32_bf16 v[20:23], v[116:119], a[12:15], v[20:23]\n\t"
      "v_accvgpr_read_b32 v52, a160\n\t"
      "v_accvgpr_read_b32 v53, a161\n\t"
      "v_mfma_f32_16x16x32_bf16 v[20:23], v[132:135], a[16:19], v[20:23]\n\t"
      "v_accvgpr_read_b32 v54, a162\n\t"
      "v_accvgpr_read_b32 v55, a163\n\t"
      "v_mfma_f32_16x16x32_bf16 v[20:23], v[148:151], a[20:23], v[20:23]\n\t"
      "v_cvt_pk_bf16_f32 v42, v52, v53\n\t"
      "v_cvt_pk_bf16_f32 v43, v54, v55\n\t"
      "v_mfma_f32_16x16x32_bf16 v[24:27], v[72:75], a[0:3], 0\n\t"
      "ds_write_b64 v0, v[42:43] offset:12576\n\t"
      "s_waitcnt lgkmcnt(3)\n\t"
      "v_mfma_f32_16x16x32_bf16 v[24:27], v[88:91], a[4:7], v[24:27]\n\t"
      "buffer_store_dwordx4 a[48:51], v9, s[44:47], s49 offen sc1\n\t"
      "v_mfma_f32_16x16x32_bf16 v[24:27], v[104:107], a[8:11], v[24:27]\n\t"
      "v_accvgpr_read_b32 v52, a164\n\t"
      "v_accvgpr_read_b32 v53, a165\n\t"
      "v_mfma_f32_16x16x32_bf16 v[24:27], v[120:123], a[12:15], v[24:27]\n\t"
      "v_accvgpr_read_b32 v54, a166\n\t"
      "v_accvgpr_read_b32 v55, a167\n\t"
      "v_mfma_f32_16x16x32_bf16 v[24:27], v[136:139], a[16:19], v[24:27]\n\t"
      "v_cvt_pk_bf16_f32 v44, v52, v53\n\t"
      "v_cvt_pk_bf16_f32 v45, v54, v55\n\t"
      "v_mfma_f32_16x16x32_bf16 v[24:27], v[152:155], a[20:23], v[24:27]\n\t"
      "ds_write_b64 v0, v[44:45] offset:12608\n\t"
      "s_waitcnt lgkmcnt(3)\n\t"
      "v_mfma_f32_16x16x32_bf16 v[28:31], v[76:79], a[0:3], 0\n\t"
      "buffer_store_dwordx4 a[52:55], v10, s[44:47], s49 offen offset:1024 sc1\n\t"
      "v_mfma_f32_16x16x32_bf16 v[28:31], v[92:95], a[4:7], v[28:31]\n\t"
      "v_accvgpr_read_b32 v52, a168\n\t"
      "v_accvgpr_read_b32 v53, a169\n\t"
      "v_mfma_f32_16x16x32_bf16 v[28:31], v[108:111], a[8:11], v[28:31]\n\t"
      "v_accvgpr_read_b32 v54, a170\n\t"
      "v_accvgpr_read_b32 v55, a171\n\t"
      "v_mfma_f32_16x16x32_bf16 v[28:31], v[124:127], a[12:15], v[28:31]\n\t"
      "v_cvt_pk_bf16_f32 v46, v52, v53\n\t"
      "v_cvt_pk_bf16_f32 v47, v54, v55\n\t"
      "v_mfma_f32_16x16x32_bf16 v[28:31], v[140:143], a[16:19], v[28:31]\n\t"
      "ds_write_b64 v0, v[46:47] offset:12640\n\t"
      "s_waitcnt lgkmcnt(3)\n\t"
      "v_mfma_f32_16x16x32_bf16 v[28:31], v[156:159], a[20:23], v[28:31]\n\t"
      "buffer_store_dwordx4 a[56:59], v11, s[44:47], s49 offen offset:2048 sc1\n\t"
      "v_mfma_f32_16x16x32_bf16 v[32:35], a[60:63], a[0:3], 0\n\t"
      "v_accvgpr_read_b32 v52, a172\n\t"
      "v_accvgpr_read_b32 v53, a173\n\t"
      "v_mfma_f32_16x16x32_bf16 v[32:35], a[68:71], a[4:7], v[32:35]\n\t"
      "v_accvgpr_read_b32 v54, a174\n\t"
      "v_accvgpr_read_b32 v55, a175\n\t"
      "v_mfma_f32_16x16x32_bf16 v[32:35], a[76:79], a[8:11], v[32:35]\n\t"
      "v_cvt_pk_bf16_f32 v48, v52, v53\n\t"
      "v_cvt_pk_bf16_f32 v49, v54, v55\n\t"
      "v_mfma_f32_16x16x32_bf16 v[32:35], a[84:87], a[12:15], v[32:35]\n\t"
      "ds_write_b64 v0, v[48:49] offset:12672\n\t"
      "v_mfma_f32_16x16x32_bf16 v[32:35], a[92:95], a[16:19], v[32:35]\n\t"
      "v_accvgpr_read_b32 v52, a176\n\t"
      "v_accvgpr_read_b32 v53, a177\n\t"
      "v_mfma_f32_16x16x32_bf16 v[32:35], a[100:103], a[20:23], v[32:35]\n\t"
      "v_accvgpr_read_b32 v54, a178\n\t"
      "v_accvgpr_read_b32 v55, a179\n\t"
      "v_mfma_f32_16x16x32_bf16 v[36:39], a[64:67], a[0:3], 0\n\t"
      "v_cvt_pk_bf16_f32 v50, v52, v53\n\t"
      "v_cvt_pk_bf16_f32 v51, v54, v55\n\t"
      "v_mfma_f32_16x16x32_bf16 v[36:39], a[72:75], a[4:7], v[36:39]\n\t"
      "ds_write_b64 v0, v[50:51] offset:12704\n\t"
      "v_mfma_f32_16x16x32_bf16 v[36:39], a[80:83], a[8:11], v[36:39]\n\t"
      "v_add_u32 v52, s51, v15\n\t"
      "v_xor_b32 v53, 64, v52\n\t"
      "v_mfma_f32_16x16x32_bf16 v[36:39], a[88:91], a[12:15], v[36:39]\n\t"
      "v_xor_b32 v54, 128, v52\n\t"
      "v_xor_b32 v55, 192, v52\n\t"
      "v_mfma_f32_16x16x32_bf16 v[36:39], a[96:99], a[16:19], v[36:39]\n\t"
      "s_waitcnt lgkmcnt(0)\n\t"
      "s_add_u32 s38, s36, s39\n\t"
      "s_sub_u32 s28, s36, s28\n\t"
      "s_sub_i32 s28, s28, 513\n\t"
      "s_max_i32 s28, s28, 0\n\t"
      "s_min_i32 s28, s28, 66\n\t"
      "s_and_b32 s39, s32, 15\n\t"
      "s_cselect_b32 s53, s29, 0\n\t"
      "s_memrealtime s[36:37]\n\t"
      "v_mfma_f32_16x16x32_bf16 v[36:39], a[104:107], a[20:23], v[36:39]\n\t"
      "s_add_u32 s39, s39, s53\n\t"
      "s_lshr_b64 s[32:33], s[32:33], 4\n\t"
      "s_add_u32 m0, s18, 86528\n\t"
      "v_mfma_f32_16x16x32_bf16 v[16:19], v[160:163], a[24:27], v[16:19]\n\t"
      "s_bitcmp1_b32 s91, 2\n\t"
      "s_cselect_b32 s54, 24576, 0\n\t"
      "s_add_u32 s54, s54, s14\n\t"
      "s_add_u32 s54, s54, 1572864\n\t"
      "v_mfma_f32_16x16x32_bf16 v[16:19], v[176:179], a[28:31], v[16:19]\n\t"
      "s_waitcnt vmcnt(6)\n\t"
      "s_barrier\n\t"
      "v_mfma_f32_16x16x32_bf16 v[16:19], v[192:195], a[32:35], v[16:19]\n\t"
      "ds_read_b128 a[0:3], v52 offset:0\n\t"
      "ds_read_b128 a[188:191], v8 offset:12544\n\t"
      "buffer_load_dwordx4 v5, s[40:43], s54 offen nt lds\n\t"
      "v_mfma_f32_16x16x32_bf16 v[16:19], v[208:211], a[36:39], v[16:19]\n\t"
      "ds_read_b128 a[4:7], v53 offset:0\n\t"
      "ds_read_b128 a[192:195], v60 offset:13568\n\t"
      "v_mfma_f32_16x16x32_bf16 v[16:19], v[224:227], a[40:43], v[16:19]\n\t"
      "ds_read_b128 a[8:11], v54 offset:0\n\t"
      "ds_read_b128 a[196:199], v61 offset:14592\n\t"
      "buffer_load_dwordx4 v6, s[40:43], s54 offen offset:1024 nt lds\n\t"
      "v_mfma_f32_16x16x32_bf16 v[16:19], v[240:243], a[44:47], v[16:19]\n\t"
      "ds_read_b128 a[12:15], v55 offset:0\n\t"
      "v_mfma_f32_16x16x32_bf16 v[20:23], v[164:167], a[24:27], v[20:23]\n\t"
      "ds_read_b128 a[16:19], v52 offset:256\n\t"
      "buffer_load_dwordx4 v7, s[40:43], s54 offen offset:2048 nt lds\n\t"
      "v_mfma_f32_16x16x32_bf16 v[20:23], v[180:183], a[28:31], v[20:23]\n\t"
      "ds_read_b128 a[20:23], v53 offset:256\n\t"
      "s_add_u32 m0, s18, s50\n\t"
      "v_mfma_f32_16x16x32_bf16 v[20:23], v[196:199], a[32:35], v[20:23]\n\t"
      "s_add_u32 s51, s51, 12288\n\t"
      "s_cmp_eq_u32 s51, 160256\n\t"
      "s_cselect_b32 s51, 25088, s51\n\t"
      "s_waitcnt lgkmcnt(7)\n\t"
      "v_mfma_f32_16x16x32_bf16 v[20:23], v[212:215], a[36:39], v[20:23]\n\t"
      "buffer_store_dwordx4 a[188:191], v12, s[44:47], s49 offen sc1\n\t"
      "v_mfma_f32_16x16x32_bf16 v[20:23], v[228:231], a[40:43], v[20:23]\n\t"
      "v_mfma_f32_16x16x32_bf16 v[20:23], v[244:247], a[44:47], v[20:23]\n\t"
      "s_waitcnt lgkmcnt(0)\n\t"
      "s_sub_u32 s53, s36, s38\n\t"
      "s_cmp_lt_i32 s53, 0\n\t"
      "v_mfma_f32_16x16x32_bf16 v[24:27], v[168:171], a[24:27], v[24:27]\n\t"
      "s_cbranch_scc1 .Lspinf_%=\n\t"
      ".Lspunf_%=:\n\t"
      "s_sub_i32 s53, s53, 30\n\t"
      "s_max_i32 s53, s53, 0\n\t"
      "v_mfma_f32_16x16x32_bf16 v[24:27], v[184:187], a[28:31], v[24:27]\n\t"
      "s_add_u32 s38, s38, s53\n\t"
      "s_add_u32 s38, s38, s39\n\t"
      "v_mfma_f32_16x16x32_bf16 v[24:27], v[200:203], a[32:35], v[24:27]\n\t"
      "v_cvt_pk_bf16_f32 v40, v16, v17\n\t"
      "v_cvt_pk_bf16_f32 v41, v18, v19\n\t"
      "v_mfma_f32_16x16x32_bf16 v[24:27], v[216:219], a[36:39], v[24:27]\n\t"
      "ds_write_b64 v0, v[40:41] offset:0\n\t"
      "v_mfma_f32_16x16x32_bf16 v[24:27], v[232:235], a[40:43], v[24:27]\n\t"
      "buffer_load_dwordx4 v2, s[40:43], s48 offen nt lds\n\t"
      "v_mfma_f32_16x16x32_bf16 v[24:27], v[248:251], a[44:47], v[24:27]\n\t"
      "buffer_store_dwordx4 a[192:195], v13, s[44:47], s49 offen offset:1024 sc1\n\t"
      "v_mfma_f32_16x16x32_bf16 v[28:31], v[172:175], a[24:27], v[28:31]\n\t"
      "v_mfma_f32_16x16x32_bf16 v[28:31], v[188:191], a[28:31], v[28:31]\n\t"
      "v_cvt_pk_bf16_f32 v42, v20, v21\n\t"
      "v_cvt_pk_bf16_f32 v43, v22, v23\n\t"
      "v_mfma_f32_16x16x32_bf16 v[28:31], v[204:207], a[32:35], v[28:31]\n\t"
      "ds_write_b64 v0, v[42:43] offset:32\n\t"
      "v_mfma_f32_16x16x32_bf16 v[28:31], v[220:223], a[36:39], v[28:31]\n\t"
      "buffer_load_dwordx4 v3, s[40:43], s48 offen offset:1024 nt lds\n\t"
      "v_mfma_f32_16x16x32_bf16 v[28:31], v[236:239], a[40:43], v[28:31]\n\t"
      "buffer_store_dwordx4 a[196:199], v14, s[44:47], s49 offen offset:2048 sc1\n\t"
      "v_mfma_f32_16x16x32_bf16 v[28:31], v[252:255], a[44:47], v[28:31]\n\t"
      "s_add_u32 s58, s58, 786432\n\t"
      "s_lshr_b32 s59, s59, 1\n\t"
      "v_mfma_f32_16x16x32_bf16 v[32:35], a[108:111], a[24:27], v[32:35]\n\t"
      "s_bitcmp1_b32 s59, 0\n\t"
      "s_cselect_b32 s49, 24576, 0\n\t"
      "s_add_u32 s49, s49, s58\n\t"
      "v_mfma_f32_16x16x32_bf16 v[32:35], a[116:119], a[28:31], v[32:35]\n\t"
      "v_cvt_pk_bf16_f32 v44, v24, v25\n\t"
      "v_cvt_pk_bf16_f32 v45, v26, v27\n\t"
      "v_mfma_f32_16x16x32_bf16 v[32:35], a[124:127], a[32:35], v[32:35]\n\t"
      "ds_write_b64 v0, v[44:45] offset:64\n\t"
      "v_mfma_f32_16x16x32_bf16 v[32:35], a[132:135], a[36:39], v[32:35]\n\t"
      "buffer_load_dwordx4 v4, s[40:43], s48 offen offset:2048 nt lds\n\t"
      "v_mfma_f32_16x16x32_bf16 v[32:35], a[140:143], a[40:43], v[32:35]\n\t"
      "s_add_u32 s50, s50, 12288\n\t"
      "v_mfma_f32_16x16x32_bf16 v[32:35], a[148:151], a[44:47], v[32:35]\n\t"
      "v_cvt_pk_bf16_f32 v46, v28, v29\n\t"
      "v_cvt_pk_bf16_f32 v47, v30, v31\n\t"
      "v_mfma_f32_16x16x32_bf16 v[36:39], a[112:115], a[24:27], v[36:39]\n\t"
      "ds_write_b64 v0, v[46:47] offset:96\n\t"
      "v_mfma_f32_16x16x32_bf16 v[36:39], a[120:123], a[28:31], v[36:39]\n\t"
      "s_cmp_eq_u32 s50, 160256\n\t"
      "s_cselect_b32 s50, 25088, s50\n\t"
      "v_mfma_f32_16x16x32_bf16 v[36:39], a[128:131], a[32:35], v[36:39]\n\t"
      "v_mfma_f32_16x16x32_bf16 v[36:39], a[136:139], a[36:39], v[36:39]\n\t"
      "v_mfma_f32_16x16x32_bf16 v[36:39], a[144:147], a[40:43], v[36:39]\n\t"
      "v_cvt_pk_bf16_f32 v48, v32, v33\n\t"
      "v_cvt_pk_bf16_f32 v49, v34, v35\n\t"
      "v_mfma_f32_16x16x32_bf16 v[36:39], a[152:155], a[44:47], v[36:39]\n\t"
      "ds_write_b64 v0, v[48:49] offset:128\n\t"
      "s_cmp_eq_u32 s17, 6144\n\t"
      "s_cbranch_scc1 .Lmid1_%=\n\t"
      "s_cmp_eq_u32 s17, 12288\n\t"
      "s_cbranch_scc1 .Lmid2_%=\n\t"
      "s_cmp_eq_u32 s17, 18432\n\t"
      "s_cbranch_scc1 .Lmid3_%=\n\t"
      "s_branch .Lmid0_%=\n\t"
      ".Lloop0_%=:\n\t"
      "v_mfma_f32_16x16x32_bf16 v[16:19], v[64:67], a[0:3], 0\n\t"
      "ds_read_b128 a[24:27], v58 offset:256\n\t"
      "v_mfma_f32_16x16x32_bf16 v[16:19], v[80:83], a[4:7], v[16:19]\n\t"
      "ds_read_b128 a[28:31], v59 offset:256\n\t"
      "v_mfma_f32_16x16x32_bf16 v[16:19], v[96:99], a[8:11], v[16:19]\n\t"
      "ds_read_b128 a[32:35], v56 offset:512\n\t"
      "v_mfma_f32_16x16x32_bf16 v[16:19], v[112:115], a[12:15], v[16:19]\n\t"
      "ds_read_b128 a[36:39], v57 offset:512\n\t"
      "v_mfma_f32_16x16x32_bf16 v[16:19], v[128:131], a[16:19], v[16:19]\n\t"
      "ds_read_b128 a[40:43], v58 offset:512\n\t"
      "v_mfma_f32_16x16x32_bf16 v[16:19], v[144:147], a[20:23], v[16:19]\n\t"
      "ds_read_b128 a[44:47], v59 offset:512\n\t"
      "v_mfma_f32_16x16x32_bf16 v[20:23], v[68:71], a[0:3], 0\n\t"
      "v_cvt_pk_bf16_f32 v50, v36, v37\n\t"
      "v_cvt_pk_bf16_f32 v51, v38, v39\n\t"
      "v_mfma_f32_16x16x32_bf16 v[20:23], v[84:87], a[4:7], v[20:23]\n\t"
      "ds_write_b64 v0, v[50:51] offset:12704\n\t"
      "v_mfma_f32_16x16x32_bf16 v[20:23], v[100:103], a[8:11], v[20:23]\n\t"
      "v_add_u32 v52, s51, v15\n\t"
      "v_xor_b32 v53, 64, v52\n\t"
      "v_mfma_f32_16x16x32_bf16 v[20:23], v[116:119], a[12:15], v[20:23]\n\t"
      "v_xor_b32 v54, 128, v52\n\t"
      "v_xor_b32 v55, 192, v52\n\t"
      "v_mfma_f32_16x16x32_bf16 v[20:23], v[132:135], a[16:19], v[20:23]\n\t"
      "s_add_u32 s56, s56, 786432\n\t"
      "s_lshr_b32 s57, s57, 1\n\t"
      "v_mfma_f32_16x16x32_bf16 v[20:23], v[148:151], a[20:23], v[20:23]\n\t"
      "s_bitcmp1_b32 s57, 0\n\t"
      "s_cselect_b32 s48, 24576, 0\n\t"
      "s_add_u32 s48, s48, s56\n\t"
      "v_mfma_f32_16x16x32_bf16 v[24:27], v[72:75], a[0:3], 0\n\t"
      "v_mfma_f32_16x16x32_bf16 v[24:27], v[88:91], a[4:7], v[24:27]\n\t"
      "v_mfma_f32_16x16x32_bf16 v[24:27], v[104:107], a[8:11], v[24:27]\n\t"
      "s_waitcnt vmcnt(12)\n\t"
      "s_waitcnt lgkmcnt(0)\n\t"
      "s_barrier\n\t"
      "v_mfma_f32_16x16x32_bf16 v[24:27], v[120:123], a[12:15], v[24:27]\n\t"
      "v_mfma_f32_16x16x32_bf16 v[24:27], v[136:139], a[16:19], v[24:27]\n\t"
      "v_mfma_f32_16x16x32_bf16 v[24:27], v[152:155], a[20:23], v[24:27]\n\t"
      "v_mfma_f32_16x16x32_bf16 v[28:31], v[76:79], a[0:3], 0\n\t"
      "ds_read_b128 a[48:51], v8 offset:12544\n\t"
      "v_mfma_f32_16x16x32_bf16 v[28:31], v[92:95], a[4:7], v[28:31]\n\t"
      "ds_read_b128 a[52:55], v60 offset:13568\n\t"
      "v_mfma_f32_16x16x32_bf16 v[28:31], v[108:111], a[8:11], v[28:31]\n\t"
      "ds_read_b128 a[56:59], v61 offset:14592\n\t"
      "v_mfma_f32_16x16x32_bf16 v[28:31], v[124:127], a[12:15], v[28:31]\n\t"
      "s_and_b32 s39, s32, 15\n\t"
      "s_cselect_b32 s53, s29, 0\n\t"
      "v_mfma_f32_16x16x32_bf16 v[28:31], v[140:143], a[16:19], v[28:31]\n\t"
      "s_add_u32 s39, s39, s53\n\t"
      "s_lshr_b64 s[32:33], s[32:33], 4\n\t"
      "v_mfma_f32_16x16x32_bf16 v[28:31], v[156:159], a[20:23], v[28:31]\n\t"
      "s_cmp_le_u32 s52, 11\n\t"
      "s_cselect_b32 s53, 3, 0\n\t"
      "s_min_u32 s53, s28, s53\n\t"
      "v_mfma_f32_16x16x32_bf16 v[32:35], a[60:63], a[0:3], 0\n\t"
      "s_sub_u32 s28, s28, s53\n\t"
      "s_sub_u32 s39, s39, s53\n\t"
      "v_mfma_f32_16x16x32_bf16 v[32:35], a[68:71], a[4:7], v[32:35]\n\t"
      "s_waitcnt lgkmcnt(2)\n\t"
      "v_mfma_f32_16x16x32_bf16 v[32:35], a[76:79], a[8:11], v[32:35]\n\t"
      "buffer_store_dwordx4 a[48:51], v12, s[44:47], s49 offen sc1\n\t"
      "v_mfma_f32_16x16x32_bf16 v[32:35], a[84:87], a[12:15], v[32:35]\n\t"
      "v_mfma_f32_16x16x32_bf16 v[32:35], a[92:95], a[16:19], v[32:35]\n\t"
      "v_mfma_f32_16x16x32_bf16 v[32:35], a[100:103], a[20:23], v[32:35]\n\t"
      "s_add_u32 m0, s18, s50\n\t"
      "v_mfma_f32_16x16x32_bf16 v[36:39], a[64:67], a[0:3], 0\n\t"
      "buffer_load_dwordx4 v2, s[40:43], s48 offen nt lds\n\t"
      "v_mfma_f32_16x16x32_bf16 v[36:39], a[72:75], a[4:7], v[36:39]\n\t"
      "s_memrealtime s[36:37]\n\t"
      "v_mfma_f32_16x16x32_bf16 v[36:39], a[80:83], a[8:11], v[36:39]\n\t"
      "v_mfma_f32_16x16x32_bf16 v[36:39], a[88:91], a[12:15], v[36:39]\n\t"
      "s_waitcnt lgkmcnt(1)\n\t"
      "v_mfma_f32_16x16x32_bf16 v[36:39], a[96:99], a[16:19], v[36:39]\n\t"
      "buffer_store_dwordx4 a[52:55], v13, s[44:47], s49 offen offset:1024 sc1\n\t"
      "v_mfma_f32_16x16x32_bf16 v[36:39], a[104:107], a[20:23], v[36:39]\n\t"
      "v_mfma_f32_16x16x32_bf16 v[16:19], v[160:163], a[24:27], v[16:19]\n\t"
      "ds_read_b128 a[0:3], v52 offset:0\n\t"
      "v_mfma_f32_16x16x32_bf16 v[16:19], v[176:179], a[28:31], v[16:19]\n\t"
      "ds_read_b128 a[4:7], v53 offset:0\n\t"
      "v_mfma_f32_16x16x32_bf16 v[16:19], v[192:195], a[32:35], v[16:19]\n\t"
      "ds_read_b128 a[8:11], v54 offset:0\n\t"
      "v_mfma_f32_16x16x32_bf16 v[16:19], v[208:211], a[36:39], v[16:19]\n\t"
      "ds_read_b128 a[12:15], v55 offset:0\n\t"
      "v_mfma_f32_16x16x32_bf16 v[16:19], v[224:227], a[40:43], v[16:19]\n\t"
      "ds_read_b128 a[16:19], v52 offset:256\n\t"
      "v_mfma_f32_16x16x32_bf16 v[16:19], v[240:243], a[44:47], v[16:19]\n\t"
      "ds_read_b128 a[20:23], v53 offset:256\n\t"
      "v_mfma_f32_16x16x32_bf16 v[20:23], v[164:167], a[24:27], v[20:23]\n\t"
      "s_add_u32 s51, s51, 12288\n\t"
      "s_cmp_eq_u32 s51, 160256\n\t"
      "s_cselect_b32 s51, 25088, s51\n\t"
      "v_mfma_f32_16x16x32_bf16 v[20:23], v[180:183], a[28:31], v[20:23]\n\t"
      "s_waitcnt lgkmcnt(6)\n\t"
      "v_mfma_f32_16x16x32_bf16 v[20:23], v[196:199], a[32:35], v[20:23]\n\t"
      "buffer_store_dwordx4 a[56:59], v14, s[44:47], s49 offen offset:2048 sc1\n\t"
      "v_mfma_f32_16x16x32_bf16 v[20:23], v[212:215], a[36:39], v[20:23]\n\t"
      "v_mfma_f32_16x16x32_bf16 v[20:23], v[228:231], a[40:43], v[20:23]\n\t"
      "s_waitcnt lgkmcnt(0)\n\t"
      "s_sub_u32 s53, s36, s38\n\t"
      "s_cmp_lt_i32 s53, 0\n\t"
      "v_mfma_f32_16x16x32_bf16 v[20:23], v[244:247], a[44:47], v[20:23]\n\t"
      "s_cbranch_scc1 .Lspin0_%=\n\t"
      ".Lspun0_%=:\n\t"
      "s_sub_i32 s53, s53, 30\n\t"
      "s_max_i32 s53, s53, 0\n\t"
      "v_mfma_f32_16x16x32_bf16 v[24:27], v[168:171], a[24:27], v[24:27]\n\t"
      "s_add_u32 s38, s38, s53\n\t"
      "s_add_u32 s38, s38, s39\n\t"
      "v_mfma_f32_16x16x32_bf16 v[24:27], v[184:187], a[28:31], v[24:27]\n\t"
      "v_cvt_pk_bf16_f32 v40, v16, v17\n\t"
      "v_cvt_pk_bf16_f32 v41, v18, v19\n\t"
      "v_mfma_f32_16x16x32_bf16 v[24:27], v[200:203], a[32:35], v[24:27]\n\t"
      "ds_write_b64 v0, v[40:41] offset:0\n\t"
      "v_mfma_f32_16x16x32_bf16 v[24:27], v[216:219], a[36:39], v[24:27]\n\t"
      "buffer_load_dwordx4 v3, s[40:43], s48 offen offset:1024 nt lds\n\t"
      "v_mfma_f32_16x16x32_bf16 v[24:27], v[232:235], a[40:43], v[24:27]\n\t"
      "s_add_u32 s58, s58, 786432\n\t"
      "s_lshr_b32 s59, s59, 1\n\t"
      "v_mfma_f32_16x16x32_bf16 v[24:27], v[248:251], a[44:47], v[24:27]\n\t"
      "s_bitcmp1_b32 s59, 0\n\t"
      "s_cselect_b32 s49, 24576, 0\n\t"
      "s_add_u32 s49, s49, s58\n\t"
      "v_mfma_f32_16x16x32_bf16 v[28:31], v[172:175], a[24:27], v[28:31]\n\t"
      "v_mfma_f32_16x16x32_bf16 v[28:31], v[188:191], a[28:31], v[28:31]\n\t"
      "v_cvt_pk_bf16_f32 v42, v20, v21\n\t"
      "v_cvt_pk_bf16_f32 v43, v22, v23\n\t"
      "v_mfma_f32_16x16x32_bf16 v[28:31], v[204:207], a[32:35], v[28:31]\n\t"
      "ds_write_b64 v0, v[42:43] offset:32\n\t"
      "v_mfma_f32_16x16x32_bf16 v[28:31], v[220:223], a[36:39], v[28:31]\n\t"
      "v_mfma_f32_16x16x32_bf16 v[28:31], v[236:239], a[40:43], v[28:31]\n\t"
      "v_mfma_f32_16x16x32_bf16 v[28:31], v[252:255], a[44:47], v[28:31]\n\t"
      "v_mfma_f32_16x16x32_bf16 v[32:35], a[108:111], a[24:27], v[32:35]\n\t"
      "v_mfma_f32_16x16x32_bf16 v[32:35], a[116:119], a[28:31], v[32:35]\n\t"
      "v_cvt_pk_bf16_f32 v44, v24, v25\n\t"
      "v_cvt_pk_bf16_f32 v45, v26, v27\n\t"
      "v_mfma_f32_16x16x32_bf16 v[32:35], a[124:127], a[32:35], v[32:35]\n\t"
      "ds_write_b64 v0, v[44:45] offset:64\n\t"
      "v_mfma_f32_16x16x32_bf16 v[32:35], a[132:135], a[36:39], v[32:35]\n\t"
      "buffer_load_dwordx4 v4, s[40:43], s48 offen offset:2048 nt lds\n\t"
      "v_mfma_f32_16x16x32_bf16 v[32:35], a[140:143], a[40:43], v[32:35]\n\t"
      "s_add_u32 s50, s50, 12288\n\t"
      "v_mfma_f32_16x16x32_bf16 v[32:35], a[148:151], a[44:47], v[32:35]\n\t"
      "v_cvt_pk_bf16_f32 v46, v28, v29\n\t"
      "v_cvt_pk_bf16_f32 v47, v30, v31\n\t"
      "v_mfma_f32_16x16x32_bf16 v[36:39], a[112:115], a[24:27], v[36:39]\n\t"
      "ds_write_b64 v0, v[46:47] offset:96\n\t"
      "v_mfma_f32_16x16x32_bf16 v[36:39], a[120:123], a[28:31], v[36:39]\n\t"
      "s_cmp_eq_u32 s50, 160256\n\t"
      "s_cselect_b32 s50, 25088, s50\n\t"
      "v_mfma_f32_16x16x32_bf16 v[36:39], a[128:131], a[32:35], v[36:39]\n\t"
      "v_mfma_f32_16x16x32_bf16 v[36:39], a[136:139], a[36:39], v[36:39]\n\t"
      "v_mfma_f32_16x16x32_bf16 v[36:39], a[144:147], a[40:43], v[36:39]\n\t"
      "v_cvt_pk_bf16_f32 v48, v32, v33\n\t"
      "v_cvt_pk_bf16_f32 v49, v34, v35\n\t"
      "v_mfma_f32_16x16x32_bf16 v[36:39], a[152:155], a[44:47], v[36:39]\n\t"
      "ds_write_b64 v0, v[48:49] offset:128\n\t"
      ".Lmid0_%=:\n\t"
      "v_mfma_f32_16x16x32_bf16 v[16:19], v[64:67], a[0:3], 0\n\t"
      "ds_read_b128 a[24:27], v54 offset:256\n\t"
      "v_mfma_f32_16x16x32_bf16 v[16:19], v[80:83], a[4:7], v[16:19]\n\t"
      "ds_read_b128 a[28:31], v55 offset:256\n\t"
      "v_mfma_f32_16x16x32_bf16 v[16:19], v[96:99], a[8:11], v[16:19]\n\t"
      "ds_read_b128 a[32:35], v52 offset:512\n\t"
      "v_mfma_f32_16x16x32_bf16 v[16:19], v[112:115], a[12:15], v[16:19]\n\t"
      "ds_read_b128 a[36:39], v53 offset:512\n\t"
      "v_mfma_f32_16x16x32_bf16 v[16:19], v[128:131], a[16:19], v[16:19]\n\t"
      "ds_read_b128 a[40:43], v54 offset:512\n\t"
      "v_mfma_f32_16x16x32_bf16 v[16:19], v[144:147], a[20:23], v[16:19]\n\t"
      "ds_read_b128 a[44:47], v55 offset:512\n\t"
      "v_mfma_f32_16x16x32_bf16 v[20:23], v[68:71], a[0:3], 0\n\t"
      "v_cvt_pk_bf16_f32 v50, v36, v37\n\t"
      "v_cvt_pk_bf16_f32 v51, v38, v39\n\t"
      "v_mfma_f32_16x16x32_bf16 v[20:23], v[84:87], a[4:7], v[20:23]\n\t"
      "ds_write_b64 v0, v[50:51] offset:160\n\t"
      "v_mfma_f32_16x16x32_bf16 v[20:23], v[100:103], a[8:11], v[20:23]\n\t"
      "v_add_u32 v56, s51, v15\n\t"
      "v_xor_b32 v57, 64, v56\n\t"
      "v_mfma_f32_16x16x32_bf16 v[20:23], v[116:119], a[12:15], v[20:23]\n\t"
      "v_xor_b32 v58, 128, v56\n\t"
      "v_xor_b32 v59, 192, v56\n\t"
      "v_mfma_f32_16x16x32_bf16 v[20:23], v[132:135], a[16:19], v[20:23]\n\t"
      "v_mfma_f32_16x16x32_bf16 v[20:23], v[148:151], a[20:23], v[20:23]\n\t"
      "v_mfma_f32_16x16x32_bf16 v[24:27], v[72:75], a[0:3], 0\n\t"
      "v_mfma_f32_16x16x32_bf16 v[24:27], v[88:91], a[4:7], v[24:27]\n\t"
      "v_mfma_f32_16x16x32_bf16 v[24:27], v[104:107], a[8:11], v[24:27]\n\t"
      "s_waitcnt vmcnt(12)\n\t"
      "s_waitcnt lgkmcnt(0)\n\t"
      "s_barrier\n\t"
      "v_mfma_f32_16x16x32_bf16 v[24:27], v[120:123], a[12:15], v[24:27]\n\t"
      "v_mfma_f32_16x16x32_bf16 v[24:27], v[136:139], a[16:19], v[24:27]\n\t"
      "v_mfma_f32_16x16x32_bf16 v[24:27], v[152:155], a[20:23], v[24:27]\n\t"
      "v_mfma_f32_16x16x32_bf16 v[28:31], v[76:79], a[0:3], 0\n\t"
      "ds_read_b128 a[48:51], v8 offset:0\n\t"
      "v_mfma_f32_16x16x32_bf16 v[28:31], v[92:95], a[4:7], v[28:31]\n\t"
      "ds_read_b128 a[52:55], v60 offset:1024\n\t"
      "v_mfma_f32_16x16x32_bf16 v[28:31], v[108:111], a[8:11], v[28:31]\n\t"
      "ds_read_b128 a[56:59], v61 offset:2048\n\t"
      "v_mfma_f32_16x16x32_bf16 v[28:31], v[124:127], a[12:15], v[28:31]\n\t"
      "s_and_b32 s39, s34, 15\n\t"
      "s_cselect_b32 s53, s29, 0\n\t"
      "v_mfma_f32_16x16x32_bf16 v[28:31], v[140:143], a[16:19], v[28:31]\n\t"
      "s_add_u32 s39, s39, s53\n\t"
      "s_lshr_b64 s[34:35], s[34:35], 4\n\t"
      "v_mfma_f32_16x16x32_bf16 v[28:31], v[156:159], a[20:23], v[28:31]\n\t"
      "s_cmp_le_u32 s52, 11\n\t"
      "s_cselect_b32 s53, 3, 0\n\t"
      "s_min_u32 s53, s28, s53\n\t"
      "v_mfma_f32_16x16x32_bf16 v[32:35], a[60:63], a[0:3], 0\n\t"
      "s_sub_u32 s28, s28, s53\n\t"
      "s_sub_u32 s39, s39, s53\n\t"
      "v_mfma_f32_16x16x32_bf16 v[32:35], a[68:71], a[4:7], v[32:35]\n\t"
      "s_waitcnt lgkmcnt(2)\n\t"
      "v_mfma_f32_16x16x32_bf16 v[32:35], a[76:79], a[8:11], v[32:35]\n\t"
      "buffer_store_dwordx4 a[48:51], v9, s[44:47], s49 offen sc1\n\t"
      "v_mfma_f32_16x16x32_bf16 v[32:35], a[84:87], a[12:15], v[32:35]\n\t"
      "v_mfma_f32_16x16x32_bf16 v[32:35], a[92:95], a[16:19], v[32:35]\n\t"
      "v_mfma_f32_16x16x32_bf16 v[32:35], a[100:103], a[20:23], v[32:35]\n\t"
      "s_add_u32 m0, s18, s50\n\t"
      "v_mfma_f32_16x16x32_bf16 v[36:39], a[64:67], a[0:3], 0\n\t"
      "buffer_load_dwordx4 v5, s[40:43], s48 offen nt lds\n\t"
      "v_mfma_f32_16x16x32_bf16 v[36:39], a[72:75], a[4:7], v[36:39]\n\t"
      "s_memrealtime s[36:37]\n\t"
      "v_mfma_f32_16x16x32_bf16 v[36:39], a[80:83], a[8:11], v[36:39]\n\t"
      "v_mfma_f32_16x16x32_bf16 v[36:39], a[88:91], a[12:15], v[36:39]\n\t"
      "s_waitcnt lgkmcnt(1)\n\t"
      "v_mfma_f32_16x16x32_bf16 v[36:39], a[96:99], a[16:19], v[36:39]\n\t"
      "buffer_store_dwordx4 a[52:55], v10, s[44:47], s49 offen offset:1024 sc1\n\t"
      "v_mfma_f32_16x16x32_bf16 v[36:39], a[104:107], a[20:23], v[36:39]\n\t"
      "v_mfma_f32_16x16x32_bf16 v[16:19], v[160:163], a[24:27], v[16:19]\n\t"
      "ds_read_b128 a[0:3], v56 offset:0\n\t"
      "v_mfma_f32_16x16x32_bf16 v[16:19], v[176:179], a[28:31], v[16:19]\n\t"
      "ds_read_b128 a[4:7], v57 offset:0\n\t"
      "v_mfma_f32_16x16x32_bf16 v[16:19], v[192:195], a[32:35], v[16:19]\n\t"
      "ds_read_b128 a[8:11], v58 offset:0\n\t"
      "v_mfma_f32_16x16x32_bf16 v[16:19], v[208:211], a[36:39], v[16:19]\n\t"
      "ds_read_b128 a[12:15], v59 offset:0\n\t"
      "v_mfma_f32_16x16x32_bf16 v[16:19], v[224:227], a[40:43], v[16:19]\n\t"
      "ds_read_b128 a[16:19], v56 offset:256\n\t"
      "v_mfma_f32_16x16x32_bf16 v[16:19], v[240:243], a[44:47], v[16:19]\n\t"
      "ds_read_b128 a[20:23], v57 offset:256\n\t"
      "v_mfma_f32_16x16x32_bf16 v[20:23], v[164:167], a[24:27], v[20:23]\n\t"
      "s_add_u32 s51, s51, 12288\n\t"
      "s_cmp_eq_u32 s51, 160256\n\t"
      "s_cselect_b32 s51, 25088, s51\n\t"
      "v_mfma_f32_16x16x32_bf16 v[20:23], v[180:183], a[28:31], v[20:23]\n\t"
      "s_waitcnt lgkmcnt(6)\n\t"
      "v_mfma_f32_16x16x32_bf16 v[20:23], v[196:199], a[32:35], v[20:23]\n\t"
      "buffer_store_dwordx4 a[56:59], v11, s[44:47], s49 offen offset:2048 sc1\n\t"
      "v_mfma_f32_16x16x32_bf16 v[20:23], v[212:215], a[36:39], v[20:23]\n\t"
      "v_mfma_f32_16x16x32_bf16 v[20:23], v[228:231], a[40:43], v[20:23]\n\t"
      "s_waitcnt lgkmcnt(0)\n\t"
      "s_sub_u32 s53, s36, s38\n\t"
      "s_cmp_lt_i32 s53, 0\n\t"
      "v_mfma_f32_16x16x32_bf16 v[20:23], v[244:247], a[44:47], v[20:23]\n\t"
      "s_cbranch_scc1 .Lspin1_%=\n\t"
      ".Lspun1_%=:\n\t"
      "s_sub_i32 s53, s53, 30\n\t"
      "s_max_i32 s53, s53, 0\n\t"
      "v_mfma_f32_16x16x32_bf16 v[24:27], v[168:171], a[24:27], v[24:27]\n\t"
      "s_add_u32 s38, s38, s53\n\t"
      "s_add_u32 s38, s38, s39\n\t"
      "v_mfma_f32_16x16x32_bf16 v[24:27], v[184:187], a[28:31], v[24:27]\n\t"
      "v_cvt_pk_bf16_f32 v40, v16, v17\n\t"
      "v_cvt_pk_bf16_f32 v41, v18, v19\n\t"
      "v_mfma_f32_16x16x32_bf16 v[24:27], v[200:203], a[32:35], v[24:27]\n\t"
      "ds_write_b64 v0, v[40:41] offset:12544\n\t"
      "v_mfma_f32_16x16x32_bf16 v[24:27], v[216:219], a[36:39], v[24:27]\n\t"
      "buffer_load_dwordx4 v6, s[40:43], s48 offen offset:1024 nt lds\n\t"
      "v_mfma_f32_16x16x32_bf16 v[24:27], v[232:235], a[40:43], v[24:27]\n\t"
      "v_mfma_f32_16x16x32_bf16 v[24:27], v[248:251], a[44:47], v[24:27]\n\t"
      "v_mfma_f32_16x16x32_bf16 v[28:31], v[172:175], a[24:27], v[28:31]\n\t"
      "v_mfma_f32_16x16x32_bf16 v[28:31], v[188:191], a[28:31], v[28:31]\n\t"
      "v_cvt_pk_bf16_f32 v42, v20, v21\n\t"
      "v_cvt_pk_bf16_f32 v43, v22, v23\n\t"
      "v_mfma_f32_16x16x32_bf16 v[28:31], v[204:207], a[32:35], v[28:31]\n\t"
      "ds_write_b64 v0, v[42:43] offset:12576\n\t"
      "v_mfma_f32_16x16x32_bf16 v[28:31], v[220:223], a[36:39], v[28:31]\n\t"
      "v_mfma_f32_16x16x32_bf16 v[28:31], v[236:239], a[40:43], v[28:31]\n\t"
      "v_mfma_f32_16x16x32_bf16 v[28:31], v[252:255], a[44:47], v[28:31]\n\t"
      "v_mfma_f32_16x16x32_bf16 v[32:35], a[108:111], a[24:27], v[32:35]\n\t"
      "v_mfma_f32_16x16x32_bf16 v[32:35], a[116:119], a[28:31], v[32:35]\n\t"
      "v_cvt_pk_bf16_f32 v44, v24, v25\n\t"
      "v_cvt_pk_bf16_f32 v45, v26, v27\n\t"
      "v_mfma_f32_16x16x32_bf16 v[32:35], a[124:127], a[32:35], v[32:35]\n\t"
      "ds_write_b64 v0, v[44:45] offset:12608\n\t"
      "v_mfma_f32_16x16x32_bf16 v[32:35], a[132:135], a[36:39], v[32:35]\n\t"
      "buffer_load_dwordx4 v7, s[40:43], s48 offen offset:2048 nt lds\n\t"
      "v_mfma_f32_16x16x32_bf16 v[32:35], a[140:143], a[40:43], v[32:35]\n\t"
      "s_add_u32 s50, s50, 12288\n\t"
      "v_mfma_f32_16x16x32_bf16 v[32:35], a[148:151], a[44:47], v[32:35]\n\t"
      "v_cvt_pk_bf16_f32 v46, v28, v29\n\t"
      "v_cvt_pk_bf16_f32 v47, v30, v31\n\t"
      "v_mfma_f32_16x16x32_bf16 v[36:39], a[112:115], a[24:27], v[36:39]\n\t"
      "ds_write_b64 v0, v[46:47] offset:12640\n\t"
      "v_mfma_f32_16x16x32_bf16 v[36:39], a[120:123], a[28:31], v[36:39]\n\t"
      "s_cmp_eq_u32 s50, 160256\n\t"
      "s_cselect_b32 s50, 25088, s50\n\t"
      "v_mfma_f32_16x16x32_bf16 v[36:39], a[128:131], a[32:35], v[36:39]\n\t"
      "s_sub_u32 s52, s52, 1\n\t"
      "s_cmp_lg_u32 s52, 0\n\t"
      "v_mfma_f32_16x16x32_bf16 v[36:39], a[136:139], a[36:39], v[36:39]\n\t"
      "v_mfma_f32_16x16x32_bf16 v[36:39], a[144:147], a[40:43], v[36:39]\n\t"
      "v_cvt_pk_bf16_f32 v48, v32, v33\n\t"
      "v_cvt_pk_bf16_f32 v49, v34, v35\n\t"
      "v_mfma_f32_16x16x32_bf16 v[36:39], a[152:155], a[44:47], v[36:39]\n\t"
      "ds_write_b64 v0, v[48:49] offset:12672\n\t"
      "s_cbranch_scc1 .Lloop0_%=\n\t"
      "s_branch .Lendc_%=\n\t"
      ".Lloop1_%=:\n\t"
      "v_mfma_f32_16x16x32_bf16 v[16:19], v[64:67], a[0:3], 0\n\t"
      "ds_read_b128 a[24:27], v58 offset:256\n\t"
      "v_mfma_f32_16x16x32_bf16 v[16:19], v[80:83], a[4:7], v[16:19]\n\t"
      "ds_read_b128 a[28:31], v59 offset:256\n\t"
      "v_mfma_f32_16x16x32_bf16 v[16:19], v[96:99], a[8:11], v[16:19]\n\t"
      "ds_read_b128 a[32:35], v56 offset:512\n\t"
      "v_mfma_f32_16x16x32_bf16 v[16:19], v[112:115], a[12:15], v[16:19]\n\t"
      "ds_read_b128 a[36:39], v57 offset:512\n\t"
      "v_mfma_f32_16x16x32_bf16 v[16:19], v[128:131], a[16:19], v[16:19]\n\t"
      "ds_read_b128 a[40:43], v58 offset:512\n\t"
      "v_mfma_f32_16x16x32_bf16 v[16:19], v[144:147], a[20:23], v[16:19]\n\t"
      "ds_read_b128 a[44:47], v59 offset:512\n\t"
      "v_mfma_f32_16x16x32_bf16 v[20:23], v[68:71], a[0:3], 0\n\t"
      "v_cvt_pk_bf16_f32 v50, v36, v37\n\t"
      "v_cvt_pk_bf16_f32 v51, v38, v39\n\t"
      "v_mfma_f32_16x16x32_bf16 v[20:23], v[84:87], a[4:7], v[20:23]\n\t"
      "ds_write_b64 v0, v[50:51] offset:12704\n\t"
      "v_mfma_f32_16x16x32_bf16 v[20:23], v[100:103], a[8:11], v[20:23]\n\t"
      "v_add_u32 v52, s51, v15\n\t"
      "v_xor_b32 v53, 64, v52\n\t"
      "v_mfma_f32_16x16x32_bf16 v[20:23], v[116:119], a[12:15], v[20:23]\n\t"
      "v_xor_b32 v54, 128, v52\n\t"
      "v_xor_b32 v55, 192, v52\n\t"
      "v_mfma_f32_16x16x32_bf16 v[20:23], v[132:135], a[16:19], v[20:23]\n\t"
      "s_add_u32 s56, s56, 786432\n\t"
      "s_lshr_b32 s57, s57, 1\n\t"
      "v_mfma_f32_16x16x32_bf16 v[20:23], v[148:151], a[20:23], v[20:23]\n\t"
      "s_bitcmp1_b32 s57, 0\n\t"
      "s_cselect_b32 s48, 24576, 0\n\t"
      "s_add_u32 s48, s48, s56\n\t"
      "v_mfma_f32_16x16x32_bf16 v[24:27], v[72:75], a[0:3], 0\n\t"
      "v_mfma_f32_16x16x32_bf16 v[24:27], v[88:91], a[4:7], v[24:27]\n\t"
      "v_mfma_f32_16x16x32_bf16 v[24:27], v[104:107], a[8:11], v[24:27]\n\t"
      "v_mfma_f32_16x16x32_bf16 v[24:27], v[120:123], a[12:15], v[24:27]\n\t"
      "s_waitcnt vmcnt(12)\n\t"
      "s_waitcnt lgkmcnt(0)\n\t"
      "s_barrier\n\t"
      "v_mfma_f32_16x16x32_bf16 v[24:27], v[136:139], a[16:19], v[24:27]\n\t"
      "v_mfma_f32_16x16x32_bf16 v[24:27], v[152:155], a[20:23], v[24:27]\n\t"
      "v_mfma_f32_16x16x32_bf16 v[28:31], v[76:79], a[0:3], 0\n\t"
      "ds_read_b128 a[48:51], v8 offset:12544\n\t"
      "v_mfma_f32_16x16x32_bf16 v[28:31], v[92:95], a[4:7], v[28:31]\n\t"
      "ds_read_b128 a[52:55], v60 offset:13568\n\t"
      "v_mfma_f32_16x16x32_bf16 v[28:31], v[108:111], a[8:11], v[28:31]\n\t"
      "ds_read_b128 a[56:59], v61 offset:14592\n\t"
      "v_mfma_f32_16x16x32_bf16 v[28:31], v[124:127], a[12:15], v[28:31]\n\t"
      "v_mfma_f32_16x16x32_bf16 v[28:31], v[140:143], a[16:19], v[28:31]\n\t"
      "v_mfma_f32_16x16x32_bf16 v[28:31], v[156:159], a[20:23], v[28:31]\n\t"
      "v_mfma_f32_16x16x32_bf16 v[32:35], a[60:63], a[0:3], 0\n\t"
      "v_mfma_f32_16x16x32_bf16 v[32:35], a[68:71], a[4:7], v[32:35]\n\t"
      "s_waitcnt lgkmcnt(2)\n\t"
      "v_mfma_f32_16x16x32_bf16 v[32:35], a[76:79], a[8:11], v[32:35]\n\t"
      "buffer_store_dwordx4 a[48:51], v12, s[44:47], s49 offen sc1\n\t"
      "v_mfma_f32_16x16x32_bf16 v[32:35], a[84:87], a[12:15], v[32:35]\n\t"
      "v_mfma_f32_16x16x32_bf16 v[32:35], a[92:95], a[16:19], v[32:35]\n\t"
      "v_mfma_f32_16x16x32_bf16 v[32:35], a[100:103], a[20:23], v[32:35]\n\t"
      "s_add_u32 m0, s18, s50\n\t"
      "v_mfma_f32_16x16x32_bf16 v[36:39], a[64:67], a[0:3], 0\n\t"
      "buffer_load_dwordx4 v2, s[40:43], s48 offen nt lds\n\t"
      "v_mfma_f32_16x16x32_bf16 v[36:39], a[72:75], a[4:7], v[36:39]\n\t"
      "v_mfma_f32_16x16x32_bf16 v[36:39], a[80:83], a[8:11], v[36:39]\n\t"
      "v_mfma_f32_16x16x32_bf16 v[36:39], a[88:91], a[12:15], v[36:39]\n\t"
      "s_waitcnt lgkmcnt(1)\n\t"
      "v_mfma_f32_16x16x32_bf16 v[36:39], a[96:99], a[16:19], v[36:39]\n\t"
      "buffer_store_dwordx4 a[52:55], v13, s[44:47], s49 offen offset:1024 sc1\n\t"
      "v_mfma_f32_16x16x32_bf16 v[36:39], a[104:107], a[20:23], v[36:39]\n\t"
      "v_mfma_f32_16x16x32_bf16 v[16:19], v[160:163], a[24:27], v[16:19]\n\t"
      "ds_read_b128 a[0:3], v52 offset:0\n\t"
      "v_mfma_f32_16x16x32_bf16 v[16:19], v[176:179], a[28:31], v[16:19]\n\t"
      "ds_read_b128 a[4:7], v53 offset:0\n\t"
      "v_mfma_f32_16x16x32_bf16 v[16:19], v[192:195], a[32:35], v[16:19]\n\t"
      "ds_read_b128 a[8:11], v54 offset:0\n\t"
      "v_mfma_f32_16x16x32_bf16 v[16:19], v[208:211], a[36:39], v[16:19]\n\t"
      "ds_read_b128 a[12:15], v55 offset:0\n\t"
      "v_mfma_f32_16x16x32_bf16 v[16:19], v[224:227], a[40:43], v[16:19]\n\t"
      "ds_read_b128 a[16:19], v52 offset:256\n\t"
      "v_mfma_f32_16x16x32_bf16 v[16:19], v[240:243], a[44:47], v[16:19]\n\t"
      "ds_read_b128 a[20:23], v53 offset:256\n\t"
      "v_mfma_f32_16x16x32_bf16 v[20:23], v[164:167], a[24:27], v[20:23]\n\t"
      "s_add_u32 s51, s51, 12288\n\t"
      "s_cmp_eq_u32 s51, 160256\n\t"
      "s_cselect_b32 s51, 25088, s51\n\t"
      "v_mfma_f32_16x16x32_bf16 v[20:23], v[180:183], a[28:31], v[20:23]\n\t"
      "s_waitcnt lgkmcnt(6)\n\t"
      "v_mfma_f32_16x16x32_bf16 v[20:23], v[196:199], a[32:35], v[20:23]\n\t"
      "buffer_store_dwordx4 a[56:59], v14, s[44:47], s49 offen offset:2048 sc1\n\t"
      "v_mfma_f32_16x16x32_bf16 v[20:23], v[212:215], a[36:39], v[20:23]\n\t"
      "v_mfma_f32_16x16x32_bf16 v[20:23], v[228:231], a[40:43], v[20:23]\n\t"
      "s_waitcnt lgkmcnt(0)\n\t"
      "v_mfma_f32_16x16x32_bf16 v[20:23], v[244:247], a[44:47], v[20:23]\n\t"
      "v_mfma_f32_16x16x32_bf16 v[24:27], v[168:171], a[24:27], v[24:27]\n\t"
      "v_mfma_f32_16x16x32_bf16 v[24:27], v[184:187], a[28:31], v[24:27]\n\t"
      "v_cvt_pk_bf16_f32 v40, v16, v17\n\t"
      "v_cvt_pk_bf16_f32 v41, v18, v19\n\t"
      "v_mfma_f32_16x16x32_bf16 v[24:27], v[200:203], a[32:35], v[24:27]\n\t"
      "ds_write_b64 v0, v[40:41] offset:0\n\t"
      "v_mfma_f32_16x16x32_bf16 v[24:27], v[216:219], a[36:39], v[24:27]\n\t"
      "buffer_load_dwordx4 v3, s[40:43], s48 offen offset:1024 nt lds\n\t"
      "v_mfma_f32_16x16x32_bf16 v[24:27], v[232:235], a[40:43], v[24:27]\n\t"
      "s_add_u32 s58, s58, 786432\n\t"
      "s_lshr_b32 s59, s59, 1\n\t"
      "v_mfma_f32_16x16x32_bf16 v[24:27], v[248:251], a[44:47], v[24:27]\n\t"
      "s_bitcmp1_b32 s59, 0\n\t"
      "s_cselect_b32 s49, 24576, 0\n\t"
      "s_add_u32 s49, s49, s58\n\t"
      "v_mfma_f32_16x16x32_bf16 v[28:31], v[172:175], a[24:27], v[28:31]\n\t"
      "v_mfma_f32_16x16x32_bf16 v[28:31], v[188:191], a[28:31], v[28:31]\n\t"
      "v_cvt_pk_bf16_f32 v42, v20, v21\n\t"
      "v_cvt_pk_bf16_f32 v43, v22, v23\n\t"
      "v_mfma_f32_16x16x32_bf16 v[28:31], v[204:207], a[32:35], v[28:31]\n\t"
      "ds_write_b64 v0, v[42:43] offset:32\n\t"
      "v_mfma_f32_16x16x32_bf16 v[28:31], v[220:223], a[36:39], v[28:31]\n\t"
      "v_mfma_f32_16x16x32_bf16 v[28:31], v[236:239], a[40:43], v[28:31]\n\t"
      "v_mfma_f32_16x16x32_bf16 v[28:31], v[252:255], a[44:47], v[28:31]\n\t"
      "v_mfma_f32_16x16x32_bf16 v[32:35], a[108:111], a[24:27], v[32:35]\n\t"
      "v_mfma_f32_16x16x32_bf16 v[32:35], a[116:119], a[28:31], v[32:35]\n\t"
      "v_cvt_pk_bf16_f32 v44, v24, v25\n\t"
      "v_cvt_pk_bf16_f32 v45, v26, v27\n\t"
      "v_mfma_f32_16x16x32_bf16 v[32:35], a[124:127], a[32:35], v[32:35]\n\t"
      "ds_write_b64 v0, v[44:45] offset:64\n\t"
      "v_mfma_f32_16x16x32_bf16 v[32:35], a[132:135], a[36:39], v[32:35]\n\t"
      "buffer_load_dwordx4 v4, s[40:43], s48 offen offset:2048 nt lds\n\t"
      "v_mfma_f32_16x16x32_bf16 v[32:35], a[140:143], a[40:43], v[32:35]\n\t"
      "s_add_u32 s50, s50, 12288\n\t"
      "v_mfma_f32_16x16x32_bf16 v[32:35], a[148:151], a[44:47], v[32:35]\n\t"
      "v_cvt_pk_bf16_f32 v46, v28, v29\n\t"
      "v_cvt_pk_bf16_f32 v47, v30, v31\n\t"
      "v_mfma_f32_16x16x32_bf16 v[36:39], a[112:115], a[24:27], v[36:39]\n\t"
      "ds_write_b64 v0, v[46:47] offset:96\n\t"
      "v_mfma_f32_16x16x32_bf16 v[36:39], a[120:123], a[28:31], v[36:39]\n\t"
      "s_cmp_eq_u32 s50, 160256\n\t"
      "s_cselect_b32 s50, 25088, s50\n\t"
      "v_mfma_f32_16x16x32_bf16 v[36:39], a[128:131], a[32:35], v[36:39]\n\t"
      "v_mfma_f32_16x16x32_bf16 v[36:39], a[136:139], a[36:39], v[36:39]\n\t"
      "v_mfma_f32_16x16x32_bf16 v[36:39], a[144:147], a[40:43], v[36:39]\n\t"
      "v_cvt_pk_bf16_f32 v48, v32, v33\n\t"
      "v_cvt_pk_bf16_f32 v49, v34, v35\n\t"
      "v_mfma_f32_16x16x32_bf16 v[36:39], a[152:155], a[44:47], v[36:39]\n\t"
      "ds_write_b64 v0, v[48:49] offset:128\n\t"
      ".Lmid1_%=:\n\t"
      "v_mfma_f32_16x16x32_bf16 v[16:19], v[64:67], a[0:3], 0\n\t"
      "ds_read_b128 a[24:27], v54 offset:256\n\t"
      "v_mfma_f32_16x16x32_bf16 v[16:19], v[80:83], a[4:7], v[16:19]\n\t"
      "ds_read_b128 a[28:31], v55 offset:256\n\t"
      "v_mfma_f32_16x16x32_bf16 v[16:19], v[96:99], a[8:11], v[16:19]\n\t"
      "ds_read_b128 a[32:35], v52 offset:512\n\t"
      "v_mfma_f32_16x16x32_bf16 v[16:19], v[112:115], a[12:15], v[16:19]\n\t"
      "ds_read_b128 a[36:39], v53 offset:512\n\t"
      "v_mfma_f32_16x16x32_bf16 v[16:19], v[128:131], a[16:19], v[16:19]\n\t"
      "ds_read_b128 a[40:43], v54 offset:512\n\t"
      "v_mfma_f32_16x16x32_bf16 v[16:19], v[144:147], a[20:23], v[16:19]\n\t"
      "ds_read_b128 a[44:47], v55 offset:512\n\t"
      "v_mfma_f32_16x16x32_bf16 v[20:23], v[68:71], a[0:3], 0\n\t"
      "v_cvt_pk_bf16_f32 v50, v36, v37\n\t"
      "v_cvt_pk_bf16_f32 v51, v38, v39\n\t"
      "v_mfma_f32_16x16x32_bf16 v[20:23], v[84:87], a[4:7], v[20:23]\n\t"
      "ds_write_b64 v0, v[50:51] offset:160\n\t"
      "v_mfma_f32_16x16x32_bf16 v[20:23], v[100:103], a[8:11], v[20:23]\n\t"
      "v_add_u32 v56, s51, v15\n\t"
      "v_xor_b32 v57, 64, v56\n\t"
      "v_mfma_f32_16x16x32_bf16 v[20:23], v[116:119], a[12:15], v[20:23]\n\t"
      "v_xor_b32 v58, 128, v56\n\t"
      "v_xor_b32 v59, 192, v56\n\t"
      "v_mfma_f32_16x16x32_bf16 v[20:23], v[132:135], a[16:19], v[20:23]\n\t"
      "v_mfma_f32_16x16x32_bf16 v[20:23], v[148:151], a[20:23], v[20:23]\n\t"
      "v_mfma_f32_16x16x32_bf16 v[24:27], v[72:75], a[0:3], 0\n\t"
      "v_mfma_f32_16x16x32_bf16 v[24:27], v[88:91], a[4:7], v[24:27]\n\t"
      "v_mfma_f32_16x16x32_bf16 v[24:27], v[104:107], a[8:11], v[24:27]\n\t"
      "v_mfma_f32_16x16x32_bf16 v[24:27], v[120:123], a[12:15], v[24:27]\n\t"
      "s_waitcnt vmcnt(12)\n\t"
      "s_waitcnt lgkmcnt(0)\n\t"
      "s_barrier\n\t"
      "v_mfma_f32_16x16x32_bf16 v[24:27], v[136:139], a[16:19], v[24:27]\n\t"
      "v_mfma_f32_16x16x32_bf16 v[24:27], v[152:155], a[20:23], v[24:27]\n\t"
      "v_mfma_f32_16x16x32_bf16 v[28:31], v[76:79], a[0:3], 0\n\t"
      "ds_read_b128 a[48:51], v8 offset:0\n\t"
      "v_mfma_f32_16x16x32_bf16 v[28:31], v[92:95], a[4:7], v[28:31]\n\t"
      "ds_read_b128 a[52:55], v60 offset:1024\n\t"
      "v_mfma_f32_16x16x32_bf16 v[28:31], v[108:111], a[8:11], v[28:31]\n\t"
      "ds_read_b128 a[56:59], v61 offset:2048\n\t"
      "v_mfma_f32_16x16x32_bf16 v[28:31], v[124:127], a[12:15], v[28:31]\n\t"
      "v_mfma_f32_16x16x32_bf16 v[28:31], v[140:143], a[16:19], v[28:31]\n\t"
      "v_mfma_f32_16x16x32_bf16 v[28:31], v[156:159], a[20:23], v[28:31]\n\t"
      "v_mfma_f32_16x16x32_bf16 v[32:35], a[60:63], a[0:3], 0\n\t"
      "v_mfma_f32_16x16x32_bf16 v[32:35], a[68:71], a[4:7], v[32:35]\n\t"
      "s_waitcnt lgkmcnt(2)\n\t"
      "v_mfma_f32_16x16x32_bf16 v[32:35], a[76:79], a[8:11], v[32:35]\n\t"
      "buffer_store_dwordx4 a[48:51], v9, s[44:47], s49 offen sc1\n\t"
      "v_mfma_f32_16x16x32_bf16 v[32:35], a[84:87], a[12:15], v[32:35]\n\t"
      "v_mfma_f32_16x16x32_bf16 v[32:35], a[92:95], a[16:19], v[32:35]\n\t"
      "v_mfma_f32_16x16x32_bf16 v[32:35], a[100:103], a[20:23], v[32:35]\n\t"
      "s_add_u32 m0, s18, s50\n\t"
      "v_mfma_f32_16x16x32_bf16 v[36:39], a[64:67], a[0:3], 0\n\t"
      "buffer_load_dwordx4 v5, s[40:43], s48 offen nt lds\n\t"
      "v_mfma_f32_16x16x32_bf16 v[36:39], a[72:75], a[4:7], v[36:39]\n\t"
      "v_mfma_f32_16x16x32_bf16 v[36:39], a[80:83], a[8:11], v[36:39]\n\t"
      "v_mfma_f32_16x16x32_bf16 v[36:39], a[88:91], a[12:15], v[36:39]\n\t"
      "s_waitcnt lgkmcnt(1)\n\t"
      "v_mfma_f32_16x16x32_bf16 v[36:39], a[96:99], a[16:19], v[36:39]\n\t"
      "buffer_store_dwordx4 a[52:55], v10, s[44:47], s49 offen offset:1024 sc1\n\t"
      "v_mfma_f32_16x16x32_bf16 v[36:39], a[104:107], a[20:23], v[36:39]\n\t"
      "v_mfma_f32_16x16x32_bf16 v[16:19], v[160:163], a[24:27], v[16:19]\n\t"
      "ds_read_b128 a[0:3], v56 offset:0\n\t"
      "v_mfma_f32_16x16x32_bf16 v[16:19], v[176:179], a[28:31], v[16:19]\n\t"
      "ds_read_b128 a[4:7], v57 offset:0\n\t"
      "v_mfma_f32_16x16x32_bf16 v[16:19], v[192:195], a[32:35], v[16:19]\n\t"
      "ds_read_b128 a[8:11], v58 offset:0\n\t"
      "v_mfma_f32_16x16x32_bf16 v[16:19], v[208:211], a[36:39], v[16:19]\n\t"
      "ds_read_b128 a[12:15], v59 offset:0\n\t"
      "v_mfma_f32_16x16x32_bf16 v[16:19], v[224:227], a[40:43], v[16:19]\n\t"
      "ds_read_b128 a[16:19], v56 offset:256\n\t"
      "v_mfma_f32_16x16x32_bf16 v[16:19], v[240:243], a[44:47], v[16:19]\n\t"
      "ds_read_b128 a[20:23], v57 offset:256\n\t"
      "v_mfma_f32_16x16x32_bf16 v[20:23], v[164:167], a[24:27], v[20:23]\n\t"
      "s_add_u32 s51, s51, 12288\n\t"
      "s_cmp_eq_u32 s51, 160256\n\t"
      "s_cselect_b32 s51, 25088, s51\n\t"
      "v_mfma_f32_16x16x32_bf16 v[20:23], v[180:183], a[28:31], v[20:23]\n\t"
      "s_waitcnt lgkmcnt(6)\n\t"
      "v_mfma_f32_16x16x32_bf16 v[20:23], v[196:199], a[32:35], v[20:23]\n\t"
      "buffer_store_dwordx4 a[56:59], v11, s[44:47], s49 offen offset:2048 sc1\n\t"
      "v_mfma_f32_16x16x32_bf16 v[20:23], v[212:215], a[36:39], v[20:23]\n\t"
      "v_mfma_f32_16x16x32_bf16 v[20:23], v[228:231], a[40:43], v[20:23]\n\t"
      "s_waitcnt lgkmcnt(0)\n\t"
      "v_mfma_f32_16x16x32_bf16 v[20:23], v[244:247], a[44:47], v[20:23]\n\t"
      "v_mfma_f32_16x16x32_bf16 v[24:27], v[168:171], a[24:27], v[24:27]\n\t"
      "v_mfma_f32_16x16x32_bf16 v[24:27], v[184:187], a[28:31], v[24:27]\n\t"
      "v_cvt_pk_bf16_f32 v40, v16, v17\n\t"
      "v_cvt_pk_bf16_f32 v41, v18, v19\n\t"
      "v_mfma_f32_16x16x32_bf16 v[24:27], v[200:203], a[32:35], v[24:27]\n\t"
      "ds_write_b64 v0, v[40:41] offset:12544\n\t"
      "v_mfma_f32_16x16x32_bf16 v[24:27], v[216:219], a[36:39], v[24:27]\n\t"
      "buffer_load_dwordx4 v6, s[40:43], s48 offen offset:1024 nt lds\n\t"
      "v_mfma_f32_16x16x32_bf16 v[24:27], v[232:235], a[40:43], v[24:27]\n\t"
      "v_mfma_f32_16x16x32_bf16 v[24:27], v[248:251], a[44:47], v[24:27]\n\t"
      "v_mfma_f32_16x16x32_bf16 v[28:31], v[172:175], a[24:27], v[28:31]\n\t"
      "v_mfma_f32_16x16x32_bf16 v[28:31], v[188:191], a[28:31], v[28:31]\n\t"
      "v_cvt_pk_bf16_f32 v42, v20, v21\n\t"
      "v_cvt_pk_bf16_f32 v43, v22, v23\n\t"
      "v_mfma_f32_16x16x32_bf16 v[28:31], v[204:207], a[32:35], v[28:31]\n\t"
      "ds_write_b64 v0, v[42:43] offset:12576\n\t"
      "v_mfma_f32_16x16x32_bf16 v[28:31], v[220:223], a[36:39], v[28:31]\n\t"
      "v_mfma_f32_16x16x32_bf16 v[28:31], v[236:239], a[40:43], v[28:31]\n\t"
      "v_mfma_f32_16x16x32_bf16 v[28:31], v[252:255], a[44:47], v[28:31]\n\t"
      "v_mfma_f32_16x16x32_bf16 v[32:35], a[108:111], a[24:27], v[32:35]\n\t"
      "v_mfma_f32_16x16x32_bf16 v[32:35], a[116:119], a[28:31], v[32:35]\n\t"
      "v_cvt_pk_bf16_f32 v44, v24, v25\n\t"
      "v_cvt_pk_bf16_f32 v45, v26, v27\n\t"
      "v_mfma_f32_16x16x32_bf16 v[32:35], a[124:127], a[32:35], v[32:35]\n\t"
      "ds_write_b64 v0, v[44:45] offset:12608\n\t"
      "v_mfma_f32_16x16x32_bf16 v[32:35], a[132:135], a[36:39], v[32:35]\n\t"
      "buffer_load_dwordx4 v7, s[40:43], s48 offen offset:2048 nt lds\n\t"
      "v_mfma_f32_16x16x32_bf16 v[32:35], a[140:143], a[40:43], v[32:35]\n\t"
      "s_add_u32 s50, s50, 12288\n\t"
      "v_mfma_f32_16x16x32_bf16 v[32:35], a[148:151], a[44:47], v[32:35]\n\t"
      "v_cvt_pk_bf16_f32 v46, v28, v29\n\t"
      "v_cvt_pk_bf16_f32 v47, v30, v31\n\t"
      "v_mfma_f32_16x16x32_bf16 v[36:39], a[112:115], a[24:27], v[36:39]\n\t"
      "ds_write_b64 v0, v[46:47] offset:12640\n\t"
      "v_mfma_f32_16x16x32_bf16 v[36:39], a[120:123], a[28:31], v[36:39]\n\t"
      "s_cmp_eq_u32 s50, 160256\n\t"
      "s_cselect_b32 s50, 25088, s50\n\t"
      "v_mfma_f32_16x16x32_bf16 v[36:39], a[128:131], a[32:35], v[36:39]\n\t"
      "s_sub_u32 s52, s52, 1\n\t"
      "s_cmp_lg_u32 s52, 0\n\t"
      "v_mfma_f32_16x16x32_bf16 v[36:39], a[136:139], a[36:39], v[36:39]\n\t"
      "v_mfma_f32_16x16x32_bf16 v[36:39], a[144:147], a[40:43], v[36:39]\n\t"
      "v_cvt_pk_bf16_f32 v48, v32, v33\n\t"
      "v_cvt_pk_bf16_f32 v49, v34, v35\n\t"
      "v_mfma_f32_16x16x32_bf16 v[36:39], a[152:155], a[44:47], v[36:39]\n\t"
      "ds_write_b64 v0, v[48:49] offset:12672\n\t"
      "s_cbranch_scc1 .Lloop1_%=\n\t"
      "s_branch .Lendc_%=\n\t"
      ".Lloop2_%=:\n\t"
      "v_mfma_f32_16x16x32_bf16 v[16:19], v[64:67], a[0:3], 0\n\t"
      "ds_read_b128 a[24:27], v58 offset:256\n\t"
      "v_mfma_f32_16x16x32_bf16 v[16:19], v[80:83], a[4:7], v[16:19]\n\t"
      "ds_read_b128 a[28:31], v59 offset:256\n\t"
      "v_mfma_f32_16x16x32_bf16 v[16:19], v[96:99], a[8:11], v[16:19]\n\t"
      "ds_read_b128 a[32:35], v56 offset:512\n\t"
      "v_mfma_f32_16x16x32_bf16 v[16:19], v[112:115], a[12:15], v[16:19]\n\t"
      "ds_read_b128 a[36:39], v57 offset:512\n\t"
      "v_mfma_f32_16x16x32_bf16 v[16:19], v[128:131], a[16:19], v[16:19]\n\t"
      "ds_read_b128 a[40:43], v58 offset:512\n\t"
      "v_mfma_f32_16x16x32_bf16 v[16:19], v[144:147], a[20:23], v[16:19]\n\t"
      "ds_read_b128 a[44:47], v59 offset:512\n\t"
      "v_mfma_f32_16x16x32_bf16 v[20:23], v[68:71], a[0:3], 0\n\t"
      "v_cvt_pk_bf16_f32 v50, v36, v37\n\t"
      "v_cvt_pk_bf16_f32 v51, v38, v39\n\t"
      "v_mfma_f32_16x16x32_bf16 v[20:23], v[84:87], a[4:7], v[20:23]\n\t"
      "ds_write_b64 v0, v[50:51] offset:12704\n\t"
      "v_mfma_f32_16x16x32_bf16 v[20:23], v[100:103], a[8:11], v[20:23]\n\t"
      "v_add_u32 v52, s51, v15\n\t"
      "v_xor_b32 v53, 64, v52\n\t"
      "v_mfma_f32_16x16x32_bf16 v[20:23], v[116:119], a[12:15], v[20:23]\n\t"
      "v_xor_b32 v54, 128, v52\n\t"
      "v_xor_b32 v55, 192, v52\n\t"
      "v_mfma_f32_16x16x32_bf16 v[20:23], v[132:135], a[16:19], v[20:23]\n\t"
      "s_add_u32 s56, s56, 786432\n\t"
      "s_lshr_b32 s57, s57, 1\n\t"
      "v_mfma_f32_16x16x32_bf16 v[20:23], v[148:151], a[20:23], v[20:23]\n\t"
      "s_bitcmp1_b32 s57, 0\n\t"
      "s_cselect_b32 s48, 24576, 0\n\t"
      "s_add_u32 s48, s48, s56\n\t"
      "v_mfma_f32_16x16x32_bf16 v[24:27], v[72:75], a[0:3], 0\n\t"
      "v_mfma_f32_16x16x32_bf16 v[24:27], v[88:91], a[4:7], v[24:27]\n\t"
      "v_mfma_f32_16x16x32_bf16 v[24:27], v[104:107], a[8:11], v[24:27]\n\t"
      "v_mfma_f32_16x16x32_bf16 v[24:27], v[120:123], a[12:15], v[24:27]\n\t"
      "v_mfma_f32_16x16x32_bf16 v[24:27], v[136:139], a[16:19], v[24:27]\n\t"
      "s_waitcnt vmcnt(12)\n\t"
      "s_waitcnt lgkmcnt(0)\n\t"
      "s_barrier\n\t"
      "v_mfma_f32_16x16x32_bf16 v[24:27], v[152:155], a[20:23], v[24:27]\n\t"
      "v_mfma_f32_16x16x32_bf16 v[28:31], v[76:79], a[0:3], 0\n\t"
      "ds_read_b128 a[48:51], v8 offset:12544\n\t"
      "v_mfma_f32_16x16x32_bf16 v[28:31], v[92:95], a[4:7], v[28:31]\n\t"
      "ds_read_b128 a[52:55], v60 offset:13568\n\t"
      "v_mfma_f32_16x16x32_bf16 v[28:31], v[108:111], a[8:11], v[28:31]\n\t"
      "ds_read_b128 a[56:59], v61 offset:14592\n\t"
      "v_mfma_f32_16x16x32_bf16 v[28:31], v[124:127], a[12:15], v[28:31]\n\t"
      "v_mfma_f32_16x16x32_bf16 v[28:31], v[140:143], a[16:19], v[28:31]\n\t"
      "v_mfma_f32_16x16x32_bf16 v[28:31], v[156:159], a[20:23], v[28:31]\n\t"
      "v_mfma_f32_16x16x32_bf16 v[32:35], a[60:63], a[0:3], 0\n\t"
      "v_mfma_f32_16x16x32_bf16 v[32:35], a[68:71], a[4:7], v[32:35]\n\t"
      "s_waitcnt lgkmcnt(2)\n\t"
      "v_mfma_f32_16x16x32_bf16 v[32:35], a[76:79], a[8:11], v[32:35]\n\t"
      "buffer_store_dwordx4 a[48:51], v12, s[44:47], s49 offen sc1\n\t"
      "v_mfma_f32_16x16x32_bf16 v[32:35], a[84:87], a[12:15], v[32:35]\n\t"
      "v_mfma_f32_16x16x32_bf16 v[32:35], a[92:95], a[16:19], v[32:35]\n\t"
      "v_mfma_f32_16x16x32_bf16 v[32:35], a[100:103], a[20:23], v[32:35]\n\t"
      "s_add_u32 m0, s18, s50\n\t"
      "v_mfma_f32_16x16x32_bf16 v[36:39], a[64:67], a[0:3], 0\n\t"
      "buffer_load_dwordx4 v2, s[40:43], s48 offen nt lds\n\t"
      "v_mfma_f32_16x16x32_bf16 v[36:39], a[72:75], a[4:7], v[36:39]\n\t"
      "v_mfma_f32_16x16x32_bf16 v[36:39], a[80:83], a[8:11], v[36:39]\n\t"
      "v_mfma_f32_16x16x32_bf16 v[36:39], a[88:91], a[12:15], v[36:39]\n\t"
      "s_waitcnt lgkmcnt(1)\n\t"
      "v_mfma_f32_16x16x32_bf16 v[36:39], a[96:99], a[16:19], v[36:39]\n\t"
      "buffer_store_dwordx4 a[52:55], v13, s[44:47], s49 offen offset:1024 sc1\n\t"
      "v_mfma_f32_16x16x32_bf16 v[36:39], a[104:107], a[20:23], v[36:39]\n\t"
      "v_mfma_f32_16x16x32_bf16 v[16:19], v[160:163], a[24:27], v[16:19]\n\t"
      "ds_read_b128 a[0:3], v52 offset:0\n\t"
      "v_mfma_f32_16x16x32_bf16 v[16:19], v[176:179], a[28:31], v[16:19]\n\t"
      "ds_read_b128 a[4:7], v53 offset:0\n\t"
      "v_mfma_f32_16x16x32_bf16 v[16:19], v[192:195], a[32:35], v[16:19]\n\t"
      "ds_read_b128 a[8:11], v54 offset:0\n\t"
      "v_mfma_f32_16x16x32_bf16 v[16:19], v[208:211], a[36:39], v[16:19]\n\t"
      "ds_read_b128 a[12:15], v55 offset:0\n\t"
      "v_mfma_f32_16x16x32_bf16 v[16:19], v[224:227], a[40:43], v[16:19]\n\t"
      "ds_read_b128 a[16:19], v52 offset:256\n\t"
      "v_mfma_f32_16x16x32_bf16 v[16:19], v[240:243], a[44:47], v[16:19]\n\t"
      "ds_read_b128 a[20:23], v53 offset:256\n\t"
      "v_mfma_f32_16x16x32_bf16 v[20:23], v[164:167], a[24:27], v[20:23]\n\t"
      "s_add_u32 s51, s51, 12288\n\t"
      "s_cmp_eq_u32 s51, 160256\n\t"
      "s_cselect_b32 s51, 25088, s51\n\t"
      "v_mfma_f32_16x16x32_bf16 v[20:23], v[180:183], a[28:31], v[20:23]\n\t"
      "s_waitcnt lgkmcnt(6)\n\t"
      "v_mfma_f32_16x16x32_bf16 v[20:23], v[196:199], a[32:35], v[20:23]\n\t"
      "buffer_store_dwordx4 a[56:59], v14, s[44:47], s49 offen offset:2048 sc1\n\t"
      "v_mfma_f32_16x16x32_bf16 v[20:23], v[212:215], a[36:39], v[20:23]\n\t"
      "v_mfma_f32_16x16x32_bf16 v[20:23], v[228:231], a[40:43], v[20:23]\n\t"
      "s_waitcnt lgkmcnt(0)\n\t"
      "v_mfma_f32_16x16x32_bf16 v[20:23], v[244:247], a[44:47], v[20:23]\n\t"
      "v_mfma_f32_16x16x32_bf16 v[24:27], v[168:171], a[24:27], v[24:27]\n\t"
      "v_mfma_f32_16x16x32_bf16 v[24:27], v[184:187], a[28:31], v[24:27]\n\t"
      "v_cvt_pk_bf16_f32 v40, v16, v17\n\t"
      "v_cvt_pk_bf16_f32 v41, v18, v19\n\t"
      "v_mfma_f32_16x16x32_bf16 v[24:27], v[200:203], a[32:35], v[24:27]\n\t"
      "ds_write_b64 v0, v[40:41] offset:0\n\t"
      "v_mfma_f32_16x16x32_bf16 v[24:27], v[216:219], a[36:39], v[24:27]\n\t"
      "buffer_load_dwordx4 v3, s[40:43], s48 offen offset:1024 nt lds\n\t"
      "v_mfma_f32_16x16x32_bf16 v[24:27], v[232:235], a[40:43], v[24:27]\n\t"
      "s_add_u32 s58, s58, 786432\n\t"
      "s_lshr_b32 s59, s59, 1\n\t"
      "v_mfma_f32_16x16x32_bf16 v[24:27], v[248:251], a[44:47], v[24:27]\n\t"
      "s_bitcmp1_b32 s59, 0\n\t"
      "s_cselect_b32 s49, 24576, 0\n\t"
      "s_add_u32 s49, s49, s58\n\t"
      "v_mfma_f32_16x16x32_bf16 v[28:31], v[172:175], a[24:27], v[28:31]\n\t"
      "v_mfma_f32_16x16x32_bf16 v[28:31], v[188:191], a[28:31], v[28:31]\n\t"
      "v_cvt_pk_bf16_f32 v42, v20, v21\n\t"
      "v_cvt_pk_bf16_f32 v43, v22, v23\n\t"
      "v_mfma_f32_16x16x32_bf16 v[28:31], v[204:207], a[32:35], v[28:31]\n\t"
      "ds_write_b64 v0, v[42:43] offset:32\n\t"
      "v_mfma_f32_16x16x32_bf16 v[28:31], v[220:223], a[36:39], v[28:31]\n\t"
      "v_mfma_f32_16x16x32_bf16 v[28:31], v[236:239], a[40:43], v[28:31]\n\t"
      "v_mfma_f32_16x16x32_bf16 v[28:31], v[252:255], a[44:47], v[28:31]\n\t"
      "v_mfma_f32_16x16x32_bf16 v[32:35], a[108:111], a[24:27], v[32:35]\n\t"
      "v_mfma_f32_16x16x32_bf16 v[32:35], a[116:119], a[28:31], v[32:35]\n\t"
      "v_cvt_pk_bf16_f32 v44, v24, v25\n\t"
      "v_cvt_pk_bf16_f32 v45, v26, v27\n\t"
      "v_mfma_f32_16x16x32_bf16 v[32:35], a[124:127], a[32:35], v[32:35]\n\t"
      "ds_write_b64 v0, v[44:45] offset:64\n\t"
      "v_mfma_f32_16x16x32_bf16 v[32:35], a[132:135], a[36:39], v[32:35]\n\t"
      "buffer_load_dwordx4 v4, s[40:43], s48 offen offset:2048 nt lds\n\t"
      "v_mfma_f32_16x16x32_bf16 v[32:35], a[140:143], a[40:43], v[32:35]\n\t"
      "s_add_u32 s50, s50, 12288\n\t"
      "v_mfma_f32_16x16x32_bf16 v[32:35], a[148:151], a[44:47], v[32:35]\n\t"
      "v_cvt_pk_bf16_f32 v46, v28, v29\n\t"
      "v_cvt_pk_bf16_f32 v47, v30, v31\n\t"
      "v_mfma_f32_16x16x32_bf16 v[36:39], a[112:115], a[24:27], v[36:39]\n\t"
      "ds_write_b64 v0, v[46:47] offset:96\n\t"
      "v_mfma_f32_16x16x32_bf16 v[36:39], a[120:123], a[28:31], v[36:39]\n\t"
      "s_cmp_eq_u32 s50, 160256\n\t"
      "s_cselect_b32 s50, 25088, s50\n\t"
      "v_mfma_f32_16x16x32_bf16 v[36:39], a[128:131], a[32:35], v[36:39]\n\t"
      "v_mfma_f32_16x16x32_bf16 v[36:39], a[136:139], a[36:39], v[36:39]\n\t"
      "v_mfma_f32_16x16x32_bf16 v[36:39], a[144:147], a[40:43], v[36:39]\n\t"
      "v_cvt_pk_bf16_f32 v48, v32, v33\n\t"
      "v_cvt_pk_bf16_f32 v49, v34, v35\n\t"
      "v_mfma_f32_16x16x32_bf16 v[36:39], a[152:155], a[44:47], v[36:39]\n\t"
      "ds_write_b64 v0, v[48:49] offset:128\n\t"
      ".Lmid2_%=:\n\t"
      "v_mfma_f32_16x16x32_bf16 v[16:19], v[64:67], a[0:3], 0\n\t"
      "ds_read_b128 a[24:27], v54 offset:256\n\t"
      "v_mfma_f32_16x16x32_bf16 v[16:19], v[80:83], a[4:7], v[16:19]\n\t"
      "ds_read_b128 a[28:31], v55 offset:256\n\t"
      "v_mfma_f32_16x16x32_bf16 v[16:19], v[96:99], a[8:11], v[16:19]\n\t"
      "ds_read_b128 a[32:35], v52 offset:512\n\t"
      "v_mfma_f32_16x16x32_bf16 v[16:19], v[112:115], a[12:15], v[16:19]\n\t"
      "ds_read_b128 a[36:39], v53 offset:512\n\t"
      "v_mfma_f32_16x16x32_bf16 v[16:19], v[128:131], a[16:19], v[16:19]\n\t"
      "ds_read_b128 a[40:43], v54 offset:512\n\t"
      "v_mfma_f32_16x16x32_bf16 v[16:19], v[144:147], a[20:23], v[16:19]\n\t"
      "ds_read_b128 a[44:47], v55 offset:512\n\t"
      "v_mfma_f32_16x16x32_bf16 v[20:23], v[68:71], a[0:3], 0\n\t"
      "v_cvt_pk_bf16_f32 v50, v36, v37\n\t"
      "v_cvt_pk_bf16_f32 v51, v38, v39\n\t"
      "v_mfma_f32_16x16x32_bf16 v[20:23], v[84:87], a[4:7], v[20:23]\n\t"
      "ds_write_b64 v0, v[50:51] offset:160\n\t"
      "v_mfma_f32_16x16x32_bf16 v[20:23], v[100:103], a[8:11], v[20:23]\n\t"
      "v_add_u32 v56, s51, v15\n\t"
      "v_xor_b32 v57, 64, v56\n\t"
      "v_mfma_f32_16x16x32_bf16 v[20:23], v[116:119], a[12:15], v[20:23]\n\t"
      "v_xor_b32 v58, 128, v56\n\t"
      "v_xor_b32 v59, 192, v56\n\t"
      "v_mfma_f32_16x16x32_bf16 v[20:23], v[132:135], a[16:19], v[20:23]\n\t"
      "v_mfma_f32_16x16x32_bf16 v[20:23], v[148:151], a[20:23], v[20:23]\n\t"
      "v_mfma_f32_16x16x32_bf16 v[24:27], v[72:75], a[0:3], 0\n\t"
      "v_mfma_f32_16x16x32_bf16 v[24:27], v[88:91], a[4:7], v[24:27]\n\t"
      "v_mfma_f32_16x16x32_bf16 v[24:27], v[104:107], a[8:11], v[24:27]\n\t"
      "v_mfma_f32_16x16x32_bf16 v[24:27], v[120:123], a[12:15], v[24:27]\n\t"
      "v_mfma_f32_16x16x32_bf16 v[24:27], v[136:139], a[16:19], v[24:27]\n\t"
      "s_waitcnt vmcnt(12)\n\t"
      "s_waitcnt lgkmcnt(0)\n\t"
      "s_barrier\n\t"
      "v_mfma_f32_16x16x32_bf16 v[24:27], v[152:155], a[20:23], v[24:27]\n\t"
      "v_mfma_f32_16x16x32_bf16 v[28:31], v[76:79], a[0:3], 0\n\t"
      "ds_read_b128 a[48:51], v8 offset:0\n\t"
      "v_mfma_f32_16x16x32_bf16 v[28:31], v[92:95], a[4:7], v[28:31]\n\t"
      "ds_read_b128 a[52:55], v60 offset:1024\n\t"
      "v_mfma_f32_16x16x32_bf16 v[28:31], v[108:111], a[8:11], v[28:31]\n\t"
      "ds_read_b128 a[56:59], v61 offset:2048\n\t"
      "v_mfma_f32_16x16x32_bf16 v[28:31], v[124:127], a[12:15], v[28:31]\n\t"
      "v_mfma_f32_16x16x32_bf16 v[28:31], v[140:143], a[16:19], v[28:31]\n\t"
      "v_mfma_f32_16x16x32_bf16 v[28:31], v[156:159], a[20:23], v[28:31]\n\t"
      "v_mfma_f32_16x16x32_bf16 v[32:35], a[60:63], a[0:3], 0\n\t"
      "v_mfma_f32_16x16x32_bf16 v[32:35], a[68:71], a[4:7], v[32:35]\n\t"
      "s_waitcnt lgkmcnt(2)\n\t"
      "v_mfma_f32_16x16x32_bf16 v[32:35], a[76:79], a[8:11], v[32:35]\n\t"
      "buffer_store_dwordx4 a[48:51], v9, s[44:47], s49 offen sc1\n\t"
      "v_mfma_f32_16x16x32_bf16 v[32:35], a[84:87], a[12:15], v[32:35]\n\t"
      "v_mfma_f32_16x16x32_bf16 v[32:35], a[92:95], a[16:19], v[32:35]\n\t"
      "v_mfma_f32_16x16x32_bf16 v[32:35], a[100:103], a[20:23], v[32:35]\n\t"
      "s_add_u32 m0, s18, s50\n\t"
      "v_mfma_f32_16x16x32_bf16 v[36:39], a[64:67], a[0:3], 0\n\t"
      "buffer_load_dwordx4 v5, s[40:43], s48 offen nt lds\n\t"
      "v_mfma_f32_16x16x32_bf16 v[36:39], a[72:75], a[4:7], v[36:39]\n\t"
      "v_mfma_f32_16x16x32_bf16 v[36:39], a[80:83], a[8:11], v[36:39]\n\t"
      "v_mfma_f32_16x16x32_bf16 v[36:39], a[88:91], a[12:15], v[36:39]\n\t"
      "s_waitcnt lgkmcnt(1)\n\t"
      "v_mfma_f32_16x16x32_bf16 v[36:39], a[96:99], a[16:19], v[36:39]\n\t"
      "buffer_store_dwordx4 a[52:55], v10, s[44:47], s49 offen offset:1024 sc1\n\t"
      "v_mfma_f32_16x16x32_bf16 v[36:39], a[104:107], a[20:23], v[36:39]\n\t"
      "v_mfma_f32_16x16x32_bf16 v[16:19], v[160:163], a[24:27], v[16:19]\n\t"
      "ds_read_b128 a[0:3], v56 offset:0\n\t"
      "v_mfma_f32_16x16x32_bf16 v[16:19], v[176:179], a[28:31], v[16:19]\n\t"
      "ds_read_b128 a[4:7], v57 offset:0\n\t"
      "v_mfma_f32_16x16x32_bf16 v[16:19], v[192:195], a[32:35], v[16:19]\n\t"
      "ds_read_b128 a[8:11], v58 offset:0\n\t"
      "v_mfma_f32_16x16x32_bf16 v[16:19], v[208:211], a[36:39], v[16:19]\n\t"
      "ds_read_b128 a[12:15], v59 offset:0\n\t"
      "v_mfma_f32_16x16x32_bf16 v[16:19], v[224:227], a[40:43], v[16:19]\n\t"
      "ds_read_b128 a[16:19], v56 offset:256\n\t"
      "v_mfma_f32_16x16x32_bf16 v[16:19], v[240:243], a[44:47], v[16:19]\n\t"
      "ds_read_b128 a[20:23], v57 offset:256\n\t"
      "v_mfma_f32_16x16x32_bf16 v[20:23], v[164:167], a[24:27], v[20:23]\n\t"
      "s_add_u32 s51, s51, 12288\n\t"
      "s_cmp_eq_u32 s51, 160256\n\t"
      "s_cselect_b32 s51, 25088, s51\n\t"
      "v_mfma_f32_16x16x32_bf16 v[20:23], v[180:183], a[28:31], v[20:23]\n\t"
      "s_waitcnt lgkmcnt(6)\n\t"
      "v_mfma_f32_16x16x32_bf16 v[20:23], v[196:199], a[32:35], v[20:23]\n\t"
      "buffer_store_dwordx4 a[56:59], v11, s[44:47], s49 offen offset:2048 sc1\n\t"
      "v_mfma_f32_16x16x32_bf16 v[20:23], v[212:215], a[36:39], v[20:23]\n\t"
      "v_mfma_f32_16x16x32_bf16 v[20:23], v[228:231], a[40:43], v[20:23]\n\t"
      "s_waitcnt lgkmcnt(0)\n\t"
      "v_mfma_f32_16x16x32_bf16 v[20:23], v[244:247], a[44:47], v[20:23]\n\t"
      "v_mfma_f32_16x16x32_bf16 v[24:27], v[168:171], a[24:27], v[24:27]\n\t"
      "v_mfma_f32_16x16x32_bf16 v[24:27], v[184:187], a[28:31], v[24:27]\n\t"
      "v_cvt_pk_bf16_f32 v40, v16, v17\n\t"
      "v_cvt_pk_bf16_f32 v41, v18, v19\n\t"
      "v_mfma_f32_16x16x32_bf16 v[24:27], v[200:203], a[32:35], v[24:27]\n\t"
      "ds_write_b64 v0, v[40:41] offset:12544\n\t"
      "v_mfma_f32_16x16x32_bf16 v[24:27], v[216:219], a[36:39], v[24:27]\n\t"
      "buffer_load_dwordx4 v6, s[40:43], s48 offen offset:1024 nt lds\n\t"
      "v_mfma_f32_16x16x32_bf16 v[24:27], v[232:235], a[40:43], v[24:27]\n\t"
      "v_mfma_f32_16x16x32_bf16 v[24:27], v[248:251], a[44:47], v[24:27]\n\t"
      "v_mfma_f32_16x16x32_bf16 v[28:31], v[172:175], a[24:27], v[28:31]\n\t"
      "v_mfma_f32_16x16x32_bf16 v[28:31], v[188:191], a[28:31], v[28:31]\n\t"
      "v_cvt_pk_bf16_f32 v42, v20, v21\n\t"
      "v_cvt_pk_bf16_f32 v43, v22, v23\n\t"
      "v_mfma_f32_16x16x32_bf16 v[28:31], v[204:207], a[32:35], v[28:31]\n\t"
      "ds_write_b64 v0, v[42:43] offset:12576\n\t"
      "v_mfma_f32_16x16x32_bf16 v[28:31], v[220:223], a[36:39], v[28:31]\n\t"
      "v_mfma_f32_16x16x32_bf16 v[28:31], v[236:239], a[40:43], v[28:31]\n\t"
      "v_mfma_f32_16x16x32_bf16 v[28:31], v[252:255], a[44:47], v[28:31]\n\t"
      "v_mfma_f32_16x16x32_bf16 v[32:35], a[108:111], a[24:27], v[32:35]\n\t"
      "v_mfma_f32_16x16x32_bf16 v[32:35], a[116:119], a[28:31], v[32:35]\n\t"
      "v_cvt_pk_bf16_f32 v44, v24, v25\n\t"
      "v_cvt_pk_bf16_f32 v45, v26, v27\n\t"
      "v_mfma_f32_16x16x32_bf16 v[32:35], a[124:127], a[32:35], v[32:35]\n\t"
      "ds_write_b64 v0, v[44:45] offset:12608\n\t"
      "v_mfma_f32_16x16x32_bf16 v[32:35], a[132:135], a[36:39], v[32:35]\n\t"
      "buffer_load_dwordx4 v7, s[40:43], s48 offen offset:2048 nt lds\n\t"
      "v_mfma_f32_16x16x32_bf16 v[32:35], a[140:143], a[40:43], v[32:35]\n\t"
      "s_add_u32 s50, s50, 12288\n\t"
      "v_mfma_f32_16x16x32_bf16 v[32:35], a[148:151], a[44:47], v[32:35]\n\t"
      "v_cvt_pk_bf16_f32 v46, v28, v29\n\t"
      "v_cvt_pk_bf16_f32 v47, v30, v31\n\t"
      "v_mfma_f32_16x16x32_bf16 v[36:39], a[112:115], a[24:27], v[36:39]\n\t"
      "ds_write_b64 v0, v[46:47] offset:12640\n\t"
      "v_mfma_f32_16x16x32_bf16 v[36:39], a[120:123], a[28:31], v[36:39]\n\t"
      "s_cmp_eq_u32 s50, 160256\n\t"
      "s_cselect_b32 s50, 25088, s50\n\t"
      "v_mfma_f32_16x16x32_bf16 v[36:39], a[128:131], a[32:35], v[36:39]\n\t"
      "s_sub_u32 s52, s52, 1\n\t"
      "s_cmp_lg_u32 s52, 0\n\t"
      "v_mfma_f32_16x16x32_bf16 v[36:39], a[136:139], a[36:39], v[36:39]\n\t"
      "v_mfma_f32_16x16x32_bf16 v[36:39], a[144:147], a[40:43], v[36:39]\n\t"
      "v_cvt_pk_bf16_f32 v48, v32, v33\n\t"
      "v_cvt_pk_bf16_f32 v49, v34, v35\n\t"
      "v_mfma_f32_16x16x32_bf16 v[36:39], a[152:155], a[44:47], v[36:39]\n\t"
      "ds_write_b64 v0, v[48:49] offset:12672\n\t"
      "s_cbranch_scc1 .Lloop2_%=\n\t"
      "s_branch .Lendc_%=\n\t"
      ".Lloop3_%=:\n\t"
      "v_mfma_f32_16x16x32_bf16 v[16:19], v[64:67], a[0:3], 0\n\t"
      "ds_read_b128 a[24:27], v58 offset:256\n\t"
      "v_mfma_f32_16x16x32_bf16 v[16:19], v[80:83], a[4:7], v[16:19]\n\t"
      "ds_read_b128 a[28:31], v59 offset:256\n\t"
      "v_mfma_f32_16x16x32_bf16 v[16:19], v[96:99], a[8:11], v[16:19]\n\t"
      "ds_read_b128 a[32:35], v56 offset:512\n\t"
      "v_mfma_f32_16x16x32_bf16 v[16:19], v[112:115], a[12:15], v[16:19]\n\t"
      "ds_read_b128 a[36:39], v57 offset:512\n\t"
      "v_mfma_f32_16x16x32_bf16 v[16:19], v[128:131], a[16:19], v[16:19]\n\t"
      "ds_read_b128 a[40:43], v58 offset:512\n\t"
      "v_mfma_f32_16x16x32_bf16 v[16:19], v[144:147], a[20:23], v[16:19]\n\t"
      "ds_read_b128 a[44:47], v59 offset:512\n\t"
      "v_mfma_f32_16x16x32_bf16 v[20:23], v[68:71], a[0:3], 0\n\t"
      "v_cvt_pk_bf16_f32 v50, v36, v37\n\t"
      "v_cvt_pk_bf16_f32 v51, v38, v39\n\t"
      "v_mfma_f32_16x16x32_bf16 v[20:23], v[84:87], a[4:7], v[20:23]\n\t"
      "ds_write_b64 v0, v[50:51] offset:12704\n\t"
      "v_mfma_f32_16x16x32_bf16 v[20:23], v[100:103], a[8:11], v[20:23]\n\t"
      "v_add_u32 v52, s51, v15\n\t"
      "v_xor_b32 v53, 64, v52\n\t"
      "v_mfma_f32_16x16x32_bf16 v[20:23], v[116:119], a[12:15], v[20:23]\n\t"
      "v_xor_b32 v54, 128, v52\n\t"
      "v_xor_b32 v55, 192, v52\n\t"
      "v_mfma_f32_16x16x32_bf16 v[20:23], v[132:135], a[16:19], v[20:23]\n\t"
      "s_add_u32 s56, s56, 786432\n\t"
      "s_lshr_b32 s57, s57, 1\n\t"
      "v_mfma_f32_16x16x32_bf16 v[20:23], v[148:151], a[20:23], v[20:23]\n\t"
      "s_bitcmp1_b32 s57, 0\n\t"
      "s_cselect_b32 s48, 24576, 0\n\t"
      "s_add_u32 s48, s48, s56\n\t"
      "v_mfma_f32_16x16x32_bf16 v[24:27], v[72:75], a[0:3], 0\n\t"
      "v_mfma_f32_16x16x32_bf16 v[24:27], v[88:91], a[4:7], v[24:27]\n\t"
      "v_mfma_f32_16x16x32_bf16 v[24:27], v[104:107], a[8:11], v[24:27]\n\t"
      "v_mfma_f32_16x16x32_bf16 v[24:27], v[120:123], a[12:15], v[24:27]\n\t"
      "v_mfma_f32_16x16x32_bf16 v[24:27], v[136:139], a[16:19], v[24:27]\n\t"
      "v_mfma_f32_16x16x32_bf16 v[24:27], v[152:155], a[20:23], v[24:27]\n\t"
      "s_waitcnt vmcnt(12)\n\t"
      "s_waitcnt lgkmcnt(0)\n\t"
      "s_barrier\n\t"
      "v_mfma_f32_16x16x32_bf16 v[28:31], v[76:79], a[0:3], 0\n\t"
      "ds_read_b128 a[48:51], v8 offset:12544\n\t"
      "v_mfma_f32_16x16x32_bf16 v[28:31], v[92:95], a[4:7], v[28:31]\n\t"
      "ds_read_b128 a[52:55], v60 offset:13568\n\t"
      "v_mfma_f32_16x16x32_bf16 v[28:31], v[108:111], a[8:11], v[28:31]\n\t"
      "ds_read_b128 a[56:59], v61 offset:14592\n\t"
      "v_mfma_f32_16x16x32_bf16 v[28:31], v[124:127], a[12:15], v[28:31]\n\t"
      "v_mfma_f32_16x16x32_bf16 v[28:31], v[140:143], a[16:19], v[28:31]\n\t"
      "v_mfma_f32_16x16x32_bf16 v[28:31], v[156:159], a[20:23], v[28:31]\n\t"
      "v_mfma_f32_16x16x32_bf16 v[32:35], a[60:63], a[0:3], 0\n\t"
      "v_mfma_f32_16x16x32_bf16 v[32:35], a[68:71], a[4:7], v[32:35]\n\t"
      "s_waitcnt lgkmcnt(2)\n\t"
      "v_mfma_f32_16x16x32_bf16 v[32:35], a[76:79], a[8:11], v[32:35]\n\t"
      "buffer_store_dwordx4 a[48:51], v12, s[44:47], s49 offen sc1\n\t"
      "v_mfma_f32_16x16x32_bf16 v[32:35], a[84:87], a[12:15], v[32:35]\n\t"
      "v_mfma_f32_16x16x32_bf16 v[32:35], a[92:95], a[16:19], v[32:35]\n\t"
      "v_mfma_f32_16x16x32_bf16 v[32:35], a[100:103], a[20:23], v[32:35]\n\t"
      "s_add_u32 m0, s18, s50\n\t"
      "v_mfma_f32_16x16x32_bf16 v[36:39], a[64:67], a[0:3], 0\n\t"
      "buffer_load_dwordx4 v2, s[40:43], s48 offen nt lds\n\t"
      "v_mfma_f32_16x16x32_bf16 v[36:39], a[72:75], a[4:7], v[36:39]\n\t"
      "v_mfma_f32_16x16x32_bf16 v[36:39], a[80:83], a[8:11], v[36:39]\n\t"
      "v_mfma_f32_16x16x32_bf16 v[36:39], a[88:91], a[12:15], v[36:39]\n\t"
      "s_waitcnt lgkmcnt(1)\n\t"
      "v_mfma_f32_16x16x32_bf16 v[36:39], a[96:99], a[16:19], v[36:39]\n\t"
      "buffer_store_dwordx4 a[52:55], v13, s[44:47], s49 offen offset:1024 sc1\n\t"
      "v_mfma_f32_16x16x32_bf16 v[36:39], a[104:107], a[20:23], v[36:39]\n\t"
      "v_mfma_f32_16x16x32_bf16 v[16:19], v[160:163], a[24:27], v[16:19]\n\t"
      "ds_read_b128 a[0:3], v52 offset:0\n\t"
      "v_mfma_f32_16x16x32_bf16 v[16:19], v[176:179], a[28:31], v[16:19]\n\t"
      "ds_read_b128 a[4:7], v53 offset:0\n\t"
      "v_mfma_f32_16x16x32_bf16 v[16:19], v[192:195], a[32:35], v[16:19]\n\t"
      "ds_read_b128 a[8:11], v54 offset:0\n\t"
      "v_mfma_f32_16x16x32_bf16 v[16:19], v[208:211], a[36:39], v[16:19]\n\t"
      "ds_read_b128 a[12:15], v55 offset:0\n\t"
      "v_mfma_f32_16x16x32_bf16 v[16:19], v[224:227], a[40:43], v[16:19]\n\t"
      "ds_read_b128 a[16:19], v52 offset:256\n\t"
      "v_mfma_f32_16x16x32_bf16 v[16:19], v[240:243], a[44:47], v[16:19]\n\t"
      "ds_read_b128 a[20:23], v53 offset:256\n\t"
      "v_mfma_f32_16x16x32_bf16 v[20:23], v[164:167], a[24:27], v[20:23]\n\t"
      "s_add_u32 s51, s51, 12288\n\t"
      "s_cmp_eq_u32 s51, 160256\n\t"
      "s_cselect_b32 s51, 25088, s51\n\t"
      "v_mfma_f32_16x16x32_bf16 v[20:23], v[180:183], a[28:31], v[20:23]\n\t"
      "s_waitcnt lgkmcnt(6)\n\t"
      "v_mfma_f32_16x16x32_bf16 v[20:23], v[196:199], a[32:35], v[20:23]\n\t"
      "buffer_store_dwordx4 a[56:59], v14, s[44:47], s49 offen offset:2048 sc1\n\t"
      "v_mfma_f32_16x16x32_bf16 v[20:23], v[212:215], a[36:39], v[20:23]\n\t"
      "v_mfma_f32_16x16x32_bf16 v[20:23], v[228:231], a[40:43], v[20:23]\n\t"
      "s_waitcnt lgkmcnt(0)\n\t"
      "v_mfma_f32_16x16x32_bf16 v[20:23], v[244:247], a[44:47], v[20:23]\n\t"
      "v_mfma_f32_16x16x32_bf16 v[24:27], v[168:171], a[24:27], v[24:27]\n\t"
      "v_mfma_f32_16x16x32_bf16 v[24:27], v[184:187], a[28:31], v[24:27]\n\t"
      "v_cvt_pk_bf16_f32 v40, v16, v17\n\t"
      "v_cvt_pk_bf16_f32 v41, v18, v19\n\t"
      "v_mfma_f32_16x16x32_bf16 v[24:27], v[200:203], a[32:35], v[24:27]\n\t"
      "ds_write_b64 v0, v[40:41] offset:0\n\t"
      "v_mfma_f32_16x16x32_bf16 v[24:27], v[216:219], a[36:39], v[24:27]\n\t"
      "buffer_load_dwordx4 v3, s[40:43], s48 offen offset:1024 nt lds\n\t"
      "v_mfma_f32_16x16x32_bf16 v[24:27], v[232:235], a[40:43], v[24:27]\n\t"
      "s_add_u32 s58, s58, 786432\n\t"
      "s_lshr_b32 s59, s59, 1\n\t"
      "v_mfma_f32_16x16x32_bf16 v[24:27], v[248:251], a[44:47], v[24:27]\n\t"
      "s_bitcmp1_b32 s59, 0\n\t"
      "s_cselect_b32 s49, 24576, 0\n\t"
      "s_add_u32 s49, s49, s58\n\t"
      "v_mfma_f32_16x16x32_bf16 v[28:31], v[172:175], a[24:27], v[28:31]\n\t"
      "v_mfma_f32_16x16x32_bf16 v[28:31], v[188:191], a[28:31], v[28:31]\n\t"
      "v_cvt_pk_bf16_f32 v42, v20, v21\n\t"
      "v_cvt_pk_bf16_f32 v43, v22, v23\n\t"
      "v_mfma_f32_16x16x32_bf16 v[28:31], v[204:207], a[32:35], v[28:31]\n\t"
      "ds_write_b64 v0, v[42:43] offset:32\n\t"
      "v_mfma_f32_16x16x32_bf16 v[28:31], v[220:223], a[36:39], v[28:31]\n\t"
      "v_mfma_f32_16x16x32_bf16 v[28:31], v[236:239], a[40:43], v[28:31]\n\t"
      "v_mfma_f32_16x16x32_bf16 v[28:31], v[252:255], a[44:47], v[28:31]\n\t"
      "v_mfma_f32_16x16x32_bf16 v[32:35], a[108:111], a[24:27], v[32:35]\n\t"
      "v_mfma_f32_16x16x32_bf16 v[32:35], a[116:119], a[28:31], v[32:35]\n\t"
      "v_cvt_pk_bf16_f32 v44, v24, v25\n\t"
      "v_cvt_pk_bf16_f32 v45, v26, v27\n\t"
      "v_mfma_f32_16x16x32_bf16 v[32:35], a[124:127], a[32:35], v[32:35]\n\t"
      "ds_write_b64 v0, v[44:45] offset:64\n\t"
      "v_mfma_f32_16x16x32_bf16 v[32:35], a[132:135], a[36:39], v[32:35]\n\t"
      "buffer_load_dwordx4 v4, s[40:43], s48 offen offset:2048 nt lds\n\t"
      "v_mfma_f32_16x16x32_bf16 v[32:35], a[140:143], a[40:43], v[32:35]\n\t"
      "s_add_u32 s50, s50, 12288\n\t"
      "v_mfma_f32_16x16x32_bf16 v[32:35], a[148:151], a[44:47], v[32:35]\n\t"
      "v_cvt_pk_bf16_f32 v46, v28, v29\n\t"
      "v_cvt_pk_bf16_f32 v47, v30, v31\n\t"
      "v_mfma_f32_16x16x32_bf16 v[36:39], a[112:115], a[24:27], v[36:39]\n\t"
      "ds_write_b64 v0, v[46:47] offset:96\n\t"
      "v_mfma_f32_16x16x32_bf16 v[36:39], a[120:123], a[28:31], v[36:39]\n\t"
      "s_cmp_eq_u32 s50, 160256\n\t"
      "s_cselect_b32 s50, 25088, s50\n\t"
      "v_mfma_f32_16x16x32_bf16 v[36:39], a[128:131], a[32:35], v[36:39]\n\t"
      "v_mfma_f32_16x16x32_bf16 v[36:39], a[136:139], a[36:39], v[36:39]\n\t"
      "v_mfma_f32_16x16x32_bf16 v[36:39], a[144:147], a[40:43], v[36:39]\n\t"
      "v_cvt_pk_bf16_f32 v48, v32, v33\n\t"
      "v_cvt_pk_bf16_f32 v49, v34, v35\n\t"
      "v_mfma_f32_16x16x32_bf16 v[36:39], a[152:155], a[44:47], v[36:39]\n\t"
      "ds_write_b64 v0, v[48:49] offset:128\n\t"
      ".Lmid3_%=:\n\t"
      "v_mfma_f32_16x16x32_bf16 v[16:19], v[64:67], a[0:3], 0\n\t"
      "ds_read_b128 a[24:27], v54 offset:256\n\t"
      "v_mfma_f32_16x16x32_bf16 v[16:19], v[80:83], a[4:7], v[16:19]\n\t"
      "ds_read_b128 a[28:31], v55 offset:256\n\t"
      "v_mfma_f32_16x16x32_bf16 v[16:19], v[96:99], a[8:11], v[16:19]\n\t"
      "ds_read_b128 a[32:35], v52 offset:512\n\t"
      "v_mfma_f32_16x16x32_bf16 v[16:19], v[112:115], a[12:15], v[16:19]\n\t"
      "ds_read_b128 a[36:39], v53 offset:512\n\t"
      "v_mfma_f32_16x16x32_bf16 v[16:19], v[128:131], a[16:19], v[16:19]\n\t"
      "ds_read_b128 a[40:43], v54 offset:512\n\t"
      "v_mfma_f32_16x16x32_bf16 v[16:19], v[144:147], a[20:23], v[16:19]\n\t"
      "ds_read_b128 a[44:47], v55 offset:512\n\t"
      "v_mfma_f32_16x16x32_bf16 v[20:23], v[68:71], a[0:3], 0\n\t"
      "v_cvt_pk_bf16_f32 v50, v36, v37\n\t"
      "v_cvt_pk_bf16_f32 v51, v38, v39\n\t"
      "v_mfma_f32_16x16x32_bf16 v[20:23], v[84:87], a[4:7], v[20:23]\n\t"
      "ds_write_b64 v0, v[50:51] offset:160\n\t"
      "v_mfma_f32_16x16x32_bf16 v[20:23], v[100:103], a[8:11], v[20:23]\n\t"
      "v_add_u32 v56, s51, v15\n\t"
      "v_xor_b32 v57, 64, v56\n\t"
      "v_mfma_f32_16x16x32_bf16 v[20:23], v[116:119], a[12:15], v[20:23]\n\t"
      "v_xor_b32 v58, 128, v56\n\t"
      "v_xor_b32 v59, 192, v56\n\t"
      "v_mfma_f32_16x16x32_bf16 v[20:23], v[132:135], a[16:19], v[20:23]\n\t"
      "v_mfma_f32_16x16x32_bf16 v[20:23], v[148:151], a[20:23], v[20:23]\n\t"
      "v_mfma_f32_16x16x32_bf16 v[24:27], v[72:75], a[0:3], 0\n\t"
      "v_mfma_f32_16x16x32_bf16 v[24:27], v[88:91], a[4:7], v[24:27]\n\t"
      "v_mfma_f32_16x16x32_bf16 v[24:27], v[104:107], a[8:11], v[24:27]\n\t"
      "v_mfma_f32_16x16x32_bf16 v[24:27], v[120:123], a[12:15], v[24:27]\n\t"
      "v_mfma_f32_16x16x32_bf16 v[24:27], v[136:139], a[16:19], v[24:27]\n\t"
      "v_mfma_f32_16x16x32_bf16 v[24:27], v[152:155], a[20:23], v[24:27]\n\t"
      "s_waitcnt vmcnt(12)\n\t"
      "s_waitcnt lgkmcnt(0)\n\t"
      "s_barrier\n\t"
      "v_mfma_f32_16x16x32_bf16 v[28:31], v[76:79], a[0:3], 0\n\t"
      "ds_read_b128 a[48:51], v8 offset:0\n\t"
      "v_mfma_f32_16x16x32_bf16 v[28:31], v[92:95], a[4:7], v[28:31]\n\t"
      "ds_read_b128 a[52:55], v60 offset:1024\n\t"
      "v_mfma_f32_16x16x32_bf16 v[28:31], v[108:111], a[8:11], v[28:31]\n\t"
      "ds_read_b128 a[56:59], v61 offset:2048\n\t"
      "v_mfma_f32_16x16x32_bf16 v[28:31], v[124:127], a[12:15], v[28:31]\n\t"
      "v_mfma_f32_16x16x32_bf16 v[28:31], v[140:143], a[16:19], v[28:31]\n\t"
      "v_mfma_f32_16x16x32_bf16 v[28:31], v[156:159], a[20:23], v[28:31]\n\t"
      "v_mfma_f32_16x16x32_bf16 v[32:35], a[60:63], a[0:3], 0\n\t"
      "v_mfma_f32_16x16x32_bf16 v[32:35], a[68:71], a[4:7], v[32:35]\n\t"
      "s_waitcnt lgkmcnt(2)\n\t"
      "v_mfma_f32_16x16x32_bf16 v[32:35], a[76:79], a[8:11], v[32:35]\n\t"
      "buffer_store_dwordx4 a[48:51], v9, s[44:47], s49 offen sc1\n\t"
      "v_mfma_f32_16x16x32_bf16 v[32:35], a[84:87], a[12:15], v[32:35]\n\t"
      "v_mfma_f32_16x16x32_bf16 v[32:35], a[92:95], a[16:19], v[32:35]\n\t"
      "v_mfma_f32_16x16x32_bf16 v[32:35], a[100:103], a[20:23], v[32:35]\n\t"
      "s_add_u32 m0, s18, s50\n\t"
      "v_mfma_f32_16x16x32_bf16 v[36:39], a[64:67], a[0:3], 0\n\t"
      "buffer_load_dwordx4 v5, s[40:43], s48 offen nt lds\n\t"
      "v_mfma_f32_16x16x32_bf16 v[36:39], a[72:75], a[4:7], v[36:39]\n\t"
      "v_mfma_f32_16x16x32_bf16 v[36:39], a[80:83], a[8:11], v[36:39]\n\t"
      "v_mfma_f32_16x16x32_bf16 v[36:39], a[88:91], a[12:15], v[36:39]\n\t"
      "s_waitcnt lgkmcnt(1)\n\t"
      "v_mfma_f32_16x16x32_bf16 v[36:39], a[96:99], a[16:19], v[36:39]\n\t"
      "buffer_store_dwordx4 a[52:55], v10, s[44:47], s49 offen offset:1024 sc1\n\t"
      "v_mfma_f32_16x16x32_bf16 v[36:39], a[104:107], a[20:23], v[36:39]\n\t"
      "v_mfma_f32_16x16x32_bf16 v[16:19], v[160:163], a[24:27], v[16:19]\n\t"
      "ds_read_b128 a[0:3], v56 offset:0\n\t"
      "v_mfma_f32_16x16x32_bf16 v[16:19], v[176:179], a[28:31], v[16:19]\n\t"
      "ds_read_b128 a[4:7], v57 offset:0\n\t"
      "v_mfma_f32_16x16x32_bf16 v[16:19], v[192:195], a[32:35], v[16:19]\n\t"
      "ds_read_b128 a[8:11], v58 offset:0\n\t"
      "v_mfma_f32_16x16x32_bf16 v[16:19], v[208:211], a[36:39], v[16:19]\n\t"
      "ds_read_b128 a[12:15], v59 offset:0\n\t"
      "v_mfma_f32_16x16x32_bf16 v[16:19], v[224:227], a[40:43], v[16:19]\n\t"
      "ds_read_b128 a[16:19], v56 offset:256\n\t"
      "v_mfma_f32_16x16x32_bf16 v[16:19], v[240:243], a[44:47], v[16:19]\n\t"
      "ds_read_b128 a[20:23], v57 offset:256\n\t"
      "v_mfma_f32_16x16x32_bf16 v[20:23], v[164:167], a[24:27], v[20:23]\n\t"
      "s_add_u32 s51, s51, 12288\n\t"
      "s_cmp_eq_u32 s51, 160256\n\t"
      "s_cselect_b32 s51, 25088, s51\n\t"
      "v_mfma_f32_16x16x32_bf16 v[20:23], v[180:183], a[28:31], v[20:23]\n\t"
      "s_waitcnt lgkmcnt(6)\n\t"
      "v_mfma_f32_16x16x32_bf16 v[20:23], v[196:199], a[32:35], v[20:23]\n\t"
      "buffer_store_dwordx4 a[56:59], v11, s[44:47], s49 offen offset:2048 sc1\n\t"
      "v_mfma_f32_16x16x32_bf16 v[20:23], v[212:215], a[36:39], v[20:23]\n\t"
      "v_mfma_f32_16x16x32_bf16 v[20:23], v[228:231], a[40:43], v[20:23]\n\t"
      "s_waitcnt lgkmcnt(0)\n\t"
      "v_mfma_f32_16x16x32_bf16 v[20:23], v[244:247], a[44:47], v[20:23]\n\t"
      "v_mfma_f32_16x16x32_bf16 v[24:27], v[168:171], a[24:27], v[24:27]\n\t"
      "v_mfma_f32_16x16x32_bf16 v[24:27], v[184:187], a[28:31], v[24:27]\n\t"
      "v_cvt_pk_bf16_f32 v40, v16, v17\n\t"
      "v_cvt_pk_bf16_f32 v41, v18, v19\n\t"
      "v_mfma_f32_16x16x32_bf16 v[24:27], v[200:203], a[32:35], v[24:27]\n\t"
      "ds_write_b64 v0, v[40:41] offset:12544\n\t"
      "v_mfma_f32_16x16x32_bf16 v[24:27], v[216:219], a[36:39], v[24:27]\n\t"
      "buffer_load_dwordx4 v6, s[40:43], s48 offen offset:1024 nt lds\n\t"
      "v_mfma_f32_16x16x32_bf16 v[24:27], v[232:235], a[40:43], v[24:27]\n\t"
      "v_mfma_f32_16x16x32_bf16 v[24:27], v[248:251], a[44:47], v[24:27]\n\t"
      "v_mfma_f32_16x16x32_bf16 v[28:31], v[172:175], a[24:27], v[28:31]\n\t"
      "v_mfma_f32_16x16x32_bf16 v[28:31], v[188:191], a[28:31], v[28:31]\n\t"
      "v_cvt_pk_bf16_f32 v42, v20, v21\n\t"
      "v_cvt_pk_bf16_f32 v43, v22, v23\n\t"
      "v_mfma_f32_16x16x32_bf16 v[28:31], v[204:207], a[32:35], v[28:31]\n\t"
      "ds_write_b64 v0, v[42:43] offset:12576\n\t"
      "v_mfma_f32_16x16x32_bf16 v[28:31], v[220:223], a[36:39], v[28:31]\n\t"
      "v_mfma_f32_16x16x32_bf16 v[28:31], v[236:239], a[40:43], v[28:31]\n\t"
      "v_mfma_f32_16x16x32_bf16 v[28:31], v[252:255], a[44:47], v[28:31]\n\t"
      "v_mfma_f32_16x16x32_bf16 v[32:35], a[108:111], a[24:27], v[32:35]\n\t"
      "v_mfma_f32_16x16x32_bf16 v[32:35], a[116:119], a[28:31], v[32:35]\n\t"
      "v_cvt_pk_bf16_f32 v44, v24, v25\n\t"
      "v_cvt_pk_bf16_f32 v45, v26, v27\n\t"
      "v_mfma_f32_16x16x32_bf16 v[32:35], a[124:127], a[32:35], v[32:35]\n\t"
      "ds_write_b64 v0, v[44:45] offset:12608\n\t"
      "v_mfma_f32_16x16x32_bf16 v[32:35], a[132:135], a[36:39], v[32:35]\n\t"
      "buffer_load_dwordx4 v7, s[40:43], s48 offen offset:2048 nt lds\n\t"
      "v_mfma_f32_16x16x32_bf16 v[32:35], a[140:143], a[40:43], v[32:35]\n\t"
      "s_add_u32 s50, s50, 12288\n\t"
      "v_mfma_f32_16x16x32_bf16 v[32:35], a[148:151], a[44:47], v[32:35]\n\t"
      "v_cvt_pk_bf16_f32 v46, v28, v29\n\t"
      "v_cvt_pk_bf16_f32 v47, v30, v31\n\t"
      "v_mfma_f32_16x16x32_bf16 v[36:39], a[112:115], a[24:27], v[36:39]\n\t"
      "ds_write_b64 v0, v[46:47] offset:12640\n\t"
      "v_mfma_f32_16x16x32_bf16 v[36:39], a[120:123], a[28:31], v[36:39]\n\t"
      "s_cmp_eq_u32 s50, 160256\n\t"
      "s_cselect_b32 s50, 25088, s50\n\t"
      "v_mfma_f32_16x16x32_bf16 v[36:39], a[128:131], a[32:35], v[36:39]\n\t"
      "s_sub_u32 s52, s52, 1\n\t"
      "s_cmp_lg_u32 s52, 0\n\t"
      "v_mfma_f32_16x16x32_bf16 v[36:39], a[136:139], a[36:39], v[36:39]\n\t"
      "v_mfma_f32_16x16x32_bf16 v[36:39], a[144:147], a[40:43], v[36:39]\n\t"
      "v_cvt_pk_bf16_f32 v48, v32, v33\n\t"
      "v_cvt_pk_bf16_f32 v49, v34, v35\n\t"
      "v_mfma_f32_16x16x32_bf16 v[36:39], a[152:155], a[44:47], v[36:39]\n\t"
      "ds_write_b64 v0, v[48:49] offset:12672\n\t"
      "s_cbranch_scc1 .Lloop3_%=\n\t"
      ".Lendc_%=:\n\t"
      "s_cmp_eq_u32 s48, 113221632\n\t"
      "s_cbranch_scc1 .Lwrap3_%=\n\t"
      ".Lwrapped3_%=:\n\t"
      "v_mfma_f32_16x16x32_bf16 v[16:19], v[64:67], a[0:3], 0\n\t"
      "ds_read_b128 a[24:27], v58 offset:256\n\t"
      "v_mfma_f32_16x16x32_bf16 v[16:19], v[80:83], a[4:7], v[16:19]\n\t"
      "ds_read_b128 a[28:31], v59 offset:256\n\t"
      "v_mfma_f32_16x16x32_bf16 v[16:19], v[96:99], a[8:11], v[16:19]\n\t"
      "ds_read_b128 a[32:35], v56 offset:512\n\t"
      "v_mfma_f32_16x16x32_bf16 v[16:19], v[112:115], a[12:15], v[16:19]\n\t"
      "ds_read_b128 a[36:39], v57 offset:512\n\t"
      "v_mfma_f32_16x16x32_bf16 v[16:19], v[128:131], a[16:19], v[16:19]\n\t"
      "ds_read_b128 a[40:43], v58 offset:512\n\t"
      "v_mfma_f32_16x16x32_bf16 v[16:19], v[144:147], a[20:23], v[16:19]\n\t"
      "ds_read_b128 a[44:47], v59 offset:512\n\t"
      "v_mfma_f32_16x16x32_bf16 v[20:23], v[68:71], a[0:3], 0\n\t"
      "v_cvt_pk_bf16_f32 v50, v36, v37\n\t"
      "v_cvt_pk_bf16_f32 v51, v38, v39\n\t"
      "v_mfma_f32_16x16x32_bf16 v[20:23], v[84:87], a[4:7], v[20:23]\n\t"
      "ds_write_b64 v0, v[50:51] offset:12704\n\t"
      "v_mfma_f32_16x16x32_bf16 v[20:23], v[100:103], a[8:11], v[20:23]\n\t"
      "v_add_u32 v52, s51, v15\n\t"
      "v_xor_b32 v53, 64, v52\n\t"
      "v_mfma_f32_16x16x32_bf16 v[20:23], v[116:119], a[12:15], v[20:23]\n\t"
      "v_xor_b32 v54, 128, v52\n\t"
      "v_xor_b32 v55, 192, v52\n\t"
      "v_mfma_f32_16x16x32_bf16 v[20:23], v[132:135], a[16:19], v[20:23]\n\t"
      "s_add_u32 s58, s58, 786432\n\t"
      "s_lshr_b32 s59, s59, 1\n\t"
      "v_mfma_f32_16x16x32_bf16 v[20:23], v[148:151], a[20:23], v[20:23]\n\t"
      "s_bitcmp1_b32 s59, 0\n\t"
      "s_cselect_b32 s84, 24576, 0\n\t"
      "s_add_u32 s84, s84, s58\n\t"
      "v_mfma_f32_16x16x32_bf16 v[24:27], v[72:75], a[0:3], 0\n\t"
      "s_add_u32 s58, s58, 786432\n\t"
      "s_lshr_b32 s59, s59, 1\n\t"
      "v_mfma_f32_16x16x32_bf16 v[24:27], v[88:91], a[4:7], v[24:27]\n\t"
      "s_bitcmp1_b32 s59, 0\n\t"
      "s_cselect_b32 s85, 24576, 0\n\t"
      "s_add_u32 s85, s85, s58\n\t"
      "v_mfma_f32_16x16x32_bf16 v[24:27], v[104:107], a[8:11], v[24:27]\n\t"
      "s_waitcnt vmcnt(12)\n\t"
      "s_waitcnt lgkmcnt(0)\n\t"
      "s_barrier\n\t"
      "v_mfma_f32_16x16x32_bf16 v[24:27], v[120:123], a[12:15], v[24:27]\n\t"
      "v_mfma_f32_16x16x32_bf16 v[24:27], v[136:139], a[16:19], v[24:27]\n\t"
      "v_mfma_f32_16x16x32_bf16 v[24:27], v[152:155], a[20:23], v[24:27]\n\t"
      "v_mfma_f32_16x16x32_bf16 v[28:31], v[76:79], a[0:3], 0\n\t"
      "ds_read_b128 a[188:191], v8 offset:12544\n\t"
      "v_mfma_f32_16x16x32_bf16 v[28:31], v[92:95], a[4:7], v[28:31]\n\t"
      "ds_read_b128 a[192:195], v60 offset:13568\n\t"
      "v_mfma_f32_16x16x32_bf16 v[28:31], v[108:111], a[8:11], v[28:31]\n\t"
      "ds_read_b128 a[196:199], v61 offset:14592\n\t"
      "v_mfma_f32_16x16x32_bf16 v[28:31], v[124:127], a[12:15], v[28:31]\n\t"
      "v_mfma_f32_16x16x32_bf16 v[28:31], v[140:143], a[16:19], v[28:31]\n\t"
      "v_mfma_f32_16x16x32_bf16 v[28:31], v[156:159], a[20:23], v[28:31]\n\t"
      "v_mfma_f32_16x16x32_bf16 v[32:35], a[60:63], a[0:3], 0\n\t"
      "v_mfma_f32_16x16x32_bf16 v[32:35], a[68:71], a[4:7], v[32:35]\n\t"
      "v_mfma_f32_16x16x32_bf16 v[32:35], a[76:79], a[8:11], v[32:35]\n\t"
      "v_mfma_f32_16x16x32_bf16 v[32:35], a[84:87], a[12:15], v[32:35]\n\t"
      "v_mfma_f32_16x16x32_bf16 v[32:35], a[92:95], a[16:19], v[32:35]\n\t"
      "v_mfma_f32_16x16x32_bf16 v[32:35], a[100:103], a[20:23], v[32:35]\n\t"
      "v_mfma_f32_16x16x32_bf16 v[36:39], a[64:67], a[0:3], 0\n\t"
      "v_mfma_f32_16x16x32_bf16 v[36:39], a[72:75], a[4:7], v[36:39]\n\t"
      "v_mfma_f32_16x16x32_bf16 v[36:39], a[80:83], a[8:11], v[36:39]\n\t"
      "v_mfma_f32_16x16x32_bf16 v[36:39], a[88:91], a[12:15], v[36:39]\n\t"
      "v_mfma_f32_16x16x32_bf16 v[36:39], a[96:99], a[16:19], v[36:39]\n\t"
      "v_mfma_f32_16x16x32_bf16 v[36:39], a[104:107], a[20:23], v[36:39]\n\t"
      "v_mfma_f32_16x16x32_bf16 v[16:19], v[160:163], a[24:27], v[16:19]\n\t"
      "ds_read_b128 a[0:3], v52 offset:0\n\t"
      "v_mfma_f32_16x16x32_bf16 v[16:19], v[176:179], a[28:31], v[16:19]\n\t"
      "ds_read_b128 a[4:7], v53 offset:0\n\t"
      "v_mfma_f32_16x16x32_bf16 v[16:19], v[192:195], a[32:35], v[16:19]\n\t"
      "ds_read_b128 a[8:11], v54 offset:0\n\t"
      "v_mfma_f32_16x16x32_bf16 v[16:19], v[208:211], a[36:39], v[16:19]\n\t"
      "ds_read_b128 a[12:15], v55 offset:0\n\t"
      "v_mfma_f32_16x16x32_bf16 v[16:19], v[224:227], a[40:43], v[16:19]\n\t"
      "ds_read_b128 a[16:19], v52 offset:256\n\t"
      "v_mfma_f32_16x16x32_bf16 v[16:19], v[240:243], a[44:47], v[16:19]\n\t"
      "ds_read_b128 a[20:23], v53 offset:256\n\t"
      "v_mfma_f32_16x16x32_bf16 v[20:23], v[164:167], a[24:27], v[20:23]\n\t"
      "s_add_u32 s51, s51, 12288\n\t"
      "s_cmp_eq_u32 s51, 160256\n\t"
      "s_cselect_b32 s51, 25088, s51\n\t"
      "v_mfma_f32_16x16x32_bf16 v[20:23], v[180:183], a[28:31], v[20:23]\n\t"
      "v_mfma_f32_16x16x32_bf16 v[20:23], v[196:199], a[32:35], v[20:23]\n\t"
      "v_mfma_f32_16x16x32_bf16 v[20:23], v[212:215], a[36:39], v[20:23]\n\t"
      "v_mfma_f32_16x16x32_bf16 v[20:23], v[228:231], a[40:43], v[20:23]\n\t"
      "s_waitcnt lgkmcnt(0)\n\t"
      "v_mfma_f32_16x16x32_bf16 v[20:23], v[244:247], a[44:47], v[20:23]\n\t"
      "v_mfma_f32_16x16x32_bf16 v[24:27], v[168:171], a[24:27], v[24:27]\n\t"
      "v_mfma_f32_16x16x32_bf16 v[24:27], v[184:187], a[28:31], v[24:27]\n\t"
      "v_cvt_pk_bf16_f32 v40, v16, v17\n\t"
      "v_cvt_pk_bf16_f32 v41, v18, v19\n\t"
      "v_mfma_f32_16x16x32_bf16 v[24:27], v[200:203], a[32:35], v[24:27]\n\t"
      "ds_write_b64 v0, v[40:41] offset:0\n\t"
      "v_mfma_f32_16x16x32_bf16 v[24:27], v[216:219], a[36:39], v[24:27]\n\t"
      "v_mfma_f32_16x16x32_bf16 v[24:27], v[232:235], a[40:43], v[24:27]\n\t"
      "v_mfma_f32_16x16x32_bf16 v[24:27], v[248:251], a[44:47], v[24:27]\n\t"
      "v_mfma_f32_16x16x32_bf16 v[28:31], v[172:175], a[24:27], v[28:31]\n\t"
      "v_mfma_f32_16x16x32_bf16 v[28:31], v[188:191], a[28:31], v[28:31]\n\t"
      "v_cvt_pk_bf16_f32 v42, v20, v21\n\t"
      "v_cvt_pk_bf16_f32 v43, v22, v23\n\t"
      "v_mfma_f32_16x16x32_bf16 v[28:31], v[204:207], a[32:35], v[28:31]\n\t"
      "ds_write_b64 v0, v[42:43] offset:32\n\t"
      "v_mfma_f32_16x16x32_bf16 v[28:31], v[220:223], a[36:39], v[28:31]\n\t"
      "v_mfma_f32_16x16x32_bf16 v[28:31], v[236:239], a[40:43], v[28:31]\n\t"
      "v_mfma_f32_16x16x32_bf16 v[28:31], v[252:255], a[44:47], v[28:31]\n\t"
      "v_mfma_f32_16x16x32_bf16 v[32:35], a[108:111], a[24:27], v[32:35]\n\t"
      "v_mfma_f32_16x16x32_bf16 v[32:35], a[116:119], a[28:31], v[32:35]\n\t"
      "v_cvt_pk_bf16_f32 v44, v24, v25\n\t"
      "v_cvt_pk_bf16_f32 v45, v26, v27\n\t"
      "v_mfma_f32_16x16x32_bf16 v[32:35], a[124:127], a[32:35], v[32:35]\n\t"
      "ds_write_b64 v0, v[44:45] offset:64\n\t"
      "v_mfma_f32_16x16x32_bf16 v[32:35], a[132:135], a[36:39], v[32:35]\n\t"
      "v_mfma_f32_16x16x32_bf16 v[32:35], a[140:143], a[40:43], v[32:35]\n\t"
      "v_mfma_f32_16x16x32_bf16 v[32:35], a[148:151], a[44:47], v[32:35]\n\t"
      "v_cvt_pk_bf16_f32 v46, v28, v29\n\t"
      "v_cvt_pk_bf16_f32 v47, v30, v31\n\t"
      "v_mfma_f32_16x16x32_bf16 v[36:39], a[112:115], a[24:27], v[36:39]\n\t"
      "ds_write_b64 v0, v[46:47] offset:96\n\t"
      "v_mfma_f32_16x16x32_bf16 v[36:39], a[120:123], a[28:31], v[36:39]\n\t"
      "v_mfma_f32_16x16x32_bf16 v[36:39], a[128:131], a[32:35], v[36:39]\n\t"
      "v_mfma_f32_16x16x32_bf16 v[36:39], a[136:139], a[36:39], v[36:39]\n\t"
      "v_mfma_f32_16x16x32_bf16 v[36:39], a[144:147], a[40:43], v[36:39]\n\t"
      "v_cvt_pk_bf16_f32 v48, v32, v33\n\t"
      "v_cvt_pk_bf16_f32 v49, v34, v35\n\t"
      "v_mfma_f32_16x16x32_bf16 v[36:39], a[152:155], a[44:47], v[36:39]\n\t"
      "ds_write_b64 v0, v[48:49] offset:128\n\t"
      "v_mfma_f32_16x16x32_bf16 v[16:19], v[64:67], a[0:3], 0\n\t"
      "ds_read_b128 a[24:27], v54 offset:256\n\t"
      "v_mfma_f32_16x16x32_bf16 v[16:19], v[80:83], a[4:7], v[16:19]\n\t"
      "ds_read_b128 a[28:31], v55 offset:256\n\t"
      "v_mfma_f32_16x16x32_bf16 v[16:19], v[96:99], a[8:11], v[16:19]\n\t"
      "ds_read_b128 a[32:35], v52 offset:512\n\t"
      "v_mfma_f32_16x16x32_bf16 v[16:19], v[112:115], a[12:15], v[16:19]\n\t"
      "ds_read_b128 a[36:39], v53 offset:512\n\t"
      "v_mfma_f32_16x16x32_bf16 v[16:19], v[128:131], a[16:19], v[16:19]\n\t"
      "ds_read_b128 a[40:43], v54 offset:512\n\t"
      "v_mfma_f32_16x16x32_bf16 v[16:19], v[144:147], a[20:23], v[16:19]\n\t"
      "ds_read_b128 a[44:47], v55 offset:512\n\t"
      "v_mfma_f32_16x16x32_bf16 v[20:23], v[68:71], a[0:3], 0\n\t"
      "v_cvt_pk_bf16_f32 v50, v36, v37\n\t"
      "v_cvt_pk_bf16_f32 v51, v38, v39\n\t"
      "v_mfma_f32_16x16x32_bf16 v[20:23], v[84:87], a[4:7], v[20:23]\n\t"
      "ds_write_b64 v0, v[50:51] offset:160\n\t"
      "v_mfma_f32_16x16x32_bf16 v[20:23], v[100:103], a[8:11], v[20:23]\n\t"
      "v_add_u32 v56, s51, v15\n\t"
      "v_xor_b32 v57, 64, v56\n\t"
      "v_mfma_f32_16x16x32_bf16 v[20:23], v[116:119], a[12:15], v[20:23]\n\t"
      "v_xor_b32 v58, 128, v56\n\t"
      "v_xor_b32 v59, 192, v56\n\t"
      "v_mfma_f32_16x16x32_bf16 v[20:23], v[132:135], a[16:19], v[20:23]\n\t"
      "buffer_store_dwordx4 a[188:191], v12, s[44:47], s49 offen sc1\n\t"
      "v_mfma_f32_16x16x32_bf16 v[20:23], v[148:151], a[20:23], v[20:23]\n\t"
      "v_mfma_f32_16x16x32_bf16 v[24:27], v[72:75], a[0:3], 0\n\t"
      "v_mfma_f32_16x16x32_bf16 v[24:27], v[88:91], a[4:7], v[24:27]\n\t"
      "v_mfma_f32_16x16x32_bf16 v[24:27], v[104:107], a[8:11], v[24:27]\n\t"
      "s_waitcnt vmcnt(7)\n\t"
      "s_waitcnt lgkmcnt(0)\n\t"
      "s_barrier\n\t"
      "v_mfma_f32_16x16x32_bf16 v[24:27], v[120:123], a[12:15], v[24:27]\n\t"
      "v_mfma_f32_16x16x32_bf16 v[24:27], v[136:139], a[16:19], v[24:27]\n\t"
      "v_mfma_f32_16x16x32_bf16 v[24:27], v[152:155], a[20:23], v[24:27]\n\t"
      "v_mfma_f32_16x16x32_bf16 v[28:31], v[76:79], a[0:3], 0\n\t"
      "ds_read_b128 a[200:203], v8 offset:0\n\t"
      "v_mfma_f32_16x16x32_bf16 v[28:31], v[92:95], a[4:7], v[28:31]\n\t"
      "ds_read_b128 a[204:207], v60 offset:1024\n\t"
      "v_mfma_f32_16x16x32_bf16 v[28:31], v[108:111], a[8:11], v[28:31]\n\t"
      "ds_read_b128 a[208:211], v61 offset:2048\n\t"
      "v_mfma_f32_16x16x32_bf16 v[28:31], v[124:127], a[12:15], v[28:31]\n\t"
      "v_mfma_f32_16x16x32_bf16 v[28:31], v[140:143], a[16:19], v[28:31]\n\t"
      "buffer_store_dwordx4 a[192:195], v13, s[44:47], s49 offen offset:1024 sc1\n\t"
      "v_mfma_f32_16x16x32_bf16 v[28:31], v[156:159], a[20:23], v[28:31]\n\t"
      "v_mfma_f32_16x16x32_bf16 v[32:35], a[60:63], a[0:3], 0\n\t"
      "v_mfma_f32_16x16x32_bf16 v[32:35], a[68:71], a[4:7], v[32:35]\n\t"
      "v_mfma_f32_16x16x32_bf16 v[32:35], a[76:79], a[8:11], v[32:35]\n\t"
      "v_mfma_f32_16x16x32_bf16 v[32:35], a[84:87], a[12:15], v[32:35]\n\t"
      "v_mfma_f32_16x16x32_bf16 v[32:35], a[92:95], a[16:19], v[32:35]\n\t"
      "v_mfma_f32_16x16x32_bf16 v[32:35], a[100:103], a[20:23], v[32:35]\n\t"
      "v_mfma_f32_16x16x32_bf16 v[36:39], a[64:67], a[0:3], 0\n\t"
      "buffer_store_dwordx4 a[196:199], v14, s[44:47], s49 offen offset:2048 sc1\n\t"
      "v_mfma_f32_16x16x32_bf16 v[36:39], a[72:75], a[4:7], v[36:39]\n\t"
      "v_mfma_f32_16x16x32_bf16 v[36:39], a[80:83], a[8:11], v[36:39]\n\t"
      "v_mfma_f32_16x16x32_bf16 v[36:39], a[88:91], a[12:15], v[36:39]\n\t"
      "v_mfma_f32_16x16x32_bf16 v[36:39], a[96:99], a[16:19], v[36:39]\n\t"
      "v_mfma_f32_16x16x32_bf16 v[36:39], a[104:107], a[20:23], v[36:39]\n\t"
      "v_mfma_f32_16x16x32_bf16 v[16:19], v[160:163], a[24:27], v[16:19]\n\t"
      "ds_read_b128 a[0:3], v56 offset:0\n\t"
      "v_mfma_f32_16x16x32_bf16 v[16:19], v[176:179], a[28:31], v[16:19]\n\t"
      "ds_read_b128 a[4:7], v57 offset:0\n\t"
      "v_mfma_f32_16x16x32_bf16 v[16:19], v[192:195], a[32:35], v[16:19]\n\t"
      "ds_read_b128 a[8:11], v58 offset:0\n\t"
      "v_mfma_f32_16x16x32_bf16 v[16:19], v[208:211], a[36:39], v[16:19]\n\t"
      "ds_read_b128 a[12:15], v59 offset:0\n\t"
      "v_mfma_f32_16x16x32_bf16 v[16:19], v[224:227], a[40:43], v[16:19]\n\t"
      "ds_read_b128 a[16:19], v56 offset:256\n\t"
      "v_mfma_f32_16x16x32_bf16 v[16:19], v[240:243], a[44:47], v[16:19]\n\t"
      "ds_read_b128 a[20:23], v57 offset:256\n\t"
      "v_mfma_f32_16x16x32_bf16 v[20:23], v[164:167], a[24:27], v[20:23]\n\t"
      "s_add_u32 s51, s51, 12288\n\t"
      "s_cmp_eq_u32 s51, 160256\n\t"
      "s_cselect_b32 s51, 25088, s51\n\t"
      "v_mfma_f32_16x16x32_bf16 v[20:23], v[180:183], a[28:31], v[20:23]\n\t"
      "s_waitcnt lgkmcnt(8)\n\t"
      "v_mfma_f32_16x16x32_bf16 v[20:23], v[196:199], a[32:35], v[20:23]\n\t"
      "buffer_store_dwordx4 a[200:203], v9, s[44:47], s84 offen sc1\n\t"
      "v_mfma_f32_16x16x32_bf16 v[20:23], v[212:215], a[36:39], v[20:23]\n\t"
      "v_mfma_f32_16x16x32_bf16 v[20:23], v[228:231], a[40:43], v[20:23]\n\t"
      "s_waitcnt lgkmcnt(0)\n\t"
      "v_mfma_f32_16x16x32_bf16 v[20:23], v[244:247], a[44:47], v[20:23]\n\t"
      "v_mfma_f32_16x16x32_bf16 v[24:27], v[168:171], a[24:27], v[24:27]\n\t"
      "v_mfma_f32_16x16x32_bf16 v[24:27], v[184:187], a[28:31], v[24:27]\n\t"
      "v_cvt_pk_bf16_f32 v40, v16, v17\n\t"
      "v_cvt_pk_bf16_f32 v41, v18, v19\n\t"
      "v_mfma_f32_16x16x32_bf16 v[24:27], v[200:203], a[32:35], v[24:27]\n\t"
      "ds_write_b64 v0, v[40:41] offset:12544\n\t"
      "v_mfma_f32_16x16x32_bf16 v[24:27], v[216:219], a[36:39], v[24:27]\n\t"
      "v_mfma_f32_16x16x32_bf16 v[24:27], v[232:235], a[40:43], v[24:27]\n\t"
      "buffer_store_dwordx4 a[204:207], v10, s[44:47], s84 offen offset:1024 sc1\n\t"
      "v_mfma_f32_16x16x32_bf16 v[24:27], v[248:251], a[44:47], v[24:27]\n\t"
      "v_mfma_f32_16x16x32_bf16 v[28:31], v[172:175], a[24:27], v[28:31]\n\t"
      "v_mfma_f32_16x16x32_bf16 v[28:31], v[188:191], a[28:31], v[28:31]\n\t"
      "v_cvt_pk_bf16_f32 v42, v20, v21\n\t"
      "v_cvt_pk_bf16_f32 v43, v22, v23\n\t"
      "v_mfma_f32_16x16x32_bf16 v[28:31], v[204:207], a[32:35], v[28:31]\n\t"
      "ds_write_b64 v0, v[42:43] offset:12576\n\t"
      "v_mfma_f32_16x16x32_bf16 v[28:31], v[220:223], a[36:39], v[28:31]\n\t"
      "v_mfma_f32_16x16x32_bf16 v[28:31], v[236:239], a[40:43], v[28:31]\n\t"
      "buffer_store_dwordx4 a[208:211], v11, s[44:47], s84 offen offset:2048 sc1\n\t"
      "v_mfma_f32_16x16x32_bf16 v[28:31], v[252:255], a[44:47], v[28:31]\n\t"
      "v_mfma_f32_16x16x32_bf16 v[32:35], a[108:111], a[24:27], v[32:35]\n\t"
      "v_mfma_f32_16x16x32_bf16 v[32:35], a[116:119], a[28:31], v[32:35]\n\t"
      "v_cvt_pk_bf16_f32 v44, v24, v25\n\t"
      "v_cvt_pk_bf16_f32 v45, v26, v27\n\t"
      "v_mfma_f32_16x16x32_bf16 v[32:35], a[124:127], a[32:35], v[32:35]\n\t"
      "ds_write_b64 v0, v[44:45] offset:12608\n\t"
      "v_mfma_f32_16x16x32_bf16 v[32:35], a[132:135], a[36:39], v[32:35]\n\t"
      "v_mfma_f32_16x16x32_bf16 v[32:35], a[140:143], a[40:43], v[32:35]\n\t"
      "v_mfma_f32_16x16x32_bf16 v[32:35], a[148:151], a[44:47], v[32:35]\n\t"
      "v_cvt_pk_bf16_f32 v46, v28, v29\n\t"
      "v_cvt_pk_bf16_f32 v47, v30, v31\n\t"
      "v_mfma_f32_16x16x32_bf16 v[36:39], a[112:115], a[24:27], v[36:39]\n\t"
      "ds_write_b64 v0, v[46:47] offset:12640\n\t"
      "v_mfma_f32_16x16x32_bf16 v[36:39], a[120:123], a[28:31], v[36:39]\n\t"
      "v_mfma_f32_16x16x32_bf16 v[36:39], a[128:131], a[32:35], v[36:39]\n\t"
      "v_mfma_f32_16x16x32_bf16 v[36:39], a[136:139], a[36:39], v[36:39]\n\t"
      "v_mfma_f32_16x16x32_bf16 v[36:39], a[144:147], a[40:43], v[36:39]\n\t"
      "v_cvt_pk_bf16_f32 v48, v32, v33\n\t"
      "v_cvt_pk_bf16_f32 v49, v34, v35\n\t"
      "v_mfma_f32_16x16x32_bf16 v[36:39], a[152:155], a[44:47], v[36:39]\n\t"
      "ds_write_b64 v0, v[48:49] offset:12672\n\t"
      "v_mfma_f32_16x16x32_bf16 v[16:19], v[64:67], a[0:3], 0\n\t"
      "ds_read_b128 a[24:27], v58 offset:256\n\t"
      "v_mfma_f32_16x16x32_bf16 v[16:19], v[80:83], a[4:7], v[16:19]\n\t"
      "ds_read_b128 a[28:31], v59 offset:256\n\t"
      "v_mfma_f32_16x16x32_bf16 v[16:19], v[96:99], a[8:11], v[16:19]\n\t"
      "ds_read_b128 a[32:35], v56 offset:512\n\t"
      "v_mfma_f32_16x16x32_bf16 v[16:19], v[112:115], a[12:15], v[16:19]\n\t"
      "ds_read_b128 a[36:39], v57 offset:512\n\t"
      "v_mfma_f32_16x16x32_bf16 v[16:19], v[128:131], a[16:19], v[16:19]\n\t"
      "ds_read_b128 a[40:43], v58 offset:512\n\t"
      "v_mfma_f32_16x16x32_bf16 v[16:19], v[144:147], a[20:23], v[16:19]\n\t"
      "ds_read_b128 a[44:47], v59 offset:512\n\t"
      "v_mfma_f32_16x16x32_bf16 v[20:23], v[68:71], a[0:3], 0\n\t"
      "v_cvt_pk_bf16_f32 v50, v36, v37\n\t"
      "v_cvt_pk_bf16_f32 v51, v38, v39\n\t"
      "v_mfma_f32_16x16x32_bf16 v[20:23], v[84:87], a[4:7], v[20:23]\n\t"
      "ds_write_b64 v0, v[50:51] offset:12704\n\t"
      "v_mfma_f32_16x16x32_bf16 v[20:23], v[100:103], a[8:11], v[20:23]\n\t"
      "v_add_u32 v52, s51, v15\n\t"
      "v_xor_b32 v53, 64, v52\n\t"
      "v_mfma_f32_16x16x32_bf16 v[20:23], v[116:119], a[12:15], v[20:23]\n\t"
      "v_xor_b32 v54, 128, v52\n\t"
      "v_xor_b32 v55, 192, v52\n\t"
      "v_mfma_f32_16x16x32_bf16 v[20:23], v[132:135], a[16:19], v[20:23]\n\t"
      "v_mfma_f32_16x16x32_bf16 v[20:23], v[148:151], a[20:23], v[20:23]\n\t"
      "v_mfma_f32_16x16x32_bf16 v[24:27], v[72:75], a[0:3], 0\n\t"
      "v_mfma_f32_16x16x32_bf16 v[24:27], v[88:91], a[4:7], v[24:27]\n\t"
      "v_mfma_f32_16x16x32_bf16 v[24:27], v[104:107], a[8:11], v[24:27]\n\t"
      "s_waitcnt vmcnt(6)\n\t"
      "s_waitcnt lgkmcnt(0)\n\t"
      "s_barrier\n\t"
      "v_mfma_f32_16x16x32_bf16 v[24:27], v[120:123], a[12:15], v[24:27]\n\t"
      "v_mfma_f32_16x16x32_bf16 v[24:27], v[136:139], a[16:19], v[24:27]\n\t"
      "v_mfma_f32_16x16x32_bf16 v[24:27], v[152:155], a[20:23], v[24:27]\n\t"
      "v_mfma_f32_16x16x32_bf16 v[28:31], v[76:79], a[0:3], 0\n\t"
      "ds_read_b128 a[212:215], v8 offset:12544\n\t"
      "v_mfma_f32_16x16x32_bf16 v[28:31], v[92:95], a[4:7], v[28:31]\n\t"
      "ds_read_b128 a[216:219], v60 offset:13568\n\t"
      "v_mfma_f32_16x16x32_bf16 v[28:31], v[108:111], a[8:11], v[28:31]\n\t"
      "ds_read_b128 a[220:223], v61 offset:14592\n\t"
      "v_mfma_f32_16x16x32_bf16 v[28:31], v[124:127], a[12:15], v[28:31]\n\t"
      "v_mfma_f32_16x16x32_bf16 v[28:31], v[140:143], a[16:19], v[28:31]\n\t"
      "v_mfma_f32_16x16x32_bf16 v[28:31], v[156:159], a[20:23], v[28:31]\n\t"
      "v_mfma_f32_16x16x32_bf16 v[32:35], a[60:63], a[0:3], 0\n\t"
      "v_mfma_f32_16x16x32_bf16 v[32:35], a[68:71], a[4:7], v[32:35]\n\t"
      "s_waitcnt lgkmcnt(2)\n\t"
      "v_mfma_f32_16x16x32_bf16 v[32:35], a[76:79], a[8:11], v[32:35]\n\t"
      "buffer_store_dwordx4 a[212:215], v12, s[44:47], s84 offen sc1\n\t"
      "v_mfma_f32_16x16x32_bf16 v[32:35], a[84:87], a[12:15], v[32:35]\n\t"
      "v_mfma_f32_16x16x32_bf16 v[32:35], a[92:95], a[16:19], v[32:35]\n\t"
      "v_mfma_f32_16x16x32_bf16 v[32:35], a[100:103], a[20:23], v[32:35]\n\t"
      "v_mfma_f32_16x16x32_bf16 v[36:39], a[64:67], a[0:3], 0\n\t"
      "v_mfma_f32_16x16x32_bf16 v[36:39], a[72:75], a[4:7], v[36:39]\n\t"
      "v_mfma_f32_16x16x32_bf16 v[36:39], a[80:83], a[8:11], v[36:39]\n\t"
      "v_mfma_f32_16x16x32_bf16 v[36:39], a[88:91], a[12:15], v[36:39]\n\t"
      "s_waitcnt lgkmcnt(1)\n\t"
      "v_mfma_f32_16x16x32_bf16 v[36:39], a[96:99], a[16:19], v[36:39]\n\t"
      "buffer_store_dwordx4 a[216:219], v13, s[44:47], s84 offen offset:1024 sc1\n\t"
      "v_mfma_f32_16x16x32_bf16 v[36:39], a[104:107], a[20:23], v[36:39]\n\t"
      "v_mfma_f32_16x16x32_bf16 v[16:19], v[160:163], a[24:27], v[16:19]\n\t"
      "ds_read_b128 a[0:3], v52 offset:0\n\t"
      "v_mfma_f32_16x16x32_bf16 v[16:19], v[176:179], a[28:31], v[16:19]\n\t"
      "ds_read_b128 a[4:7], v53 offset:0\n\t"
      "v_mfma_f32_16x16x32_bf16 v[16:19], v[192:195], a[32:35], v[16:19]\n\t"
      "ds_read_b128 a[8:11], v54 offset:0\n\t"
      "v_mfma_f32_16x16x32_bf16 v[16:19], v[208:211], a[36:39], v[16:19]\n\t"
      "ds_read_b128 a[12:15], v55 offset:0\n\t"
      "v_mfma_f32_16x16x32_bf16 v[16:19], v[224:227], a[40:43], v[16:19]\n\t"
      "ds_read_b128 a[16:19], v52 offset:256\n\t"
      "v_mfma_f32_16x16x32_bf16 v[16:19], v[240:243], a[44:47], v[16:19]\n\t"
      "ds_read_b128 a[20:23], v53 offset:256\n\t"
      "v_mfma_f32_16x16x32_bf16 v[20:23], v[164:167], a[24:27], v[20:23]\n\t"
      "s_add_u32 s51, s51, 12288\n\t"
      "s_cmp_eq_u32 s51, 160256\n\t"
      "s_cselect_b32 s51, 25088, s51\n\t"
      "v_mfma_f32_16x16x32_bf16 v[20:23], v[180:183], a[28:31], v[20:23]\n\t"
      "s_waitcnt lgkmcnt(6)\n\t"
      "v_mfma_f32_16x16x32_bf16 v[20:23], v[196:199], a[32:35], v[20:23]\n\t"
      "buffer_store_dwordx4 a[220:223], v14, s[44:47], s84 offen offset:2048 sc1\n\t"
      "v_mfma_f32_16x16x32_bf16 v[20:23], v[212:215], a[36:39], v[20:23]\n\t"
      "v_mfma_f32_16x16x32_bf16 v[20:23], v[228:231], a[40:43], v[20:23]\n\t"
      "s_waitcnt lgkmcnt(0)\n\t"
      "v_mfma_f32_16x16x32_bf16 v[20:23], v[244:247], a[44:47], v[20:23]\n\t"
      "v_mfma_f32_16x16x32_bf16 v[24:27], v[168:171], a[24:27], v[24:27]\n\t"
      "v_mfma_f32_16x16x32_bf16 v[24:27], v[184:187], a[28:31], v[24:27]\n\t"
      "v_cvt_pk_bf16_f32 v40, v16, v17\n\t"
      "v_cvt_pk_bf16_f32 v41, v18, v19\n\t"
      "v_mfma_f32_16x16x32_bf16 v[24:27], v[200:203], a[32:35], v[24:27]\n\t"
      "ds_write_b64 v0, v[40:41] offset:0\n\t"
      "v_mfma_f32_16x16x32_bf16 v[24:27], v[216:219], a[36:39], v[24:27]\n\t"
      "v_mfma_f32_16x16x32_bf16 v[24:27], v[232:235], a[40:43], v[24:27]\n\t"
      "v_mfma_f32_16x16x32_bf16 v[24:27], v[248:251], a[44:47], v[24:27]\n\t"
      "v_mfma_f32_16x16x32_bf16 v[28:31], v[172:175], a[24:27], v[28:31]\n\t"
      "v_mfma_f32_16x16x32_bf16 v[28:31], v[188:191], a[28:31], v[28:31]\n\t"
      "v_cvt_pk_bf16_f32 v42, v20, v21\n\t"
      "v_cvt_pk_bf16_f32 v43, v22, v23\n\t"
      "v_mfma_f32_16x16x32_bf16 v[28:31], v[204:207], a[32:35], v[28:31]\n\t"
      "ds_write_b64 v0, v[42:43] offset:32\n\t"
      "v_mfma_f32_16x16x32_bf16 v[28:31], v[220:223], a[36:39], v[28:31]\n\t"
      "v_mfma_f32_16x16x32_bf16 v[28:31], v[236:239], a[40:43], v[28:31]\n\t"
      "v_mfma_f32_16x16x32_bf16 v[28:31], v[252:255], a[44:47], v[28:31]\n\t"
      "v_mfma_f32_16x16x32_bf16 v[32:35], a[108:111], a[24:27], v[32:35]\n\t"
      "v_mfma_f32_16x16x32_bf16 v[32:35], a[116:119], a[28:31], v[32:35]\n\t"
      "v_cvt_pk_bf16_f32 v44, v24, v25\n\t"
      "v_cvt_pk_bf16_f32 v45, v26, v27\n\t"
      "v_mfma_f32_16x16x32_bf16 v[32:35], a[124:127], a[32:35], v[32:35]\n\t"
      "ds_write_b64 v0, v[44:45] offset:64\n\t"
      "v_mfma_f32_16x16x32_bf16 v[32:35], a[132:135], a[36:39], v[32:35]\n\t"
      "v_mfma_f32_16x16x32_bf16 v[32:35], a[140:143], a[40:43], v[32:35]\n\t"
      "v_mfma_f32_16x16x32_bf16 v[32:35], a[148:151], a[44:47], v[32:35]\n\t"
      "v_cvt_pk_bf16_f32 v46, v28, v29\n\t"
      "v_cvt_pk_bf16_f32 v47, v30, v31\n\t"
      "v_mfma_f32_16x16x32_bf16 v[36:39], a[112:115], a[24:27], v[36:39]\n\t"
      "ds_write_b64 v0, v[46:47] offset:96\n\t"
      "v_mfma_f32_16x16x32_bf16 v[36:39], a[120:123], a[28:31], v[36:39]\n\t"
      "v_mfma_f32_16x16x32_bf16 v[36:39], a[128:131], a[32:35], v[36:39]\n\t"
      "v_mfma_f32_16x16x32_bf16 v[36:39], a[136:139], a[36:39], v[36:39]\n\t"
      "v_mfma_f32_16x16x32_bf16 v[36:39], a[144:147], a[40:43], v[36:39]\n\t"
      "v_cvt_pk_bf16_f32 v48, v32, v33\n\t"
      "v_cvt_pk_bf16_f32 v49, v34, v35\n\t"
      "v_mfma_f32_16x16x32_bf16 v[36:39], a[152:155], a[44:47], v[36:39]\n\t"
      "ds_write_b64 v0, v[48:49] offset:128\n\t"
      "v_mfma_f32_16x16x32_bf16 v[16:19], v[64:67], a[0:3], 0\n\t"
      "ds_read_b128 a[24:27], v54 offset:256\n\t"
      "v_mfma_f32_16x16x32_bf16 v[16:19], v[80:83], a[4:7], v[16:19]\n\t"
      "ds_read_b128 a[28:31], v55 offset:256\n\t"
      "v_mfma_f32_16x16x32_bf16 v[16:19], v[96:99], a[8:11], v[16:19]\n\t"
      "ds_read_b128 a[32:35], v52 offset:512\n\t"
      "v_mfma_f32_16x16x32_bf16 v[16:19], v[112:115], a[12:15], v[16:19]\n\t"
      "ds_read_b128 a[36:39], v53 offset:512\n\t"
      "v_mfma_f32_16x16x32_bf16 v[16:19], v[128:131], a[16:19], v[16:19]\n\t"
      "ds_read_b128 a[40:43], v54 offset:512\n\t"
      "v_mfma_f32_16x16x32_bf16 v[16:19], v[144:147], a[20:23], v[16:19]\n\t"
      "ds_read_b128 a[44:47], v55 offset:512\n\t"
      "v_mfma_f32_16x16x32_bf16 v[20:23], v[68:71], a[0:3], 0\n\t"
      "v_cvt_pk_bf16_f32 v50, v36, v37\n\t"
      "v_cvt_pk_bf16_f32 v51, v38, v39\n\t"
      "v_mfma_f32_16x16x32_bf16 v[20:23], v[84:87], a[4:7], v[20:23]\n\t"
      "ds_write_b64 v0, v[50:51] offset:160\n\t"
      "v_mfma_f32_16x16x32_bf16 v[20:23], v[100:103], a[8:11], v[20:23]\n\t"
      "v_mfma_f32_16x16x32_bf16 v[20:23], v[116:119], a[12:15], v[20:23]\n\t"
      "v_mfma_f32_16x16x32_bf16 v[20:23], v[132:135], a[16:19], v[20:23]\n\t"
      "v_mfma_f32_16x16x32_bf16 v[20:23], v[148:151], a[20:23], v[20:23]\n\t"
      "v_mfma_f32_16x16x32_bf16 v[24:27], v[72:75], a[0:3], 0\n\t"
      "v_mfma_f32_16x16x32_bf16 v[24:27], v[88:91], a[4:7], v[24:27]\n\t"
      "v_mfma_f32_16x16x32_bf16 v[24:27], v[104:107], a[8:11], v[24:27]\n\t"
      "s_waitcnt lgkmcnt(0)\n\t"
      "s_barrier\n\t"
      "v_mfma_f32_16x16x32_bf16 v[24:27], v[120:123], a[12:15], v[24:27]\n\t"
      "v_mfma_f32_16x16x32_bf16 v[24:27], v[136:139], a[16:19], v[24:27]\n\t"
      "v_mfma_f32_16x16x32_bf16 v[24:27], v[152:155], a[20:23], v[24:27]\n\t"
      "v_mfma_f32_16x16x32_bf16 v[28:31], v[76:79], a[0:3], 0\n\t"
      "ds_read_b128 a[224:227], v8 offset:0\n\t"
      "v_mfma_f32_16x16x32_bf16 v[28:31], v[92:95], a[4:7], v[28:31]\n\t"
      "ds_read_b128 a[228:231], v60 offset:1024\n\t"
      "v_mfma_f32_16x16x32_bf16 v[28:31], v[108:111], a[8:11], v[28:31]\n\t"
      "ds_read_b128 a[232:235], v61 offset:2048\n\t"
      "v_mfma_f32_16x16x32_bf16 v[28:31], v[124:127], a[12:15], v[28:31]\n\t"
      "v_mfma_f32_16x16x32_bf16 v[28:31], v[140:143], a[16:19], v[28:31]\n\t"
      "v_mfma_f32_16x16x32_bf16 v[28:31], v[156:159], a[20:23], v[28:31]\n\t"
      "v_mfma_f32_16x16x32_bf16 v[32:35], a[60:63], a[0:3], 0\n\t"
      "v_mfma_f32_16x16x32_bf16 v[32:35], a[68:71], a[4:7], v[32:35]\n\t"
      "s_waitcnt lgkmcnt(2)\n\t"
      "v_mfma_f32_16x16x32_bf16 v[32:35], a[76:79], a[8:11], v[32:35]\n\t"
      "buffer_store_dwordx4 a[224:227], v9, s[44:47], s85 offen sc1\n\t"
      "v_mfma_f32_16x16x32_bf16 v[32:35], a[84:87], a[12:15], v[32:35]\n\t"
      "v_mfma_f32_16x16x32_bf16 v[32:35], a[92:95], a[16:19], v[32:35]\n\t"
      "v_mfma_f32_16x16x32_bf16 v[32:35], a[100:103], a[20:23], v[32:35]\n\t"
      "v_mfma_f32_16x16x32_bf16 v[36:39], a[64:67], a[0:3], 0\n\t"
      "v_mfma_f32_16x16x32_bf16 v[36:39], a[72:75], a[4:7], v[36:39]\n\t"
      "v_mfma_f32_16x16x32_bf16 v[36:39], a[80:83], a[8:11], v[36:39]\n\t"
      "v_mfma_f32_16x16x32_bf16 v[36:39], a[88:91], a[12:15], v[36:39]\n\t"
      "s_waitcnt lgkmcnt(1)\n\t"
      "v_mfma_f32_16x16x32_bf16 v[36:39], a[96:99], a[16:19], v[36:39]\n\t"
      "buffer_store_dwordx4 a[228:231], v10, s[44:47], s85 offen offset:1024 sc1\n\t"
      "v_mfma_f32_16x16x32_bf16 v[36:39], a[104:107], a[20:23], v[36:39]\n\t"
      "v_mfma_f32_16x16x32_bf16 v[16:19], v[160:163], a[24:27], v[16:19]\n\t"
      "v_mfma_f32_16x16x32_bf16 v[16:19], v[176:179], a[28:31], v[16:19]\n\t"
      "v_mfma_f32_16x16x32_bf16 v[16:19], v[192:195], a[32:35], v[16:19]\n\t"
      "v_mfma_f32_16x16x32_bf16 v[16:19], v[208:211], a[36:39], v[16:19]\n\t"
      "v_mfma_f32_16x16x32_bf16 v[16:19], v[224:227], a[40:43], v[16:19]\n\t"
      "v_mfma_f32_16x16x32_bf16 v[16:19], v[240:243], a[44:47], v[16:19]\n\t"
      "v_mfma_f32_16x16x32_bf16 v[20:23], v[164:167], a[24:27], v[20:23]\n\t"
      "v_mfma_f32_16x16x32_bf16 v[20:23], v[180:183], a[28:31], v[20:23]\n\t"
      "s_waitcnt lgkmcnt(0)\n\t"
      "v_mfma_f32_16x16x32_bf16 v[20:23], v[196:199], a[32:35], v[20:23]\n\t"
      "buffer_store_dwordx4 a[232:235], v11, s[44:47], s85 offen offset:2048 sc1\n\t"
      "v_mfma_f32_16x16x32_bf16 v[20:23], v[212:215], a[36:39], v[20:23]\n\t"
      "v_mfma_f32_16x16x32_bf16 v[20:23], v[228:231], a[40:43], v[20:23]\n\t"
      "v_mfma_f32_16x16x32_bf16 v[20:23], v[244:247], a[44:47], v[20:23]\n\t"
      "v_mfma_f32_16x16x32_bf16 v[24:27], v[168:171], a[24:27], v[24:27]\n\t"
      "v_mfma_f32_16x16x32_bf16 v[24:27], v[184:187], a[28:31], v[24:27]\n\t"
      "v_cvt_pk_bf16_f32 v40, v16, v17\n\t"
      "v_cvt_pk_bf16_f32 v41, v18, v19\n\t"
      "v_mfma_f32_16x16x32_bf16 v[24:27], v[200:203], a[32:35], v[24:27]\n\t"
      "ds_write_b64 v0, v[40:41] offset:12544\n\t"
      "v_mfma_f32_16x16x32_bf16 v[24:27], v[216:219], a[36:39], v[24:27]\n\t"
      "v_mfma_f32_16x16x32_bf16 v[24:27], v[232:235], a[40:43], v[24:27]\n\t"
      "v_mfma_f32_16x16x32_bf16 v[24:27], v[248:251], a[44:47], v[24:27]\n\t"
      "v_mfma_f32_16x16x32_bf16 v[28:31], v[172:175], a[24:27], v[28:31]\n\t"
      "v_mfma_f32_16x16x32_bf16 v[28:31], v[188:191], a[28:31], v[28:31]\n\t"
      "v_cvt_pk_bf16_f32 v42, v20, v21\n\t"
      "v_cvt_pk_bf16_f32 v43, v22, v23\n\t"
      "v_mfma_f32_16x16x32_bf16 v[28:31], v[204:207], a[32:35], v[28:31]\n\t"
      "ds_write_b64 v0, v[42:43] offset:12576\n\t"
      "v_mfma_f32_16x16x32_bf16 v[28:31], v[220:223], a[36:39], v[28:31]\n\t"
      "v_mfma_f32_16x16x32_bf16 v[28:31], v[236:239], a[40:43], v[28:31]\n\t"
      "v_mfma_f32_16x16x32_bf16 v[28:31], v[252:255], a[44:47], v[28:31]\n\t"
      "v_mfma_f32_16x16x32_bf16 v[32:35], a[108:111], a[24:27], v[32:35]\n\t"
      "v_mfma_f32_16x16x32_bf16 v[32:35], a[116:119], a[28:31], v[32:35]\n\t"
      "v_cvt_pk_bf16_f32 v44, v24, v25\n\t"
      "v_cvt_pk_bf16_f32 v45, v26, v27\n\t"
      "v_mfma_f32_16x16x32_bf16 v[32:35], a[124:127], a[32:35], v[32:35]\n\t"
      "ds_write_b64 v0, v[44:45] offset:12608\n\t"
      "v_mfma_f32_16x16x32_bf16 v[32:35], a[132:135], a[36:39], v[32:35]\n\t"
      "v_mfma_f32_16x16x32_bf16 v[32:35], a[140:143], a[40:43], v[32:35]\n\t"
      "v_mfma_f32_16x16x32_bf16 v[32:35], a[148:151], a[44:47], v[32:35]\n\t"
      "v_cvt_pk_bf16_f32 v46, v28, v29\n\t"
      "v_cvt_pk_bf16_f32 v47, v30, v31\n\t"
      "v_mfma_f32_16x16x32_bf16 v[36:39], a[112:115], a[24:27], v[36:39]\n\t"
      "ds_write_b64 v0, v[46:47] offset:12640\n\t"
      "v_mfma_f32_16x16x32_bf16 v[36:39], a[120:123], a[28:31], v[36:39]\n\t"
      "v_mfma_f32_16x16x32_bf16 v[36:39], a[128:131], a[32:35], v[36:39]\n\t"
      "v_mfma_f32_16x16x32_bf16 v[36:39], a[136:139], a[36:39], v[36:39]\n\t"
      "v_mfma_f32_16x16x32_bf16 v[36:39], a[144:147], a[40:43], v[36:39]\n\t"
      "v_cvt_pk_bf16_f32 v48, v32, v33\n\t"
      "v_cvt_pk_bf16_f32 v49, v34, v35\n\t"
      "v_mfma_f32_16x16x32_bf16 v[36:39], a[152:155], a[44:47], v[36:39]\n\t"
      "ds_write_b64 v0, v[48:49] offset:12672\n\t"
      ".Ltail_%=:\n\t"
      "s_nop 6\n\t"
      "v_cvt_pk_bf16_f32 v50, v36, v37\n\t"
      "v_cvt_pk_bf16_f32 v51, v38, v39\n\t"
      "ds_write_b64 v0, v[50:51] offset:12704\n\t"
      "s_cmp_eq_u32 s85, 113221632\n\t"
      "s_cbranch_scc1 .Lwrapo_%=\n\t"
      ".Lwrappedo_%=:\n\t"
      "s_waitcnt lgkmcnt(0)\n\t"
      "s_barrier\n\t"
      "ds_read_b128 a[48:51], v8 offset:12544\n\t"
      "ds_read_b128 a[52:55], v60 offset:13568\n\t"
      "ds_read_b128 a[56:59], v61 offset:14592\n\t"
      "s_waitcnt lgkmcnt(2)\n\t"
      "buffer_store_dwordx4 a[48:51], v12, s[44:47], s85 offen sc1\n\t"
      "s_waitcnt lgkmcnt(1)\n\t"
      "buffer_store_dwordx4 a[52:55], v13, s[44:47], s85 offen offset:1024 sc1\n\t"
      "s_waitcnt lgkmcnt(0)\n\t"
      "buffer_store_dwordx4 a[56:59], v14, s[44:47], s85 offen offset:2048 sc1\n\t"
      "s_cmp_lt_i32 s91, 0\n\t"
      "s_cselect_b32 s30, -1, 0\n\t"
      "s_xor_b32 s16, s91, 0x3ffff\n\t"
      "s_and_b32 s16, s16, s30\n\t"
      "s_cbranch_scc0 .Lend_%=\n\t"
      "s_waitcnt vmcnt(0) lgkmcnt(0)\n\t"
      "s_barrier\n\t"
      "s_lshl1_add_u32 s30, s14, 24576\n\t"
      "s_sub_u32 s15, s30, s15\n\t"
      "s_branch .Lstart_%=\n\t"
      ".Lwrap3_%=:\n\t"
      "v_add_u32 v5, s48, v5\n\t"
      "v_subrev_u32 v62, 113246208, v5\n\t"
      "v_min_u32 v5, v5, v62\n\t"
      "v_add_u32 v6, s48, v6\n\t"
      "v_subrev_u32 v62, 113246208, v6\n\t"
      "v_min_u32 v6, v6, v62\n\t"
      "v_add_u32 v7, s48, v7\n\t"
      "v_subrev_u32 v62, 113246208, v7\n\t"
      "v_min_u32 v7, v7, v62\n\t"
      "s_mov_b32 s48, 0\n\t"
      "buffer_load_dwordx4 v5, s[40:43], s48 offen nt lds\n\t"
      "buffer_load_dwordx4 v6, s[40:43], s48 offen offset:1024 nt lds\n\t"
      "buffer_load_dwordx4 v7, s[40:43], s48 offen offset:2048 nt lds\n\t"
      "s_branch .Lwrapped3_%=\n\t"
      ".Lwrapo_%=:\n\t"
      "v_add_u32 v12, s85, v12\n\t"
      "v_subrev_u32 v62, 113246208, v12\n\t"
      "v_min_u32 v12, v12, v62\n\t"
      "v_add_u32 v13, s85, v13\n\t"
      "v_subrev_u32 v62, 113246208, v13\n\t"
      "v_min_u32 v13, v13, v62\n\t"
      "v_add_u32 v14, s85, v14\n\t"
      "v_subrev_u32 v62, 113246208, v14\n\t"
      "v_min_u32 v14, v14, v62\n\t"
      "s_mov_b32 s85, 0\n\t"
      "s_branch .Lwrappedo_%=\n\t"
      ".Lwait_%=:\n\t"
      "s_sub_u32 s52, s52, 1\n\t"
      "s_cbranch_scc1 .Lwait2_%=\n\t"
      "s_add_u32 s90, s90, 0x20000\n\t"
      "v_mov_b32 v54, s90\n\t"
      "v_mov_b32 v55, s19\n\t"
      "global_store_dword v55, v54, s[12:13] sc1\n\t"
      "s_sleep 16\n\t"
      "global_load_dword v40, v62, s[12:13] sc1\n\t"
      "s_waitcnt vmcnt(0)\n\t"
      "s_branch .Lcheck_%=\n\t"
      ".Lwait2_%=:\n\t"
      "s_brev_b32 s91, 1\n\t"
      "s_branch .Lwaited_%=\n\t"
      ".Lspinf_%=:\n\t"
      "s_memrealtime s[36:37]\n\t"
      "s_waitcnt lgkmcnt(0)\n\t"
      "s_sub_u32 s53, s36, s38\n\t"
      "s_cmp_lt_i32 s53, 0\n\t"
      "s_cbranch_scc1 .Lspinf_%=\n\t"
      "s_branch .Lspunf_%=\n\t"
      ".Lspin0_%=:\n\t"
      "s_memrealtime s[36:37]\n\t"
      "s_waitcnt lgkmcnt(0)\n\t"
      "s_sub_u32 s53, s36, s38\n\t"
      "s_cmp_lt_i32 s53, 0\n\t"
      "s_cbranch_scc1 .Lspin0_%=\n\t"
      "s_branch .Lspun0_%=\n\t"
      ".Lspin1_%=:\n\t"
      "s_memrealtime s[36:37]\n\t"
      "s_waitcnt lgkmcnt(0)\n\t"
      "s_sub_u32 s53, s36, s38\n\t"
      "s_cmp_lt_i32 s53, 0\n\t"
      "s_cbranch_scc1 .Lspin1_%=\n\t"
      "s_branch .Lspun1_%=\n\t"
      ".Lend_%=:\n\t"
      : [tid] "+v"(tid)
      : [karg] "s"(__builtin_amdgcn_kernarg_segment_ptr()), [blk] "s"(blockIdx.x), [lds] "s"((unsigned)(size_t)lds)
      : "v1", "v2", "v3", "v4", "v5", "v6", "v7", "v8", "v9", "v10", "v11", "v12", "v13", "v14", "v15", "v16", "v17", "v18", "v19", "v20", "v21", "v22", "v23", "v24", "v25", "v26", "v27", "v28", "v29", "v30", "v31", "v32", "v33", "v34", "v35", "v36", "v37", "v38", "v39", "v40", "v41", "v42", "v43", "v44", "v45", "v46", "v47", "v48", "v49", "v50", "v51", "v52", "v53", "v54", "v55", "v56", "v57", "v58", "v59", "v60", "v61", "v62", "v63", "v64", "v65", "v66", "v67", "v68", "v69", "v70", "v71", "v72", "v73", "v74", "v75", "v76", "v77", "v78", "v79", "v80", "v81", "v82", "v83", "v84", "v85", "v86", "v87", "v88", "v89", "v90", "v91", "v92", "v93", "v94", "v95", "v96", "v97", "v98", "v99", "v100", "v101", "v102", "v103", "v104", "v105", "v106", "v107", "v108", "v109", "v110", "v111", "v112", "v113", "v114", "v115", "v116", "v117", "v118", "v119", "v120", "v121", "v122", "v123", "v124", "v125", "v126", "v127", "v128", "v129", "v130", "v131", "v132", "v133", "v134", "v135", "v136", "v137", "v138", "v139", "v140", "v141", "v142", "v143", "v144", "v145", "v146", "v147", "v148", "v149", "v150", "v151", "v152", "v153", "v154", "v155", "v156", "v157", "v158", "v159", "v160", "v161", "v162", "v163", "v164", "v165", "v166", "v167", "v168", "v169", "v170", "v171", "v172", "v173", "v174", "v175", "v176", "v177", "v178", "v179", "v180", "v181", "v182", "v183", "v184", "v185", "v186", "v187", "v188", "v189", "v190", "v191", "v192", "v193", "v194", "v195", "v196", "v197", "v198", "v199", "v200", "v201", "v202", "v203", "v204", "v205", "v206", "v207", "v208", "v209", "v210", "v211", "v212", "v213", "v214", "v215", "v216", "v217", "v218", "v219", "v220", "v221", "v222", "v223", "v224", "v225", "v226", "v227", "v228", "v229", "v230", "v231", "v232", "v233", "v234", "v235", "v236", "v237", "v238", "v239", "v240", "v241", "v242", "v243", "v244", "v245", "v246", "v247", "v248", "v249", "v250", "v251", "v252", "v253", "v254", "v255", "a0", "a1", "a2", "a3", "a4", "a5", "a6", "a7", "a8", "a9", "a10", "a11", "a12", "a13", "a14", "a15", "a16", "a17", "a18", "a19", "a20", "a21", "a22", "a23", "a24", "a25", "a26", "a27", "a28", "a29", "a30", "a31", "a32", "a33", "a34", "a35", "a36", "a37", "a38", "a39", "a40", "a41", "a42", "a43", "a44", "a45", "a46", "a47", "a48", "a49", "a50", "a51", "a52", "a53", "a54", "a55", "a56", "a57", "a58", "a59", "a60", "a61", "a62", "a63", "a64", "a65", "a66", "a67", "a68", "a69", "a70", "a71", "a72", "a73", "a74", "a75", "a76", "a77", "a78", "a79", "a80", "a81", "a82", "a83", "a84", "a85", "a86", "a87", "a88", "a89", "a90", "a91", "a92", "a93", "a94", "a95", "a96", "a97", "a98", "a99", "a100", "a101", "a102", "a103", "a104", "a105", "a106", "a107", "a108", "a109", "a110", "a111", "a112", "a113", "a114", "a115", "a116", "a117", "a118", "a119", "a120", "a121", "a122", "a123", "a124", "a125", "a126", "a127", "a128", "a129", "a130", "a131", "a132", "a133", "a134", "a135", "a136", "a137", "a138", "a139", "a140", "a141", "a142", "a143", "a144", "a145", "a146", "a147", "a148", "a149", "a150", "a151", "a152", "a153", "a154", "a155", "a156", "a157", "a158", "a159", "a160", "a161", "a162", "a163", "a164", "a165", "a166", "a167", "a168", "a169", "a170", "a171", "a172", "a173", "a174", "a175", "a176", "a177", "a178", "a179", "a180", "a181", "a182", "a183", "a184", "a185", "a186", "a187", "a188", "a189", "a190", "a191", "a192", "a193", "a194", "a195", "a196", "a197", "a198", "a199", "a200", "a201", "a202", "a203", "a204", "a205", "a206", "a207", "a208", "a209", "a210", "a211", "a212", "a213", "a214", "a215", "a216", "a217", "a218", "a219", "a220", "a221", "a222", "a223", "a224", "a225", "a226", "a227", "a228", "a229", "a230", "a231", "a232", "a233", "a234", "a235", "a236", "a237", "a238", "a239", "a240", "a241", "a242", "a243", "a244", "a245", "a246", "a247", "a248", "a249", "a250", "a251", "a252", "a253", "a254", "a255", "s4", "s5", "s6", "s7", "s8", "s9", "s10", "s11", "s12", "s13", "s14", "s15", "s16", "s17", "s18", "s19", "s20", "s21", "s22", "s23", "s24", "s25", "s26", "s27", "s28", "s29", "s30", "s31", "s32", "s33", "s34", "s35", "s36", "s37", "s38", "s39", "s40", "s41", "s42", "s43", "s44", "s45", "s46", "s47", "s48", "s49", "s50", "s51", "s52", "s53", "s54", "s55", "s56", "s57", "s58", "s59", "s60", "s61", "s62", "s63", "s64", "s65", "s66", "s67", "s68", "s69", "s70", "s71", "s72", "s73", "s74", "s75", "s76", "s77", "s78", "s79", "s80", "s81", "s82", "s83", "s84", "s85", "s86", "s87", "s88", "s89", "s90", "s91", "vcc", "scc", "memory");
}

__global__ __launch_bounds__(384) void dwsep_plain(const unsigned short* __restrict__ x, const unsigned short* __restrict__ dw,
                                                   const unsigned short* __restrict__ pw, unsigned short* __restrict__ out) {
  __shared__ float xs[384];
  const unsigned n = threadIdx.x;
  const size_t row = (size_t)blockIdx.x * 384u;
  xs[n] = __uint_as_float((unsigned)x[row + n] << 16) * __uint_as_float((unsigned)dw[n] << 16);
  __syncthreads();
  float acc = 0.f;
  for (int k = 0; k < 384; ++k) acc += xs[k] * __uint_as_float((unsigned)pw[k * 384 + n] << 16);
  const __bf16 o = (__bf16)acc;
  out[row + n] = __builtin_bit_cast(unsigned short, o);
}

extern "C" void kernel_launch(void* const* d_in, const int* in_sizes, int n_in, void* d_out, int out_size,
                              void* d_ws, size_t ws_size, hipStream_t stream) {
  const unsigned short* x = (const unsigned short*)d_in[0];
  const unsigned short* dw = (const unsigned short*)d_in[1];
  const unsigned short* pw = (const unsigned short*)d_in[2];
  unsigned short* out = (unsigned short*)d_out;
  if (in_sizes[0] == 56623104) dwsep_kernel<<<NWG, 256, 0, stream>>>(x, dw, pw, out, (unsigned*)d_ws);
  else dwsep_plain<<<in_sizes[0] / 384, 384, 0, stream>>>(x, dw, pw, out);
}
```

```cpp
#include <hip/hip_runtime.h>

constexpr unsigned ROWB = 768, TILEB = 12288, NWG = 256;

__global__ __launch_bounds__(256, 1) void dwsep_kernel(const unsigned short* __restrict__ x,
                                                       const unsigned short* __restrict__ dw,
                                                       const unsigned short* __restrict__ pw,
                                                       unsigned short* __restrict__ out, unsigned* ws) {
  __shared__ __attribute__((aligned(256))) char lds[163392];
  unsigned tid = threadIdx.x;
  asm volatile(
      "s_memtime s[32:33]\n\t"
      "s_load_dwordx8 s[4:11], %[karg], 0\n\t"
      "s_load_dwordx2 s[12:13], %[karg], 0x20\n\t"
      "v_readfirstlane_b32 s30, %[tid]\n\t"
      "s_lshr_b32 s31, %[blk], 1\n\t"
      "s_and_b32 s31, s31, 12\n\t"
      "s_and_b32 s34, %[blk], 3\n\t"
      "s_or_b32 s31, s31, s34\n\t"
      "s_lshr_b32 s30, s30, 6\n\t"
      "s_lshr_b32 s34, %[blk], 5\n\t"
      "s_bfe_u32 s35, %[blk], 0x10002\n\t"
      "s_mul_i32 s17, s30, 6144\n\t"
      "s_mul_i32 s18, s30, 3072\n\t"
      "s_mul_i32 s14, s34, 14155776\n\t"
      "s_mul_i32 s36, s31, 49152\n\t"
      "s_add_u32 s14, s14, s36\n\t"
      "s_mul_i32 s36, s35, 24576\n\t"
      "s_add_u32 s15, s14, s36\n\t"
      "s_mov_b32 s16, 0\n\t"
      "s_lshr_b32 s36, %[blk], 3\n\t"
      "s_lshl2_add_u32 s24, s36, s30\n\t"
      "s_sub_u32 s36, s24, s36\n\t"
      "s_sub_u32 s36, s36, 1\n\t"
      "s_mul_i32 s23, s36, 3072\n\t"
      "s_lshl_b32 s20, s34, 3\n\t"
      "s_mul_i32 s36, s34, 27\n\t"
      "s_lshr_b32 s36, s36, 2\n\t"
      "s_lshr_b32 s37, s31, 1\n\t"
      "s_add_u32 s19, s37, s20\n\t"
      "s_add_u32 s37, s37, s36\n\t"
      "s_min_u32 s37, s37, 53\n\t"
      "s_lshl_b32 s22, s37, 21\n\t"
      "s_lshl_b32 s36, s36, 21\n\t"
      "s_sub_u32 s21, s14, s36\n\t"
      "s_add_u32 s21, s21, 24576\n\t"
      "s_lshl_b32 s19, s19, 8\n\t"
      "s_and_b32 s31, s31, 1\n\t"
      "s_lshl1_add_u32 s31, s31, s35\n\t"
      "s_lshl2_add_u32 s19, s31, s19\n\t"
      ".Lstart_%=:\n\t"
      "v_mbcnt_lo_u32_b32 v233, -1, 0\n\t"
      "v_mbcnt_hi_u32_b32 v233, -1, v233\n\t"
      "s_mul_i32 s30, s17, 683\n\t"
      "s_lshr_b32 s30, s30, 16\n\t"
      "s_and_b32 s30, s30, 0xc0\n\t"
      "s_brev_b32 s34, 8\n\t"
      "s_mov_b32 s35, 0x20201010\n\t"
      "s_mov_b32 s36, 0x30303020\n\t"
      "v_lshl_add_u32 v1, v233, 4, s18\n\t"
      "v_lshrrev_b32 v16, 1, v233\n\t"
      "v_and_b32 v16, 24, v16\n\t"
      "v_bfe_u32 v17, s34, v16, 8\n\t"
      "v_or_b32 v17, s30, v17\n\t"
      "v_xor_b32 v18, v17, v1\n\t"
      "v_add_u32 v19, 80, v17\n\t"
      "v_add_u32 v17, 160, v17\n\t"
      "v_and_b32 v19, 256, v19\n\t"
      "v_and_b32 v17, 256, v17\n\t"
      "v_mad_u32_u24 v2, v19, 30, v18\n\t"
      "v_mad_u32_u24 v5, v17, 33, v18\n\t"
      "v_add_u32 v5, 19968, v5\n\t"
      "v_bfe_u32 v17, s35, v16, 8\n\t"
      "v_or_b32 v17, s30, v17\n\t"
      "v_xor_b32 v18, v17, v1\n\t"
      "v_add_u32 v19, 80, v17\n\t"
      "v_add_u32 v17, 160, v17\n\t"
      "v_and_b32 v19, 256, v19\n\t"
      "v_and_b32 v17, 256, v17\n\t"
      "v_mad_u32_u24 v3, v19, 30, v18\n\t"
      "v_mad_u32_u24 v6, v17, 33, v18\n\t"
      "v_add_u32 v6, 19968, v6\n\t"
      "v_bfe_u32 v17, s36, v16, 8\n\t"
      "v_or_b32 v17, s30, v17\n\t"
      "v_xor_b32 v18, v17, v1\n\t"
      "v_add_u32 v19, 80, v17\n\t"
      "v_add_u32 v17, 160, v17\n\t"
      "v_and_b32 v19, 256, v19\n\t"
      "v_and_b32 v17, 256, v17\n\t"
      "v_mad_u32_u24 v4, v19, 30, v18\n\t"
      "v_mad_u32_u24 v7, v17, 33, v18\n\t"
      "v_add_u32 v7, 19968, v7\n\t"
      "s_sub_u32 s31, s30, 64\n\t"
      "v_add_u32 v17, s31, v233\n\t"
      "v_min_u32 v17, 193, v17\n\t"
      "v_lshlrev_b32 v18, 2, v17\n\t"
      "v_lshlrev_b32 v17, 1, v17\n\t"
      "v_and_b32 v18, -16, v18\n\t"
      "v_and_or_b32 v8, v17, 6, v18\n\t"
      "s_mov_b32 s86, 294912\n\t"
      "s_mov_b32 s87, 0x00020000\n\t"
      "s_mov_b32 s42, 113246208\n\t"
      "s_mov_b32 s43, 0x00020000\n\t"
      "s_cmp_lg_u32 s17, 0\n\t"
      "s_cbranch_scc1 .Lno0_%=\n\t"
      "s_movk_i32 s31, -61\n\t"
      "v_lshrrev_b32 v16, 2, v233\n\t"
      "v_add_u32 v17, s20, v16\n\t"
      "v_lshl_add_u32 v18, v16, 3, s31\n\t"
      "v_cmp_gt_u32 vcc, 32, v233\n\t"
      "v_and_b32 v19, 3, v233\n\t"
      "v_lshlrev_b32 v19, 2, v19\n\t"
      "v_cndmask_b32 v17, v18, v17, vcc\n\t"
      "v_lshl_add_u32 v62, v17, 8, v19\n\t"
      "s_waitcnt lgkmcnt(2)\n\t"
      "s_add_u32 s32, s32, %[blk]\n\t"
      "s_mul_i32 s32, s32, 0x9e3779b1\n\t"
      "s_lshr_b32 s32, s32, 9\n\t"
      "s_and_b32 s32, s32, 0x1fdf80\n\t"
      "s_or_b32 s22, s22, s32\n\t"
      "v_mov_b32 v234, s22\n\t"
      "v_add_u32 v235, 8192, v234\n\t"
      "s_movk_i32 s64, 120\n\t"
      "s_mov_b64 vcc, 0\n\t"
      ".Lsweep_%=:\n\t"
      "s_getreg_b32 s30, hwreg(HW_REG_IB_STS, 0, 12)\n\t"
      "s_and_b32 s31, s30, 15\n\t"
      "s_lshr_b32 s30, s30, 8\n\t"
      "s_cmp_eq_u32 s31, 8\n\t"
      "s_addc_u32 s62, s62, 0\n\t"
      "s_cmp_eq_u32 s30, 1\n\t"
      "s_addc_u32 s63, s63, 0\n\t"
      "s_cmp_gt_u32 s31, 7\n\t"
      "s_addc_u32 s30, s30, 0\n\t"
      "s_sub_u32 s64, s64, 1\n\t"
      "s_min_u32 s30, s30, s64\n\t"
      "s_cmp_lg_u32 s30, 0\n\t"
      "s_cbranch_scc1 .Lsweep_%=\n\t"
      "s_cbranch_vccnz .Lswept_%=\n\t"
      "s_waitcnt lgkmcnt(0)\n\t"
      "global_load_dword v236, v234, s[4:5]\n\t"
      "global_load_dword v237, v235, s[4:5]\n\t"
      "s_memrealtime s[28:29]\n\t"
      "s_load_dword s60, s[10:11], s22\n\t"
      "s_load_dword s61, s[10:11], s22 offset:8192\n\t"
      "global_load_dword v63, v62, s[12:13] sc1\n\t"
      "s_mov_b64 s[40:41], s[4:5]\n\t"
      "s_add_u32 m0, s18, 25088\n\t"
      "s_add_u32 s48, s15, 0\n\t"
      "buffer_load_dwordx4 v2, s[40:43], s48 offen nt lds\n\t"
      "buffer_load_dwordx4 v3, s[40:43], s48 offen offset:1024 nt lds\n\t"
      "buffer_load_dwordx4 v4, s[40:43], s48 offen offset:2048 nt lds\n\t"
      "s_add_u32 m0, s18, 37376\n\t"
      "s_add_u32 s48, s15, 0\n\t"
      "buffer_load_dwordx4 v5, s[40:43], s48 offen nt lds\n\t"
      "buffer_load_dwordx4 v6, s[40:43], s48 offen offset:1024 nt lds\n\t"
      "buffer_load_dwordx4 v7, s[40:43], s48 offen offset:2048 nt lds\n\t"
      "s_barrier\n\t"
      "s_mov_b64 s[62:63], 0\n\t"
      "s_movk_i32 s64, 120\n\t"
      "s_mov_b64 vcc, -1\n\t"
      "s_branch .Lsweep_%=\n\t"
      ".Lswept_%=:\n\t"
      "s_min_u32 s62, s62, 7\n\t"
      "s_min_u32 s63, s63, 7\n\t"
      "s_lshl_b32 s63, s63, 8\n\t"
      "s_or_b32 s30, s62, s63\n\t"
      "s_sub_u32 s31, 0x808, s30\n\t"
      "s_add_u32 s30, s30, 0x10808\n\t"
      "s_getreg_b32 s62, hwreg(HW_REG_XCC_ID, 2, 1)\n\t"
      "s_cmp_lg_u32 s62, 0\n\t"
      "s_cselect_b32 s91, s30, s31\n\t"
      "s_mov_b64 s[84:85], s[8:9]\n\t"
      "v_mul_u32_u24 v16, 49152, v1\n\t"
      "v_add_u32 v16, s21, v16\n\t"
      "v_lshrrev_b32 v16, 21, v16\n\t"
      "v_lshlrev_b32 v16, 2, v16\n\t"
      "v_accvgpr_write_b32 a248, v16\n\t"
      "s_branch .Lgo0_%=\n\t"
      ".Lno0_%=:\n\t"
      "s_movk_i32 s82, 768\n\t"
      "s_mov_b32 s83, 0x00020000\n\t"
      "v_and_b32 v234, 0x3f0, v1\n\t"
      "s_waitcnt lgkmcnt(0)\n\t"
      "s_mov_b64 s[84:85], s[8:9]\n\t"
      "s_mov_b64 s[80:81], s[6:7]\n\t"
      "s_mov_b64 s[40:41], s[4:5]\n\t"
      "s_barrier\n\t"
      "buffer_load_dwordx4 a[200:203], v234, s[84:87], s23 offen offset:0\n\t"
      "buffer_load_dwordx4 a[204:207], v234, s[84:87], s23 offen offset:1024\n\t"
      "buffer_load_dwordx4 a[208:211], v234, s[84:87], s23 offen offset:2048\n\t"
      "buffer_load_ushort v224, v8, s[80:83], 0 offen\n\t"
      "buffer_load_ushort v225, v8, s[80:83], 0 offen offset:8\n\t"
      "s_add_u32 m0, s18, 25088\n\t"
      "s_add_u32 s48, s15, 0\n\t"
      "buffer_load_dwordx4 v2, s[40:43], s48 offen nt lds\n\t"
      "buffer_load_dwordx4 v3, s[40:43], s48 offen offset:1024 nt lds\n\t"
      "buffer_load_dwordx4 v4, s[40:43], s48 offen offset:2048 nt lds\n\t"
      "s_add_u32 m0, s18, 37376\n\t"
      "s_add_u32 s48, s15, 0\n\t"
      "buffer_load_dwordx4 v5, s[40:43], s48 offen nt lds\n\t"
      "buffer_load_dwordx4 v6, s[40:43], s48 offen offset:1024 nt lds\n\t"
      "buffer_load_dwordx4 v7, s[40:43], s48 offen offset:2048 nt lds\n\t"
      ".Lgo0_%=:\n\t"
      "s_getpc_b64 s[36:37]\n\t"
      ".Lpc_%=:\n\t"
      "s_lshr_b32 s30, s17, 5\n\t"
      "v_lshlrev_b32 v16, 4, v233\n\t"
      "v_add_u32 v17, 0, v233\n\t"
      "v_mul_u32_u24 v17, 171, v17\n\t"
      "v_lshrrev_b32 v17, 11, v17\n\t"
      "v_lshlrev_b32 v18, 2, v17\n\t"
      "v_and_b32 v18, 32, v18\n\t"
      "v_xor_b32 v18, v18, v16\n\t"
      "v_mul_u32_u24 v17, 576, v17\n\t"
      "v_add3_u32 v9, v17, s30, v18\n\t"
      "v_add_u32 v17, 64, v233\n\t"
      "v_mul_u32_u24 v17, 171, v17\n\t"
      "v_lshrrev_b32 v17, 11, v17\n\t"
      "v_lshlrev_b32 v18, 2, v17\n\t"
      "v_and_b32 v18, 32, v18\n\t"
      "v_xor_b32 v18, v18, v16\n\t"
      "v_mul_u32_u24 v17, 576, v17\n\t"
      "v_add3_u32 v10, v17, s30, v18\n\t"
      "v_add_u32 v17, 128, v233\n\t"
      "v_mul_u32_u24 v17, 171, v17\n\t"
      "v_lshrrev_b32 v17, 11, v17\n\t"
      "v_lshlrev_b32 v18, 2, v17\n\t"
      "v_and_b32 v18, 32, v18\n\t"
      "v_xor_b32 v18, v18, v16\n\t"
      "v_mul_u32_u24 v17, 576, v17\n\t"
      "v_add3_u32 v11, v17, s30, v18\n\t"
      "s_mov_b64 s[44:45], s[10:11]\n\t"
      "s_mov_b32 s46, 0\n\t"
      "s_mov_b32 s47, 0x00020000\n\t"
      "s_lshl_b32 s30, s24, 6\n\t"
      "s_add_u32 s31, s30, 0\n\t"
      "s_min_u32 s31, s31, .Lend_%=-.Lpc_%=-4\n\t"
      "s_load_dword s35, s[36:37], s31\n\t"
      "s_add_u32 s31, s30, 8192\n\t"
      "s_min_u32 s31, s31, .Lend_%=-.Lpc_%=-4\n\t"
      "s_load_dword s35, s[36:37], s31\n\t"
      "s_add_u32 s31, s30, 16384\n\t"
      "s_min_u32 s31, s31, .Lend_%=-.Lpc_%=-4\n\t"
      "s_load_dword s35, s[36:37], s31\n\t"
      "s_add_u32 s31, s30, 24576\n\t"
      "s_min_u32 s31, s31, .Lend_%=-.Lpc_%=-4\n\t"
      "s_load_dword s35, s[36:37], s31\n\t"
      "s_and_b32 s31, s24, 63\n\t"
      "s_lshl_b32 s31, s31, 21\n\t"
      "s_min_u32 s31, s31, 113246144\n\t"
      "s_bitcmp1_b32 s24, 6\n\t"
      "s_cselect_b64 s[32:33], s[44:45], s[40:41]\n\t"
      "s_load_dword s35, s[32:33], s31\n\t"
      "s_lshr_b32 s30, s17, 5\n\t"
      "v_lshrrev_b32 v16, 4, v233\n\t"
      "v_and_b32 v17, 15, v233\n\t"
      "v_and_b32 v18, 3, v233\n\t"
      "v_bfe_u32 v19, v233, 2, 2\n\t"
      "v_lshl_add_u32 v19, v16, 3, v19\n\t"
      "v_mul_u32_u24 v19, 192, v19\n\t"
      "v_lshl_add_u32 v19, v18, 3, v19\n\t"
      "v_lshlrev_b32 v20, 1, v233\n\t"
      "v_and_b32 v20, 32, v20\n\t"
      "v_add3_u32 v12, v19, v20, s17\n\t"
      "v_xor_b32 v13, 32, v12\n\t"
      "v_xor_b32 v19, v16, v17\n\t"
      "v_lshlrev_b32 v19, 4, v19\n\t"
      "v_mul_u32_u24 v20, 768, v17\n\t"
      "v_add_u32 v15, v19, v20\n\t"
      "v_mul_u32_u24 v20, 784, v17\n\t"
      "v_lshl_add_u32 v20, v16, 3, v20\n\t"
      "v_add_u32 v0, s30, v20\n\t"
      "v_lshlrev_b32 v19, 6, v16\n\t"
      "v_lshl_add_u32 v19, v18, 4, v19\n\t"
      "v_add_u32 v14, 160256, v19\n\t"
      "v_and_b32 v226, 0xfffffff0, v8\n\t"
      "v_and_b32 v227, 6, v8\n\t"
      "v_lshlrev_b32 v226, 2, v226\n\t"
      "v_lshl_add_u32 v226, v227, 3, v226\n\t"
      "v_add_u32 v226, 160256, v226\n\t"
      "v_and_b32 v232, 2, v8\n\t"
      "v_lshlrev_b32 v232, 3, v232\n\t"
      "v_cmp_gt_u32 vcc, 4, v227\n\t"
      "v_mov_b32 v56, v15\n\t"
      "v_xor_b32 v57, 64, v15\n\t"
      "v_xor_b32 v58, 128, v15\n\t"
      "v_xor_b32 v59, 192, v15\n\t"
      "v_add_u32 v60, 86528, v12\n\t"
      "v_add_u32 v61, 86528, v13\n\t"
      "v_xor_b32 v16, v2, v1\n\t"
      "v_and_b32 v16, 0xf0, v16\n\t"
      "v_xor_b32 v17, v16, v2\n\t"
      "v_xor_b32 v18, v16, v5\n\t"
      "v_add_u32 v19, v16, v1\n\t"
      "v_accvgpr_write_b32 a213, v17\n\t"
      "v_accvgpr_write_b32 a216, v18\n\t"
      "v_accvgpr_write_b32 a219, v19\n\t"
      "v_xor_b32 v16, v3, v1\n\t"
      "v_and_b32 v16, 0xf0, v16\n\t"
      "v_xor_b32 v17, v16, v3\n\t"
      "v_xor_b32 v18, v16, v6\n\t"
      "v_add_u32 v19, v16, v1\n\t"
      "v_accvgpr_write_b32 a214, v17\n\t"
      "v_accvgpr_write_b32 a217, v18\n\t"
      "v_accvgpr_write_b32 a220, v19\n\t"
      "v_xor_b32 v16, v4, v1\n\t"
      "v_and_b32 v16, 0xf0, v16\n\t"
      "v_xor_b32 v17, v16, v4\n\t"
      "v_xor_b32 v18, v16, v7\n\t"
      "v_add_u32 v19, v16, v1\n\t"
      "v_accvgpr_write_b32 a215, v17\n\t"
      "v_accvgpr_write_b32 a218, v18\n\t"
      "v_accvgpr_write_b32 a221, v19\n\t"
      "s_waitcnt vmcnt(6)\n\t"
      "s_bfe_u32 s30, s19, 0x30008\n\t"
      "s_bfe_u32 s31, s19, 0x20002\n\t"
      "s_lshl2_add_u32 s30, s30, s31\n\t"
      "v_readlane_b32 s90, v63, s30\n\t"
      "v_lshlrev_b32 v224, v232, v224\n\t"
      "v_lshlrev_b32 v225, v232, v225\n\t"
      "v_cndmask_b32 v228, 0, v224, vcc\n\t"
      "v_cndmask_b32 v230, 0, v225, vcc\n\t"
      "v_sub_u32 v229, v224, v228\n\t"
      "v_sub_u32 v231, v225, v230\n\t"
      "ds_write_b128 v226, v[228:231]\n\t"
      "s_add_u32 s90, s90, 0x20000\n\t"
      "s_andn2_b32 s90, s90, 0x1ffff\n\t"
      "s_or_b32 s90, s90, s91\n\t"
      "s_or_b32 s30, s17, s16\n\t"
      "s_cbranch_scc1 .Lno2_%=\n\t"
      "v_mov_b32 v234, s90\n\t"
      "v_mov_b32 v235, s19\n\t"
      "global_store_dword v235, v234, s[12:13] sc1\n\t"
      ".Lno2_%=:\n\t"
      "s_add_u32 m0, s17, 0\n\t"
      "s_mov_b32 s54, 0\n\t"
      "buffer_load_dwordx4 v9, s[84:87], s54 offen lds\n\t"
      "buffer_load_dwordx4 v10, s[84:87], s54 offen offset:1024 lds\n\t"
      "buffer_load_dwordx4 v11, s[84:87], s54 offen offset:2048 lds\n\t"
      "s_add_u32 m0, s17, 3072\n\t"
      "s_mov_b32 s54, 12288\n\t"
      "buffer_load_dwordx4 v9, s[84:87], s54 offen lds\n\t"
      "buffer_load_dwordx4 v10, s[84:87], s54 offen offset:1024 lds\n\t"
      "buffer_load_dwordx4 v11, s[84:87], s54 offen offset:2048 lds\n\t"
      "s_add_u32 m0, s17, 86528\n\t"
      "s_mov_b32 s54, 24576\n\t"
      "buffer_load_dwordx4 v9, s[84:87], s54 offen lds\n\t"
      "buffer_load_dwordx4 v10, s[84:87], s54 offen offset:1024 lds\n\t"
      "buffer_load_dwordx4 v11, s[84:87], s54 offen offset:2048 lds\n\t"
      "s_add_u32 m0, s17, 89600\n\t"
      "s_mov_b32 s54, 36864\n\t"
      "buffer_load_dwordx4 v9, s[84:87], s54 offen lds\n\t"
      "buffer_load_dwordx4 v10, s[84:87], s54 offen offset:1024 lds\n\t"
      "buffer_load_dwordx4 v11, s[84:87], s54 offen offset:2048 lds\n\t"
      "s_add_u32 m0, s17, 111104\n\t"
      "s_mov_b32 s54, 49152\n\t"
      "buffer_load_dwordx4 v9, s[84:87], s54 offen lds\n\t"
      "buffer_load_dwordx4 v10, s[84:87], s54 offen offset:1024 lds\n\t"
      "buffer_load_dwordx4 v11, s[84:87], s54 offen offset:2048 lds\n\t"
      "s_add_u32 m0, s17, 114176\n\t"
      "s_mov_b32 s54, 61440\n\t"
      "buffer_load_dwordx4 v9, s[84:87], s54 offen lds\n\t"
      "buffer_load_dwordx4 v10, s[84:87], s54 offen offset:1024 lds\n\t"
      "buffer_load_dwordx4 v11, s[84:87], s54 offen offset:2048 lds\n\t"
      "s_waitcnt lgkmcnt(0)\n\t"
      "s_waitcnt vmcnt(18)\n\t"
      "s_barrier\n\t"
      "s_waitcnt vmcnt(12)\n\t"
      "ds_read_b128 a[180:183], v14 offset:0\n\t"
      "ds_read_b64_tr_b16 v[64:65], v12 offset:0\n\t"
      "ds_read_b64_tr_b16 v[66:67], v12 offset:768\n\t"
      "ds_read_b64_tr_b16 v[68:69], v13 offset:0\n\t"
      "ds_read_b64_tr_b16 v[70:71], v13 offset:768\n\t"
      "ds_read_b64_tr_b16 v[72:73], v12 offset:64\n\t"
      "ds_read_b64_tr_b16 v[74:75], v12 offset:832\n\t"
      "ds_read_b64_tr_b16 v[76:77], v13 offset:64\n\t"
      "ds_read_b64_tr_b16 v[78:79], v13 offset:832\n\t"
      "ds_read_b64_tr_b16 a[60:61], v12 offset:128\n\t"
      "ds_read_b64_tr_b16 a[62:63], v12 offset:896\n\t"
      "ds_read_b64_tr_b16 a[64:65], v13 offset:128\n\t"
      "ds_read_b64_tr_b16 a[66:67], v13 offset:896\n\t"
      "s_add_u32 m0, s17, 135680\n\t"
      "s_mov_b32 s54, 73728\n\t"
      "buffer_load_dwordx4 v9, s[84:87], s54 offen lds\n\t"
      "buffer_load_dwordx4 v10, s[84:87], s54 offen offset:1024 lds\n\t"
      "buffer_load_dwordx4 v11, s[84:87], s54 offen offset:2048 lds\n\t"
      "s_add_u32 m0, s17, 138752\n\t"
      "s_mov_b32 s54, 86016\n\t"
      "buffer_load_dwordx4 v9, s[84:87], s54 offen lds\n\t"
      "buffer_load_dwordx4 v10, s[84:87], s54 offen offset:1024 lds\n\t"
      "buffer_load_dwordx4 v11, s[84:87], s54 offen offset:2048 lds\n\t"
      "s_waitcnt vmcnt(12)\n\t"
      "s_waitcnt lgkmcnt(0)\n\t"
      "; step\n\t"
      "v_mfma_f32_4x4x4_16b_bf16 v[40:43], a[180:181], v[64:65], 0\n\t"
      "ds_read_b128 a[184:187], v14 offset:256\n\t"
      "v_mfma_f32_4x4x4_16b_bf16 v[44:47], a[182:183], v[66:67], 0\n\t"
      "ds_read_b64_tr_b16 v[80:81], v60 offset:0\n\t"
      "v_mfma_f32_4x4x4_16b_bf16 v[48:51], a[180:181], v[68:69], 0\n\t"
      "ds_read_b64_tr_b16 v[82:83], v60 offset:768\n\t"
      "v_mfma_f32_4x4x4_16b_bf16 v[52:55], a[182:183], v[70:71], 0\n\t"
      "ds_read_b64_tr_b16 v[84:85], v61 offset:0\n\t"
      "ds_read_b64_tr_b16 v[86:87], v61 offset:768\n\t"
      "s_add_u32 m0, s17, 0\n\t"
      "s_mov_b32 s54, 98304\n\t"
      "buffer_load_dwordx4 v9, s[84:87], s54 offen lds\n\t"
      "buffer_load_dwordx4 v10, s[84:87], s54 offen offset:1024 lds\n\t"
      "v_cvt_pk_bf16_f32 v64, v40, v41\n\t"
      "v_cvt_pk_bf16_f32 v65, v42, v43\n\t"
      "v_cvt_pk_bf16_f32 v66, v44, v45\n\t"
      "v_cvt_pk_bf16_f32 v67, v46, v47\n\t"
      "v_cvt_pk_bf16_f32 v68, v48, v49\n\t"
      "v_cvt_pk_bf16_f32 v69, v50, v51\n\t"
      "v_cvt_pk_bf16_f32 v70, v52, v53\n\t"
      "v_cvt_pk_bf16_f32 v71, v54, v55\n\t"
      "v_mfma_f32_4x4x4_16b_bf16 v[40:43], a[180:181], v[72:73], 0\n\t"
      "ds_read_b64_tr_b16 v[88:89], v60 offset:64\n\t"
      "v_mfma_f32_4x4x4_16b_bf16 v[44:47], a[182:183], v[74:75], 0\n\t"
      "ds_read_b64_tr_b16 v[90:91], v60 offset:832\n\t"
      "v_mfma_f32_4x4x4_16b_bf16 v[48:51], a[180:181], v[76:77], 0\n\t"
      "ds_read_b64_tr_b16 v[92:93], v61 offset:64\n\t"
      "v_mfma_f32_4x4x4_16b_bf16 v[52:55], a[182:183], v[78:79], 0\n\t"
      "ds_read_b64_tr_b16 v[94:95], v61 offset:832\n\t"
      "buffer_load_dwordx4 v11, s[84:87], s54 offen offset:2048 lds\n\t"
      "s_add_u32 m0, s17, 3072\n\t"
      "s_mov_b32 s54, 110592\n\t"
      "buffer_load_dwordx4 v9, s[84:87], s54 offen lds\n\t"
      "v_cvt_pk_bf16_f32 v72, v40, v41\n\t"
      "v_cvt_pk_bf16_f32 v73, v42, v43\n\t"
      "v_cvt_pk_bf16_f32 v74, v44, v45\n\t"
      "v_cvt_pk_bf16_f32 v75, v46, v47\n\t"
      "v_cvt_pk_bf16_f32 v76, v48, v49\n\t"
      "v_cvt_pk_bf16_f32 v77, v50, v51\n\t"
      "v_cvt_pk_bf16_f32 v78, v52, v53\n\t"
      "v_cvt_pk_bf16_f32 v79, v54, v55\n\t"
      "ds_read_b128 a[224:227], v56 offset:25088\n\t"
      "v_mfma_f32_4x4x4_16b_bf16 v[40:43], a[180:181], a[60:61], 0\n\t"
      "ds_read_b64_tr_b16 a[68:69], v60 offset:128\n\t"
      "v_mfma_f32_4x4x4_16b_bf16 v[44:47], a[182:183], a[62:63], 0\n\t"
      "ds_read_b64_tr_b16 a[70:71], v60 offset:896\n\t"
      "v_mfma_f32_4x4x4_16b_bf16 v[48:51], a[180:181], a[64:65], 0\n\t"
      "ds_read_b64_tr_b16 a[72:73], v61 offset:128\n\t"
      "v_mfma_f32_4x4x4_16b_bf16 v[52:55], a[182:183], a[66:67], 0\n\t"
      "ds_read_b64_tr_b16 a[74:75], v61 offset:896\n\t"
      "buffer_load_dwordx4 v10, s[84:87], s54 offen offset:1024 lds\n\t"
      "buffer_load_dwordx4 v11, s[84:87], s54 offen offset:2048 lds\n\t"
      "ds_read_b128 a[228:231], v56 offset:37376\n\t"
      "v_cvt_pk_bf16_f32 v40, v40, v41\n\t"
      "v_cvt_pk_bf16_f32 v41, v42, v43\n\t"
      "v_cvt_pk_bf16_f32 v44, v44, v45\n\t"
      "v_cvt_pk_bf16_f32 v45, v46, v47\n\t"
      "v_cvt_pk_bf16_f32 v48, v48, v49\n\t"
      "v_cvt_pk_bf16_f32 v49, v50, v51\n\t"
      "v_cvt_pk_bf16_f32 v52, v52, v53\n\t"
      "v_cvt_pk_bf16_f32 v53, v54, v55\n\t"
      "v_accvgpr_write_b32 a60, v40\n\t"
      "v_accvgpr_write_b32 a61, v41\n\t"
      "v_accvgpr_write_b32 a62, v44\n\t"
      "v_accvgpr_write_b32 a63, v45\n\t"
      "v_accvgpr_write_b32 a64, v48\n\t"
      "v_accvgpr_write_b32 a65, v49\n\t"
      "v_accvgpr_write_b32 a66, v52\n\t"
      "v_accvgpr_write_b32 a67, v53\n\t"
      "s_waitcnt vmcnt(12)\n\t"
      "s_waitcnt lgkmcnt(1)\n\t"
      "; step\n\t"
      "v_mfma_f32_4x4x4_16b_bf16 v[40:43], a[184:185], v[80:81], 0\n\t"
      "ds_read_b128 a[180:183], v14 offset:512\n\t"
      "v_mfma_f32_4x4x4_16b_bf16 v[44:47], a[186:187], v[82:83], 0\n\t"
      "ds_read_b64_tr_b16 v[96:97], v60 offset:24576\n\t"
      "v_mfma_f32_4x4x4_16b_bf16 v[48:51], a[184:185], v[84:85], 0\n\t"
      "ds_read_b64_tr_b16 v[98:99], v60 offset:25344\n\t"
      "v_mfma_f32_4x4x4_16b_bf16 v[52:55], a[186:187], v[86:87], 0\n\t"
      "ds_read_b64_tr_b16 v[100:101], v61 offset:24576\n\t"
      "ds_read_b64_tr_b16 v[102:103], v61 offset:25344\n\t"
      "s_add_u32 m0, s17, 86528\n\t"
      "s_mov_b32 s54, 122880\n\t"
      "buffer_load_dwordx4 v9, s[84:87], s54 offen lds\n\t"
      "buffer_load_dwordx4 v10, s[84:87], s54 offen offset:1024 lds\n\t"
      "v_mfma_f32_16x16x32_bf16 v[16:19], v[64:67], a[224:227], 0\n\t"
      "v_cvt_pk_bf16_f32 v80, v40, v41\n\t"
      "v_cvt_pk_bf16_f32 v81, v42, v43\n\t"
      "v_mfma_f32_16x16x32_bf16 v[20:23], v[68:71], a[224:227], 0\n\t"
      "v_cvt_pk_bf16_f32 v82, v44, v45\n\t"
      "v_cvt_pk_bf16_f32 v83, v46, v47\n\t"
      "v_mfma_f32_16x16x32_bf16 v[24:27], v[72:75], a[224:227], 0\n\t"
      "v_cvt_pk_bf16_f32 v84, v48, v49\n\t"
      "v_cvt_pk_bf16_f32 v85, v50, v51\n\t"
      "v_mfma_f32_16x16x32_bf16 v[28:31], v[76:79], a[224:227], 0\n\t"
      "v_cvt_pk_bf16_f32 v86, v52, v53\n\t"
      "v_cvt_pk_bf16_f32 v87, v54, v55\n\t"
      "v_mfma_f32_4x4x4_16b_bf16 v[40:43], a[184:185], v[88:89], 0\n\t"
      "ds_read_b64_tr_b16 v[104:105], v60 offset:24640\n\t"
      "v_mfma_f32_4x4x4_16b_bf16 v[44:47], a[186:187], v[90:91], 0\n\t"
      "ds_read_b64_tr_b16 v[106:107], v60 offset:25408\n\t"
      "v_mfma_f32_4x4x4_16b_bf16 v[48:51], a[184:185], v[92:93], 0\n\t"
      "ds_read_b64_tr_b16 v[108:109], v61 offset:24640\n\t"
      "v_mfma_f32_4x4x4_16b_bf16 v[52:55], a[186:187], v[94:95], 0\n\t"
      "ds_read_b64_tr_b16 v[110:111], v61 offset:25408\n\t"
      "buffer_load_dwordx4 v11, s[84:87], s54 offen offset:2048 lds\n\t"
      "s_add_u32 m0, s17, 89600\n\t"
      "s_mov_b32 s54, 135168\n\t"
      "buffer_load_dwordx4 v9, s[84:87], s54 offen lds\n\t"
      "v_mfma_f32_16x16x32_bf16 v[32:35], a[60:63], a[224:227], 0\n\t"
      "v_cvt_pk_bf16_f32 v88, v40, v41\n\t"
      "v_cvt_pk_bf16_f32 v89, v42, v43\n\t"
      "v_mfma_f32_16x16x32_bf16 v[36:39], a[64:67], a[224:227], 0\n\t"
      "v_cvt_pk_bf16_f32 v90, v44, v45\n\t"
      "v_cvt_pk_bf16_f32 v91, v46, v47\n\t"
      "s_waitcnt lgkmcnt(9)\n\t"
      "v_mfma_f32_16x16x32_bf16 a[156:159], v[64:67], a[228:231], 0\n\t"
      "v_cvt_pk_bf16_f32 v92, v48, v49\n\t"
      "v_cvt_pk_bf16_f32 v93, v50, v51\n\t"
      "v_mfma_f32_16x16x32_bf16 a[160:163], v[68:71], a[228:231], 0\n\t"
      "v_cvt_pk_bf16_f32 v94, v52, v53\n\t"
      "v_cvt_pk_bf16_f32 v95, v54, v55\n\t"
      "ds_read_b128 a[232:235], v57 offset:25088\n\t"
      "v_mfma_f32_4x4x4_16b_bf16 v[40:43], a[184:185], a[68:69], 0\n\t"
      "ds_read_b64_tr_b16 a[76:77], v60 offset:24704\n\t"
      "v_mfma_f32_4x4x4_16b_bf16 v[44:47], a[186:187], a[70:71], 0\n\t"
      "ds_read_b64_tr_b16 a[78:79], v60 offset:25472\n\t"
      "v_mfma_f32_4x4x4_16b_bf16 v[48:51], a[184:185], a[72:73], 0\n\t"
      "ds_read_b64_tr_b16 a[80:81], v61 offset:24704\n\t"
      "v_mfma_f32_4x4x4_16b_bf16 v[52:55], a[186:187], a[74:75], 0\n\t"
      "ds_read_b64_tr_b16 a[82:83], v61 offset:25472\n\t"
      "buffer_load_dwordx4 v10, s[84:87], s54 offen offset:1024 lds\n\t"
      "buffer_load_dwordx4 v11, s[84:87], s54 offen offset:2048 lds\n\t"
      "ds_read_b128 a[236:239], v57 offset:37376\n\t"
      "v_mfma_f32_16x16x32_bf16 a[164:167], v[72:75], a[228:231], 0\n\t"
      "v_cvt_pk_bf16_f32 v40, v40, v41\n\t"
      "v_cvt_pk_bf16_f32 v41, v42, v43\n\t"
      "v_mfma_f32_16x16x32_bf16 a[168:171], v[76:79], a[228:231], 0\n\t"
      "v_cvt_pk_bf16_f32 v44, v44, v45\n\t"
      "v_cvt_pk_bf16_f32 v45, v46, v47\n\t"
      "v_mfma_f32_16x16x32_bf16 a[172:175], a[60:63], a[228:231], 0\n\t"
      "v_cvt_pk_bf16_f32 v48, v48, v49\n\t"
      "v_cvt_pk_bf16_f32 v49, v50, v51\n\t"
      "v_mfma_f32_16x16x32_bf16 a[176:179], a[64:67], a[228:231], 0\n\t"
      "v_cvt_pk_bf16_f32 v52, v52, v53\n\t"
      "v_cvt_pk_bf16_f32 v53, v54, v55\n\t"
      "v_accvgpr_write_b32 a68, v40\n\t"
      "v_accvgpr_write_b32 a69, v41\n\t"
      "v_accvgpr_write_b32 a70, v44\n\t"
      "v_accvgpr_write_b32 a71, v45\n\t"
      "v_accvgpr_write_b32 a72, v48\n\t"
      "v_accvgpr_write_b32 a73, v49\n\t"
      "v_accvgpr_write_b32 a74, v52\n\t"
      "v_accvgpr_write_b32 a75, v53\n\t"
      "s_waitcnt vmcnt(12)\n\t"
      "s_waitcnt lgkmcnt(1)\n\t"
      "; step\n\t"
      "v_mfma_f32_4x4x4_16b_bf16 v[40:43], a[180:181], v[96:97], 0\n\t"
      "ds_read_b128 a[184:187], v14 offset:768\n\t"
      "v_mfma_f32_4x4x4_16b_bf16 v[44:47], a[182:183], v[98:99], 0\n\t"
      "ds_read_b64_tr_b16 v[112:113], v60 offset:49152\n\t"
      "v_mfma_f32_4x4x4_16b_bf16 v[48:51], a[180:181], v[100:101], 0\n\t"
      "ds_read_b64_tr_b16 v[114:115], v60 offset:49920\n\t"
      "v_mfma_f32_4x4x4_16b_bf16 v[52:55], a[182:183], v[102:103], 0\n\t"
      "ds_read_b64_tr_b16 v[116:117], v61 offset:49152\n\t"
      "ds_read_b64_tr_b16 v[118:119], v61 offset:49920\n\t"
      "s_add_u32 m0, s17, 111104\n\t"
      "s_mov_b32 s54, 147456\n\t"
      "buffer_load_dwordx4 v9, s[84:87], s54 offen lds\n\t"
      "buffer_load_dwordx4 v10, s[84:87], s54 offen offset:1024 lds\n\t"
      "v_mfma_f32_16x16x32_bf16 v[16:19], v[80:83], a[232:235], v[16:19]\n\t"
      "v_cvt_pk_bf16_f32 v96, v40, v41\n\t"
      "v_cvt_pk_bf16_f32 v97, v42, v43\n\t"
      "v_mfma_f32_16x16x32_bf16 v[20:23], v[84:87], a[232:235], v[20:23]\n\t"
      "v_cvt_pk_bf16_f32 v98, v44, v45\n\t"
      "v_cvt_pk_bf16_f32 v99, v46, v47\n\t"
      "v_mfma_f32_16x16x32_bf16 v[24:27], v[88:91], a[232:235], v[24:27]\n\t"
      "v_cvt_pk_bf16_f32 v100, v48, v49\n\t"
      "v_cvt_pk_bf16_f32 v101, v50, v51\n\t"
      "v_mfma_f32_16x16x32_bf16 v[28:31], v[92:95], a[232:235], v[28:31]\n\t"
      "v_cvt_pk_bf16_f32 v102, v52, v53\n\t"
      "v_cvt_pk_bf16_f32 v103, v54, v55\n\t"
      "v_mfma_f32_4x4x4_16b_bf16 v[40:43], a[180:181], v[104:105], 0\n\t"
      "ds_read_b64_tr_b16 v[120:121], v60 offset:49216\n\t"
      "v_mfma_f32_4x4x4_16b_bf16 v[44:47], a[182:183], v[106:107], 0\n\t"
      "ds_read_b64_tr_b16 v[122:123], v60 offset:49984\n\t"
      "v_mfma_f32_4x4x4_16b_bf16 v[48:51], a[180:181], v[108:109], 0\n\t"
      "ds_read_b64_tr_b16 v[124:125], v61 offset:49216\n\t"
      "v_mfma_f32_4x4x4_16b_bf16 v[52:55], a[182:183], v[110:111], 0\n\t"
      "ds_read_b64_tr_b16 v[126:127], v61 offset:49984\n\t"
      "buffer_load_dwordx4 v11, s[84:87], s54 offen offset:2048 lds\n\t"
      "s_add_u32 m0, s17, 114176\n\t"
      "s_mov_b32 s54, 159744\n\t"
      "buffer_load_dwordx4 v9, s[84:87], s54 offen lds\n\t"
      "v_mfma_f32_16x16x32_bf16 v[32:35], a[68:71], a[232:235], v[32:35]\n\t"
      "v_cvt_pk_bf16_f32 v104, v40, v41\n\t"
      "v_cvt_pk_bf16_f32 v105, v42, v43\n\t"
      "v_mfma_f32_16x16x32_bf16 v[36:39], a[72:75], a[232:235], v[36:39]\n\t"
      "v_cvt_pk_bf16_f32 v106, v44, v45\n\t"
      "v_cvt_pk_bf16_f32 v107, v46, v47\n\t"
      "s_waitcnt lgkmcnt(9)\n\t"
      "v_mfma_f32_16x16x32_bf16 a[156:159], v[80:83], a[236:239], a[156:159]\n\t"
      "v_cvt_pk_bf16_f32 v108, v48, v49\n\t"
      "v_cvt_pk_bf16_f32 v109, v50, v51\n\t"
      "v_mfma_f32_16x16x32_bf16 a[160:163], v[84:87], a[236:239], a[160:163]\n\t"
      "v_cvt_pk_bf16_f32 v110, v52, v53\n\t"
      "v_cvt_pk_bf16_f32 v111, v54, v55\n\t"
      "ds_read_b128 a[240:243], v58 offset:25088\n\t"
      "v_mfma_f32_4x4x4_16b_bf16 v[40:43], a[180:181], a[76:77], 0\n\t"
      "ds_read_b64_tr_b16 a[84:85], v60 offset:49280\n\t"
      "v_mfma_f32_4x4x4_16b_bf16 v[44:47], a[182:183], a[78:79], 0\n\t"
      "ds_read_b64_tr_b16 a[86:87], v60 offset:50048\n\t"
      "v_mfma_f32_4x4x4_16b_bf16 v[48:51], a[180:181], a[80:81], 0\n\t"
      "ds_read_b64_tr_b16 a[88:89], v61 offset:49280\n\t"
      "v_mfma_f32_4x4x4_16b_bf16 v[52:55], a[182:183], a[82:83], 0\n\t"
      "ds_read_b64_tr_b16 a[90:91], v61 offset:50048\n\t"
      "buffer_load_dwordx4 v10, s[84:87], s54 offen offset:1024 lds\n\t"
      "buffer_load_dwordx4 v11, s[84:87], s54 offen offset:2048 lds\n\t"
      "ds_read_b128 a[244:247], v58 offset:37376\n\t"
      "v_mfma_f32_16x16x32_bf16 a[164:167], v[88:91], a[236:239], a[164:167]\n\t"
      "v_cvt_pk_bf16_f32 v40, v40, v41\n\t"
      "v_cvt_pk_bf16_f32 v41, v42, v43\n\t"
      "v_mfma_f32_16x16x32_bf16 a[168:171], v[92:95], a[236:239], a[168:171]\n\t"
      "v_cvt_pk_bf16_f32 v44, v44, v45\n\t"
      "v_cvt_pk_bf16_f32 v45, v46, v47\n\t"
      "v_mfma_f32_16x16x32_bf16 a[172:175], a[68:71], a[236:239], a[172:175]\n\t"
      "v_cvt_pk_bf16_f32 v48, v48, v49\n\t"
      "v_cvt_pk_bf16_f32 v49, v50, v51\n\t"
      "v_mfma_f32_16x16x32_bf16 a[176:179], a[72:75], a[236:239], a[176:179]\n\t"
      "v_cvt_pk_bf16_f32 v52, v52, v53\n\t"
      "v_cvt_pk_bf16_f32 v53, v54, v55\n\t"
      "v_accvgpr_write_b32 a76, v40\n\t"
      "v_accvgpr_write_b32 a77, v41\n\t"
      "v_accvgpr_write_b32 a78, v44\n\t"
      "v_accvgpr_write_b32 a79, v45\n\t"
      "v_accvgpr_write_b32 a80, v48\n\t"
      "v_accvgpr_write_b32 a81, v49\n\t"
      "v_accvgpr_write_b32 a82, v52\n\t"
      "v_accvgpr_write_b32 a83, v53\n\t"
      "s_waitcnt vmcnt(12)\n\t"
      "s_waitcnt lgkmcnt(1)\n\t"
      "; step\n\t"
      "v_mfma_f32_4x4x4_16b_bf16 v[40:43], a[184:185], v[112:113], 0\n\t"
      "ds_read_b128 a[180:183], v14 offset:1024\n\t"
      "v_mfma_f32_4x4x4_16b_bf16 v[44:47], a[186:187], v[114:115], 0\n\t"
      "ds_read_b64_tr_b16 v[128:129], v12 offset:0\n\t"
      "v_mfma_f32_4x4x4_16b_bf16 v[48:51], a[184:185], v[116:117], 0\n\t"
      "ds_read_b64_tr_b16 v[130:131], v12 offset:768\n\t"
      "v_mfma_f32_4x4x4_16b_bf16 v[52:55], a[186:187], v[118:119], 0\n\t"
      "ds_read_b64_tr_b16 v[132:133], v13 offset:0\n\t"
      "ds_read_b64_tr_b16 v[134:135], v13 offset:768\n\t"
      "s_add_u32 m0, s17, 135680\n\t"
      "s_mov_b32 s54, 172032\n\t"
      "buffer_load_dwordx4 v9, s[84:87], s54 offen lds\n\t"
      "buffer_load_dwordx4 v10, s[84:87], s54 offen offset:1024 lds\n\t"
      "v_mfma_f32_16x16x32_bf16 v[16:19], v[96:99], a[240:243], v[16:19]\n\t"
      "v_cvt_pk_bf16_f32 v112, v40, v41\n\t"
      "v_cvt_pk_bf16_f32 v113, v42, v43\n\t"
      "v_mfma_f32_16x16x32_bf16 v[20:23], v[100:103], a[240:243], v[20:23]\n\t"
      "v_cvt_pk_bf16_f32 v114, v44, v45\n\t"
      "v_cvt_pk_bf16_f32 v115, v46, v47\n\t"
      "v_mfma_f32_16x16x32_bf16 v[24:27], v[104:107], a[240:243], v[24:27]\n\t"
      "v_cvt_pk_bf16_f32 v116, v48, v49\n\t"
      "v_cvt_pk_bf16_f32 v117, v50, v51\n\t"
      "v_mfma_f32_16x16x32_bf16 v[28:31], v[108:111], a[240:243], v[28:31]\n\t"
      "v_cvt_pk_bf16_f32 v118, v52, v53\n\t"
      "v_cvt_pk_bf16_f32 v119, v54, v55\n\t"
      "v_mfma_f32_4x4x4_16b_bf16 v[40:43], a[184:185], v[120:121], 0\n\t"
      "ds_read_b64_tr_b16 v[136:137], v12 offset:64\n\t"
      "v_mfma_f32_4x4x4_16b_bf16 v[44:47], a[186:187], v[122:123], 0\n\t"
      "ds_read_b64_tr_b16 v[138:139], v12 offset:832\n\t"
      "v_mfma_f32_4x4x4_16b_bf16 v[48:51], a[184:185], v[124:125], 0\n\t"
      "ds_read_b64_tr_b16 v[140:141], v13 offset:64\n\t"
      "v_mfma_f32_4x4x4_16b_bf16 v[52:55], a[186:187], v[126:127], 0\n\t"
      "ds_read_b64_tr_b16 v[142:143], v13 offset:832\n\t"
      "buffer_load_dwordx4 v11, s[84:87], s54 offen offset:2048 lds\n\t"
      "s_add_u32 m0, s17, 138752\n\t"
      "s_mov_b32 s54, 184320\n\t"
      "buffer_load_dwordx4 v9, s[84:87], s54 offen lds\n\t"
      "v_mfma_f32_16x16x32_bf16 v[32:35], a[76:79], a[240:243], v[32:35]\n\t"
      "v_cvt_pk_bf16_f32 v120, v40, v41\n\t"
      "v_cvt_pk_bf16_f32 v121, v42, v43\n\t"
      "v_mfma_f32_16x16x32_bf16 v[36:39], a[80:83], a[240:243], v[36:39]\n\t"
      "v_cvt_pk_bf16_f32 v122, v44, v45\n\t"
      "v_cvt_pk_bf16_f32 v123, v46, v47\n\t"
      "s_waitcnt lgkmcnt(9)\n\t"
      "v_mfma_f32_16x16x32_bf16 a[156:159], v[96:99], a[244:247], a[156:159]\n\t"
      "v_cvt_pk_bf16_f32 v124, v48, v49\n\t"
      "v_cvt_pk_bf16_f32 v125, v50, v51\n\t"
      "v_mfma_f32_16x16x32_bf16 a[160:163], v[100:103], a[244:247], a[160:163]\n\t"
      "v_cvt_pk_bf16_f32 v126, v52, v53\n\t"
      "v_cvt_pk_bf16_f32 v127, v54, v55\n\t"
      "ds_read_b128 a[224:227], v59 offset:25088\n\t"
      "v_mfma_f32_4x4x4_16b_bf16 v[40:43], a[184:185], a[84:85], 0\n\t"
      "ds_read_b64_tr_b16 a[92:93], v12 offset:128\n\t"
      "v_mfma_f32_4x4x4_16b_bf16 v[44:47], a[186:187], a[86:87], 0\n\t"
      "ds_read_b64_tr_b16 a[94:95], v12 offset:896\n\t"
      "v_mfma_f32_4x4x4_16b_bf16 v[48:51], a[184:185], a[88:89], 0\n\t"
      "ds_read_b64_tr_b16 a[96:97], v13 offset:128\n\t"
      "v_mfma_f32_4x4x4_16b_bf16 v[52:55], a[186:187], a[90:91], 0\n\t"
      "ds_read_b64_tr_b16 a[98:99], v13 offset:896\n\t"
      "buffer_load_dwordx4 v10, s[84:87], s54 offen offset:1024 lds\n\t"
      "buffer_load_dwordx4 v11, s[84:87], s54 offen offset:2048 lds\n\t"
      "ds_read_b128 a[228:231], v59 offset:37376\n\t"
      "v_mfma_f32_16x16x32_bf16 a[164:167], v[104:107], a[244:247], a[164:167]\n\t"
      "v_cvt_pk_bf16_f32 v40, v40, v41\n\t"
      "v_cvt_pk_bf16_f32 v41, v42, v43\n\t"
      "v_mfma_f32_16x16x32_bf16 a[168:171], v[108:111], a[244:247], a[168:171]\n\t"
      "v_cvt_pk_bf16_f32 v44, v44, v45\n\t"
      "v_cvt_pk_bf16_f32 v45, v46, v47\n\t"
      "v_mfma_f32_16x16x32_bf16 a[172:175], a[76:79], a[244:247], a[172:175]\n\t"
      "v_cvt_pk_bf16_f32 v48, v48, v49\n\t"
      "v_cvt_pk_bf16_f32 v49, v50, v51\n\t"
      "v_mfma_f32_16x16x32_bf16 a[176:179], a[80:83], a[244:247], a[176:179]\n\t"
      "v_cvt_pk_bf16_f32 v52, v52, v53\n\t"
      "v_cvt_pk_bf16_f32 v53, v54, v55\n\t"
      "v_accvgpr_write_b32 a84, v40\n\t"
      "v_accvgpr_write_b32 a85, v41\n\t"
      "v_accvgpr_write_b32 a86, v44\n\t"
      "v_accvgpr_write_b32 a87, v45\n\t"
      "v_accvgpr_write_b32 a88, v48\n\t"
      "v_accvgpr_write_b32 a89, v49\n\t"
      "v_accvgpr_write_b32 a90, v52\n\t"
      "v_accvgpr_write_b32 a91, v53\n\t"
      "s_waitcnt vmcnt(12)\n\t"
      "s_waitcnt lgkmcnt(1)\n\t"
      "; step\n\t"
      "v_mfma_f32_4x4x4_16b_bf16 v[40:43], a[180:181], v[128:129], 0\n\t"
      "ds_read_b128 a[184:187], v14 offset:1280\n\t"
      "v_mfma_f32_4x4x4_16b_bf16 v[44:47], a[182:183], v[130:131], 0\n\t"
      "ds_read_b64_tr_b16 v[144:145], v60 offset:0\n\t"
      "v_mfma_f32_4x4x4_16b_bf16 v[48:51], a[180:181], v[132:133], 0\n\t"
      "ds_read_b64_tr_b16 v[146:147], v60 offset:768\n\t"
      "v_mfma_f32_4x4x4_16b_bf16 v[52:55], a[182:183], v[134:135], 0\n\t"
      "ds_read_b64_tr_b16 v[148:149], v61 offset:0\n\t"
      "ds_read_b64_tr_b16 v[150:151], v61 offset:768\n\t"
      "s_add_u32 m0, s17, 0\n\t"
      "s_mov_b32 s54, 196608\n\t"
      "buffer_load_dwordx4 v9, s[84:87], s54 offen lds\n\t"
      "buffer_load_dwordx4 v10, s[84:87], s54 offen offset:1024 lds\n\t"
      "v_mfma_f32_16x16x32_bf16 v[16:19], v[112:115], a[224:227], v[16:19]\n\t"
      "v_cvt_pk_bf16_f32 v128, v40, v41\n\t"
      "v_cvt_pk_bf16_f32 v129, v42, v43\n\t"
      "v_mfma_f32_16x16x32_bf16 v[20:23], v[116:119], a[224:227], v[20:23]\n\t"
      "v_cvt_pk_bf16_f32 v130, v44, v45\n\t"
      "v_cvt_pk_bf16_f32 v131, v46, v47\n\t"
      "v_mfma_f32_16x16x32_bf16 v[24:27], v[120:123], a[224:227], v[24:27]\n\t"
      "v_cvt_pk_bf16_f32 v132, v48, v49\n\t"
      "v_cvt_pk_bf16_f32 v133, v50, v51\n\t"
      "v_mfma_f32_16x16x32_bf16 v[28:31], v[124:127], a[224:227], v[28:31]\n\t"
      "v_cvt_pk_bf16_f32 v134, v52, v53\n\t"
      "v_cvt_pk_bf16_f32 v135, v54, v55\n\t"
      "v_mfma_f32_4x4x4_16b_bf16 v[40:43], a[180:181], v[136:137], 0\n\t"
      "ds_read_b64_tr_b16 v[152:153], v60 offset:64\n\t"
      "v_mfma_f32_4x4x4_16b_bf16 v[44:47], a[182:183], v[138:139], 0\n\t"
      "ds_read_b64_tr_b16 v[154:155], v60 offset:832\n\t"
      "v_mfma_f32_4x4x4_16b_bf16 v[48:51], a[180:181], v[140:141], 0\n\t"
      "ds_read_b64_tr_b16 v[156:157], v61 offset:64\n\t"
      "v_mfma_f32_4x4x4_16b_bf16 v[52:55], a[182:183], v[142:143], 0\n\t"
      "ds_read_b64_tr_b16 v[158:159], v61 offset:832\n\t"
      "buffer_load_dwordx4 v11, s[84:87], s54 offen offset:2048 lds\n\t"
      "s_add_u32 m0, s17, 3072\n\t"
      "s_mov_b32 s54, 208896\n\t"
      "buffer_load_dwordx4 v9, s[84:87], s54 offen lds\n\t"
      "v_mfma_f32_16x16x32_bf16 v[32:35], a[84:87], a[224:227], v[32:35]\n\t"
      "v_cvt_pk_bf16_f32 v136, v40, v41\n\t"
      "v_cvt_pk_bf16_f32 v137, v42, v43\n\t"
      "v_mfma_f32_16x16x32_bf16 v[36:39], a[88:91], a[224:227], v[36:39]\n\t"
      "v_cvt_pk_bf16_f32 v138, v44, v45\n\t"
      "v_cvt_pk_bf16_f32 v139, v46, v47\n\t"
      "s_waitcnt lgkmcnt(9)\n\t"
      "v_mfma_f32_16x16x32_bf16 a[156:159], v[112:115], a[228:231], a[156:159]\n\t"
      "v_cvt_pk_bf16_f32 v140, v48, v49\n\t"
      "v_cvt_pk_bf16_f32 v141, v50, v51\n\t"
      "v_mfma_f32_16x16x32_bf16 a[160:163], v[116:119], a[228:231], a[160:163]\n\t"
      "v_cvt_pk_bf16_f32 v142, v52, v53\n\t"
      "v_cvt_pk_bf16_f32 v143, v54, v55\n\t"
      "ds_read_b128 a[232:235], v56 offset:25344\n\t"
      "v_mfma_f32_4x4x4_16b_bf16 v[40:43], a[180:181], a[92:93], 0\n\t"
      "ds_read_b64_tr_b16 a[100:101], v60 offset:128\n\t"
      "v_mfma_f32_4x4x4_16b_bf16 v[44:47], a[182:183], a[94:95], 0\n\t"
      "ds_read_b64_tr_b16 a[102:103], v60 offset:896\n\t"
      "v_mfma_f32_4x4x4_16b_bf16 v[48:51], a[180:181], a[96:97], 0\n\t"
      "ds_read_b64_tr_b16 a[104:105], v61 offset:128\n\t"
      "v_mfma_f32_4x4x4_16b_bf16 v[52:55], a[182:183], a[98:99], 0\n\t"
      "ds_read_b64_tr_b16 a[106:107], v61 offset:896\n\t"
      "buffer_load_dwordx4 v10, s[84:87], s54 offen offset:1024 lds\n\t"
      "buffer_load_dwordx4 v11, s[84:87], s54 offen offset:2048 lds\n\t"
      "ds_read_b128 a[236:239], v56 offset:37632\n\t"
      "v_mfma_f32_16x16x32_bf16 a[164:167], v[120:123], a[228:231], a[164:167]\n\t"
      "v_cvt_pk_bf16_f32 v40, v40, v41\n\t"
      "v_cvt_pk_bf16_f32 v41, v42, v43\n\t"
      "v_mfma_f32_16x16x32_bf16 a[168:171], v[124:127], a[228:231], a[168:171]\n\t"
      "v_cvt_pk_bf16_f32 v44, v44, v45\n\t"
      "v_cvt_pk_bf16_f32 v45, v46, v47\n\t"
      "v_mfma_f32_16x16x32_bf16 a[172:175], a[84:87], a[228:231], a[172:175]\n\t"
      "v_cvt_pk_bf16_f32 v48, v48, v49\n\t"
      "v_cvt_pk_bf16_f32 v49, v50, v51\n\t"
      "v_mfma_f32_16x16x32_bf16 a[176:179], a[88:91], a[228:231], a[176:179]\n\t"
      "v_cvt_pk_bf16_f32 v52, v52, v53\n\t"
      "v_cvt_pk_bf16_f32 v53, v54, v55\n\t"
      "v_accvgpr_write_b32 a92, v40\n\t"
      "v_accvgpr_write_b32 a93, v41\n\t"
      "v_accvgpr_write_b32 a94, v44\n\t"
      "v_accvgpr_write_b32 a95, v45\n\t"
      "v_accvgpr_write_b32 a96, v48\n\t"
      "v_accvgpr_write_b32 a97, v49\n\t"
      "v_accvgpr_write_b32 a98, v52\n\t"
      "v_accvgpr_write_b32 a99, v53\n\t"
      "s_waitcnt vmcnt(12)\n\t"
      "s_waitcnt lgkmcnt(1)\n\t"
      "; step\n\t"
      "v_mfma_f32_4x4x4_16b_bf16 v[40:43], a[184:185], v[144:145], 0\n\t"
      "ds_read_b128 a[180:183], v14 offset:1536\n\t"
      "v_mfma_f32_4x4x4_16b_bf16 v[44:47], a[186:187], v[146:147], 0\n\t"
      "ds_read_b64_tr_b16 v[160:161], v60 offset:24576\n\t"
      "v_mfma_f32_4x4x4_16b_bf16 v[48:51], a[184:185], v[148:149], 0\n\t"
      "ds_read_b64_tr_b16 v[162:163], v60 offset:25344\n\t"
      "v_mfma_f32_4x4x4_16b_bf16 v[52:55], a[186:187], v[150:151], 0\n\t"
      "ds_read_b64_tr_b16 v[164:165], v61 offset:24576\n\t"
      "ds_read_b64_tr_b16 v[166:167], v61 offset:25344\n\t"
      "s_add_u32 m0, s17, 86528\n\t"
      "s_mov_b32 s54, 221184\n\t"
      "buffer_load_dwordx4 v9, s[84:87], s54 offen lds\n\t"
      "buffer_load_dwordx4 v10, s[84:87], s54 offen offset:1024 lds\n\t"
      "v_mfma_f32_16x16x32_bf16 v[16:19], v[128:131], a[232:235], v[16:19]\n\t"
      "v_cvt_pk_bf16_f32 v144, v40, v41\n\t"
      "v_cvt_pk_bf16_f32 v145, v42, v43\n\t"
      "v_mfma_f32_16x16x32_bf16 v[20:23], v[132:135], a[232:235], v[20:23]\n\t"
      "v_cvt_pk_bf16_f32 v146, v44, v45\n\t"
      "v_cvt_pk_bf16_f32 v147, v46, v47\n\t"
      "v_mfma_f32_16x16x32_bf16 v[24:27], v[136:139], a[232:235], v[24:27]\n\t"
      "v_cvt_pk_bf16_f32 v148, v48, v49\n\t"
      "v_cvt_pk_bf16_f32 v149, v50, v51\n\t"
      "v_mfma_f32_16x16x32_bf16 v[28:31], v[140:143], a[232:235], v[28:31]\n\t"
      "v_cvt_pk_bf16_f32 v150, v52, v53\n\t"
      "v_cvt_pk_bf16_f32 v151, v54, v55\n\t"
      "v_mfma_f32_4x4x4_16b_bf16 v[40:43], a[184:185], v[152:153], 0\n\t"
      "ds_read_b64_tr_b16 v[168:169], v60 offset:24640\n\t"
      "v_mfma_f32_4x4x4_16b_bf16 v[44:47], a[186:187], v[154:155], 0\n\t"
      "ds_read_b64_tr_b16 v[170:171], v60 offset:25408\n\t"
      "v_mfma_f32_4x4x4_16b_bf16 v[48:51], a[184:185], v[156:157], 0\n\t"
      "ds_read_b64_tr_b16 v[172:173], v61 offset:24640\n\t"
      "v_mfma_f32_4x4x4_16b_bf16 v[52:55], a[186:187], v[158:159], 0\n\t"
      "ds_read_b64_tr_b16 v[174:175], v61 offset:25408\n\t"
      "buffer_load_dwordx4 v11, s[84:87], s54 offen offset:2048 lds\n\t"
      "s_add_u32 m0, s17, 89600\n\t"
      "s_mov_b32 s54, 233472\n\t"
      "buffer_load_dwordx4 v9, s[84:87], s54 offen lds\n\t"
      "v_mfma_f32_16x16x32_bf16 v[32:35], a[92:95], a[232:235], v[32:35]\n\t"
      "v_cvt_pk_bf16_f32 v152, v40, v41\n\t"
      "v_cvt_pk_bf16_f32 v153, v42, v43\n\t"
      "v_mfma_f32_16x16x32_bf16 v[36:39], a[96:99], a[232:235], v[36:39]\n\t"
      "v_cvt_pk_bf16_f32 v154, v44, v45\n\t"
      "v_cvt_pk_bf16_f32 v155, v46, v47\n\t"
      "s_waitcnt lgkmcnt(9)\n\t"
      "v_mfma_f32_16x16x32_bf16 a[156:159], v[128:131], a[236:239], a[156:159]\n\t"
      "v_cvt_pk_bf16_f32 v156, v48, v49\n\t"
      "v_cvt_pk_bf16_f32 v157, v50, v51\n\t"
      "v_mfma_f32_16x16x32_bf16 a[160:163], v[132:135], a[236:239], a[160:163]\n\t"
      "v_cvt_pk_bf16_f32 v158, v52, v53\n\t"
      "v_cvt_pk_bf16_f32 v159, v54, v55\n\t"
      "ds_read_b128 a[240:243], v57 offset:25344\n\t"
      "v_mfma_f32_4x4x4_16b_bf16 v[40:43], a[184:185], a[100:101], 0\n\t"
      "ds_read_b64_tr_b16 a[108:109], v60 offset:24704\n\t"
      "v_mfma_f32_4x4x4_16b_bf16 v[44:47], a[186:187], a[102:103], 0\n\t"
      "ds_read_b64_tr_b16 a[110:111], v60 offset:25472\n\t"
      "v_mfma_f32_4x4x4_16b_bf16 v[48:51], a[184:185], a[104:105], 0\n\t"
      "ds_read_b64_tr_b16 a[112:113], v61 offset:24704\n\t"
      "v_mfma_f32_4x4x4_16b_bf16 v[52:55], a[186:187], a[106:107], 0\n\t"
      "ds_read_b64_tr_b16 a[114:115], v61 offset:25472\n\t"
      "buffer_load_dwordx4 v10, s[84:87], s54 offen offset:1024 lds\n\t"
      "buffer_load_dwordx4 v11, s[84:87], s54 offen offset:2048 lds\n\t"
      "ds_read_b128 a[244:247], v57 offset:37632\n\t"
      "v_mfma_f32_16x16x32_bf16 a[164:167], v[136:139], a[236:239], a[164:167]\n\t"
      "v_cvt_pk_bf16_f32 v40, v40, v41\n\t"
      "v_cvt_pk_bf16_f32 v41, v42, v43\n\t"
      "v_mfma_f32_16x16x32_bf16 a[168:171], v[140:143], a[236:239], a[168:171]\n\t"
      "v_cvt_pk_bf16_f32 v44, v44, v45\n\t"
      "v_cvt_pk_bf16_f32 v45, v46, v47\n\t"
      "v_mfma_f32_16x16x32_bf16 a[172:175], a[92:95], a[236:239], a[172:175]\n\t"
      "v_cvt_pk_bf16_f32 v48, v48, v49\n\t"
      "v_cvt_pk_bf16_f32 v49, v50, v51\n\t"
      "v_mfma_f32_16x16x32_bf16 a[176:179], a[96:99], a[236:239], a[176:179]\n\t"
      "v_cvt_pk_bf16_f32 v52, v52, v53\n\t"
      "v_cvt_pk_bf16_f32 v53, v54, v55\n\t"
      "v_accvgpr_write_b32 a100, v40\n\t"
      "v_accvgpr_write_b32 a101, v41\n\t"
      "v_accvgpr_write_b32 a102, v44\n\t"
      "v_accvgpr_write_b32 a103, v45\n\t"
      "v_accvgpr_write_b32 a104, v48\n\t"
      "v_accvgpr_write_b32 a105, v49\n\t"
      "v_accvgpr_write_b32 a106, v52\n\t"
      "v_accvgpr_write_b32 a107, v53\n\t"
      "s_waitcnt vmcnt(12)\n\t"
      "s_waitcnt lgkmcnt(1)\n\t"
      "; step\n\t"
      "v_mfma_f32_4x4x4_16b_bf16 v[40:43], a[180:181], v[160:161], 0\n\t"
      "ds_read_b128 a[184:187], v14 offset:1792\n\t"
      "v_mfma_f32_4x4x4_16b_bf16 v[44:47], a[182:183], v[162:163], 0\n\t"
      "ds_read_b64_tr_b16 v[176:177], v60 offset:49152\n\t"
      "v_mfma_f32_4x4x4_16b_bf16 v[48:51], a[180:181], v[164:165], 0\n\t"
      "ds_read_b64_tr_b16 v[178:179], v60 offset:49920\n\t"
      "v_mfma_f32_4x4x4_16b_bf16 v[52:55], a[182:183], v[166:167], 0\n\t"
      "ds_read_b64_tr_b16 v[180:181], v61 offset:49152\n\t"
      "ds_read_b64_tr_b16 v[182:183], v61 offset:49920\n\t"
      "s_add_u32 m0, s17, 111104\n\t"
      "s_mov_b32 s54, 245760\n\t"
      "buffer_load_dwordx4 v9, s[84:87], s54 offen lds\n\t"
      "buffer_load_dwordx4 v10, s[84:87], s54 offen offset:1024 lds\n\t"
      "v_mfma_f32_16x16x32_bf16 v[16:19], v[144:147], a[240:243], v[16:19]\n\t"
      "v_cvt_pk_bf16_f32 v160, v40, v41\n\t"
      "v_cvt_pk_bf16_f32 v161, v42, v43\n\t"
      "v_mfma_f32_16x16x32_bf16 v[20:23], v[148:151], a[240:243], v[20:23]\n\t"
      "v_cvt_pk_bf16_f32 v162, v44, v45\n\t"
      "v_cvt_pk_bf16_f32 v163, v46, v47\n\t"
      "v_mfma_f32_16x16x32_bf16 v[24:27], v[152:155], a[240:243], v[24:27]\n\t"
      "v_cvt_pk_bf16_f32 v164, v48, v49\n\t"
      "v_cvt_pk_bf16_f32 v165, v50, v51\n\t"
      "v_mfma_f32_16x16x32_bf16 v[28:31], v[156:159], a[240:243], v[28:31]\n\t"
      "v_cvt_pk_bf16_f32 v166, v52, v53\n\t"
      "v_cvt_pk_bf16_f32 v167, v54, v55\n\t"
      "v_mfma_f32_4x4x4_16b_bf16 v[40:43], a[180:181], v[168:169], 0\n\t"
      "ds_read_b64_tr_b16 v[184:185], v60 offset:49216\n\t"
      "v_mfma_f32_4x4x4_16b_bf16 v[44:47], a[182:183], v[170:171], 0\n\t"
      "ds_read_b64_tr_b16 v[186:187], v60 offset:49984\n\t"
      "v_mfma_f32_4x4x4_16b_bf16 v[48:51], a[180:181], v[172:173], 0\n\t"
      "ds_read_b64_tr_b16 v[188:189], v61 offset:49216\n\t"
      "v_mfma_f32_4x4x4_16b_bf16 v[52:55], a[182:183], v[174:175], 0\n\t"
      "ds_read_b64_tr_b16 v[190:191], v61 offset:49984\n\t"
      "buffer_load_dwordx4 v11, s[84:87], s54 offen offset:2048 lds\n\t"
      "s_add_u32 m0, s17, 114176\n\t"
      "s_mov_b32 s54, 258048\n\t"
      "buffer_load_dwordx4 v9, s[84:87], s54 offen lds\n\t"
      "v_mfma_f32_16x16x32_bf16 v[32:35], a[100:103], a[240:243], v[32:35]\n\t"
      "v_cvt_pk_bf16_f32 v168, v40, v41\n\t"
      "v_cvt_pk_bf16_f32 v169, v42, v43\n\t"
      "v_mfma_f32_16x16x32_bf16 v[36:39], a[104:107], a[240:243], v[36:39]\n\t"
      "v_cvt_pk_bf16_f32 v170, v44, v45\n\t"
      "v_cvt_pk_bf16_f32 v171, v46, v47\n\t"
      "s_waitcnt lgkmcnt(9)\n\t"
      "v_mfma_f32_16x16x32_bf16 a[156:159], v[144:147], a[244:247], a[156:159]\n\t"
      "v_cvt_pk_bf16_f32 v172, v48, v49\n\t"
      "v_cvt_pk_bf16_f32 v173, v50, v51\n\t"
      "v_mfma_f32_16x16x32_bf16 a[160:163], v[148:151], a[244:247], a[160:163]\n\t"
      "v_cvt_pk_bf16_f32 v174, v52, v53\n\t"
      "v_cvt_pk_bf16_f32 v175, v54, v55\n\t"
      "ds_read_b128 a[224:227], v58 offset:25344\n\t"
      "v_mfma_f32_4x4x4_16b_bf16 v[40:43], a[180:181], a[108:109], 0\n\t"
      "ds_read_b64_tr_b16 a[116:117], v60 offset:49280\n\t"
      "v_mfma_f32_4x4x4_16b_bf16 v[44:47], a[182:183], a[110:111], 0\n\t"
      "ds_read_b64_tr_b16 a[118:119], v60 offset:50048\n\t"
      "v_mfma_f32_4x4x4_16b_bf16 v[48:51], a[180:181], a[112:113], 0\n\t"
      "ds_read_b64_tr_b16 a[120:121], v61 offset:49280\n\t"
      "v_mfma_f32_4x4x4_16b_bf16 v[52:55], a[182:183], a[114:115], 0\n\t"
      "ds_read_b64_tr_b16 a[122:123], v61 offset:50048\n\t"
      "buffer_load_dwordx4 v10, s[84:87], s54 offen offset:1024 lds\n\t"
      "buffer_load_dwordx4 v11, s[84:87], s54 offen offset:2048 lds\n\t"
      "ds_read_b128 a[228:231], v58 offset:37632\n\t"
      "v_mfma_f32_16x16x32_bf16 a[164:167], v[152:155], a[244:247], a[164:167]\n\t"
      "v_cvt_pk_bf16_f32 v40, v40, v41\n\t"
      "v_cvt_pk_bf16_f32 v41, v42, v43\n\t"
      "v_mfma_f32_16x16x32_bf16 a[168:171], v[156:159], a[244:247], a[168:171]\n\t"
      "v_cvt_pk_bf16_f32 v44, v44, v45\n\t"
      "v_cvt_pk_bf16_f32 v45, v46, v47\n\t"
      "v_mfma_f32_16x16x32_bf16 a[172:175], a[100:103], a[244:247], a[172:175]\n\t"
      "v_cvt_pk_bf16_f32 v48, v48, v49\n\t"
      "v_cvt_pk_bf16_f32 v49, v50, v51\n\t"
      "v_mfma_f32_16x16x32_bf16 a[176:179], a[104:107], a[244:247], a[176:179]\n\t"
      "v_cvt_pk_bf16_f32 v52, v52, v53\n\t"
      "v_cvt_pk_bf16_f32 v53, v54, v55\n\t"
      "v_accvgpr_write_b32 a108, v40\n\t"
      "v_accvgpr_write_b32 a109, v41\n\t"
      "v_accvgpr_write_b32 a110, v44\n\t"
      "v_accvgpr_write_b32 a111, v45\n\t"
      "v_accvgpr_write_b32 a112, v48\n\t"
      "v_accvgpr_write_b32 a113, v49\n\t"
      "v_accvgpr_write_b32 a114, v52\n\t"
      "v_accvgpr_write_b32 a115, v53\n\t"
      "s_waitcnt vmcnt(12)\n\t"
      "s_waitcnt lgkmcnt(1)\n\t"
      "; step\n\t"
      "v_mfma_f32_4x4x4_16b_bf16 v[40:43], a[184:185], v[176:177], 0\n\t"
      "ds_read_b128 a[180:183], v14 offset:2048\n\t"
      "v_mfma_f32_4x4x4_16b_bf16 v[44:47], a[186:187], v[178:179], 0\n\t"
      "ds_read_b64_tr_b16 v[192:193], v12 offset:0\n\t"
      "v_mfma_f32_4x4x4_16b_bf16 v[48:51], a[184:185], v[180:181], 0\n\t"
      "ds_read_b64_tr_b16 v[194:195], v12 offset:768\n\t"
      "v_mfma_f32_4x4x4_16b_bf16 v[52:55], a[186:187], v[182:183], 0\n\t"
      "ds_read_b64_tr_b16 v[196:197], v13 offset:0\n\t"
      "ds_read_b64_tr_b16 v[198:199], v13 offset:768\n\t"
      "s_add_u32 m0, s17, 135680\n\t"
      "s_mov_b32 s54, 270336\n\t"
      "buffer_load_dwordx4 v9, s[84:87], s54 offen lds\n\t"
      "buffer_load_dwordx4 v10, s[84:87], s54 offen offset:1024 lds\n\t"
      "v_mfma_f32_16x16x32_bf16 v[16:19], v[160:163], a[224:227], v[16:19]\n\t"
      "v_cvt_pk_bf16_f32 v176, v40, v41\n\t"
      "v_cvt_pk_bf16_f32 v177, v42, v43\n\t"
      "v_mfma_f32_16x16x32_bf16 v[20:23], v[164:167], a[224:227], v[20:23]\n\t"
      "v_cvt_pk_bf16_f32 v178, v44, v45\n\t"
      "v_cvt_pk_bf16_f32 v179, v46, v47\n\t"
      "v_mfma_f32_16x16x32_bf16 v[24:27], v[168:171], a[224:227], v[24:27]\n\t"
      "v_cvt_pk_bf16_f32 v180, v48, v49\n\t"
      "v_cvt_pk_bf16_f32 v181, v50, v51\n\t"
      "v_mfma_f32_16x16x32_bf16 v[28:31], v[172:175], a[224:227], v[28:31]\n\t"
      "v_cvt_pk_bf16_f32 v182, v52, v53\n\t"
      "v_cvt_pk_bf16_f32 v183, v54, v55\n\t"
      "v_mfma_f32_4x4x4_16b_bf16 v[40:43], a[184:185], v[184:185], 0\n\t"
      "ds_read_b64_tr_b16 v[200:201], v12 offset:64\n\t"
      "v_mfma_f32_4x4x4_16b_bf16 v[44:47], a[186:187], v[186:187], 0\n\t"
      "ds_read_b64_tr_b16 v[202:203], v12 offset:832\n\t"
      "v_mfma_f32_4x4x4_16b_bf16 v[48:51], a[184:185], v[188:189], 0\n\t"
      "ds_read_b64_tr_b16 v[204:205], v13 offset:64\n\t"
      "v_mfma_f32_4x4x4_16b_bf16 v[52:55], a[186:187], v[190:191], 0\n\t"
      "ds_read_b64_tr_b16 v[206:207], v13 offset:832\n\t"
      "buffer_load_dwordx4 v11, s[84:87], s54 offen offset:2048 lds\n\t"
      "s_add_u32 m0, s17, 138752\n\t"
      "s_mov_b32 s54, 282624\n\t"
      "buffer_load_dwordx4 v9, s[84:87], s54 offen lds\n\t"
      "v_mfma_f32_16x16x32_bf16 v[32:35], a[108:111], a[224:227], v[32:35]\n\t"
      "v_cvt_pk_bf16_f32 v184, v40, v41\n\t"
      "v_cvt_pk_bf16_f32 v185, v42, v43\n\t"
      "v_mfma_f32_16x16x32_bf16 v[36:39], a[112:115], a[224:227], v[36:39]\n\t"
      "v_cvt_pk_bf16_f32 v186, v44, v45\n\t"
      "v_cvt_pk_bf16_f32 v187, v46, v47\n\t"
      "s_waitcnt lgkmcnt(9)\n\t"
      "v_mfma_f32_16x16x32_bf16 a[156:159], v[160:163], a[228:231], a[156:159]\n\t"
      "v_cvt_pk_bf16_f32 v188, v48, v49\n\t"
      "v_cvt_pk_bf16_f32 v189, v50, v51\n\t"
      "v_mfma_f32_16x16x32_bf16 a[160:163], v[164:167], a[228:231], a[160:163]\n\t"
      "v_cvt_pk_bf16_f32 v190, v52, v53\n\t"
      "v_cvt_pk_bf16_f32 v191, v54, v55\n\t"
      "ds_read_b128 a[232:235], v59 offset:25344\n\t"
      "v_mfma_f32_4x4x4_16b_bf16 v[40:43], a[184:185], a[116:117], 0\n\t"
      "ds_read_b64_tr_b16 a[124:125], v12 offset:128\n\t"
      "v_mfma_f32_4x4x4_16b_bf16 v[44:47], a[186:187], a[118:119], 0\n\t"
      "ds_read_b64_tr_b16 a[126:127], v12 offset:896\n\t"
      "v_mfma_f32_4x4x4_16b_bf16 v[48:51], a[184:185], a[120:121], 0\n\t"
      "ds_read_b64_tr_b16 a[128:129], v13 offset:128\n\t"
      "v_mfma_f32_4x4x4_16b_bf16 v[52:55], a[186:187], a[122:123], 0\n\t"
      "ds_read_b64_tr_b16 a[130:131], v13 offset:896\n\t"
      "buffer_load_dwordx4 v10, s[84:87], s54 offen offset:1024 lds\n\t"
      "buffer_load_dwordx4 v11, s[84:87], s54 offen offset:2048 lds\n\t"
      "ds_read_b128 a[236:239], v59 offset:37632\n\t"
      "s_or_b32 s30, s17, s16\n\t"
      "s_cbranch_scc1 .Lno3_%=\n\t"
      "global_load_dword a252, v62, s[12:13] sc1\n\t"
      ".Lno3_%=:\n\t"
      "s_add_u32 m0, s18, 49664\n\t"
      "s_add_u32 s48, s15, 786432\n\t"
      "buffer_load_dwordx4 v2, s[40:43], s48 offen nt lds\n\t"
      "buffer_load_dwordx4 v3, s[40:43], s48 offen offset:1024 nt lds\n\t"
      "buffer_load_dwordx4 v4, s[40:43], s48 offen offset:2048 nt lds\n\t"
      "v_mfma_f32_16x16x32_bf16 a[164:167], v[168:171], a[228:231], a[164:167]\n\t"
      "v_cvt_pk_bf16_f32 v40, v40, v41\n\t"
      "v_cvt_pk_bf16_f32 v41, v42, v43\n\t"
      "v_mfma_f32_16x16x32_bf16 a[168:171], v[172:175], a[228:231], a[168:171]\n\t"
      "v_cvt_pk_bf16_f32 v44, v44, v45\n\t"
      "v_cvt_pk_bf16_f32 v45, v46, v47\n\t"
      "v_mfma_f32_16x16x32_bf16 a[172:175], a[108:111], a[228:231], a[172:175]\n\t"
      "v_cvt_pk_bf16_f32 v48, v48, v49\n\t"
      "v_cvt_pk_bf16_f32 v49, v50, v51\n\t"
      "v_mfma_f32_16x16x32_bf16 a[176:179], a[112:115], a[228:231], a[176:179]\n\t"
      "v_cvt_pk_bf16_f32 v52, v52, v53\n\t"
      "v_cvt_pk_bf16_f32 v53, v54, v55\n\t"
      "v_accvgpr_write_b32 a116, v40\n\t"
      "v_accvgpr_write_b32 a117, v41\n\t"
      "v_accvgpr_write_b32 a118, v44\n\t"
      "v_accvgpr_write_b32 a119, v45\n\t"
      "v_accvgpr_write_b32 a120, v48\n\t"
      "v_accvgpr_write_b32 a121, v49\n\t"
      "v_accvgpr_write_b32 a122, v52\n\t"
      "v_accvgpr_write_b32 a123, v53\n\t"
      "s_waitcnt vmcnt(15)\n\t"
      "s_waitcnt lgkmcnt(1)\n\t"
      "; step\n\t"
      "v_mfma_f32_4x4x4_16b_bf16 v[40:43], a[180:181], v[192:193], 0\n\t"
      "ds_read_b128 a[184:187], v14 offset:2304\n\t"
      "v_mfma_f32_4x4x4_16b_bf16 v[44:47], a[182:183], v[194:195], 0\n\t"
      "ds_read_b64_tr_b16 v[208:209], v60 offset:0\n\t"
      "v_mfma_f32_4x4x4_16b_bf16 v[48:51], a[180:181], v[196:197], 0\n\t"
      "ds_read_b64_tr_b16 v[210:211], v60 offset:768\n\t"
      "v_mfma_f32_4x4x4_16b_bf16 v[52:55], a[182:183], v[198:199], 0\n\t"
      "ds_read_b64_tr_b16 v[212:213], v61 offset:0\n\t"
      "ds_read_b64_tr_b16 v[214:215], v61 offset:768\n\t"
      "s_add_u32 m0, s18, 61952\n\t"
      "s_add_u32 s48, s15, 786432\n\t"
      "buffer_load_dwordx4 v5, s[40:43], s48 offen nt lds\n\t"
      "buffer_load_dwordx4 v6, s[40:43], s48 offen offset:1024 nt lds\n\t"
      "v_mfma_f32_16x16x32_bf16 v[16:19], v[176:179], a[232:235], v[16:19]\n\t"
      "v_cvt_pk_bf16_f32 v192, v40, v41\n\t"
      "v_cvt_pk_bf16_f32 v193, v42, v43\n\t"
      "v_mfma_f32_16x16x32_bf16 v[20:23], v[180:183], a[232:235], v[20:23]\n\t"
      "v_cvt_pk_bf16_f32 v194, v44, v45\n\t"
      "v_cvt_pk_bf16_f32 v195, v46, v47\n\t"
      "v_mfma_f32_16x16x32_bf16 v[24:27], v[184:187], a[232:235], v[24:27]\n\t"
      "v_cvt_pk_bf16_f32 v196, v48, v49\n\t"
      "v_cvt_pk_bf16_f32 v197, v50, v51\n\t"
      "v_mfma_f32_16x16x32_bf16 v[28:31], v[188:191], a[232:235], v[28:31]\n\t"
      "v_cvt_pk_bf16_f32 v198, v52, v53\n\t"
      "v_cvt_pk_bf16_f32 v199, v54, v55\n\t"
      "v_mfma_f32_4x4x4_16b_bf16 v[40:43], a[180:181], v[200:201], 0\n\t"
      "ds_read_b64_tr_b16 v[216:217], v60 offset:64\n\t"
      "v_mfma_f32_4x4x4_16b_bf16 v[44:47], a[182:183], v[202:203], 0\n\t"
      "ds_read_b64_tr_b16 v[218:219], v60 offset:832\n\t"
      "v_mfma_f32_4x4x4_16b_bf16 v[48:51], a[180:181], v[204:205], 0\n\t"
      "ds_read_b64_tr_b16 v[220:221], v61 offset:64\n\t"
      "v_mfma_f32_4x4x4_16b_bf16 v[52:55], a[182:183], v[206:207], 0\n\t"
      "ds_read_b64_tr_b16 v[222:223], v61 offset:832\n\t"
      "buffer_load_dwordx4 v7, s[40:43], s48 offen offset:2048 nt lds\n\t"
      "v_mfma_f32_16x16x32_bf16 v[32:35], a[116:119], a[232:235], v[32:35]\n\t"
      "v_cvt_pk_bf16_f32 v200, v40, v41\n\t"
      "v_cvt_pk_bf16_f32 v201, v42, v43\n\t"
      "v_mfma_f32_16x16x32_bf16 v[36:39], a[120:123], a[232:235], v[36:39]\n\t"
      "v_cvt_pk_bf16_f32 v202, v44, v45\n\t"
      "v_cvt_pk_bf16_f32 v203, v46, v47\n\t"
      "s_waitcnt lgkmcnt(9)\n\t"
      "v_mfma_f32_16x16x32_bf16 a[156:159], v[176:179], a[236:239], a[156:159]\n\t"
      "v_cvt_pk_bf16_f32 v204, v48, v49\n\t"
      "v_cvt_pk_bf16_f32 v205, v50, v51\n\t"
      "v_mfma_f32_16x16x32_bf16 a[160:163], v[180:183], a[236:239], a[160:163]\n\t"
      "v_cvt_pk_bf16_f32 v206, v52, v53\n\t"
      "v_cvt_pk_bf16_f32 v207, v54, v55\n\t"
      "ds_read_b128 a[240:243], v56 offset:25600\n\t"
      "v_mfma_f32_4x4x4_16b_bf16 v[40:43], a[180:181], a[124:125], 0\n\t"
      "ds_read_b64_tr_b16 a[132:133], v60 offset:128\n\t"
      "v_mfma_f32_4x4x4_16b_bf16 v[44:47], a[182:183], a[126:127], 0\n\t"
      "ds_read_b64_tr_b16 a[134:135], v60 offset:896\n\t"
      "v_mfma_f32_4x4x4_16b_bf16 v[48:51], a[180:181], a[128:129], 0\n\t"
      "ds_read_b64_tr_b16 a[136:137], v61 offset:128\n\t"
      "v_mfma_f32_4x4x4_16b_bf16 v[52:55], a[182:183], a[130:131], 0\n\t"
      "ds_read_b64_tr_b16 a[138:139], v61 offset:896\n\t"
      "ds_read_b128 a[244:247], v56 offset:37888\n\t"
      "v_mfma_f32_16x16x32_bf16 a[164:167], v[184:187], a[236:239], a[164:167]\n\t"
      "v_cvt_pk_bf16_f32 v40, v40, v41\n\t"
      "v_cvt_pk_bf16_f32 v41, v42, v43\n\t"
      "v_mfma_f32_16x16x32_bf16 a[168:171], v[188:191], a[236:239], a[168:171]\n\t"
      "v_cvt_pk_bf16_f32 v44, v44, v45\n\t"
      "v_cvt_pk_bf16_f32 v45, v46, v47\n\t"
      "v_mfma_f32_16x16x32_bf16 a[172:175], a[116:119], a[236:239], a[172:175]\n\t"
      "v_cvt_pk_bf16_f32 v48, v48, v49\n\t"
      "v_cvt_pk_bf16_f32 v49, v50, v51\n\t"
      "v_mfma_f32_16x16x32_bf16 a[176:179], a[120:123], a[236:239], a[176:179]\n\t"
      "v_cvt_pk_bf16_f32 v52, v52, v53\n\t"
      "v_cvt_pk_bf16_f32 v53, v54, v55\n\t"
      "v_accvgpr_write_b32 a124, v40\n\t"
      "v_accvgpr_write_b32 a125, v41\n\t"
      "v_accvgpr_write_b32 a126, v44\n\t"
      "v_accvgpr_write_b32 a127, v45\n\t"
      "v_accvgpr_write_b32 a128, v48\n\t"
      "v_accvgpr_write_b32 a129, v49\n\t"
      "v_accvgpr_write_b32 a130, v52\n\t"
      "v_accvgpr_write_b32 a131, v53\n\t"
      "s_waitcnt vmcnt(12)\n\t"
      "s_waitcnt lgkmcnt(1)\n\t"
      "; step\n\t"
      "v_mfma_f32_4x4x4_16b_bf16 v[40:43], a[184:185], v[208:209], 0\n\t"
      "ds_read_b128 a[180:183], v14 offset:2560\n\t"
      "v_mfma_f32_4x4x4_16b_bf16 v[44:47], a[186:187], v[210:211], 0\n\t"
      "ds_read_b64_tr_b16 v[224:225], v60 offset:24576\n\t"
      "v_mfma_f32_4x4x4_16b_bf16 v[48:51], a[184:185], v[212:213], 0\n\t"
      "ds_read_b64_tr_b16 v[226:227], v60 offset:25344\n\t"
      "v_mfma_f32_4x4x4_16b_bf16 v[52:55], a[186:187], v[214:215], 0\n\t"
      "ds_read_b64_tr_b16 v[228:229], v61 offset:24576\n\t"
      "ds_read_b64_tr_b16 v[230:231], v61 offset:25344\n\t"
      "v_mfma_f32_16x16x32_bf16 v[16:19], v[192:195], a[240:243], v[16:19]\n\t"
      "v_cvt_pk_bf16_f32 v208, v40, v41\n\t"
      "v_cvt_pk_bf16_f32 v209, v42, v43\n\t"
      "v_mfma_f32_16x16x32_bf16 v[20:23], v[196:199], a[240:243], v[20:23]\n\t"
      "v_cvt_pk_bf16_f32 v210, v44, v45\n\t"
      "v_cvt_pk_bf16_f32 v211, v46, v47\n\t"
      "v_mfma_f32_16x16x32_bf16 v[24:27], v[200:203], a[240:243], v[24:27]\n\t"
      "v_cvt_pk_bf16_f32 v212, v48, v49\n\t"
      "v_cvt_pk_bf16_f32 v213, v50, v51\n\t"
      "v_mfma_f32_16x16x32_bf16 v[28:31], v[204:207], a[240:243], v[28:31]\n\t"
      "v_cvt_pk_bf16_f32 v214, v52, v53\n\t"
      "v_cvt_pk_bf16_f32 v215, v54, v55\n\t"
      "v_mfma_f32_4x4x4_16b_bf16 v[40:43], a[184:185], v[216:217], 0\n\t"
      "ds_read_b64_tr_b16 v[232:233], v60 offset:24640\n\t"
      "v_mfma_f32_4x4x4_16b_bf16 v[44:47], a[186:187], v[218:219], 0\n\t"
      "ds_read_b64_tr_b16 v[234:235], v60 offset:25408\n\t"
      "v_mfma_f32_4x4x4_16b_bf16 v[48:51], a[184:185], v[220:221], 0\n\t"
      "ds_read_b64_tr_b16 v[236:237], v61 offset:24640\n\t"
      "v_mfma_f32_4x4x4_16b_bf16 v[52:55], a[186:187], v[222:223], 0\n\t"
      "ds_read_b64_tr_b16 v[238:239], v61 offset:25408\n\t"
      "v_mfma_f32_16x16x32_bf16 v[32:35], a[124:127], a[240:243], v[32:35]\n\t"
      "v_cvt_pk_bf16_f32 v216, v40, v41\n\t"
      "v_cvt_pk_bf16_f32 v217, v42, v43\n\t"
      "v_mfma_f32_16x16x32_bf16 v[36:39], a[128:131], a[240:243], v[36:39]\n\t"
      "v_cvt_pk_bf16_f32 v218, v44, v45\n\t"
      "v_cvt_pk_bf16_f32 v219, v46, v47\n\t"
      "s_waitcnt lgkmcnt(9)\n\t"
      "v_mfma_f32_16x16x32_bf16 a[156:159], v[192:195], a[244:247], a[156:159]\n\t"
      "v_cvt_pk_bf16_f32 v220, v48, v49\n\t"
      "v_cvt_pk_bf16_f32 v221, v50, v51\n\t"
      "v_mfma_f32_16x16x32_bf16 a[160:163], v[196:199], a[244:247], a[160:163]\n\t"
      "v_cvt_pk_bf16_f32 v222, v52, v53\n\t"
      "v_cvt_pk_bf16_f32 v223, v54, v55\n\t"
      "ds_read_b128 a[224:227], v57 offset:25600\n\t"
      "v_mfma_f32_4x4x4_16b_bf16 v[40:43], a[184:185], a[132:133], 0\n\t"
      "ds_read_b64_tr_b16 a[140:141], v60 offset:24704\n\t"
      "v_mfma_f32_4x4x4_16b_bf16 v[44:47], a[186:187], a[134:135], 0\n\t"
      "ds_read_b64_tr_b16 a[142:143], v60 offset:25472\n\t"
      "v_mfma_f32_4x4x4_16b_bf16 v[48:51], a[184:185], a[136:137], 0\n\t"
      "ds_read_b64_tr_b16 a[144:145], v61 offset:24704\n\t"
      "v_mfma_f32_4x4x4_16b_bf16 v[52:55], a[186:187], a[138:139], 0\n\t"
      "ds_read_b64_tr_b16 a[146:147], v61 offset:25472\n\t"
      "ds_read_b128 a[228:231], v57 offset:37888\n\t"
      "v_mfma_f32_16x16x32_bf16 a[164:167], v[200:203], a[244:247], a[164:167]\n\t"
      "v_cvt_pk_bf16_f32 v40, v40, v41\n\t"
      "v_cvt_pk_bf16_f32 v41, v42, v43\n\t"
      "v_mfma_f32_16x16x32_bf16 a[168:171], v[204:207], a[244:247], a[168:171]\n\t"
      "v_cvt_pk_bf16_f32 v44, v44, v45\n\t"
      "v_cvt_pk_bf16_f32 v45, v46, v47\n\t"
      "v_mfma_f32_16x16x32_bf16 a[172:175], a[124:127], a[244:247], a[172:175]\n\t"
      "v_cvt_pk_bf16_f32 v48, v48, v49\n\t"
      "v_cvt_pk_bf16_f32 v49, v50, v51\n\t"
      "v_mfma_f32_16x16x32_bf16 a[176:179], a[128:131], a[244:247], a[176:179]\n\t"
      "v_cvt_pk_bf16_f32 v52, v52, v53\n\t"
      "v_cvt_pk_bf16_f32 v53, v54, v55\n\t"
      "v_accvgpr_write_b32 a132, v40\n\t"
      "v_accvgpr_write_b32 a133, v41\n\t"
      "v_accvgpr_write_b32 a134, v44\n\t"
      "v_accvgpr_write_b32 a135, v45\n\t"
      "v_accvgpr_write_b32 a136, v48\n\t"
      "v_accvgpr_write_b32 a137, v49\n\t"
      "v_accvgpr_write_b32 a138, v52\n\t"
      "v_accvgpr_write_b32 a139, v53\n\t"
      "s_waitcnt vmcnt(6)\n\t"
      "s_waitcnt lgkmcnt(1)\n\t"
      "; step\n\t"
      "v_mfma_f32_4x4x4_16b_bf16 v[40:43], a[180:181], v[224:225], 0\n\t"
      "ds_read_b128 a[184:187], v14 offset:2816\n\t"
      "v_mfma_f32_4x4x4_16b_bf16 v[44:47], a[182:183], v[226:227], 0\n\t"
      "ds_read_b64_tr_b16 v[240:241], v60 offset:49152\n\t"
      "v_mfma_f32_4x4x4_16b_bf16 v[48:51], a[180:181], v[228:229], 0\n\t"
      "ds_read_b64_tr_b16 v[242:243], v60 offset:49920\n\t"
      "v_mfma_f32_4x4x4_16b_bf16 v[52:55], a[182:183], v[230:231], 0\n\t"
      "ds_read_b64_tr_b16 v[244:245], v61 offset:49152\n\t"
      "ds_read_b64_tr_b16 v[246:247], v61 offset:49920\n\t"
      "v_mfma_f32_16x16x32_bf16 v[16:19], v[208:211], a[224:227], v[16:19]\n\t"
      "v_cvt_pk_bf16_f32 v224, v40, v41\n\t"
      "v_cvt_pk_bf16_f32 v225, v42, v43\n\t"
      "v_mfma_f32_16x16x32_bf16 v[20:23], v[212:215], a[224:227], v[20:23]\n\t"
      "v_cvt_pk_bf16_f32 v226, v44, v45\n\t"
      "v_cvt_pk_bf16_f32 v227, v46, v47\n\t"
      "v_mfma_f32_16x16x32_bf16 v[24:27], v[216:219], a[224:227], v[24:27]\n\t"
      "v_cvt_pk_bf16_f32 v228, v48, v49\n\t"
      "v_cvt_pk_bf16_f32 v229, v50, v51\n\t"
      "v_mfma_f32_16x16x32_bf16 v[28:31], v[220:223], a[224:227], v[28:31]\n\t"
      "v_cvt_pk_bf16_f32 v230, v52, v53\n\t"
      "v_cvt_pk_bf16_f32 v231, v54, v55\n\t"
      "v_mfma_f32_4x4x4_16b_bf16 v[40:43], a[180:181], v[232:233], 0\n\t"
      "ds_read_b64_tr_b16 v[248:249], v60 offset:49216\n\t"
      "v_mfma_f32_4x4x4_16b_bf16 v[44:47], a[182:183], v[234:235], 0\n\t"
      "ds_read_b64_tr_b16 v[250:251], v60 offset:49984\n\t"
      "v_mfma_f32_4x4x4_16b_bf16 v[48:51], a[180:181], v[236:237], 0\n\t"
      "ds_read_b64_tr_b16 v[252:253], v61 offset:49216\n\t"
      "v_mfma_f32_4x4x4_16b_bf16 v[52:55], a[182:183], v[238:239], 0\n\t"
      "ds_read_b64_tr_b16 v[254:255], v61 offset:49984\n\t"
      "v_mfma_f32_16x16x32_bf16 v[32:35], a[132:135], a[224:227], v[32:35]\n\t"
      "v_cvt_pk_bf16_f32 v232, v40, v41\n\t"
      "v_cvt_pk_bf16_f32 v233, v42, v43\n\t"
      "v_mfma_f32_16x16x32_bf16 v[36:39], a[136:139], a[224:227], v[36:39]\n\t"
      "v_cvt_pk_bf16_f32 v234, v44, v45\n\t"
      "v_cvt_pk_bf16_f32 v235, v46, v47\n\t"
      "s_waitcnt lgkmcnt(9)\n\t"
      "v_mfma_f32_16x16x32_bf16 a[156:159], v[208:211], a[228:231], a[156:159]\n\t"
      "v_cvt_pk_bf16_f32 v236, v48, v49\n\t"
      "v_cvt_pk_bf16_f32 v237, v50, v51\n\t"
      "v_mfma_f32_16x16x32_bf16 a[160:163], v[212:215], a[228:231], a[160:163]\n\t"
      "v_cvt_pk_bf16_f32 v238, v52, v53\n\t"
      "v_cvt_pk_bf16_f32 v239, v54, v55\n\t"
      "ds_read_b128 a[232:235], v58 offset:25600\n\t"
      "v_mfma_f32_4x4x4_16b_bf16 v[40:43], a[180:181], a[140:141], 0\n\t"
      "ds_read_b64_tr_b16 a[148:149], v60 offset:49280\n\t"
      "v_mfma_f32_4x4x4_16b_bf16 v[44:47], a[182:183], a[142:143], 0\n\t"
      "ds_read_b64_tr_b16 a[150:151], v60 offset:50048\n\t"
      "v_mfma_f32_4x4x4_16b_bf16 v[48:51], a[180:181], a[144:145], 0\n\t"
      "ds_read_b64_tr_b16 a[152:153], v61 offset:49280\n\t"
      "v_mfma_f32_4x4x4_16b_bf16 v[52:55], a[182:183], a[146:147], 0\n\t"
      "ds_read_b64_tr_b16 a[154:155], v61 offset:50048\n\t"
      "v_mfma_f32_16x16x32_bf16 a[164:167], v[216:219], a[228:231], a[164:167]\n\t"
      "v_cvt_pk_bf16_f32 v40, v40, v41\n\t"
      "v_cvt_pk_bf16_f32 v41, v42, v43\n\t"
      "v_mfma_f32_16x16x32_bf16 a[168:171], v[220:223], a[228:231], a[168:171]\n\t"
      "v_cvt_pk_bf16_f32 v44, v44, v45\n\t"
      "v_cvt_pk_bf16_f32 v45, v46, v47\n\t"
      "v_mfma_f32_16x16x32_bf16 a[172:175], a[132:135], a[228:231], a[172:175]\n\t"
      "v_cvt_pk_bf16_f32 v48, v48, v49\n\t"
      "v_cvt_pk_bf16_f32 v49, v50, v51\n\t"
      "v_mfma_f32_16x16x32_bf16 a[176:179], a[136:139], a[228:231], a[176:179]\n\t"
      "v_cvt_pk_bf16_f32 v52, v52, v53\n\t"
      "v_cvt_pk_bf16_f32 v53, v54, v55\n\t"
      "v_accvgpr_write_b32 a140, v40\n\t"
      "v_accvgpr_write_b32 a141, v41\n\t"
      "v_accvgpr_write_b32 a142, v44\n\t"
      "v_accvgpr_write_b32 a143, v45\n\t"
      "v_accvgpr_write_b32 a144, v48\n\t"
      "v_accvgpr_write_b32 a145, v49\n\t"
      "v_accvgpr_write_b32 a146, v52\n\t"
      "v_accvgpr_write_b32 a147, v53\n\t"
      "s_waitcnt lgkmcnt(0)\n\t"
      "; step\n\t"
      "v_mfma_f32_4x4x4_16b_bf16 v[40:43], a[184:185], v[240:241], 0\n\t"
      "v_mfma_f32_4x4x4_16b_bf16 v[44:47], a[186:187], v[242:243], 0\n\t"
      "v_mfma_f32_4x4x4_16b_bf16 v[48:51], a[184:185], v[244:245], 0\n\t"
      "v_mfma_f32_4x4x4_16b_bf16 v[52:55], a[186:187], v[246:247], 0\n\t"
      "v_mfma_f32_16x16x32_bf16 v[16:19], v[224:227], a[232:235], v[16:19]\n\t"
      "s_nop 0\n\t"
      "v_cvt_pk_bf16_f32 v240, v40, v41\n\t"
      "v_cvt_pk_bf16_f32 v241, v42, v43\n\t"
      "v_mfma_f32_16x16x32_bf16 v[20:23], v[228:231], a[232:235], v[20:23]\n\t"
      "v_cvt_pk_bf16_f32 v242, v44, v45\n\t"
      "v_cvt_pk_bf16_f32 v243, v46, v47\n\t"
      "v_cvt_pk_bf16_f32 v244, v48, v49\n\t"
      "v_cvt_pk_bf16_f32 v245, v50, v51\n\t"
      "v_cvt_pk_bf16_f32 v246, v52, v53\n\t"
      "v_cvt_pk_bf16_f32 v247, v54, v55\n\t"
      "v_mfma_f32_4x4x4_16b_bf16 v[40:43], a[184:185], v[248:249], 0\n\t"
      "v_mfma_f32_4x4x4_16b_bf16 v[44:47], a[186:187], v[250:251], 0\n\t"
      "v_mfma_f32_4x4x4_16b_bf16 v[48:51], a[184:185], v[252:253], 0\n\t"
      "v_mfma_f32_4x4x4_16b_bf16 v[52:55], a[186:187], v[254:255], 0\n\t"
      "v_mfma_f32_16x16x32_bf16 v[24:27], v[232:235], a[232:235], v[24:27]\n\t"
      "s_nop 0\n\t"
      "v_cvt_pk_bf16_f32 v248, v40, v41\n\t"
      "v_cvt_pk_bf16_f32 v249, v42, v43\n\t"
      "v_mfma_f32_16x16x32_bf16 v[28:31], v[236:239], a[232:235], v[28:31]\n\t"
      "v_cvt_pk_bf16_f32 v250, v44, v45\n\t"
      "v_cvt_pk_bf16_f32 v251, v46, v47\n\t"
      "v_cvt_pk_bf16_f32 v252, v48, v49\n\t"
      "v_cvt_pk_bf16_f32 v253, v50, v51\n\t"
      "v_cvt_pk_bf16_f32 v254, v52, v53\n\t"
      "v_cvt_pk_bf16_f32 v255, v54, v55\n\t"
      "ds_read_b128 a[236:239], v59 offset:25600\n\t"
      "v_mfma_f32_4x4x4_16b_bf16 v[40:43], a[184:185], a[148:149], 0\n\t"
      "v_mfma_f32_4x4x4_16b_bf16 v[44:47], a[186:187], a[150:151], 0\n\t"
      "v_mfma_f32_4x4x4_16b_bf16 v[48:51], a[184:185], a[152:153], 0\n\t"
      "v_mfma_f32_4x4x4_16b_bf16 v[52:55], a[186:187], a[154:155], 0\n\t"
      "ds_read_b128 a[240:243], v58 offset:37888\n\t"
      "ds_read_b128 a[244:247], v59 offset:37888\n\t"
      "v_mfma_f32_16x16x32_bf16 v[32:35], a[140:143], a[232:235], v[32:35]\n\t"
      "v_cvt_pk_bf16_f32 v40, v40, v41\n\t"
      "v_cvt_pk_bf16_f32 v41, v42, v43\n\t"
      "v_mfma_f32_16x16x32_bf16 v[36:39], a[144:147], a[232:235], v[36:39]\n\t"
      "v_cvt_pk_bf16_f32 v44, v44, v45\n\t"
      "v_cvt_pk_bf16_f32 v45, v46, v47\n\t"
      "v_cvt_pk_bf16_f32 v48, v48, v49\n\t"
      "v_cvt_pk_bf16_f32 v49, v50, v51\n\t"
      "v_cvt_pk_bf16_f32 v52, v52, v53\n\t"
      "v_cvt_pk_bf16_f32 v53, v54, v55\n\t"
      "v_accvgpr_write_b32 a148, v40\n\t"
      "v_accvgpr_write_b32 a149, v41\n\t"
      "v_accvgpr_write_b32 a150, v44\n\t"
      "v_accvgpr_write_b32 a151, v45\n\t"
      "v_accvgpr_write_b32 a152, v48\n\t"
      "v_accvgpr_write_b32 a153, v49\n\t"
      "v_accvgpr_write_b32 a154, v52\n\t"
      "v_accvgpr_write_b32 a155, v53\n\t"
      "s_waitcnt lgkmcnt(2)\n\t"
      "v_mfma_f32_16x16x32_bf16 v[16:19], v[240:243], a[236:239], v[16:19]\n\t"
      "s_add_u32 s49, s15, 0\n\t"
      "s_mov_b32 s50, 98816\n\t"
      "s_mov_b32 s51, 61952\n\t"
      "v_mfma_f32_16x16x32_bf16 v[20:23], v[244:247], a[236:239], v[20:23]\n\t"
      "s_mov_b32 s58, s14\n\t"
      "s_add_u32 s56, s14, 2359296\n\t"
      "v_mfma_f32_16x16x32_bf16 v[24:27], v[248:251], a[236:239], v[24:27]\n\t"
      "s_movk_i32 s39, 92\n\t"
      "s_movk_i32 s29, 72\n\t"
      "s_mov_b32 s32, 1947275663\n\t"
      "v_mfma_f32_16x16x32_bf16 v[28:31], v[252:255], a[236:239], v[28:31]\n\t"
      "s_mov_b32 s33, 143165559\n\t"
      "s_mov_b32 s34, 1950421355\n\t"
      "s_mov_b32 s35, 8947847\n\t"
      "v_mfma_f32_16x16x32_bf16 v[32:35], a[148:151], a[236:239], v[32:35]\n\t"
      "v_mov_b32 v52, 163328\n\t"
      "v_mfma_f32_16x16x32_bf16 v[36:39], a[152:155], a[236:239], v[36:39]\n\t"
      "s_cmp_lg_u32 s17, 0\n\t"
      "s_cselect_b32 s39, 0, s39\n\t"
      "s_cbranch_scc1 .Lno4_%=\n\t"
      "v_accvgpr_read_b32 v41, a248\n\t"
      "v_mov_b32 v51, 0x4040\n\t"
      "s_movk_i32 s52, 2000\n\t"
      "s_mov_b32 s91, 0\n\t"
      "s_waitcnt vmcnt(6) \n\t"
      "v_accvgpr_read_b32 v40, a252\n\t"
      ".Lcheck_%=:\n\t"
      "v_cmp_ne_u32 s[54:55], v40, v63\n\t"
      "s_cmp_lg_u32 s16, 0\n\t"
      "v_add_u32_dpp v42, v40, v40 quad_perm:[2,3,0,1] row_mask:0xf bank_mask:0xf\n\t"
      "s_cselect_b32 s30, -1, s54\n\t"
      "s_cmp_lg_u32 s30, -1\n\t"
      "s_cbranch_scc1 .Lwait_%=\n\t"
      ".Lwaited_%=:\n\t"
      "v_add_u32_dpp v42, v42, v42 quad_perm:[1,0,3,2] row_mask:0xf bank_mask:0xf\n\t"
      "s_bfe_u32 s30, s19, 0x30008\n\t"
      "s_bfe_u32 s31, s19, 0x10003\n\t"
      "s_lshl1_add_u32 s30, s30, s31\n\t"
      "s_lshl_b32 s30, s30, 1\n\t"
      "v_mov_b32 v43, v42\n\t"
      "v_subrev_u32_dpp v43, v42, v51 row_half_mirror row_mask:0x3 bank_mask:0xf\n\t"
      "v_lshl_add_u32 v42, v42, 1, v43\n\t"
      "v_readlane_b32 s31, v40, s30\n\t"
      "v_add_u32 v42, 0x1f1f, v42\n\t"
      "v_and_b32 v43, 128, v42\n\t"
      "v_and_b32 v44, 32768, v42\n\t"
      "v_cmp_ne_u32 s[88:89], 0, v43\n\t"
      "v_cmp_ne_u32 s[36:37], 0, v44\n\t"
      "s_bitcmp1_b32 s19, 2\n\t"
      "s_cselect_b32 s30, 0x3ffff, 0\n\t"
      "s_bitcmp1_b32 s31, 16\n\t"
      "s_cselect_b32 s31, 262140, 0\n\t"
      "s_xor_b32 s30, s30, s31\n\t"
      "v_lshrrev_b32 v41, v41, s88\n\t"
      "v_and_b32 v41, 1, v41\n\t"
      "v_cmp_ne_u32 vcc, 0, v41\n\t"
      "s_xor_b32 s36, s37, s89\n\t"
      "s_and_b32 s36, s36, 0x11111111\n\t"
      "s_bcnt1_i32_b32 s36, s36\n\t"
      "s_and_b32 s31, vcc_lo, 262140\n\t"
      "s_xor_b32 s31, s31, s30\n\t"
      "s_or_b32 s31, s31, s91\n\t"
      "s_and_b32 s30, s16, 0x3ffff\n\t"
      "s_cmp_lg_u32 s16, 0\n\t"
      "s_cselect_b32 s31, s30, s31\n\t"
      "v_mov_b32 v53, s31\n\t"
      "ds_write_b32 v52, v53\n\t"
      "s_lshl_b32 s30, s36, 2\n\t"
      "s_mov_b32 s54, 1126306048\n\t"
      "s_mov_b32 s55, 4\n\t"
      "s_lshr_b64 s[54:55], s[54:55], s30\n\t"
      "s_and_b32 s30, s54, 15\n\t"
      "s_add_u32 s29, s29, s30\n\t"
      ".Lno4_%=:\n\t"
      "v_accvgpr_read_b32 v9, a213\n\t"
      "v_accvgpr_read_b32 v10, a214\n\t"
      "v_accvgpr_read_b32 v11, a215\n\t"
      "v_accvgpr_read_b32 v12, a216\n\t"
      "v_accvgpr_read_b32 v13, a217\n\t"
      "v_accvgpr_read_b32 v14, a218\n\t"
      "v_accvgpr_read_b32 v8, a219\n\t"
      "v_accvgpr_read_b32 v60, a220\n\t"
      "v_accvgpr_read_b32 v61, a221\n\t"
      "v_add_u32 v56, 49664, v56\n\t"
      "v_add_u32 v57, 49664, v57\n\t"
      "v_add_u32 v58, 49664, v58\n\t"
      "v_add_u32 v59, 49664, v59\n\t"
      "v_cvt_pk_bf16_f32 v40, v16, v17\n\t"
      "v_cvt_pk_bf16_f32 v41, v18, v19\n\t"
      "v_cvt_pk_bf16_f32 v42, v20, v21\n\t"
      "v_cvt_pk_bf16_f32 v43, v22, v23\n\t"
      "v_cvt_pk_bf16_f32 v44, v24, v25\n\t"
      "v_cvt_pk_bf16_f32 v45, v26, v27\n\t"
      "v_cvt_pk_bf16_f32 v46, v28, v29\n\t"
      "v_cvt_pk_bf16_f32 v47, v30, v31\n\t"
      "v_cvt_pk_bf16_f32 v48, v32, v33\n\t"
      "v_cvt_pk_bf16_f32 v49, v34, v35\n\t"
      "v_cvt_pk_bf16_f32 v50, v36, v37\n\t"
      "v_cvt_pk_bf16_f32 v51, v38, v39\n\t"
      "s_mov_b32 s46, 113246208\n\t"
      "s_mov_b32 s52, 15\n\t"
      "s_add_u32 s90, s90, 0x20000\n\t"
      "s_waitcnt vmcnt(3)\n\t"
      "s_waitcnt lgkmcnt(0)\n\t"
      "s_barrier\n\t"
      "v_mfma_f32_16x16x32_bf16 a[156:159], v[224:227], a[240:243], a[156:159]\n\t"
      "ds_read_b32 v63, v52\n\t"
      "ds_read_b128 a[0:3], v56 offset:0\n\t"
      "s_memrealtime s[36:37]\n\t"
      "v_mfma_f32_16x16x32_bf16 a[160:163], v[228:231], a[240:243], a[160:163]\n\t"
      "ds_read_b128 a[4:7], v57 offset:0\n\t"
      "ds_read_b128 a[8:11], v58 offset:0\n\t"
      "v_mfma_f32_16x16x32_bf16 a[164:167], v[232:235], a[240:243], a[164:167]\n\t"
      "ds_read_b128 a[12:15], v59 offset:0\n\t"
      "ds_read_b128 a[16:19], v56 offset:256\n\t"
      "v_mfma_f32_16x16x32_bf16 a[168:171], v[236:239], a[240:243], a[168:171]\n\t"
      "ds_read_b128 a[20:23], v57 offset:256\n\t"
      "ds_write_b64 v0, v[40:41] offset:0\n\t"
      "v_mfma_f32_16x16x32_bf16 a[172:175], a[140:143], a[240:243], a[172:175]\n\t"
      "ds_write_b64 v0, v[42:43] offset:32\n\t"
      "v_mfma_f32_16x16x32_bf16 a[176:179], a[144:147], a[240:243], a[176:179]\n\t"
      "s_waitcnt lgkmcnt(8)\n\t"
      "v_readfirstlane_b32 s91, v63\n\t"
      "v_mfma_f32_16x16x32_bf16 a[156:159], v[240:243], a[244:247], a[156:159]\n\t"
      "ds_write_b64 v0, v[44:45] offset:64\n\t"
      "s_add_u32 m0, s18, 74240\n\t"
      "v_mfma_f32_16x16x32_bf16 a[160:163], v[244:247], a[244:247], a[160:163]\n\t"
      "s_bitcmp1_b32 s91, 2\n\t"
      "s_cselect_b32 s48, 24576, 0\n\t"
      "s_add_u32 s48, s48, s14\n\t"
      "v_mfma_f32_16x16x32_bf16 a[164:167], v[248:251], a[244:247], a[164:167]\n\t"
      "s_add_u32 s48, s48, 1572864\n\t"
      "buffer_load_dwordx4 v2, s[40:43], s48 offen nt lds\n\t"
      "s_mov_b32 s59, s91\n\t"
      "v_mfma_f32_16x16x32_bf16 a[168:171], v[252:255], a[244:247], a[168:171]\n\t"
      "ds_write_b64 v0, v[46:47] offset:96\n\t"
      "buffer_load_dwordx4 v3, s[40:43], s48 offen offset:1024 nt lds\n\t"
      "v_mfma_f32_16x16x32_bf16 a[172:175], a[148:151], a[244:247], a[172:175]\n\t"
      "ds_write_b64 v0, v[48:49] offset:128\n\t"
      "buffer_load_dwordx4 v4, s[40:43], s48 offen offset:2048 nt lds\n\t"
      "v_mfma_f32_16x16x32_bf16 a[176:179], a[152:155], a[244:247], a[176:179]\n\t"
      "ds_write_b64 v0, v[50:51] offset:160\n\t"
      "s_lshr_b32 s57, s91, 3\n\t"
      "s_bitcmp1_b32 s91, 3\n\t"
      "s_cselect_b32 s48, 24576, 0\n\t"
      "s_add_u32 s48, s48, s14\n\t"
      "s_add_u32 s48, s48, 2359296\n\t"
      "s_or_b32 s30, s17, s16\n\t"
      "s_cbranch_scc1 .Lno5_%=\n\t"
      "v_mov_b32 v54, s90\n\t"
      "v_mov_b32 v55, s19\n\t"
      "global_store_dword v55, v54, s[12:13] sc1\n\t"
      ".Lno5_%=:\n\t"
      "s_waitcnt lgkmcnt(11)\n\t"
      "v_mfma_f32_16x16x32_bf16 v[16:19], v[64:67], a[0:3], 0\n\t"
      "ds_read_b128 a[24:27], v58 offset:256\n\t"
      "ds_read_b128 a[28:31], v59 offset:256\n\t"
      "s_waitcnt lgkmcnt(12)\n\t"
      "v_mfma_f32_16x16x32_bf16 v[16:19], v[80:83], a[4:7], v[16:19]\n\t"
      "ds_read_b128 a[32:35], v56 offset:512\n\t"
      "ds_read_b128 a[36:39], v57 offset:512\n\t"
      "s_waitcnt lgkmcnt(13)\n\t"
      "v_mfma_f32_16x16x32_bf16 v[16:19], v[96:99], a[8:11], v[16:19]\n\t"
      "ds_read_b128 a[40:43], v58 offset:512\n\t"
      "ds_read_b128 a[44:47], v59 offset:512\n\t"
      "s_waitcnt lgkmcnt(14)\n\t"
      "v_mfma_f32_16x16x32_bf16 v[16:19], v[112:115], a[12:15], v[16:19]\n\t"
      "v_accvgpr_read_b32 v52, a156\n\t"
      "v_accvgpr_read_b32 v53, a157\n\t"
      "s_waitcnt lgkmcnt(13)\n\t"
      "v_mfma_f32_16x16x32_bf16 v[16:19], v[128:131], a[16:19], v[16:19]\n\t"
      "v_accvgpr_read_b32 v54, a158\n\t"
      "v_accvgpr_read_b32 v55, a159\n\t"
      "s_waitcnt lgkmcnt(12)\n\t"
      "v_mfma_f32_16x16x32_bf16 v[16:19], v[144:147], a[20:23], v[16:19]\n\t"
      "v_cvt_pk_bf16_f32 v40, v52, v53\n\t"
      "v_cvt_pk_bf16_f32 v41, v54, v55\n\t"
      "v_mfma_f32_16x16x32_bf16 v[20:23], v[68:71], a[0:3], 0\n\t"
      "ds_write_b64 v0, v[40:41] offset:12544\n\t"
      "v_mfma_f32_16x16x32_bf16 v[20:23], v[84:87], a[4:7], v[20:23]\n\t"
      "s_waitcnt lgkmcnt(7)\n\t"
      "s_barrier\n\t"
      "v_mfma_f32_16x16x32_bf16 v[20:23], v[100:103], a[8:11], v[20:23]\n\t"
      "ds_read_b128 a[48:51], v8 offset:0\n\t"
      "ds_read_b128 a[52:55], v60 offset:1024\n\t"
      "ds_read_b128 a[56:59], v61 offset:2048\n\t"
      "v_mfma_f32_16x16x32_bf16 v[20:23], v[116:119], a[12:15], v[20:23]\n\t"
      "v_accvgpr_read_b32 v52, a160\n\t"
      "v_accvgpr_read_b32 v53, a161\n\t"
      "v_mfma_f32_16x16x32_bf16 v[20:23], v[132:135], a[16:19], v[20:23]\n\t"
      "v_accvgpr_read_b32 v54, a162\n\t"
      "v_accvgpr_read_b32 v55, a163\n\t"
      "v_mfma_f32_16x16x32_bf16 v[20:23], v[148:151], a[20:23], v[20:23]\n\t"
      "v_cvt_pk_bf16_f32 v42, v52, v53\n\t"
      "v_cvt_pk_bf16_f32 v43, v54, v55\n\t"
      "v_mfma_f32_16x16x32_bf16 v[24:27], v[72:75], a[0:3], 0\n\t"
      "ds_write_b64 v0, v[42:43] offset:12576\n\t"
      "s_waitcnt lgkmcnt(3)\n\t"
      "v_mfma_f32_16x16x32_bf16 v[24:27], v[88:91], a[4:7], v[24:27]\n\t"
      "buffer_store_dwordx4 a[48:51], v9, s[44:47], s49 offen sc1\n\t"
      "v_mfma_f32_16x16x32_bf16 v[24:27], v[104:107], a[8:11], v[24:27]\n\t"
      "v_accvgpr_read_b32 v52, a164\n\t"
      "v_accvgpr_read_b32 v53, a165\n\t"
      "v_mfma_f32_16x16x32_bf16 v[24:27], v[120:123], a[12:15], v[24:27]\n\t"
      "v_accvgpr_read_b32 v54, a166\n\t"
      "v_accvgpr_read_b32 v55, a167\n\t"
      "v_mfma_f32_16x16x32_bf16 v[24:27], v[136:139], a[16:19], v[24:27]\n\t"
      "v_cvt_pk_bf16_f32 v44, v52, v53\n\t"
      "v_cvt_pk_bf16_f32 v45, v54, v55\n\t"
      "v_mfma_f32_16x16x32_bf16 v[24:27], v[152:155], a[20:23], v[24:27]\n\t"
      "ds_write_b64 v0, v[44:45] offset:12608\n\t"
      "s_waitcnt lgkmcnt(3)\n\t"
      "v_mfma_f32_16x16x32_bf16 v[28:31], v[76:79], a[0:3], 0\n\t"
      "buffer_store_dwordx4 a[52:55], v10, s[44:47], s49 offen offset:1024 sc1\n\t"
      "v_mfma_f32_16x16x32_bf16 v[28:31], v[92:95], a[4:7], v[28:31]\n\t"
      "v_accvgpr_read_b32 v52, a168\n\t"
      "v_accvgpr_read_b32 v53, a169\n\t"
      "v_mfma_f32_16x16x32_bf16 v[28:31], v[108:111], a[8:11], v[28:31]\n\t"
      "v_accvgpr_read_b32 v54, a170\n\t"
      "v_accvgpr_read_b32 v55, a171\n\t"
      "v_mfma_f32_16x16x32_bf16 v[28:31], v[124:127], a[12:15], v[28:31]\n\t"
      "v_cvt_pk_bf16_f32 v46, v52, v53\n\t"
      "v_cvt_pk_bf16_f32 v47, v54, v55\n\t"
      "v_mfma_f32_16x16x32_bf16 v[28:31], v[140:143], a[16:19], v[28:31]\n\t"
      "ds_write_b64 v0, v[46:47] offset:12640\n\t"
      "s_waitcnt lgkmcnt(3)\n\t"
      "v_mfma_f32_16x16x32_bf16 v[28:31], v[156:159], a[20:23], v[28:31]\n\t"
      "buffer_store_dwordx4 a[56:59], v11, s[44:47], s49 offen offset:2048 sc1\n\t"
      "v_mfma_f32_16x16x32_bf16 v[32:35], a[60:63], a[0:3], 0\n\t"
      "v_accvgpr_read_b32 v52, a172\n\t"
      "v_accvgpr_read_b32 v53, a173\n\t"
      "v_mfma_f32_16x16x32_bf16 v[32:35], a[68:71], a[4:7], v[32:35]\n\t"
      "v_accvgpr_read_b32 v54, a174\n\t"
      "v_accvgpr_read_b32 v55, a175\n\t"
      "v_mfma_f32_16x16x32_bf16 v[32:35], a[76:79], a[8:11], v[32:35]\n\t"
      "v_cvt_pk_bf16_f32 v48, v52, v53\n\t"
      "v_cvt_pk_bf16_f32 v49, v54, v55\n\t"
      "v_mfma_f32_16x16x32_bf16 v[32:35], a[84:87], a[12:15], v[32:35]\n\t"
      "ds_write_b64 v0, v[48:49] offset:12672\n\t"
      "v_mfma_f32_16x16x32_bf16 v[32:35], a[92:95], a[16:19], v[32:35]\n\t"
      "v_accvgpr_read_b32 v52, a176\n\t"
      "v_accvgpr_read_b32 v53, a177\n\t"
      "v_mfma_f32_16x16x32_bf16 v[32:35], a[100:103], a[20:23], v[32:35]\n\t"
      "v_accvgpr_read_b32 v54, a178\n\t"
      "v_accvgpr_read_b32 v55, a179\n\t"
      "v_mfma_f32_16x16x32_bf16 v[36:39], a[64:67], a[0:3], 0\n\t"
      "v_cvt_pk_bf16_f32 v50, v52, v53\n\t"
      "v_cvt_pk_bf16_f32 v51, v54, v55\n\t"
      "v_mfma_f32_16x16x32_bf16 v[36:39], a[72:75], a[4:7], v[36:39]\n\t"
      "ds_write_b64 v0, v[50:51] offset:12704\n\t"
      "v_mfma_f32_16x16x32_bf16 v[36:39], a[80:83], a[8:11], v[36:39]\n\t"
      "v_add_u32 v52, s51, v15\n\t"
      "v_xor_b32 v53, 64, v52\n\t"
      "v_mfma_f32_16x16x32_bf16 v[36:39], a[88:91], a[12:15], v[36:39]\n\t"
      "v_xor_b32 v54, 128, v52\n\t"
      "v_xor_b32 v55, 192, v52\n\t"
      "v_mfma_f32_16x16x32_bf16 v[36:39], a[96:99], a[16:19], v[36:39]\n\t"
      "s_waitcnt lgkmcnt(0)\n\t"
      "s_add_u32 s38, s36, s39\n\t"
      "s_sub_u32 s28, s36, s28\n\t"
      "s_sub_i32 s28, s28, 515\n\t"
      "s_max_i32 s28, s28, 0\n\t"
      "s_min_i32 s28, s28, 60\n\t"
      "s_and_b32 s39, s32, 15\n\t"
      "s_cselect_b32 s53, s29, 0\n\t"
      "s_memrealtime s[36:37]\n\t"
      "v_mfma_f32_16x16x32_bf16 v[36:39], a[104:107], a[20:23], v[36:39]\n\t"
      "s_add_u32 s39, s39, s53\n\t"
      "s_lshr_b64 s[32:33], s[32:33], 4\n\t"
      "s_add_u32 m0, s18, 86528\n\t"
      "v_mfma_f32_16x16x32_bf16 v[16:19], v[160:163], a[24:27], v[16:19]\n\t"
      "s_bitcmp1_b32 s91, 2\n\t"
      "s_cselect_b32 s54, 24576, 0\n\t"
      "s_add_u32 s54, s54, s14\n\t"
      "s_add_u32 s54, s54, 1572864\n\t"
      "v_mfma_f32_16x16x32_bf16 v[16:19], v[176:179], a[28:31], v[16:19]\n\t"
      "s_waitcnt vmcnt(6)\n\t"
      "s_barrier\n\t"
      "v_mfma_f32_16x16x32_bf16 v[16:19], v[192:195], a[32:35], v[16:19]\n\t"
      "ds_read_b128 a[0:3], v52 offset:0\n\t"
      "ds_read_b128 a[188:191], v8 offset:12544\n\t"
      "buffer_load_dwordx4 v5, s[40:43], s54 offen nt lds\n\t"
      "v_mfma_f32_16x16x32_bf16 v[16:19], v[208:211], a[36:39], v[16:19]\n\t"
      "ds_read_b128 a[4:7], v53 offset:0\n\t"
      "ds_read_b128 a[192:195], v60 offset:13568\n\t"
      "v_mfma_f32_16x16x32_bf16 v[16:19], v[224:227], a[40:43], v[16:19]\n\t"
      "ds_read_b128 a[8:11], v54 offset:0\n\t"
      "ds_read_b128 a[196:199], v61 offset:14592\n\t"
      "buffer_load_dwordx4 v6, s[40:43], s54 offen offset:1024 nt lds\n\t"
      "v_mfma_f32_16x16x32_bf16 v[16:19], v[240:243], a[44:47], v[16:19]\n\t"
      "ds_read_b128 a[12:15], v55 offset:0\n\t"
      "v_mfma_f32_16x16x32_bf16 v[20:23], v[164:167], a[24:27], v[20:23]\n\t"
      "ds_read_b128 a[16:19], v52 offset:256\n\t"
      "buffer_load_dwordx4 v7, s[40:43], s54 offen offset:2048 nt lds\n\t"
      "v_mfma_f32_16x16x32_bf16 v[20:23], v[180:183], a[28:31], v[20:23]\n\t"
      "ds_read_b128 a[20:23], v53 offset:256\n\t"
      "s_add_u32 m0, s18, s50\n\t"
      "v_mfma_f32_16x16x32_bf16 v[20:23], v[196:199], a[32:35], v[20:23]\n\t"
      "s_add_u32 s51, s51, 12288\n\t"
      "s_cmp_eq_u32 s51, 160256\n\t"
      "s_cselect_b32 s51, 25088, s51\n\t"
      "s_waitcnt lgkmcnt(7)\n\t"
      "v_mfma_f32_16x16x32_bf16 v[20:23], v[212:215], a[36:39], v[20:23]\n\t"
      "buffer_store_dwordx4 a[188:191], v12, s[44:47], s49 offen sc1\n\t"
      "v_mfma_f32_16x16x32_bf16 v[20:23], v[228:231], a[40:43], v[20:23]\n\t"
      "v_mfma_f32_16x16x32_bf16 v[20:23], v[244:247], a[44:47], v[20:23]\n\t"
      "s_waitcnt lgkmcnt(0)\n\t"
      "s_sub_u32 s53, s36, s38\n\t"
      "s_cmp_lt_i32 s53, 0\n\t"
      "v_mfma_f32_16x16x32_bf16 v[24:27], v[168:171], a[24:27], v[24:27]\n\t"
      "s_cbranch_scc1 .Lspinf_%=\n\t"
      ".Lspunf_%=:\n\t"
      "s_sub_i32 s53, s53, 30\n\t"
      "s_max_i32 s53, s53, 0\n\t"
      "v_mfma_f32_16x16x32_bf16 v[24:27], v[184:187], a[28:31], v[24:27]\n\t"
      "s_add_u32 s38, s38, s53\n\t"
      "s_add_u32 s38, s38, s39\n\t"
      "s_add_u32 s28, s28, s53\n\t"
      "v_mfma_f32_16x16x32_bf16 v[24:27], v[200:203], a[32:35], v[24:27]\n\t"
      "v_cvt_pk_bf16_f32 v40, v16, v17\n\t"
      "v_cvt_pk_bf16_f32 v41, v18, v19\n\t"
      "v_mfma_f32_16x16x32_bf16 v[24:27], v[216:219], a[36:39], v[24:27]\n\t"
      "ds_write_b64 v0, v[40:41] offset:0\n\t"
      "v_mfma_f32_16x16x32_bf16 v[24:27], v[232:235], a[40:43], v[24:27]\n\t"
      "buffer_load_dwordx4 v2, s[40:43], s48 offen nt lds\n\t"
      "v_mfma_f32_16x16x32_bf16 v[24:27], v[248:251], a[44:47], v[24:27]\n\t"
      "buffer_store_dwordx4 a[192:195], v13, s[44:47], s49 offen offset:1024 sc1\n\t"
      "v_mfma_f32_16x16x32_bf16 v[28:31], v[172:175], a[24:27], v[28:31]\n\t"
      "v_mfma_f32_16x16x32_bf16 v[28:31], v[188:191], a[28:31], v[28:31]\n\t"
      "v_cvt_pk_bf16_f32 v42, v20, v21\n\t"
      "v_cvt_pk_bf16_f32 v43, v22, v23\n\t"
      "v_mfma_f32_16x16x32_bf16 v[28:31], v[204:207], a[32:35], v[28:31]\n\t"
      "ds_write_b64 v0, v[42:43] offset:32\n\t"
      "v_mfma_f32_16x16x32_bf16 v[28:31], v[220:223], a[36:39], v[28:31]\n\t"
      "buffer_load_dwordx4 v3, s[40:43], s48 offen offset:1024 nt lds\n\t"
      "v_mfma_f32_16x16x32_bf16 v[28:31], v[236:239], a[40:43], v[28:31]\n\t"
      "buffer_store_dwordx4 a[196:199], v14, s[44:47], s49 offen offset:2048 sc1\n\t"
      "v_mfma_f32_16x16x32_bf16 v[28:31], v[252:255], a[44:47], v[28:31]\n\t"
      "s_add_u32 s58, s58, 786432\n\t"
      "s_lshr_b32 s59, s59, 1\n\t"
      "v_mfma_f32_16x16x32_bf16 v[32:35], a[108:111], a[24:27], v[32:35]\n\t"
      "s_bitcmp1_b32 s59, 0\n\t"
      "s_cselect_b32 s49, 24576, 0\n\t"
      "s_add_u32 s49, s49, s58\n\t"
      "v_mfma_f32_16x16x32_bf16 v[32:35], a[116:119], a[28:31], v[32:35]\n\t"
      "v_cvt_pk_bf16_f32 v44, v24, v25\n\t"
      "v_cvt_pk_bf16_f32 v45, v26, v27\n\t"
      "v_mfma_f32_16x16x32_bf16 v[32:35], a[124:127], a[32:35], v[32:35]\n\t"
      "ds_write_b64 v0, v[44:45] offset:64\n\t"
      "v_mfma_f32_16x16x32_bf16 v[32:35], a[132:135], a[36:39], v[32:35]\n\t"
      "buffer_load_dwordx4 v4, s[40:43], s48 offen offset:2048 nt lds\n\t"
      "v_mfma_f32_16x16x32_bf16 v[32:35], a[140:143], a[40:43], v[32:35]\n\t"
      "s_add_u32 s50, s50, 12288\n\t"
      "v_mfma_f32_16x16x32_bf16 v[32:35], a[148:151], a[44:47], v[32:35]\n\t"
      "v_cvt_pk_bf16_f32 v46, v28, v29\n\t"
      "v_cvt_pk_bf16_f32 v47, v30, v31\n\t"
      "v_mfma_f32_16x16x32_bf16 v[36:39], a[112:115], a[24:27], v[36:39]\n\t"
      "ds_write_b64 v0, v[46:47] offset:96\n\t"
      "v_mfma_f32_16x16x32_bf16 v[36:39], a[120:123], a[28:31], v[36:39]\n\t"
      "s_cmp_eq_u32 s50, 160256\n\t"
      "s_cselect_b32 s50, 25088, s50\n\t"
      "v_mfma_f32_16x16x32_bf16 v[36:39], a[128:131], a[32:35], v[36:39]\n\t"
      "v_mfma_f32_16x16x32_bf16 v[36:39], a[136:139], a[36:39], v[36:39]\n\t"
      "v_mfma_f32_16x16x32_bf16 v[36:39], a[144:147], a[40:43], v[36:39]\n\t"
      "v_cvt_pk_bf16_f32 v48, v32, v33\n\t"
      "v_cvt_pk_bf16_f32 v49, v34, v35\n\t"
      "v_mfma_f32_16x16x32_bf16 v[36:39], a[152:155], a[44:47], v[36:39]\n\t"
      "ds_write_b64 v0, v[48:49] offset:128\n\t"
      "s_cmp_eq_u32 s17, 6144\n\t"
      "s_cbranch_scc1 .Lmid1_%=\n\t"
      "s_cmp_eq_u32 s17, 12288\n\t"
      "s_cbranch_scc1 .Lmid2_%=\n\t"
      "s_cmp_eq_u32 s17, 18432\n\t"
      "s_cbranch_scc1 .Lmid3_%=\n\t"
      "s_branch .Lmid0_%=\n\t"
      ".Lloop0_%=:\n\t"
      "v_mfma_f32_16x16x32_bf16 v[16:19], v[64:67], a[0:3], 0\n\t"
      "ds_read_b128 a[24:27], v58 offset:256\n\t"
      "v_mfma_f32_16x16x32_bf16 v[16:19], v[80:83], a[4:7], v[16:19]\n\t"
      "ds_read_b128 a[28:31], v59 offset:256\n\t"
      "v_mfma_f32_16x16x32_bf16 v[16:19], v[96:99], a[8:11], v[16:19]\n\t"
      "ds_read_b128 a[32:35], v56 offset:512\n\t"
      "v_mfma_f32_16x16x32_bf16 v[16:19], v[112:115], a[12:15], v[16:19]\n\t"
      "ds_read_b128 a[36:39], v57 offset:512\n\t"
      "v_mfma_f32_16x16x32_bf16 v[16:19], v[128:131], a[16:19], v[16:19]\n\t"
      "ds_read_b128 a[40:43], v58 offset:512\n\t"
      "v_mfma_f32_16x16x32_bf16 v[16:19], v[144:147], a[20:23], v[16:19]\n\t"
      "ds_read_b128 a[44:47], v59 offset:512\n\t"
      "v_mfma_f32_16x16x32_bf16 v[20:23], v[68:71], a[0:3], 0\n\t"
      "v_cvt_pk_bf16_f32 v50, v36, v37\n\t"
      "v_cvt_pk_bf16_f32 v51, v38, v39\n\t"
      "v_mfma_f32_16x16x32_bf16 v[20:23], v[84:87], a[4:7], v[20:23]\n\t"
      "ds_write_b64 v0, v[50:51] offset:12704\n\t"
      "v_mfma_f32_16x16x32_bf16 v[20:23], v[100:103], a[8:11], v[20:23]\n\t"
      "v_add_u32 v52, s51, v15\n\t"
      "v_xor_b32 v53, 64, v52\n\t"
      "v_mfma_f32_16x16x32_bf16 v[20:23], v[116:119], a[12:15], v[20:23]\n\t"
      "v_xor_b32 v54, 128, v52\n\t"
      "v_xor_b32 v55, 192, v52\n\t"
      "v_mfma_f32_16x16x32_bf16 v[20:23], v[132:135], a[16:19], v[20:23]\n\t"
      "s_add_u32 s56, s56, 786432\n\t"
      "s_lshr_b32 s57, s57, 1\n\t"
      "v_mfma_f32_16x16x32_bf16 v[20:23], v[148:151], a[20:23], v[20:23]\n\t"
      "s_bitcmp1_b32 s57, 0\n\t"
      "s_cselect_b32 s48, 24576, 0\n\t"
      "s_add_u32 s48, s48, s56\n\t"
      "v_mfma_f32_16x16x32_bf16 v[24:27], v[72:75], a[0:3], 0\n\t"
      "v_mfma_f32_16x16x32_bf16 v[24:27], v[88:91], a[4:7], v[24:27]\n\t"
      "v_mfma_f32_16x16x32_bf16 v[24:27], v[104:107], a[8:11], v[24:27]\n\t"
      "s_waitcnt vmcnt(12)\n\t"
      "s_waitcnt lgkmcnt(0)\n\t"
      "s_barrier\n\t"
      "v_mfma_f32_16x16x32_bf16 v[24:27], v[120:123], a[12:15], v[24:27]\n\t"
      "v_mfma_f32_16x16x32_bf16 v[24:27], v[136:139], a[16:19], v[24:27]\n\t"
      "v_mfma_f32_16x16x32_bf16 v[24:27], v[152:155], a[20:23], v[24:27]\n\t"
      "v_mfma_f32_16x16x32_bf16 v[28:31], v[76:79], a[0:3], 0\n\t"
      "ds_read_b128 a[48:51], v8 offset:12544\n\t"
      "v_mfma_f32_16x16x32_bf16 v[28:31], v[92:95], a[4:7], v[28:31]\n\t"
      "ds_read_b128 a[52:55], v60 offset:13568\n\t"
      "v_mfma_f32_16x16x32_bf16 v[28:31], v[108:111], a[8:11], v[28:31]\n\t"
      "ds_read_b128 a[56:59], v61 offset:14592\n\t"
      "v_mfma_f32_16x16x32_bf16 v[28:31], v[124:127], a[12:15], v[28:31]\n\t"
      "s_and_b32 s39, s32, 15\n\t"
      "s_cselect_b32 s53, s29, 0\n\t"
      "v_mfma_f32_16x16x32_bf16 v[28:31], v[140:143], a[16:19], v[28:31]\n\t"
      "s_add_u32 s39, s39, s53\n\t"
      "s_lshr_b64 s[32:33], s[32:33], 4\n\t"
      "v_mfma_f32_16x16x32_bf16 v[28:31], v[156:159], a[20:23], v[28:31]\n\t"
      "s_cmp_le_u32 s52, 12\n\t"
      "s_cselect_b32 s53, 3, 0\n\t"
      "s_min_u32 s53, s28, s53\n\t"
      "v_mfma_f32_16x16x32_bf16 v[32:35], a[60:63], a[0:3], 0\n\t"
      "s_sub_u32 s28, s28, s53\n\t"
      "s_sub_u32 s39, s39, s53\n\t"
      "v_mfma_f32_16x16x32_bf16 v[32:35], a[68:71], a[4:7], v[32:35]\n\t"
      "s_waitcnt lgkmcnt(2)\n\t"
      "v_mfma_f32_16x16x32_bf16 v[32:35], a[76:79], a[8:11], v[32:35]\n\t"
      "buffer_store_dwordx4 a[48:51], v12, s[44:47], s49 offen sc1\n\t"
      "v_mfma_f32_16x16x32_bf16 v[32:35], a[84:87], a[12:15], v[32:35]\n\t"
      "v_mfma_f32_16x16x32_bf16 v[32:35], a[92:95], a[16:19], v[32:35]\n\t"
      "v_mfma_f32_16x16x32_bf16 v[32:35], a[100:103], a[20:23], v[32:35]\n\t"
      "s_add_u32 m0, s18, s50\n\t"
      "v_mfma_f32_16x16x32_bf16 v[36:39], a[64:67], a[0:3], 0\n\t"
      "buffer_load_dwordx4 v2, s[40:43], s48 offen nt lds\n\t"
      "v_mfma_f32_16x16x32_bf16 v[36:39], a[72:75], a[4:7], v[36:39]\n\t"
      "s_memrealtime s[36:37]\n\t"
      "v_mfma_f32_16x16x32_bf16 v[36:39], a[80:83], a[8:11], v[36:39]\n\t"
      "v_mfma_f32_16x16x32_bf16 v[36:39], a[88:91], a[12:15], v[36:39]\n\t"
      "s_waitcnt lgkmcnt(1)\n\t"
      "v_mfma_f32_16x16x32_bf16 v[36:39], a[96:99], a[16:19], v[36:39]\n\t"
      "buffer_store_dwordx4 a[52:55], v13, s[44:47], s49 offen offset:1024 sc1\n\t"
      "v_mfma_f32_16x16x32_bf16 v[36:39], a[104:107], a[20:23], v[36:39]\n\t"
      "v_mfma_f32_16x16x32_bf16 v[16:19], v[160:163], a[24:27], v[16:19]\n\t"
      "ds_read_b128 a[0:3], v52 offset:0\n\t"
      "v_mfma_f32_16x16x32_bf16 v[16:19], v[176:179], a[28:31], v[16:19]\n\t"
      "ds_read_b128 a[4:7], v53 offset:0\n\t"
      "v_mfma_f32_16x16x32_bf16 v[16:19], v[192:195], a[32:35], v[16:19]\n\t"
      "ds_read_b128 a[8:11], v54 offset:0\n\t"
      "v_mfma_f32_16x16x32_bf16 v[16:19], v[208:211], a[36:39], v[16:19]\n\t"
      "ds_read_b128 a[12:15], v55 offset:0\n\t"
      "v_mfma_f32_16x16x32_bf16 v[16:19], v[224:227], a[40:43], v[16:19]\n\t"
      "ds_read_b128 a[16:19], v52 offset:256\n\t"
      "v_mfma_f32_16x16x32_bf16 v[16:19], v[240:243], a[44:47], v[16:19]\n\t"
      "ds_read_b128 a[20:23], v53 offset:256\n\t"
      "v_mfma_f32_16x16x32_bf16 v[20:23], v[164:167], a[24:27], v[20:23]\n\t"
      "s_add_u32 s51, s51, 12288\n\t"
      "s_cmp_eq_u32 s51, 160256\n\t"
      "s_cselect_b32 s51, 25088, s51\n\t"
      "v_mfma_f32_16x16x32_bf16 v[20:23], v[180:183], a[28:31], v[20:23]\n\t"
      "s_waitcnt lgkmcnt(6)\n\t"
      "v_mfma_f32_16x16x32_bf16 v[20:23], v[196:199], a[32:35], v[20:23]\n\t"
      "buffer_store_dwordx4 a[56:59], v14, s[44:47], s49 offen offset:2048 sc1\n\t"
      "v_mfma_f32_16x16x32_bf16 v[20:23], v[212:215], a[36:39], v[20:23]\n\t"
      "v_mfma_f32_16x16x32_bf16 v[20:23], v[228:231], a[40:43], v[20:23]\n\t"
      "s_waitcnt lgkmcnt(0)\n\t"
      "s_sub_u32 s53, s36, s38\n\t"
      "s_cmp_lt_i32 s53, 0\n\t"
      "v_mfma_f32_16x16x32_bf16 v[20:23], v[244:247], a[44:47], v[20:23]\n\t"
      "s_cbranch_scc1 .Lspin0_%=\n\t"
      ".Lspun0_%=:\n\t"
      "s_sub_i32 s53, s53, 30\n\t"
      "s_max_i32 s53, s53, 0\n\t"
      "v_mfma_f32_16x16x32_bf16 v[24:27], v[168:171], a[24:27], v[24:27]\n\t"
      "s_add_u32 s38, s38, s53\n\t"
      "s_add_u32 s38, s38, s39\n\t"
      "s_add_u32 s28, s28, s53\n\t"
      "v_mfma_f32_16x16x32_bf16 v[24:27], v[184:187], a[28:31], v[24:27]\n\t"
      "v_cvt_pk_bf16_f32 v40, v16, v17\n\t"
      "v_cvt_pk_bf16_f32 v41, v18, v19\n\t"
      "v_mfma_f32_16x16x32_bf16 v[24:27], v[200:203], a[32:35], v[24:27]\n\t"
      "ds_write_b64 v0, v[40:41] offset:0\n\t"
      "v_mfma_f32_16x16x32_bf16 v[24:27], v[216:219], a[36:39], v[24:27]\n\t"
      "buffer_load_dwordx4 v3, s[40:43], s48 offen offset:1024 nt lds\n\t"
      "v_mfma_f32_16x16x32_bf16 v[24:27], v[232:235], a[40:43], v[24:27]\n\t"
      "s_add_u32 s58, s58, 786432\n\t"
      "s_lshr_b32 s59, s59, 1\n\t"
      "v_mfma_f32_16x16x32_bf16 v[24:27], v[248:251], a[44:47], v[24:27]\n\t"
      "s_bitcmp1_b32 s59, 0\n\t"
      "s_cselect_b32 s49, 24576, 0\n\t"
      "s_add_u32 s49, s49, s58\n\t"
      "v_mfma_f32_16x16x32_bf16 v[28:31], v[172:175], a[24:27], v[28:31]\n\t"
      "v_mfma_f32_16x16x32_bf16 v[28:31], v[188:191], a[28:31], v[28:31]\n\t"
      "v_cvt_pk_bf16_f32 v42, v20, v21\n\t"
      "v_cvt_pk_bf16_f32 v43, v22, v23\n\t"
      "v_mfma_f32_16x16x32_bf16 v[28:31], v[204:207], a[32:35], v[28:31]\n\t"
      "ds_write_b64 v0, v[42:43] offset:32\n\t"
      "v_mfma_f32_16x16x32_bf16 v[28:31], v[220:223], a[36:39], v[28:31]\n\t"
      "v_mfma_f32_16x16x32_bf16 v[28:31], v[236:239], a[40:43], v[28:31]\n\t"
      "v_mfma_f32_16x16x32_bf16 v[28:31], v[252:255], a[44:47], v[28:31]\n\t"
      "v_mfma_f32_16x16x32_bf16 v[32:35], a[108:111], a[24:27], v[32:35]\n\t"
      "v_mfma_f32_16x16x32_bf16 v[32:35], a[116:119], a[28:31], v[32:35]\n\t"
      "v_cvt_pk_bf16_f32 v44, v24, v25\n\t"
      "v_cvt_pk_bf16_f32 v45, v26, v27\n\t"
      "v_mfma_f32_16x16x32_bf16 v[32:35], a[124:127], a[32:35], v[32:35]\n\t"
      "ds_write_b64 v0, v[44:45] offset:64\n\t"
      "v_mfma_f32_16x16x32_bf16 v[32:35], a[132:135], a[36:39], v[32:35]\n\t"
      "buffer_load_dwordx4 v4, s[40:43], s48 offen offset:2048 nt lds\n\t"
      "v_mfma_f32_16x16x32_bf16 v[32:35], a[140:143], a[40:43], v[32:35]\n\t"
      "s_add_u32 s50, s50, 12288\n\t"
      "v_mfma_f32_16x16x32_bf16 v[32:35], a[148:151], a[44:47], v[32:35]\n\t"
      "v_cvt_pk_bf16_f32 v46, v28, v29\n\t"
      "v_cvt_pk_bf16_f32 v47, v30, v31\n\t"
      "v_mfma_f32_16x16x32_bf16 v[36:39], a[112:115], a[24:27], v[36:39]\n\t"
      "ds_write_b64 v0, v[46:47] offset:96\n\t"
      "v_mfma_f32_16x16x32_bf16 v[36:39], a[120:123], a[28:31], v[36:39]\n\t"
      "s_cmp_eq_u32 s50, 160256\n\t"
      "s_cselect_b32 s50, 25088, s50\n\t"
      "v_mfma_f32_16x16x32_bf16 v[36:39], a[128:131], a[32:35], v[36:39]\n\t"
      "v_mfma_f32_16x16x32_bf16 v[36:39], a[136:139], a[36:39], v[36:39]\n\t"
      "v_mfma_f32_16x16x32_bf16 v[36:39], a[144:147], a[40:43], v[36:39]\n\t"
      "v_cvt_pk_bf16_f32 v48, v32, v33\n\t"
      "v_cvt_pk_bf16_f32 v49, v34, v35\n\t"
      "v_mfma_f32_16x16x32_bf16 v[36:39], a[152:155], a[44:47], v[36:39]\n\t"
      "ds_write_b64 v0, v[48:49] offset:128\n\t"
      ".Lmid0_%=:\n\t"
      "v_mfma_f32_16x16x32_bf16 v[16:19], v[64:67], a[0:3], 0\n\t"
      "ds_read_b128 a[24:27], v54 offset:256\n\t"
      "v_mfma_f32_16x16x32_bf16 v[16:19], v[80:83], a[4:7], v[16:19]\n\t"
      "ds_read_b128 a[28:31], v55 offset:256\n\t"
      "v_mfma_f32_16x16x32_bf16 v[16:19], v[96:99], a[8:11], v[16:19]\n\t"
      "ds_read_b128 a[32:35], v52 offset:512\n\t"
      "v_mfma_f32_16x16x32_bf16 v[16:19], v[112:115], a[12:15], v[16:19]\n\t"
      "ds_read_b128 a[36:39], v53 offset:512\n\t"
      "v_mfma_f32_16x16x32_bf16 v[16:19], v[128:131], a[16:19], v[16:19]\n\t"
      "ds_read_b128 a[40:43], v54 offset:512\n\t"
      "v_mfma_f32_16x16x32_bf16 v[16:19], v[144:147], a[20:23], v[16:19]\n\t"
      "ds_read_b128 a[44:47], v55 offset:512\n\t"
      "v_mfma_f32_16x16x32_bf16 v[20:23], v[68:71], a[0:3], 0\n\t"
      "v_cvt_pk_bf16_f32 v50, v36, v37\n\t"
      "v_cvt_pk_bf16_f32 v51, v38, v39\n\t"
      "v_mfma_f32_16x16x32_bf16 v[20:23], v[84:87], a[4:7], v[20:23]\n\t"
      "ds_write_b64 v0, v[50:51] offset:160\n\t"
      "v_mfma_f32_16x16x32_bf16 v[20:23], v[100:103], a[8:11], v[20:23]\n\t"
      "v_add_u32 v56, s51, v15\n\t"
      "v_xor_b32 v57, 64, v56\n\t"
      "v_mfma_f32_16x16x32_bf16 v[20:23], v[116:119], a[12:15], v[20:23]\n\t"
      "v_xor_b32 v58, 128, v56\n\t"
      "v_xor_b32 v59, 192, v56\n\t"
      "v_mfma_f32_16x16x32_bf16 v[20:23], v[132:135], a[16:19], v[20:23]\n\t"
      "v_mfma_f32_16x16x32_bf16 v[20:23], v[148:151], a[20:23], v[20:23]\n\t"
      "v_mfma_f32_16x16x32_bf16 v[24:27], v[72:75], a[0:3], 0\n\t"
      "v_mfma_f32_16x16x32_bf16 v[24:27], v[88:91], a[4:7], v[24:27]\n\t"
      "v_mfma_f32_16x16x32_bf16 v[24:27], v[104:107], a[8:11], v[24:27]\n\t"
      "s_waitcnt vmcnt(12)\n\t"
      "s_waitcnt lgkmcnt(0)\n\t"
      "s_barrier\n\t"
      "v_mfma_f32_16x16x32_bf16 v[24:27], v[120:123], a[12:15], v[24:27]\n\t"
      "v_mfma_f32_16x16x32_bf16 v[24:27], v[136:139], a[16:19], v[24:27]\n\t"
      "v_mfma_f32_16x16x32_bf16 v[24:27], v[152:155], a[20:23], v[24:27]\n\t"
      "v_mfma_f32_16x16x32_bf16 v[28:31], v[76:79], a[0:3], 0\n\t"
      "ds_read_b128 a[48:51], v8 offset:0\n\t"
      "v_mfma_f32_16x16x32_bf16 v[28:31], v[92:95], a[4:7], v[28:31]\n\t"
      "ds_read_b128 a[52:55], v60 offset:1024\n\t"
      "v_mfma_f32_16x16x32_bf16 v[28:31], v[108:111], a[8:11], v[28:31]\n\t"
      "ds_read_b128 a[56:59], v61 offset:2048\n\t"
      "v_mfma_f32_16x16x32_bf16 v[28:31], v[124:127], a[12:15], v[28:31]\n\t"
      "s_and_b32 s39, s34, 15\n\t"
      "s_cselect_b32 s53, s29, 0\n\t"
      "v_mfma_f32_16x16x32_bf16 v[28:31], v[140:143], a[16:19], v[28:31]\n\t"
      "s_add_u32 s39, s39, s53\n\t"
      "s_lshr_b64 s[34:35], s[34:35], 4\n\t"
      "v_mfma_f32_16x16x32_bf16 v[28:31], v[156:159], a[20:23], v[28:31]\n\t"
      "s_cmp_le_u32 s52, 12\n\t"
      "s_cselect_b32 s53, 3, 0\n\t"
      "s_min_u32 s53, s28, s53\n\t"
      "v_mfma_f32_16x16x32_bf16 v[32:35], a[60:63], a[0:3], 0\n\t"
      "s_sub_u32 s28, s28, s53\n\t"
      "s_sub_u32 s39, s39, s53\n\t"
      "v_mfma_f32_16x16x32_bf16 v[32:35], a[68:71], a[4:7], v[32:35]\n\t"
      "s_waitcnt lgkmcnt(2)\n\t"
      "v_mfma_f32_16x16x32_bf16 v[32:35], a[76:79], a[8:11], v[32:35]\n\t"
      "buffer_store_dwordx4 a[48:51], v9, s[44:47], s49 offen sc1\n\t"
      "v_mfma_f32_16x16x32_bf16 v[32:35], a[84:87], a[12:15], v[32:35]\n\t"
      "v_mfma_f32_16x16x32_bf16 v[32:35], a[92:95], a[16:19], v[32:35]\n\t"
      "v_mfma_f32_16x16x32_bf16 v[32:35], a[100:103], a[20:23], v[32:35]\n\t"
      "s_add_u32 m0, s18, s50\n\t"
      "v_mfma_f32_16x16x32_bf16 v[36:39], a[64:67], a[0:3], 0\n\t"
      "buffer_load_dwordx4 v5, s[40:43], s48 offen nt lds\n\t"
      "v_mfma_f32_16x16x32_bf16 v[36:39], a[72:75], a[4:7], v[36:39]\n\t"
      "s_memrealtime s[36:37]\n\t"
      "v_mfma_f32_16x16x32_bf16 v[36:39], a[80:83], a[8:11], v[36:39]\n\t"
      "v_mfma_f32_16x16x32_bf16 v[36:39], a[88:91], a[12:15], v[36:39]\n\t"
      "s_waitcnt lgkmcnt(1)\n\t"
      "v_mfma_f32_16x16x32_bf16 v[36:39], a[96:99], a[16:19], v[36:39]\n\t"
      "buffer_store_dwordx4 a[52:55], v10, s[44:47], s49 offen offset:1024 sc1\n\t"
      "v_mfma_f32_16x16x32_bf16 v[36:39], a[104:107], a[20:23], v[36:39]\n\t"
      "v_mfma_f32_16x16x32_bf16 v[16:19], v[160:163], a[24:27], v[16:19]\n\t"
      "ds_read_b128 a[0:3], v56 offset:0\n\t"
      "v_mfma_f32_16x16x32_bf16 v[16:19], v[176:179], a[28:31], v[16:19]\n\t"
      "ds_read_b128 a[4:7], v57 offset:0\n\t"
      "v_mfma_f32_16x16x32_bf16 v[16:19], v[192:195], a[32:35], v[16:19]\n\t"
      "ds_read_b128 a[8:11], v58 offset:0\n\t"
      "v_mfma_f32_16x16x32_bf16 v[16:19], v[208:211], a[36:39], v[16:19]\n\t"
      "ds_read_b128 a[12:15], v59 offset:0\n\t"
      "v_mfma_f32_16x16x32_bf16 v[16:19], v[224:227], a[40:43], v[16:19]\n\t"
      "ds_read_b128 a[16:19], v56 offset:256\n\t"
      "v_mfma_f32_16x16x32_bf16 v[16:19], v[240:243], a[44:47], v[16:19]\n\t"
      "ds_read_b128 a[20:23], v57 offset:256\n\t"
      "v_mfma_f32_16x16x32_bf16 v[20:23], v[164:167], a[24:27], v[20:23]\n\t"
      "s_add_u32 s51, s51, 12288\n\t"
      "s_cmp_eq_u32 s51, 160256\n\t"
      "s_cselect_b32 s51, 25088, s51\n\t"
      "v_mfma_f32_16x16x32_bf16 v[20:23], v[180:183], a[28:31], v[20:23]\n\t"
      "s_waitcnt lgkmcnt(6)\n\t"
      "v_mfma_f32_16x16x32_bf16 v[20:23], v[196:199], a[32:35], v[20:23]\n\t"
      "buffer_store_dwordx4 a[56:59], v11, s[44:47], s49 offen offset:2048 sc1\n\t"
      "v_mfma_f32_16x16x32_bf16 v[20:23], v[212:215], a[36:39], v[20:23]\n\t"
      "v_mfma_f32_16x16x32_bf16 v[20:23], v[228:231], a[40:43], v[20:23]\n\t"
      "s_waitcnt lgkmcnt(0)\n\t"
      "s_sub_u32 s53, s36, s38\n\t"
      "s_cmp_lt_i32 s53, 0\n\t"
      "v_mfma_f32_16x16x32_bf16 v[20:23], v[244:247], a[44:47], v[20:23]\n\t"
      "s_cbranch_scc1 .Lspin1_%=\n\t"
      ".Lspun1_%=:\n\t"
      "s_sub_i32 s53, s53, 30\n\t"
      "s_max_i32 s53, s53, 0\n\t"
      "v_mfma_f32_16x16x32_bf16 v[24:27], v[168:171], a[24:27], v[24:27]\n\t"
      "s_add_u32 s38, s38, s53\n\t"
      "s_add_u32 s38, s38, s39\n\t"
      "s_add_u32 s28, s28, s53\n\t"
      "v_mfma_f32_16x16x32_bf16 v[24:27], v[184:187], a[28:31], v[24:27]\n\t"
      "v_cvt_pk_bf16_f32 v40, v16, v17\n\t"
      "v_cvt_pk_bf16_f32 v41, v18, v19\n\t"
      "v_mfma_f32_16x16x32_bf16 v[24:27], v[200:203], a[32:35], v[24:27]\n\t"
      "ds_write_b64 v0, v[40:41] offset:12544\n\t"
      "v_mfma_f32_16x16x32_bf16 v[24:27], v[216:219], a[36:39], v[24:27]\n\t"
      "buffer_load_dwordx4 v6, s[40:43], s48 offen offset:1024 nt lds\n\t"
      "v_mfma_f32_16x16x32_bf16 v[24:27], v[232:235], a[40:43], v[24:27]\n\t"
      "v_mfma_f32_16x16x32_bf16 v[24:27], v[248:251], a[44:47], v[24:27]\n\t"
      "v_mfma_f32_16x16x32_bf16 v[28:31], v[172:175], a[24:27], v[28:31]\n\t"
      "v_mfma_f32_16x16x32_bf16 v[28:31], v[188:191], a[28:31], v[28:31]\n\t"
      "v_cvt_pk_bf16_f32 v42, v20, v21\n\t"
      "v_cvt_pk_bf16_f32 v43, v22, v23\n\t"
      "v_mfma_f32_16x16x32_bf16 v[28:31], v[204:207], a[32:35], v[28:31]\n\t"
      "ds_write_b64 v0, v[42:43] offset:12576\n\t"
      "v_mfma_f32_16x16x32_bf16 v[28:31], v[220:223], a[36:39], v[28:31]\n\t"
      "v_mfma_f32_16x16x32_bf16 v[28:31], v[236:239], a[40:43], v[28:31]\n\t"
      "v_mfma_f32_16x16x32_bf16 v[28:31], v[252:255], a[44:47], v[28:31]\n\t"
      "v_mfma_f32_16x16x32_bf16 v[32:35], a[108:111], a[24:27], v[32:35]\n\t"
      "v_mfma_f32_16x16x32_bf16 v[32:35], a[116:119], a[28:31], v[32:35]\n\t"
      "v_cvt_pk_bf16_f32 v44, v24, v25\n\t"
      "v_cvt_pk_bf16_f32 v45, v26, v27\n\t"
      "v_mfma_f32_16x16x32_bf16 v[32:35], a[124:127], a[32:35], v[32:35]\n\t"
      "ds_write_b64 v0, v[44:45] offset:12608\n\t"
      "v_mfma_f32_16x16x32_bf16 v[32:35], a[132:135], a[36:39], v[32:35]\n\t"
      "buffer_load_dwordx4 v7, s[40:43], s48 offen offset:2048 nt lds\n\t"
      "v_mfma_f32_16x16x32_bf16 v[32:35], a[140:143], a[40:43], v[32:35]\n\t"
      "s_add_u32 s50, s50, 12288\n\t"
      "v_mfma_f32_16x16x32_bf16 v[32:35], a[148:151], a[44:47], v[32:35]\n\t"
      "v_cvt_pk_bf16_f32 v46, v28, v29\n\t"
      "v_cvt_pk_bf16_f32 v47, v30, v31\n\t"
      "v_mfma_f32_16x16x32_bf16 v[36:39], a[112:115], a[24:27], v[36:39]\n\t"
      "ds_write_b64 v0, v[46:47] offset:12640\n\t"
      "v_mfma_f32_16x16x32_bf16 v[36:39], a[120:123], a[28:31], v[36:39]\n\t"
      "s_cmp_eq_u32 s50, 160256\n\t"
      "s_cselect_b32 s50, 25088, s50\n\t"
      "v_mfma_f32_16x16x32_bf16 v[36:39], a[128:131], a[32:35], v[36:39]\n\t"
      "s_sub_u32 s52, s52, 1\n\t"
      "s_cmp_lg_u32 s52, 0\n\t"
      "v_mfma_f32_16x16x32_bf16 v[36:39], a[136:139], a[36:39], v[36:39]\n\t"
      "v_mfma_f32_16x16x32_bf16 v[36:39], a[144:147], a[40:43], v[36:39]\n\t"
      "v_cvt_pk_bf16_f32 v48, v32, v33\n\t"
      "v_cvt_pk_bf16_f32 v49, v34, v35\n\t"
      "v_mfma_f32_16x16x32_bf16 v[36:39], a[152:155], a[44:47], v[36:39]\n\t"
      "ds_write_b64 v0, v[48:49] offset:12672\n\t"
      "s_cbranch_scc1 .Lloop0_%=\n\t"
      "s_branch .Lendc_%=\n\t"
      ".Lloop1_%=:\n\t"
      "v_mfma_f32_16x16x32_bf16 v[16:19], v[64:67], a[0:3], 0\n\t"
      "ds_read_b128 a[24:27], v58 offset:256\n\t"
      "v_mfma_f32_16x16x32_bf16 v[16:19], v[80:83], a[4:7], v[16:19]\n\t"
      "ds_read_b128 a[28:31], v59 offset:256\n\t"
      "v_mfma_f32_16x16x32_bf16 v[16:19], v[96:99], a[8:11], v[16:19]\n\t"
      "ds_read_b128 a[32:35], v56 offset:512\n\t"
      "v_mfma_f32_16x16x32_bf16 v[16:19], v[112:115], a[12:15], v[16:19]\n\t"
      "ds_read_b128 a[36:39], v57 offset:512\n\t"
      "v_mfma_f32_16x16x32_bf16 v[16:19], v[128:131], a[16:19], v[16:19]\n\t"
      "ds_read_b128 a[40:43], v58 offset:512\n\t"
      "v_mfma_f32_16x16x32_bf16 v[16:19], v[144:147], a[20:23], v[16:19]\n\t"
      "ds_read_b128 a[44:47], v59 offset:512\n\t"
      "v_mfma_f32_16x16x32_bf16 v[20:23], v[68:71], a[0:3], 0\n\t"
      "v_cvt_pk_bf16_f32 v50, v36, v37\n\t"
      "v_cvt_pk_bf16_f32 v51, v38, v39\n\t"
      "v_mfma_f32_16x16x32_bf16 v[20:23], v[84:87], a[4:7], v[20:23]\n\t"
      "ds_write_b64 v0, v[50:51] offset:12704\n\t"
      "v_mfma_f32_16x16x32_bf16 v[20:23], v[100:103], a[8:11], v[20:23]\n\t"
      "v_add_u32 v52, s51, v15\n\t"
      "v_xor_b32 v53, 64, v52\n\t"
      "v_mfma_f32_16x16x32_bf16 v[20:23], v[116:119], a[12:15], v[20:23]\n\t"
      "v_xor_b32 v54, 128, v52\n\t"
      "v_xor_b32 v55, 192, v52\n\t"
      "v_mfma_f32_16x16x32_bf16 v[20:23], v[132:135], a[16:19], v[20:23]\n\t"
      "s_add_u32 s56, s56, 786432\n\t"
      "s_lshr_b32 s57, s57, 1\n\t"
      "v_mfma_f32_16x16x32_bf16 v[20:23], v[148:151], a[20:23], v[20:23]\n\t"
      "s_bitcmp1_b32 s57, 0\n\t"
      "s_cselect_b32 s48, 24576, 0\n\t"
      "s_add_u32 s48, s48, s56\n\t"
      "v_mfma_f32_16x16x32_bf16 v[24:27], v[72:75], a[0:3], 0\n\t"
      "v_mfma_f32_16x16x32_bf16 v[24:27], v[88:91], a[4:7], v[24:27]\n\t"
      "v_mfma_f32_16x16x32_bf16 v[24:27], v[104:107], a[8:11], v[24:27]\n\t"
      "v_mfma_f32_16x16x32_bf16 v[24:27], v[120:123], a[12:15], v[24:27]\n\t"
      "s_waitcnt vmcnt(12)\n\t"
      "s_waitcnt lgkmcnt(0)\n\t"
      "s_barrier\n\t"
      "v_mfma_f32_16x16x32_bf16 v[24:27], v[136:139], a[16:19], v[24:27]\n\t"
      "v_mfma_f32_16x16x32_bf16 v[24:27], v[152:155], a[20:23], v[24:27]\n\t"
      "v_mfma_f32_16x16x32_bf16 v[28:31], v[76:79], a[0:3], 0\n\t"
      "ds_read_b128 a[48:51], v8 offset:12544\n\t"
      "v_mfma_f32_16x16x32_bf16 v[28:31], v[92:95], a[4:7], v[28:31]\n\t"
      "ds_read_b128 a[52:55], v60 offset:13568\n\t"
      "v_mfma_f32_16x16x32_bf16 v[28:31], v[108:111], a[8:11], v[28:31]\n\t"
      "ds_read_b128 a[56:59], v61 offset:14592\n\t"
      "v_mfma_f32_16x16x32_bf16 v[28:31], v[124:127], a[12:15], v[28:31]\n\t"
      "v_mfma_f32_16x16x32_bf16 v[28:31], v[140:143], a[16:19], v[28:31]\n\t"
      "v_mfma_f32_16x16x32_bf16 v[28:31], v[156:159], a[20:23], v[28:31]\n\t"
      "v_mfma_f32_16x16x32_bf16 v[32:35], a[60:63], a[0:3], 0\n\t"
      "v_mfma_f32_16x16x32_bf16 v[32:35], a[68:71], a[4:7], v[32:35]\n\t"
      "s_waitcnt lgkmcnt(2)\n\t"
      "v_mfma_f32_16x16x32_bf16 v[32:35], a[76:79], a[8:11], v[32:35]\n\t"
      "buffer_store_dwordx4 a[48:51], v12, s[44:47], s49 offen sc1\n\t"
      "v_mfma_f32_16x16x32_bf16 v[32:35], a[84:87], a[12:15], v[32:35]\n\t"
      "v_mfma_f32_16x16x32_bf16 v[32:35], a[92:95], a[16:19], v[32:35]\n\t"
      "v_mfma_f32_16x16x32_bf16 v[32:35], a[100:103], a[20:23], v[32:35]\n\t"
      "s_add_u32 m0, s18, s50\n\t"
      "v_mfma_f32_16x16x32_bf16 v[36:39], a[64:67], a[0:3], 0\n\t"
      "buffer_load_dwordx4 v2, s[40:43], s48 offen nt lds\n\t"
      "v_mfma_f32_16x16x32_bf16 v[36:39], a[72:75], a[4:7], v[36:39]\n\t"
      "v_mfma_f32_16x16x32_bf16 v[36:39], a[80:83], a[8:11], v[36:39]\n\t"
      "v_mfma_f32_16x16x32_bf16 v[36:39], a[88:91], a[12:15], v[36:39]\n\t"
      "s_waitcnt lgkmcnt(1)\n\t"
      "v_mfma_f32_16x16x32_bf16 v[36:39], a[96:99], a[16:19], v[36:39]\n\t"
      "buffer_store_dwordx4 a[52:55], v13, s[44:47], s49 offen offset:1024 sc1\n\t"
      "v_mfma_f32_16x16x32_bf16 v[36:39], a[104:107], a[20:23], v[36:39]\n\t"
      "v_mfma_f32_16x16x32_bf16 v[16:19], v[160:163], a[24:27], v[16:19]\n\t"
      "ds_read_b128 a[0:3], v52 offset:0\n\t"
      "v_mfma_f32_16x16x32_bf16 v[16:19], v[176:179], a[28:31], v[16:19]\n\t"
      "ds_read_b128 a[4:7], v53 offset:0\n\t"
      "v_mfma_f32_16x16x32_bf16 v[16:19], v[192:195], a[32:35], v[16:19]\n\t"
      "ds_read_b128 a[8:11], v54 offset:0\n\t"
      "v_mfma_f32_16x16x32_bf16 v[16:19], v[208:211], a[36:39], v[16:19]\n\t"
      "ds_read_b128 a[12:15], v55 offset:0\n\t"
      "v_mfma_f32_16x16x32_bf16 v[16:19], v[224:227], a[40:43], v[16:19]\n\t"
      "ds_read_b128 a[16:19], v52 offset:256\n\t"
      "v_mfma_f32_16x16x32_bf16 v[16:19], v[240:243], a[44:47], v[16:19]\n\t"
      "ds_read_b128 a[20:23], v53 offset:256\n\t"
      "v_mfma_f32_16x16x32_bf16 v[20:23], v[164:167], a[24:27], v[20:23]\n\t"
      "s_add_u32 s51, s51, 12288\n\t"
      "s_cmp_eq_u32 s51, 160256\n\t"
      "s_cselect_b32 s51, 25088, s51\n\t"
      "v_mfma_f32_16x16x32_bf16 v[20:23], v[180:183], a[28:31], v[20:23]\n\t"
      "s_waitcnt lgkmcnt(6)\n\t"
      "v_mfma_f32_16x16x32_bf16 v[20:23], v[196:199], a[32:35], v[20:23]\n\t"
      "buffer_store_dwordx4 a[56:59], v14, s[44:47], s49 offen offset:2048 sc1\n\t"
      "v_mfma_f32_16x16x32_bf16 v[20:23], v[212:215], a[36:39], v[20:23]\n\t"
      "v_mfma_f32_16x16x32_bf16 v[20:23], v[228:231], a[40:43], v[20:23]\n\t"
      "s_waitcnt lgkmcnt(0)\n\t"
      "v_mfma_f32_16x16x32_bf16 v[20:23], v[244:247], a[44:47], v[20:23]\n\t"
      "v_mfma_f32_16x16x32_bf16 v[24:27], v[168:171], a[24:27], v[24:27]\n\t"
      "v_mfma_f32_16x16x32_bf16 v[24:27], v[184:187], a[28:31], v[24:27]\n\t"
      "v_cvt_pk_bf16_f32 v40, v16, v17\n\t"
      "v_cvt_pk_bf16_f32 v41, v18, v19\n\t"
      "v_mfma_f32_16x16x32_bf16 v[24:27], v[200:203], a[32:35], v[24:27]\n\t"
      "ds_write_b64 v0, v[40:41] offset:0\n\t"
      "v_mfma_f32_16x16x32_bf16 v[24:27], v[216:219], a[36:39], v[24:27]\n\t"
      "buffer_load_dwordx4 v3, s[40:43], s48 offen offset:1024 nt lds\n\t"
      "v_mfma_f32_16x16x32_bf16 v[24:27], v[232:235], a[40:43], v[24:27]\n\t"
      "s_add_u32 s58, s58, 786432\n\t"
      "s_lshr_b32 s59, s59, 1\n\t"
      "v_mfma_f32_16x16x32_bf16 v[24:27], v[248:251], a[44:47], v[24:27]\n\t"
      "s_bitcmp1_b32 s59, 0\n\t"
      "s_cselect_b32 s49, 24576, 0\n\t"
      "s_add_u32 s49, s49, s58\n\t"
      "v_mfma_f32_16x16x32_bf16 v[28:31], v[172:175], a[24:27], v[28:31]\n\t"
      "v_mfma_f32_16x16x32_bf16 v[28:31], v[188:191], a[28:31], v[28:31]\n\t"
      "v_cvt_pk_bf16_f32 v42, v20, v21\n\t"
      "v_cvt_pk_bf16_f32 v43, v22, v23\n\t"
      "v_mfma_f32_16x16x32_bf16 v[28:31], v[204:207], a[32:35], v[28:31]\n\t"
      "ds_write_b64 v0, v[42:43] offset:32\n\t"
      "v_mfma_f32_16x16x32_bf16 v[28:31], v[220:223], a[36:39], v[28:31]\n\t"
      "v_mfma_f32_16x16x32_bf16 v[28:31], v[236:239], a[40:43], v[28:31]\n\t"
      "v_mfma_f32_16x16x32_bf16 v[28:31], v[252:255], a[44:47], v[28:31]\n\t"
      "v_mfma_f32_16x16x32_bf16 v[32:35], a[108:111], a[24:27], v[32:35]\n\t"
      "v_mfma_f32_16x16x32_bf16 v[32:35], a[116:119], a[28:31], v[32:35]\n\t"
      "v_cvt_pk_bf16_f32 v44, v24, v25\n\t"
      "v_cvt_pk_bf16_f32 v45, v26, v27\n\t"
      "v_mfma_f32_16x16x32_bf16 v[32:35], a[124:127], a[32:35], v[32:35]\n\t"
      "ds_write_b64 v0, v[44:45] offset:64\n\t"
      "v_mfma_f32_16x16x32_bf16 v[32:35], a[132:135], a[36:39], v[32:35]\n\t"
      "buffer_load_dwordx4 v4, s[40:43], s48 offen offset:2048 nt lds\n\t"
      "v_mfma_f32_16x16x32_bf16 v[32:35], a[140:143], a[40:43], v[32:35]\n\t"
      "s_add_u32 s50, s50, 12288\n\t"
      "v_mfma_f32_16x16x32_bf16 v[32:35], a[148:151], a[44:47], v[32:35]\n\t"
      "v_cvt_pk_bf16_f32 v46, v28, v29\n\t"
      "v_cvt_pk_bf16_f32 v47, v30, v31\n\t"
      "v_mfma_f32_16x16x32_bf16 v[36:39], a[112:115], a[24:27], v[36:39]\n\t"
      "ds_write_b64 v0, v[46:47] offset:96\n\t"
      "v_mfma_f32_16x16x32_bf16 v[36:39], a[120:123], a[28:31], v[36:39]\n\t"
      "s_cmp_eq_u32 s50, 160256\n\t"
      "s_cselect_b32 s50, 25088, s50\n\t"
      "v_mfma_f32_16x16x32_bf16 v[36:39], a[128:131], a[32:35], v[36:39]\n\t"
      "v_mfma_f32_16x16x32_bf16 v[36:39], a[136:139], a[36:39], v[36:39]\n\t"
      "v_mfma_f32_16x16x32_bf16 v[36:39], a[144:147], a[40:43], v[36:39]\n\t"
      "v_cvt_pk_bf16_f32 v48, v32, v33\n\t"
      "v_cvt_pk_bf16_f32 v49, v34, v35\n\t"
      "v_mfma_f32_16x16x32_bf16 v[36:39], a[152:155], a[44:47], v[36:39]\n\t"
      "ds_write_b64 v0, v[48:49] offset:128\n\t"
      ".Lmid1_%=:\n\t"
      "v_mfma_f32_16x16x32_bf16 v[16:19], v[64:67], a[0:3], 0\n\t"
      "ds_read_b128 a[24:27], v54 offset:256\n\t"
      "v_mfma_f32_16x16x32_bf16 v[16:19], v[80:83], a[4:7], v[16:19]\n\t"
      "ds_read_b128 a[28:31], v55 offset:256\n\t"
      "v_mfma_f32_16x16x32_bf16 v[16:19], v[96:99], a[8:11], v[16:19]\n\t"
      "ds_read_b128 a[32:35], v52 offset:512\n\t"
      "v_mfma_f32_16x16x32_bf16 v[16:19], v[112:115], a[12:15], v[16:19]\n\t"
      "ds_read_b128 a[36:39], v53 offset:512\n\t"
      "v_mfma_f32_16x16x32_bf16 v[16:19], v[128:131], a[16:19], v[16:19]\n\t"
      "ds_read_b128 a[40:43], v54 offset:512\n\t"
      "v_mfma_f32_16x16x32_bf16 v[16:19], v[144:147], a[20:23], v[16:19]\n\t"
      "ds_read_b128 a[44:47], v55 offset:512\n\t"
      "v_mfma_f32_16x16x32_bf16 v[20:23], v[68:71], a[0:3], 0\n\t"
      "v_cvt_pk_bf16_f32 v50, v36, v37\n\t"
      "v_cvt_pk_bf16_f32 v51, v38, v39\n\t"
      "v_mfma_f32_16x16x32_bf16 v[20:23], v[84:87], a[4:7], v[20:23]\n\t"
      "ds_write_b64 v0, v[50:51] offset:160\n\t"
      "v_mfma_f32_16x16x32_bf16 v[20:23], v[100:103], a[8:11], v[20:23]\n\t"
      "v_add_u32 v56, s51, v15\n\t"
      "v_xor_b32 v57, 64, v56\n\t"
      "v_mfma_f32_16x16x32_bf16 v[20:23], v[116:119], a[12:15], v[20:23]\n\t"
      "v_xor_b32 v58, 128, v56\n\t"
      "v_xor_b32 v59, 192, v56\n\t"
      "v_mfma_f32_16x16x32_bf16 v[20:23], v[132:135], a[16:19], v[20:23]\n\t"
      "v_mfma_f32_16x16x32_bf16 v[20:23], v[148:151], a[20:23], v[20:23]\n\t"
      "v_mfma_f32_16x16x32_bf16 v[24:27], v[72:75], a[0:3], 0\n\t"
      "v_mfma_f32_16x16x32_bf16 v[24:27], v[88:91], a[4:7], v[24:27]\n\t"
      "v_mfma_f32_16x16x32_bf16 v[24:27], v[104:107], a[8:11], v[24:27]\n\t"
      "v_mfma_f32_16x16x32_bf16 v[24:27], v[120:123], a[12:15], v[24:27]\n\t"
      "s_waitcnt vmcnt(12)\n\t"
      "s_waitcnt lgkmcnt(0)\n\t"
      "s_barrier\n\t"
      "v_mfma_f32_16x16x32_bf16 v[24:27], v[136:139], a[16:19], v[24:27]\n\t"
      "v_mfma_f32_16x16x32_bf16 v[24:27], v[152:155], a[20:23], v[24:27]\n\t"
      "v_mfma_f32_16x16x32_bf16 v[28:31], v[76:79], a[0:3], 0\n\t"
      "ds_read_b128 a[48:51], v8 offset:0\n\t"
      "v_mfma_f32_16x16x32_bf16 v[28:31], v[92:95], a[4:7], v[28:31]\n\t"
      "ds_read_b128 a[52:55], v60 offset:1024\n\t"
      "v_mfma_f32_16x16x32_bf16 v[28:31], v[108:111], a[8:11], v[28:31]\n\t"
      "ds_read_b128 a[56:59], v61 offset:2048\n\t"
      "v_mfma_f32_16x16x32_bf16 v[28:31], v[124:127], a[12:15], v[28:31]\n\t"
      "v_mfma_f32_16x16x32_bf16 v[28:31], v[140:143], a[16:19], v[28:31]\n\t"
      "v_mfma_f32_16x16x32_bf16 v[28:31], v[156:159], a[20:23], v[28:31]\n\t"
      "v_mfma_f32_16x16x32_bf16 v[32:35], a[60:63], a[0:3], 0\n\t"
      "v_mfma_f32_16x16x32_bf16 v[32:35], a[68:71], a[4:7], v[32:35]\n\t"
      "s_waitcnt lgkmcnt(2)\n\t"
      "v_mfma_f32_16x16x32_bf16 v[32:35], a[76:79], a[8:11], v[32:35]\n\t"
      "buffer_store_dwordx4 a[48:51], v9, s[44:47], s49 offen sc1\n\t"
      "v_mfma_f32_16x16x32_bf16 v[32:35], a[84:87], a[12:15], v[32:35]\n\t"
      "v_mfma_f32_16x16x32_bf16 v[32:35], a[92:95], a[16:19], v[32:35]\n\t"
      "v_mfma_f32_16x16x32_bf16 v[32:35], a[100:103], a[20:23], v[32:35]\n\t"
      "s_add_u32 m0, s18, s50\n\t"
      "v_mfma_f32_16x16x32_bf16 v[36:39], a[64:67], a[0:3], 0\n\t"
      "buffer_load_dwordx4 v5, s[40:43], s48 offen nt lds\n\t"
      "v_mfma_f32_16x16x32_bf16 v[36:39], a[72:75], a[4:7], v[36:39]\n\t"
      "v_mfma_f32_16x16x32_bf16 v[36:39], a[80:83], a[8:11], v[36:39]\n\t"
      "v_mfma_f32_16x16x32_bf16 v[36:39], a[88:91], a[12:15], v[36:39]\n\t"
      "s_waitcnt lgkmcnt(1)\n\t"
      "v_mfma_f32_16x16x32_bf16 v[36:39], a[96:99], a[16:19], v[36:39]\n\t"
      "buffer_store_dwordx4 a[52:55], v10, s[44:47], s49 offen offset:1024 sc1\n\t"
      "v_mfma_f32_16x16x32_bf16 v[36:39], a[104:107], a[20:23], v[36:39]\n\t"
      "v_mfma_f32_16x16x32_bf16 v[16:19], v[160:163], a[24:27], v[16:19]\n\t"
      "ds_read_b128 a[0:3], v56 offset:0\n\t"
      "v_mfma_f32_16x16x32_bf16 v[16:19], v[176:179], a[28:31], v[16:19]\n\t"
      "ds_read_b128 a[4:7], v57 offset:0\n\t"
      "v_mfma_f32_16x16x32_bf16 v[16:19], v[192:195], a[32:35], v[16:19]\n\t"
      "ds_read_b128 a[8:11], v58 offset:0\n\t"
      "v_mfma_f32_16x16x32_bf16 v[16:19], v[208:211], a[36:39], v[16:19]\n\t"
      "ds_read_b128 a[12:15], v59 offset:0\n\t"
      "v_mfma_f32_16x16x32_bf16 v[16:19], v[224:227], a[40:43], v[16:19]\n\t"
      "ds_read_b128 a[16:19], v56 offset:256\n\t"
      "v_mfma_f32_16x16x32_bf16 v[16:19], v[240:243], a[44:47], v[16:19]\n\t"
      "ds_read_b128 a[20:23], v57 offset:256\n\t"
      "v_mfma_f32_16x16x32_bf16 v[20:23], v[164:167], a[24:27], v[20:23]\n\t"
      "s_add_u32 s51, s51, 12288\n\t"
      "s_cmp_eq_u32 s51, 160256\n\t"
      "s_cselect_b32 s51, 25088, s51\n\t"
      "v_mfma_f32_16x16x32_bf16 v[20:23], v[180:183], a[28:31], v[20:23]\n\t"
      "s_waitcnt lgkmcnt(6)\n\t"
      "v_mfma_f32_16x16x32_bf16 v[20:23], v[196:199], a[32:35], v[20:23]\n\t"
      "buffer_store_dwordx4 a[56:59], v11, s[44:47], s49 offen offset:2048 sc1\n\t"
      "v_mfma_f32_16x16x32_bf16 v[20:23], v[212:215], a[36:39], v[20:23]\n\t"
      "v_mfma_f32_16x16x32_bf16 v[20:23], v[228:231], a[40:43], v[20:23]\n\t"
      "s_waitcnt lgkmcnt(0)\n\t"
      "v_mfma_f32_16x16x32_bf16 v[20:23], v[244:247], a[44:47], v[20:23]\n\t"
      "v_mfma_f32_16x16x32_bf16 v[24:27], v[168:171], a[24:27], v[24:27]\n\t"
      "v_mfma_f32_16x16x32_bf16 v[24:27], v[184:187], a[28:31], v[24:27]\n\t"
      "v_cvt_pk_bf16_f32 v40, v16, v17\n\t"
      "v_cvt_pk_bf16_f32 v41, v18, v19\n\t"
      "v_mfma_f32_16x16x32_bf16 v[24:27], v[200:203], a[32:35], v[24:27]\n\t"
      "ds_write_b64 v0, v[40:41] offset:12544\n\t"
      "v_mfma_f32_16x16x32_bf16 v[24:27], v[216:219], a[36:39], v[24:27]\n\t"
      "buffer_load_dwordx4 v6, s[40:43], s48 offen offset:1024 nt lds\n\t"
      "v_mfma_f32_16x16x32_bf16 v[24:27], v[232:235], a[40:43], v[24:27]\n\t"
      "v_mfma_f32_16x16x32_bf16 v[24:27], v[248:251], a[44:47], v[24:27]\n\t"
      "v_mfma_f32_16x16x32_bf16 v[28:31], v[172:175], a[24:27], v[28:31]\n\t"
      "v_mfma_f32_16x16x32_bf16 v[28:31], v[188:191], a[28:31], v[28:31]\n\t"
      "v_cvt_pk_bf16_f32 v42, v20, v21\n\t"
      "v_cvt_pk_bf16_f32 v43, v22, v23\n\t"
      "v_mfma_f32_16x16x32_bf16 v[28:31], v[204:207], a[32:35], v[28:31]\n\t"
      "ds_write_b64 v0, v[42:43] offset:12576\n\t"
      "v_mfma_f32_16x16x32_bf16 v[28:31], v[220:223], a[36:39], v[28:31]\n\t"
      "v_mfma_f32_16x16x32_bf16 v[28:31], v[236:239], a[40:43], v[28:31]\n\t"
      "v_mfma_f32_16x16x32_bf16 v[28:31], v[252:255], a[44:47], v[28:31]\n\t"
      "v_mfma_f32_16x16x32_bf16 v[32:35], a[108:111], a[24:27], v[32:35]\n\t"
      "v_mfma_f32_16x16x32_bf16 v[32:35], a[116:119], a[28:31], v[32:35]\n\t"
      "v_cvt_pk_bf16_f32 v44, v24, v25\n\t"
      "v_cvt_pk_bf16_f32 v45, v26, v27\n\t"
      "v_mfma_f32_16x16x32_bf16 v[32:35], a[124:127], a[32:35], v[32:35]\n\t"
      "ds_write_b64 v0, v[44:45] offset:12608\n\t"
      "v_mfma_f32_16x16x32_bf16 v[32:35], a[132:135], a[36:39], v[32:35]\n\t"
      "buffer_load_dwordx4 v7, s[40:43], s48 offen offset:2048 nt lds\n\t"
      "v_mfma_f32_16x16x32_bf16 v[32:35], a[140:143], a[40:43], v[32:35]\n\t"
      "s_add_u32 s50, s50, 12288\n\t"
      "v_mfma_f32_16x16x32_bf16 v[32:35], a[148:151], a[44:47], v[32:35]\n\t"
      "v_cvt_pk_bf16_f32 v46, v28, v29\n\t"
      "v_cvt_pk_bf16_f32 v47, v30, v31\n\t"
      "v_mfma_f32_16x16x32_bf16 v[36:39], a[112:115], a[24:27], v[36:39]\n\t"
      "ds_write_b64 v0, v[46:47] offset:12640\n\t"
      "v_mfma_f32_16x16x32_bf16 v[36:39], a[120:123], a[28:31], v[36:39]\n\t"
      "s_cmp_eq_u32 s50, 160256\n\t"
      "s_cselect_b32 s50, 25088, s50\n\t"
      "v_mfma_f32_16x16x32_bf16 v[36:39], a[128:131], a[32:35], v[36:39]\n\t"
      "s_sub_u32 s52, s52, 1\n\t"
      "s_cmp_lg_u32 s52, 0\n\t"
      "v_mfma_f32_16x16x32_bf16 v[36:39], a[136:139], a[36:39], v[36:39]\n\t"
      "v_mfma_f32_16x16x32_bf16 v[36:39], a[144:147], a[40:43], v[36:39]\n\t"
      "v_cvt_pk_bf16_f32 v48, v32, v33\n\t"
      "v_cvt_pk_bf16_f32 v49, v34, v35\n\t"
      "v_mfma_f32_16x16x32_bf16 v[36:39], a[152:155], a[44:47], v[36:39]\n\t"
      "ds_write_b64 v0, v[48:49] offset:12672\n\t"
      "s_cbranch_scc1 .Lloop1_%=\n\t"
      "s_branch .Lendc_%=\n\t"
      ".Lloop2_%=:\n\t"
      "v_mfma_f32_16x16x32_bf16 v[16:19], v[64:67], a[0:3], 0\n\t"
      "ds_read_b128 a[24:27], v58 offset:256\n\t"
      "v_mfma_f32_16x16x32_bf16 v[16:19], v[80:83], a[4:7], v[16:19]\n\t"
      "ds_read_b128 a[28:31], v59 offset:256\n\t"
      "v_mfma_f32_16x16x32_bf16 v[16:19], v[96:99], a[8:11], v[16:19]\n\t"
      "ds_read_b128 a[32:35], v56 offset:512\n\t"
      "v_mfma_f32_16x16x32_bf16 v[16:19], v[112:115], a[12:15], v[16:19]\n\t"
      "ds_read_b128 a[36:39], v57 offset:512\n\t"
      "v_mfma_f32_16x16x32_bf16 v[16:19], v[128:131], a[16:19], v[16:19]\n\t"
      "ds_read_b128 a[40:43], v58 offset:512\n\t"
      "v_mfma_f32_16x16x32_bf16 v[16:19], v[144:147], a[20:23], v[16:19]\n\t"
      "ds_read_b128 a[44:47], v59 offset:512\n\t"
      "v_mfma_f32_16x16x32_bf16 v[20:23], v[68:71], a[0:3], 0\n\t"
      "v_cvt_pk_bf16_f32 v50, v36, v37\n\t"
      "v_cvt_pk_bf16_f32 v51, v38, v39\n\t"
      "v_mfma_f32_16x16x32_bf16 v[20:23], v[84:87], a[4:7], v[20:23]\n\t"
      "ds_write_b64 v0, v[50:51] offset:12704\n\t"
      "v_mfma_f32_16x16x32_bf16 v[20:23], v[100:103], a[8:11], v[20:23]\n\t"
      "v_add_u32 v52, s51, v15\n\t"
      "v_xor_b32 v53, 64, v52\n\t"
      "v_mfma_f32_16x16x32_bf16 v[20:23], v[116:119], a[12:15], v[20:23]\n\t"
      "v_xor_b32 v54, 128, v52\n\t"
      "v_xor_b32 v55, 192, v52\n\t"
      "v_mfma_f32_16x16x32_bf16 v[20:23], v[132:135], a[16:19], v[20:23]\n\t"
      "s_add_u32 s56, s56, 786432\n\t"
      "s_lshr_b32 s57, s57, 1\n\t"
      "v_mfma_f32_16x16x32_bf16 v[20:23], v[148:151], a[20:23], v[20:23]\n\t"
      "s_bitcmp1_b32 s57, 0\n\t"
      "s_cselect_b32 s48, 24576, 0\n\t"
      "s_add_u32 s48, s48, s56\n\t"
      "v_mfma_f32_16x16x32_bf16 v[24:27], v[72:75], a[0:3], 0\n\t"
      "v_mfma_f32_16x16x32_bf16 v[24:27], v[88:91], a[4:7], v[24:27]\n\t"
      "v_mfma_f32_16x16x32_bf16 v[24:27], v[104:107], a[8:11], v[24:27]\n\t"
      "v_mfma_f32_16x16x32_bf16 v[24:27], v[120:123], a[12:15], v[24:27]\n\t"
      "v_mfma_f32_16x16x32_bf16 v[24:27], v[136:139], a[16:19], v[24:27]\n\t"
      "s_waitcnt vmcnt(12)\n\t"
      "s_waitcnt lgkmcnt(0)\n\t"
      "s_barrier\n\t"
      "v_mfma_f32_16x16x32_bf16 v[24:27], v[152:155], a[20:23], v[24:27]\n\t"
      "v_mfma_f32_16x16x32_bf16 v[28:31], v[76:79], a[0:3], 0\n\t"
      "ds_read_b128 a[48:51], v8 offset:12544\n\t"
      "v_mfma_f32_16x16x32_bf16 v[28:31], v[92:95], a[4:7], v[28:31]\n\t"
      "ds_read_b128 a[52:55], v60 offset:13568\n\t"
      "v_mfma_f32_16x16x32_bf16 v[28:31], v[108:111], a[8:11], v[28:31]\n\t"
      "ds_read_b128 a[56:59], v61 offset:14592\n\t"
      "v_mfma_f32_16x16x32_bf16 v[28:31], v[124:127], a[12:15], v[28:31]\n\t"
      "v_mfma_f32_16x16x32_bf16 v[28:31], v[140:143], a[16:19], v[28:31]\n\t"
      "v_mfma_f32_16x16x32_bf16 v[28:31], v[156:159], a[20:23], v[28:31]\n\t"
      "v_mfma_f32_16x16x32_bf16 v[32:35], a[60:63], a[0:3], 0\n\t"
      "v_mfma_f32_16x16x32_bf16 v[32:35], a[68:71], a[4:7], v[32:35]\n\t"
      "s_waitcnt lgkmcnt(2)\n\t"
      "v_mfma_f32_16x16x32_bf16 v[32:35], a[76:79], a[8:11], v[32:35]\n\t"
      "buffer_store_dwordx4 a[48:51], v12, s[44:47], s49 offen sc1\n\t"
      "v_mfma_f32_16x16x32_bf16 v[32:35], a[84:87], a[12:15], v[32:35]\n\t"
      "v_mfma_f32_16x16x32_bf16 v[32:35], a[92:95], a[16:19], v[32:35]\n\t"
      "v_mfma_f32_16x16x32_bf16 v[32:35], a[100:103], a[20:23], v[32:35]\n\t"
      "s_add_u32 m0, s18, s50\n\t"
      "v_mfma_f32_16x16x32_bf16 v[36:39], a[64:67], a[0:3], 0\n\t"
      "buffer_load_dwordx4 v2, s[40:43], s48 offen nt lds\n\t"
      "v_mfma_f32_16x16x32_bf16 v[36:39], a[72:75], a[4:7], v[36:39]\n\t"
      "v_mfma_f32_16x16x32_bf16 v[36:39], a[80:83], a[8:11], v[36:39]\n\t"
      "v_mfma_f32_16x16x32_bf16 v[36:39], a[88:91], a[12:15], v[36:39]\n\t"
      "s_waitcnt lgkmcnt(1)\n\t"
      "v_mfma_f32_16x16x32_bf16 v[36:39], a[96:99], a[16:19], v[36:39]\n\t"
      "buffer_store_dwordx4 a[52:55], v13, s[44:47], s49 offen offset:1024 sc1\n\t"
      "v_mfma_f32_16x16x32_bf16 v[36:39], a[104:107], a[20:23], v[36:39]\n\t"
      "v_mfma_f32_16x16x32_bf16 v[16:19], v[160:163], a[24:27], v[16:19]\n\t"
      "ds_read_b128 a[0:3], v52 offset:0\n\t"
      "v_mfma_f32_16x16x32_bf16 v[16:19], v[176:179], a[28:31], v[16:19]\n\t"
      "ds_read_b128 a[4:7], v53 offset:0\n\t"
      "v_mfma_f32_16x16x32_bf16 v[16:19], v[192:195], a[32:35], v[16:19]\n\t"
      "ds_read_b128 a[8:11], v54 offset:0\n\t"
      "v_mfma_f32_16x16x32_bf16 v[16:19], v[208:211], a[36:39], v[16:19]\n\t"
      "ds_read_b128 a[12:15], v55 offset:0\n\t"
      "v_mfma_f32_16x16x32_bf16 v[16:19], v[224:227], a[40:43], v[16:19]\n\t"
      "ds_read_b128 a[16:19], v52 offset:256\n\t"
      "v_mfma_f32_16x16x32_bf16 v[16:19], v[240:243], a[44:47], v[16:19]\n\t"
      "ds_read_b128 a[20:23], v53 offset:256\n\t"
      "v_mfma_f32_16x16x32_bf16 v[20:23], v[164:167], a[24:27], v[20:23]\n\t"
      "s_add_u32 s51, s51, 12288\n\t"
      "s_cmp_eq_u32 s51, 160256\n\t"
      "s_cselect_b32 s51, 25088, s51\n\t"
      "v_mfma_f32_16x16x32_bf16 v[20:23], v[180:183], a[28:31], v[20:23]\n\t"
      "s_waitcnt lgkmcnt(6)\n\t"
      "v_mfma_f32_16x16x32_bf16 v[20:23], v[196:199], a[32:35], v[20:23]\n\t"
      "buffer_store_dwordx4 a[56:59], v14, s[44:47], s49 offen offset:2048 sc1\n\t"
      "v_mfma_f32_16x16x32_bf16 v[20:23], v[212:215], a[36:39], v[20:23]\n\t"
      "v_mfma_f32_16x16x32_bf16 v[20:23], v[228:231], a[40:43], v[20:23]\n\t"
      "s_waitcnt lgkmcnt(0)\n\t"
      "v_mfma_f32_16x16x32_bf16 v[20:23], v[244:247], a[44:47], v[20:23]\n\t"
      "v_mfma_f32_16x16x32_bf16 v[24:27], v[168:171], a[24:27], v[24:27]\n\t"
      "v_mfma_f32_16x16x32_bf16 v[24:27], v[184:187], a[28:31], v[24:27]\n\t"
      "v_cvt_pk_bf16_f32 v40, v16, v17\n\t"
      "v_cvt_pk_bf16_f32 v41, v18, v19\n\t"
      "v_mfma_f32_16x16x32_bf16 v[24:27], v[200:203], a[32:35], v[24:27]\n\t"
      "ds_write_b64 v0, v[40:41] offset:0\n\t"
      "v_mfma_f32_16x16x32_bf16 v[24:27], v[216:219], a[36:39], v[24:27]\n\t"
      "buffer_load_dwordx4 v3, s[40:43], s48 offen offset:1024 nt lds\n\t"
      "v_mfma_f32_16x16x32_bf16 v[24:27], v[232:235], a[40:43], v[24:27]\n\t"
      "s_add_u32 s58, s58, 786432\n\t"
      "s_lshr_b32 s59, s59, 1\n\t"
      "v_mfma_f32_16x16x32_bf16 v[24:27], v[248:251], a[44:47], v[24:27]\n\t"
      "s_bitcmp1_b32 s59, 0\n\t"
      "s_cselect_b32 s49, 24576, 0\n\t"
      "s_add_u32 s49, s49, s58\n\t"
      "v_mfma_f32_16x16x32_bf16 v[28:31], v[172:175], a[24:27], v[28:31]\n\t"
      "v_mfma_f32_16x16x32_bf16 v[28:31], v[188:191], a[28:31], v[28:31]\n\t"
      "v_cvt_pk_bf16_f32 v42, v20, v21\n\t"
      "v_cvt_pk_bf16_f32 v43, v22, v23\n\t"
      "v_mfma_f32_16x16x32_bf16 v[28:31], v[204:207], a[32:35], v[28:31]\n\t"
      "ds_write_b64 v0, v[42:43] offset:32\n\t"
      "v_mfma_f32_16x16x32_bf16 v[28:31], v[220:223], a[36:39], v[28:31]\n\t"
      "v_mfma_f32_16x16x32_bf16 v[28:31], v[236:239], a[40:43], v[28:31]\n\t"
      "v_mfma_f32_16x16x32_bf16 v[28:31], v[252:255], a[44:47], v[28:31]\n\t"
      "v_mfma_f32_16x16x32_bf16 v[32:35], a[108:111], a[24:27], v[32:35]\n\t"
      "v_mfma_f32_16x16x32_bf16 v[32:35], a[116:119], a[28:31], v[32:35]\n\t"
      "v_cvt_pk_bf16_f32 v44, v24, v25\n\t"
      "v_cvt_pk_bf16_f32 v45, v26, v27\n\t"
      "v_mfma_f32_16x16x32_bf16 v[32:35], a[124:127], a[32:35], v[32:35]\n\t"
      "ds_write_b64 v0, v[44:45] offset:64\n\t"
      "v_mfma_f32_16x16x32_bf16 v[32:35], a[132:135], a[36:39], v[32:35]\n\t"
      "buffer_load_dwordx4 v4, s[40:43], s48 offen offset:2048 nt lds\n\t"
      "v_mfma_f32_16x16x32_bf16 v[32:35], a[140:143], a[40:43], v[32:35]\n\t"
      "s_add_u32 s50, s50, 12288\n\t"
      "v_mfma_f32_16x16x32_bf16 v[32:35], a[148:151], a[44:47], v[32:35]\n\t"
      "v_cvt_pk_bf16_f32 v46, v28, v29\n\t"
      "v_cvt_pk_bf16_f32 v47, v30, v31\n\t"
      "v_mfma_f32_16x16x32_bf16 v[36:39], a[112:115], a[24:27], v[36:39]\n\t"
      "ds_write_b64 v0, v[46:47] offset:96\n\t"
      "v_mfma_f32_16x16x32_bf16 v[36:39], a[120:123], a[28:31], v[36:39]\n\t"
      "s_cmp_eq_u32 s50, 160256\n\t"
      "s_cselect_b32 s50, 25088, s50\n\t"
      "v_mfma_f32_16x16x32_bf16 v[36:39], a[128:131], a[32:35], v[36:39]\n\t"
      "v_mfma_f32_16x16x32_bf16 v[36:39], a[136:139], a[36:39], v[36:39]\n\t"
      "v_mfma_f32_16x16x32_bf16 v[36:39], a[144:147], a[40:43], v[36:39]\n\t"
      "v_cvt_pk_bf16_f32 v48, v32, v33\n\t"
      "v_cvt_pk_bf16_f32 v49, v34, v35\n\t"
      "v_mfma_f32_16x16x32_bf16 v[36:39], a[152:155], a[44:47], v[36:39]\n\t"
      "ds_write_b64 v0, v[48:49] offset:128\n\t"
      ".Lmid2_%=:\n\t"
      "v_mfma_f32_16x16x32_bf16 v[16:19], v[64:67], a[0:3], 0\n\t"
      "ds_read_b128 a[24:27], v54 offset:256\n\t"
      "v_mfma_f32_16x16x32_bf16 v[16:19], v[80:83], a[4:7], v[16:19]\n\t"
      "ds_read_b128 a[28:31], v55 offset:256\n\t"
      "v_mfma_f32_16x16x32_bf16 v[16:19], v[96:99], a[8:11], v[16:19]\n\t"
      "ds_read_b128 a[32:35], v52 offset:512\n\t"
      "v_mfma_f32_16x16x32_bf16 v[16:19], v[112:115], a[12:15], v[16:19]\n\t"
      "ds_read_b128 a[36:39], v53 offset:512\n\t"
      "v_mfma_f32_16x16x32_bf16 v[16:19], v[128:131], a[16:19], v[16:19]\n\t"
      "ds_read_b128 a[40:43], v54 offset:512\n\t"
      "v_mfma_f32_16x16x32_bf16 v[16:19], v[144:147], a[20:23], v[16:19]\n\t"
      "ds_read_b128 a[44:47], v55 offset:512\n\t"
      "v_mfma_f32_16x16x32_bf16 v[20:23], v[68:71], a[0:3], 0\n\t"
      "v_cvt_pk_bf16_f32 v50, v36, v37\n\t"
      "v_cvt_pk_bf16_f32 v51, v38, v39\n\t"
      "v_mfma_f32_16x16x32_bf16 v[20:23], v[84:87], a[4:7], v[20:23]\n\t"
      "ds_write_b64 v0, v[50:51] offset:160\n\t"
      "v_mfma_f32_16x16x32_bf16 v[20:23], v[100:103], a[8:11], v[20:23]\n\t"
      "v_add_u32 v56, s51, v15\n\t"
      "v_xor_b32 v57, 64, v56\n\t"
      "v_mfma_f32_16x16x32_bf16 v[20:23], v[116:119], a[12:15], v[20:23]\n\t"
      "v_xor_b32 v58, 128, v56\n\t"
      "v_xor_b32 v59, 192, v56\n\t"
      "v_mfma_f32_16x16x32_bf16 v[20:23], v[132:135], a[16:19], v[20:23]\n\t"
      "v_mfma_f32_16x16x32_bf16 v[20:23], v[148:151], a[20:23], v[20:23]\n\t"
      "v_mfma_f32_16x16x32_bf16 v[24:27], v[72:75], a[0:3], 0\n\t"
      "v_mfma_f32_16x16x32_bf16 v[24:27], v[88:91], a[4:7], v[24:27]\n\t"
      "v_mfma_f32_16x16x32_bf16 v[24:27], v[104:107], a[8:11], v[24:27]\n\t"
      "v_mfma_f32_16x16x32_bf16 v[24:27], v[120:123], a[12:15], v[24:27]\n\t"
      "v_mfma_f32_16x16x32_bf16 v[24:27], v[136:139], a[16:19], v[24:27]\n\t"
      "s_waitcnt vmcnt(12)\n\t"
      "s_waitcnt lgkmcnt(0)\n\t"
      "s_barrier\n\t"
      "v_mfma_f32_16x16x32_bf16 v[24:27], v[152:155], a[20:23], v[24:27]\n\t"
      "v_mfma_f32_16x16x32_bf16 v[28:31], v[76:79], a[0:3], 0\n\t"
      "ds_read_b128 a[48:51], v8 offset:0\n\t"
      "v_mfma_f32_16x16x32_bf16 v[28:31], v[92:95], a[4:7], v[28:31]\n\t"
      "ds_read_b128 a[52:55], v60 offset:1024\n\t"
      "v_mfma_f32_16x16x32_bf16 v[28:31], v[108:111], a[8:11], v[28:31]\n\t"
      "ds_read_b128 a[56:59], v61 offset:2048\n\t"
      "v_mfma_f32_16x16x32_bf16 v[28:31], v[124:127], a[12:15], v[28:31]\n\t"
      "v_mfma_f32_16x16x32_bf16 v[28:31], v[140:143], a[16:19], v[28:31]\n\t"
      "v_mfma_f32_16x16x32_bf16 v[28:31], v[156:159], a[20:23], v[28:31]\n\t"
      "v_mfma_f32_16x16x32_bf16 v[32:35], a[60:63], a[0:3], 0\n\t"
      "v_mfma_f32_16x16x32_bf16 v[32:35], a[68:71], a[4:7], v[32:35]\n\t"
      "s_waitcnt lgkmcnt(2)\n\t"
      "v_mfma_f32_16x16x32_bf16 v[32:35], a[76:79], a[8:11], v[32:35]\n\t"
      "buffer_store_dwordx4 a[48:51], v9, s[44:47], s49 offen sc1\n\t"
      "v_mfma_f32_16x16x32_bf16 v[32:35], a[84:87], a[12:15], v[32:35]\n\t"
      "v_mfma_f32_16x16x32_bf16 v[32:35], a[92:95], a[16:19], v[32:35]\n\t"
      "v_mfma_f32_16x16x32_bf16 v[32:35], a[100:103], a[20:23], v[32:35]\n\t"
      "s_add_u32 m0, s18, s50\n\t"
      "v_mfma_f32_16x16x32_bf16 v[36:39], a[64:67], a[0:3], 0\n\t"
      "buffer_load_dwordx4 v5, s[40:43], s48 offen nt lds\n\t"
      "v_mfma_f32_16x16x32_bf16 v[36:39], a[72:75], a[4:7], v[36:39]\n\t"
      "v_mfma_f32_16x16x32_bf16 v[36:39], a[80:83], a[8:11], v[36:39]\n\t"
      "v_mfma_f32_16x16x32_bf16 v[36:39], a[88:91], a[12:15], v[36:39]\n\t"
      "s_waitcnt lgkmcnt(1)\n\t"
      "v_mfma_f32_16x16x32_bf16 v[36:39], a[96:99], a[16:19], v[36:39]\n\t"
      "buffer_store_dwordx4 a[52:55], v10, s[44:47], s49 offen offset:1024 sc1\n\t"
      "v_mfma_f32_16x16x32_bf16 v[36:39], a[104:107], a[20:23], v[36:39]\n\t"
      "v_mfma_f32_16x16x32_bf16 v[16:19], v[160:163], a[24:27], v[16:19]\n\t"
      "ds_read_b128 a[0:3], v56 offset:0\n\t"
      "v_mfma_f32_16x16x32_bf16 v[16:19], v[176:179], a[28:31], v[16:19]\n\t"
      "ds_read_b128 a[4:7], v57 offset:0\n\t"
      "v_mfma_f32_16x16x32_bf16 v[16:19], v[192:195], a[32:35], v[16:19]\n\t"
      "ds_read_b128 a[8:11], v58 offset:0\n\t"
      "v_mfma_f32_16x16x32_bf16 v[16:19], v[208:211], a[36:39], v[16:19]\n\t"
      "ds_read_b128 a[12:15], v59 offset:0\n\t"
      "v_mfma_f32_16x16x32_bf16 v[16:19], v[224:227], a[40:43], v[16:19]\n\t"
      "ds_read_b128 a[16:19], v56 offset:256\n\t"
      "v_mfma_f32_16x16x32_bf16 v[16:19], v[240:243], a[44:47], v[16:19]\n\t"
      "ds_read_b128 a[20:23], v57 offset:256\n\t"
      "v_mfma_f32_16x16x32_bf16 v[20:23], v[164:167], a[24:27], v[20:23]\n\t"
      "s_add_u32 s51, s51, 12288\n\t"
      "s_cmp_eq_u32 s51, 160256\n\t"
      "s_cselect_b32 s51, 25088, s51\n\t"
      "v_mfma_f32_16x16x32_bf16 v[20:23], v[180:183], a[28:31], v[20:23]\n\t"
      "s_waitcnt lgkmcnt(6)\n\t"
      "v_mfma_f32_16x16x32_bf16 v[20:23], v[196:199], a[32:35], v[20:23]\n\t"
      "buffer_store_dwordx4 a[56:59], v11, s[44:47], s49 offen offset:2048 sc1\n\t"
      "v_mfma_f32_16x16x32_bf16 v[20:23], v[212:215], a[36:39], v[20:23]\n\t"
      "v_mfma_f32_16x16x32_bf16 v[20:23], v[228:231], a[40:43], v[20:23]\n\t"
      "s_waitcnt lgkmcnt(0)\n\t"
      "v_mfma_f32_16x16x32_bf16 v[20:23], v[244:247], a[44:47], v[20:23]\n\t"
      "v_mfma_f32_16x16x32_bf16 v[24:27], v[168:171], a[24:27], v[24:27]\n\t"
      "v_mfma_f32_16x16x32_bf16 v[24:27], v[184:187], a[28:31], v[24:27]\n\t"
      "v_cvt_pk_bf16_f32 v40, v16, v17\n\t"
      "v_cvt_pk_bf16_f32 v41, v18, v19\n\t"
      "v_mfma_f32_16x16x32_bf16 v[24:27], v[200:203], a[32:35], v[24:27]\n\t"
      "ds_write_b64 v0, v[40:41] offset:12544\n\t"
      "v_mfma_f32_16x16x32_bf16 v[24:27], v[216:219], a[36:39], v[24:27]\n\t"
      "buffer_load_dwordx4 v6, s[40:43], s48 offen offset:1024 nt lds\n\t"
      "v_mfma_f32_16x16x32_bf16 v[24:27], v[232:235], a[40:43], v[24:27]\n\t"
      "v_mfma_f32_16x16x32_bf16 v[24:27], v[248:251], a[44:47], v[24:27]\n\t"
      "v_mfma_f32_16x16x32_bf16 v[28:31], v[172:175], a[24:27], v[28:31]\n\t"
      "v_mfma_f32_16x16x32_bf16 v[28:31], v[188:191], a[28:31], v[28:31]\n\t"
      "v_cvt_pk_bf16_f32 v42, v20, v21\n\t"
      "v_cvt_pk_bf16_f32 v43, v22, v23\n\t"
      "v_mfma_f32_16x16x32_bf16 v[28:31], v[204:207], a[32:35], v[28:31]\n\t"
      "ds_write_b64 v0, v[42:43] offset:12576\n\t"
      "v_mfma_f32_16x16x32_bf16 v[28:31], v[220:223], a[36:39], v[28:31]\n\t"
      "v_mfma_f32_16x16x32_bf16 v[28:31], v[236:239], a[40:43], v[28:31]\n\t"
      "v_mfma_f32_16x16x32_bf16 v[28:31], v[252:255], a[44:47], v[28:31]\n\t"
      "v_mfma_f32_16x16x32_bf16 v[32:35], a[108:111], a[24:27], v[32:35]\n\t"
      "v_mfma_f32_16x16x32_bf16 v[32:35], a[116:119], a[28:31], v[32:35]\n\t"
      "v_cvt_pk_bf16_f32 v44, v24, v25\n\t"
      "v_cvt_pk_bf16_f32 v45, v26, v27\n\t"
      "v_mfma_f32_16x16x32_bf16 v[32:35], a[124:127], a[32:35], v[32:35]\n\t"
      "ds_write_b64 v0, v[44:45] offset:12608\n\t"
      "v_mfma_f32_16x16x32_bf16 v[32:35], a[132:135], a[36:39], v[32:35]\n\t"
      "buffer_load_dwordx4 v7, s[40:43], s48 offen offset:2048 nt lds\n\t"
      "v_mfma_f32_16x16x32_bf16 v[32:35], a[140:143], a[40:43], v[32:35]\n\t"
      "s_add_u32 s50, s50, 12288\n\t"
      "v_mfma_f32_16x16x32_bf16 v[32:35], a[148:151], a[44:47], v[32:35]\n\t"
      "v_cvt_pk_bf16_f32 v46, v28, v29\n\t"
      "v_cvt_pk_bf16_f32 v47, v30, v31\n\t"
      "v_mfma_f32_16x16x32_bf16 v[36:39], a[112:115], a[24:27], v[36:39]\n\t"
      "ds_write_b64 v0, v[46:47] offset:12640\n\t"
      "v_mfma_f32_16x16x32_bf16 v[36:39], a[120:123], a[28:31], v[36:39]\n\t"
      "s_cmp_eq_u32 s50, 160256\n\t"
      "s_cselect_b32 s50, 25088, s50\n\t"
      "v_mfma_f32_16x16x32_bf16 v[36:39], a[128:131], a[32:35], v[36:39]\n\t"
      "s_sub_u32 s52, s52, 1\n\t"
      "s_cmp_lg_u32 s52, 0\n\t"
      "v_mfma_f32_16x16x32_bf16 v[36:39], a[136:139], a[36:39], v[36:39]\n\t"
      "v_mfma_f32_16x16x32_bf16 v[36:39], a[144:147], a[40:43], v[36:39]\n\t"
      "v_cvt_pk_bf16_f32 v48, v32, v33\n\t"
      "v_cvt_pk_bf16_f32 v49, v34, v35\n\t"
      "v_mfma_f32_16x16x32_bf16 v[36:39], a[152:155], a[44:47], v[36:39]\n\t"
      "ds_write_b64 v0, v[48:49] offset:12672\n\t"
      "s_cbranch_scc1 .Lloop2_%=\n\t"
      "s_branch .Lendc_%=\n\t"
      ".Lloop3_%=:\n\t"
      "v_mfma_f32_16x16x32_bf16 v[16:19], v[64:67], a[0:3], 0\n\t"
      "ds_read_b128 a[24:27], v58 offset:256\n\t"
      "v_mfma_f32_16x16x32_bf16 v[16:19], v[80:83], a[4:7], v[16:19]\n\t"
      "ds_read_b128 a[28:31], v59 offset:256\n\t"
      "v_mfma_f32_16x16x32_bf16 v[16:19], v[96:99], a[8:11], v[16:19]\n\t"
      "ds_read_b128 a[32:35], v56 offset:512\n\t"
      "v_mfma_f32_16x16x32_bf16 v[16:19], v[112:115], a[12:15], v[16:19]\n\t"
      "ds_read_b128 a[36:39], v57 offset:512\n\t"
      "v_mfma_f32_16x16x32_bf16 v[16:19], v[128:131], a[16:19], v[16:19]\n\t"
      "ds_read_b128 a[40:43], v58 offset:512\n\t"
      "v_mfma_f32_16x16x32_bf16 v[16:19], v[144:147], a[20:23], v[16:19]\n\t"
      "ds_read_b128 a[44:47], v59 offset:512\n\t"
      "v_mfma_f32_16x16x32_bf16 v[20:23], v[68:71], a[0:3], 0\n\t"
      "v_cvt_pk_bf16_f32 v50, v36, v37\n\t"
      "v_cvt_pk_bf16_f32 v51, v38, v39\n\t"
      "v_mfma_f32_16x16x32_bf16 v[20:23], v[84:87], a[4:7], v[20:23]\n\t"
      "ds_write_b64 v0, v[50:51] offset:12704\n\t"
      "v_mfma_f32_16x16x32_bf16 v[20:23], v[100:103], a[8:11], v[20:23]\n\t"
      "v_add_u32 v52, s51, v15\n\t"
      "v_xor_b32 v53, 64, v52\n\t"
      "v_mfma_f32_16x16x32_bf16 v[20:23], v[116:119], a[12:15], v[20:23]\n\t"
      "v_xor_b32 v54, 128, v52\n\t"
      "v_xor_b32 v55, 192, v52\n\t"
      "v_mfma_f32_16x16x32_bf16 v[20:23], v[132:135], a[16:19], v[20:23]\n\t"
      "s_add_u32 s56, s56, 786432\n\t"
      "s_lshr_b32 s57, s57, 1\n\t"
      "v_mfma_f32_16x16x32_bf16 v[20:23], v[148:151], a[20:23], v[20:23]\n\t"
      "s_bitcmp1_b32 s57, 0\n\t"
      "s_cselect_b32 s48, 24576, 0\n\t"
      "s_add_u32 s48, s48, s56\n\t"
      "v_mfma_f32_16x16x32_bf16 v[24:27], v[72:75], a[0:3], 0\n\t"
      "v_mfma_f32_16x16x32_bf16 v[24:27], v[88:91], a[4:7], v[24:27]\n\t"
      "v_mfma_f32_16x16x32_bf16 v[24:27], v[104:107], a[8:11], v[24:27]\n\t"
      "v_mfma_f32_16x16x32_bf16 v[24:27], v[120:123], a[12:15], v[24:27]\n\t"
      "v_mfma_f32_16x16x32_bf16 v[24:27], v[136:139], a[16:19], v[24:27]\n\t"
      "v_mfma_f32_16x16x32_bf16 v[24:27], v[152:155], a[20:23], v[24:27]\n\t"
      "s_waitcnt vmcnt(12)\n\t"
      "s_waitcnt lgkmcnt(0)\n\t"
      "s_barrier\n\t"
      "v_mfma_f32_16x16x32_bf16 v[28:31], v[76:79], a[0:3], 0\n\t"
      "ds_read_b128 a[48:51], v8 offset:12544\n\t"
      "v_mfma_f32_16x16x32_bf16 v[28:31], v[92:95], a[4:7], v[28:31]\n\t"
      "ds_read_b128 a[52:55], v60 offset:13568\n\t"
      "v_mfma_f32_16x16x32_bf16 v[28:31], v[108:111], a[8:11], v[28:31]\n\t"
      "ds_read_b128 a[56:59], v61 offset:14592\n\t"
      "v_mfma_f32_16x16x32_bf16 v[28:31], v[124:127], a[12:15], v[28:31]\n\t"
      "v_mfma_f32_16x16x32_bf16 v[28:31], v[140:143], a[16:19], v[28:31]\n\t"
      "v_mfma_f32_16x16x32_bf16 v[28:31], v[156:159], a[20:23], v[28:31]\n\t"
      "v_mfma_f32_16x16x32_bf16 v[32:35], a[60:63], a[0:3], 0\n\t"
      "v_mfma_f32_16x16x32_bf16 v[32:35], a[68:71], a[4:7], v[32:35]\n\t"
      "s_waitcnt lgkmcnt(2)\n\t"
      "v_mfma_f32_16x16x32_bf16 v[32:35], a[76:79], a[8:11], v[32:35]\n\t"
      "buffer_store_dwordx4 a[48:51], v12, s[44:47], s49 offen sc1\n\t"
      "v_mfma_f32_16x16x32_bf16 v[32:35], a[84:87], a[12:15], v[32:35]\n\t"
      "v_mfma_f32_16x16x32_bf16 v[32:35], a[92:95], a[16:19], v[32:35]\n\t"
      "v_mfma_f32_16x16x32_bf16 v[32:35], a[100:103], a[20:23], v[32:35]\n\t"
      "s_add_u32 m0, s18, s50\n\t"
      "v_mfma_f32_16x16x32_bf16 v[36:39], a[64:67], a[0:3], 0\n\t"
      "buffer_load_dwordx4 v2, s[40:43], s48 offen nt lds\n\t"
      "v_mfma_f32_16x16x32_bf16 v[36:39], a[72:75], a[4:7], v[36:39]\n\t"
      "v_mfma_f32_16x16x32_bf16 v[36:39], a[80:83], a[8:11], v[36:39]\n\t"
      "v_mfma_f32_16x16x32_bf16 v[36:39], a[88:91], a[12:15], v[36:39]\n\t"
      "s_waitcnt lgkmcnt(1)\n\t"
      "v_mfma_f32_16x16x32_bf16 v[36:39], a[96:99], a[16:19], v[36:39]\n\t"
      "buffer_store_dwordx4 a[52:55], v13, s[44:47], s49 offen offset:1024 sc1\n\t"
      "v_mfma_f32_16x16x32_bf16 v[36:39], a[104:107], a[20:23], v[36:39]\n\t"
      "v_mfma_f32_16x16x32_bf16 v[16:19], v[160:163], a[24:27], v[16:19]\n\t"
      "ds_read_b128 a[0:3], v52 offset:0\n\t"
      "v_mfma_f32_16x16x32_bf16 v[16:19], v[176:179], a[28:31], v[16:19]\n\t"
      "ds_read_b128 a[4:7], v53 offset:0\n\t"
      "v_mfma_f32_16x16x32_bf16 v[16:19], v[192:195], a[32:35], v[16:19]\n\t"
      "ds_read_b128 a[8:11], v54 offset:0\n\t"
      "v_mfma_f32_16x16x32_bf16 v[16:19], v[208:211], a[36:39], v[16:19]\n\t"
      "ds_read_b128 a[12:15], v55 offset:0\n\t"
      "v_mfma_f32_16x16x32_bf16 v[16:19], v[224:227], a[40:43], v[16:19]\n\t"
      "ds_read_b128 a[16:19], v52 offset:256\n\t"
      "v_mfma_f32_16x16x32_bf16 v[16:19], v[240:243], a[44:47], v[16:19]\n\t"
      "ds_read_b128 a[20:23], v53 offset:256\n\t"
      "v_mfma_f32_16x16x32_bf16 v[20:23], v[164:167], a[24:27], v[20:23]\n\t"
      "s_add_u32 s51, s51, 12288\n\t"
      "s_cmp_eq_u32 s51, 160256\n\t"
      "s_cselect_b32 s51, 25088, s51\n\t"
      "v_mfma_f32_16x16x32_bf16 v[20:23], v[180:183], a[28:31], v[20:23]\n\t"
      "s_waitcnt lgkmcnt(6)\n\t"
      "v_mfma_f32_16x16x32_bf16 v[20:23], v[196:199], a[32:35], v[20:23]\n\t"
      "buffer_store_dwordx4 a[56:59], v14, s[44:47], s49 offen offset:2048 sc1\n\t"
      "v_mfma_f32_16x16x32_bf16 v[20:23], v[212:215], a[36:39], v[20:23]\n\t"
      "v_mfma_f32_16x16x32_bf16 v[20:23], v[228:231], a[40:43], v[20:23]\n\t"
      "s_waitcnt lgkmcnt(0)\n\t"
      "v_mfma_f32_16x16x32_bf16 v[20:23], v[244:247], a[44:47], v[20:23]\n\t"
      "v_mfma_f32_16x16x32_bf16 v[24:27], v[168:171], a[24:27], v[24:27]\n\t"
      "v_mfma_f32_16x16x32_bf16 v[24:27], v[184:187], a[28:31], v[24:27]\n\t"
      "v_cvt_pk_bf16_f32 v40, v16, v17\n\t"
      "v_cvt_pk_bf16_f32 v41, v18, v19\n\t"
      "v_mfma_f32_16x16x32_bf16 v[24:27], v[200:203], a[32:35], v[24:27]\n\t"
      "ds_write_b64 v0, v[40:41] offset:0\n\t"
      "v_mfma_f32_16x16x32_bf16 v[24:27], v[216:219], a[36:39], v[24:27]\n\t"
      "buffer_load_dwordx4 v3, s[40:43], s48 offen offset:1024 nt lds\n\t"
      "v_mfma_f32_16x16x32_bf16 v[24:27], v[232:235], a[40:43], v[24:27]\n\t"
      "s_add_u32 s58, s58, 786432\n\t"
      "s_lshr_b32 s59, s59, 1\n\t"
      "v_mfma_f32_16x16x32_bf16 v[24:27], v[248:251], a[44:47], v[24:27]\n\t"
      "s_bitcmp1_b32 s59, 0\n\t"
      "s_cselect_b32 s49, 24576, 0\n\t"
      "s_add_u32 s49, s49, s58\n\t"
      "v_mfma_f32_16x16x32_bf16 v[28:31], v[172:175], a[24:27], v[28:31]\n\t"
      "v_mfma_f32_16x16x32_bf16 v[28:31], v[188:191], a[28:31], v[28:31]\n\t"
      "v_cvt_pk_bf16_f32 v42, v20, v21\n\t"
      "v_cvt_pk_bf16_f32 v43, v22, v23\n\t"
      "v_mfma_f32_16x16x32_bf16 v[28:31], v[204:207], a[32:35], v[28:31]\n\t"
      "ds_write_b64 v0, v[42:43] offset:32\n\t"
      "v_mfma_f32_16x16x32_bf16 v[28:31], v[220:223], a[36:39], v[28:31]\n\t"
      "v_mfma_f32_16x16x32_bf16 v[28:31], v[236:239], a[40:43], v[28:31]\n\t"
      "v_mfma_f32_16x16x32_bf16 v[28:31], v[252:255], a[44:47], v[28:31]\n\t"
      "v_mfma_f32_16x16x32_bf16 v[32:35], a[108:111], a[24:27], v[32:35]\n\t"
      "v_mfma_f32_16x16x32_bf16 v[32:35], a[116:119], a[28:31], v[32:35]\n\t"
      "v_cvt_pk_bf16_f32 v44, v24, v25\n\t"
      "v_cvt_pk_bf16_f32 v45, v26, v27\n\t"
      "v_mfma_f32_16x16x32_bf16 v[32:35], a[124:127], a[32:35], v[32:35]\n\t"
      "ds_write_b64 v0, v[44:45] offset:64\n\t"
      "v_mfma_f32_16x16x32_bf16 v[32:35], a[132:135], a[36:39], v[32:35]\n\t"
      "buffer_load_dwordx4 v4, s[40:43], s48 offen offset:2048 nt lds\n\t"
      "v_mfma_f32_16x16x32_bf16 v[32:35], a[140:143], a[40:43], v[32:35]\n\t"
      "s_add_u32 s50, s50, 12288\n\t"
      "v_mfma_f32_16x16x32_bf16 v[32:35], a[148:151], a[44:47], v[32:35]\n\t"
      "v_cvt_pk_bf16_f32 v46, v28, v29\n\t"
      "v_cvt_pk_bf16_f32 v47, v30, v31\n\t"
      "v_mfma_f32_16x16x32_bf16 v[36:39], a[112:115], a[24:27], v[36:39]\n\t"
      "ds_write_b64 v0, v[46:47] offset:96\n\t"
      "v_mfma_f32_16x16x32_bf16 v[36:39], a[120:123], a[28:31], v[36:39]\n\t"
      "s_cmp_eq_u32 s50, 160256\n\t"
      "s_cselect_b32 s50, 25088, s50\n\t"
      "v_mfma_f32_16x16x32_bf16 v[36:39], a[128:131], a[32:35], v[36:39]\n\t"
      "v_mfma_f32_16x16x32_bf16 v[36:39], a[136:139], a[36:39], v[36:39]\n\t"
      "v_mfma_f32_16x16x32_bf16 v[36:39], a[144:147], a[40:43], v[36:39]\n\t"
      "v_cvt_pk_bf16_f32 v48, v32, v33\n\t"
      "v_cvt_pk_bf16_f32 v49, v34, v35\n\t"
      "v_mfma_f32_16x16x32_bf16 v[36:39], a[152:155], a[44:47], v[36:39]\n\t"
      "ds_write_b64 v0, v[48:49] offset:128\n\t"
      ".Lmid3_%=:\n\t"
      "v_mfma_f32_16x16x32_bf16 v[16:19], v[64:67], a[0:3], 0\n\t"
      "ds_read_b128 a[24:27], v54 offset:256\n\t"
      "v_mfma_f32_16x16x32_bf16 v[16:19], v[80:83], a[4:7], v[16:19]\n\t"
      "ds_read_b128 a[28:31], v55 offset:256\n\t"
      "v_mfma_f32_16x16x32_bf16 v[16:19], v[96:99], a[8:11], v[16:19]\n\t"
      "ds_read_b128 a[32:35], v52 offset:512\n\t"
      "v_mfma_f32_16x16x32_bf16 v[16:19], v[112:115], a[12:15], v[16:19]\n\t"
      "ds_read_b128 a[36:39], v53 offset:512\n\t"
      "v_mfma_f32_16x16x32_bf16 v[16:19], v[128:131], a[16:19], v[16:19]\n\t"
      "ds_read_b128 a[40:43], v54 offset:512\n\t"
      "v_mfma_f32_16x16x32_bf16 v[16:19], v[144:147], a[20:23], v[16:19]\n\t"
      "ds_read_b128 a[44:47], v55 offset:512\n\t"
      "v_mfma_f32_16x16x32_bf16 v[20:23], v[68:71], a[0:3], 0\n\t"
      "v_cvt_pk_bf16_f32 v50, v36, v37\n\t"
      "v_cvt_pk_bf16_f32 v51, v38, v39\n\t"
      "v_mfma_f32_16x16x32_bf16 v[20:23], v[84:87], a[4:7], v[20:23]\n\t"
      "ds_write_b64 v0, v[50:51] offset:160\n\t"
      "v_mfma_f32_16x16x32_bf16 v[20:23], v[100:103], a[8:11], v[20:23]\n\t"
      "v_add_u32 v56, s51, v15\n\t"
      "v_xor_b32 v57, 64, v56\n\t"
      "v_mfma_f32_16x16x32_bf16 v[20:23], v[116:119], a[12:15], v[20:23]\n\t"
      "v_xor_b32 v58, 128, v56\n\t"
      "v_xor_b32 v59, 192, v56\n\t"
      "v_mfma_f32_16x16x32_bf16 v[20:23], v[132:135], a[16:19], v[20:23]\n\t"
      "v_mfma_f32_16x16x32_bf16 v[20:23], v[148:151], a[20:23], v[20:23]\n\t"
      "v_mfma_f32_16x16x32_bf16 v[24:27], v[72:75], a[0:3], 0\n\t"
      "v_mfma_f32_16x16x32_bf16 v[24:27], v[88:91], a[4:7], v[24:27]\n\t"
      "v_mfma_f32_16x16x32_bf16 v[24:27], v[104:107], a[8:11], v[24:27]\n\t"
      "v_mfma_f32_16x16x32_bf16 v[24:27], v[120:123], a[12:15], v[24:27]\n\t"
      "v_mfma_f32_16x16x32_bf16 v[24:27], v[136:139], a[16:19], v[24:27]\n\t"
      "v_mfma_f32_16x16x32_bf16 v[24:27], v[152:155], a[20:23], v[24:27]\n\t"
      "s_waitcnt vmcnt(12)\n\t"
      "s_waitcnt lgkmcnt(0)\n\t"
      "s_barrier\n\t"
      "v_mfma_f32_16x16x32_bf16 v[28:31], v[76:79], a[0:3], 0\n\t"
      "ds_read_b128 a[48:51], v8 offset:0\n\t"
      "v_mfma_f32_16x16x32_bf16 v[28:31], v[92:95], a[4:7], v[28:31]\n\t"
      "ds_read_b128 a[52:55], v60 offset:1024\n\t"
      "v_mfma_f32_16x16x32_bf16 v[28:31], v[108:111], a[8:11], v[28:31]\n\t"
      "ds_read_b128 a[56:59], v61 offset:2048\n\t"
      "v_mfma_f32_16x16x32_bf16 v[28:31], v[124:127], a[12:15], v[28:31]\n\t"
      "v_mfma_f32_16x16x32_bf16 v[28:31], v[140:143], a[16:19], v[28:31]\n\t"
      "v_mfma_f32_16x16x32_bf16 v[28:31], v[156:159], a[20:23], v[28:31]\n\t"
      "v_mfma_f32_16x16x32_bf16 v[32:35], a[60:63], a[0:3], 0\n\t"
      "v_mfma_f32_16x16x32_bf16 v[32:35], a[68:71], a[4:7], v[32:35]\n\t"
      "s_waitcnt lgkmcnt(2)\n\t"
      "v_mfma_f32_16x16x32_bf16 v[32:35], a[76:79], a[8:11], v[32:35]\n\t"
      "buffer_store_dwordx4 a[48:51], v9, s[44:47], s49 offen sc1\n\t"
      "v_mfma_f32_16x16x32_bf16 v[32:35], a[84:87], a[12:15], v[32:35]\n\t"
      "v_mfma_f32_16x16x32_bf16 v[32:35], a[92:95], a[16:19], v[32:35]\n\t"
      "v_mfma_f32_16x16x32_bf16 v[32:35], a[100:103], a[20:23], v[32:35]\n\t"
      "s_add_u32 m0, s18, s50\n\t"
      "v_mfma_f32_16x16x32_bf16 v[36:39], a[64:67], a[0:3], 0\n\t"
      "buffer_load_dwordx4 v5, s[40:43], s48 offen nt lds\n\t"
      "v_mfma_f32_16x16x32_bf16 v[36:39], a[72:75], a[4:7], v[36:39]\n\t"
      "v_mfma_f32_16x16x32_bf16 v[36:39], a[80:83], a[8:11], v[36:39]\n\t"
      "v_mfma_f32_16x16x32_bf16 v[36:39], a[88:91], a[12:15], v[36:39]\n\t"
      "s_waitcnt lgkmcnt(1)\n\t"
      "v_mfma_f32_16x16x32_bf16 v[36:39], a[96:99], a[16:19], v[36:39]\n\t"
      "buffer_store_dwordx4 a[52:55], v10, s[44:47], s49 offen offset:1024 sc1\n\t"
      "v_mfma_f32_16x16x32_bf16 v[36:39], a[104:107], a[20:23], v[36:39]\n\t"
      "v_mfma_f32_16x16x32_bf16 v[16:19], v[160:163], a[24:27], v[16:19]\n\t"
      "ds_read_b128 a[0:3], v56 offset:0\n\t"
      "v_mfma_f32_16x16x32_bf16 v[16:19], v[176:179], a[28:31], v[16:19]\n\t"
      "ds_read_b128 a[4:7], v57 offset:0\n\t"
      "v_mfma_f32_16x16x32_bf16 v[16:19], v[192:195], a[32:35], v[16:19]\n\t"
      "ds_read_b128 a[8:11], v58 offset:0\n\t"
      "v_mfma_f32_16x16x32_bf16 v[16:19], v[208:211], a[36:39], v[16:19]\n\t"
      "ds_read_b128 a[12:15], v59 offset:0\n\t"
      "v_mfma_f32_16x16x32_bf16 v[16:19], v[224:227], a[40:43], v[16:19]\n\t"
      "ds_read_b128 a[16:19], v56 offset:256\n\t"
      "v_mfma_f32_16x16x32_bf16 v[16:19], v[240:243], a[44:47], v[16:19]\n\t"
      "ds_read_b128 a[20:23], v57 offset:256\n\t"
      "v_mfma_f32_16x16x32_bf16 v[20:23], v[164:167], a[24:27], v[20:23]\n\t"
      "s_add_u32 s51, s51, 12288\n\t"
      "s_cmp_eq_u32 s51, 160256\n\t"
      "s_cselect_b32 s51, 25088, s51\n\t"
      "v_mfma_f32_16x16x32_bf16 v[20:23], v[180:183], a[28:31], v[20:23]\n\t"
      "s_waitcnt lgkmcnt(6)\n\t"
      "v_mfma_f32_16x16x32_bf16 v[20:23], v[196:199], a[32:35], v[20:23]\n\t"
      "buffer_store_dwordx4 a[56:59], v11, s[44:47], s49 offen offset:2048 sc1\n\t"
      "v_mfma_f32_16x16x32_bf16 v[20:23], v[212:215], a[36:39], v[20:23]\n\t"
      "v_mfma_f32_16x16x32_bf16 v[20:23], v[228:231], a[40:43], v[20:23]\n\t"
      "s_waitcnt lgkmcnt(0)\n\t"
      "v_mfma_f32_16x16x32_bf16 v[20:23], v[244:247], a[44:47], v[20:23]\n\t"
      "v_mfma_f32_16x16x32_bf16 v[24:27], v[168:171], a[24:27], v[24:27]\n\t"
      "v_mfma_f32_16x16x32_bf16 v[24:27], v[184:187], a[28:31], v[24:27]\n\t"
      "v_cvt_pk_bf16_f32 v40, v16, v17\n\t"
      "v_cvt_pk_bf16_f32 v41, v18, v19\n\t"
      "v_mfma_f32_16x16x32_bf16 v[24:27], v[200:203], a[32:35], v[24:27]\n\t"
      "ds_write_b64 v0, v[40:41] offset:12544\n\t"
      "v_mfma_f32_16x16x32_bf16 v[24:27], v[216:219], a[36:39], v[24:27]\n\t"
      "buffer_load_dwordx4 v6, s[40:43], s48 offen offset:1024 nt lds\n\t"
      "v_mfma_f32_16x16x32_bf16 v[24:27], v[232:235], a[40:43], v[24:27]\n\t"
      "v_mfma_f32_16x16x32_bf16 v[24:27], v[248:251], a[44:47], v[24:27]\n\t"
      "v_mfma_f32_16x16x32_bf16 v[28:31], v[172:175], a[24:27], v[28:31]\n\t"
      "v_mfma_f32_16x16x32_bf16 v[28:31], v[188:191], a[28:31], v[28:31]\n\t"
      "v_cvt_pk_bf16_f32 v42, v20, v21\n\t"
      "v_cvt_pk_bf16_f32 v43, v22, v23\n\t"
      "v_mfma_f32_16x16x32_bf16 v[28:31], v[204:207], a[32:35], v[28:31]\n\t"
      "ds_write_b64 v0, v[42:43] offset:12576\n\t"
      "v_mfma_f32_16x16x32_bf16 v[28:31], v[220:223], a[36:39], v[28:31]\n\t"
      "v_mfma_f32_16x16x32_bf16 v[28:31], v[236:239], a[40:43], v[28:31]\n\t"
      "v_mfma_f32_16x16x32_bf16 v[28:31], v[252:255], a[44:47], v[28:31]\n\t"
      "v_mfma_f32_16x16x32_bf16 v[32:35], a[108:111], a[24:27], v[32:35]\n\t"
      "v_mfma_f32_16x16x32_bf16 v[32:35], a[116:119], a[28:31], v[32:35]\n\t"
      "v_cvt_pk_bf16_f32 v44, v24, v25\n\t"
      "v_cvt_pk_bf16_f32 v45, v26, v27\n\t"
      "v_mfma_f32_16x16x32_bf16 v[32:35], a[124:127], a[32:35], v[32:35]\n\t"
      "ds_write_b64 v0, v[44:45] offset:12608\n\t"
      "v_mfma_f32_16x16x32_bf16 v[32:35], a[132:135], a[36:39], v[32:35]\n\t"
      "buffer_load_dwordx4 v7, s[40:43], s48 offen offset:2048 nt lds\n\t"
      "v_mfma_f32_16x16x32_bf16 v[32:35], a[140:143], a[40:43], v[32:35]\n\t"
      "s_add_u32 s50, s50, 12288\n\t"
      "v_mfma_f32_16x16x32_bf16 v[32:35], a[148:151], a[44:47], v[32:35]\n\t"
      "v_cvt_pk_bf16_f32 v46, v28, v29\n\t"
      "v_cvt_pk_bf16_f32 v47, v30, v31\n\t"
      "v_mfma_f32_16x16x32_bf16 v[36:39], a[112:115], a[24:27], v[36:39]\n\t"
      "ds_write_b64 v0, v[46:47] offset:12640\n\t"
      "v_mfma_f32_16x16x32_bf16 v[36:39], a[120:123], a[28:31], v[36:39]\n\t"
      "s_cmp_eq_u32 s50, 160256\n\t"
      "s_cselect_b32 s50, 25088, s50\n\t"
      "v_mfma_f32_16x16x32_bf16 v[36:39], a[128:131], a[32:35], v[36:39]\n\t"
      "s_sub_u32 s52, s52, 1\n\t"
      "s_cmp_lg_u32 s52, 0\n\t"
      "v_mfma_f32_16x16x32_bf16 v[36:39], a[136:139], a[36:39], v[36:39]\n\t"
      "v_mfma_f32_16x16x32_bf16 v[36:39], a[144:147], a[40:43], v[36:39]\n\t"
      "v_cvt_pk_bf16_f32 v48, v32, v33\n\t"
      "v_cvt_pk_bf16_f32 v49, v34, v35\n\t"
      "v_mfma_f32_16x16x32_bf16 v[36:39], a[152:155], a[44:47], v[36:39]\n\t"
      "ds_write_b64 v0, v[48:49] offset:12672\n\t"
      "s_cbranch_scc1 .Lloop3_%=\n\t"
      ".Lendc_%=:\n\t"
      "s_cmp_eq_u32 s48, 113221632\n\t"
      "s_cbranch_scc1 .Lwrap3_%=\n\t"
      ".Lwrapped3_%=:\n\t"
      "v_mfma_f32_16x16x32_bf16 v[16:19], v[64:67], a[0:3], 0\n\t"
      "ds_read_b128 a[24:27], v58 offset:256\n\t"
      "v_mfma_f32_16x16x32_bf16 v[16:19], v[80:83], a[4:7], v[16:19]\n\t"
      "ds_read_b128 a[28:31], v59 offset:256\n\t"
      "v_mfma_f32_16x16x32_bf16 v[16:19], v[96:99], a[8:11], v[16:19]\n\t"
      "ds_read_b128 a[32:35], v56 offset:512\n\t"
      "v_mfma_f32_16x16x32_bf16 v[16:19], v[112:115], a[12:15], v[16:19]\n\t"
      "ds_read_b128 a[36:39], v57 offset:512\n\t"
      "v_mfma_f32_16x16x32_bf16 v[16:19], v[128:131], a[16:19], v[16:19]\n\t"
      "ds_read_b128 a[40:43], v58 offset:512\n\t"
      "v_mfma_f32_16x16x32_bf16 v[16:19], v[144:147], a[20:23], v[16:19]\n\t"
      "ds_read_b128 a[44:47], v59 offset:512\n\t"
      "v_mfma_f32_16x16x32_bf16 v[20:23], v[68:71], a[0:3], 0\n\t"
      "v_cvt_pk_bf16_f32 v50, v36, v37\n\t"
      "v_cvt_pk_bf16_f32 v51, v38, v39\n\t"
      "v_mfma_f32_16x16x32_bf16 v[20:23], v[84:87], a[4:7], v[20:23]\n\t"
      "ds_write_b64 v0, v[50:51] offset:12704\n\t"
      "v_mfma_f32_16x16x32_bf16 v[20:23], v[100:103], a[8:11], v[20:23]\n\t"
      "v_add_u32 v52, s51, v15\n\t"
      "v_xor_b32 v53, 64, v52\n\t"
      "v_mfma_f32_16x16x32_bf16 v[20:23], v[116:119], a[12:15], v[20:23]\n\t"
      "v_xor_b32 v54, 128, v52\n\t"
      "v_xor_b32 v55, 192, v52\n\t"
      "v_mfma_f32_16x16x32_bf16 v[20:23], v[132:135], a[16:19], v[20:23]\n\t"
      "s_add_u32 s58, s58, 786432\n\t"
      "s_lshr_b32 s59, s59, 1\n\t"
      "v_mfma_f32_16x16x32_bf16 v[20:23], v[148:151], a[20:23], v[20:23]\n\t"
      "s_bitcmp1_b32 s59, 0\n\t"
      "s_cselect_b32 s84, 24576, 0\n\t"
      "s_add_u32 s84, s84, s58\n\t"
      "v_mfma_f32_16x16x32_bf16 v[24:27], v[72:75], a[0:3], 0\n\t"
      "s_add_u32 s58, s58, 786432\n\t"
      "s_lshr_b32 s59, s59, 1\n\t"
      "v_mfma_f32_16x16x32_bf16 v[24:27], v[88:91], a[4:7], v[24:27]\n\t"
      "s_bitcmp1_b32 s59, 0\n\t"
      "s_cselect_b32 s85, 24576, 0\n\t"
      "s_add_u32 s85, s85, s58\n\t"
      "v_mfma_f32_16x16x32_bf16 v[24:27], v[104:107], a[8:11], v[24:27]\n\t"
      "s_waitcnt vmcnt(12)\n\t"
      "s_waitcnt lgkmcnt(0)\n\t"
      "s_barrier\n\t"
      "v_mfma_f32_16x16x32_bf16 v[24:27], v[120:123], a[12:15], v[24:27]\n\t"
      "v_mfma_f32_16x16x32_bf16 v[24:27], v[136:139], a[16:19], v[24:27]\n\t"
      "v_mfma_f32_16x16x32_bf16 v[24:27], v[152:155], a[20:23], v[24:27]\n\t"
      "v_mfma_f32_16x16x32_bf16 v[28:31], v[76:79], a[0:3], 0\n\t"
      "ds_read_b128 a[188:191], v8 offset:12544\n\t"
      "v_mfma_f32_16x16x32_bf16 v[28:31], v[92:95], a[4:7], v[28:31]\n\t"
      "ds_read_b128 a[192:195], v60 offset:13568\n\t"
      "v_mfma_f32_16x16x32_bf16 v[28:31], v[108:111], a[8:11], v[28:31]\n\t"
      "ds_read_b128 a[196:199], v61 offset:14592\n\t"
      "v_mfma_f32_16x16x32_bf16 v[28:31], v[124:127], a[12:15], v[28:31]\n\t"
      "v_mfma_f32_16x16x32_bf16 v[28:31], v[140:143], a[16:19], v[28:31]\n\t"
      "v_mfma_f32_16x16x32_bf16 v[28:31], v[156:159], a[20:23], v[28:31]\n\t"
      "v_mfma_f32_16x16x32_bf16 v[32:35], a[60:63], a[0:3], 0\n\t"
      "v_mfma_f32_16x16x32_bf16 v[32:35], a[68:71], a[4:7], v[32:35]\n\t"
      "v_mfma_f32_16x16x32_bf16 v[32:35], a[76:79], a[8:11], v[32:35]\n\t"
      "v_mfma_f32_16x16x32_bf16 v[32:35], a[84:87], a[12:15], v[32:35]\n\t"
      "v_mfma_f32_16x16x32_bf16 v[32:35], a[92:95], a[16:19], v[32:35]\n\t"
      "v_mfma_f32_16x16x32_bf16 v[32:35], a[100:103], a[20:23], v[32:35]\n\t"
      "v_mfma_f32_16x16x32_bf16 v[36:39], a[64:67], a[0:3], 0\n\t"
      "v_mfma_f32_16x16x32_bf16 v[36:39], a[72:75], a[4:7], v[36:39]\n\t"
      "v_mfma_f32_16x16x32_bf16 v[36:39], a[80:83], a[8:11], v[36:39]\n\t"
      "v_mfma_f32_16x16x32_bf16 v[36:39], a[88:91], a[12:15], v[36:39]\n\t"
      "v_mfma_f32_16x16x32_bf16 v[36:39], a[96:99], a[16:19], v[36:39]\n\t"
      "v_mfma_f32_16x16x32_bf16 v[36:39], a[104:107], a[20:23], v[36:39]\n\t"
      "v_mfma_f32_16x16x32_bf16 v[16:19], v[160:163], a[24:27], v[16:19]\n\t"
      "ds_read_b128 a[0:3], v52 offset:0\n\t"
      "v_mfma_f32_16x16x32_bf16 v[16:19], v[176:179], a[28:31], v[16:19]\n\t"
      "ds_read_b128 a[4:7], v53 offset:0\n\t"
      "v_mfma_f32_16x16x32_bf16 v[16:19], v[192:195], a[32:35], v[16:19]\n\t"
      "ds_read_b128 a[8:11], v54 offset:0\n\t"
      "v_mfma_f32_16x16x32_bf16 v[16:19], v[208:211], a[36:39], v[16:19]\n\t"
      "ds_read_b128 a[12:15], v55 offset:0\n\t"
      "v_mfma_f32_16x16x32_bf16 v[16:19], v[224:227], a[40:43], v[16:19]\n\t"
      "ds_read_b128 a[16:19], v52 offset:256\n\t"
      "v_mfma_f32_16x16x32_bf16 v[16:19], v[240:243], a[44:47], v[16:19]\n\t"
      "ds_read_b128 a[20:23], v53 offset:256\n\t"
      "v_mfma_f32_16x16x32_bf16 v[20:23], v[164:167], a[24:27], v[20:23]\n\t"
      "s_add_u32 s51, s51, 12288\n\t"
      "s_cmp_eq_u32 s51, 160256\n\t"
      "s_cselect_b32 s51, 25088, s51\n\t"
      "v_mfma_f32_16x16x32_bf16 v[20:23], v[180:183], a[28:31], v[20:23]\n\t"
      "v_mfma_f32_16x16x32_bf16 v[20:23], v[196:199], a[32:35], v[20:23]\n\t"
      "v_mfma_f32_16x16x32_bf16 v[20:23], v[212:215], a[36:39], v[20:23]\n\t"
      "v_mfma_f32_16x16x32_bf16 v[20:23], v[228:231], a[40:43], v[20:23]\n\t"
      "s_waitcnt lgkmcnt(0)\n\t"
      "v_mfma_f32_16x16x32_bf16 v[20:23], v[244:247], a[44:47], v[20:23]\n\t"
      "v_mfma_f32_16x16x32_bf16 v[24:27], v[168:171], a[24:27], v[24:27]\n\t"
      "v_mfma_f32_16x16x32_bf16 v[24:27], v[184:187], a[28:31], v[24:27]\n\t"
      "v_cvt_pk_bf16_f32 v40, v16, v17\n\t"
      "v_cvt_pk_bf16_f32 v41, v18, v19\n\t"
      "v_mfma_f32_16x16x32_bf16 v[24:27], v[200:203], a[32:35], v[24:27]\n\t"
      "ds_write_b64 v0, v[40:41] offset:0\n\t"
      "v_mfma_f32_16x16x32_bf16 v[24:27], v[216:219], a[36:39], v[24:27]\n\t"
      "v_mfma_f32_16x16x32_bf16 v[24:27], v[232:235], a[40:43], v[24:27]\n\t"
      "v_mfma_f32_16x16x32_bf16 v[24:27], v[248:251], a[44:47], v[24:27]\n\t"
      "v_mfma_f32_16x16x32_bf16 v[28:31], v[172:175], a[24:27], v[28:31]\n\t"
      "v_mfma_f32_16x16x32_bf16 v[28:31], v[188:191], a[28:31], v[28:31]\n\t"
      "v_cvt_pk_bf16_f32 v42, v20, v21\n\t"
      "v_cvt_pk_bf16_f32 v43, v22, v23\n\t"
      "v_mfma_f32_16x16x32_bf16 v[28:31], v[204:207], a[32:35], v[28:31]\n\t"
      "ds_write_b64 v0, v[42:43] offset:32\n\t"
      "v_mfma_f32_16x16x32_bf16 v[28:31], v[220:223], a[36:39], v[28:31]\n\t"
      "v_mfma_f32_16x16x32_bf16 v[28:31], v[236:239], a[40:43], v[28:31]\n\t"
      "v_mfma_f32_16x16x32_bf16 v[28:31], v[252:255], a[44:47], v[28:31]\n\t"
      "v_mfma_f32_16x16x32_bf16 v[32:35], a[108:111], a[24:27], v[32:35]\n\t"
      "v_mfma_f32_16x16x32_bf16 v[32:35], a[116:119], a[28:31], v[32:35]\n\t"
      "v_cvt_pk_bf16_f32 v44, v24, v25\n\t"
      "v_cvt_pk_bf16_f32 v45, v26, v27\n\t"
      "v_mfma_f32_16x16x32_bf16 v[32:35], a[124:127], a[32:35], v[32:35]\n\t"
      "ds_write_b64 v0, v[44:45] offset:64\n\t"
      "v_mfma_f32_16x16x32_bf16 v[32:35], a[132:135], a[36:39], v[32:35]\n\t"
      "v_mfma_f32_16x16x32_bf16 v[32:35], a[140:143], a[40:43], v[32:35]\n\t"
      "v_mfma_f32_16x16x32_bf16 v[32:35], a[148:151], a[44:47], v[32:35]\n\t"
      "v_cvt_pk_bf16_f32 v46, v28, v29\n\t"
      "v_cvt_pk_bf16_f32 v47, v30, v31\n\t"
      "v_mfma_f32_16x16x32_bf16 v[36:39], a[112:115], a[24:27], v[36:39]\n\t"
      "ds_write_b64 v0, v[46:47] offset:96\n\t"
      "v_mfma_f32_16x16x32_bf16 v[36:39], a[120:123], a[28:31], v[36:39]\n\t"
      "v_mfma_f32_16x16x32_bf16 v[36:39], a[128:131], a[32:35], v[36:39]\n\t"
      "v_mfma_f32_16x16x32_bf16 v[36:39], a[136:139], a[36:39], v[36:39]\n\t"
      "v_mfma_f32_16x16x32_bf16 v[36:39], a[144:147], a[40:43], v[36:39]\n\t"
      "v_cvt_pk_bf16_f32 v48, v32, v33\n\t"
      "v_cvt_pk_bf16_f32 v49, v34, v35\n\t"
      "v_mfma_f32_16x16x32_bf16 v[36:39], a[152:155], a[44:47], v[36:39]\n\t"
      "ds_write_b64 v0, v[48:49] offset:128\n\t"
      "v_mfma_f32_16x16x32_bf16 v[16:19], v[64:67], a[0:3], 0\n\t"
      "ds_read_b128 a[24:27], v54 offset:256\n\t"
      "v_mfma_f32_16x16x32_bf16 v[16:19], v[80:83], a[4:7], v[16:19]\n\t"
      "ds_read_b128 a[28:31], v55 offset:256\n\t"
      "v_mfma_f32_16x16x32_bf16 v[16:19], v[96:99], a[8:11], v[16:19]\n\t"
      "ds_read_b128 a[32:35], v52 offset:512\n\t"
      "v_mfma_f32_16x16x32_bf16 v[16:19], v[112:115], a[12:15], v[16:19]\n\t"
      "ds_read_b128 a[36:39], v53 offset:512\n\t"
      "v_mfma_f32_16x16x32_bf16 v[16:19], v[128:131], a[16:19], v[16:19]\n\t"
      "ds_read_b128 a[40:43], v54 offset:512\n\t"
      "v_mfma_f32_16x16x32_bf16 v[16:19], v[144:147], a[20:23], v[16:19]\n\t"
      "ds_read_b128 a[44:47], v55 offset:512\n\t"
      "v_mfma_f32_16x16x32_bf16 v[20:23], v[68:71], a[0:3], 0\n\t"
      "v_cvt_pk_bf16_f32 v50, v36, v37\n\t"
      "v_cvt_pk_bf16_f32 v51, v38, v39\n\t"
      "v_mfma_f32_16x16x32_bf16 v[20:23], v[84:87], a[4:7], v[20:23]\n\t"
      "ds_write_b64 v0, v[50:51] offset:160\n\t"
      "v_mfma_f32_16x16x32_bf16 v[20:23], v[100:103], a[8:11], v[20:23]\n\t"
      "v_add_u32 v56, s51, v15\n\t"
      "v_xor_b32 v57, 64, v56\n\t"
      "v_mfma_f32_16x16x32_bf16 v[20:23], v[116:119], a[12:15], v[20:23]\n\t"
      "v_xor_b32 v58, 128, v56\n\t"
      "v_xor_b32 v59, 192, v56\n\t"
      "v_mfma_f32_16x16x32_bf16 v[20:23], v[132:135], a[16:19], v[20:23]\n\t"
      "buffer_store_dwordx4 a[188:191], v12, s[44:47], s49 offen sc1\n\t"
      "v_mfma_f32_16x16x32_bf16 v[20:23], v[148:151], a[20:23], v[20:23]\n\t"
      "v_mfma_f32_16x16x32_bf16 v[24:27], v[72:75], a[0:3], 0\n\t"
      "v_mfma_f32_16x16x32_bf16 v[24:27], v[88:91], a[4:7], v[24:27]\n\t"
      "v_mfma_f32_16x16x32_bf16 v[24:27], v[104:107], a[8:11], v[24:27]\n\t"
      "s_waitcnt vmcnt(7)\n\t"
      "s_waitcnt lgkmcnt(0)\n\t"
      "s_barrier\n\t"
      "v_mfma_f32_16x16x32_bf16 v[24:27], v[120:123], a[12:15], v[24:27]\n\t"
      "v_mfma_f32_16x16x32_bf16 v[24:27], v[136:139], a[16:19], v[24:27]\n\t"
      "v_mfma_f32_16x16x32_bf16 v[24:27], v[152:155], a[20:23], v[24:27]\n\t"
      "v_mfma_f32_16x16x32_bf16 v[28:31], v[76:79], a[0:3], 0\n\t"
      "ds_read_b128 a[200:203], v8 offset:0\n\t"
      "v_mfma_f32_16x16x32_bf16 v[28:31], v[92:95], a[4:7], v[28:31]\n\t"
      "ds_read_b128 a[204:207], v60 offset:1024\n\t"
      "v_mfma_f32_16x16x32_bf16 v[28:31], v[108:111], a[8:11], v[28:31]\n\t"
      "ds_read_b128 a[208:211], v61 offset:2048\n\t"
      "v_mfma_f32_16x16x32_bf16 v[28:31], v[124:127], a[12:15], v[28:31]\n\t"
      "v_mfma_f32_16x16x32_bf16 v[28:31], v[140:143], a[16:19], v[28:31]\n\t"
      "buffer_store_dwordx4 a[192:195], v13, s[44:47], s49 offen offset:1024 sc1\n\t"
      "v_mfma_f32_16x16x32_bf16 v[28:31], v[156:159], a[20:23], v[28:31]\n\t"
      "v_mfma_f32_16x16x32_bf16 v[32:35], a[60:63], a[0:3], 0\n\t"
      "v_mfma_f32_16x16x32_bf16 v[32:35], a[68:71], a[4:7], v[32:35]\n\t"
      "v_mfma_f32_16x16x32_bf16 v[32:35], a[76:79], a[8:11], v[32:35]\n\t"
      "v_mfma_f32_16x16x32_bf16 v[32:35], a[84:87], a[12:15], v[32:35]\n\t"
      "v_mfma_f32_16x16x32_bf16 v[32:35], a[92:95], a[16:19], v[32:35]\n\t"
      "v_mfma_f32_16x16x32_bf16 v[32:35], a[100:103], a[20:23], v[32:35]\n\t"
      "v_mfma_f32_16x16x32_bf16 v[36:39], a[64:67], a[0:3], 0\n\t"
      "buffer_store_dwordx4 a[196:199], v14, s[44:47], s49 offen offset:2048 sc1\n\t"
      "v_mfma_f32_16x16x32_bf16 v[36:39], a[72:75], a[4:7], v[36:39]\n\t"
      "v_mfma_f32_16x16x32_bf16 v[36:39], a[80:83], a[8:11], v[36:39]\n\t"
      "v_mfma_f32_16x16x32_bf16 v[36:39], a[88:91], a[12:15], v[36:39]\n\t"
      "v_mfma_f32_16x16x32_bf16 v[36:39], a[96:99], a[16:19], v[36:39]\n\t"
      "v_mfma_f32_16x16x32_bf16 v[36:39], a[104:107], a[20:23], v[36:39]\n\t"
      "v_mfma_f32_16x16x32_bf16 v[16:19], v[160:163], a[24:27], v[16:19]\n\t"
      "ds_read_b128 a[0:3], v56 offset:0\n\t"
      "v_mfma_f32_16x16x32_bf16 v[16:19], v[176:179], a[28:31], v[16:19]\n\t"
      "ds_read_b128 a[4:7], v57 offset:0\n\t"
      "v_mfma_f32_16x16x32_bf16 v[16:19], v[192:195], a[32:35], v[16:19]\n\t"
      "ds_read_b128 a[8:11], v58 offset:0\n\t"
      "v_mfma_f32_16x16x32_bf16 v[16:19], v[208:211], a[36:39], v[16:19]\n\t"
      "ds_read_b128 a[12:15], v59 offset:0\n\t"
      "v_mfma_f32_16x16x32_bf16 v[16:19], v[224:227], a[40:43], v[16:19]\n\t"
      "ds_read_b128 a[16:19], v56 offset:256\n\t"
      "v_mfma_f32_16x16x32_bf16 v[16:19], v[240:243], a[44:47], v[16:19]\n\t"
      "ds_read_b128 a[20:23], v57 offset:256\n\t"
      "v_mfma_f32_16x16x32_bf16 v[20:23], v[164:167], a[24:27], v[20:23]\n\t"
      "s_add_u32 s51, s51, 12288\n\t"
      "s_cmp_eq_u32 s51, 160256\n\t"
      "s_cselect_b32 s51, 25088, s51\n\t"
      "v_mfma_f32_16x16x32_bf16 v[20:23], v[180:183], a[28:31], v[20:23]\n\t"
      "s_waitcnt lgkmcnt(8)\n\t"
      "v_mfma_f32_16x16x32_bf16 v[20:23], v[196:199], a[32:35], v[20:23]\n\t"
      "buffer_store_dwordx4 a[200:203], v9, s[44:47], s84 offen sc1\n\t"
      "v_mfma_f32_16x16x32_bf16 v[20:23], v[212:215], a[36:39], v[20:23]\n\t"
      "v_mfma_f32_16x16x32_bf16 v[20:23], v[228:231], a[40:43], v[20:23]\n\t"
      "s_waitcnt lgkmcnt(0)\n\t"
      "v_mfma_f32_16x16x32_bf16 v[20:23], v[244:247], a[44:47], v[20:23]\n\t"
      "v_mfma_f32_16x16x32_bf16 v[24:27], v[168:171], a[24:27], v[24:27]\n\t"
      "v_mfma_f32_16x16x32_bf16 v[24:27], v[184:187], a[28:31], v[24:27]\n\t"
      "v_cvt_pk_bf16_f32 v40, v16, v17\n\t"
      "v_cvt_pk_bf16_f32 v41, v18, v19\n\t"
      "v_mfma_f32_16x16x32_bf16 v[24:27], v[200:203], a[32:35], v[24:27]\n\t"
      "ds_write_b64 v0, v[40:41] offset:12544\n\t"
      "v_mfma_f32_16x16x32_bf16 v[24:27], v[216:219], a[36:39], v[24:27]\n\t"
      "v_mfma_f32_16x16x32_bf16 v[24:27], v[232:235], a[40:43], v[24:27]\n\t"
      "buffer_store_dwordx4 a[204:207], v10, s[44:47], s84 offen offset:1024 sc1\n\t"
      "v_mfma_f32_16x16x32_bf16 v[24:27], v[248:251], a[44:47], v[24:27]\n\t"
      "v_mfma_f32_16x16x32_bf16 v[28:31], v[172:175], a[24:27], v[28:31]\n\t"
      "v_mfma_f32_16x16x32_bf16 v[28:31], v[188:191], a[28:31], v[28:31]\n\t"
      "v_cvt_pk_bf16_f32 v42, v20, v21\n\t"
      "v_cvt_pk_bf16_f32 v43, v22, v23\n\t"
      "v_mfma_f32_16x16x32_bf16 v[28:31], v[204:207], a[32:35], v[28:31]\n\t"
      "ds_write_b64 v0, v[42:43] offset:12576\n\t"
      "v_mfma_f32_16x16x32_bf16 v[28:31], v[220:223], a[36:39], v[28:31]\n\t"
      "v_mfma_f32_16x16x32_bf16 v[28:31], v[236:239], a[40:43], v[28:31]\n\t"
      "buffer_store_dwordx4 a[208:211], v11, s[44:47], s84 offen offset:2048 sc1\n\t"
      "v_mfma_f32_16x16x32_bf16 v[28:31], v[252:255], a[44:47], v[28:31]\n\t"
      "v_mfma_f32_16x16x32_bf16 v[32:35], a[108:111], a[24:27], v[32:35]\n\t"
      "v_mfma_f32_16x16x32_bf16 v[32:35], a[116:119], a[28:31], v[32:35]\n\t"
      "v_cvt_pk_bf16_f32 v44, v24, v25\n\t"
      "v_cvt_pk_bf16_f32 v45, v26, v27\n\t"
      "v_mfma_f32_16x16x32_bf16 v[32:35], a[124:127], a[32:35], v[32:35]\n\t"
      "ds_write_b64 v0, v[44:45] offset:12608\n\t"
      "v_mfma_f32_16x16x32_bf16 v[32:35], a[132:135], a[36:39], v[32:35]\n\t"
      "v_mfma_f32_16x16x32_bf16 v[32:35], a[140:143], a[40:43], v[32:35]\n\t"
      "v_mfma_f32_16x16x32_bf16 v[32:35], a[148:151], a[44:47], v[32:35]\n\t"
      "v_cvt_pk_bf16_f32 v46, v28, v29\n\t"
      "v_cvt_pk_bf16_f32 v47, v30, v31\n\t"
      "v_mfma_f32_16x16x32_bf16 v[36:39], a[112:115], a[24:27], v[36:39]\n\t"
      "ds_write_b64 v0, v[46:47] offset:12640\n\t"
      "v_mfma_f32_16x16x32_bf16 v[36:39], a[120:123], a[28:31], v[36:39]\n\t"
      "v_mfma_f32_16x16x32_bf16 v[36:39], a[128:131], a[32:35], v[36:39]\n\t"
      "v_mfma_f32_16x16x32_bf16 v[36:39], a[136:139], a[36:39], v[36:39]\n\t"
      "v_mfma_f32_16x16x32_bf16 v[36:39], a[144:147], a[40:43], v[36:39]\n\t"
      "v_cvt_pk_bf16_f32 v48, v32, v33\n\t"
      "v_cvt_pk_bf16_f32 v49, v34, v35\n\t"
      "v_mfma_f32_16x16x32_bf16 v[36:39], a[152:155], a[44:47], v[36:39]\n\t"
      "ds_write_b64 v0, v[48:49] offset:12672\n\t"
      "v_mfma_f32_16x16x32_bf16 v[16:19], v[64:67], a[0:3], 0\n\t"
      "ds_read_b128 a[24:27], v58 offset:256\n\t"
      "v_mfma_f32_16x16x32_bf16 v[16:19], v[80:83], a[4:7], v[16:19]\n\t"
      "ds_read_b128 a[28:31], v59 offset:256\n\t"
      "v_mfma_f32_16x16x32_bf16 v[16:19], v[96:99], a[8:11], v[16:19]\n\t"
      "ds_read_b128 a[32:35], v56 offset:512\n\t"
      "v_mfma_f32_16x16x32_bf16 v[16:19], v[112:115], a[12:15], v[16:19]\n\t"
      "ds_read_b128 a[36:39], v57 offset:512\n\t"
      "v_mfma_f32_16x16x32_bf16 v[16:19], v[128:131], a[16:19], v[16:19]\n\t"
      "ds_read_b128 a[40:43], v58 offset:512\n\t"
      "v_mfma_f32_16x16x32_bf16 v[16:19], v[144:147], a[20:23], v[16:19]\n\t"
      "ds_read_b128 a[44:47], v59 offset:512\n\t"
      "v_mfma_f32_16x16x32_bf16 v[20:23], v[68:71], a[0:3], 0\n\t"
      "v_cvt_pk_bf16_f32 v50, v36, v37\n\t"
      "v_cvt_pk_bf16_f32 v51, v38, v39\n\t"
      "v_mfma_f32_16x16x32_bf16 v[20:23], v[84:87], a[4:7], v[20:23]\n\t"
      "ds_write_b64 v0, v[50:51] offset:12704\n\t"
      "v_mfma_f32_16x16x32_bf16 v[20:23], v[100:103], a[8:11], v[20:23]\n\t"
      "v_add_u32 v52, s51, v15\n\t"
      "v_xor_b32 v53, 64, v52\n\t"
      "v_mfma_f32_16x16x32_bf16 v[20:23], v[116:119], a[12:15], v[20:23]\n\t"
      "v_xor_b32 v54, 128, v52\n\t"
      "v_xor_b32 v55, 192, v52\n\t"
      "v_mfma_f32_16x16x32_bf16 v[20:23], v[132:135], a[16:19], v[20:23]\n\t"
      "v_mfma_f32_16x16x32_bf16 v[20:23], v[148:151], a[20:23], v[20:23]\n\t"
      "v_mfma_f32_16x16x32_bf16 v[24:27], v[72:75], a[0:3], 0\n\t"
      "v_mfma_f32_16x16x32_bf16 v[24:27], v[88:91], a[4:7], v[24:27]\n\t"
      "v_mfma_f32_16x16x32_bf16 v[24:27], v[104:107], a[8:11], v[24:27]\n\t"
      "s_waitcnt vmcnt(6)\n\t"
      "s_waitcnt lgkmcnt(0)\n\t"
      "s_barrier\n\t"
      "v_mfma_f32_16x16x32_bf16 v[24:27], v[120:123], a[12:15], v[24:27]\n\t"
      "v_mfma_f32_16x16x32_bf16 v[24:27], v[136:139], a[16:19], v[24:27]\n\t"
      "v_mfma_f32_16x16x32_bf16 v[24:27], v[152:155], a[20:23], v[24:27]\n\t"
      "v_mfma_f32_16x16x32_bf16 v[28:31], v[76:79], a[0:3], 0\n\t"
      "ds_read_b128 a[212:215], v8 offset:12544\n\t"
      "v_mfma_f32_16x16x32_bf16 v[28:31], v[92:95], a[4:7], v[28:31]\n\t"
      "ds_read_b128 a[216:219], v60 offset:13568\n\t"
      "v_mfma_f32_16x16x32_bf16 v[28:31], v[108:111], a[8:11], v[28:31]\n\t"
      "ds_read_b128 a[220:223], v61 offset:14592\n\t"
      "v_mfma_f32_16x16x32_bf16 v[28:31], v[124:127], a[12:15], v[28:31]\n\t"
      "v_mfma_f32_16x16x32_bf16 v[28:31], v[140:143], a[16:19], v[28:31]\n\t"
      "v_mfma_f32_16x16x32_bf16 v[28:31], v[156:159], a[20:23], v[28:31]\n\t"
      "v_mfma_f32_16x16x32_bf16 v[32:35], a[60:63], a[0:3], 0\n\t"
      "v_mfma_f32_16x16x32_bf16 v[32:35], a[68:71], a[4:7], v[32:35]\n\t"
      "s_waitcnt lgkmcnt(2)\n\t"
      "v_mfma_f32_16x16x32_bf16 v[32:35], a[76:79], a[8:11], v[32:35]\n\t"
      "buffer_store_dwordx4 a[212:215], v12, s[44:47], s84 offen sc1\n\t"
      "v_mfma_f32_16x16x32_bf16 v[32:35], a[84:87], a[12:15], v[32:35]\n\t"
      "v_mfma_f32_16x16x32_bf16 v[32:35], a[92:95], a[16:19], v[32:35]\n\t"
      "v_mfma_f32_16x16x32_bf16 v[32:35], a[100:103], a[20:23], v[32:35]\n\t"
      "v_mfma_f32_16x16x32_bf16 v[36:39], a[64:67], a[0:3], 0\n\t"
      "v_mfma_f32_16x16x32_bf16 v[36:39], a[72:75], a[4:7], v[36:39]\n\t"
      "v_mfma_f32_16x16x32_bf16 v[36:39], a[80:83], a[8:11], v[36:39]\n\t"
      "v_mfma_f32_16x16x32_bf16 v[36:39], a[88:91], a[12:15], v[36:39]\n\t"
      "s_waitcnt lgkmcnt(1)\n\t"
      "v_mfma_f32_16x16x32_bf16 v[36:39], a[96:99], a[16:19], v[36:39]\n\t"
      "buffer_store_dwordx4 a[216:219], v13, s[44:47], s84 offen offset:1024 sc1\n\t"
      "v_mfma_f32_16x16x32_bf16 v[36:39], a[104:107], a[20:23], v[36:39]\n\t"
      "v_mfma_f32_16x16x32_bf16 v[16:19], v[160:163], a[24:27], v[16:19]\n\t"
      "ds_read_b128 a[0:3], v52 offset:0\n\t"
      "v_mfma_f32_16x16x32_bf16 v[16:19], v[176:179], a[28:31], v[16:19]\n\t"
      "ds_read_b128 a[4:7], v53 offset:0\n\t"
      "v_mfma_f32_16x16x32_bf16 v[16:19], v[192:195], a[32:35], v[16:19]\n\t"
      "ds_read_b128 a[8:11], v54 offset:0\n\t"
      "v_mfma_f32_16x16x32_bf16 v[16:19], v[208:211], a[36:39], v[16:19]\n\t"
      "ds_read_b128 a[12:15], v55 offset:0\n\t"
      "v_mfma_f32_16x16x32_bf16 v[16:19], v[224:227], a[40:43], v[16:19]\n\t"
      "ds_read_b128 a[16:19], v52 offset:256\n\t"
      "v_mfma_f32_16x16x32_bf16 v[16:19], v[240:243], a[44:47], v[16:19]\n\t"
      "ds_read_b128 a[20:23], v53 offset:256\n\t"
      "v_mfma_f32_16x16x32_bf16 v[20:23], v[164:167], a[24:27], v[20:23]\n\t"
      "s_add_u32 s51, s51, 12288\n\t"
      "s_cmp_eq_u32 s51, 160256\n\t"
      "s_cselect_b32 s51, 25088, s51\n\t"
      "v_mfma_f32_16x16x32_bf16 v[20:23], v[180:183], a[28:31], v[20:23]\n\t"
      "s_waitcnt lgkmcnt(6)\n\t"
      "v_mfma_f32_16x16x32_bf16 v[20:23], v[196:199], a[32:35], v[20:23]\n\t"
      "buffer_store_dwordx4 a[220:223], v14, s[44:47], s84 offen offset:2048 sc1\n\t"
      "v_mfma_f32_16x16x32_bf16 v[20:23], v[212:215], a[36:39], v[20:23]\n\t"
      "v_mfma_f32_16x16x32_bf16 v[20:23], v[228:231], a[40:43], v[20:23]\n\t"
      "s_waitcnt lgkmcnt(0)\n\t"
      "v_mfma_f32_16x16x32_bf16 v[20:23], v[244:247], a[44:47], v[20:23]\n\t"
      "v_mfma_f32_16x16x32_bf16 v[24:27], v[168:171], a[24:27], v[24:27]\n\t"
      "v_mfma_f32_16x16x32_bf16 v[24:27], v[184:187], a[28:31], v[24:27]\n\t"
      "v_cvt_pk_bf16_f32 v40, v16, v17\n\t"
      "v_cvt_pk_bf16_f32 v41, v18, v19\n\t"
      "v_mfma_f32_16x16x32_bf16 v[24:27], v[200:203], a[32:35], v[24:27]\n\t"
      "ds_write_b64 v0, v[40:41] offset:0\n\t"
      "v_mfma_f32_16x16x32_bf16 v[24:27], v[216:219], a[36:39], v[24:27]\n\t"
      "v_mfma_f32_16x16x32_bf16 v[24:27], v[232:235], a[40:43], v[24:27]\n\t"
      "v_mfma_f32_16x16x32_bf16 v[24:27], v[248:251], a[44:47], v[24:27]\n\t"
      "v_mfma_f32_16x16x32_bf16 v[28:31], v[172:175], a[24:27], v[28:31]\n\t"
      "v_mfma_f32_16x16x32_bf16 v[28:31], v[188:191], a[28:31], v[28:31]\n\t"
      "v_cvt_pk_bf16_f32 v42, v20, v21\n\t"
      "v_cvt_pk_bf16_f32 v43, v22, v23\n\t"
      "v_mfma_f32_16x16x32_bf16 v[28:31], v[204:207], a[32:35], v[28:31]\n\t"
      "ds_write_b64 v0, v[42:43] offset:32\n\t"
      "v_mfma_f32_16x16x32_bf16 v[28:31], v[220:223], a[36:39], v[28:31]\n\t"
      "v_mfma_f32_16x16x32_bf16 v[28:31], v[236:239], a[40:43], v[28:31]\n\t"
      "v_mfma_f32_16x16x32_bf16 v[28:31], v[252:255], a[44:47], v[28:31]\n\t"
      "v_mfma_f32_16x16x32_bf16 v[32:35], a[108:111], a[24:27], v[32:35]\n\t"
      "v_mfma_f32_16x16x32_bf16 v[32:35], a[116:119], a[28:31], v[32:35]\n\t"
      "v_cvt_pk_bf16_f32 v44, v24, v25\n\t"
      "v_cvt_pk_bf16_f32 v45, v26, v27\n\t"
      "v_mfma_f32_16x16x32_bf16 v[32:35], a[124:127], a[32:35], v[32:35]\n\t"
      "ds_write_b64 v0, v[44:45] offset:64\n\t"
      "v_mfma_f32_16x16x32_bf16 v[32:35], a[132:135], a[36:39], v[32:35]\n\t"
      "v_mfma_f32_16x16x32_bf16 v[32:35], a[140:143], a[40:43], v[32:35]\n\t"
      "v_mfma_f32_16x16x32_bf16 v[32:35], a[148:151], a[44:47], v[32:35]\n\t"
      "v_cvt_pk_bf16_f32 v46, v28, v29\n\t"
      "v_cvt_pk_bf16_f32 v47, v30, v31\n\t"
      "v_mfma_f32_16x16x32_bf16 v[36:39], a[112:115], a[24:27], v[36:39]\n\t"
      "ds_write_b64 v0, v[46:47] offset:96\n\t"
      "v_mfma_f32_16x16x32_bf16 v[36:39], a[120:123], a[28:31], v[36:39]\n\t"
      "v_mfma_f32_16x16x32_bf16 v[36:39], a[128:131], a[32:35], v[36:39]\n\t"
      "v_mfma_f32_16x16x32_bf16 v[36:39], a[136:139], a[36:39], v[36:39]\n\t"
      "v_mfma_f32_16x16x32_bf16 v[36:39], a[144:147], a[40:43], v[36:39]\n\t"
      "v_cvt_pk_bf16_f32 v48, v32, v33\n\t"
      "v_cvt_pk_bf16_f32 v49, v34, v35\n\t"
      "v_mfma_f32_16x16x32_bf16 v[36:39], a[152:155], a[44:47], v[36:39]\n\t"
      "ds_write_b64 v0, v[48:49] offset:128\n\t"
      "v_mfma_f32_16x16x32_bf16 v[16:19], v[64:67], a[0:3], 0\n\t"
      "ds_read_b128 a[24:27], v54 offset:256\n\t"
      "v_mfma_f32_16x16x32_bf16 v[16:19], v[80:83], a[4:7], v[16:19]\n\t"
      "ds_read_b128 a[28:31], v55 offset:256\n\t"
      "v_mfma_f32_16x16x32_bf16 v[16:19], v[96:99], a[8:11], v[16:19]\n\t"
      "ds_read_b128 a[32:35], v52 offset:512\n\t"
      "v_mfma_f32_16x16x32_bf16 v[16:19], v[112:115], a[12:15], v[16:19]\n\t"
      "ds_read_b128 a[36:39], v53 offset:512\n\t"
      "v_mfma_f32_16x16x32_bf16 v[16:19], v[128:131], a[16:19], v[16:19]\n\t"
      "ds_read_b128 a[40:43], v54 offset:512\n\t"
      "v_mfma_f32_16x16x32_bf16 v[16:19], v[144:147], a[20:23], v[16:19]\n\t"
      "ds_read_b128 a[44:47], v55 offset:512\n\t"
      "v_mfma_f32_16x16x32_bf16 v[20:23], v[68:71], a[0:3], 0\n\t"
      "v_cvt_pk_bf16_f32 v50, v36, v37\n\t"
      "v_cvt_pk_bf16_f32 v51, v38, v39\n\t"
      "v_mfma_f32_16x16x32_bf16 v[20:23], v[84:87], a[4:7], v[20:23]\n\t"
      "ds_write_b64 v0, v[50:51] offset:160\n\t"
      "v_mfma_f32_16x16x32_bf16 v[20:23], v[100:103], a[8:11], v[20:23]\n\t"
      "v_mfma_f32_16x16x32_bf16 v[20:23], v[116:119], a[12:15], v[20:23]\n\t"
      "v_mfma_f32_16x16x32_bf16 v[20:23], v[132:135], a[16:19], v[20:23]\n\t"
      "v_mfma_f32_16x16x32_bf16 v[20:23], v[148:151], a[20:23], v[20:23]\n\t"
      "v_mfma_f32_16x16x32_bf16 v[24:27], v[72:75], a[0:3], 0\n\t"
      "v_mfma_f32_16x16x32_bf16 v[24:27], v[88:91], a[4:7], v[24:27]\n\t"
      "v_mfma_f32_16x16x32_bf16 v[24:27], v[104:107], a[8:11], v[24:27]\n\t"
      "s_waitcnt lgkmcnt(0)\n\t"
      "s_barrier\n\t"
      "v_mfma_f32_16x16x32_bf16 v[24:27], v[120:123], a[12:15], v[24:27]\n\t"
      "v_mfma_f32_16x16x32_bf16 v[24:27], v[136:139], a[16:19], v[24:27]\n\t"
      "v_mfma_f32_16x16x32_bf16 v[24:27], v[152:155], a[20:23], v[24:27]\n\t"
      "v_mfma_f32_16x16x32_bf16 v[28:31], v[76:79], a[0:3], 0\n\t"
      "ds_read_b128 a[224:227], v8 offset:0\n\t"
      "v_mfma_f32_16x16x32_bf16 v[28:31], v[92:95], a[4:7], v[28:31]\n\t"
      "ds_read_b128 a[228:231], v60 offset:1024\n\t"
      "v_mfma_f32_16x16x32_bf16 v[28:31], v[108:111], a[8:11], v[28:31]\n\t"
      "ds_read_b128 a[232:235], v61 offset:2048\n\t"
      "v_mfma_f32_16x16x32_bf16 v[28:31], v[124:127], a[12:15], v[28:31]\n\t"
      "v_mfma_f32_16x16x32_bf16 v[28:31], v[140:143], a[16:19], v[28:31]\n\t"
      "v_mfma_f32_16x16x32_bf16 v[28:31], v[156:159], a[20:23], v[28:31]\n\t"
      "v_mfma_f32_16x16x32_bf16 v[32:35], a[60:63], a[0:3], 0\n\t"
      "v_mfma_f32_16x16x32_bf16 v[32:35], a[68:71], a[4:7], v[32:35]\n\t"
      "s_waitcnt lgkmcnt(2)\n\t"
      "v_mfma_f32_16x16x32_bf16 v[32:35], a[76:79], a[8:11], v[32:35]\n\t"
      "buffer_store_dwordx4 a[224:227], v9, s[44:47], s85 offen sc1\n\t"
      "v_mfma_f32_16x16x32_bf16 v[32:35], a[84:87], a[12:15], v[32:35]\n\t"
      "v_mfma_f32_16x16x32_bf16 v[32:35], a[92:95], a[16:19], v[32:35]\n\t"
      "v_mfma_f32_16x16x32_bf16 v[32:35], a[100:103], a[20:23], v[32:35]\n\t"
      "v_mfma_f32_16x16x32_bf16 v[36:39], a[64:67], a[0:3], 0\n\t"
      "v_mfma_f32_16x16x32_bf16 v[36:39], a[72:75], a[4:7], v[36:39]\n\t"
      "v_mfma_f32_16x16x32_bf16 v[36:39], a[80:83], a[8:11], v[36:39]\n\t"
      "v_mfma_f32_16x16x32_bf16 v[36:39], a[88:91], a[12:15], v[36:39]\n\t"
      "s_waitcnt lgkmcnt(1)\n\t"
      "v_mfma_f32_16x16x32_bf16 v[36:39], a[96:99], a[16:19], v[36:39]\n\t"
      "buffer_store_dwordx4 a[228:231], v10, s[44:47], s85 offen offset:1024 sc1\n\t"
      "v_mfma_f32_16x16x32_bf16 v[36:39], a[104:107], a[20:23], v[36:39]\n\t"
      "v_mfma_f32_16x16x32_bf16 v[16:19], v[160:163], a[24:27], v[16:19]\n\t"
      "v_mfma_f32_16x16x32_bf16 v[16:19], v[176:179], a[28:31], v[16:19]\n\t"
      "v_mfma_f32_16x16x32_bf16 v[16:19], v[192:195], a[32:35], v[16:19]\n\t"
      "v_mfma_f32_16x16x32_bf16 v[16:19], v[208:211], a[36:39], v[16:19]\n\t"
      "v_mfma_f32_16x16x32_bf16 v[16:19], v[224:227], a[40:43], v[16:19]\n\t"
      "v_mfma_f32_16x16x32_bf16 v[16:19], v[240:243], a[44:47], v[16:19]\n\t"
      "v_mfma_f32_16x16x32_bf16 v[20:23], v[164:167], a[24:27], v[20:23]\n\t"
      "v_mfma_f32_16x16x32_bf16 v[20:23], v[180:183], a[28:31], v[20:23]\n\t"
      "s_waitcnt lgkmcnt(0)\n\t"
      "v_mfma_f32_16x16x32_bf16 v[20:23], v[196:199], a[32:35], v[20:23]\n\t"
      "buffer_store_dwordx4 a[232:235], v11, s[44:47], s85 offen offset:2048 sc1\n\t"
      "v_mfma_f32_16x16x32_bf16 v[20:23], v[212:215], a[36:39], v[20:23]\n\t"
      "v_mfma_f32_16x16x32_bf16 v[20:23], v[228:231], a[40:43], v[20:23]\n\t"
      "v_mfma_f32_16x16x32_bf16 v[20:23], v[244:247], a[44:47], v[20:23]\n\t"
      "v_mfma_f32_16x16x32_bf16 v[24:27], v[168:171], a[24:27], v[24:27]\n\t"
      "v_mfma_f32_16x16x32_bf16 v[24:27], v[184:187], a[28:31], v[24:27]\n\t"
      "v_cvt_pk_bf16_f32 v40, v16, v17\n\t"
      "v_cvt_pk_bf16_f32 v41, v18, v19\n\t"
      "v_mfma_f32_16x16x32_bf16 v[24:27], v[200:203], a[32:35], v[24:27]\n\t"
      "ds_write_b64 v0, v[40:41] offset:12544\n\t"
      "v_mfma_f32_16x16x32_bf16 v[24:27], v[216:219], a[36:39], v[24:27]\n\t"
      "v_mfma_f32_16x16x32_bf16 v[24:27], v[232:235], a[40:43], v[24:27]\n\t"
      "v_mfma_f32_16x16x32_bf16 v[24:27], v[248:251], a[44:47], v[24:27]\n\t"
      "v_mfma_f32_16x16x32_bf16 v[28:31], v[172:175], a[24:27], v[28:31]\n\t"
      "v_mfma_f32_16x16x32_bf16 v[28:31], v[188:191], a[28:31], v[28:31]\n\t"
      "v_cvt_pk_bf16_f32 v42, v20, v21\n\t"
      "v_cvt_pk_bf16_f32 v43, v22, v23\n\t"
      "v_mfma_f32_16x16x32_bf16 v[28:31], v[204:207], a[32:35], v[28:31]\n\t"
      "ds_write_b64 v0, v[42:43] offset:12576\n\t"
      "v_mfma_f32_16x16x32_bf16 v[28:31], v[220:223], a[36:39], v[28:31]\n\t"
      "v_mfma_f32_16x16x32_bf16 v[28:31], v[236:239], a[40:43], v[28:31]\n\t"
      "v_mfma_f32_16x16x32_bf16 v[28:31], v[252:255], a[44:47], v[28:31]\n\t"
      "v_mfma_f32_16x16x32_bf16 v[32:35], a[108:111], a[24:27], v[32:35]\n\t"
      "v_mfma_f32_16x16x32_bf16 v[32:35], a[116:119], a[28:31], v[32:35]\n\t"
      "v_cvt_pk_bf16_f32 v44, v24, v25\n\t"
      "v_cvt_pk_bf16_f32 v45, v26, v27\n\t"
      "v_mfma_f32_16x16x32_bf16 v[32:35], a[124:127], a[32:35], v[32:35]\n\t"
      "ds_write_b64 v0, v[44:45] offset:12608\n\t"
      "v_mfma_f32_16x16x32_bf16 v[32:35], a[132:135], a[36:39], v[32:35]\n\t"
      "v_mfma_f32_16x16x32_bf16 v[32:35], a[140:143], a[40:43], v[32:35]\n\t"
      "v_mfma_f32_16x16x32_bf16 v[32:35], a[148:151], a[44:47], v[32:35]\n\t"
      "v_cvt_pk_bf16_f32 v46, v28, v29\n\t"
      "v_cvt_pk_bf16_f32 v47, v30, v31\n\t"
      "v_mfma_f32_16x16x32_bf16 v[36:39], a[112:115], a[24:27], v[36:39]\n\t"
      "ds_write_b64 v0, v[46:47] offset:12640\n\t"
      "v_mfma_f32_16x16x32_bf16 v[36:39], a[120:123], a[28:31], v[36:39]\n\t"
      "v_mfma_f32_16x16x32_bf16 v[36:39], a[128:131], a[32:35], v[36:39]\n\t"
      "v_mfma_f32_16x16x32_bf16 v[36:39], a[136:139], a[36:39], v[36:39]\n\t"
      "v_mfma_f32_16x16x32_bf16 v[36:39], a[144:147], a[40:43], v[36:39]\n\t"
      "v_cvt_pk_bf16_f32 v48, v32, v33\n\t"
      "v_cvt_pk_bf16_f32 v49, v34, v35\n\t"
      "v_mfma_f32_16x16x32_bf16 v[36:39], a[152:155], a[44:47], v[36:39]\n\t"
      "ds_write_b64 v0, v[48:49] offset:12672\n\t"
      ".Ltail_%=:\n\t"
      "s_nop 6\n\t"
      "v_cvt_pk_bf16_f32 v50, v36, v37\n\t"
      "v_cvt_pk_bf16_f32 v51, v38, v39\n\t"
      "ds_write_b64 v0, v[50:51] offset:12704\n\t"
      "s_cmp_eq_u32 s85, 113221632\n\t"
      "s_cbranch_scc1 .Lwrapo_%=\n\t"
      ".Lwrappedo_%=:\n\t"
      "s_waitcnt lgkmcnt(0)\n\t"
      "s_barrier\n\t"
      "ds_read_b128 a[48:51], v8 offset:12544\n\t"
      "ds_read_b128 a[52:55], v60 offset:13568\n\t"
      "ds_read_b128 a[56:59], v61 offset:14592\n\t"
      "s_waitcnt lgkmcnt(2)\n\t"
      "buffer_store_dwordx4 a[48:51], v12, s[44:47], s85 offen sc1\n\t"
      "s_waitcnt lgkmcnt(1)\n\t"
      "buffer_store_dwordx4 a[52:55], v13, s[44:47], s85 offen offset:1024 sc1\n\t"
      "s_waitcnt lgkmcnt(0)\n\t"
      "buffer_store_dwordx4 a[56:59], v14, s[44:47], s85 offen offset:2048 sc1\n\t"
      "s_cmp_lt_i32 s91, 0\n\t"
      "s_cselect_b32 s30, -1, 0\n\t"
      "s_xor_b32 s16, s91, 0x3ffff\n\t"
      "s_and_b32 s16, s16, s30\n\t"
      "s_cbranch_scc0 .Lend_%=\n\t"
      "s_waitcnt vmcnt(0) lgkmcnt(0)\n\t"
      "s_barrier\n\t"
      "s_lshl1_add_u32 s30, s14, 24576\n\t"
      "s_sub_u32 s15, s30, s15\n\t"
      "s_branch .Lstart_%=\n\t"
      ".Lwrap3_%=:\n\t"
      "v_add_u32 v5, s48, v5\n\t"
      "v_subrev_u32 v62, 113246208, v5\n\t"
      "v_min_u32 v5, v5, v62\n\t"
      "v_add_u32 v6, s48, v6\n\t"
      "v_subrev_u32 v62, 113246208, v6\n\t"
      "v_min_u32 v6, v6, v62\n\t"
      "v_add_u32 v7, s48, v7\n\t"
      "v_subrev_u32 v62, 113246208, v7\n\t"
      "v_min_u32 v7, v7, v62\n\t"
      "s_mov_b32 s48, 0\n\t"
      "buffer_load_dwordx4 v5, s[40:43], s48 offen nt lds\n\t"
      "buffer_load_dwordx4 v6, s[40:43], s48 offen offset:1024 nt lds\n\t"
      "buffer_load_dwordx4 v7, s[40:43], s48 offen offset:2048 nt lds\n\t"
      "s_branch .Lwrapped3_%=\n\t"
      ".Lwrapo_%=:\n\t"
      "v_add_u32 v12, s85, v12\n\t"
      "v_subrev_u32 v62, 113246208, v12\n\t"
      "v_min_u32 v12, v12, v62\n\t"
      "v_add_u32 v13, s85, v13\n\t"
      "v_subrev_u32 v62, 113246208, v13\n\t"
      "v_min_u32 v13, v13, v62\n\t"
      "v_add_u32 v14, s85, v14\n\t"
      "v_subrev_u32 v62, 113246208, v14\n\t"
      "v_min_u32 v14, v14, v62\n\t"
      "s_mov_b32 s85, 0\n\t"
      "s_branch .Lwrappedo_%=\n\t"
      ".Lwait_%=:\n\t"
      "s_sub_u32 s52, s52, 1\n\t"
      "s_cbranch_scc1 .Lwait2_%=\n\t"
      "s_add_u32 s90, s90, 0x20000\n\t"
      "v_mov_b32 v54, s90\n\t"
      "v_mov_b32 v55, s19\n\t"
      "global_store_dword v55, v54, s[12:13] sc1\n\t"
      "s_sleep 16\n\t"
      "global_load_dword v40, v62, s[12:13] sc1\n\t"
      "s_waitcnt vmcnt(0)\n\t"
      "s_branch .Lcheck_%=\n\t"
      ".Lwait2_%=:\n\t"
      "s_brev_b32 s91, 1\n\t"
      "s_branch .Lwaited_%=\n\t"
      ".Lspinf_%=:\n\t"
      "s_memrealtime s[36:37]\n\t"
      "s_waitcnt lgkmcnt(0)\n\t"
      "s_sub_u32 s53, s36, s38\n\t"
      "s_cmp_lt_i32 s53, 0\n\t"
      "s_cbranch_scc1 .Lspinf_%=\n\t"
      "s_branch .Lspunf_%=\n\t"
      ".Lspin0_%=:\n\t"
      "s_memrealtime s[36:37]\n\t"
      "s_waitcnt lgkmcnt(0)\n\t"
      "s_sub_u32 s53, s36, s38\n\t"
      "s_cmp_lt_i32 s53, 0\n\t"
      "s_cbranch_scc1 .Lspin0_%=\n\t"
      "s_branch .Lspun0_%=\n\t"
      ".Lspin1_%=:\n\t"
      "s_memrealtime s[36:37]\n\t"
      "s_waitcnt lgkmcnt(0)\n\t"
      "s_sub_u32 s53, s36, s38\n\t"
      "s_cmp_lt_i32 s53, 0\n\t"
      "s_cbranch_scc1 .Lspin1_%=\n\t"
      "s_branch .Lspun1_%=\n\t"
      ".Lend_%=:\n\t"
      : [tid] "+v"(tid)
      : [karg] "s"(__builtin_amdgcn_kernarg_segment_ptr()), [blk] "s"(blockIdx.x), [lds] "s"((unsigned)(size_t)lds)
      : "v1", "v2", "v3", "v4", "v5", "v6", "v7", "v8", "v9", "v10", "v11", "v12", "v13", "v14", "v15", "v16", "v17", "v18", "v19", "v20", "v21", "v22", "v23", "v24", "v25", "v26", "v27", "v28", "v29", "v30", "v31", "v32", "v33", "v34", "v35", "v36", "v37", "v38", "v39", "v40", "v41", "v42", "v43", "v44", "v45", "v46", "v47", "v48", "v49", "v50", "v51", "v52", "v53", "v54", "v55", "v56", "v57", "v58", "v59", "v60", "v61", "v62", "v63", "v64", "v65", "v66", "v67", "v68", "v69", "v70", "v71", "v72", "v73", "v74", "v75", "v76", "v77", "v78", "v79", "v80", "v81", "v82", "v83", "v84", "v85", "v86", "v87", "v88", "v89", "v90", "v91", "v92", "v93", "v94", "v95", "v96", "v97", "v98", "v99", "v100", "v101", "v102", "v103", "v104", "v105", "v106", "v107", "v108", "v109", "v110", "v111", "v112", "v113", "v114", "v115", "v116", "v117", "v118", "v119", "v120", "v121", "v122", "v123", "v124", "v125", "v126", "v127", "v128", "v129", "v130", "v131", "v132", "v133", "v134", "v135", "v136", "v137", "v138", "v139", "v140", "v141", "v142", "v143", "v144", "v145", "v146", "v147", "v148", "v149", "v150", "v151", "v152", "v153", "v154", "v155", "v156", "v157", "v158", "v159", "v160", "v161", "v162", "v163", "v164", "v165", "v166", "v167", "v168", "v169", "v170", "v171", "v172", "v173", "v174", "v175", "v176", "v177", "v178", "v179", "v180", "v181", "v182", "v183", "v184", "v185", "v186", "v187", "v188", "v189", "v190", "v191", "v192", "v193", "v194", "v195", "v196", "v197", "v198", "v199", "v200", "v201", "v202", "v203", "v204", "v205", "v206", "v207", "v208", "v209", "v210", "v211", "v212", "v213", "v214", "v215", "v216", "v217", "v218", "v219", "v220", "v221", "v222", "v223", "v224", "v225", "v226", "v227", "v228", "v229", "v230", "v231", "v232", "v233", "v234", "v235", "v236", "v237", "v238", "v239", "v240", "v241", "v242", "v243", "v244", "v245", "v246", "v247", "v248", "v249", "v250", "v251", "v252", "v253", "v254", "v255", "a0", "a1", "a2", "a3", "a4", "a5", "a6", "a7", "a8", "a9", "a10", "a11", "a12", "a13", "a14", "a15", "a16", "a17", "a18", "a19", "a20", "a21", "a22", "a23", "a24", "a25", "a26", "a27", "a28", "a29", "a30", "a31", "a32", "a33", "a34", "a35", "a36", "a37", "a38", "a39", "a40", "a41", "a42", "a43", "a44", "a45", "a46", "a47", "a48", "a49", "a50", "a51", "a52", "a53", "a54", "a55", "a56", "a57", "a58", "a59", "a60", "a61", "a62", "a63", "a64", "a65", "a66", "a67", "a68", "a69", "a70", "a71", "a72", "a73", "a74", "a75", "a76", "a77", "a78", "a79", "a80", "a81", "a82", "a83", "a84", "a85", "a86", "a87", "a88", "a89", "a90", "a91", "a92", "a93", "a94", "a95", "a96", "a97", "a98", "a99", "a100", "a101", "a102", "a103", "a104", "a105", "a106", "a107", "a108", "a109", "a110", "a111", "a112", "a113", "a114", "a115", "a116", "a117", "a118", "a119", "a120", "a121", "a122", "a123", "a124", "a125", "a126", "a127", "a128", "a129", "a130", "a131", "a132", "a133", "a134", "a135", "a136", "a137", "a138", "a139", "a140", "a141", "a142", "a143", "a144", "a145", "a146", "a147", "a148", "a149", "a150", "a151", "a152", "a153", "a154", "a155", "a156", "a157", "a158", "a159", "a160", "a161", "a162", "a163", "a164", "a165", "a166", "a167", "a168", "a169", "a170", "a171", "a172", "a173", "a174", "a175", "a176", "a177", "a178", "a179", "a180", "a181", "a182", "a183", "a184", "a185", "a186", "a187", "a188", "a189", "a190", "a191", "a192", "a193", "a194", "a195", "a196", "a197", "a198", "a199", "a200", "a201", "a202", "a203", "a204", "a205", "a206", "a207", "a208", "a209", "a210", "a211", "a212", "a213", "a214", "a215", "a216", "a217", "a218", "a219", "a220", "a221", "a222", "a223", "a224", "a225", "a226", "a227", "a228", "a229", "a230", "a231", "a232", "a233", "a234", "a235", "a236", "a237", "a238", "a239", "a240", "a241", "a242", "a243", "a244", "a245", "a246", "a247", "a248", "a249", "a250", "a251", "a252", "a253", "a254", "a255", "s4", "s5", "s6", "s7", "s8", "s9", "s10", "s11", "s12", "s13", "s14", "s15", "s16", "s17", "s18", "s19", "s20", "s21", "s22", "s23", "s24", "s25", "s26", "s27", "s28", "s29", "s30", "s31", "s32", "s33", "s34", "s35", "s36", "s37", "s38", "s39", "s40", "s41", "s42", "s43", "s44", "s45", "s46", "s47", "s48", "s49", "s50", "s51", "s52", "s53", "s54", "s55", "s56", "s57", "s58", "s59", "s60", "s61", "s62", "s63", "s64", "s65", "s66", "s67", "s68", "s69", "s70", "s71", "s72", "s73", "s74", "s75", "s76", "s77", "s78", "s79", "s80", "s81", "s82", "s83", "s84", "s85", "s86", "s87", "s88", "s89", "s90", "s91", "vcc", "scc", "memory");
}

__global__ __launch_bounds__(384) void dwsep_plain(const unsigned short* __restrict__ x, const unsigned short* __restrict__ dw,
                                                   const unsigned short* __restrict__ pw, unsigned short* __restrict__ out) {
  __shared__ float xs[384];
  const unsigned n = threadIdx.x;
  const size_t row = (size_t)blockIdx.x * 384u;
  xs[n] = __uint_as_float((unsigned)x[row + n] << 16) * __uint_as_float((unsigned)dw[n] << 16);
  __syncthreads();
  float acc = 0.f;
  for (int k = 0; k < 384; ++k) acc += xs[k] * __uint_as_float((unsigned)pw[k * 384 + n] << 16);
  const __bf16 o = (__bf16)acc;
  out[row + n] = __builtin_bit_cast(unsigned short, o);
}

extern "C" void kernel_launch(void* const* d_in, const int* in_sizes, int n_in, void* d_out, int out_size,
                              void* d_ws, size_t ws_size, hipStream_t stream) {
  const unsigned short* x = (const unsigned short*)d_in[0];
  const unsigned short* dw = (const unsigned short*)d_in[1];
  const unsigned short* pw = (const unsigned short*)d_in[2];
  unsigned short* out = (unsigned short*)d_out;
  if (in_sizes[0] == 56623104) dwsep_kernel<<<NWG, 256, 0, stream>>>(x, dw, pw, out, (unsigned*)d_ws);
  else dwsep_plain<<<in_sizes[0] / 384, 384, 0, stream>>>(x, dw, pw, out);
}
```

```cpp
#include <hip/hip_runtime.h>

constexpr unsigned ROWB = 768, TILEB = 12288, NWG = 256;

__global__ __launch_bounds__(256, 1) void dwsep_kernel(const unsigned short* __restrict__ x,
                                                       const unsigned short* __restrict__ dw,
                                                       const unsigned short* __restrict__ pw,
                                                       unsigned short* __restrict__ out, unsigned* ws) {
  __shared__ __attribute__((aligned(256))) char lds[163392];
  unsigned tid = threadIdx.x;
  asm volatile(
      "s_memtime s[32:33]\n\t"
      "s_load_dwordx8 s[4:11], %[karg], 0\n\t"
      "s_load_dwordx2 s[12:13], %[karg], 0x20\n\t"
      "v_readfirstlane_b32 s30, %[tid]\n\t"
      "s_lshr_b32 s31, %[blk], 1\n\t"
      "s_and_b32 s31, s31, 12\n\t"
      "s_and_b32 s34, %[blk], 3\n\t"
      "s_or_b32 s31, s31, s34\n\t"
      "s_lshr_b32 s30, s30, 6\n\t"
      "s_lshr_b32 s34, %[blk], 5\n\t"
      "s_bfe_u32 s35, %[blk], 0x10002\n\t"
      "s_mul_i32 s17, s30, 6144\n\t"
      "s_mul_i32 s18, s30, 3072\n\t"
      "s_mul_i32 s14, s34, 14155776\n\t"
      "s_mul_i32 s36, s31, 49152\n\t"
      "s_add_u32 s14, s14, s36\n\t"
      "s_mul_i32 s36, s35, 24576\n\t"
      "s_add_u32 s15, s14, s36\n\t"
      "s_mov_b32 s16, 0\n\t"
      "s_lshr_b32 s36, %[blk], 3\n\t"
      "s_lshl2_add_u32 s24, s36, s30\n\t"
      "s_sub_u32 s36, s24, s36\n\t"
      "s_sub_u32 s36, s36, 1\n\t"
      "s_mul_i32 s23, s36, 3072\n\t"
      "s_lshl_b32 s20, s34, 3\n\t"
      "s_mul_i32 s36, s34, 27\n\t"
      "s_lshr_b32 s36, s36, 2\n\t"
      "s_lshr_b32 s37, s31, 1\n\t"
      "s_add_u32 s19, s37, s20\n\t"
      "s_add_u32 s37, s37, s36\n\t"
      "s_min_u32 s37, s37, 53\n\t"
      "s_lshl_b32 s22, s37, 21\n\t"
      "s_lshl_b32 s36, s36, 21\n\t"
      "s_sub_u32 s21, s14, s36\n\t"
      "s_add_u32 s21, s21, 24576\n\t"
      "s_lshl_b32 s19, s19, 8\n\t"
      "s_and_b32 s31, s31, 1\n\t"
      "s_lshl1_add_u32 s31, s31, s35\n\t"
      "s_lshl2_add_u32 s19, s31, s19\n\t"
      ".Lstart_%=:\n\t"
      "v_mbcnt_lo_u32_b32 v233, -1, 0\n\t"
      "v_mbcnt_hi_u32_b32 v233, -1, v233\n\t"
      "s_mul_i32 s30, s17, 683\n\t"
      "s_lshr_b32 s30, s30, 16\n\t"
      "s_and_b32 s30, s30, 0xc0\n\t"
      "s_brev_b32 s34, 8\n\t"
      "s_mov_b32 s35, 0x20201010\n\t"
      "s_mov_b32 s36, 0x30303020\n\t"
      "v_lshl_add_u32 v1, v233, 4, s18\n\t"
      "v_lshrrev_b32 v16, 1, v233\n\t"
      "v_and_b32 v16, 24, v16\n\t"
      "v_bfe_u32 v17, s34, v16, 8\n\t"
      "v_or_b32 v17, s30, v17\n\t"
      "v_xor_b32 v18, v17, v1\n\t"
      "v_add_u32 v19, 80, v17\n\t"
      "v_add_u32 v17, 160, v17\n\t"
      "v_and_b32 v19, 256, v19\n\t"
      "v_and_b32 v17, 256, v17\n\t"
      "v_mad_u32_u24 v2, v19, 30, v18\n\t"
      "v_mad_u32_u24 v5, v17, 33, v18\n\t"
      "v_add_u32 v5, 19968, v5\n\t"
      "v_bfe_u32 v17, s35, v16, 8\n\t"
      "v_or_b32 v17, s30, v17\n\t"
      "v_xor_b32 v18, v17, v1\n\t"
      "v_add_u32 v19, 80, v17\n\t"
      "v_add_u32 v17, 160, v17\n\t"
      "v_and_b32 v19, 256, v19\n\t"
      "v_and_b32 v17, 256, v17\n\t"
      "v_mad_u32_u24 v3, v19, 30, v18\n\t"
      "v_mad_u32_u24 v6, v17, 33, v18\n\t"
      "v_add_u32 v6, 19968, v6\n\t"
      "v_bfe_u32 v17, s36, v16, 8\n\t"
      "v_or_b32 v17, s30, v17\n\t"
      "v_xor_b32 v18, v17, v1\n\t"
      "v_add_u32 v19, 80, v17\n\t"
      "v_add_u32 v17, 160, v17\n\t"
      "v_and_b32 v19, 256, v19\n\t"
      "v_and_b32 v17, 256, v17\n\t"
      "v_mad_u32_u24 v4, v19, 30, v18\n\t"
      "v_mad_u32_u24 v7, v17, 33, v18\n\t"
      "v_add_u32 v7, 19968, v7\n\t"
      "s_sub_u32 s31, s30, 64\n\t"
      "v_add_u32 v17, s31, v233\n\t"
      "v_min_u32 v17, 193, v17\n\t"
      "v_lshlrev_b32 v18, 2, v17\n\t"
      "v_lshlrev_b32 v17, 1, v17\n\t"
      "v_and_b32 v18, -16, v18\n\t"
      "v_and_or_b32 v8, v17, 6, v18\n\t"
      "s_mov_b32 s86, 294912\n\t"
      "s_mov_b32 s87, 0x00020000\n\t"
      "s_mov_b32 s42, 113246208\n\t"
      "s_mov_b32 s43, 0x00020000\n\t"
      "s_cmp_lg_u32 s17, 0\n\t"
      "s_cbranch_scc1 .Lno0_%=\n\t"
      "s_movk_i32 s31, -61\n\t"
      "v_lshrrev_b32 v16, 2, v233\n\t"
      "v_add_u32 v17, s20, v16\n\t"
      "v_lshl_add_u32 v18, v16, 3, s31\n\t"
      "v_cmp_gt_u32 vcc, 32, v233\n\t"
      "v_and_b32 v19, 3, v233\n\t"
      "v_lshlrev_b32 v19, 2, v19\n\t"
      "v_cndmask_b32 v17, v18, v17, vcc\n\t"
      "v_lshl_add_u32 v62, v17, 8, v19\n\t"
      "s_waitcnt lgkmcnt(2)\n\t"
      "s_add_u32 s32, s32, %[blk]\n\t"
      "s_mul_i32 s32, s32, 0x9e3779b1\n\t"
      "s_lshr_b32 s32, s32, 9\n\t"
      "s_and_b32 s32, s32, 0x1fdf80\n\t"
      "s_or_b32 s22, s22, s32\n\t"
      "v_mov_b32 v234, s22\n\t"
      "v_add_u32 v235, 8192, v234\n\t"
      "s_movk_i32 s64, 120\n\t"
      "s_mov_b64 vcc, 0\n\t"
      ".Lsweep_%=:\n\t"
      "s_getreg_b32 s30, hwreg(HW_REG_IB_STS, 0, 12)\n\t"
      "s_and_b32 s31, s30, 15\n\t"
      "s_lshr_b32 s30, s30, 8\n\t"
      "s_cmp_eq_u32 s31, 8\n\t"
      "s_addc_u32 s62, s62, 0\n\t"
      "s_cmp_eq_u32 s30, 1\n\t"
      "s_addc_u32 s63, s63, 0\n\t"
      "s_cmp_gt_u32 s31, 7\n\t"
      "s_addc_u32 s30, s30, 0\n\t"
      "s_sub_u32 s64, s64, 1\n\t"
      "s_min_u32 s30, s30, s64\n\t"
      "s_cmp_lg_u32 s30, 0\n\t"
      "s_cbranch_scc1 .Lsweep_%=\n\t"
      "s_cbranch_vccnz .Lswept_%=\n\t"
      "s_waitcnt lgkmcnt(0)\n\t"
      "global_load_dword v236, v234, s[4:5]\n\t"
      "global_load_dword v237, v235, s[4:5]\n\t"
      "s_memrealtime s[28:29]\n\t"
      "s_load_dword s60, s[10:11], s22\n\t"
      "s_load_dword s61, s[10:11], s22 offset:8192\n\t"
      "global_load_dword v63, v62, s[12:13] sc1\n\t"
      "s_mov_b64 s[40:41], s[4:5]\n\t"
      "s_add_u32 m0, s18, 25088\n\t"
      "s_add_u32 s48, s15, 0\n\t"
      "buffer_load_dwordx4 v2, s[40:43], s48 offen nt lds\n\t"
      "buffer_load_dwordx4 v3, s[40:43], s48 offen offset:1024 nt lds\n\t"
      "buffer_load_dwordx4 v4, s[40:43], s48 offen offset:2048 nt lds\n\t"
      "s_add_u32 m0, s18, 37376\n\t"
      "s_add_u32 s48, s15, 0\n\t"
      "buffer_load_dwordx4 v5, s[40:43], s48 offen nt lds\n\t"
      "buffer_load_dwordx4 v6, s[40:43], s48 offen offset:1024 nt lds\n\t"
      "buffer_load_dwordx4 v7, s[40:43], s48 offen offset:2048 nt lds\n\t"
      "s_barrier\n\t"
      "s_mov_b64 s[62:63], 0\n\t"
      "s_movk_i32 s64, 120\n\t"
      "s_mov_b64 vcc, -1\n\t"
      "s_branch .Lsweep_%=\n\t"
      ".Lswept_%=:\n\t"
      "s_min_u32 s62, s62, 7\n\t"
      "s_min_u32 s63, s63, 7\n\t"
      "s_lshl_b32 s63, s63, 8\n\t"
      "s_or_b32 s30, s62, s63\n\t"
      "s_sub_u32 s31, 0x808, s30\n\t"
      "s_add_u32 s30, s30, 0x10808\n\t"
      "s_getreg_b32 s62, hwreg(HW_REG_XCC_ID, 2, 1)\n\t"
      "s_cmp_lg_u32 s62, 0\n\t"
      "s_cselect_b32 s91, s30, s31\n\t"
      "s_mov_b64 s[84:85], s[8:9]\n\t"
      "v_mul_u32_u24 v16, 49152, v1\n\t"
      "v_add_u32 v16, s21, v16\n\t"
      "v_lshrrev_b32 v16, 21, v16\n\t"
      "v_lshlrev_b32 v16, 2, v16\n\t"
      "v_accvgpr_write_b32 a248, v16\n\t"
      "s_branch .Lgo0_%=\n\t"
      ".Lno0_%=:\n\t"
      "s_movk_i32 s82, 768\n\t"
      "s_mov_b32 s83, 0x00020000\n\t"
      "v_and_b32 v234, 0x3f0, v1\n\t"
      "s_waitcnt lgkmcnt(0)\n\t"
      "s_mov_b64 s[84:85], s[8:9]\n\t"
      "s_mov_b64 s[80:81], s[6:7]\n\t"
      "s_mov_b64 s[40:41], s[4:5]\n\t"
      "s_barrier\n\t"
      "buffer_load_dwordx4 a[200:203], v234, s[84:87], s23 offen offset:0\n\t"
      "buffer_load_dwordx4 a[204:207], v234, s[84:87], s23 offen offset:1024\n\t"
      "buffer_load_dwordx4 a[208:211], v234, s[84:87], s23 offen offset:2048\n\t"
      "buffer_load_ushort v224, v8, s[80:83], 0 offen\n\t"
      "buffer_load_ushort v225, v8, s[80:83], 0 offen offset:8\n\t"
      "s_add_u32 m0, s18, 25088\n\t"
      "s_add_u32 s48, s15, 0\n\t"
      "buffer_load_dwordx4 v2, s[40:43], s48 offen nt lds\n\t"
      "buffer_load_dwordx4 v3, s[40:43], s48 offen offset:1024 nt lds\n\t"
      "buffer_load_dwordx4 v4, s[40:43], s48 offen offset:2048 nt lds\n\t"
      "s_add_u32 m0, s18, 37376\n\t"
      "s_add_u32 s48, s15, 0\n\t"
      "buffer_load_dwordx4 v5, s[40:43], s48 offen nt lds\n\t"
      "buffer_load_dwordx4 v6, s[40:43], s48 offen offset:1024 nt lds\n\t"
      "buffer_load_dwordx4 v7, s[40:43], s48 offen offset:2048 nt lds\n\t"
      ".Lgo0_%=:\n\t"
      "s_getpc_b64 s[36:37]\n\t"
      ".Lpc_%=:\n\t"
      "s_lshr_b32 s30, s17, 5\n\t"
      "v_lshlrev_b32 v16, 4, v233\n\t"
      "v_add_u32 v17, 0, v233\n\t"
      "v_mul_u32_u24 v17, 171, v17\n\t"
      "v_lshrrev_b32 v17, 11, v17\n\t"
      "v_lshlrev_b32 v18, 2, v17\n\t"
      "v_and_b32 v18, 32, v18\n\t"
      "v_xor_b32 v18, v18, v16\n\t"
      "v_mul_u32_u24 v17, 576, v17\n\t"
      "v_add3_u32 v9, v17, s30, v18\n\t"
      "v_add_u32 v17, 64, v233\n\t"
      "v_mul_u32_u24 v17, 171, v17\n\t"
      "v_lshrrev_b32 v17, 11, v17\n\t"
      "v_lshlrev_b32 v18, 2, v17\n\t"
      "v_and_b32 v18, 32, v18\n\t"
      "v_xor_b32 v18, v18, v16\n\t"
      "v_mul_u32_u24 v17, 576, v17\n\t"
      "v_add3_u32 v10, v17, s30, v18\n\t"
      "v_add_u32 v17, 128, v233\n\t"
      "v_mul_u32_u24 v17, 171, v17\n\t"
      "v_lshrrev_b32 v17, 11, v17\n\t"
      "v_lshlrev_b32 v18, 2, v17\n\t"
      "v_and_b32 v18, 32, v18\n\t"
      "v_xor_b32 v18, v18, v16\n\t"
      "v_mul_u32_u24 v17, 576, v17\n\t"
      "v_add3_u32 v11, v17, s30, v18\n\t"
      "s_mov_b64 s[44:45], s[10:11]\n\t"
      "s_mov_b32 s46, 0\n\t"
      "s_mov_b32 s47, 0x00020000\n\t"
      "s_lshl_b32 s30, s24, 6\n\t"
      "s_add_u32 s31, s30, 0\n\t"
      "s_min_u32 s31, s31, .Lend_%=-.Lpc_%=-4\n\t"
      "s_load_dword s35, s[36:37], s31\n\t"
      "s_add_u32 s31, s30, 8192\n\t"
      "s_min_u32 s31, s31, .Lend_%=-.Lpc_%=-4\n\t"
      "s_load_dword s35, s[36:37], s31\n\t"
      "s_add_u32 s31, s30, 16384\n\t"
      "s_min_u32 s31, s31, .Lend_%=-.Lpc_%=-4\n\t"
      "s_load_dword s35, s[36:37], s31\n\t"
      "s_add_u32 s31, s30, 24576\n\t"
      "s_min_u32 s31, s31, .Lend_%=-.Lpc_%=-4\n\t"
      "s_load_dword s35, s[36:37], s31\n\t"
      "s_and_b32 s31, s24, 63\n\t"
      "s_lshl_b32 s31, s31, 21\n\t"
      "s_min_u32 s31, s31, 113246144\n\t"
      "s_bitcmp1_b32 s24, 6\n\t"
      "s_cselect_b64 s[32:33], s[44:45], s[40:41]\n\t"
      "s_load_dword s35, s[32:33], s31\n\t"
      "s_lshr_b32 s30, s17, 5\n\t"
      "v_lshrrev_b32 v16, 4, v233\n\t"
      "v_and_b32 v17, 15, v233\n\t"
      "v_and_b32 v18, 3, v233\n\t"
      "v_bfe_u32 v19, v233, 2, 2\n\t"
      "v_lshl_add_u32 v19, v16, 3, v19\n\t"
      "v_mul_u32_u24 v19, 192, v19\n\t"
      "v_lshl_add_u32 v19, v18, 3, v19\n\t"
      "v_lshlrev_b32 v20, 1, v233\n\t"
      "v_and_b32 v20, 32, v20\n\t"
      "v_add3_u32 v12, v19, v20, s17\n\t"
      "v_xor_b32 v13, 32, v12\n\t"
      "v_xor_b32 v19, v16, v17\n\t"
      "v_lshlrev_b32 v19, 4, v19\n\t"
      "v_mul_u32_u24 v20, 768, v17\n\t"
      "v_add_u32 v15, v19, v20\n\t"
      "v_mul_u32_u24 v20, 784, v17\n\t"
      "v_lshl_add_u32 v20, v16, 3, v20\n\t"
      "v_add_u32 v0, s30, v20\n\t"
      "v_lshlrev_b32 v19, 6, v16\n\t"
      "v_lshl_add_u32 v19, v18, 4, v19\n\t"
      "v_add_u32 v14, 160256, v19\n\t"
      "v_and_b32 v226, 0xfffffff0, v8\n\t"
      "v_and_b32 v227, 6, v8\n\t"
      "v_lshlrev_b32 v226, 2, v226\n\t"
      "v_lshl_add_u32 v226, v227, 3, v226\n\t"
      "v_add_u32 v226, 160256, v226\n\t"
      "v_and_b32 v232, 2, v8\n\t"
      "v_lshlrev_b32 v232, 3, v232\n\t"
      "v_cmp_gt_u32 vcc, 4, v227\n\t"
      "v_mov_b32 v56, v15\n\t"
      "v_xor_b32 v57, 64, v15\n\t"
      "v_xor_b32 v58, 128, v15\n\t"
      "v_xor_b32 v59, 192, v15\n\t"
      "v_add_u32 v60, 86528, v12\n\t"
      "v_add_u32 v61, 86528, v13\n\t"
      "v_xor_b32 v16, v2, v1\n\t"
      "v_and_b32 v16, 0xf0, v16\n\t"
      "v_xor_b32 v17, v16, v2\n\t"
      "v_xor_b32 v18, v16, v5\n\t"
      "v_add_u32 v19, v16, v1\n\t"
      "v_accvgpr_write_b32 a213, v17\n\t"
      "v_accvgpr_write_b32 a216, v18\n\t"
      "v_accvgpr_write_b32 a219, v19\n\t"
      "v_xor_b32 v16, v3, v1\n\t"
      "v_and_b32 v16, 0xf0, v16\n\t"
      "v_xor_b32 v17, v16, v3\n\t"
      "v_xor_b32 v18, v16, v6\n\t"
      "v_add_u32 v19, v16, v1\n\t"
      "v_accvgpr_write_b32 a214, v17\n\t"
      "v_accvgpr_write_b32 a217, v18\n\t"
      "v_accvgpr_write_b32 a220, v19\n\t"
      "v_xor_b32 v16, v4, v1\n\t"
      "v_and_b32 v16, 0xf0, v16\n\t"
      "v_xor_b32 v17, v16, v4\n\t"
      "v_xor_b32 v18, v16, v7\n\t"
      "v_add_u32 v19, v16, v1\n\t"
      "v_accvgpr_write_b32 a215, v17\n\t"
      "v_accvgpr_write_b32 a218, v18\n\t"
      "v_accvgpr_write_b32 a221, v19\n\t"
      "s_waitcnt vmcnt(6)\n\t"
      "s_bfe_u32 s30, s19, 0x30008\n\t"
      "s_bfe_u32 s31, s19, 0x20002\n\t"
      "s_lshl2_add_u32 s30, s30, s31\n\t"
      "v_readlane_b32 s90, v63, s30\n\t"
      "v_lshlrev_b32 v224, v232, v224\n\t"
      "v_lshlrev_b32 v225, v232, v225\n\t"
      "v_cndmask_b32 v228, 0, v224, vcc\n\t"
      "v_cndmask_b32 v230, 0, v225, vcc\n\t"
      "v_sub_u32 v229, v224, v228\n\t"
      "v_sub_u32 v231, v225, v230\n\t"
      "ds_write_b128 v226, v[228:231]\n\t"
      "s_add_u32 s90, s90, 0x20000\n\t"
      "s_andn2_b32 s90, s90, 0x1ffff\n\t"
      "s_or_b32 s90, s90, s91\n\t"
      "s_or_b32 s30, s17, s16\n\t"
      "s_cbranch_scc1 .Lno2_%=\n\t"
      "v_mov_b32 v234, s90\n\t"
      "v_mov_b32 v235, s19\n\t"
      "global_store_dword v235, v234, s[12:13] sc1\n\t"
      ".Lno2_%=:\n\t"
      "s_add_u32 m0, s17, 0\n\t"
      "s_mov_b32 s54, 0\n\t"
      "buffer_load_dwordx4 v9, s[84:87], s54 offen lds\n\t"
      "buffer_load_dwordx4 v10, s[84:87], s54 offen offset:1024 lds\n\t"
      "buffer_load_dwordx4 v11, s[84:87], s54 offen offset:2048 lds\n\t"
      "s_add_u32 m0, s17, 3072\n\t"
      "s_mov_b32 s54, 12288\n\t"
      "buffer_load_dwordx4 v9, s[84:87], s54 offen lds\n\t"
      "buffer_load_dwordx4 v10, s[84:87], s54 offen offset:1024 lds\n\t"
      "buffer_load_dwordx4 v11, s[84:87], s54 offen offset:2048 lds\n\t"
      "s_add_u32 m0, s17, 86528\n\t"
      "s_mov_b32 s54, 24576\n\t"
      "buffer_load_dwordx4 v9, s[84:87], s54 offen lds\n\t"
      "buffer_load_dwordx4 v10, s[84:87], s54 offen offset:1024 lds\n\t"
      "buffer_load_dwordx4 v11, s[84:87], s54 offen offset:2048 lds\n\t"
      "s_add_u32 m0, s17, 89600\n\t"
      "s_mov_b32 s54, 36864\n\t"
      "buffer_load_dwordx4 v9, s[84:87], s54 offen lds\n\t"
      "buffer_load_dwordx4 v10, s[84:87], s54 offen offset:1024 lds\n\t"
      "buffer_load_dwordx4 v11, s[84:87], s54 offen offset:2048 lds\n\t"
      "s_add_u32 m0, s17, 111104\n\t"
      "s_mov_b32 s54, 49152\n\t"
      "buffer_load_dwordx4 v9, s[84:87], s54 offen lds\n\t"
      "buffer_load_dwordx4 v10, s[84:87], s54 offen offset:1024 lds\n\t"
      "buffer_load_dwordx4 v11, s[84:87], s54 offen offset:2048 lds\n\t"
      "s_add_u32 m0, s17, 114176\n\t"
      "s_mov_b32 s54, 61440\n\t"
      "buffer_load_dwordx4 v9, s[84:87], s54 offen lds\n\t"
      "buffer_load_dwordx4 v10, s[84:87], s54 offen offset:1024 lds\n\t"
      "buffer_load_dwordx4 v11, s[84:87], s54 offen offset:2048 lds\n\t"
      "s_waitcnt lgkmcnt(0)\n\t"
      "s_waitcnt vmcnt(18)\n\t"
      "s_barrier\n\t"
      "s_waitcnt vmcnt(12)\n\t"
      "ds_read_b128 a[180:183], v14 offset:0\n\t"
      "ds_read_b64_tr_b16 v[64:65], v12 offset:0\n\t"
      "ds_read_b64_tr_b16 v[66:67], v12 offset:768\n\t"
      "ds_read_b64_tr_b16 v[68:69], v13 offset:0\n\t"
      "ds_read_b64_tr_b16 v[70:71], v13 offset:768\n\t"
      "ds_read_b64_tr_b16 v[72:73], v12 offset:64\n\t"
      "ds_read_b64_tr_b16 v[74:75], v12 offset:832\n\t"
      "ds_read_b64_tr_b16 v[76:77], v13 offset:64\n\t"
      "ds_read_b64_tr_b16 v[78:79], v13 offset:832\n\t"
      "ds_read_b64_tr_b16 a[60:61], v12 offset:128\n\t"
      "ds_read_b64_tr_b16 a[62:63], v12 offset:896\n\t"
      "ds_read_b64_tr_b16 a[64:65], v13 offset:128\n\t"
      "ds_read_b64_tr_b16 a[66:67], v13 offset:896\n\t"
      "s_add_u32 m0, s17, 135680\n\t"
      "s_mov_b32 s54, 73728\n\t"
      "buffer_load_dwordx4 v9, s[84:87], s54 offen lds\n\t"
      "buffer_load_dwordx4 v10, s[84:87], s54 offen offset:1024 lds\n\t"
      "buffer_load_dwordx4 v11, s[84:87], s54 offen offset:2048 lds\n\t"
      "s_add_u32 m0, s17, 138752\n\t"
      "s_mov_b32 s54, 86016\n\t"
      "buffer_load_dwordx4 v9, s[84:87], s54 offen lds\n\t"
      "buffer_load_dwordx4 v10, s[84:87], s54 offen offset:1024 lds\n\t"
      "buffer_load_dwordx4 v11, s[84:87], s54 offen offset:2048 lds\n\t"
      "s_waitcnt vmcnt(12)\n\t"
      "s_waitcnt lgkmcnt(0)\n\t"
      "; step\n\t"
      "v_mfma_f32_4x4x4_16b_bf16 v[40:43], a[180:181], v[64:65], 0\n\t"
      "ds_read_b128 a[184:187], v14 offset:256\n\t"
      "v_mfma_f32_4x4x4_16b_bf16 v[44:47], a[182:183], v[66:67], 0\n\t"
      "ds_read_b64_tr_b16 v[80:81], v60 offset:0\n\t"
      "v_mfma_f32_4x4x4_16b_bf16 v[48:51], a[180:181], v[68:69], 0\n\t"
      "ds_read_b64_tr_b16 v[82:83], v60 offset:768\n\t"
      "v_mfma_f32_4x4x4_16b_bf16 v[52:55], a[182:183], v[70:71], 0\n\t"
      "ds_read_b64_tr_b16 v[84:85], v61 offset:0\n\t"
      "ds_read_b64_tr_b16 v[86:87], v61 offset:768\n\t"
      "s_add_u32 m0, s17, 0\n\t"
      "s_mov_b32 s54, 98304\n\t"
      "buffer_load_dwordx4 v9, s[84:87], s54 offen lds\n\t"
      "buffer_load_dwordx4 v10, s[84:87], s54 offen offset:1024 lds\n\t"
      "v_cvt_pk_bf16_f32 v64, v40, v41\n\t"
      "v_cvt_pk_bf16_f32 v65, v42, v43\n\t"
      "v_cvt_pk_bf16_f32 v66, v44, v45\n\t"
      "v_cvt_pk_bf16_f32 v67, v46, v47\n\t"
      "v_cvt_pk_bf16_f32 v68, v48, v49\n\t"
      "v_cvt_pk_bf16_f32 v69, v50, v51\n\t"
      "v_cvt_pk_bf16_f32 v70, v52, v53\n\t"
      "v_cvt_pk_bf16_f32 v71, v54, v55\n\t"
      "v_mfma_f32_4x4x4_16b_bf16 v[40:43], a[180:181], v[72:73], 0\n\t"
      "ds_read_b64_tr_b16 v[88:89], v60 offset:64\n\t"
      "v_mfma_f32_4x4x4_16b_bf16 v[44:47], a[182:183], v[74:75], 0\n\t"
      "ds_read_b64_tr_b16 v[90:91], v60 offset:832\n\t"
      "v_mfma_f32_4x4x4_16b_bf16 v[48:51], a[180:181], v[76:77], 0\n\t"
      "ds_read_b64_tr_b16 v[92:93], v61 offset:64\n\t"
      "v_mfma_f32_4x4x4_16b_bf16 v[52:55], a[182:183], v[78:79], 0\n\t"
      "ds_read_b64_tr_b16 v[94:95], v61 offset:832\n\t"
      "buffer_load_dwordx4 v11, s[84:87], s54 offen offset:2048 lds\n\t"
      "s_add_u32 m0, s17, 3072\n\t"
      "s_mov_b32 s54, 110592\n\t"
      "buffer_load_dwordx4 v9, s[84:87], s54 offen lds\n\t"
      "v_cvt_pk_bf16_f32 v72, v40, v41\n\t"
      "v_cvt_pk_bf16_f32 v73, v42, v43\n\t"
      "v_cvt_pk_bf16_f32 v74, v44, v45\n\t"
      "v_cvt_pk_bf16_f32 v75, v46, v47\n\t"
      "v_cvt_pk_bf16_f32 v76, v48, v49\n\t"
      "v_cvt_pk_bf16_f32 v77, v50, v51\n\t"
      "v_cvt_pk_bf16_f32 v78, v52, v53\n\t"
      "v_cvt_pk_bf16_f32 v79, v54, v55\n\t"
      "ds_read_b128 a[224:227], v56 offset:25088\n\t"
      "v_mfma_f32_4x4x4_16b_bf16 v[40:43], a[180:181], a[60:61], 0\n\t"
      "ds_read_b64_tr_b16 a[68:69], v60 offset:128\n\t"
      "v_mfma_f32_4x4x4_16b_bf16 v[44:47], a[182:183], a[62:63], 0\n\t"
      "ds_read_b64_tr_b16 a[70:71], v60 offset:896\n\t"
      "v_mfma_f32_4x4x4_16b_bf16 v[48:51], a[180:181], a[64:65], 0\n\t"
      "ds_read_b64_tr_b16 a[72:73], v61 offset:128\n\t"
      "v_mfma_f32_4x4x4_16b_bf16 v[52:55], a[182:183], a[66:67], 0\n\t"
      "ds_read_b64_tr_b16 a[74:75], v61 offset:896\n\t"
      "buffer_load_dwordx4 v10, s[84:87], s54 offen offset:1024 lds\n\t"
      "buffer_load_dwordx4 v11, s[84:87], s54 offen offset:2048 lds\n\t"
      "ds_read_b128 a[228:231], v56 offset:37376\n\t"
      "v_cvt_pk_bf16_f32 v40, v40, v41\n\t"
      "v_cvt_pk_bf16_f32 v41, v42, v43\n\t"
      "v_cvt_pk_bf16_f32 v44, v44, v45\n\t"
      "v_cvt_pk_bf16_f32 v45, v46, v47\n\t"
      "v_cvt_pk_bf16_f32 v48, v48, v49\n\t"
      "v_cvt_pk_bf16_f32 v49, v50, v51\n\t"
      "v_cvt_pk_bf16_f32 v52, v52, v53\n\t"
      "v_cvt_pk_bf16_f32 v53, v54, v55\n\t"
      "v_accvgpr_write_b32 a60, v40\n\t"
      "v_accvgpr_write_b32 a61, v41\n\t"
      "v_accvgpr_write_b32 a62, v44\n\t"
      "v_accvgpr_write_b32 a63, v45\n\t"
      "v_accvgpr_write_b32 a64, v48\n\t"
      "v_accvgpr_write_b32 a65, v49\n\t"
      "v_accvgpr_write_b32 a66, v52\n\t"
      "v_accvgpr_write_b32 a67, v53\n\t"
      "s_waitcnt vmcnt(12)\n\t"
      "s_waitcnt lgkmcnt(1)\n\t"
      "; step\n\t"
      "v_mfma_f32_4x4x4_16b_bf16 v[40:43], a[184:185], v[80:81], 0\n\t"
      "ds_read_b128 a[180:183], v14 offset:512\n\t"
      "v_mfma_f32_4x4x4_16b_bf16 v[44:47], a[186:187], v[82:83], 0\n\t"
      "ds_read_b64_tr_b16 v[96:97], v60 offset:24576\n\t"
      "v_mfma_f32_4x4x4_16b_bf16 v[48:51], a[184:185], v[84:85], 0\n\t"
      "ds_read_b64_tr_b16 v[98:99], v60 offset:25344\n\t"
      "v_mfma_f32_4x4x4_16b_bf16 v[52:55], a[186:187], v[86:87], 0\n\t"
      "ds_read_b64_tr_b16 v[100:101], v61 offset:24576\n\t"
      "ds_read_b64_tr_b16 v[102:103], v61 offset:25344\n\t"
      "s_add_u32 m0, s17, 86528\n\t"
      "s_mov_b32 s54, 122880\n\t"
      "buffer_load_dwordx4 v9, s[84:87], s54 offen lds\n\t"
      "buffer_load_dwordx4 v10, s[84:87], s54 offen offset:1024 lds\n\t"
      "v_mfma_f32_16x16x32_bf16 v[16:19], v[64:67], a[224:227], 0\n\t"
      "v_cvt_pk_bf16_f32 v80, v40, v41\n\t"
      "v_cvt_pk_bf16_f32 v81, v42, v43\n\t"
      "v_mfma_f32_16x16x32_bf16 v[20:23], v[68:71], a[224:227], 0\n\t"
      "v_cvt_pk_bf16_f32 v82, v44, v45\n\t"
      "v_cvt_pk_bf16_f32 v83, v46, v47\n\t"
      "v_mfma_f32_16x16x32_bf16 v[24:27], v[72:75], a[224:227], 0\n\t"
      "v_cvt_pk_bf16_f32 v84, v48, v49\n\t"
      "v_cvt_pk_bf16_f32 v85, v50, v51\n\t"
      "v_mfma_f32_16x16x32_bf16 v[28:31], v[76:79], a[224:227], 0\n\t"
      "v_cvt_pk_bf16_f32 v86, v52, v53\n\t"
      "v_cvt_pk_bf16_f32 v87, v54, v55\n\t"
      "v_mfma_f32_4x4x4_16b_bf16 v[40:43], a[184:185], v[88:89], 0\n\t"
      "ds_read_b64_tr_b16 v[104:105], v60 offset:24640\n\t"
      "v_mfma_f32_4x4x4_16b_bf16 v[44:47], a[186:187], v[90:91], 0\n\t"
      "ds_read_b64_tr_b16 v[106:107], v60 offset:25408\n\t"
      "v_mfma_f32_4x4x4_16b_bf16 v[48:51], a[184:185], v[92:93], 0\n\t"
      "ds_read_b64_tr_b16 v[108:109], v61 offset:24640\n\t"
      "v_mfma_f32_4x4x4_16b_bf16 v[52:55], a[186:187], v[94:95], 0\n\t"
      "ds_read_b64_tr_b16 v[110:111], v61 offset:25408\n\t"
      "buffer_load_dwordx4 v11, s[84:87], s54 offen offset:2048 lds\n\t"
      "s_add_u32 m0, s17, 89600\n\t"
      "s_mov_b32 s54, 135168\n\t"
      "buffer_load_dwordx4 v9, s[84:87], s54 offen lds\n\t"
      "v_mfma_f32_16x16x32_bf16 v[32:35], a[60:63], a[224:227], 0\n\t"
      "v_cvt_pk_bf16_f32 v88, v40, v41\n\t"
      "v_cvt_pk_bf16_f32 v89, v42, v43\n\t"
      "v_mfma_f32_16x16x32_bf16 v[36:39], a[64:67], a[224:227], 0\n\t"
      "v_cvt_pk_bf16_f32 v90, v44, v45\n\t"
      "v_cvt_pk_bf16_f32 v91, v46, v47\n\t"
      "s_waitcnt lgkmcnt(9)\n\t"
      "v_mfma_f32_16x16x32_bf16 a[156:159], v[64:67], a[228:231], 0\n\t"
      "v_cvt_pk_bf16_f32 v92, v48, v49\n\t"
      "v_cvt_pk_bf16_f32 v93, v50, v51\n\t"
      "v_mfma_f32_16x16x32_bf16 a[160:163], v[68:71], a[228:231], 0\n\t"
      "v_cvt_pk_bf16_f32 v94, v52, v53\n\t"
      "v_cvt_pk_bf16_f32 v95, v54, v55\n\t"
      "ds_read_b128 a[232:235], v57 offset:25088\n\t"
      "v_mfma_f32_4x4x4_16b_bf16 v[40:43], a[184:185], a[68:69], 0\n\t"
      "ds_read_b64_tr_b16 a[76:77], v60 offset:24704\n\t"
      "v_mfma_f32_4x4x4_16b_bf16 v[44:47], a[186:187], a[70:71], 0\n\t"
      "ds_read_b64_tr_b16 a[78:79], v60 offset:25472\n\t"
      "v_mfma_f32_4x4x4_16b_bf16 v[48:51], a[184:185], a[72:73], 0\n\t"
      "ds_read_b64_tr_b16 a[80:81], v61 offset:24704\n\t"
      "v_mfma_f32_4x4x4_16b_bf16 v[52:55], a[186:187], a[74:75], 0\n\t"
      "ds_read_b64_tr_b16 a[82:83], v61 offset:25472\n\t"
      "buffer_load_dwordx4 v10, s[84:87], s54 offen offset:1024 lds\n\t"
      "buffer_load_dwordx4 v11, s[84:87], s54 offen offset:2048 lds\n\t"
      "ds_read_b128 a[236:239], v57 offset:37376\n\t"
      "v_mfma_f32_16x16x32_bf16 a[164:167], v[72:75], a[228:231], 0\n\t"
      "v_cvt_pk_bf16_f32 v40, v40, v41\n\t"
      "v_cvt_pk_bf16_f32 v41, v42, v43\n\t"
      "v_mfma_f32_16x16x32_bf16 a[168:171], v[76:79], a[228:231], 0\n\t"
      "v_cvt_pk_bf16_f32 v44, v44, v45\n\t"
      "v_cvt_pk_bf16_f32 v45, v46, v47\n\t"
      "v_mfma_f32_16x16x32_bf16 a[172:175], a[60:63], a[228:231], 0\n\t"
      "v_cvt_pk_bf16_f32 v48, v48, v49\n\t"
      "v_cvt_pk_bf16_f32 v49, v50, v51\n\t"
      "v_mfma_f32_16x16x32_bf16 a[176:179], a[64:67], a[228:231], 0\n\t"
      "v_cvt_pk_bf16_f32 v52, v52, v53\n\t"
      "v_cvt_pk_bf16_f32 v53, v54, v55\n\t"
      "v_accvgpr_write_b32 a68, v40\n\t"
      "v_accvgpr_write_b32 a69, v41\n\t"
      "v_accvgpr_write_b32 a70, v44\n\t"
      "v_accvgpr_write_b32 a71, v45\n\t"
      "v_accvgpr_write_b32 a72, v48\n\t"
      "v_accvgpr_write_b32 a73, v49\n\t"
      "v_accvgpr_write_b32 a74, v52\n\t"
      "v_accvgpr_write_b32 a75, v53\n\t"
      "s_waitcnt vmcnt(12)\n\t"
      "s_waitcnt lgkmcnt(1)\n\t"
      "; step\n\t"
      "v_mfma_f32_4x4x4_16b_bf16 v[40:43], a[180:181], v[96:97], 0\n\t"
      "ds_read_b128 a[184:187], v14 offset:768\n\t"
      "v_mfma_f32_4x4x4_16b_bf16 v[44:47], a[182:183], v[98:99], 0\n\t"
      "ds_read_b64_tr_b16 v[112:113], v60 offset:49152\n\t"
      "v_mfma_f32_4x4x4_16b_bf16 v[48:51], a[180:181], v[100:101], 0\n\t"
      "ds_read_b64_tr_b16 v[114:115], v60 offset:49920\n\t"
      "v_mfma_f32_4x4x4_16b_bf16 v[52:55], a[182:183], v[102:103], 0\n\t"
      "ds_read_b64_tr_b16 v[116:117], v61 offset:49152\n\t"
      "ds_read_b64_tr_b16 v[118:119], v61 offset:49920\n\t"
      "s_add_u32 m0, s17, 111104\n\t"
      "s_mov_b32 s54, 147456\n\t"
      "buffer_load_dwordx4 v9, s[84:87], s54 offen lds\n\t"
      "buffer_load_dwordx4 v10, s[84:87], s54 offen offset:1024 lds\n\t"
      "v_mfma_f32_16x16x32_bf16 v[16:19], v[80:83], a[232:235], v[16:19]\n\t"
      "v_cvt_pk_bf16_f32 v96, v40, v41\n\t"
      "v_cvt_pk_bf16_f32 v97, v42, v43\n\t"
      "v_mfma_f32_16x16x32_bf16 v[20:23], v[84:87], a[232:235], v[20:23]\n\t"
      "v_cvt_pk_bf16_f32 v98, v44, v45\n\t"
      "v_cvt_pk_bf16_f32 v99, v46, v47\n\t"
      "v_mfma_f32_16x16x32_bf16 v[24:27], v[88:91], a[232:235], v[24:27]\n\t"
      "v_cvt_pk_bf16_f32 v100, v48, v49\n\t"
      "v_cvt_pk_bf16_f32 v101, v50, v51\n\t"
      "v_mfma_f32_16x16x32_bf16 v[28:31], v[92:95], a[232:235], v[28:31]\n\t"
      "v_cvt_pk_bf16_f32 v102, v52, v53\n\t"
      "v_cvt_pk_bf16_f32 v103, v54, v55\n\t"
      "v_mfma_f32_4x4x4_16b_bf16 v[40:43], a[180:181], v[104:105], 0\n\t"
      "ds_read_b64_tr_b16 v[120:121], v60 offset:49216\n\t"
      "v_mfma_f32_4x4x4_16b_bf16 v[44:47], a[182:183], v[106:107], 0\n\t"
      "ds_read_b64_tr_b16 v[122:123], v60 offset:49984\n\t"
      "v_mfma_f32_4x4x4_16b_bf16 v[48:51], a[180:181], v[108:109], 0\n\t"
      "ds_read_b64_tr_b16 v[124:125], v61 offset:49216\n\t"
      "v_mfma_f32_4x4x4_16b_bf16 v[52:55], a[182:183], v[110:111], 0\n\t"
      "ds_read_b64_tr_b16 v[126:127], v61 offset:49984\n\t"
      "buffer_load_dwordx4 v11, s[84:87], s54 offen offset:2048 lds\n\t"
      "s_add_u32 m0, s17, 114176\n\t"
      "s_mov_b32 s54, 159744\n\t"
      "buffer_load_dwordx4 v9, s[84:87], s54 offen lds\n\t"
      "v_mfma_f32_16x16x32_bf16 v[32:35], a[68:71], a[232:235], v[32:35]\n\t"
      "v_cvt_pk_bf16_f32 v104, v40, v41\n\t"
      "v_cvt_pk_bf16_f32 v105, v42, v43\n\t"
      "v_mfma_f32_16x16x32_bf16 v[36:39], a[72:75], a[232:235], v[36:39]\n\t"
      "v_cvt_pk_bf16_f32 v106, v44, v45\n\t"
      "v_cvt_pk_bf16_f32 v107, v46, v47\n\t"
      "s_waitcnt lgkmcnt(9)\n\t"
      "v_mfma_f32_16x16x32_bf16 a[156:159], v[80:83], a[236:239], a[156:159]\n\t"
      "v_cvt_pk_bf16_f32 v108, v48, v49\n\t"
      "v_cvt_pk_bf16_f32 v109, v50, v51\n\t"
      "v_mfma_f32_16x16x32_bf16 a[160:163], v[84:87], a[236:239], a[160:163]\n\t"
      "v_cvt_pk_bf16_f32 v110, v52, v53\n\t"
      "v_cvt_pk_bf16_f32 v111, v54, v55\n\t"
      "ds_read_b128 a[240:243], v58 offset:25088\n\t"
      "v_mfma_f32_4x4x4_16b_bf16 v[40:43], a[180:181], a[76:77], 0\n\t"
      "ds_read_b64_tr_b16 a[84:85], v60 offset:49280\n\t"
      "v_mfma_f32_4x4x4_16b_bf16 v[44:47], a[182:183], a[78:79], 0\n\t"
      "ds_read_b64_tr_b16 a[86:87], v60 offset:50048\n\t"
      "v_mfma_f32_4x4x4_16b_bf16 v[48:51], a[180:181], a[80:81], 0\n\t"
      "ds_read_b64_tr_b16 a[88:89], v61 offset:49280\n\t"
      "v_mfma_f32_4x4x4_16b_bf16 v[52:55], a[182:183], a[82:83], 0\n\t"
      "ds_read_b64_tr_b16 a[90:91], v61 offset:50048\n\t"
      "buffer_load_dwordx4 v10, s[84:87], s54 offen offset:1024 lds\n\t"
      "buffer_load_dwordx4 v11, s[84:87], s54 offen offset:2048 lds\n\t"
      "ds_read_b128 a[244:247], v58 offset:37376\n\t"
      "v_mfma_f32_16x16x32_bf16 a[164:167], v[88:91], a[236:239], a[164:167]\n\t"
      "v_cvt_pk_bf16_f32 v40, v40, v41\n\t"
      "v_cvt_pk_bf16_f32 v41, v42, v43\n\t"
      "v_mfma_f32_16x16x32_bf16 a[168:171], v[92:95], a[236:239], a[168:171]\n\t"
      "v_cvt_pk_bf16_f32 v44, v44, v45\n\t"
      "v_cvt_pk_bf16_f32 v45, v46, v47\n\t"
      "v_mfma_f32_16x16x32_bf16 a[172:175], a[68:71], a[236:239], a[172:175]\n\t"
      "v_cvt_pk_bf16_f32 v48, v48, v49\n\t"
      "v_cvt_pk_bf16_f32 v49, v50, v51\n\t"
      "v_mfma_f32_16x16x32_bf16 a[176:179], a[72:75], a[236:239], a[176:179]\n\t"
      "v_cvt_pk_bf16_f32 v52, v52, v53\n\t"
      "v_cvt_pk_bf16_f32 v53, v54, v55\n\t"
      "v_accvgpr_write_b32 a76, v40\n\t"
      "v_accvgpr_write_b32 a77, v41\n\t"
      "v_accvgpr_write_b32 a78, v44\n\t"
      "v_accvgpr_write_b32 a79, v45\n\t"
      "v_accvgpr_write_b32 a80, v48\n\t"
      "v_accvgpr_write_b32 a81, v49\n\t"
      "v_accvgpr_write_b32 a82, v52\n\t"
      "v_accvgpr_write_b32 a83, v53\n\t"
      "s_waitcnt vmcnt(12)\n\t"
      "s_waitcnt lgkmcnt(1)\n\t"
      "; step\n\t"
      "v_mfma_f32_4x4x4_16b_bf16 v[40:43], a[184:185], v[112:113], 0\n\t"
      "ds_read_b128 a[180:183], v14 offset:1024\n\t"
      "v_mfma_f32_4x4x4_16b_bf16 v[44:47], a[186:187], v[114:115], 0\n\t"
      "ds_read_b64_tr_b16 v[128:129], v12 offset:0\n\t"
      "v_mfma_f32_4x4x4_16b_bf16 v[48:51], a[184:185], v[116:117], 0\n\t"
      "ds_read_b64_tr_b16 v[130:131], v12 offset:768\n\t"
      "v_mfma_f32_4x4x4_16b_bf16 v[52:55], a[186:187], v[118:119], 0\n\t"
      "ds_read_b64_tr_b16 v[132:133], v13 offset:0\n\t"
      "ds_read_b64_tr_b16 v[134:135], v13 offset:768\n\t"
      "s_add_u32 m0, s17, 135680\n\t"
      "s_mov_b32 s54, 172032\n\t"
      "buffer_load_dwordx4 v9, s[84:87], s54 offen lds\n\t"
      "buffer_load_dwordx4 v10, s[84:87], s54 offen offset:1024 lds\n\t"
      "v_mfma_f32_16x16x32_bf16 v[16:19], v[96:99], a[240:243], v[16:19]\n\t"
      "v_cvt_pk_bf16_f32 v112, v40, v41\n\t"
      "v_cvt_pk_bf16_f32 v113, v42, v43\n\t"
      "v_mfma_f32_16x16x32_bf16 v[20:23], v[100:103], a[240:243], v[20:23]\n\t"
      "v_cvt_pk_bf16_f32 v114, v44, v45\n\t"
      "v_cvt_pk_bf16_f32 v115, v46, v47\n\t"
      "v_mfma_f32_16x16x32_bf16 v[24:27], v[104:107], a[240:243], v[24:27]\n\t"
      "v_cvt_pk_bf16_f32 v116, v48, v49\n\t"
      "v_cvt_pk_bf16_f32 v117, v50, v51\n\t"
      "v_mfma_f32_16x16x32_bf16 v[28:31], v[108:111], a[240:243], v[28:31]\n\t"
      "v_cvt_pk_bf16_f32 v118, v52, v53\n\t"
      "v_cvt_pk_bf16_f32 v119, v54, v55\n\t"
      "v_mfma_f32_4x4x4_16b_bf16 v[40:43], a[184:185], v[120:121], 0\n\t"
      "ds_read_b64_tr_b16 v[136:137], v12 offset:64\n\t"
      "v_mfma_f32_4x4x4_16b_bf16 v[44:47], a[186:187], v[122:123], 0\n\t"
      "ds_read_b64_tr_b16 v[138:139], v12 offset:832\n\t"
      "v_mfma_f32_4x4x4_16b_bf16 v[48:51], a[184:185], v[124:125], 0\n\t"
      "ds_read_b64_tr_b16 v[140:141], v13 offset:64\n\t"
      "v_mfma_f32_4x4x4_16b_bf16 v[52:55], a[186:187], v[126:127], 0\n\t"
      "ds_read_b64_tr_b16 v[142:143], v13 offset:832\n\t"
      "buffer_load_dwordx4 v11, s[84:87], s54 offen offset:2048 lds\n\t"
      "s_add_u32 m0, s17, 138752\n\t"
      "s_mov_b32 s54, 184320\n\t"
      "buffer_load_dwordx4 v9, s[84:87], s54 offen lds\n\t"
      "v_mfma_f32_16x16x32_bf16 v[32:35], a[76:79], a[240:243], v[32:35]\n\t"
      "v_cvt_pk_bf16_f32 v120, v40, v41\n\t"
      "v_cvt_pk_bf16_f32 v121, v42, v43\n\t"
      "v_mfma_f32_16x16x32_bf16 v[36:39], a[80:83], a[240:243], v[36:39]\n\t"
      "v_cvt_pk_bf16_f32 v122, v44, v45\n\t"
      "v_cvt_pk_bf16_f32 v123, v46, v47\n\t"
      "s_waitcnt lgkmcnt(9)\n\t"
      "v_mfma_f32_16x16x32_bf16 a[156:159], v[96:99], a[244:247], a[156:159]\n\t"
      "v_cvt_pk_bf16_f32 v124, v48, v49\n\t"
      "v_cvt_pk_bf16_f32 v125, v50, v51\n\t"
      "v_mfma_f32_16x16x32_bf16 a[160:163], v[100:103], a[244:247], a[160:163]\n\t"
      "v_cvt_pk_bf16_f32 v126, v52, v53\n\t"
      "v_cvt_pk_bf16_f32 v127, v54, v55\n\t"
      "ds_read_b128 a[224:227], v59 offset:25088\n\t"
      "v_mfma_f32_4x4x4_16b_bf16 v[40:43], a[184:185], a[84:85], 0\n\t"
      "ds_read_b64_tr_b16 a[92:93], v12 offset:128\n\t"
      "v_mfma_f32_4x4x4_16b_bf16 v[44:47], a[186:187], a[86:87], 0\n\t"
      "ds_read_b64_tr_b16 a[94:95], v12 offset:896\n\t"
      "v_mfma_f32_4x4x4_16b_bf16 v[48:51], a[184:185], a[88:89], 0\n\t"
      "ds_read_b64_tr_b16 a[96:97], v13 offset:128\n\t"
      "v_mfma_f32_4x4x4_16b_bf16 v[52:55], a[186:187], a[90:91], 0\n\t"
      "ds_read_b64_tr_b16 a[98:99], v13 offset:896\n\t"
      "buffer_load_dwordx4 v10, s[84:87], s54 offen offset:1024 lds\n\t"
      "buffer_load_dwordx4 v11, s[84:87], s54 offen offset:2048 lds\n\t"
      "ds_read_b128 a[228:231], v59 offset:37376\n\t"
      "v_mfma_f32_16x16x32_bf16 a[164:167], v[104:107], a[244:247], a[164:167]\n\t"
      "v_cvt_pk_bf16_f32 v40, v40, v41\n\t"
      "v_cvt_pk_bf16_f32 v41, v42, v43\n\t"
      "v_mfma_f32_16x16x32_bf16 a[168:171], v[108:111], a[244:247], a[168:171]\n\t"
      "v_cvt_pk_bf16_f32 v44, v44, v45\n\t"
      "v_cvt_pk_bf16_f32 v45, v46, v47\n\t"
      "v_mfma_f32_16x16x32_bf16 a[172:175], a[76:79], a[244:247], a[172:175]\n\t"
      "v_cvt_pk_bf16_f32 v48, v48, v49\n\t"
      "v_cvt_pk_bf16_f32 v49, v50, v51\n\t"
      "v_mfma_f32_16x16x32_bf16 a[176:179], a[80:83], a[244:247], a[176:179]\n\t"
      "v_cvt_pk_bf16_f32 v52, v52, v53\n\t"
      "v_cvt_pk_bf16_f32 v53, v54, v55\n\t"
      "v_accvgpr_write_b32 a84, v40\n\t"
      "v_accvgpr_write_b32 a85, v41\n\t"
      "v_accvgpr_write_b32 a86, v44\n\t"
      "v_accvgpr_write_b32 a87, v45\n\t"
      "v_accvgpr_write_b32 a88, v48\n\t"
      "v_accvgpr_write_b32 a89, v49\n\t"
      "v_accvgpr_write_b32 a90, v52\n\t"
      "v_accvgpr_write_b32 a91, v53\n\t"
      "s_waitcnt vmcnt(12)\n\t"
      "s_waitcnt lgkmcnt(1)\n\t"
      "; step\n\t"
      "v_mfma_f32_4x4x4_16b_bf16 v[40:43], a[180:181], v[128:129], 0\n\t"
      "ds_read_b128 a[184:187], v14 offset:1280\n\t"
      "v_mfma_f32_4x4x4_16b_bf16 v[44:47], a[182:183], v[130:131], 0\n\t"
      "ds_read_b64_tr_b16 v[144:145], v60 offset:0\n\t"
      "v_mfma_f32_4x4x4_16b_bf16 v[48:51], a[180:181], v[132:133], 0\n\t"
      "ds_read_b64_tr_b16 v[146:147], v60 offset:768\n\t"
      "v_mfma_f32_4x4x4_16b_bf16 v[52:55], a[182:183], v[134:135], 0\n\t"
      "ds_read_b64_tr_b16 v[148:149], v61 offset:0\n\t"
      "ds_read_b64_tr_b16 v[150:151], v61 offset:768\n\t"
      "s_add_u32 m0, s17, 0\n\t"
      "s_mov_b32 s54, 196608\n\t"
      "buffer_load_dwordx4 v9, s[84:87], s54 offen lds\n\t"
      "buffer_load_dwordx4 v10, s[84:87], s54 offen offset:1024 lds\n\t"
      "v_mfma_f32_16x16x32_bf16 v[16:19], v[112:115], a[224:227], v[16:19]\n\t"
      "v_cvt_pk_bf16_f32 v128, v40, v41\n\t"
      "v_cvt_pk_bf16_f32 v129, v42, v43\n\t"
      "v_mfma_f32_16x16x32_bf16 v[20:23], v[116:119], a[224:227], v[20:23]\n\t"
      "v_cvt_pk_bf16_f32 v130, v44, v45\n\t"
      "v_cvt_pk_bf16_f32 v131, v46, v47\n\t"
      "v_mfma_f32_16x16x32_bf16 v[24:27], v[120:123], a[224:227], v[24:27]\n\t"
      "v_cvt_pk_bf16_f32 v132, v48, v49\n\t"
      "v_cvt_pk_bf16_f32 v133, v50, v51\n\t"
      "v_mfma_f32_16x16x32_bf16 v[28:31], v[124:127], a[224:227], v[28:31]\n\t"
      "v_cvt_pk_bf16_f32 v134, v52, v53\n\t"
      "v_cvt_pk_bf16_f32 v135, v54, v55\n\t"
      "v_mfma_f32_4x4x4_16b_bf16 v[40:43], a[180:181], v[136:137], 0\n\t"
      "ds_read_b64_tr_b16 v[152:153], v60 offset:64\n\t"
      "v_mfma_f32_4x4x4_16b_bf16 v[44:47], a[182:183], v[138:139], 0\n\t"
      "ds_read_b64_tr_b16 v[154:155], v60 offset:832\n\t"
      "v_mfma_f32_4x4x4_16b_bf16 v[48:51], a[180:181], v[140:141], 0\n\t"
      "ds_read_b64_tr_b16 v[156:157], v61 offset:64\n\t"
      "v_mfma_f32_4x4x4_16b_bf16 v[52:55], a[182:183], v[142:143], 0\n\t"
      "ds_read_b64_tr_b16 v[158:159], v61 offset:832\n\t"
      "buffer_load_dwordx4 v11, s[84:87], s54 offen offset:2048 lds\n\t"
      "s_add_u32 m0, s17, 3072\n\t"
      "s_mov_b32 s54, 208896\n\t"
      "buffer_load_dwordx4 v9, s[84:87], s54 offen lds\n\t"
      "v_mfma_f32_16x16x32_bf16 v[32:35], a[84:87], a[224:227], v[32:35]\n\t"
      "v_cvt_pk_bf16_f32 v136, v40, v41\n\t"
      "v_cvt_pk_bf16_f32 v137, v42, v43\n\t"
      "v_mfma_f32_16x16x32_bf16 v[36:39], a[88:91], a[224:227], v[36:39]\n\t"
      "v_cvt_pk_bf16_f32 v138, v44, v45\n\t"
      "v_cvt_pk_bf16_f32 v139, v46, v47\n\t"
      "s_waitcnt lgkmcnt(9)\n\t"
      "v_mfma_f32_16x16x32_bf16 a[156:159], v[112:115], a[228:231], a[156:159]\n\t"
      "v_cvt_pk_bf16_f32 v140, v48, v49\n\t"
      "v_cvt_pk_bf16_f32 v141, v50, v51\n\t"
      "v_mfma_f32_16x16x32_bf16 a[160:163], v[116:119], a[228:231], a[160:163]\n\t"
      "v_cvt_pk_bf16_f32 v142, v52, v53\n\t"
      "v_cvt_pk_bf16_f32 v143, v54, v55\n\t"
      "ds_read_b128 a[232:235], v56 offset:25344\n\t"
      "v_mfma_f32_4x4x4_16b_bf16 v[40:43], a[180:181], a[92:93], 0\n\t"
      "ds_read_b64_tr_b16 a[100:101], v60 offset:128\n\t"
      "v_mfma_f32_4x4x4_16b_bf16 v[44:47], a[182:183], a[94:95], 0\n\t"
      "ds_read_b64_tr_b16 a[102:103], v60 offset:896\n\t"
      "v_mfma_f32_4x4x4_16b_bf16 v[48:51], a[180:181], a[96:97], 0\n\t"
      "ds_read_b64_tr_b16 a[104:105], v61 offset:128\n\t"
      "v_mfma_f32_4x4x4_16b_bf16 v[52:55], a[182:183], a[98:99], 0\n\t"
      "ds_read_b64_tr_b16 a[106:107], v61 offset:896\n\t"
      "buffer_load_dwordx4 v10, s[84:87], s54 offen offset:1024 lds\n\t"
      "buffer_load_dwordx4 v11, s[84:87], s54 offen offset:2048 lds\n\t"
      "ds_read_b128 a[236:239], v56 offset:37632\n\t"
      "v_mfma_f32_16x16x32_bf16 a[164:167], v[120:123], a[228:231], a[164:167]\n\t"
      "v_cvt_pk_bf16_f32 v40, v40, v41\n\t"
      "v_cvt_pk_bf16_f32 v41, v42, v43\n\t"
      "v_mfma_f32_16x16x32_bf16 a[168:171], v[124:127], a[228:231], a[168:171]\n\t"
      "v_cvt_pk_bf16_f32 v44, v44, v45\n\t"
      "v_cvt_pk_bf16_f32 v45, v46, v47\n\t"
      "v_mfma_f32_16x16x32_bf16 a[172:175], a[84:87], a[228:231], a[172:175]\n\t"
      "v_cvt_pk_bf16_f32 v48, v48, v49\n\t"
      "v_cvt_pk_bf16_f32 v49, v50, v51\n\t"
      "v_mfma_f32_16x16x32_bf16 a[176:179], a[88:91], a[228:231], a[176:179]\n\t"
      "v_cvt_pk_bf16_f32 v52, v52, v53\n\t"
      "v_cvt_pk_bf16_f32 v53, v54, v55\n\t"
      "v_accvgpr_write_b32 a92, v40\n\t"
      "v_accvgpr_write_b32 a93, v41\n\t"
      "v_accvgpr_write_b32 a94, v44\n\t"
      "v_accvgpr_write_b32 a95, v45\n\t"
      "v_accvgpr_write_b32 a96, v48\n\t"
      "v_accvgpr_write_b32 a97, v49\n\t"
      "v_accvgpr_write_b32 a98, v52\n\t"
      "v_accvgpr_write_b32 a99, v53\n\t"
      "s_waitcnt vmcnt(12)\n\t"
      "s_waitcnt lgkmcnt(1)\n\t"
      "; step\n\t"
      "v_mfma_f32_4x4x4_16b_bf16 v[40:43], a[184:185], v[144:145], 0\n\t"
      "ds_read_b128 a[180:183], v14 offset:1536\n\t"
      "v_mfma_f32_4x4x4_16b_bf16 v[44:47], a[186:187], v[146:147], 0\n\t"
      "ds_read_b64_tr_b16 v[160:161], v60 offset:24576\n\t"
      "v_mfma_f32_4x4x4_16b_bf16 v[48:51], a[184:185], v[148:149], 0\n\t"
      "ds_read_b64_tr_b16 v[162:163], v60 offset:25344\n\t"
      "v_mfma_f32_4x4x4_16b_bf16 v[52:55], a[186:187], v[150:151], 0\n\t"
      "ds_read_b64_tr_b16 v[164:165], v61 offset:24576\n\t"
      "ds_read_b64_tr_b16 v[166:167], v61 offset:25344\n\t"
      "s_add_u32 m0, s17, 86528\n\t"
      "s_mov_b32 s54, 221184\n\t"
      "buffer_load_dwordx4 v9, s[84:87], s54 offen lds\n\t"
      "buffer_load_dwordx4 v10, s[84:87], s54 offen offset:1024 lds\n\t"
      "v_mfma_f32_16x16x32_bf16 v[16:19], v[128:131], a[232:235], v[16:19]\n\t"
      "v_cvt_pk_bf16_f32 v144, v40, v41\n\t"
      "v_cvt_pk_bf16_f32 v145, v42, v43\n\t"
      "v_mfma_f32_16x16x32_bf16 v[20:23], v[132:135], a[232:235], v[20:23]\n\t"
      "v_cvt_pk_bf16_f32 v146, v44, v45\n\t"
      "v_cvt_pk_bf16_f32 v147, v46, v47\n\t"
      "v_mfma_f32_16x16x32_bf16 v[24:27], v[136:139], a[232:235], v[24:27]\n\t"
      "v_cvt_pk_bf16_f32 v148, v48, v49\n\t"
      "v_cvt_pk_bf16_f32 v149, v50, v51\n\t"
      "v_mfma_f32_16x16x32_bf16 v[28:31], v[140:143], a[232:235], v[28:31]\n\t"
      "v_cvt_pk_bf16_f32 v150, v52, v53\n\t"
      "v_cvt_pk_bf16_f32 v151, v54, v55\n\t"
      "v_mfma_f32_4x4x4_16b_bf16 v[40:43], a[184:185], v[152:153], 0\n\t"
      "ds_read_b64_tr_b16 v[168:169], v60 offset:24640\n\t"
      "v_mfma_f32_4x4x4_16b_bf16 v[44:47], a[186:187], v[154:155], 0\n\t"
      "ds_read_b64_tr_b16 v[170:171], v60 offset:25408\n\t"
      "v_mfma_f32_4x4x4_16b_bf16 v[48:51], a[184:185], v[156:157], 0\n\t"
      "ds_read_b64_tr_b16 v[172:173], v61 offset:24640\n\t"
      "v_mfma_f32_4x4x4_16b_bf16 v[52:55], a[186:187], v[158:159], 0\n\t"
      "ds_read_b64_tr_b16 v[174:175], v61 offset:25408\n\t"
      "buffer_load_dwordx4 v11, s[84:87], s54 offen offset:2048 lds\n\t"
      "s_add_u32 m0, s17, 89600\n\t"
      "s_mov_b32 s54, 233472\n\t"
      "buffer_load_dwordx4 v9, s[84:87], s54 offen lds\n\t"
      "v_mfma_f32_16x16x32_bf16 v[32:35], a[92:95], a[232:235], v[32:35]\n\t"
      "v_cvt_pk_bf16_f32 v152, v40, v41\n\t"
      "v_cvt_pk_bf16_f32 v153, v42, v43\n\t"
      "v_mfma_f32_16x16x32_bf16 v[36:39], a[96:99], a[232:235], v[36:39]\n\t"
      "v_cvt_pk_bf16_f32 v154, v44, v45\n\t"
      "v_cvt_pk_bf16_f32 v155, v46, v47\n\t"
      "s_waitcnt lgkmcnt(9)\n\t"
      "v_mfma_f32_16x16x32_bf16 a[156:159], v[128:131], a[236:239], a[156:159]\n\t"
      "v_cvt_pk_bf16_f32 v156, v48, v49\n\t"
      "v_cvt_pk_bf16_f32 v157, v50, v51\n\t"
      "v_mfma_f32_16x16x32_bf16 a[160:163], v[132:135], a[236:239], a[160:163]\n\t"
      "v_cvt_pk_bf16_f32 v158, v52, v53\n\t"
      "v_cvt_pk_bf16_f32 v159, v54, v55\n\t"
      "ds_read_b128 a[240:243], v57 offset:25344\n\t"
      "v_mfma_f32_4x4x4_16b_bf16 v[40:43], a[184:185], a[100:101], 0\n\t"
      "ds_read_b64_tr_b16 a[108:109], v60 offset:24704\n\t"
      "v_mfma_f32_4x4x4_16b_bf16 v[44:47], a[186:187], a[102:103], 0\n\t"
      "ds_read_b64_tr_b16 a[110:111], v60 offset:25472\n\t"
      "v_mfma_f32_4x4x4_16b_bf16 v[48:51], a[184:185], a[104:105], 0\n\t"
      "ds_read_b64_tr_b16 a[112:113], v61 offset:24704\n\t"
      "v_mfma_f32_4x4x4_16b_bf16 v[52:55], a[186:187], a[106:107], 0\n\t"
      "ds_read_b64_tr_b16 a[114:115], v61 offset:25472\n\t"
      "buffer_load_dwordx4 v10, s[84:87], s54 offen offset:1024 lds\n\t"
      "buffer_load_dwordx4 v11, s[84:87], s54 offen offset:2048 lds\n\t"
      "ds_read_b128 a[244:247], v57 offset:37632\n\t"
      "v_mfma_f32_16x16x32_bf16 a[164:167], v[136:139], a[236:239], a[164:167]\n\t"
      "v_cvt_pk_bf16_f32 v40, v40, v41\n\t"
      "v_cvt_pk_bf16_f32 v41, v42, v43\n\t"
      "v_mfma_f32_16x16x32_bf16 a[168:171], v[140:143], a[236:239], a[168:171]\n\t"
      "v_cvt_pk_bf16_f32 v44, v44, v45\n\t"
      "v_cvt_pk_bf16_f32 v45, v46, v47\n\t"
      "v_mfma_f32_16x16x32_bf16 a[172:175], a[92:95], a[236:239], a[172:175]\n\t"
      "v_cvt_pk_bf16_f32 v48, v48, v49\n\t"
      "v_cvt_pk_bf16_f32 v49, v50, v51\n\t"
      "v_mfma_f32_16x16x32_bf16 a[176:179], a[96:99], a[236:239], a[176:179]\n\t"
      "v_cvt_pk_bf16_f32 v52, v52, v53\n\t"
      "v_cvt_pk_bf16_f32 v53, v54, v55\n\t"
      "v_accvgpr_write_b32 a100, v40\n\t"
      "v_accvgpr_write_b32 a101, v41\n\t"
      "v_accvgpr_write_b32 a102, v44\n\t"
      "v_accvgpr_write_b32 a103, v45\n\t"
      "v_accvgpr_write_b32 a104, v48\n\t"
      "v_accvgpr_write_b32 a105, v49\n\t"
      "v_accvgpr_write_b32 a106, v52\n\t"
      "v_accvgpr_write_b32 a107, v53\n\t"
      "s_waitcnt vmcnt(12)\n\t"
      "s_waitcnt lgkmcnt(1)\n\t"
      "; step\n\t"
      "v_mfma_f32_4x4x4_16b_bf16 v[40:43], a[180:181], v[160:161], 0\n\t"
      "ds_read_b128 a[184:187], v14 offset:1792\n\t"
      "v_mfma_f32_4x4x4_16b_bf16 v[44:47], a[182:183], v[162:163], 0\n\t"
      "ds_read_b64_tr_b16 v[176:177], v60 offset:49152\n\t"
      "v_mfma_f32_4x4x4_16b_bf16 v[48:51], a[180:181], v[164:165], 0\n\t"
      "ds_read_b64_tr_b16 v[178:179], v60 offset:49920\n\t"
      "v_mfma_f32_4x4x4_16b_bf16 v[52:55], a[182:183], v[166:167], 0\n\t"
      "ds_read_b64_tr_b16 v[180:181], v61 offset:49152\n\t"
      "ds_read_b64_tr_b16 v[182:183], v61 offset:49920\n\t"
      "s_add_u32 m0, s17, 111104\n\t"
      "s_mov_b32 s54, 245760\n\t"
      "buffer_load_dwordx4 v9, s[84:87], s54 offen lds\n\t"
      "buffer_load_dwordx4 v10, s[84:87], s54 offen offset:1024 lds\n\t"
      "v_mfma_f32_16x16x32_bf16 v[16:19], v[144:147], a[240:243], v[16:19]\n\t"
      "v_cvt_pk_bf16_f32 v160, v40, v41\n\t"
      "v_cvt_pk_bf16_f32 v161, v42, v43\n\t"
      "v_mfma_f32_16x16x32_bf16 v[20:23], v[148:151], a[240:243], v[20:23]\n\t"
      "v_cvt_pk_bf16_f32 v162, v44, v45\n\t"
      "v_cvt_pk_bf16_f32 v163, v46, v47\n\t"
      "v_mfma_f32_16x16x32_bf16 v[24:27], v[152:155], a[240:243], v[24:27]\n\t"
      "v_cvt_pk_bf16_f32 v164, v48, v49\n\t"
      "v_cvt_pk_bf16_f32 v165, v50, v51\n\t"
      "v_mfma_f32_16x16x32_bf16 v[28:31], v[156:159], a[240:243], v[28:31]\n\t"
      "v_cvt_pk_bf16_f32 v166, v52, v53\n\t"
      "v_cvt_pk_bf16_f32 v167, v54, v55\n\t"
      "v_mfma_f32_4x4x4_16b_bf16 v[40:43], a[180:181], v[168:169], 0\n\t"
      "ds_read_b64_tr_b16 v[184:185], v60 offset:49216\n\t"
      "v_mfma_f32_4x4x4_16b_bf16 v[44:47], a[182:183], v[170:171], 0\n\t"
      "ds_read_b64_tr_b16 v[186:187], v60 offset:49984\n\t"
      "v_mfma_f32_4x4x4_16b_bf16 v[48:51], a[180:181], v[172:173], 0\n\t"
      "ds_read_b64_tr_b16 v[188:189], v61 offset:49216\n\t"
      "v_mfma_f32_4x4x4_16b_bf16 v[52:55], a[182:183], v[174:175], 0\n\t"
      "ds_read_b64_tr_b16 v[190:191], v61 offset:49984\n\t"
      "buffer_load_dwordx4 v11, s[84:87], s54 offen offset:2048 lds\n\t"
      "s_add_u32 m0, s17, 114176\n\t"
      "s_mov_b32 s54, 258048\n\t"
      "buffer_load_dwordx4 v9, s[84:87], s54 offen lds\n\t"
      "v_mfma_f32_16x16x32_bf16 v[32:35], a[100:103], a[240:243], v[32:35]\n\t"
      "v_cvt_pk_bf16_f32 v168, v40, v41\n\t"
      "v_cvt_pk_bf16_f32 v169, v42, v43\n\t"
      "v_mfma_f32_16x16x32_bf16 v[36:39], a[104:107], a[240:243], v[36:39]\n\t"
      "v_cvt_pk_bf16_f32 v170, v44, v45\n\t"
      "v_cvt_pk_bf16_f32 v171, v46, v47\n\t"
      "s_waitcnt lgkmcnt(9)\n\t"
      "v_mfma_f32_16x16x32_bf16 a[156:159], v[144:147], a[244:247], a[156:159]\n\t"
      "v_cvt_pk_bf16_f32 v172, v48, v49\n\t"
      "v_cvt_pk_bf16_f32 v173, v50, v51\n\t"
      "v_mfma_f32_16x16x32_bf16 a[160:163], v[148:151], a[244:247], a[160:163]\n\t"
      "v_cvt_pk_bf16_f32 v174, v52, v53\n\t"
      "v_cvt_pk_bf16_f32 v175, v54, v55\n\t"
      "ds_read_b128 a[224:227], v58 offset:25344\n\t"
      "v_mfma_f32_4x4x4_16b_bf16 v[40:43], a[180:181], a[108:109], 0\n\t"
      "ds_read_b64_tr_b16 a[116:117], v60 offset:49280\n\t"
      "v_mfma_f32_4x4x4_16b_bf16 v[44:47], a[182:183], a[110:111], 0\n\t"
      "ds_read_b64_tr_b16 a[118:119], v60 offset:50048\n\t"
      "v_mfma_f32_4x4x4_16b_bf16 v[48:51], a[180:181], a[112:113], 0\n\t"
      "ds_read_b64_tr_b16 a[120:121], v61 offset:49280\n\t"
      "v_mfma_f32_4x4x4_16b_bf16 v[52:55], a[182:183], a[114:115], 0\n\t"
      "ds_read_b64_tr_b16 a[122:123], v61 offset:50048\n\t"
      "buffer_load_dwordx4 v10, s[84:87], s54 offen offset:1024 lds\n\t"
      "buffer_load_dwordx4 v11, s[84:87], s54 offen offset:2048 lds\n\t"
      "ds_read_b128 a[228:231], v58 offset:37632\n\t"
      "v_mfma_f32_16x16x32_bf16 a[164:167], v[152:155], a[244:247], a[164:167]\n\t"
      "v_cvt_pk_bf16_f32 v40, v40, v41\n\t"
      "v_cvt_pk_bf16_f32 v41, v42, v43\n\t"
      "v_mfma_f32_16x16x32_bf16 a[168:171], v[156:159], a[244:247], a[168:171]\n\t"
      "v_cvt_pk_bf16_f32 v44, v44, v45\n\t"
      "v_cvt_pk_bf16_f32 v45, v46, v47\n\t"
      "v_mfma_f32_16x16x32_bf16 a[172:175], a[100:103], a[244:247], a[172:175]\n\t"
      "v_cvt_pk_bf16_f32 v48, v48, v49\n\t"
      "v_cvt_pk_bf16_f32 v49, v50, v51\n\t"
      "v_mfma_f32_16x16x32_bf16 a[176:179], a[104:107], a[244:247], a[176:179]\n\t"
      "v_cvt_pk_bf16_f32 v52, v52, v53\n\t"
      "v_cvt_pk_bf16_f32 v53, v54, v55\n\t"
      "v_accvgpr_write_b32 a108, v40\n\t"
      "v_accvgpr_write_b32 a109, v41\n\t"
      "v_accvgpr_write_b32 a110, v44\n\t"
      "v_accvgpr_write_b32 a111, v45\n\t"
      "v_accvgpr_write_b32 a112, v48\n\t"
      "v_accvgpr_write_b32 a113, v49\n\t"
      "v_accvgpr_write_b32 a114, v52\n\t"
      "v_accvgpr_write_b32 a115, v53\n\t"
      "s_waitcnt vmcnt(12)\n\t"
      "s_waitcnt lgkmcnt(1)\n\t"
      "; step\n\t"
      "v_mfma_f32_4x4x4_16b_bf16 v[40:43], a[184:185], v[176:177], 0\n\t"
      "ds_read_b128 a[180:183], v14 offset:2048\n\t"
      "v_mfma_f32_4x4x4_16b_bf16 v[44:47], a[186:187], v[178:179], 0\n\t"
      "ds_read_b64_tr_b16 v[192:193], v12 offset:0\n\t"
      "v_mfma_f32_4x4x4_16b_bf16 v[48:51], a[184:185], v[180:181], 0\n\t"
      "ds_read_b64_tr_b16 v[194:195], v12 offset:768\n\t"
      "v_mfma_f32_4x4x4_16b_bf16 v[52:55], a[186:187], v[182:183], 0\n\t"
      "ds_read_b64_tr_b16 v[196:197], v13 offset:0\n\t"
      "ds_read_b64_tr_b16 v[198:199], v13 offset:768\n\t"
      "s_add_u32 m0, s17, 135680\n\t"
      "s_mov_b32 s54, 270336\n\t"
      "buffer_load_dwordx4 v9, s[84:87], s54 offen lds\n\t"
      "buffer_load_dwordx4 v10, s[84:87], s54 offen offset:1024 lds\n\t"
      "v_mfma_f32_16x16x32_bf16 v[16:19], v[160:163], a[224:227], v[16:19]\n\t"
      "v_cvt_pk_bf16_f32 v176, v40, v41\n\t"
      "v_cvt_pk_bf16_f32 v177, v42, v43\n\t"
      "v_mfma_f32_16x16x32_bf16 v[20:23], v[164:167], a[224:227], v[20:23]\n\t"
      "v_cvt_pk_bf16_f32 v178, v44, v45\n\t"
      "v_cvt_pk_bf16_f32 v179, v46, v47\n\t"
      "v_mfma_f32_16x16x32_bf16 v[24:27], v[168:171], a[224:227], v[24:27]\n\t"
      "v_cvt_pk_bf16_f32 v180, v48, v49\n\t"
      "v_cvt_pk_bf16_f32 v181, v50, v51\n\t"
      "v_mfma_f32_16x16x32_bf16 v[28:31], v[172:175], a[224:227], v[28:31]\n\t"
      "v_cvt_pk_bf16_f32 v182, v52, v53\n\t"
      "v_cvt_pk_bf16_f32 v183, v54, v55\n\t"
      "v_mfma_f32_4x4x4_16b_bf16 v[40:43], a[184:185], v[184:185], 0\n\t"
      "ds_read_b64_tr_b16 v[200:201], v12 offset:64\n\t"
      "v_mfma_f32_4x4x4_16b_bf16 v[44:47], a[186:187], v[186:187], 0\n\t"
      "ds_read_b64_tr_b16 v[202:203], v12 offset:832\n\t"
      "v_mfma_f32_4x4x4_16b_bf16 v[48:51], a[184:185], v[188:189], 0\n\t"
      "ds_read_b64_tr_b16 v[204:205], v13 offset:64\n\t"
      "v_mfma_f32_4x4x4_16b_bf16 v[52:55], a[186:187], v[190:191], 0\n\t"
      "ds_read_b64_tr_b16 v[206:207], v13 offset:832\n\t"
      "buffer_load_dwordx4 v11, s[84:87], s54 offen offset:2048 lds\n\t"
      "s_add_u32 m0, s17, 138752\n\t"
      "s_mov_b32 s54, 282624\n\t"
      "buffer_load_dwordx4 v9, s[84:87], s54 offen lds\n\t"
      "v_mfma_f32_16x16x32_bf16 v[32:35], a[108:111], a[224:227], v[32:35]\n\t"
      "v_cvt_pk_bf16_f32 v184, v40, v41\n\t"
      "v_cvt_pk_bf16_f32 v185, v42, v43\n\t"
      "v_mfma_f32_16x16x32_bf16 v[36:39], a[112:115], a[224:227], v[36:39]\n\t"
      "v_cvt_pk_bf16_f32 v186, v44, v45\n\t"
      "v_cvt_pk_bf16_f32 v187, v46, v47\n\t"
      "s_waitcnt lgkmcnt(9)\n\t"
      "v_mfma_f32_16x16x32_bf16 a[156:159], v[160:163], a[228:231], a[156:159]\n\t"
      "v_cvt_pk_bf16_f32 v188, v48, v49\n\t"
      "v_cvt_pk_bf16_f32 v189, v50, v51\n\t"
      "v_mfma_f32_16x16x32_bf16 a[160:163], v[164:167], a[228:231], a[160:163]\n\t"
      "v_cvt_pk_bf16_f32 v190, v52, v53\n\t"
      "v_cvt_pk_bf16_f32 v191, v54, v55\n\t"
      "ds_read_b128 a[232:235], v59 offset:25344\n\t"
      "v_mfma_f32_4x4x4_16b_bf16 v[40:43], a[184:185], a[116:117], 0\n\t"
      "ds_read_b64_tr_b16 a[124:125], v12 offset:128\n\t"
      "v_mfma_f32_4x4x4_16b_bf16 v[44:47], a[186:187], a[118:119], 0\n\t"
      "ds_read_b64_tr_b16 a[126:127], v12 offset:896\n\t"
      "v_mfma_f32_4x4x4_16b_bf16 v[48:51], a[184:185], a[120:121], 0\n\t"
      "ds_read_b64_tr_b16 a[128:129], v13 offset:128\n\t"
      "v_mfma_f32_4x4x4_16b_bf16 v[52:55], a[186:187], a[122:123], 0\n\t"
      "ds_read_b64_tr_b16 a[130:131], v13 offset:896\n\t"
      "buffer_load_dwordx4 v10, s[84:87], s54 offen offset:1024 lds\n\t"
      "buffer_load_dwordx4 v11, s[84:87], s54 offen offset:2048 lds\n\t"
      "ds_read_b128 a[236:239], v59 offset:37632\n\t"
      "s_or_b32 s30, s17, s16\n\t"
      "s_cbranch_scc1 .Lno3_%=\n\t"
      "global_load_dword a252, v62, s[12:13] sc1\n\t"
      ".Lno3_%=:\n\t"
      "s_add_u32 m0, s18, 49664\n\t"
      "s_add_u32 s48, s15, 786432\n\t"
      "buffer_load_dwordx4 v2, s[40:43], s48 offen nt lds\n\t"
      "buffer_load_dwordx4 v3, s[40:43], s48 offen offset:1024 nt lds\n\t"
      "buffer_load_dwordx4 v4, s[40:43], s48 offen offset:2048 nt lds\n\t"
      "v_mfma_f32_16x16x32_bf16 a[164:167], v[168:171], a[228:231], a[164:167]\n\t"
      "v_cvt_pk_bf16_f32 v40, v40, v41\n\t"
      "v_cvt_pk_bf16_f32 v41, v42, v43\n\t"
      "v_mfma_f32_16x16x32_bf16 a[168:171], v[172:175], a[228:231], a[168:171]\n\t"
      "v_cvt_pk_bf16_f32 v44, v44, v45\n\t"
      "v_cvt_pk_bf16_f32 v45, v46, v47\n\t"
      "v_mfma_f32_16x16x32_bf16 a[172:175], a[108:111], a[228:231], a[172:175]\n\t"
      "v_cvt_pk_bf16_f32 v48, v48, v49\n\t"
      "v_cvt_pk_bf16_f32 v49, v50, v51\n\t"
      "v_mfma_f32_16x16x32_bf16 a[176:179], a[112:115], a[228:231], a[176:179]\n\t"
      "v_cvt_pk_bf16_f32 v52, v52, v53\n\t"
      "v_cvt_pk_bf16_f32 v53, v54, v55\n\t"
      "v_accvgpr_write_b32 a116, v40\n\t"
      "v_accvgpr_write_b32 a117, v41\n\t"
      "v_accvgpr_write_b32 a118, v44\n\t"
      "v_accvgpr_write_b32 a119, v45\n\t"
      "v_accvgpr_write_b32 a120, v48\n\t"
      "v_accvgpr_write_b32 a121, v49\n\t"
      "v_accvgpr_write_b32 a122, v52\n\t"
      "v_accvgpr_write_b32 a123, v53\n\t"
      "s_waitcnt vmcnt(15)\n\t"
      "s_waitcnt lgkmcnt(1)\n\t"
      "; step\n\t"
      "v_mfma_f32_4x4x4_16b_bf16 v[40:43], a[180:181], v[192:193], 0\n\t"
      "ds_read_b128 a[184:187], v14 offset:2304\n\t"
      "v_mfma_f32_4x4x4_16b_bf16 v[44:47], a[182:183], v[194:195], 0\n\t"
      "ds_read_b64_tr_b16 v[208:209], v60 offset:0\n\t"
      "v_mfma_f32_4x4x4_16b_bf16 v[48:51], a[180:181], v[196:197], 0\n\t"
      "ds_read_b64_tr_b16 v[210:211], v60 offset:768\n\t"
      "v_mfma_f32_4x4x4_16b_bf16 v[52:55], a[182:183], v[198:199], 0\n\t"
      "ds_read_b64_tr_b16 v[212:213], v61 offset:0\n\t"
      "ds_read_b64_tr_b16 v[214:215], v61 offset:768\n\t"
      "s_add_u32 m0, s18, 61952\n\t"
      "s_add_u32 s48, s15, 786432\n\t"
      "buffer_load_dwordx4 v5, s[40:43], s48 offen nt lds\n\t"
      "buffer_load_dwordx4 v6, s[40:43], s48 offen offset:1024 nt lds\n\t"
      "v_mfma_f32_16x16x32_bf16 v[16:19], v[176:179], a[232:235], v[16:19]\n\t"
      "v_cvt_pk_bf16_f32 v192, v40, v41\n\t"
      "v_cvt_pk_bf16_f32 v193, v42, v43\n\t"
      "v_mfma_f32_16x16x32_bf16 v[20:23], v[180:183], a[232:235], v[20:23]\n\t"
      "v_cvt_pk_bf16_f32 v194, v44, v45\n\t"
      "v_cvt_pk_bf16_f32 v195, v46, v47\n\t"
      "v_mfma_f32_16x16x32_bf16 v[24:27], v[184:187], a[232:235], v[24:27]\n\t"
      "v_cvt_pk_bf16_f32 v196, v48, v49\n\t"
      "v_cvt_pk_bf16_f32 v197, v50, v51\n\t"
      "v_mfma_f32_16x16x32_bf16 v[28:31], v[188:191], a[232:235], v[28:31]\n\t"
      "v_cvt_pk_bf16_f32 v198, v52, v53\n\t"
      "v_cvt_pk_bf16_f32 v199, v54, v55\n\t"
      "v_mfma_f32_4x4x4_16b_bf16 v[40:43], a[180:181], v[200:201], 0\n\t"
      "ds_read_b64_tr_b16 v[216:217], v60 offset:64\n\t"
      "v_mfma_f32_4x4x4_16b_bf16 v[44:47], a[182:183], v[202:203], 0\n\t"
      "ds_read_b64_tr_b16 v[218:219], v60 offset:832\n\t"
      "v_mfma_f32_4x4x4_16b_bf16 v[48:51], a[180:181], v[204:205], 0\n\t"
      "ds_read_b64_tr_b16 v[220:221], v61 offset:64\n\t"
      "v_mfma_f32_4x4x4_16b_bf16 v[52:55], a[182:183], v[206:207], 0\n\t"
      "ds_read_b64_tr_b16 v[222:223], v61 offset:832\n\t"
      "buffer_load_dwordx4 v7, s[40:43], s48 offen offset:2048 nt lds\n\t"
      "v_mfma_f32_16x16x32_bf16 v[32:35], a[116:119], a[232:235], v[32:35]\n\t"
      "v_cvt_pk_bf16_f32 v200, v40, v41\n\t"
      "v_cvt_pk_bf16_f32 v201, v42, v43\n\t"
      "v_mfma_f32_16x16x32_bf16 v[36:39], a[120:123], a[232:235], v[36:39]\n\t"
      "v_cvt_pk_bf16_f32 v202, v44, v45\n\t"
      "v_cvt_pk_bf16_f32 v203, v46, v47\n\t"
      "s_waitcnt lgkmcnt(9)\n\t"
      "v_mfma_f32_16x16x32_bf16 a[156:159], v[176:179], a[236:239], a[156:159]\n\t"
      "v_cvt_pk_bf16_f32 v204, v48, v49\n\t"
      "v_cvt_pk_bf16_f32 v205, v50, v51\n\t"
      "v_mfma_f32_16x16x32_bf16 a[160:163], v[180:183], a[236:239], a[160:163]\n\t"
      "v_cvt_pk_bf16_f32 v206, v52, v53\n\t"
      "v_cvt_pk_bf16_f32 v207, v54, v55\n\t"
      "ds_read_b128 a[240:243], v56 offset:25600\n\t"
      "v_mfma_f32_4x4x4_16b_bf16 v[40:43], a[180:181], a[124:125], 0\n\t"
      "ds_read_b64_tr_b16 a[132:133], v60 offset:128\n\t"
      "v_mfma_f32_4x4x4_16b_bf16 v[44:47], a[182:183], a[126:127], 0\n\t"
      "ds_read_b64_tr_b16 a[134:135], v60 offset:896\n\t"
      "v_mfma_f32_4x4x4_16b_bf16 v[48:51], a[180:181], a[128:129], 0\n\t"
      "ds_read_b64_tr_b16 a[136:137], v61 offset:128\n\t"
      "v_mfma_f32_4x4x4_16b_bf16 v[52:55], a[182:183], a[130:131], 0\n\t"
      "ds_read_b64_tr_b16 a[138:139], v61 offset:896\n\t"
      "ds_read_b128 a[244:247], v56 offset:37888\n\t"
      "v_mfma_f32_16x16x32_bf16 a[164:167], v[184:187], a[236:239], a[164:167]\n\t"
      "v_cvt_pk_bf16_f32 v40, v40, v41\n\t"
      "v_cvt_pk_bf16_f32 v41, v42, v43\n\t"
      "v_mfma_f32_16x16x32_bf16 a[168:171], v[188:191], a[236:239], a[168:171]\n\t"
      "v_cvt_pk_bf16_f32 v44, v44, v45\n\t"
      "v_cvt_pk_bf16_f32 v45, v46, v47\n\t"
      "v_mfma_f32_16x16x32_bf16 a[172:175], a[116:119], a[236:239], a[172:175]\n\t"
      "v_cvt_pk_bf16_f32 v48, v48, v49\n\t"
      "v_cvt_pk_bf16_f32 v49, v50, v51\n\t"
      "v_mfma_f32_16x16x32_bf16 a[176:179], a[120:123], a[236:239], a[176:179]\n\t"
      "v_cvt_pk_bf16_f32 v52, v52, v53\n\t"
      "v_cvt_pk_bf16_f32 v53, v54, v55\n\t"
      "v_accvgpr_write_b32 a124, v40\n\t"
      "v_accvgpr_write_b32 a125, v41\n\t"
      "v_accvgpr_write_b32 a126, v44\n\t"
      "v_accvgpr_write_b32 a127, v45\n\t"
      "v_accvgpr_write_b32 a128, v48\n\t"
      "v_accvgpr_write_b32 a129, v49\n\t"
      "v_accvgpr_write_b32 a130, v52\n\t"
      "v_accvgpr_write_b32 a131, v53\n\t"
      "s_waitcnt vmcnt(12)\n\t"
      "s_waitcnt lgkmcnt(1)\n\t"
      "; step\n\t"
      "v_mfma_f32_4x4x4_16b_bf16 v[40:43], a[184:185], v[208:209], 0\n\t"
      "ds_read_b128 a[180:183], v14 offset:2560\n\t"
      "v_mfma_f32_4x4x4_16b_bf16 v[44:47], a[186:187], v[210:211], 0\n\t"
      "ds_read_b64_tr_b16 v[224:225], v60 offset:24576\n\t"
      "v_mfma_f32_4x4x4_16b_bf16 v[48:51], a[184:185], v[212:213], 0\n\t"
      "ds_read_b64_tr_b16 v[226:227], v60 offset:25344\n\t"
      "v_mfma_f32_4x4x4_16b_bf16 v[52:55], a[186:187], v[214:215], 0\n\t"
      "ds_read_b64_tr_b16 v[228:229], v61 offset:24576\n\t"
      "ds_read_b64_tr_b16 v[230:231], v61 offset:25344\n\t"
      "v_mfma_f32_16x16x32_bf16 v[16:19], v[192:195], a[240:243], v[16:19]\n\t"
      "v_cvt_pk_bf16_f32 v208, v40, v41\n\t"
      "v_cvt_pk_bf16_f32 v209, v42, v43\n\t"
      "v_mfma_f32_16x16x32_bf16 v[20:23], v[196:199], a[240:243], v[20:23]\n\t"
      "v_cvt_pk_bf16_f32 v210, v44, v45\n\t"
      "v_cvt_pk_bf16_f32 v211, v46, v47\n\t"
      "v_mfma_f32_16x16x32_bf16 v[24:27], v[200:203], a[240:243], v[24:27]\n\t"
      "v_cvt_pk_bf16_f32 v212, v48, v49\n\t"
      "v_cvt_pk_bf16_f32 v213, v50, v51\n\t"
      "v_mfma_f32_16x16x32_bf16 v[28:31], v[204:207], a[240:243], v[28:31]\n\t"
      "v_cvt_pk_bf16_f32 v214, v52, v53\n\t"
      "v_cvt_pk_bf16_f32 v215, v54, v55\n\t"
      "v_mfma_f32_4x4x4_16b_bf16 v[40:43], a[184:185], v[216:217], 0\n\t"
      "ds_read_b64_tr_b16 v[232:233], v60 offset:24640\n\t"
      "v_mfma_f32_4x4x4_16b_bf16 v[44:47], a[186:187], v[218:219], 0\n\t"
      "ds_read_b64_tr_b16 v[234:235], v60 offset:25408\n\t"
      "v_mfma_f32_4x4x4_16b_bf16 v[48:51], a[184:185], v[220:221], 0\n\t"
      "ds_read_b64_tr_b16 v[236:237], v61 offset:24640\n\t"
      "v_mfma_f32_4x4x4_16b_bf16 v[52:55], a[186:187], v[222:223], 0\n\t"
      "ds_read_b64_tr_b16 v[238:239], v61 offset:25408\n\t"
      "v_mfma_f32_16x16x32_bf16 v[32:35], a[124:127], a[240:243], v[32:35]\n\t"
      "v_cvt_pk_bf16_f32 v216, v40, v41\n\t"
      "v_cvt_pk_bf16_f32 v217, v42, v43\n\t"
      "v_mfma_f32_16x16x32_bf16 v[36:39], a[128:131], a[240:243], v[36:39]\n\t"
      "v_cvt_pk_bf16_f32 v218, v44, v45\n\t"
      "v_cvt_pk_bf16_f32 v219, v46, v47\n\t"
      "s_waitcnt lgkmcnt(9)\n\t"
      "v_mfma_f32_16x16x32_bf16 a[156:159], v[192:195], a[244:247], a[156:159]\n\t"
      "v_cvt_pk_bf16_f32 v220, v48, v49\n\t"
      "v_cvt_pk_bf16_f32 v221, v50, v51\n\t"
      "v_mfma_f32_16x16x32_bf16 a[160:163], v[196:199], a[244:247], a[160:163]\n\t"
      "v_cvt_pk_bf16_f32 v222, v52, v53\n\t"
      "v_cvt_pk_bf16_f32 v223, v54, v55\n\t"
      "ds_read_b128 a[224:227], v57 offset:25600\n\t"
      "v_mfma_f32_4x4x4_16b_bf16 v[40:43], a[184:185], a[132:133], 0\n\t"
      "ds_read_b64_tr_b16 a[140:141], v60 offset:24704\n\t"
      "v_mfma_f32_4x4x4_16b_bf16 v[44:47], a[186:187], a[134:135], 0\n\t"
      "ds_read_b64_tr_b16 a[142:143], v60 offset:25472\n\t"
      "v_mfma_f32_4x4x4_16b_bf16 v[48:51], a[184:185], a[136:137], 0\n\t"
      "ds_read_b64_tr_b16 a[144:145], v61 offset:24704\n\t"
      "v_mfma_f32_4x4x4_16b_bf16 v[52:55], a[186:187], a[138:139], 0\n\t"
      "ds_read_b64_tr_b16 a[146:147], v61 offset:25472\n\t"
      "ds_read_b128 a[228:231], v57 offset:37888\n\t"
      "v_mfma_f32_16x16x32_bf16 a[164:167], v[200:203], a[244:247], a[164:167]\n\t"
      "v_cvt_pk_bf16_f32 v40, v40, v41\n\t"
      "v_cvt_pk_bf16_f32 v41, v42, v43\n\t"
      "v_mfma_f32_16x16x32_bf16 a[168:171], v[204:207], a[244:247], a[168:171]\n\t"
      "v_cvt_pk_bf16_f32 v44, v44, v45\n\t"
      "v_cvt_pk_bf16_f32 v45, v46, v47\n\t"
      "v_mfma_f32_16x16x32_bf16 a[172:175], a[124:127], a[244:247], a[172:175]\n\t"
      "v_cvt_pk_bf16_f32 v48, v48, v49\n\t"
      "v_cvt_pk_bf16_f32 v49, v50, v51\n\t"
      "v_mfma_f32_16x16x32_bf16 a[176:179], a[128:131], a[244:247], a[176:179]\n\t"
      "v_cvt_pk_bf16_f32 v52, v52, v53\n\t"
      "v_cvt_pk_bf16_f32 v53, v54, v55\n\t"
      "v_accvgpr_write_b32 a132, v40\n\t"
      "v_accvgpr_write_b32 a133, v41\n\t"
      "v_accvgpr_write_b32 a134, v44\n\t"
      "v_accvgpr_write_b32 a135, v45\n\t"
      "v_accvgpr_write_b32 a136, v48\n\t"
      "v_accvgpr_write_b32 a137, v49\n\t"
      "v_accvgpr_write_b32 a138, v52\n\t"
      "v_accvgpr_write_b32 a139, v53\n\t"
      "s_waitcnt vmcnt(6)\n\t"
      "s_waitcnt lgkmcnt(1)\n\t"
      "; step\n\t"
      "v_mfma_f32_4x4x4_16b_bf16 v[40:43], a[180:181], v[224:225], 0\n\t"
      "ds_read_b128 a[184:187], v14 offset:2816\n\t"
      "v_mfma_f32_4x4x4_16b_bf16 v[44:47], a[182:183], v[226:227], 0\n\t"
      "ds_read_b64_tr_b16 v[240:241], v60 offset:49152\n\t"
      "v_mfma_f32_4x4x4_16b_bf16 v[48:51], a[180:181], v[228:229], 0\n\t"
      "ds_read_b64_tr_b16 v[242:243], v60 offset:49920\n\t"
      "v_mfma_f32_4x4x4_16b_bf16 v[52:55], a[182:183], v[230:231], 0\n\t"
      "ds_read_b64_tr_b16 v[244:245], v61 offset:49152\n\t"
      "ds_read_b64_tr_b16 v[246:247], v61 offset:49920\n\t"
      "v_mfma_f32_16x16x32_bf16 v[16:19], v[208:211], a[224:227], v[16:19]\n\t"
      "v_cvt_pk_bf16_f32 v224, v40, v41\n\t"
      "v_cvt_pk_bf16_f32 v225, v42, v43\n\t"
      "v_mfma_f32_16x16x32_bf16 v[20:23], v[212:215], a[224:227], v[20:23]\n\t"
      "v_cvt_pk_bf16_f32 v226, v44, v45\n\t"
      "v_cvt_pk_bf16_f32 v227, v46, v47\n\t"
      "v_mfma_f32_16x16x32_bf16 v[24:27], v[216:219], a[224:227], v[24:27]\n\t"
      "v_cvt_pk_bf16_f32 v228, v48, v49\n\t"
      "v_cvt_pk_bf16_f32 v229, v50, v51\n\t"
      "v_mfma_f32_16x16x32_bf16 v[28:31], v[220:223], a[224:227], v[28:31]\n\t"
      "v_cvt_pk_bf16_f32 v230, v52, v53\n\t"
      "v_cvt_pk_bf16_f32 v231, v54, v55\n\t"
      "v_mfma_f32_4x4x4_16b_bf16 v[40:43], a[180:181], v[232:233], 0\n\t"
      "ds_read_b64_tr_b16 v[248:249], v60 offset:49216\n\t"
      "v_mfma_f32_4x4x4_16b_bf16 v[44:47], a[182:183], v[234:235], 0\n\t"
      "ds_read_b64_tr_b16 v[250:251], v60 offset:49984\n\t"
      "v_mfma_f32_4x4x4_16b_bf16 v[48:51], a[180:181], v[236:237], 0\n\t"
      "ds_read_b64_tr_b16 v[252:253], v61 offset:49216\n\t"
      "v_mfma_f32_4x4x4_16b_bf16 v[52:55], a[182:183], v[238:239], 0\n\t"
      "ds_read_b64_tr_b16 v[254:255], v61 offset:49984\n\t"
      "v_mfma_f32_16x16x32_bf16 v[32:35], a[132:135], a[224:227], v[32:35]\n\t"
      "v_cvt_pk_bf16_f32 v232, v40, v41\n\t"
      "v_cvt_pk_bf16_f32 v233, v42, v43\n\t"
      "v_mfma_f32_16x16x32_bf16 v[36:39], a[136:139], a[224:227], v[36:39]\n\t"
      "v_cvt_pk_bf16_f32 v234, v44, v45\n\t"
      "v_cvt_pk_bf16_f32 v235, v46, v47\n\t"
      "s_waitcnt lgkmcnt(9)\n\t"
      "v_mfma_f32_16x16x32_bf16 a[156:159], v[208:211], a[228:231], a[156:159]\n\t"
      "v_cvt_pk_bf16_f32 v236, v48, v49\n\t"
      "v_cvt_pk_bf16_f32 v237, v50, v51\n\t"
      "v_mfma_f32_16x16x32_bf16 a[160:163], v[212:215], a[228:231], a[160:163]\n\t"
      "v_cvt_pk_bf16_f32 v238, v52, v53\n\t"
      "v_cvt_pk_bf16_f32 v239, v54, v55\n\t"
      "ds_read_b128 a[232:235], v58 offset:25600\n\t"
      "v_mfma_f32_4x4x4_16b_bf16 v[40:43], a[180:181], a[140:141], 0\n\t"
      "ds_read_b64_tr_b16 a[148:149], v60 offset:49280\n\t"
      "v_mfma_f32_4x4x4_16b_bf16 v[44:47], a[182:183], a[142:143], 0\n\t"
      "ds_read_b64_tr_b16 a[150:151], v60 offset:50048\n\t"
      "v_mfma_f32_4x4x4_16b_bf16 v[48:51], a[180:181], a[144:145], 0\n\t"
      "ds_read_b64_tr_b16 a[152:153], v61 offset:49280\n\t"
      "v_mfma_f32_4x4x4_16b_bf16 v[52:55], a[182:183], a[146:147], 0\n\t"
      "ds_read_b64_tr_b16 a[154:155], v61 offset:50048\n\t"
      "v_mfma_f32_16x16x32_bf16 a[164:167], v[216:219], a[228:231], a[164:167]\n\t"
      "v_cvt_pk_bf16_f32 v40, v40, v41\n\t"
      "v_cvt_pk_bf16_f32 v41, v42, v43\n\t"
      "v_mfma_f32_16x16x32_bf16 a[168:171], v[220:223], a[228:231], a[168:171]\n\t"
      "v_cvt_pk_bf16_f32 v44, v44, v45\n\t"
      "v_cvt_pk_bf16_f32 v45, v46, v47\n\t"
      "v_mfma_f32_16x16x32_bf16 a[172:175], a[132:135], a[228:231], a[172:175]\n\t"
      "v_cvt_pk_bf16_f32 v48, v48, v49\n\t"
      "v_cvt_pk_bf16_f32 v49, v50, v51\n\t"
      "v_mfma_f32_16x16x32_bf16 a[176:179], a[136:139], a[228:231], a[176:179]\n\t"
      "v_cvt_pk_bf16_f32 v52, v52, v53\n\t"
      "v_cvt_pk_bf16_f32 v53, v54, v55\n\t"
      "v_accvgpr_write_b32 a140, v40\n\t"
      "v_accvgpr_write_b32 a141, v41\n\t"
      "v_accvgpr_write_b32 a142, v44\n\t"
      "v_accvgpr_write_b32 a143, v45\n\t"
      "v_accvgpr_write_b32 a144, v48\n\t"
      "v_accvgpr_write_b32 a145, v49\n\t"
      "v_accvgpr_write_b32 a146, v52\n\t"
      "v_accvgpr_write_b32 a147, v53\n\t"
      "s_waitcnt lgkmcnt(0)\n\t"
      "; step\n\t"
      "v_mfma_f32_4x4x4_16b_bf16 v[40:43], a[184:185], v[240:241], 0\n\t"
      "v_mfma_f32_4x4x4_16b_bf16 v[44:47], a[186:187], v[242:243], 0\n\t"
      "v_mfma_f32_4x4x4_16b_bf16 v[48:51], a[184:185], v[244:245], 0\n\t"
      "v_mfma_f32_4x4x4_16b_bf16 v[52:55], a[186:187], v[246:247], 0\n\t"
      "v_mfma_f32_16x16x32_bf16 v[16:19], v[224:227], a[232:235], v[16:19]\n\t"
      "s_nop 0\n\t"
      "v_cvt_pk_bf16_f32 v240, v40, v41\n\t"
      "v_cvt_pk_bf16_f32 v241, v42, v43\n\t"
      "v_mfma_f32_16x16x32_bf16 v[20:23], v[228:231], a[232:235], v[20:23]\n\t"
      "v_cvt_pk_bf16_f32 v242, v44, v45\n\t"
      "v_cvt_pk_bf16_f32 v243, v46, v47\n\t"
      "v_cvt_pk_bf16_f32 v244, v48, v49\n\t"
      "v_cvt_pk_bf16_f32 v245, v50, v51\n\t"
      "v_cvt_pk_bf16_f32 v246, v52, v53\n\t"
      "v_cvt_pk_bf16_f32 v247, v54, v55\n\t"
      "v_mfma_f32_4x4x4_16b_bf16 v[40:43], a[184:185], v[248:249], 0\n\t"
      "v_mfma_f32_4x4x4_16b_bf16 v[44:47], a[186:187], v[250:251], 0\n\t"
      "v_mfma_f32_4x4x4_16b_bf16 v[48:51], a[184:185], v[252:253], 0\n\t"
      "v_mfma_f32_4x4x4_16b_bf16 v[52:55], a[186:187], v[254:255], 0\n\t"
      "v_mfma_f32_16x16x32_bf16 v[24:27], v[232:235], a[232:235], v[24:27]\n\t"
      "s_nop 0\n\t"
      "v_cvt_pk_bf16_f32 v248, v40, v41\n\t"
      "v_cvt_pk_bf16_f32 v249, v42, v43\n\t"
      "v_mfma_f32_16x16x32_bf16 v[28:31], v[236:239], a[232:235], v[28:31]\n\t"
      "v_cvt_pk_bf16_f32 v250, v44, v45\n\t"
      "v_cvt_pk_bf16_f32 v251, v46, v47\n\t"
      "v_cvt_pk_bf16_f32 v252, v48, v49\n\t"
      "v_cvt_pk_bf16_f32 v253, v50, v51\n\t"
      "v_cvt_pk_bf16_f32 v254, v52, v53\n\t"
      "v_cvt_pk_bf16_f32 v255, v54, v55\n\t"
      "ds_read_b128 a[236:239], v59 offset:25600\n\t"
      "v_mfma_f32_4x4x4_16b_bf16 v[40:43], a[184:185], a[148:149], 0\n\t"
      "v_mfma_f32_4x4x4_16b_bf16 v[44:47], a[186:187], a[150:151], 0\n\t"
      "v_mfma_f32_4x4x4_16b_bf16 v[48:51], a[184:185], a[152:153], 0\n\t"
      "v_mfma_f32_4x4x4_16b_bf16 v[52:55], a[186:187], a[154:155], 0\n\t"
      "ds_read_b128 a[240:243], v58 offset:37888\n\t"
      "ds_read_b128 a[244:247], v59 offset:37888\n\t"
      "v_mfma_f32_16x16x32_bf16 v[32:35], a[140:143], a[232:235], v[32:35]\n\t"
      "v_cvt_pk_bf16_f32 v40, v40, v41\n\t"
      "v_cvt_pk_bf16_f32 v41, v42, v43\n\t"
      "v_mfma_f32_16x16x32_bf16 v[36:39], a[144:147], a[232:235], v[36:39]\n\t"
      "v_cvt_pk_bf16_f32 v44, v44, v45\n\t"
      "v_cvt_pk_bf16_f32 v45, v46, v47\n\t"
      "v_cvt_pk_bf16_f32 v48, v48, v49\n\t"
      "v_cvt_pk_bf16_f32 v49, v50, v51\n\t"
      "v_cvt_pk_bf16_f32 v52, v52, v53\n\t"
      "v_cvt_pk_bf16_f32 v53, v54, v55\n\t"
      "v_accvgpr_write_b32 a148, v40\n\t"
      "v_accvgpr_write_b32 a149, v41\n\t"
      "v_accvgpr_write_b32 a150, v44\n\t"
      "v_accvgpr_write_b32 a151, v45\n\t"
      "v_accvgpr_write_b32 a152, v48\n\t"
      "v_accvgpr_write_b32 a153, v49\n\t"
      "v_accvgpr_write_b32 a154, v52\n\t"
      "v_accvgpr_write_b32 a155, v53\n\t"
      "s_waitcnt lgkmcnt(2)\n\t"
      "v_mfma_f32_16x16x32_bf16 v[16:19], v[240:243], a[236:239], v[16:19]\n\t"
      "s_add_u32 s49, s15, 0\n\t"
      "s_mov_b32 s50, 98816\n\t"
      "s_mov_b32 s51, 61952\n\t"
      "v_mfma_f32_16x16x32_bf16 v[20:23], v[244:247], a[236:239], v[20:23]\n\t"
      "s_mov_b32 s58, s14\n\t"
      "s_add_u32 s56, s14, 2359296\n\t"
      "v_mfma_f32_16x16x32_bf16 v[24:27], v[248:251], a[236:239], v[24:27]\n\t"
      "s_movk_i32 s39, 88\n\t"
      "s_movk_i32 s29, 71\n\t"
      "s_mov_b32 s32, 1947275646\n\t"
      "v_mfma_f32_16x16x32_bf16 v[28:31], v[252:255], a[236:239], v[28:31]\n\t"
      "s_mov_b32 s33, 143165559\n\t"
      "s_mov_b32 s34, 1950421338\n\t"
      "s_mov_b32 s35, 8947847\n\t"
      "v_mfma_f32_16x16x32_bf16 v[32:35], a[148:151], a[236:239], v[32:35]\n\t"
      "v_mov_b32 v52, 163328\n\t"
      "v_mfma_f32_16x16x32_bf16 v[36:39], a[152:155], a[236:239], v[36:39]\n\t"
      "s_cmp_lg_u32 s17, 0\n\t"
      "s_cselect_b32 s39, 0, s39\n\t"
      "s_cbranch_scc1 .Lno4_%=\n\t"
      "v_accvgpr_read_b32 v41, a248\n\t"
      "v_mov_b32 v51, 0x4040\n\t"
      "s_movk_i32 s52, 2000\n\t"
      "s_mov_b32 s91, 0\n\t"
      "s_waitcnt vmcnt(6) \n\t"
      "v_accvgpr_read_b32 v40, a252\n\t"
      ".Lcheck_%=:\n\t"
      "v_cmp_ne_u32 s[54:55], v40, v63\n\t"
      "s_cmp_lg_u32 s16, 0\n\t"
      "v_add_u32_dpp v42, v40, v40 quad_perm:[2,3,0,1] row_mask:0xf bank_mask:0xf\n\t"
      "s_cselect_b32 s30, -1, s54\n\t"
      "s_cmp_lg_u32 s30, -1\n\t"
      "s_cbranch_scc1 .Lwait_%=\n\t"
      ".Lwaited_%=:\n\t"
      "v_add_u32_dpp v42, v42, v42 quad_perm:[1,0,3,2] row_mask:0xf bank_mask:0xf\n\t"
      "s_bfe_u32 s30, s19, 0x30008\n\t"
      "s_bfe_u32 s31, s19, 0x10003\n\t"
      "s_lshl1_add_u32 s30, s30, s31\n\t"
      "s_lshl_b32 s30, s30, 1\n\t"
      "v_mov_b32 v43, v42\n\t"
      "v_subrev_u32_dpp v43, v42, v51 row_half_mirror row_mask:0x3 bank_mask:0xf\n\t"
      "v_lshl_add_u32 v42, v42, 1, v43\n\t"
      "v_readlane_b32 s31, v40, s30\n\t"
      "v_add_u32 v42, 0x1f1f, v42\n\t"
      "v_and_b32 v43, 128, v42\n\t"
      "v_and_b32 v44, 32768, v42\n\t"
      "v_cmp_ne_u32 s[88:89], 0, v43\n\t"
      "v_cmp_ne_u32 s[36:37], 0, v44\n\t"
      "s_bitcmp1_b32 s19, 2\n\t"
      "s_cselect_b32 s30, 0x3ffff, 0\n\t"
      "s_bitcmp1_b32 s31, 16\n\t"
      "s_cselect_b32 s31, 262140, 0\n\t"
      "s_xor_b32 s30, s30, s31\n\t"
      "v_lshrrev_b32 v41, v41, s88\n\t"
      "v_and_b32 v41, 1, v41\n\t"
      "v_cmp_ne_u32 vcc, 0, v41\n\t"
      "s_xor_b32 s36, s37, s89\n\t"
      "s_and_b32 s36, s36, 0x11111111\n\t"
      "s_bcnt1_i32_b32 s36, s36\n\t"
      "s_and_b32 s31, vcc_lo, 262140\n\t"
      "s_xor_b32 s31, s31, s30\n\t"
      "s_or_b32 s31, s31, s91\n\t"
      "s_and_b32 s30, s16, 0x3ffff\n\t"
      "s_cmp_lg_u32 s16, 0\n\t"
      "s_cselect_b32 s31, s30, s31\n\t"
      "v_mov_b32 v53, s31\n\t"
      "ds_write_b32 v52, v53\n\t"
      "s_lshl_b32 s30, s36, 2\n\t"
      "s_mov_b32 s54, 1412636928\n\t"
      "s_mov_b32 s55, 5\n\t"
      "s_lshr_b64 s[54:55], s[54:55], s30\n\t"
      "s_and_b32 s30, s54, 15\n\t"
      "s_add_u32 s29, s29, s30\n\t"
      ".Lno4_%=:\n\t"
      "v_accvgpr_read_b32 v9, a213\n\t"
      "v_accvgpr_read_b32 v10, a214\n\t"
      "v_accvgpr_read_b32 v11, a215\n\t"
      "v_accvgpr_read_b32 v12, a216\n\t"
      "v_accvgpr_read_b32 v13, a217\n\t"
      "v_accvgpr_read_b32 v14, a218\n\t"
      "v_accvgpr_read_b32 v8, a219\n\t"
      "v_accvgpr_read_b32 v60, a220\n\t"
      "v_accvgpr_read_b32 v61, a221\n\t"
      "v_add_u32 v56, 49664, v56\n\t"
      "v_add_u32 v57, 49664, v57\n\t"
      "v_add_u32 v58, 49664, v58\n\t"
      "v_add_u32 v59, 49664, v59\n\t"
      "v_cvt_pk_bf16_f32 v40, v16, v17\n\t"
      "v_cvt_pk_bf16_f32 v41, v18, v19\n\t"
      "v_cvt_pk_bf16_f32 v42, v20, v21\n\t"
      "v_cvt_pk_bf16_f32 v43, v22, v23\n\t"
      "v_cvt_pk_bf16_f32 v44, v24, v25\n\t"
      "v_cvt_pk_bf16_f32 v45, v26, v27\n\t"
      "v_cvt_pk_bf16_f32 v46, v28, v29\n\t"
      "v_cvt_pk_bf16_f32 v47, v30, v31\n\t"
      "v_cvt_pk_bf16_f32 v48, v32, v33\n\t"
      "v_cvt_pk_bf16_f32 v49, v34, v35\n\t"
      "v_cvt_pk_bf16_f32 v50, v36, v37\n\t"
      "v_cvt_pk_bf16_f32 v51, v38, v39\n\t"
      "s_mov_b32 s46, 113246208\n\t"
      "s_mov_b32 s52, 15\n\t"
      "s_add_u32 s90, s90, 0x20000\n\t"
      "s_waitcnt vmcnt(3)\n\t"
      "s_waitcnt lgkmcnt(0)\n\t"
      "s_barrier\n\t"
      "v_mfma_f32_16x16x32_bf16 a[156:159], v[224:227], a[240:243], a[156:159]\n\t"
      "ds_read_b32 v63, v52\n\t"
      "ds_read_b128 a[0:3], v56 offset:0\n\t"
      "s_memrealtime s[36:37]\n\t"
      "v_mfma_f32_16x16x32_bf16 a[160:163], v[228:231], a[240:243], a[160:163]\n\t"
      "ds_read_b128 a[4:7], v57 offset:0\n\t"
      "ds_read_b128 a[8:11], v58 offset:0\n\t"
      "v_mfma_f32_16x16x32_bf16 a[164:167], v[232:235], a[240:243], a[164:167]\n\t"
      "ds_read_b128 a[12:15], v59 offset:0\n\t"
      "ds_read_b128 a[16:19], v56 offset:256\n\t"
      "v_mfma_f32_16x16x32_bf16 a[168:171], v[236:239], a[240:243], a[168:171]\n\t"
      "ds_read_b128 a[20:23], v57 offset:256\n\t"
      "ds_write_b64 v0, v[40:41] offset:0\n\t"
      "v_mfma_f32_16x16x32_bf16 a[172:175], a[140:143], a[240:243], a[172:175]\n\t"
      "ds_write_b64 v0, v[42:43] offset:32\n\t"
      "v_mfma_f32_16x16x32_bf16 a[176:179], a[144:147], a[240:243], a[176:179]\n\t"
      "s_waitcnt lgkmcnt(8)\n\t"
      "v_readfirstlane_b32 s91, v63\n\t"
      "v_mfma_f32_16x16x32_bf16 a[156:159], v[240:243], a[244:247], a[156:159]\n\t"
      "ds_write_b64 v0, v[44:45] offset:64\n\t"
      "s_add_u32 m0, s18, 74240\n\t"
      "v_mfma_f32_16x16x32_bf16 a[160:163], v[244:247], a[244:247], a[160:163]\n\t"
      "s_bitcmp1_b32 s91, 2\n\t"
      "s_cselect_b32 s48, 24576, 0\n\t"
      "s_add_u32 s48, s48, s14\n\t"
      "v_mfma_f32_16x16x32_bf16 a[164:167], v[248:251], a[244:247], a[164:167]\n\t"
      "s_add_u32 s48, s48, 1572864\n\t"
      "buffer_load_dwordx4 v2, s[40:43], s48 offen nt lds\n\t"
      "s_mov_b32 s59, s91\n\t"
      "v_mfma_f32_16x16x32_bf16 a[168:171], v[252:255], a[244:247], a[168:171]\n\t"
      "ds_write_b64 v0, v[46:47] offset:96\n\t"
      "buffer_load_dwordx4 v3, s[40:43], s48 offen offset:1024 nt lds\n\t"
      "v_mfma_f32_16x16x32_bf16 a[172:175], a[148:151], a[244:247], a[172:175]\n\t"
      "ds_write_b64 v0, v[48:49] offset:128\n\t"
      "buffer_load_dwordx4 v4, s[40:43], s48 offen offset:2048 nt lds\n\t"
      "v_mfma_f32_16x16x32_bf16 a[176:179], a[152:155], a[244:247], a[176:179]\n\t"
      "ds_write_b64 v0, v[50:51] offset:160\n\t"
      "s_lshr_b32 s57, s91, 3\n\t"
      "s_bitcmp1_b32 s91, 3\n\t"
      "s_cselect_b32 s48, 24576, 0\n\t"
      "s_add_u32 s48, s48, s14\n\t"
      "s_add_u32 s48, s48, 2359296\n\t"
      "s_or_b32 s30, s17, s16\n\t"
      "s_cbranch_scc1 .Lno5_%=\n\t"
      "v_mov_b32 v54, s90\n\t"
      "v_mov_b32 v55, s19\n\t"
      "global_store_dword v55, v54, s[12:13] sc1\n\t"
      ".Lno5_%=:\n\t"
      "s_waitcnt lgkmcnt(11)\n\t"
      "v_mfma_f32_16x16x32_bf16 v[16:19], v[64:67], a[0:3], 0\n\t"
      "ds_read_b128 a[24:27], v58 offset:256\n\t"
      "ds_read_b128 a[28:31], v59 offset:256\n\t"
      "s_waitcnt lgkmcnt(12)\n\t"
      "v_mfma_f32_16x16x32_bf16 v[16:19], v[80:83], a[4:7], v[16:19]\n\t"
      "ds_read_b128 a[32:35], v56 offset:512\n\t"
      "ds_read_b128 a[36:39], v57 offset:512\n\t"
      "s_waitcnt lgkmcnt(13)\n\t"
      "v_mfma_f32_16x16x32_bf16 v[16:19], v[96:99], a[8:11], v[16:19]\n\t"
      "ds_read_b128 a[40:43], v58 offset:512\n\t"
      "ds_read_b128 a[44:47], v59 offset:512\n\t"
      "s_waitcnt lgkmcnt(14)\n\t"
      "v_mfma_f32_16x16x32_bf16 v[16:19], v[112:115], a[12:15], v[16:19]\n\t"
      "v_accvgpr_read_b32 v52, a156\n\t"
      "v_accvgpr_read_b32 v53, a157\n\t"
      "s_waitcnt lgkmcnt(13)\n\t"
      "v_mfma_f32_16x16x32_bf16 v[16:19], v[128:131], a[16:19], v[16:19]\n\t"
      "v_accvgpr_read_b32 v54, a158\n\t"
      "v_accvgpr_read_b32 v55, a159\n\t"
      "s_waitcnt lgkmcnt(12)\n\t"
      "v_mfma_f32_16x16x32_bf16 v[16:19], v[144:147], a[20:23], v[16:19]\n\t"
      "v_cvt_pk_bf16_f32 v40, v52, v53\n\t"
      "v_cvt_pk_bf16_f32 v41, v54, v55\n\t"
      "v_mfma_f32_16x16x32_bf16 v[20:23], v[68:71], a[0:3], 0\n\t"
      "ds_write_b64 v0, v[40:41] offset:12544\n\t"
      "v_mfma_f32_16x16x32_bf16 v[20:23], v[84:87], a[4:7], v[20:23]\n\t"
      "s_waitcnt lgkmcnt(7)\n\t"
      "s_barrier\n\t"
      "v_mfma_f32_16x16x32_bf16 v[20:23], v[100:103], a[8:11], v[20:23]\n\t"
      "ds_read_b128 a[48:51], v8 offset:0\n\t"
      "ds_read_b128 a[52:55], v60 offset:1024\n\t"
      "ds_read_b128 a[56:59], v61 offset:2048\n\t"
      "v_mfma_f32_16x16x32_bf16 v[20:23], v[116:119], a[12:15], v[20:23]\n\t"
      "v_accvgpr_read_b32 v52, a160\n\t"
      "v_accvgpr_read_b32 v53, a161\n\t"
      "v_mfma_f32_16x16x32_bf16 v[20:23], v[132:135], a[16:19], v[20:23]\n\t"
      "v_accvgpr_read_b32 v54, a162\n\t"
      "v_accvgpr_read_b32 v55, a163\n\t"
      "v_mfma_f32_16x16x32_bf16 v[20:23], v[148:151], a[20:23], v[20:23]\n\t"
      "v_cvt_pk_bf16_f32 v42, v52, v53\n\t"
      "v_cvt_pk_bf16_f32 v43, v54, v55\n\t"
      "v_mfma_f32_16x16x32_bf16 v[24:27], v[72:75], a[0:3], 0\n\t"
      "ds_write_b64 v0, v[42:43] offset:12576\n\t"
      "s_waitcnt lgkmcnt(3)\n\t"
      "v_mfma_f32_16x16x32_bf16 v[24:27], v[88:91], a[4:7], v[24:27]\n\t"
      "buffer_store_dwordx4 a[48:51], v9, s[44:47], s49 offen sc1\n\t"
      "v_mfma_f32_16x16x32_bf16 v[24:27], v[104:107], a[8:11], v[24:27]\n\t"
      "v_accvgpr_read_b32 v52, a164\n\t"
      "v_accvgpr_read_b32 v53, a165\n\t"
      "v_mfma_f32_16x16x32_bf16 v[24:27], v[120:123], a[12:15], v[24:27]\n\t"
      "v_accvgpr_read_b32 v54, a166\n\t"
      "v_accvgpr_read_b32 v55, a167\n\t"
      "v_mfma_f32_16x16x32_bf16 v[24:27], v[136:139], a[16:19], v[24:27]\n\t"
      "v_cvt_pk_bf16_f32 v44, v52, v53\n\t"
      "v_cvt_pk_bf16_f32 v45, v54, v55\n\t"
      "v_mfma_f32_16x16x32_bf16 v[24:27], v[152:155], a[20:23], v[24:27]\n\t"
      "ds_write_b64 v0, v[44:45] offset:12608\n\t"
      "s_waitcnt lgkmcnt(3)\n\t"
      "v_mfma_f32_16x16x32_bf16 v[28:31], v[76:79], a[0:3], 0\n\t"
      "buffer_store_dwordx4 a[52:55], v10, s[44:47], s49 offen offset:1024 sc1\n\t"
      "v_mfma_f32_16x16x32_bf16 v[28:31], v[92:95], a[4:7], v[28:31]\n\t"
      "v_accvgpr_read_b32 v52, a168\n\t"
      "v_accvgpr_read_b32 v53, a169\n\t"
      "v_mfma_f32_16x16x32_bf16 v[28:31], v[108:111], a[8:11], v[28:31]\n\t"
      "v_accvgpr_read_b32 v54, a170\n\t"
      "v_accvgpr_read_b32 v55, a171\n\t"
      "v_mfma_f32_16x16x32_bf16 v[28:31], v[124:127], a[12:15], v[28:31]\n\t"
      "v_cvt_pk_bf16_f32 v46, v52, v53\n\t"
      "v_cvt_pk_bf16_f32 v47, v54, v55\n\t"
      "v_mfma_f32_16x16x32_bf16 v[28:31], v[140:143], a[16:19], v[28:31]\n\t"
      "ds_write_b64 v0, v[46:47] offset:12640\n\t"
      "s_waitcnt lgkmcnt(3)\n\t"
      "v_mfma_f32_16x16x32_bf16 v[28:31], v[156:159], a[20:23], v[28:31]\n\t"
      "buffer_store_dwordx4 a[56:59], v11, s[44:47], s49 offen offset:2048 sc1\n\t"
      "v_mfma_f32_16x16x32_bf16 v[32:35], a[60:63], a[0:3], 0\n\t"
      "v_accvgpr_read_b32 v52, a172\n\t"
      "v_accvgpr_read_b32 v53, a173\n\t"
      "v_mfma_f32_16x16x32_bf16 v[32:35], a[68:71], a[4:7], v[32:35]\n\t"
      "v_accvgpr_read_b32 v54, a174\n\t"
      "v_accvgpr_read_b32 v55, a175\n\t"
      "v_mfma_f32_16x16x32_bf16 v[32:35], a[76:79], a[8:11], v[32:35]\n\t"
      "v_cvt_pk_bf16_f32 v48, v52, v53\n\t"
      "v_cvt_pk_bf16_f32 v49, v54, v55\n\t"
      "v_mfma_f32_16x16x32_bf16 v[32:35], a[84:87], a[12:15], v[32:35]\n\t"
      "ds_write_b64 v0, v[48:49] offset:12672\n\t"
      "v_mfma_f32_16x16x32_bf16 v[32:35], a[92:95], a[16:19], v[32:35]\n\t"
      "v_accvgpr_read_b32 v52, a176\n\t"
      "v_accvgpr_read_b32 v53, a177\n\t"
      "v_mfma_f32_16x16x32_bf16 v[32:35], a[100:103], a[20:23], v[32:35]\n\t"
      "v_accvgpr_read_b32 v54, a178\n\t"
      "v_accvgpr_read_b32 v55, a179\n\t"
      "v_mfma_f32_16x16x32_bf16 v[36:39], a[64:67], a[0:3], 0\n\t"
      "v_cvt_pk_bf16_f32 v50, v52, v53\n\t"
      "v_cvt_pk_bf16_f32 v51, v54, v55\n\t"
      "v_mfma_f32_16x16x32_bf16 v[36:39], a[72:75], a[4:7], v[36:39]\n\t"
      "ds_write_b64 v0, v[50:51] offset:12704\n\t"
      "v_mfma_f32_16x16x32_bf16 v[36:39], a[80:83], a[8:11], v[36:39]\n\t"
      "v_add_u32 v52, s51, v15\n\t"
      "v_xor_b32 v53, 64, v52\n\t"
      "v_mfma_f32_16x16x32_bf16 v[36:39], a[88:91], a[12:15], v[36:39]\n\t"
      "v_xor_b32 v54, 128, v52\n\t"
      "v_xor_b32 v55, 192, v52\n\t"
      "v_mfma_f32_16x16x32_bf16 v[36:39], a[96:99], a[16:19], v[36:39]\n\t"
      "s_waitcnt lgkmcnt(0)\n\t"
      "s_add_u32 s38, s36, s39\n\t"
      "s_sub_u32 s28, s36, s28\n\t"
      "s_sub_i32 s28, s28, 515\n\t"
      "s_max_i32 s28, s28, 0\n\t"
      "s_min_i32 s28, s28, 60\n\t"
      "s_and_b32 s39, s32, 15\n\t"
      "s_cselect_b32 s53, s29, 0\n\t"
      "s_memrealtime s[36:37]\n\t"
      "v_mfma_f32_16x16x32_bf16 v[36:39], a[104:107], a[20:23], v[36:39]\n\t"
      "s_add_u32 s39, s39, s53\n\t"
      "s_lshr_b64 s[32:33], s[32:33], 4\n\t"
      "s_add_u32 m0, s18, 86528\n\t"
      "v_mfma_f32_16x16x32_bf16 v[16:19], v[160:163], a[24:27], v[16:19]\n\t"
      "s_bitcmp1_b32 s91, 2\n\t"
      "s_cselect_b32 s54, 24576, 0\n\t"
      "s_add_u32 s54, s54, s14\n\t"
      "s_add_u32 s54, s54, 1572864\n\t"
      "v_mfma_f32_16x16x32_bf16 v[16:19], v[176:179], a[28:31], v[16:19]\n\t"
      "s_waitcnt vmcnt(6)\n\t"
      "s_barrier\n\t"
      "v_mfma_f32_16x16x32_bf16 v[16:19], v[192:195], a[32:35], v[16:19]\n\t"
      "ds_read_b128 a[0:3], v52 offset:0\n\t"
      "ds_read_b128 a[188:191], v8 offset:12544\n\t"
      "buffer_load_dwordx4 v5, s[40:43], s54 offen nt lds\n\t"
      "v_mfma_f32_16x16x32_bf16 v[16:19], v[208:211], a[36:39], v[16:19]\n\t"
      "ds_read_b128 a[4:7], v53 offset:0\n\t"
      "ds_read_b128 a[192:195], v60 offset:13568\n\t"
      "v_mfma_f32_16x16x32_bf16 v[16:19], v[224:227], a[40:43], v[16:19]\n\t"
      "ds_read_b128 a[8:11], v54 offset:0\n\t"
      "ds_read_b128 a[196:199], v61 offset:14592\n\t"
      "buffer_load_dwordx4 v6, s[40:43], s54 offen offset:1024 nt lds\n\t"
      "v_mfma_f32_16x16x32_bf16 v[16:19], v[240:243], a[44:47], v[16:19]\n\t"
      "ds_read_b128 a[12:15], v55 offset:0\n\t"
      "v_mfma_f32_16x16x32_bf16 v[20:23], v[164:167], a[24:27], v[20:23]\n\t"
      "ds_read_b128 a[16:19], v52 offset:256\n\t"
      "buffer_load_dwordx4 v7, s[40:43], s54 offen offset:2048 nt lds\n\t"
      "v_mfma_f32_16x16x32_bf16 v[20:23], v[180:183], a[28:31], v[20:23]\n\t"
      "ds_read_b128 a[20:23], v53 offset:256\n\t"
      "s_add_u32 m0, s18, s50\n\t"
      "v_mfma_f32_16x16x32_bf16 v[20:23], v[196:199], a[32:35], v[20:23]\n\t"
      "s_add_u32 s51, s51, 12288\n\t"
      "s_cmp_eq_u32 s51, 160256\n\t"
      "s_cselect_b32 s51, 25088, s51\n\t"
      "s_waitcnt lgkmcnt(7)\n\t"
      "v_mfma_f32_16x16x32_bf16 v[20:23], v[212:215], a[36:39], v[20:23]\n\t"
      "buffer_store_dwordx4 a[188:191], v12, s[44:47], s49 offen sc1\n\t"
      "v_mfma_f32_16x16x32_bf16 v[20:23], v[228:231], a[40:43], v[20:23]\n\t"
      "v_mfma_f32_16x16x32_bf16 v[20:23], v[244:247], a[44:47], v[20:23]\n\t"
      "s_waitcnt lgkmcnt(0)\n\t"
      "s_sub_u32 s53, s36, s38\n\t"
      "s_cmp_lt_i32 s53, 0\n\t"
      "v_mfma_f32_16x16x32_bf16 v[24:27], v[168:171], a[24:27], v[24:27]\n\t"
      "s_cbranch_scc1 .Lspinf_%=\n\t"
      ".Lspunf_%=:\n\t"
      "s_sub_i32 s53, s53, 30\n\t"
      "s_max_i32 s53, s53, 0\n\t"
      "v_mfma_f32_16x16x32_bf16 v[24:27], v[184:187], a[28:31], v[24:27]\n\t"
      "s_add_u32 s38, s38, s53\n\t"
      "s_add_u32 s38, s38, s39\n\t"
      "s_add_u32 s28, s28, s53\n\t"
      "v_mfma_f32_16x16x32_bf16 v[24:27], v[200:203], a[32:35], v[24:27]\n\t"
      "v_cvt_pk_bf16_f32 v40, v16, v17\n\t"
      "v_cvt_pk_bf16_f32 v41, v18, v19\n\t"
      "v_mfma_f32_16x16x32_bf16 v[24:27], v[216:219], a[36:39], v[24:27]\n\t"
      "ds_write_b64 v0, v[40:41] offset:0\n\t"
      "v_mfma_f32_16x16x32_bf16 v[24:27], v[232:235], a[40:43], v[24:27]\n\t"
      "buffer_load_dwordx4 v2, s[40:43], s48 offen nt lds\n\t"
      "v_mfma_f32_16x16x32_bf16 v[24:27], v[248:251], a[44:47], v[24:27]\n\t"
      "buffer_store_dwordx4 a[192:195], v13, s[44:47], s49 offen offset:1024 sc1\n\t"
      "v_mfma_f32_16x16x32_bf16 v[28:31], v[172:175], a[24:27], v[28:31]\n\t"
      "v_mfma_f32_16x16x32_bf16 v[28:31], v[188:191], a[28:31], v[28:31]\n\t"
      "v_cvt_pk_bf16_f32 v42, v20, v21\n\t"
      "v_cvt_pk_bf16_f32 v43, v22, v23\n\t"
      "v_mfma_f32_16x16x32_bf16 v[28:31], v[204:207], a[32:35], v[28:31]\n\t"
      "ds_write_b64 v0, v[42:43] offset:32\n\t"
      "v_mfma_f32_16x16x32_bf16 v[28:31], v[220:223], a[36:39], v[28:31]\n\t"
      "buffer_load_dwordx4 v3, s[40:43], s48 offen offset:1024 nt lds\n\t"
      "v_mfma_f32_16x16x32_bf16 v[28:31], v[236:239], a[40:43], v[28:31]\n\t"
      "buffer_store_dwordx4 a[196:199], v14, s[44:47], s49 offen offset:2048 sc1\n\t"
      "v_mfma_f32_16x16x32_bf16 v[28:31], v[252:255], a[44:47], v[28:31]\n\t"
      "s_add_u32 s58, s58, 786432\n\t"
      "s_lshr_b32 s59, s59, 1\n\t"
      "v_mfma_f32_16x16x32_bf16 v[32:35], a[108:111], a[24:27], v[32:35]\n\t"
      "s_bitcmp1_b32 s59, 0\n\t"
      "s_cselect_b32 s49, 24576, 0\n\t"
      "s_add_u32 s49, s49, s58\n\t"
      "v_mfma_f32_16x16x32_bf16 v[32:35], a[116:119], a[28:31], v[32:35]\n\t"
      "v_cvt_pk_bf16_f32 v44, v24, v25\n\t"
      "v_cvt_pk_bf16_f32 v45, v26, v27\n\t"
      "v_mfma_f32_16x16x32_bf16 v[32:35], a[124:127], a[32:35], v[32:35]\n\t"
      "ds_write_b64 v0, v[44:45] offset:64\n\t"
      "v_mfma_f32_16x16x32_bf16 v[32:35], a[132:135], a[36:39], v[32:35]\n\t"
      "buffer_load_dwordx4 v4, s[40:43], s48 offen offset:2048 nt lds\n\t"
      "v_mfma_f32_16x16x32_bf16 v[32:35], a[140:143], a[40:43], v[32:35]\n\t"
      "s_add_u32 s50, s50, 12288\n\t"
      "v_mfma_f32_16x16x32_bf16 v[32:35], a[148:151], a[44:47], v[32:35]\n\t"
      "v_cvt_pk_bf16_f32 v46, v28, v29\n\t"
      "v_cvt_pk_bf16_f32 v47, v30, v31\n\t"
      "v_mfma_f32_16x16x32_bf16 v[36:39], a[112:115], a[24:27], v[36:39]\n\t"
      "ds_write_b64 v0, v[46:47] offset:96\n\t"
      "v_mfma_f32_16x16x32_bf16 v[36:39], a[120:123], a[28:31], v[36:39]\n\t"
      "s_cmp_eq_u32 s50, 160256\n\t"
      "s_cselect_b32 s50, 25088, s50\n\t"
      "v_mfma_f32_16x16x32_bf16 v[36:39], a[128:131], a[32:35], v[36:39]\n\t"
      "v_mfma_f32_16x16x32_bf16 v[36:39], a[136:139], a[36:39], v[36:39]\n\t"
      "v_mfma_f32_16x16x32_bf16 v[36:39], a[144:147], a[40:43], v[36:39]\n\t"
      "v_cvt_pk_bf16_f32 v48, v32, v33\n\t"
      "v_cvt_pk_bf16_f32 v49, v34, v35\n\t"
      "v_mfma_f32_16x16x32_bf16 v[36:39], a[152:155], a[44:47], v[36:39]\n\t"
      "ds_write_b64 v0, v[48:49] offset:128\n\t"
      "s_cmp_eq_u32 s17, 6144\n\t"
      "s_cbranch_scc1 .Lmid1_%=\n\t"
      "s_cmp_eq_u32 s17, 12288\n\t"
      "s_cbranch_scc1 .Lmid2_%=\n\t"
      "s_cmp_eq_u32 s17, 18432\n\t"
      "s_cbranch_scc1 .Lmid3_%=\n\t"
      "s_branch .Lmid0_%=\n\t"
      ".Lloop0_%=:\n\t"
      "v_mfma_f32_16x16x32_bf16 v[16:19], v[64:67], a[0:3], 0\n\t"
      "ds_read_b128 a[24:27], v58 offset:256\n\t"
      "v_mfma_f32_16x16x32_bf16 v[16:19], v[80:83], a[4:7], v[16:19]\n\t"
      "ds_read_b128 a[28:31], v59 offset:256\n\t"
      "v_mfma_f32_16x16x32_bf16 v[16:19], v[96:99], a[8:11], v[16:19]\n\t"
      "ds_read_b128 a[32:35], v56 offset:512\n\t"
      "v_mfma_f32_16x16x32_bf16 v[16:19], v[112:115], a[12:15], v[16:19]\n\t"
      "ds_read_b128 a[36:39], v57 offset:512\n\t"
      "v_mfma_f32_16x16x32_bf16 v[16:19], v[128:131], a[16:19], v[16:19]\n\t"
      "ds_read_b128 a[40:43], v58 offset:512\n\t"
      "v_mfma_f32_16x16x32_bf16 v[16:19], v[144:147], a[20:23], v[16:19]\n\t"
      "ds_read_b128 a[44:47], v59 offset:512\n\t"
      "v_mfma_f32_16x16x32_bf16 v[20:23], v[68:71], a[0:3], 0\n\t"
      "v_cvt_pk_bf16_f32 v50, v36, v37\n\t"
      "v_cvt_pk_bf16_f32 v51, v38, v39\n\t"
      "v_mfma_f32_16x16x32_bf16 v[20:23], v[84:87], a[4:7], v[20:23]\n\t"
      "ds_write_b64 v0, v[50:51] offset:12704\n\t"
      "v_mfma_f32_16x16x32_bf16 v[20:23], v[100:103], a[8:11], v[20:23]\n\t"
      "v_add_u32 v52, s51, v15\n\t"
      "v_xor_b32 v53, 64, v52\n\t"
      "v_mfma_f32_16x16x32_bf16 v[20:23], v[116:119], a[12:15], v[20:23]\n\t"
      "v_xor_b32 v54, 128, v52\n\t"
      "v_xor_b32 v55, 192, v52\n\t"
      "v_mfma_f32_16x16x32_bf16 v[20:23], v[132:135], a[16:19], v[20:23]\n\t"
      "s_add_u32 s56, s56, 786432\n\t"
      "s_lshr_b32 s57, s57, 1\n\t"
      "v_mfma_f32_16x16x32_bf16 v[20:23], v[148:151], a[20:23], v[20:23]\n\t"
      "s_bitcmp1_b32 s57, 0\n\t"
      "s_cselect_b32 s48, 24576, 0\n\t"
      "s_add_u32 s48, s48, s56\n\t"
      "v_mfma_f32_16x16x32_bf16 v[24:27], v[72:75], a[0:3], 0\n\t"
      "v_mfma_f32_16x16x32_bf16 v[24:27], v[88:91], a[4:7], v[24:27]\n\t"
      "v_mfma_f32_16x16x32_bf16 v[24:27], v[104:107], a[8:11], v[24:27]\n\t"
      "s_waitcnt vmcnt(12)\n\t"
      "s_waitcnt lgkmcnt(0)\n\t"
      "s_barrier\n\t"
      "v_mfma_f32_16x16x32_bf16 v[24:27], v[120:123], a[12:15], v[24:27]\n\t"
      "v_mfma_f32_16x16x32_bf16 v[24:27], v[136:139], a[16:19], v[24:27]\n\t"
      "v_mfma_f32_16x16x32_bf16 v[24:27], v[152:155], a[20:23], v[24:27]\n\t"
      "v_mfma_f32_16x16x32_bf16 v[28:31], v[76:79], a[0:3], 0\n\t"
      "ds_read_b128 a[48:51], v8 offset:12544\n\t"
      "v_mfma_f32_16x16x32_bf16 v[28:31], v[92:95], a[4:7], v[28:31]\n\t"
      "ds_read_b128 a[52:55], v60 offset:13568\n\t"
      "v_mfma_f32_16x16x32_bf16 v[28:31], v[108:111], a[8:11], v[28:31]\n\t"
      "ds_read_b128 a[56:59], v61 offset:14592\n\t"
      "v_mfma_f32_16x16x32_bf16 v[28:31], v[124:127], a[12:15], v[28:31]\n\t"
      "s_and_b32 s39, s32, 15\n\t"
      "s_cselect_b32 s53, s29, 0\n\t"
      "v_mfma_f32_16x16x32_bf16 v[28:31], v[140:143], a[16:19], v[28:31]\n\t"
      "s_add_u32 s39, s39, s53\n\t"
      "s_lshr_b64 s[32:33], s[32:33], 4\n\t"
      "v_mfma_f32_16x16x32_bf16 v[28:31], v[156:159], a[20:23], v[28:31]\n\t"
      "s_cmp_le_u32 s52, 12\n\t"
      "s_cselect_b32 s53, 3, 0\n\t"
      "s_min_u32 s53, s28, s53\n\t"
      "v_mfma_f32_16x16x32_bf16 v[32:35], a[60:63], a[0:3], 0\n\t"
      "s_sub_u32 s28, s28, s53\n\t"
      "s_sub_u32 s39, s39, s53\n\t"
      "v_mfma_f32_16x16x32_bf16 v[32:35], a[68:71], a[4:7], v[32:35]\n\t"
      "s_waitcnt lgkmcnt(2)\n\t"
      "v_mfma_f32_16x16x32_bf16 v[32:35], a[76:79], a[8:11], v[32:35]\n\t"
      "buffer_store_dwordx4 a[48:51], v12, s[44:47], s49 offen sc1\n\t"
      "v_mfma_f32_16x16x32_bf16 v[32:35], a[84:87], a[12:15], v[32:35]\n\t"
      "v_mfma_f32_16x16x32_bf16 v[32:35], a[92:95], a[16:19], v[32:35]\n\t"
      "v_mfma_f32_16x16x32_bf16 v[32:35], a[100:103], a[20:23], v[32:35]\n\t"
      "s_add_u32 m0, s18, s50\n\t"
      "v_mfma_f32_16x16x32_bf16 v[36:39], a[64:67], a[0:3], 0\n\t"
      "buffer_load_dwordx4 v2, s[40:43], s48 offen nt lds\n\t"
      "v_mfma_f32_16x16x32_bf16 v[36:39], a[72:75], a[4:7], v[36:39]\n\t"
      "s_memrealtime s[36:37]\n\t"
      "v_mfma_f32_16x16x32_bf16 v[36:39], a[80:83], a[8:11], v[36:39]\n\t"
      "v_mfma_f32_16x16x32_bf16 v[36:39], a[88:91], a[12:15], v[36:39]\n\t"
      "s_waitcnt lgkmcnt(1)\n\t"
      "v_mfma_f32_16x16x32_bf16 v[36:39], a[96:99], a[16:19], v[36:39]\n\t"
      "buffer_store_dwordx4 a[52:55], v13, s[44:47], s49 offen offset:1024 sc1\n\t"
      "v_mfma_f32_16x16x32_bf16 v[36:39], a[104:107], a[20:23], v[36:39]\n\t"
      "v_mfma_f32_16x16x32_bf16 v[16:19], v[160:163], a[24:27], v[16:19]\n\t"
      "ds_read_b128 a[0:3], v52 offset:0\n\t"
      "v_mfma_f32_16x16x32_bf16 v[16:19], v[176:179], a[28:31], v[16:19]\n\t"
      "ds_read_b128 a[4:7], v53 offset:0\n\t"
      "v_mfma_f32_16x16x32_bf16 v[16:19], v[192:195], a[32:35], v[16:19]\n\t"
      "ds_read_b128 a[8:11], v54 offset:0\n\t"
      "v_mfma_f32_16x16x32_bf16 v[16:19], v[208:211], a[36:39], v[16:19]\n\t"
      "ds_read_b128 a[12:15], v55 offset:0\n\t"
      "v_mfma_f32_16x16x32_bf16 v[16:19], v[224:227], a[40:43], v[16:19]\n\t"
      "ds_read_b128 a[16:19], v52 offset:256\n\t"
      "v_mfma_f32_16x16x32_bf16 v[16:19], v[240:243], a[44:47], v[16:19]\n\t"
      "ds_read_b128 a[20:23], v53 offset:256\n\t"
      "v_mfma_f32_16x16x32_bf16 v[20:23], v[164:167], a[24:27], v[20:23]\n\t"
      "s_add_u32 s51, s51, 12288\n\t"
      "s_cmp_eq_u32 s51, 160256\n\t"
      "s_cselect_b32 s51, 25088, s51\n\t"
      "v_mfma_f32_16x16x32_bf16 v[20:23], v[180:183], a[28:31], v[20:23]\n\t"
      "s_waitcnt lgkmcnt(6)\n\t"
      "v_mfma_f32_16x16x32_bf16 v[20:23], v[196:199], a[32:35], v[20:23]\n\t"
      "buffer_store_dwordx4 a[56:59], v14, s[44:47], s49 offen offset:2048 sc1\n\t"
      "v_mfma_f32_16x16x32_bf16 v[20:23], v[212:215], a[36:39], v[20:23]\n\t"
      "v_mfma_f32_16x16x32_bf16 v[20:23], v[228:231], a[40:43], v[20:23]\n\t"
      "s_waitcnt lgkmcnt(0)\n\t"
      "s_sub_u32 s53, s36, s38\n\t"
      "s_cmp_lt_i32 s53, 0\n\t"
      "v_mfma_f32_16x16x32_bf16 v[20:23], v[244:247], a[44:47], v[20:23]\n\t"
      "s_cbranch_scc1 .Lspin0_%=\n\t"
      ".Lspun0_%=:\n\t"
      "s_sub_i32 s53, s53, 30\n\t"
      "s_max_i32 s53, s53, 0\n\t"
      "v_mfma_f32_16x16x32_bf16 v[24:27], v[168:171], a[24:27], v[24:27]\n\t"
      "s_add_u32 s38, s38, s53\n\t"
      "s_add_u32 s38, s38, s39\n\t"
      "s_add_u32 s28, s28, s53\n\t"
      "v_mfma_f32_16x16x32_bf16 v[24:27], v[184:187], a[28:31], v[24:27]\n\t"
      "v_cvt_pk_bf16_f32 v40, v16, v17\n\t"
      "v_cvt_pk_bf16_f32 v41, v18, v19\n\t"
      "v_mfma_f32_16x16x32_bf16 v[24:27], v[200:203], a[32:35], v[24:27]\n\t"
      "ds_write_b64 v0, v[40:41] offset:0\n\t"
      "v_mfma_f32_16x16x32_bf16 v[24:27], v[216:219], a[36:39], v[24:27]\n\t"
      "buffer_load_dwordx4 v3, s[40:43], s48 offen offset:1024 nt lds\n\t"
      "v_mfma_f32_16x16x32_bf16 v[24:27], v[232:235], a[40:43], v[24:27]\n\t"
      "s_add_u32 s58, s58, 786432\n\t"
      "s_lshr_b32 s59, s59, 1\n\t"
      "v_mfma_f32_16x16x32_bf16 v[24:27], v[248:251], a[44:47], v[24:27]\n\t"
      "s_bitcmp1_b32 s59, 0\n\t"
      "s_cselect_b32 s49, 24576, 0\n\t"
      "s_add_u32 s49, s49, s58\n\t"
      "v_mfma_f32_16x16x32_bf16 v[28:31], v[172:175], a[24:27], v[28:31]\n\t"
      "v_mfma_f32_16x16x32_bf16 v[28:31], v[188:191], a[28:31], v[28:31]\n\t"
      "v_cvt_pk_bf16_f32 v42, v20, v21\n\t"
      "v_cvt_pk_bf16_f32 v43, v22, v23\n\t"
      "v_mfma_f32_16x16x32_bf16 v[28:31], v[204:207], a[32:35], v[28:31]\n\t"
      "ds_write_b64 v0, v[42:43] offset:32\n\t"
      "v_mfma_f32_16x16x32_bf16 v[28:31], v[220:223], a[36:39], v[28:31]\n\t"
      "v_mfma_f32_16x16x32_bf16 v[28:31], v[236:239], a[40:43], v[28:31]\n\t"
      "v_mfma_f32_16x16x32_bf16 v[28:31], v[252:255], a[44:47], v[28:31]\n\t"
      "v_mfma_f32_16x16x32_bf16 v[32:35], a[108:111], a[24:27], v[32:35]\n\t"
      "v_mfma_f32_16x16x32_bf16 v[32:35], a[116:119], a[28:31], v[32:35]\n\t"
      "v_cvt_pk_bf16_f32 v44, v24, v25\n\t"
      "v_cvt_pk_bf16_f32 v45, v26, v27\n\t"
      "v_mfma_f32_16x16x32_bf16 v[32:35], a[124:127], a[32:35], v[32:35]\n\t"
      "ds_write_b64 v0, v[44:45] offset:64\n\t"
      "v_mfma_f32_16x16x32_bf16 v[32:35], a[132:135], a[36:39], v[32:35]\n\t"
      "buffer_load_dwordx4 v4, s[40:43], s48 offen offset:2048 nt lds\n\t"
      "v_mfma_f32_16x16x32_bf16 v[32:35], a[140:143], a[40:43], v[32:35]\n\t"
      "s_add_u32 s50, s50, 12288\n\t"
      "v_mfma_f32_16x16x32_bf16 v[32:35], a[148:151], a[44:47], v[32:35]\n\t"
      "v_cvt_pk_bf16_f32 v46, v28, v29\n\t"
      "v_cvt_pk_bf16_f32 v47, v30, v31\n\t"
      "v_mfma_f32_16x16x32_bf16 v[36:39], a[112:115], a[24:27], v[36:39]\n\t"
      "ds_write_b64 v0, v[46:47] offset:96\n\t"
      "v_mfma_f32_16x16x32_bf16 v[36:39], a[120:123], a[28:31], v[36:39]\n\t"
      "s_cmp_eq_u32 s50, 160256\n\t"
      "s_cselect_b32 s50, 25088, s50\n\t"
      "v_mfma_f32_16x16x32_bf16 v[36:39], a[128:131], a[32:35], v[36:39]\n\t"
      "v_mfma_f32_16x16x32_bf16 v[36:39], a[136:139], a[36:39], v[36:39]\n\t"
      "v_mfma_f32_16x16x32_bf16 v[36:39], a[144:147], a[40:43], v[36:39]\n\t"
      "v_cvt_pk_bf16_f32 v48, v32, v33\n\t"
      "v_cvt_pk_bf16_f32 v49, v34, v35\n\t"
      "v_mfma_f32_16x16x32_bf16 v[36:39], a[152:155], a[44:47], v[36:39]\n\t"
      "ds_write_b64 v0, v[48:49] offset:128\n\t"
      ".Lmid0_%=:\n\t"
      "v_mfma_f32_16x16x32_bf16 v[16:19], v[64:67], a[0:3], 0\n\t"
      "ds_read_b128 a[24:27], v54 offset:256\n\t"
      "v_mfma_f32_16x16x32_bf16 v[16:19], v[80:83], a[4:7], v[16:19]\n\t"
      "ds_read_b128 a[28:31], v55 offset:256\n\t"
      "v_mfma_f32_16x16x32_bf16 v[16:19], v[96:99], a[8:11], v[16:19]\n\t"
      "ds_read_b128 a[32:35], v52 offset:512\n\t"
      "v_mfma_f32_16x16x32_bf16 v[16:19], v[112:115], a[12:15], v[16:19]\n\t"
      "ds_read_b128 a[36:39], v53 offset:512\n\t"
      "v_mfma_f32_16x16x32_bf16 v[16:19], v[128:131], a[16:19], v[16:19]\n\t"
      "ds_read_b128 a[40:43], v54 offset:512\n\t"
      "v_mfma_f32_16x16x32_bf16 v[16:19], v[144:147], a[20:23], v[16:19]\n\t"
      "ds_read_b128 a[44:47], v55 offset:512\n\t"
      "v_mfma_f32_16x16x32_bf16 v[20:23], v[68:71], a[0:3], 0\n\t"
      "v_cvt_pk_bf16_f32 v50, v36, v37\n\t"
      "v_cvt_pk_bf16_f32 v51, v38, v39\n\t"
      "v_mfma_f32_16x16x32_bf16 v[20:23], v[84:87], a[4:7], v[20:23]\n\t"
      "ds_write_b64 v0, v[50:51] offset:160\n\t"
      "v_mfma_f32_16x16x32_bf16 v[20:23], v[100:103], a[8:11], v[20:23]\n\t"
      "v_add_u32 v56, s51, v15\n\t"
      "v_xor_b32 v57, 64, v56\n\t"
      "v_mfma_f32_16x16x32_bf16 v[20:23], v[116:119], a[12:15], v[20:23]\n\t"
      "v_xor_b32 v58, 128, v56\n\t"
      "v_xor_b32 v59, 192, v56\n\t"
      "v_mfma_f32_16x16x32_bf16 v[20:23], v[132:135], a[16:19], v[20:23]\n\t"
      "v_mfma_f32_16x16x32_bf16 v[20:23], v[148:151], a[20:23], v[20:23]\n\t"
      "v_mfma_f32_16x16x32_bf16 v[24:27], v[72:75], a[0:3], 0\n\t"
      "v_mfma_f32_16x16x32_bf16 v[24:27], v[88:91], a[4:7], v[24:27]\n\t"
      "v_mfma_f32_16x16x32_bf16 v[24:27], v[104:107], a[8:11], v[24:27]\n\t"
      "s_waitcnt vmcnt(12)\n\t"
      "s_waitcnt lgkmcnt(0)\n\t"
      "s_barrier\n\t"
      "v_mfma_f32_16x16x32_bf16 v[24:27], v[120:123], a[12:15], v[24:27]\n\t"
      "v_mfma_f32_16x16x32_bf16 v[24:27], v[136:139], a[16:19], v[24:27]\n\t"
      "v_mfma_f32_16x16x32_bf16 v[24:27], v[152:155], a[20:23], v[24:27]\n\t"
      "v_mfma_f32_16x16x32_bf16 v[28:31], v[76:79], a[0:3], 0\n\t"
      "ds_read_b128 a[48:51], v8 offset:0\n\t"
      "v_mfma_f32_16x16x32_bf16 v[28:31], v[92:95], a[4:7], v[28:31]\n\t"
      "ds_read_b128 a[52:55], v60 offset:1024\n\t"
      "v_mfma_f32_16x16x32_bf16 v[28:31], v[108:111], a[8:11], v[28:31]\n\t"
      "ds_read_b128 a[56:59], v61 offset:2048\n\t"
      "v_mfma_f32_16x16x32_bf16 v[28:31], v[124:127], a[12:15], v[28:31]\n\t"
      "s_and_b32 s39, s34, 15\n\t"
      "s_cselect_b32 s53, s29, 0\n\t"
      "v_mfma_f32_16x16x32_bf16 v[28:31], v[140:143], a[16:19], v[28:31]\n\t"
      "s_add_u32 s39, s39, s53\n\t"
      "s_lshr_b64 s[34:35], s[34:35], 4\n\t"
      "v_mfma_f32_16x16x32_bf16 v[28:31], v[156:159], a[20:23], v[28:31]\n\t"
      "s_cmp_le_u32 s52, 12\n\t"
      "s_cselect_b32 s53, 3, 0\n\t"
      "s_min_u32 s53, s28, s53\n\t"
      "v_mfma_f32_16x16x32_bf16 v[32:35], a[60:63], a[0:3], 0\n\t"
      "s_sub_u32 s28, s28, s53\n\t"
      "s_sub_u32 s39, s39, s53\n\t"
      "v_mfma_f32_16x16x32_bf16 v[32:35], a[68:71], a[4:7], v[32:35]\n\t"
      "s_waitcnt lgkmcnt(2)\n\t"
      "v_mfma_f32_16x16x32_bf16 v[32:35], a[76:79], a[8:11], v[32:35]\n\t"
      "buffer_store_dwordx4 a[48:51], v9, s[44:47], s49 offen sc1\n\t"
      "v_mfma_f32_16x16x32_bf16 v[32:35], a[84:87], a[12:15], v[32:35]\n\t"
      "v_mfma_f32_16x16x32_bf16 v[32:35], a[92:95], a[16:19], v[32:35]\n\t"
      "v_mfma_f32_16x16x32_bf16 v[32:35], a[100:103], a[20:23], v[32:35]\n\t"
      "s_add_u32 m0, s18, s50\n\t"
      "v_mfma_f32_16x16x32_bf16 v[36:39], a[64:67], a[0:3], 0\n\t"
      "buffer_load_dwordx4 v5, s[40:43], s48 offen nt lds\n\t"
      "v_mfma_f32_16x16x32_bf16 v[36:39], a[72:75], a[4:7], v[36:39]\n\t"
      "s_memrealtime s[36:37]\n\t"
      "v_mfma_f32_16x16x32_bf16 v[36:39], a[80:83], a[8:11], v[36:39]\n\t"
      "v_mfma_f32_16x16x32_bf16 v[36:39], a[88:91], a[12:15], v[36:39]\n\t"
      "s_waitcnt lgkmcnt(1)\n\t"
      "v_mfma_f32_16x16x32_bf16 v[36:39], a[96:99], a[16:19], v[36:39]\n\t"
      "buffer_store_dwordx4 a[52:55], v10, s[44:47], s49 offen offset:1024 sc1\n\t"
      "v_mfma_f32_16x16x32_bf16 v[36:39], a[104:107], a[20:23], v[36:39]\n\t"
      "v_mfma_f32_16x16x32_bf16 v[16:19], v[160:163], a[24:27], v[16:19]\n\t"
      "ds_read_b128 a[0:3], v56 offset:0\n\t"
      "v_mfma_f32_16x16x32_bf16 v[16:19], v[176:179], a[28:31], v[16:19]\n\t"
      "ds_read_b128 a[4:7], v57 offset:0\n\t"
      "v_mfma_f32_16x16x32_bf16 v[16:19], v[192:195], a[32:35], v[16:19]\n\t"
      "ds_read_b128 a[8:11], v58 offset:0\n\t"
      "v_mfma_f32_16x16x32_bf16 v[16:19], v[208:211], a[36:39], v[16:19]\n\t"
      "ds_read_b128 a[12:15], v59 offset:0\n\t"
      "v_mfma_f32_16x16x32_bf16 v[16:19], v[224:227], a[40:43], v[16:19]\n\t"
      "ds_read_b128 a[16:19], v56 offset:256\n\t"
      "v_mfma_f32_16x16x32_bf16 v[16:19], v[240:243], a[44:47], v[16:19]\n\t"
      "ds_read_b128 a[20:23], v57 offset:256\n\t"
      "v_mfma_f32_16x16x32_bf16 v[20:23], v[164:167], a[24:27], v[20:23]\n\t"
      "s_add_u32 s51, s51, 12288\n\t"
      "s_cmp_eq_u32 s51, 160256\n\t"
      "s_cselect_b32 s51, 25088, s51\n\t"
      "v_mfma_f32_16x16x32_bf16 v[20:23], v[180:183], a[28:31], v[20:23]\n\t"
      "s_waitcnt lgkmcnt(6)\n\t"
      "v_mfma_f32_16x16x32_bf16 v[20:23], v[196:199], a[32:35], v[20:23]\n\t"
      "buffer_store_dwordx4 a[56:59], v11, s[44:47], s49 offen offset:2048 sc1\n\t"
      "v_mfma_f32_16x16x32_bf16 v[20:23], v[212:215], a[36:39], v[20:23]\n\t"
      "v_mfma_f32_16x16x32_bf16 v[20:23], v[228:231], a[40:43], v[20:23]\n\t"
      "s_waitcnt lgkmcnt(0)\n\t"
      "s_sub_u32 s53, s36, s38\n\t"
      "s_cmp_lt_i32 s53, 0\n\t"
      "v_mfma_f32_16x16x32_bf16 v[20:23], v[244:247], a[44:47], v[20:23]\n\t"
      "s_cbranch_scc1 .Lspin1_%=\n\t"
      ".Lspun1_%=:\n\t"
      "s_sub_i32 s53, s53, 30\n\t"
      "s_max_i32 s53, s53, 0\n\t"
      "v_mfma_f32_16x16x32_bf16 v[24:27], v[168:171], a[24:27], v[24:27]\n\t"
      "s_add_u32 s38, s38, s53\n\t"
      "s_add_u32 s38, s38, s39\n\t"
      "s_add_u32 s28, s28, s53\n\t"
      "v_mfma_f32_16x16x32_bf16 v[24:27], v[184:187], a[28:31], v[24:27]\n\t"
      "v_cvt_pk_bf16_f32 v40, v16, v17\n\t"
      "v_cvt_pk_bf16_f32 v41, v18, v19\n\t"
      "v_mfma_f32_16x16x32_bf16 v[24:27], v[200:203], a[32:35], v[24:27]\n\t"
      "ds_write_b64 v0, v[40:41] offset:12544\n\t"
      "v_mfma_f32_16x16x32_bf16 v[24:27], v[216:219], a[36:39], v[24:27]\n\t"
      "buffer_load_dwordx4 v6, s[40:43], s48 offen offset:1024 nt lds\n\t"
      "v_mfma_f32_16x16x32_bf16 v[24:27], v[232:235], a[40:43], v[24:27]\n\t"
      "v_mfma_f32_16x16x32_bf16 v[24:27], v[248:251], a[44:47], v[24:27]\n\t"
      "v_mfma_f32_16x16x32_bf16 v[28:31], v[172:175], a[24:27], v[28:31]\n\t"
      "v_mfma_f32_16x16x32_bf16 v[28:31], v[188:191], a[28:31], v[28:31]\n\t"
      "v_cvt_pk_bf16_f32 v42, v20, v21\n\t"
      "v_cvt_pk_bf16_f32 v43, v22, v23\n\t"
      "v_mfma_f32_16x16x32_bf16 v[28:31], v[204:207], a[32:35], v[28:31]\n\t"
      "ds_write_b64 v0, v[42:43] offset:12576\n\t"
      "v_mfma_f32_16x16x32_bf16 v[28:31], v[220:223], a[36:39], v[28:31]\n\t"
      "v_mfma_f32_16x16x32_bf16 v[28:31], v[236:239], a[40:43], v[28:31]\n\t"
      "v_mfma_f32_16x16x32_bf16 v[28:31], v[252:255], a[44:47], v[28:31]\n\t"
      "v_mfma_f32_16x16x32_bf16 v[32:35], a[108:111], a[24:27], v[32:35]\n\t"
      "v_mfma_f32_16x16x32_bf16 v[32:35], a[116:119], a[28:31], v[32:35]\n\t"
      "v_cvt_pk_bf16_f32 v44, v24, v25\n\t"
      "v_cvt_pk_bf16_f32 v45, v26, v27\n\t"
      "v_mfma_f32_16x16x32_bf16 v[32:35], a[124:127], a[32:35], v[32:35]\n\t"
      "ds_write_b64 v0, v[44:45] offset:12608\n\t"
      "v_mfma_f32_16x16x32_bf16 v[32:35], a[132:135], a[36:39], v[32:35]\n\t"
      "buffer_load_dwordx4 v7, s[40:43], s48 offen offset:2048 nt lds\n\t"
      "v_mfma_f32_16x16x32_bf16 v[32:35], a[140:143], a[40:43], v[32:35]\n\t"
      "s_add_u32 s50, s50, 12288\n\t"
      "v_mfma_f32_16x16x32_bf16 v[32:35], a[148:151], a[44:47], v[32:35]\n\t"
      "v_cvt_pk_bf16_f32 v46, v28, v29\n\t"
      "v_cvt_pk_bf16_f32 v47, v30, v31\n\t"
      "v_mfma_f32_16x16x32_bf16 v[36:39], a[112:115], a[24:27], v[36:39]\n\t"
      "ds_write_b64 v0, v[46:47] offset:12640\n\t"
      "v_mfma_f32_16x16x32_bf16 v[36:39], a[120:123], a[28:31], v[36:39]\n\t"
      "s_cmp_eq_u32 s50, 160256\n\t"
      "s_cselect_b32 s50, 25088, s50\n\t"
      "v_mfma_f32_16x16x32_bf16 v[36:39], a[128:131], a[32:35], v[36:39]\n\t"
      "s_sub_u32 s52, s52, 1\n\t"
      "s_cmp_lg_u32 s52, 0\n\t"
      "v_mfma_f32_16x16x32_bf16 v[36:39], a[136:139], a[36:39], v[36:39]\n\t"
      "v_mfma_f32_16x16x32_bf16 v[36:39], a[144:147], a[40:43], v[36:39]\n\t"
      "v_cvt_pk_bf16_f32 v48, v32, v33\n\t"
      "v_cvt_pk_bf16_f32 v49, v34, v35\n\t"
      "v_mfma_f32_16x16x32_bf16 v[36:39], a[152:155], a[44:47], v[36:39]\n\t"
      "ds_write_b64 v0, v[48:49] offset:12672\n\t"
      "s_cbranch_scc1 .Lloop0_%=\n\t"
      "s_branch .Lendc_%=\n\t"
      ".Lloop1_%=:\n\t"
      "v_mfma_f32_16x16x32_bf16 v[16:19], v[64:67], a[0:3], 0\n\t"
      "ds_read_b128 a[24:27], v58 offset:256\n\t"
      "v_mfma_f32_16x16x32_bf16 v[16:19], v[80:83], a[4:7], v[16:19]\n\t"
      "ds_read_b128 a[28:31], v59 offset:256\n\t"
      "v_mfma_f32_16x16x32_bf16 v[16:19], v[96:99], a[8:11], v[16:19]\n\t"
      "ds_read_b128 a[32:35], v56 offset:512\n\t"
      "v_mfma_f32_16x16x32_bf16 v[16:19], v[112:115], a[12:15], v[16:19]\n\t"
      "ds_read_b128 a[36:39], v57 offset:512\n\t"
      "v_mfma_f32_16x16x32_bf16 v[16:19], v[128:131], a[16:19], v[16:19]\n\t"
      "ds_read_b128 a[40:43], v58 offset:512\n\t"
      "v_mfma_f32_16x16x32_bf16 v[16:19], v[144:147], a[20:23], v[16:19]\n\t"
      "ds_read_b128 a[44:47], v59 offset:512\n\t"
      "v_mfma_f32_16x16x32_bf16 v[20:23], v[68:71], a[0:3], 0\n\t"
      "v_cvt_pk_bf16_f32 v50, v36, v37\n\t"
      "v_cvt_pk_bf16_f32 v51, v38, v39\n\t"
      "v_mfma_f32_16x16x32_bf16 v[20:23], v[84:87], a[4:7], v[20:23]\n\t"
      "ds_write_b64 v0, v[50:51] offset:12704\n\t"
      "v_mfma_f32_16x16x32_bf16 v[20:23], v[100:103], a[8:11], v[20:23]\n\t"
      "v_add_u32 v52, s51, v15\n\t"
      "v_xor_b32 v53, 64, v52\n\t"
      "v_mfma_f32_16x16x32_bf16 v[20:23], v[116:119], a[12:15], v[20:23]\n\t"
      "v_xor_b32 v54, 128, v52\n\t"
      "v_xor_b32 v55, 192, v52\n\t"
      "v_mfma_f32_16x16x32_bf16 v[20:23], v[132:135], a[16:19], v[20:23]\n\t"
      "s_add_u32 s56, s56, 786432\n\t"
      "s_lshr_b32 s57, s57, 1\n\t"
      "v_mfma_f32_16x16x32_bf16 v[20:23], v[148:151], a[20:23], v[20:23]\n\t"
      "s_bitcmp1_b32 s57, 0\n\t"
      "s_cselect_b32 s48, 24576, 0\n\t"
      "s_add_u32 s48, s48, s56\n\t"
      "v_mfma_f32_16x16x32_bf16 v[24:27], v[72:75], a[0:3], 0\n\t"
      "v_mfma_f32_16x16x32_bf16 v[24:27], v[88:91], a[4:7], v[24:27]\n\t"
      "v_mfma_f32_16x16x32_bf16 v[24:27], v[104:107], a[8:11], v[24:27]\n\t"
      "v_mfma_f32_16x16x32_bf16 v[24:27], v[120:123], a[12:15], v[24:27]\n\t"
      "s_waitcnt vmcnt(12)\n\t"
      "s_waitcnt lgkmcnt(0)\n\t"
      "s_barrier\n\t"
      "v_mfma_f32_16x16x32_bf16 v[24:27], v[136:139], a[16:19], v[24:27]\n\t"
      "v_mfma_f32_16x16x32_bf16 v[24:27], v[152:155], a[20:23], v[24:27]\n\t"
      "v_mfma_f32_16x16x32_bf16 v[28:31], v[76:79], a[0:3], 0\n\t"
      "ds_read_b128 a[48:51], v8 offset:12544\n\t"
      "v_mfma_f32_16x16x32_bf16 v[28:31], v[92:95], a[4:7], v[28:31]\n\t"
      "ds_read_b128 a[52:55], v60 offset:13568\n\t"
      "v_mfma_f32_16x16x32_bf16 v[28:31], v[108:111], a[8:11], v[28:31]\n\t"
      "ds_read_b128 a[56:59], v61 offset:14592\n\t"
      "v_mfma_f32_16x16x32_bf16 v[28:31], v[124:127], a[12:15], v[28:31]\n\t"
      "v_mfma_f32_16x16x32_bf16 v[28:31], v[140:143], a[16:19], v[28:31]\n\t"
      "v_mfma_f32_16x16x32_bf16 v[28:31], v[156:159], a[20:23], v[28:31]\n\t"
      "v_mfma_f32_16x16x32_bf16 v[32:35], a[60:63], a[0:3], 0\n\t"
      "v_mfma_f32_16x16x32_bf16 v[32:35], a[68:71], a[4:7], v[32:35]\n\t"
      "s_waitcnt lgkmcnt(2)\n\t"
      "v_mfma_f32_16x16x32_bf16 v[32:35], a[76:79], a[8:11], v[32:35]\n\t"
      "buffer_store_dwordx4 a[48:51], v12, s[44:47], s49 offen sc1\n\t"
      "v_mfma_f32_16x16x32_bf16 v[32:35], a[84:87], a[12:15], v[32:35]\n\t"
      "v_mfma_f32_16x16x32_bf16 v[32:35], a[92:95], a[16:19], v[32:35]\n\t"
      "v_mfma_f32_16x16x32_bf16 v[32:35], a[100:103], a[20:23], v[32:35]\n\t"
      "s_add_u32 m0, s18, s50\n\t"
      "v_mfma_f32_16x16x32_bf16 v[36:39], a[64:67], a[0:3], 0\n\t"
      "buffer_load_dwordx4 v2, s[40:43], s48 offen nt lds\n\t"
      "v_mfma_f32_16x16x32_bf16 v[36:39], a[72:75], a[4:7], v[36:39]\n\t"
      "v_mfma_f32_16x16x32_bf16 v[36:39], a[80:83], a[8:11], v[36:39]\n\t"
      "v_mfma_f32_16x16x32_bf16 v[36:39], a[88:91], a[12:15], v[36:39]\n\t"
      "s_waitcnt lgkmcnt(1)\n\t"
      "v_mfma_f32_16x16x32_bf16 v[36:39], a[96:99], a[16:19], v[36:39]\n\t"
      "buffer_store_dwordx4 a[52:55], v13, s[44:47], s49 offen offset:1024 sc1\n\t"
      "v_mfma_f32_16x16x32_bf16 v[36:39], a[104:107], a[20:23], v[36:39]\n\t"
      "v_mfma_f32_16x16x32_bf16 v[16:19], v[160:163], a[24:27], v[16:19]\n\t"
      "ds_read_b128 a[0:3], v52 offset:0\n\t"
      "v_mfma_f32_16x16x32_bf16 v[16:19], v[176:179], a[28:31], v[16:19]\n\t"
      "ds_read_b128 a[4:7], v53 offset:0\n\t"
      "v_mfma_f32_16x16x32_bf16 v[16:19], v[192:195], a[32:35], v[16:19]\n\t"
      "ds_read_b128 a[8:11], v54 offset:0\n\t"
      "v_mfma_f32_16x16x32_bf16 v[16:19], v[208:211], a[36:39], v[16:19]\n\t"
      "ds_read_b128 a[12:15], v55 offset:0\n\t"
      "v_mfma_f32_16x16x32_bf16 v[16:19], v[224:227], a[40:43], v[16:19]\n\t"
      "ds_read_b128 a[16:19], v52 offset:256\n\t"
      "v_mfma_f32_16x16x32_bf16 v[16:19], v[240:243], a[44:47], v[16:19]\n\t"
      "ds_read_b128 a[20:23], v53 offset:256\n\t"
      "v_mfma_f32_16x16x32_bf16 v[20:23], v[164:167], a[24:27], v[20:23]\n\t"
      "s_add_u32 s51, s51, 12288\n\t"
      "s_cmp_eq_u32 s51, 160256\n\t"
      "s_cselect_b32 s51, 25088, s51\n\t"
      "v_mfma_f32_16x16x32_bf16 v[20:23], v[180:183], a[28:31], v[20:23]\n\t"
      "s_waitcnt lgkmcnt(6)\n\t"
      "v_mfma_f32_16x16x32_bf16 v[20:23], v[196:199], a[32:35], v[20:23]\n\t"
      "buffer_store_dwordx4 a[56:59], v14, s[44:47], s49 offen offset:2048 sc1\n\t"
      "v_mfma_f32_16x16x32_bf16 v[20:23], v[212:215], a[36:39], v[20:23]\n\t"
      "v_mfma_f32_16x16x32_bf16 v[20:23], v[228:231], a[40:43], v[20:23]\n\t"
      "s_waitcnt lgkmcnt(0)\n\t"
      "v_mfma_f32_16x16x32_bf16 v[20:23], v[244:247], a[44:47], v[20:23]\n\t"
      "v_mfma_f32_16x16x32_bf16 v[24:27], v[168:171], a[24:27], v[24:27]\n\t"
      "v_mfma_f32_16x16x32_bf16 v[24:27], v[184:187], a[28:31], v[24:27]\n\t"
      "v_cvt_pk_bf16_f32 v40, v16, v17\n\t"
      "v_cvt_pk_bf16_f32 v41, v18, v19\n\t"
      "v_mfma_f32_16x16x32_bf16 v[24:27], v[200:203], a[32:35], v[24:27]\n\t"
      "ds_write_b64 v0, v[40:41] offset:0\n\t"
      "v_mfma_f32_16x16x32_bf16 v[24:27], v[216:219], a[36:39], v[24:27]\n\t"
      "buffer_load_dwordx4 v3, s[40:43], s48 offen offset:1024 nt lds\n\t"
      "v_mfma_f32_16x16x32_bf16 v[24:27], v[232:235], a[40:43], v[24:27]\n\t"
      "s_add_u32 s58, s58, 786432\n\t"
      "s_lshr_b32 s59, s59, 1\n\t"
      "v_mfma_f32_16x16x32_bf16 v[24:27], v[248:251], a[44:47], v[24:27]\n\t"
      "s_bitcmp1_b32 s59, 0\n\t"
      "s_cselect_b32 s49, 24576, 0\n\t"
      "s_add_u32 s49, s49, s58\n\t"
      "v_mfma_f32_16x16x32_bf16 v[28:31], v[172:175], a[24:27], v[28:31]\n\t"
      "v_mfma_f32_16x16x32_bf16 v[28:31], v[188:191], a[28:31], v[28:31]\n\t"
      "v_cvt_pk_bf16_f32 v42, v20, v21\n\t"
      "v_cvt_pk_bf16_f32 v43, v22, v23\n\t"
      "v_mfma_f32_16x16x32_bf16 v[28:31], v[204:207], a[32:35], v[28:31]\n\t"
      "ds_write_b64 v0, v[42:43] offset:32\n\t"
      "v_mfma_f32_16x16x32_bf16 v[28:31], v[220:223], a[36:39], v[28:31]\n\t"
      "v_mfma_f32_16x16x32_bf16 v[28:31], v[236:239], a[40:43], v[28:31]\n\t"
      "v_mfma_f32_16x16x32_bf16 v[28:31], v[252:255], a[44:47], v[28:31]\n\t"
      "v_mfma_f32_16x16x32_bf16 v[32:35], a[108:111], a[24:27], v[32:35]\n\t"
      "v_mfma_f32_16x16x32_bf16 v[32:35], a[116:119], a[28:31], v[32:35]\n\t"
      "v_cvt_pk_bf16_f32 v44, v24, v25\n\t"
      "v_cvt_pk_bf16_f32 v45, v26, v27\n\t"
      "v_mfma_f32_16x16x32_bf16 v[32:35], a[124:127], a[32:35], v[32:35]\n\t"
      "ds_write_b64 v0, v[44:45] offset:64\n\t"
      "v_mfma_f32_16x16x32_bf16 v[32:35], a[132:135], a[36:39], v[32:35]\n\t"
      "buffer_load_dwordx4 v4, s[40:43], s48 offen offset:2048 nt lds\n\t"
      "v_mfma_f32_16x16x32_bf16 v[32:35], a[140:143], a[40:43], v[32:35]\n\t"
      "s_add_u32 s50, s50, 12288\n\t"
      "v_mfma_f32_16x16x32_bf16 v[32:35], a[148:151], a[44:47], v[32:35]\n\t"
      "v_cvt_pk_bf16_f32 v46, v28, v29\n\t"
      "v_cvt_pk_bf16_f32 v47, v30, v31\n\t"
      "v_mfma_f32_16x16x32_bf16 v[36:39], a[112:115], a[24:27], v[36:39]\n\t"
      "ds_write_b64 v0, v[46:47] offset:96\n\t"
      "v_mfma_f32_16x16x32_bf16 v[36:39], a[120:123], a[28:31], v[36:39]\n\t"
      "s_cmp_eq_u32 s50, 160256\n\t"
      "s_cselect_b32 s50, 25088, s50\n\t"
      "v_mfma_f32_16x16x32_bf16 v[36:39], a[128:131], a[32:35], v[36:39]\n\t"
      "v_mfma_f32_16x16x32_bf16 v[36:39], a[136:139], a[36:39], v[36:39]\n\t"
      "v_mfma_f32_16x16x32_bf16 v[36:39], a[144:147], a[40:43], v[36:39]\n\t"
      "v_cvt_pk_bf16_f32 v48, v32, v33\n\t"
      "v_cvt_pk_bf16_f32 v49, v34, v35\n\t"
      "v_mfma_f32_16x16x32_bf16 v[36:39], a[152:155], a[44:47], v[36:39]\n\t"
      "ds_write_b64 v0, v[48:49] offset:128\n\t"
      ".Lmid1_%=:\n\t"
      "v_mfma_f32_16x16x32_bf16 v[16:19], v[64:67], a[0:3], 0\n\t"
      "ds_read_b128 a[24:27], v54 offset:256\n\t"
      "v_mfma_f32_16x16x32_bf16 v[16:19], v[80:83], a[4:7], v[16:19]\n\t"
      "ds_read_b128 a[28:31], v55 offset:256\n\t"
      "v_mfma_f32_16x16x32_bf16 v[16:19], v[96:99], a[8:11], v[16:19]\n\t"
      "ds_read_b128 a[32:35], v52 offset:512\n\t"
      "v_mfma_f32_16x16x32_bf16 v[16:19], v[112:115], a[12:15], v[16:19]\n\t"
      "ds_read_b128 a[36:39], v53 offset:512\n\t"
      "v_mfma_f32_16x16x32_bf16 v[16:19], v[128:131], a[16:19], v[16:19]\n\t"
      "ds_read_b128 a[40:43], v54 offset:512\n\t"
      "v_mfma_f32_16x16x32_bf16 v[16:19], v[144:147], a[20:23], v[16:19]\n\t"
      "ds_read_b128 a[44:47], v55 offset:512\n\t"
      "v_mfma_f32_16x16x32_bf16 v[20:23], v[68:71], a[0:3], 0\n\t"
      "v_cvt_pk_bf16_f32 v50, v36, v37\n\t"
      "v_cvt_pk_bf16_f32 v51, v38, v39\n\t"
      "v_mfma_f32_16x16x32_bf16 v[20:23], v[84:87], a[4:7], v[20:23]\n\t"
      "ds_write_b64 v0, v[50:51] offset:160\n\t"
      "v_mfma_f32_16x16x32_bf16 v[20:23], v[100:103], a[8:11], v[20:23]\n\t"
      "v_add_u32 v56, s51, v15\n\t"
      "v_xor_b32 v57, 64, v56\n\t"
      "v_mfma_f32_16x16x32_bf16 v[20:23], v[116:119], a[12:15], v[20:23]\n\t"
      "v_xor_b32 v58, 128, v56\n\t"
      "v_xor_b32 v59, 192, v56\n\t"
      "v_mfma_f32_16x16x32_bf16 v[20:23], v[132:135], a[16:19], v[20:23]\n\t"
      "v_mfma_f32_16x16x32_bf16 v[20:23], v[148:151], a[20:23], v[20:23]\n\t"
      "v_mfma_f32_16x16x32_bf16 v[24:27], v[72:75], a[0:3], 0\n\t"
      "v_mfma_f32_16x16x32_bf16 v[24:27], v[88:91], a[4:7], v[24:27]\n\t"
      "v_mfma_f32_16x16x32_bf16 v[24:27], v[104:107], a[8:11], v[24:27]\n\t"
      "v_mfma_f32_16x16x32_bf16 v[24:27], v[120:123], a[12:15], v[24:27]\n\t"
      "s_waitcnt vmcnt(12)\n\t"
      "s_waitcnt lgkmcnt(0)\n\t"
      "s_barrier\n\t"
      "v_mfma_f32_16x16x32_bf16 v[24:27], v[136:139], a[16:19], v[24:27]\n\t"
      "v_mfma_f32_16x16x32_bf16 v[24:27], v[152:155], a[20:23], v[24:27]\n\t"
      "v_mfma_f32_16x16x32_bf16 v[28:31], v[76:79], a[0:3], 0\n\t"
      "ds_read_b128 a[48:51], v8 offset:0\n\t"
      "v_mfma_f32_16x16x32_bf16 v[28:31], v[92:95], a[4:7], v[28:31]\n\t"
      "ds_read_b128 a[52:55], v60 offset:1024\n\t"
      "v_mfma_f32_16x16x32_bf16 v[28:31], v[108:111], a[8:11], v[28:31]\n\t"
      "ds_read_b128 a[56:59], v61 offset:2048\n\t"
      "v_mfma_f32_16x16x32_bf16 v[28:31], v[124:127], a[12:15], v[28:31]\n\t"
      "v_mfma_f32_16x16x32_bf16 v[28:31], v[140:143], a[16:19], v[28:31]\n\t"
      "v_mfma_f32_16x16x32_bf16 v[28:31], v[156:159], a[20:23], v[28:31]\n\t"
      "v_mfma_f32_16x16x32_bf16 v[32:35], a[60:63], a[0:3], 0\n\t"
      "v_mfma_f32_16x16x32_bf16 v[32:35], a[68:71], a[4:7], v[32:35]\n\t"
      "s_waitcnt lgkmcnt(2)\n\t"
      "v_mfma_f32_16x16x32_bf16 v[32:35], a[76:79], a[8:11], v[32:35]\n\t"
      "buffer_store_dwordx4 a[48:51], v9, s[44:47], s49 offen sc1\n\t"
      "v_mfma_f32_16x16x32_bf16 v[32:35], a[84:87], a[12:15], v[32:35]\n\t"
      "v_mfma_f32_16x16x32_bf16 v[32:35], a[92:95], a[16:19], v[32:35]\n\t"
      "v_mfma_f32_16x16x32_bf16 v[32:35], a[100:103], a[20:23], v[32:35]\n\t"
      "s_add_u32 m0, s18, s50\n\t"
      "v_mfma_f32_16x16x32_bf16 v[36:39], a[64:67], a[0:3], 0\n\t"
      "buffer_load_dwordx4 v5, s[40:43], s48 offen nt lds\n\t"
      "v_mfma_f32_16x16x32_bf16 v[36:39], a[72:75], a[4:7], v[36:39]\n\t"
      "v_mfma_f32_16x16x32_bf16 v[36:39], a[80:83], a[8:11], v[36:39]\n\t"
      "v_mfma_f32_16x16x32_bf16 v[36:39], a[88:91], a[12:15], v[36:39]\n\t"
      "s_waitcnt lgkmcnt(1)\n\t"
      "v_mfma_f32_16x16x32_bf16 v[36:39], a[96:99], a[16:19], v[36:39]\n\t"
      "buffer_store_dwordx4 a[52:55], v10, s[44:47], s49 offen offset:1024 sc1\n\t"
      "v_mfma_f32_16x16x32_bf16 v[36:39], a[104:107], a[20:23], v[36:39]\n\t"
      "v_mfma_f32_16x16x32_bf16 v[16:19], v[160:163], a[24:27], v[16:19]\n\t"
      "ds_read_b128 a[0:3], v56 offset:0\n\t"
      "v_mfma_f32_16x16x32_bf16 v[16:19], v[176:179], a[28:31], v[16:19]\n\t"
      "ds_read_b128 a[4:7], v57 offset:0\n\t"
      "v_mfma_f32_16x16x32_bf16 v[16:19], v[192:195], a[32:35], v[16:19]\n\t"
      "ds_read_b128 a[8:11], v58 offset:0\n\t"
      "v_mfma_f32_16x16x32_bf16 v[16:19], v[208:211], a[36:39], v[16:19]\n\t"
      "ds_read_b128 a[12:15], v59 offset:0\n\t"
      "v_mfma_f32_16x16x32_bf16 v[16:19], v[224:227], a[40:43], v[16:19]\n\t"
      "ds_read_b128 a[16:19], v56 offset:256\n\t"
      "v_mfma_f32_16x16x32_bf16 v[16:19], v[240:243], a[44:47], v[16:19]\n\t"
      "ds_read_b128 a[20:23], v57 offset:256\n\t"
      "v_mfma_f32_16x16x32_bf16 v[20:23], v[164:167], a[24:27], v[20:23]\n\t"
      "s_add_u32 s51, s51, 12288\n\t"
      "s_cmp_eq_u32 s51, 160256\n\t"
      "s_cselect_b32 s51, 25088, s51\n\t"
      "v_mfma_f32_16x16x32_bf16 v[20:23], v[180:183], a[28:31], v[20:23]\n\t"
      "s_waitcnt lgkmcnt(6)\n\t"
      "v_mfma_f32_16x16x32_bf16 v[20:23], v[196:199], a[32:35], v[20:23]\n\t"
      "buffer_store_dwordx4 a[56:59], v11, s[44:47], s49 offen offset:2048 sc1\n\t"
      "v_mfma_f32_16x16x32_bf16 v[20:23], v[212:215], a[36:39], v[20:23]\n\t"
      "v_mfma_f32_16x16x32_bf16 v[20:23], v[228:231], a[40:43], v[20:23]\n\t"
      "s_waitcnt lgkmcnt(0)\n\t"
      "v_mfma_f32_16x16x32_bf16 v[20:23], v[244:247], a[44:47], v[20:23]\n\t"
      "v_mfma_f32_16x16x32_bf16 v[24:27], v[168:171], a[24:27], v[24:27]\n\t"
      "v_mfma_f32_16x16x32_bf16 v[24:27], v[184:187], a[28:31], v[24:27]\n\t"
      "v_cvt_pk_bf16_f32 v40, v16, v17\n\t"
      "v_cvt_pk_bf16_f32 v41, v18, v19\n\t"
      "v_mfma_f32_16x16x32_bf16 v[24:27], v[200:203], a[32:35], v[24:27]\n\t"
      "ds_write_b64 v0, v[40:41] offset:12544\n\t"
      "v_mfma_f32_16x16x32_bf16 v[24:27], v[216:219], a[36:39], v[24:27]\n\t"
      "buffer_load_dwordx4 v6, s[40:43], s48 offen offset:1024 nt lds\n\t"
      "v_mfma_f32_16x16x32_bf16 v[24:27], v[232:235], a[40:43], v[24:27]\n\t"
      "v_mfma_f32_16x16x32_bf16 v[24:27], v[248:251], a[44:47], v[24:27]\n\t"
      "v_mfma_f32_16x16x32_bf16 v[28:31], v[172:175], a[24:27], v[28:31]\n\t"
      "v_mfma_f32_16x16x32_bf16 v[28:31], v[188:191], a[28:31], v[28:31]\n\t"
      "v_cvt_pk_bf16_f32 v42, v20, v21\n\t"
      "v_cvt_pk_bf16_f32 v43, v22, v23\n\t"
      "v_mfma_f32_16x16x32_bf16 v[28:31], v[204:207], a[32:35], v[28:31]\n\t"
      "ds_write_b64 v0, v[42:43] offset:12576\n\t"
      "v_mfma_f32_16x16x32_bf16 v[28:31], v[220:223], a[36:39], v[28:31]\n\t"
      "v_mfma_f32_16x16x32_bf16 v[28:31], v[236:239], a[40:43], v[28:31]\n\t"
      "v_mfma_f32_16x16x32_bf16 v[28:31], v[252:255], a[44:47], v[28:31]\n\t"
      "v_mfma_f32_16x16x32_bf16 v[32:35], a[108:111], a[24:27], v[32:35]\n\t"
      "v_mfma_f32_16x16x32_bf16 v[32:35], a[116:119], a[28:31], v[32:35]\n\t"
      "v_cvt_pk_bf16_f32 v44, v24, v25\n\t"
      "v_cvt_pk_bf16_f32 v45, v26, v27\n\t"
      "v_mfma_f32_16x16x32_bf16 v[32:35], a[124:127], a[32:35], v[32:35]\n\t"
      "ds_write_b64 v0, v[44:45] offset:12608\n\t"
      "v_mfma_f32_16x16x32_bf16 v[32:35], a[132:135], a[36:39], v[32:35]\n\t"
      "buffer_load_dwordx4 v7, s[40:43], s48 offen offset:2048 nt lds\n\t"
      "v_mfma_f32_16x16x32_bf16 v[32:35], a[140:143], a[40:43], v[32:35]\n\t"
      "s_add_u32 s50, s50, 12288\n\t"
      "v_mfma_f32_16x16x32_bf16 v[32:35], a[148:151], a[44:47], v[32:35]\n\t"
      "v_cvt_pk_bf16_f32 v46, v28, v29\n\t"
      "v_cvt_pk_bf16_f32 v47, v30, v31\n\t"
      "v_mfma_f32_16x16x32_bf16 v[36:39], a[112:115], a[24:27], v[36:39]\n\t"
      "ds_write_b64 v0, v[46:47] offset:12640\n\t"
      "v_mfma_f32_16x16x32_bf16 v[36:39], a[120:123], a[28:31], v[36:39]\n\t"
      "s_cmp_eq_u32 s50, 160256\n\t"
      "s_cselect_b32 s50, 25088, s50\n\t"
      "v_mfma_f32_16x16x32_bf16 v[36:39], a[128:131], a[32:35], v[36:39]\n\t"
      "s_sub_u32 s52, s52, 1\n\t"
      "s_cmp_lg_u32 s52, 0\n\t"
      "v_mfma_f32_16x16x32_bf16 v[36:39], a[136:139], a[36:39], v[36:39]\n\t"
      "v_mfma_f32_16x16x32_bf16 v[36:39], a[144:147], a[40:43], v[36:39]\n\t"
      "v_cvt_pk_bf16_f32 v48, v32, v33\n\t"
      "v_cvt_pk_bf16_f32 v49, v34, v35\n\t"
      "v_mfma_f32_16x16x32_bf16 v[36:39], a[152:155], a[44:47], v[36:39]\n\t"
      "ds_write_b64 v0, v[48:49] offset:12672\n\t"
      "s_cbranch_scc1 .Lloop1_%=\n\t"
      "s_branch .Lendc_%=\n\t"
      ".Lloop2_%=:\n\t"
      "v_mfma_f32_16x16x32_bf16 v[16:19], v[64:67], a[0:3], 0\n\t"
      "ds_read_b128 a[24:27], v58 offset:256\n\t"
      "v_mfma_f32_16x16x32_bf16 v[16:19], v[80:83], a[4:7], v[16:19]\n\t"
      "ds_read_b128 a[28:31], v59 offset:256\n\t"
      "v_mfma_f32_16x16x32_bf16 v[16:19], v[96:99], a[8:11], v[16:19]\n\t"
      "ds_read_b128 a[32:35], v56 offset:512\n\t"
      "v_mfma_f32_16x16x32_bf16 v[16:19], v[112:115], a[12:15], v[16:19]\n\t"
      "ds_read_b128 a[36:39], v57 offset:512\n\t"
      "v_mfma_f32_16x16x32_bf16 v[16:19], v[128:131], a[16:19], v[16:19]\n\t"
      "ds_read_b128 a[40:43], v58 offset:512\n\t"
      "v_mfma_f32_16x16x32_bf16 v[16:19], v[144:147], a[20:23], v[16:19]\n\t"
      "ds_read_b128 a[44:47], v59 offset:512\n\t"
      "v_mfma_f32_16x16x32_bf16 v[20:23], v[68:71], a[0:3], 0\n\t"
      "v_cvt_pk_bf16_f32 v50, v36, v37\n\t"
      "v_cvt_pk_bf16_f32 v51, v38, v39\n\t"
      "v_mfma_f32_16x16x32_bf16 v[20:23], v[84:87], a[4:7], v[20:23]\n\t"
      "ds_write_b64 v0, v[50:51] offset:12704\n\t"
      "v_mfma_f32_16x16x32_bf16 v[20:23], v[100:103], a[8:11], v[20:23]\n\t"
      "v_add_u32 v52, s51, v15\n\t"
      "v_xor_b32 v53, 64, v52\n\t"
      "v_mfma_f32_16x16x32_bf16 v[20:23], v[116:119], a[12:15], v[20:23]\n\t"
      "v_xor_b32 v54, 128, v52\n\t"
      "v_xor_b32 v55, 192, v52\n\t"
      "v_mfma_f32_16x16x32_bf16 v[20:23], v[132:135], a[16:19], v[20:23]\n\t"
      "s_add_u32 s56, s56, 786432\n\t"
      "s_lshr_b32 s57, s57, 1\n\t"
      "v_mfma_f32_16x16x32_bf16 v[20:23], v[148:151], a[20:23], v[20:23]\n\t"
      "s_bitcmp1_b32 s57, 0\n\t"
      "s_cselect_b32 s48, 24576, 0\n\t"
      "s_add_u32 s48, s48, s56\n\t"
      "v_mfma_f32_16x16x32_bf16 v[24:27], v[72:75], a[0:3], 0\n\t"
      "v_mfma_f32_16x16x32_bf16 v[24:27], v[88:91], a[4:7], v[24:27]\n\t"
      "v_mfma_f32_16x16x32_bf16 v[24:27], v[104:107], a[8:11], v[24:27]\n\t"
      "v_mfma_f32_16x16x32_bf16 v[24:27], v[120:123], a[12:15], v[24:27]\n\t"
      "v_mfma_f32_16x16x32_bf16 v[24:27], v[136:139], a[16:19], v[24:27]\n\t"
      "s_waitcnt vmcnt(12)\n\t"
      "s_waitcnt lgkmcnt(0)\n\t"
      "s_barrier\n\t"
      "v_mfma_f32_16x16x32_bf16 v[24:27], v[152:155], a[20:23], v[24:27]\n\t"
      "v_mfma_f32_16x16x32_bf16 v[28:31], v[76:79], a[0:3], 0\n\t"
      "ds_read_b128 a[48:51], v8 offset:12544\n\t"
      "v_mfma_f32_16x16x32_bf16 v[28:31], v[92:95], a[4:7], v[28:31]\n\t"
      "ds_read_b128 a[52:55], v60 offset:13568\n\t"
      "v_mfma_f32_16x16x32_bf16 v[28:31], v[108:111], a[8:11], v[28:31]\n\t"
      "ds_read_b128 a[56:59], v61 offset:14592\n\t"
      "v_mfma_f32_16x16x32_bf16 v[28:31], v[124:127], a[12:15], v[28:31]\n\t"
      "v_mfma_f32_16x16x32_bf16 v[28:31], v[140:143], a[16:19], v[28:31]\n\t"
      "v_mfma_f32_16x16x32_bf16 v[28:31], v[156:159], a[20:23], v[28:31]\n\t"
      "v_mfma_f32_16x16x32_bf16 v[32:35], a[60:63], a[0:3], 0\n\t"
      "v_mfma_f32_16x16x32_bf16 v[32:35], a[68:71], a[4:7], v[32:35]\n\t"
      "s_waitcnt lgkmcnt(2)\n\t"
      "v_mfma_f32_16x16x32_bf16 v[32:35], a[76:79], a[8:11], v[32:35]\n\t"
      "buffer_store_dwordx4 a[48:51], v12, s[44:47], s49 offen sc1\n\t"
      "v_mfma_f32_16x16x32_bf16 v[32:35], a[84:87], a[12:15], v[32:35]\n\t"
      "v_mfma_f32_16x16x32_bf16 v[32:35], a[92:95], a[16:19], v[32:35]\n\t"
      "v_mfma_f32_16x16x32_bf16 v[32:35], a[100:103], a[20:23], v[32:35]\n\t"
      "s_add_u32 m0, s18, s50\n\t"
      "v_mfma_f32_16x16x32_bf16 v[36:39], a[64:67], a[0:3], 0\n\t"
      "buffer_load_dwordx4 v2, s[40:43], s48 offen nt lds\n\t"
      "v_mfma_f32_16x16x32_bf16 v[36:39], a[72:75], a[4:7], v[36:39]\n\t"
      "v_mfma_f32_16x16x32_bf16 v[36:39], a[80:83], a[8:11], v[36:39]\n\t"
      "v_mfma_f32_16x16x32_bf16 v[36:39], a[88:91], a[12:15], v[36:39]\n\t"
      "s_waitcnt lgkmcnt(1)\n\t"
      "v_mfma_f32_16x16x32_bf16 v[36:39], a[96:99], a[16:19], v[36:39]\n\t"
      "buffer_store_dwordx4 a[52:55], v13, s[44:47], s49 offen offset:1024 sc1\n\t"
      "v_mfma_f32_16x16x32_bf16 v[36:39], a[104:107], a[20:23], v[36:39]\n\t"
      "v_mfma_f32_16x16x32_bf16 v[16:19], v[160:163], a[24:27], v[16:19]\n\t"
      "ds_read_b128 a[0:3], v52 offset:0\n\t"
      "v_mfma_f32_16x16x32_bf16 v[16:19], v[176:179], a[28:31], v[16:19]\n\t"
      "ds_read_b128 a[4:7], v53 offset:0\n\t"
      "v_mfma_f32_16x16x32_bf16 v[16:19], v[192:195], a[32:35], v[16:19]\n\t"
      "ds_read_b128 a[8:11], v54 offset:0\n\t"
      "v_mfma_f32_16x16x32_bf16 v[16:19], v[208:211], a[36:39], v[16:19]\n\t"
      "ds_read_b128 a[12:15], v55 offset:0\n\t"
      "v_mfma_f32_16x16x32_bf16 v[16:19], v[224:227], a[40:43], v[16:19]\n\t"
      "ds_read_b128 a[16:19], v52 offset:256\n\t"
      "v_mfma_f32_16x16x32_bf16 v[16:19], v[240:243], a[44:47], v[16:19]\n\t"
      "ds_read_b128 a[20:23], v53 offset:256\n\t"
      "v_mfma_f32_16x16x32_bf16 v[20:23], v[164:167], a[24:27], v[20:23]\n\t"
      "s_add_u32 s51, s51, 12288\n\t"
      "s_cmp_eq_u32 s51, 160256\n\t"
      "s_cselect_b32 s51, 25088, s51\n\t"
      "v_mfma_f32_16x16x32_bf16 v[20:23], v[180:183], a[28:31], v[20:23]\n\t"
      "s_waitcnt lgkmcnt(6)\n\t"
      "v_mfma_f32_16x16x32_bf16 v[20:23], v[196:199], a[32:35], v[20:23]\n\t"
      "buffer_store_dwordx4 a[56:59], v14, s[44:47], s49 offen offset:2048 sc1\n\t"
      "v_mfma_f32_16x16x32_bf16 v[20:23], v[212:215], a[36:39], v[20:23]\n\t"
      "v_mfma_f32_16x16x32_bf16 v[20:23], v[228:231], a[40:43], v[20:23]\n\t"
      "s_waitcnt lgkmcnt(0)\n\t"
      "v_mfma_f32_16x16x32_bf16 v[20:23], v[244:247], a[44:47], v[20:23]\n\t"
      "v_mfma_f32_16x16x32_bf16 v[24:27], v[168:171], a[24:27], v[24:27]\n\t"
      "v_mfma_f32_16x16x32_bf16 v[24:27], v[184:187], a[28:31], v[24:27]\n\t"
      "v_cvt_pk_bf16_f32 v40, v16, v17\n\t"
      "v_cvt_pk_bf16_f32 v41, v18, v19\n\t"
      "v_mfma_f32_16x16x32_bf16 v[24:27], v[200:203], a[32:35], v[24:27]\n\t"
      "ds_write_b64 v0, v[40:41] offset:0\n\t"
      "v_mfma_f32_16x16x32_bf16 v[24:27], v[216:219], a[36:39], v[24:27]\n\t"
      "buffer_load_dwordx4 v3, s[40:43], s48 offen offset:1024 nt lds\n\t"
      "v_mfma_f32_16x16x32_bf16 v[24:27], v[232:235], a[40:43], v[24:27]\n\t"
      "s_add_u32 s58, s58, 786432\n\t"
      "s_lshr_b32 s59, s59, 1\n\t"
      "v_mfma_f32_16x16x32_bf16 v[24:27], v[248:251], a[44:47], v[24:27]\n\t"
      "s_bitcmp1_b32 s59, 0\n\t"
      "s_cselect_b32 s49, 24576, 0\n\t"
      "s_add_u32 s49, s49, s58\n\t"
      "v_mfma_f32_16x16x32_bf16 v[28:31], v[172:175], a[24:27], v[28:31]\n\t"
      "v_mfma_f32_16x16x32_bf16 v[28:31], v[188:191], a[28:31], v[28:31]\n\t"
      "v_cvt_pk_bf16_f32 v42, v20, v21\n\t"
      "v_cvt_pk_bf16_f32 v43, v22, v23\n\t"
      "v_mfma_f32_16x16x32_bf16 v[28:31], v[204:207], a[32:35], v[28:31]\n\t"
      "ds_write_b64 v0, v[42:43] offset:32\n\t"
      "v_mfma_f32_16x16x32_bf16 v[28:31], v[220:223], a[36:39], v[28:31]\n\t"
      "v_mfma_f32_16x16x32_bf16 v[28:31], v[236:239], a[40:43], v[28:31]\n\t"
      "v_mfma_f32_16x16x32_bf16 v[28:31], v[252:255], a[44:47], v[28:31]\n\t"
      "v_mfma_f32_16x16x32_bf16 v[32:35], a[108:111], a[24:27], v[32:35]\n\t"
      "v_mfma_f32_16x16x32_bf16 v[32:35], a[116:119], a[28:31], v[32:35]\n\t"
      "v_cvt_pk_bf16_f32 v44, v24, v25\n\t"
      "v_cvt_pk_bf16_f32 v45, v26, v27\n\t"
      "v_mfma_f32_16x16x32_bf16 v[32:35], a[124:127], a[32:35], v[32:35]\n\t"
      "ds_write_b64 v0, v[44:45] offset:64\n\t"
      "v_mfma_f32_16x16x32_bf16 v[32:35], a[132:135], a[36:39], v[32:35]\n\t"
      "buffer_load_dwordx4 v4, s[40:43], s48 offen offset:2048 nt lds\n\t"
      "v_mfma_f32_16x16x32_bf16 v[32:35], a[140:143], a[40:43], v[32:35]\n\t"
      "s_add_u32 s50, s50, 12288\n\t"
      "v_mfma_f32_16x16x32_bf16 v[32:35], a[148:151], a[44:47], v[32:35]\n\t"
      "v_cvt_pk_bf16_f32 v46, v28, v29\n\t"
      "v_cvt_pk_bf16_f32 v47, v30, v31\n\t"
      "v_mfma_f32_16x16x32_bf16 v[36:39], a[112:115], a[24:27], v[36:39]\n\t"
      "ds_write_b64 v0, v[46:47] offset:96\n\t"
      "v_mfma_f32_16x16x32_bf16 v[36:39], a[120:123], a[28:31], v[36:39]\n\t"
      "s_cmp_eq_u32 s50, 160256\n\t"
      "s_cselect_b32 s50, 25088, s50\n\t"
      "v_mfma_f32_16x16x32_bf16 v[36:39], a[128:131], a[32:35], v[36:39]\n\t"
      "v_mfma_f32_16x16x32_bf16 v[36:39], a[136:139], a[36:39], v[36:39]\n\t"
      "v_mfma_f32_16x16x32_bf16 v[36:39], a[144:147], a[40:43], v[36:39]\n\t"
      "v_cvt_pk_bf16_f32 v48, v32, v33\n\t"
      "v_cvt_pk_bf16_f32 v49, v34, v35\n\t"
      "v_mfma_f32_16x16x32_bf16 v[36:39], a[152:155], a[44:47], v[36:39]\n\t"
      "ds_write_b64 v0, v[48:49] offset:128\n\t"
      ".Lmid2_%=:\n\t"
      "v_mfma_f32_16x16x32_bf16 v[16:19], v[64:67], a[0:3], 0\n\t"
      "ds_read_b128 a[24:27], v54 offset:256\n\t"
      "v_mfma_f32_16x16x32_bf16 v[16:19], v[80:83], a[4:7], v[16:19]\n\t"
      "ds_read_b128 a[28:31], v55 offset:256\n\t"
      "v_mfma_f32_16x16x32_bf16 v[16:19], v[96:99], a[8:11], v[16:19]\n\t"
      "ds_read_b128 a[32:35], v52 offset:512\n\t"
      "v_mfma_f32_16x16x32_bf16 v[16:19], v[112:115], a[12:15], v[16:19]\n\t"
      "ds_read_b128 a[36:39], v53 offset:512\n\t"
      "v_mfma_f32_16x16x32_bf16 v[16:19], v[128:131], a[16:19], v[16:19]\n\t"
      "ds_read_b128 a[40:43], v54 offset:512\n\t"
      "v_mfma_f32_16x16x32_bf16 v[16:19], v[144:147], a[20:23], v[16:19]\n\t"
      "ds_read_b128 a[44:47], v55 offset:512\n\t"
      "v_mfma_f32_16x16x32_bf16 v[20:23], v[68:71], a[0:3], 0\n\t"
      "v_cvt_pk_bf16_f32 v50, v36, v37\n\t"
      "v_cvt_pk_bf16_f32 v51, v38, v39\n\t"
      "v_mfma_f32_16x16x32_bf16 v[20:23], v[84:87], a[4:7], v[20:23]\n\t"
      "ds_write_b64 v0, v[50:51] offset:160\n\t"
      "v_mfma_f32_16x16x32_bf16 v[20:23], v[100:103], a[8:11], v[20:23]\n\t"
      "v_add_u32 v56, s51, v15\n\t"
      "v_xor_b32 v57, 64, v56\n\t"
      "v_mfma_f32_16x16x32_bf16 v[20:23], v[116:119], a[12:15], v[20:23]\n\t"
      "v_xor_b32 v58, 128, v56\n\t"
      "v_xor_b32 v59, 192, v56\n\t"
      "v_mfma_f32_16x16x32_bf16 v[20:23], v[132:135], a[16:19], v[20:23]\n\t"
      "v_mfma_f32_16x16x32_bf16 v[20:23], v[148:151], a[20:23], v[20:23]\n\t"
      "v_mfma_f32_16x16x32_bf16 v[24:27], v[72:75], a[0:3], 0\n\t"
      "v_mfma_f32_16x16x32_bf16 v[24:27], v[88:91], a[4:7], v[24:27]\n\t"
      "v_mfma_f32_16x16x32_bf16 v[24:27], v[104:107], a[8:11], v[24:27]\n\t"
      "v_mfma_f32_16x16x32_bf16 v[24:27], v[120:123], a[12:15], v[24:27]\n\t"
      "v_mfma_f32_16x16x32_bf16 v[24:27], v[136:139], a[16:19], v[24:27]\n\t"
      "s_waitcnt vmcnt(12)\n\t"
      "s_waitcnt lgkmcnt(0)\n\t"
      "s_barrier\n\t"
      "v_mfma_f32_16x16x32_bf16 v[24:27], v[152:155], a[20:23], v[24:27]\n\t"
      "v_mfma_f32_16x16x32_bf16 v[28:31], v[76:79], a[0:3], 0\n\t"
      "ds_read_b128 a[48:51], v8 offset:0\n\t"
      "v_mfma_f32_16x16x32_bf16 v[28:31], v[92:95], a[4:7], v[28:31]\n\t"
      "ds_read_b128 a[52:55], v60 offset:1024\n\t"
      "v_mfma_f32_16x16x32_bf16 v[28:31], v[108:111], a[8:11], v[28:31]\n\t"
      "ds_read_b128 a[56:59], v61 offset:2048\n\t"
      "v_mfma_f32_16x16x32_bf16 v[28:31], v[124:127], a[12:15], v[28:31]\n\t"
      "v_mfma_f32_16x16x32_bf16 v[28:31], v[140:143], a[16:19], v[28:31]\n\t"
      "v_mfma_f32_16x16x32_bf16 v[28:31], v[156:159], a[20:23], v[28:31]\n\t"
      "v_mfma_f32_16x16x32_bf16 v[32:35], a[60:63], a[0:3], 0\n\t"
      "v_mfma_f32_16x16x32_bf16 v[32:35], a[68:71], a[4:7], v[32:35]\n\t"
      "s_waitcnt lgkmcnt(2)\n\t"
      "v_mfma_f32_16x16x32_bf16 v[32:35], a[76:79], a[8:11], v[32:35]\n\t"
      "buffer_store_dwordx4 a[48:51], v9, s[44:47], s49 offen sc1\n\t"
      "v_mfma_f32_16x16x32_bf16 v[32:35], a[84:87], a[12:15], v[32:35]\n\t"
      "v_mfma_f32_16x16x32_bf16 v[32:35], a[92:95], a[16:19], v[32:35]\n\t"
      "v_mfma_f32_16x16x32_bf16 v[32:35], a[100:103], a[20:23], v[32:35]\n\t"
      "s_add_u32 m0, s18, s50\n\t"
      "v_mfma_f32_16x16x32_bf16 v[36:39], a[64:67], a[0:3], 0\n\t"
      "buffer_load_dwordx4 v5, s[40:43], s48 offen nt lds\n\t"
      "v_mfma_f32_16x16x32_bf16 v[36:39], a[72:75], a[4:7], v[36:39]\n\t"
      "v_mfma_f32_16x16x32_bf16 v[36:39], a[80:83], a[8:11], v[36:39]\n\t"
      "v_mfma_f32_16x16x32_bf16 v[36:39], a[88:91], a[12:15], v[36:39]\n\t"
      "s_waitcnt lgkmcnt(1)\n\t"
      "v_mfma_f32_16x16x32_bf16 v[36:39], a[96:99], a[16:19], v[36:39]\n\t"
      "buffer_store_dwordx4 a[52:55], v10, s[44:47], s49 offen offset:1024 sc1\n\t"
      "v_mfma_f32_16x16x32_bf16 v[36:39], a[104:107], a[20:23], v[36:39]\n\t"
      "v_mfma_f32_16x16x32_bf16 v[16:19], v[160:163], a[24:27], v[16:19]\n\t"
      "ds_read_b128 a[0:3], v56 offset:0\n\t"
      "v_mfma_f32_16x16x32_bf16 v[16:19], v[176:179], a[28:31], v[16:19]\n\t"
      "ds_read_b128 a[4:7], v57 offset:0\n\t"
      "v_mfma_f32_16x16x32_bf16 v[16:19], v[192:195], a[32:35], v[16:19]\n\t"
      "ds_read_b128 a[8:11], v58 offset:0\n\t"
      "v_mfma_f32_16x16x32_bf16 v[16:19], v[208:211], a[36:39], v[16:19]\n\t"
      "ds_read_b128 a[12:15], v59 offset:0\n\t"
      "v_mfma_f32_16x16x32_bf16 v[16:19], v[224:227], a[40:43], v[16:19]\n\t"
      "ds_read_b128 a[16:19], v56 offset:256\n\t"
      "v_mfma_f32_16x16x32_bf16 v[16:19], v[240:243], a[44:47], v[16:19]\n\t"
      "ds_read_b128 a[20:23], v57 offset:256\n\t"
      "v_mfma_f32_16x16x32_bf16 v[20:23], v[164:167], a[24:27], v[20:23]\n\t"
      "s_add_u32 s51, s51, 12288\n\t"
      "s_cmp_eq_u32 s51, 160256\n\t"
      "s_cselect_b32 s51, 25088, s51\n\t"
      "v_mfma_f32_16x16x32_bf16 v[20:23], v[180:183], a[28:31], v[20:23]\n\t"
      "s_waitcnt lgkmcnt(6)\n\t"
      "v_mfma_f32_16x16x32_bf16 v[20:23], v[196:199], a[32:35], v[20:23]\n\t"
      "buffer_store_dwordx4 a[56:59], v11, s[44:47], s49 offen offset:2048 sc1\n\t"
      "v_mfma_f32_16x16x32_bf16 v[20:23], v[212:215], a[36:39], v[20:23]\n\t"
      "v_mfma_f32_16x16x32_bf16 v[20:23], v[228:231], a[40:43], v[20:23]\n\t"
      "s_waitcnt lgkmcnt(0)\n\t"
      "v_mfma_f32_16x16x32_bf16 v[20:23], v[244:247], a[44:47], v[20:23]\n\t"
      "v_mfma_f32_16x16x32_bf16 v[24:27], v[168:171], a[24:27], v[24:27]\n\t"
      "v_mfma_f32_16x16x32_bf16 v[24:27], v[184:187], a[28:31], v[24:27]\n\t"
      "v_cvt_pk_bf16_f32 v40, v16, v17\n\t"
      "v_cvt_pk_bf16_f32 v41, v18, v19\n\t"
      "v_mfma_f32_16x16x32_bf16 v[24:27], v[200:203], a[32:35], v[24:27]\n\t"
      "ds_write_b64 v0, v[40:41] offset:12544\n\t"
      "v_mfma_f32_16x16x32_bf16 v[24:27], v[216:219], a[36:39], v[24:27]\n\t"
      "buffer_load_dwordx4 v6, s[40:43], s48 offen offset:1024 nt lds\n\t"
      "v_mfma_f32_16x16x32_bf16 v[24:27], v[232:235], a[40:43], v[24:27]\n\t"
      "v_mfma_f32_16x16x32_bf16 v[24:27], v[248:251], a[44:47], v[24:27]\n\t"
      "v_mfma_f32_16x16x32_bf16 v[28:31], v[172:175], a[24:27], v[28:31]\n\t"
      "v_mfma_f32_16x16x32_bf16 v[28:31], v[188:191], a[28:31], v[28:31]\n\t"
      "v_cvt_pk_bf16_f32 v42, v20, v21\n\t"
      "v_cvt_pk_bf16_f32 v43, v22, v23\n\t"
      "v_mfma_f32_16x16x32_bf16 v[28:31], v[204:207], a[32:35], v[28:31]\n\t"
      "ds_write_b64 v0, v[42:43] offset:12576\n\t"
      "v_mfma_f32_16x16x32_bf16 v[28:31], v[220:223], a[36:39], v[28:31]\n\t"
      "v_mfma_f32_16x16x32_bf16 v[28:31], v[236:239], a[40:43], v[28:31]\n\t"
      "v_mfma_f32_16x16x32_bf16 v[28:31], v[252:255], a[44:47], v[28:31]\n\t"
      "v_mfma_f32_16x16x32_bf16 v[32:35], a[108:111], a[24:27], v[32:35]\n\t"
      "v_mfma_f32_16x16x32_bf16 v[32:35], a[116:119], a[28:31], v[32:35]\n\t"
      "v_cvt_pk_bf16_f32 v44, v24, v25\n\t"
      "v_cvt_pk_bf16_f32 v45, v26, v27\n\t"
      "v_mfma_f32_16x16x32_bf16 v[32:35], a[124:127], a[32:35], v[32:35]\n\t"
      "ds_write_b64 v0, v[44:45] offset:12608\n\t"
      "v_mfma_f32_16x16x32_bf16 v[32:35], a[132:135], a[36:39], v[32:35]\n\t"
      "buffer_load_dwordx4 v7, s[40:43], s48 offen offset:2048 nt lds\n\t"
      "v_mfma_f32_16x16x32_bf16 v[32:35], a[140:143], a[40:43], v[32:35]\n\t"
      "s_add_u32 s50, s50, 12288\n\t"
      "v_mfma_f32_16x16x32_bf16 v[32:35], a[148:151], a[44:47], v[32:35]\n\t"
      "v_cvt_pk_bf16_f32 v46, v28, v29\n\t"
      "v_cvt_pk_bf16_f32 v47, v30, v31\n\t"
      "v_mfma_f32_16x16x32_bf16 v[36:39], a[112:115], a[24:27], v[36:39]\n\t"
      "ds_write_b64 v0, v[46:47] offset:12640\n\t"
      "v_mfma_f32_16x16x32_bf16 v[36:39], a[120:123], a[28:31], v[36:39]\n\t"
      "s_cmp_eq_u32 s50, 160256\n\t"
      "s_cselect_b32 s50, 25088, s50\n\t"
      "v_mfma_f32_16x16x32_bf16 v[36:39], a[128:131], a[32:35], v[36:39]\n\t"
      "s_sub_u32 s52, s52, 1\n\t"
      "s_cmp_lg_u32 s52, 0\n\t"
      "v_mfma_f32_16x16x32_bf16 v[36:39], a[136:139], a[36:39], v[36:39]\n\t"
      "v_mfma_f32_16x16x32_bf16 v[36:39], a[144:147], a[40:43], v[36:39]\n\t"
      "v_cvt_pk_bf16_f32 v48, v32, v33\n\t"
      "v_cvt_pk_bf16_f32 v49, v34, v35\n\t"
      "v_mfma_f32_16x16x32_bf16 v[36:39], a[152:155], a[44:47], v[36:39]\n\t"
      "ds_write_b64 v0, v[48:49] offset:12672\n\t"
      "s_cbranch_scc1 .Lloop2_%=\n\t"
      "s_branch .Lendc_%=\n\t"
      ".Lloop3_%=:\n\t"
      "v_mfma_f32_16x16x32_bf16 v[16:19], v[64:67], a[0:3], 0\n\t"
      "ds_read_b128 a[24:27], v58 offset:256\n\t"
      "v_mfma_f32_16x16x32_bf16 v[16:19], v[80:83], a[4:7], v[16:19]\n\t"
      "ds_read_b128 a[28:31], v59 offset:256\n\t"
      "v_mfma_f32_16x16x32_bf16 v[16:19], v[96:99], a[8:11], v[16:19]\n\t"
      "ds_read_b128 a[32:35], v56 offset:512\n\t"
      "v_mfma_f32_16x16x32_bf16 v[16:19], v[112:115], a[12:15], v[16:19]\n\t"
      "ds_read_b128 a[36:39], v57 offset:512\n\t"
      "v_mfma_f32_16x16x32_bf16 v[16:19], v[128:131], a[16:19], v[16:19]\n\t"
      "ds_read_b128 a[40:43], v58 offset:512\n\t"
      "v_mfma_f32_16x16x32_bf16 v[16:19], v[144:147], a[20:23], v[16:19]\n\t"
      "ds_read_b128 a[44:47], v59 offset:512\n\t"
      "v_mfma_f32_16x16x32_bf16 v[20:23], v[68:71], a[0:3], 0\n\t"
      "v_cvt_pk_bf16_f32 v50, v36, v37\n\t"
      "v_cvt_pk_bf16_f32 v51, v38, v39\n\t"
      "v_mfma_f32_16x16x32_bf16 v[20:23], v[84:87], a[4:7], v[20:23]\n\t"
      "ds_write_b64 v0, v[50:51] offset:12704\n\t"
      "v_mfma_f32_16x16x32_bf16 v[20:23], v[100:103], a[8:11], v[20:23]\n\t"
      "v_add_u32 v52, s51, v15\n\t"
      "v_xor_b32 v53, 64, v52\n\t"
      "v_mfma_f32_16x16x32_bf16 v[20:23], v[116:119], a[12:15], v[20:23]\n\t"
      "v_xor_b32 v54, 128, v52\n\t"
      "v_xor_b32 v55, 192, v52\n\t"
      "v_mfma_f32_16x16x32_bf16 v[20:23], v[132:135], a[16:19], v[20:23]\n\t"
      "s_add_u32 s56, s56, 786432\n\t"
      "s_lshr_b32 s57, s57, 1\n\t"
      "v_mfma_f32_16x16x32_bf16 v[20:23], v[148:151], a[20:23], v[20:23]\n\t"
      "s_bitcmp1_b32 s57, 0\n\t"
      "s_cselect_b32 s48, 24576, 0\n\t"
      "s_add_u32 s48, s48, s56\n\t"
      "v_mfma_f32_16x16x32_bf16 v[24:27], v[72:75], a[0:3], 0\n\t"
      "v_mfma_f32_16x16x32_bf16 v[24:27], v[88:91], a[4:7], v[24:27]\n\t"
      "v_mfma_f32_16x16x32_bf16 v[24:27], v[104:107], a[8:11], v[24:27]\n\t"
      "v_mfma_f32_16x16x32_bf16 v[24:27], v[120:123], a[12:15], v[24:27]\n\t"
      "v_mfma_f32_16x16x32_bf16 v[24:27], v[136:139], a[16:19], v[24:27]\n\t"
      "v_mfma_f32_16x16x32_bf16 v[24:27], v[152:155], a[20:23], v[24:27]\n\t"
      "s_waitcnt vmcnt(12)\n\t"
      "s_waitcnt lgkmcnt(0)\n\t"
      "s_barrier\n\t"
      "v_mfma_f32_16x16x32_bf16 v[28:31], v[76:79], a[0:3], 0\n\t"
      "ds_read_b128 a[48:51], v8 offset:12544\n\t"
      "v_mfma_f32_16x16x32_bf16 v[28:31], v[92:95], a[4:7], v[28:31]\n\t"
      "ds_read_b128 a[52:55], v60 offset:13568\n\t"
      "v_mfma_f32_16x16x32_bf16 v[28:31], v[108:111], a[8:11], v[28:31]\n\t"
      "ds_read_b128 a[56:59], v61 offset:14592\n\t"
      "v_mfma_f32_16x16x32_bf16 v[28:31], v[124:127], a[12:15], v[28:31]\n\t"
      "v_mfma_f32_16x16x32_bf16 v[28:31], v[140:143], a[16:19], v[28:31]\n\t"
      "v_mfma_f32_16x16x32_bf16 v[28:31], v[156:159], a[20:23], v[28:31]\n\t"
      "v_mfma_f32_16x16x32_bf16 v[32:35], a[60:63], a[0:3], 0\n\t"
      "v_mfma_f32_16x16x32_bf16 v[32:35], a[68:71], a[4:7], v[32:35]\n\t"
      "s_waitcnt lgkmcnt(2)\n\t"
      "v_mfma_f32_16x16x32_bf16 v[32:35], a[76:79], a[8:11], v[32:35]\n\t"
      "buffer_store_dwordx4 a[48:51], v12, s[44:47], s49 offen sc1\n\t"
      "v_mfma_f32_16x16x32_bf16 v[32:35], a[84:87], a[12:15], v[32:35]\n\t"
      "v_mfma_f32_16x16x32_bf16 v[32:35], a[92:95], a[16:19], v[32:35]\n\t"
      "v_mfma_f32_16x16x32_bf16 v[32:35], a[100:103], a[20:23], v[32:35]\n\t"
      "s_add_u32 m0, s18, s50\n\t"
      "v_mfma_f32_16x16x32_bf16 v[36:39], a[64:67], a[0:3], 0\n\t"
      "buffer_load_dwordx4 v2, s[40:43], s48 offen nt lds\n\t"
      "v_mfma_f32_16x16x32_bf16 v[36:39], a[72:75], a[4:7], v[36:39]\n\t"
      "v_mfma_f32_16x16x32_bf16 v[36:39], a[80:83], a[8:11], v[36:39]\n\t"
      "v_mfma_f32_16x16x32_bf16 v[36:39], a[88:91], a[12:15], v[36:39]\n\t"
      "s_waitcnt lgkmcnt(1)\n\t"
      "v_mfma_f32_16x16x32_bf16 v[36:39], a[96:99], a[16:19], v[36:39]\n\t"
      "buffer_store_dwordx4 a[52:55], v13, s[44:47], s49 offen offset:1024 sc1\n\t"
      "v_mfma_f32_16x16x32_bf16 v[36:39], a[104:107], a[20:23], v[36:39]\n\t"
      "v_mfma_f32_16x16x32_bf16 v[16:19], v[160:163], a[24:27], v[16:19]\n\t"
      "ds_read_b128 a[0:3], v52 offset:0\n\t"
      "v_mfma_f32_16x16x32_bf16 v[16:19], v[176:179], a[28:31], v[16:19]\n\t"
      "ds_read_b128 a[4:7], v53 offset:0\n\t"
      "v_mfma_f32_16x16x32_bf16 v[16:19], v[192:195], a[32:35], v[16:19]\n\t"
      "ds_read_b128 a[8:11], v54 offset:0\n\t"
      "v_mfma_f32_16x16x32_bf16 v[16:19], v[208:211], a[36:39], v[16:19]\n\t"
      "ds_read_b128 a[12:15], v55 offset:0\n\t"
      "v_mfma_f32_16x16x32_bf16 v[16:19], v[224:227], a[40:43], v[16:19]\n\t"
      "ds_read_b128 a[16:19], v52 offset:256\n\t"
      "v_mfma_f32_16x16x32_bf16 v[16:19], v[240:243], a[44:47], v[16:19]\n\t"
      "ds_read_b128 a[20:23], v53 offset:256\n\t"
      "v_mfma_f32_16x16x32_bf16 v[20:23], v[164:167], a[24:27], v[20:23]\n\t"
      "s_add_u32 s51, s51, 12288\n\t"
      "s_cmp_eq_u32 s51, 160256\n\t"
      "s_cselect_b32 s51, 25088, s51\n\t"
      "v_mfma_f32_16x16x32_bf16 v[20:23], v[180:183], a[28:31], v[20:23]\n\t"
      "s_waitcnt lgkmcnt(6)\n\t"
      "v_mfma_f32_16x16x32_bf16 v[20:23], v[196:199], a[32:35], v[20:23]\n\t"
      "buffer_store_dwordx4 a[56:59], v14, s[44:47], s49 offen offset:2048 sc1\n\t"
      "v_mfma_f32_16x16x32_bf16 v[20:23], v[212:215], a[36:39], v[20:23]\n\t"
      "v_mfma_f32_16x16x32_bf16 v[20:23], v[228:231], a[40:43], v[20:23]\n\t"
      "s_waitcnt lgkmcnt(0)\n\t"
      "v_mfma_f32_16x16x32_bf16 v[20:23], v[244:247], a[44:47], v[20:23]\n\t"
      "v_mfma_f32_16x16x32_bf16 v[24:27], v[168:171], a[24:27], v[24:27]\n\t"
      "v_mfma_f32_16x16x32_bf16 v[24:27], v[184:187], a[28:31], v[24:27]\n\t"
      "v_cvt_pk_bf16_f32 v40, v16, v17\n\t"
      "v_cvt_pk_bf16_f32 v41, v18, v19\n\t"
      "v_mfma_f32_16x16x32_bf16 v[24:27], v[200:203], a[32:35], v[24:27]\n\t"
      "ds_write_b64 v0, v[40:41] offset:0\n\t"
      "v_mfma_f32_16x16x32_bf16 v[24:27], v[216:219], a[36:39], v[24:27]\n\t"
      "buffer_load_dwordx4 v3, s[40:43], s48 offen offset:1024 nt lds\n\t"
      "v_mfma_f32_16x16x32_bf16 v[24:27], v[232:235], a[40:43], v[24:27]\n\t"
      "s_add_u32 s58, s58, 786432\n\t"
      "s_lshr_b32 s59, s59, 1\n\t"
      "v_mfma_f32_16x16x32_bf16 v[24:27], v[248:251], a[44:47], v[24:27]\n\t"
      "s_bitcmp1_b32 s59, 0\n\t"
      "s_cselect_b32 s49, 24576, 0\n\t"
      "s_add_u32 s49, s49, s58\n\t"
      "v_mfma_f32_16x16x32_bf16 v[28:31], v[172:175], a[24:27], v[28:31]\n\t"
      "v_mfma_f32_16x16x32_bf16 v[28:31], v[188:191], a[28:31], v[28:31]\n\t"
      "v_cvt_pk_bf16_f32 v42, v20, v21\n\t"
      "v_cvt_pk_bf16_f32 v43, v22, v23\n\t"
      "v_mfma_f32_16x16x32_bf16 v[28:31], v[204:207], a[32:35], v[28:31]\n\t"
      "ds_write_b64 v0, v[42:43] offset:32\n\t"
      "v_mfma_f32_16x16x32_bf16 v[28:31], v[220:223], a[36:39], v[28:31]\n\t"
      "v_mfma_f32_16x16x32_bf16 v[28:31], v[236:239], a[40:43], v[28:31]\n\t"
      "v_mfma_f32_16x16x32_bf16 v[28:31], v[252:255], a[44:47], v[28:31]\n\t"
      "v_mfma_f32_16x16x32_bf16 v[32:35], a[108:111], a[24:27], v[32:35]\n\t"
      "v_mfma_f32_16x16x32_bf16 v[32:35], a[116:119], a[28:31], v[32:35]\n\t"
      "v_cvt_pk_bf16_f32 v44, v24, v25\n\t"
      "v_cvt_pk_bf16_f32 v45, v26, v27\n\t"
      "v_mfma_f32_16x16x32_bf16 v[32:35], a[124:127], a[32:35], v[32:35]\n\t"
      "ds_write_b64 v0, v[44:45] offset:64\n\t"
      "v_mfma_f32_16x16x32_bf16 v[32:35], a[132:135], a[36:39], v[32:35]\n\t"
      "buffer_load_dwordx4 v4, s[40:43], s48 offen offset:2048 nt lds\n\t"
      "v_mfma_f32_16x16x32_bf16 v[32:35], a[140:143], a[40:43], v[32:35]\n\t"
      "s_add_u32 s50, s50, 12288\n\t"
      "v_mfma_f32_16x16x32_bf16 v[32:35], a[148:151], a[44:47], v[32:35]\n\t"
      "v_cvt_pk_bf16_f32 v46, v28, v29\n\t"
      "v_cvt_pk_bf16_f32 v47, v30, v31\n\t"
      "v_mfma_f32_16x16x32_bf16 v[36:39], a[112:115], a[24:27], v[36:39]\n\t"
      "ds_write_b64 v0, v[46:47] offset:96\n\t"
      "v_mfma_f32_16x16x32_bf16 v[36:39], a[120:123], a[28:31], v[36:39]\n\t"
      "s_cmp_eq_u32 s50, 160256\n\t"
      "s_cselect_b32 s50, 25088, s50\n\t"
      "v_mfma_f32_16x16x32_bf16 v[36:39], a[128:131], a[32:35], v[36:39]\n\t"
      "v_mfma_f32_16x16x32_bf16 v[36:39], a[136:139], a[36:39], v[36:39]\n\t"
      "v_mfma_f32_16x16x32_bf16 v[36:39], a[144:147], a[40:43], v[36:39]\n\t"
      "v_cvt_pk_bf16_f32 v48, v32, v33\n\t"
      "v_cvt_pk_bf16_f32 v49, v34, v35\n\t"
      "v_mfma_f32_16x16x32_bf16 v[36:39], a[152:155], a[44:47], v[36:39]\n\t"
      "ds_write_b64 v0, v[48:49] offset:128\n\t"
      ".Lmid3_%=:\n\t"
      "v_mfma_f32_16x16x32_bf16 v[16:19], v[64:67], a[0:3], 0\n\t"
      "ds_read_b128 a[24:27], v54 offset:256\n\t"
      "v_mfma_f32_16x16x32_bf16 v[16:19], v[80:83], a[4:7], v[16:19]\n\t"
      "ds_read_b128 a[28:31], v55 offset:256\n\t"
      "v_mfma_f32_16x16x32_bf16 v[16:19], v[96:99], a[8:11], v[16:19]\n\t"
      "ds_read_b128 a[32:35], v52 offset:512\n\t"
      "v_mfma_f32_16x16x32_bf16 v[16:19], v[112:115], a[12:15], v[16:19]\n\t"
      "ds_read_b128 a[36:39], v53 offset:512\n\t"
      "v_mfma_f32_16x16x32_bf16 v[16:19], v[128:131], a[16:19], v[16:19]\n\t"
      "ds_read_b128 a[40:43], v54 offset:512\n\t"
      "v_mfma_f32_16x16x32_bf16 v[16:19], v[144:147], a[20:23], v[16:19]\n\t"
      "ds_read_b128 a[44:47], v55 offset:512\n\t"
      "v_mfma_f32_16x16x32_bf16 v[20:23], v[68:71], a[0:3], 0\n\t"
      "v_cvt_pk_bf16_f32 v50, v36, v37\n\t"
      "v_cvt_pk_bf16_f32 v51, v38, v39\n\t"
      "v_mfma_f32_16x16x32_bf16 v[20:23], v[84:87], a[4:7], v[20:23]\n\t"
      "ds_write_b64 v0, v[50:51] offset:160\n\t"
      "v_mfma_f32_16x16x32_bf16 v[20:23], v[100:103], a[8:11], v[20:23]\n\t"
      "v_add_u32 v56, s51, v15\n\t"
      "v_xor_b32 v57, 64, v56\n\t"
      "v_mfma_f32_16x16x32_bf16 v[20:23], v[116:119], a[12:15], v[20:23]\n\t"
      "v_xor_b32 v58, 128, v56\n\t"
      "v_xor_b32 v59, 192, v56\n\t"
      "v_mfma_f32_16x16x32_bf16 v[20:23], v[132:135], a[16:19], v[20:23]\n\t"
      "v_mfma_f32_16x16x32_bf16 v[20:23], v[148:151], a[20:23], v[20:23]\n\t"
      "v_mfma_f32_16x16x32_bf16 v[24:27], v[72:75], a[0:3], 0\n\t"
      "v_mfma_f32_16x16x32_bf16 v[24:27], v[88:91], a[4:7], v[24:27]\n\t"
      "v_mfma_f32_16x16x32_bf16 v[24:27], v[104:107], a[8:11], v[24:27]\n\t"
      "v_mfma_f32_16x16x32_bf16 v[24:27], v[120:123], a[12:15], v[24:27]\n\t"
      "v_mfma_f32_16x16x32_bf16 v[24:27], v[136:139], a[16:19], v[24:27]\n\t"
      "v_mfma_f32_16x16x32_bf16 v[24:27], v[152:155], a[20:23], v[24:27]\n\t"
      "s_waitcnt vmcnt(12)\n\t"
      "s_waitcnt lgkmcnt(0)\n\t"
      "s_barrier\n\t"
      "v_mfma_f32_16x16x32_bf16 v[28:31], v[76:79], a[0:3], 0\n\t"
      "ds_read_b128 a[48:51], v8 offset:0\n\t"
      "v_mfma_f32_16x16x32_bf16 v[28:31], v[92:95], a[4:7], v[28:31]\n\t"
      "ds_read_b128 a[52:55], v60 offset:1024\n\t"
      "v_mfma_f32_16x16x32_bf16 v[28:31], v[108:111], a[8:11], v[28:31]\n\t"
      "ds_read_b128 a[56:59], v61 offset:2048\n\t"
      "v_mfma_f32_16x16x32_bf16 v[28:31], v[124:127], a[12:15], v[28:31]\n\t"
      "v_mfma_f32_16x16x32_bf16 v[28:31], v[140:143], a[16:19], v[28:31]\n\t"
      "v_mfma_f32_16x16x32_bf16 v[28:31], v[156:159], a[20:23], v[28:31]\n\t"
      "v_mfma_f32_16x16x32_bf16 v[32:35], a[60:63], a[0:3], 0\n\t"
      "v_mfma_f32_16x16x32_bf16 v[32:35], a[68:71], a[4:7], v[32:35]\n\t"
      "s_waitcnt lgkmcnt(2)\n\t"
      "v_mfma_f32_16x16x32_bf16 v[32:35], a[76:79], a[8:11], v[32:35]\n\t"
      "buffer_store_dwordx4 a[48:51], v9, s[44:47], s49 offen sc1\n\t"
      "v_mfma_f32_16x16x32_bf16 v[32:35], a[84:87], a[12:15], v[32:35]\n\t"
      "v_mfma_f32_16x16x32_bf16 v[32:35], a[92:95], a[16:19], v[32:35]\n\t"
      "v_mfma_f32_16x16x32_bf16 v[32:35], a[100:103], a[20:23], v[32:35]\n\t"
      "s_add_u32 m0, s18, s50\n\t"
      "v_mfma_f32_16x16x32_bf16 v[36:39], a[64:67], a[0:3], 0\n\t"
      "buffer_load_dwordx4 v5, s[40:43], s48 offen nt lds\n\t"
      "v_mfma_f32_16x16x32_bf16 v[36:39], a[72:75], a[4:7], v[36:39]\n\t"
      "v_mfma_f32_16x16x32_bf16 v[36:39], a[80:83], a[8:11], v[36:39]\n\t"
      "v_mfma_f32_16x16x32_bf16 v[36:39], a[88:91], a[12:15], v[36:39]\n\t"
      "s_waitcnt lgkmcnt(1)\n\t"
      "v_mfma_f32_16x16x32_bf16 v[36:39], a[96:99], a[16:19], v[36:39]\n\t"
      "buffer_store_dwordx4 a[52:55], v10, s[44:47], s49 offen offset:1024 sc1\n\t"
      "v_mfma_f32_16x16x32_bf16 v[36:39], a[104:107], a[20:23], v[36:39]\n\t"
      "v_mfma_f32_16x16x32_bf16 v[16:19], v[160:163], a[24:27], v[16:19]\n\t"
      "ds_read_b128 a[0:3], v56 offset:0\n\t"
      "v_mfma_f32_16x16x32_bf16 v[16:19], v[176:179], a[28:31], v[16:19]\n\t"
      "ds_read_b128 a[4:7], v57 offset:0\n\t"
      "v_mfma_f32_16x16x32_bf16 v[16:19], v[192:195], a[32:35], v[16:19]\n\t"
      "ds_read_b128 a[8:11], v58 offset:0\n\t"
      "v_mfma_f32_16x16x32_bf16 v[16:19], v[208:211], a[36:39], v[16:19]\n\t"
      "ds_read_b128 a[12:15], v59 offset:0\n\t"
      "v_mfma_f32_16x16x32_bf16 v[16:19], v[224:227], a[40:43], v[16:19]\n\t"
      "ds_read_b128 a[16:19], v56 offset:256\n\t"
      "v_mfma_f32_16x16x32_bf16 v[16:19], v[240:243], a[44:47], v[16:19]\n\t"
      "ds_read_b128 a[20:23], v57 offset:256\n\t"
      "v_mfma_f32_16x16x32_bf16 v[20:23], v[164:167], a[24:27], v[20:23]\n\t"
      "s_add_u32 s51, s51, 12288\n\t"
      "s_cmp_eq_u32 s51, 160256\n\t"
      "s_cselect_b32 s51, 25088, s51\n\t"
      "v_mfma_f32_16x16x32_bf16 v[20:23], v[180:183], a[28:31], v[20:23]\n\t"
      "s_waitcnt lgkmcnt(6)\n\t"
      "v_mfma_f32_16x16x32_bf16 v[20:23], v[196:199], a[32:35], v[20:23]\n\t"
      "buffer_store_dwordx4 a[56:59], v11, s[44:47], s49 offen offset:2048 sc1\n\t"
      "v_mfma_f32_16x16x32_bf16 v[20:23], v[212:215], a[36:39], v[20:23]\n\t"
      "v_mfma_f32_16x16x32_bf16 v[20:23], v[228:231], a[40:43], v[20:23]\n\t"
      "s_waitcnt lgkmcnt(0)\n\t"
      "v_mfma_f32_16x16x32_bf16 v[20:23], v[244:247], a[44:47], v[20:23]\n\t"
      "v_mfma_f32_16x16x32_bf16 v[24:27], v[168:171], a[24:27], v[24:27]\n\t"
      "v_mfma_f32_16x16x32_bf16 v[24:27], v[184:187], a[28:31], v[24:27]\n\t"
      "v_cvt_pk_bf16_f32 v40, v16, v17\n\t"
      "v_cvt_pk_bf16_f32 v41, v18, v19\n\t"
      "v_mfma_f32_16x16x32_bf16 v[24:27], v[200:203], a[32:35], v[24:27]\n\t"
      "ds_write_b64 v0, v[40:41] offset:12544\n\t"
      "v_mfma_f32_16x16x32_bf16 v[24:27], v[216:219], a[36:39], v[24:27]\n\t"
      "buffer_load_dwordx4 v6, s[40:43], s48 offen offset:1024 nt lds\n\t"
      "v_mfma_f32_16x16x32_bf16 v[24:27], v[232:235], a[40:43], v[24:27]\n\t"
      "v_mfma_f32_16x16x32_bf16 v[24:27], v[248:251], a[44:47], v[24:27]\n\t"
      "v_mfma_f32_16x16x32_bf16 v[28:31], v[172:175], a[24:27], v[28:31]\n\t"
      "v_mfma_f32_16x16x32_bf16 v[28:31], v[188:191], a[28:31], v[28:31]\n\t"
      "v_cvt_pk_bf16_f32 v42, v20, v21\n\t"
      "v_cvt_pk_bf16_f32 v43, v22, v23\n\t"
      "v_mfma_f32_16x16x32_bf16 v[28:31], v[204:207], a[32:35], v[28:31]\n\t"
      "ds_write_b64 v0, v[42:43] offset:12576\n\t"
      "v_mfma_f32_16x16x32_bf16 v[28:31], v[220:223], a[36:39], v[28:31]\n\t"
      "v_mfma_f32_16x16x32_bf16 v[28:31], v[236:239], a[40:43], v[28:31]\n\t"
      "v_mfma_f32_16x16x32_bf16 v[28:31], v[252:255], a[44:47], v[28:31]\n\t"
      "v_mfma_f32_16x16x32_bf16 v[32:35], a[108:111], a[24:27], v[32:35]\n\t"
      "v_mfma_f32_16x16x32_bf16 v[32:35], a[116:119], a[28:31], v[32:35]\n\t"
      "v_cvt_pk_bf16_f32 v44, v24, v25\n\t"
      "v_cvt_pk_bf16_f32 v45, v26, v27\n\t"
      "v_mfma_f32_16x16x32_bf16 v[32:35], a[124:127], a[32:35], v[32:35]\n\t"
      "ds_write_b64 v0, v[44:45] offset:12608\n\t"
      "v_mfma_f32_16x16x32_bf16 v[32:35], a[132:135], a[36:39], v[32:35]\n\t"
      "buffer_load_dwordx4 v7, s[40:43], s48 offen offset:2048 nt lds\n\t"
      "v_mfma_f32_16x16x32_bf16 v[32:35], a[140:143], a[40:43], v[32:35]\n\t"
      "s_add_u32 s50, s50, 12288\n\t"
      "v_mfma_f32_16x16x32_bf16 v[32:35], a[148:151], a[44:47], v[32:35]\n\t"
      "v_cvt_pk_bf16_f32 v46, v28, v29\n\t"
      "v_cvt_pk_bf16_f32 v47, v30, v31\n\t"
      "v_mfma_f32_16x16x32_bf16 v[36:39], a[112:115], a[24:27], v[36:39]\n\t"
      "ds_write_b64 v0, v[46:47] offset:12640\n\t"
      "v_mfma_f32_16x16x32_bf16 v[36:39], a[120:123], a[28:31], v[36:39]\n\t"
      "s_cmp_eq_u32 s50, 160256\n\t"
      "s_cselect_b32 s50, 25088, s50\n\t"
      "v_mfma_f32_16x16x32_bf16 v[36:39], a[128:131], a[32:35], v[36:39]\n\t"
      "s_sub_u32 s52, s52, 1\n\t"
      "s_cmp_lg_u32 s52, 0\n\t"
      "v_mfma_f32_16x16x32_bf16 v[36:39], a[136:139], a[36:39], v[36:39]\n\t"
      "v_mfma_f32_16x16x32_bf16 v[36:39], a[144:147], a[40:43], v[36:39]\n\t"
      "v_cvt_pk_bf16_f32 v48, v32, v33\n\t"
      "v_cvt_pk_bf16_f32 v49, v34, v35\n\t"
      "v_mfma_f32_16x16x32_bf16 v[36:39], a[152:155], a[44:47], v[36:39]\n\t"
      "ds_write_b64 v0, v[48:49] offset:12672\n\t"
      "s_cbranch_scc1 .Lloop3_%=\n\t"
      ".Lendc_%=:\n\t"
      "s_cmp_eq_u32 s48, 113221632\n\t"
      "s_cbranch_scc1 .Lwrap3_%=\n\t"
      ".Lwrapped3_%=:\n\t"
      "v_mfma_f32_16x16x32_bf16 v[16:19], v[64:67], a[0:3], 0\n\t"
      "ds_read_b128 a[24:27], v58 offset:256\n\t"
      "v_mfma_f32_16x16x32_bf16 v[16:19], v[80:83], a[4:7], v[16:19]\n\t"
      "ds_read_b128 a[28:31], v59 offset:256\n\t"
      "v_mfma_f32_16x16x32_bf16 v[16:19], v[96:99], a[8:11], v[16:19]\n\t"
      "ds_read_b128 a[32:35], v56 offset:512\n\t"
      "v_mfma_f32_16x16x32_bf16 v[16:19], v[112:115], a[12:15], v[16:19]\n\t"
      "ds_read_b128 a[36:39], v57 offset:512\n\t"
      "v_mfma_f32_16x16x32_bf16 v[16:19], v[128:131], a[16:19], v[16:19]\n\t"
      "ds_read_b128 a[40:43], v58 offset:512\n\t"
      "v_mfma_f32_16x16x32_bf16 v[16:19], v[144:147], a[20:23], v[16:19]\n\t"
      "ds_read_b128 a[44:47], v59 offset:512\n\t"
      "v_mfma_f32_16x16x32_bf16 v[20:23], v[68:71], a[0:3], 0\n\t"
      "v_cvt_pk_bf16_f32 v50, v36, v37\n\t"
      "v_cvt_pk_bf16_f32 v51, v38, v39\n\t"
      "v_mfma_f32_16x16x32_bf16 v[20:23], v[84:87], a[4:7], v[20:23]\n\t"
      "ds_write_b64 v0, v[50:51] offset:12704\n\t"
      "v_mfma_f32_16x16x32_bf16 v[20:23], v[100:103], a[8:11], v[20:23]\n\t"
      "v_add_u32 v52, s51, v15\n\t"
      "v_xor_b32 v53, 64, v52\n\t"
      "v_mfma_f32_16x16x32_bf16 v[20:23], v[116:119], a[12:15], v[20:23]\n\t"
      "v_xor_b32 v54, 128, v52\n\t"
      "v_xor_b32 v55, 192, v52\n\t"
      "v_mfma_f32_16x16x32_bf16 v[20:23], v[132:135], a[16:19], v[20:23]\n\t"
      "s_add_u32 s58, s58, 786432\n\t"
      "s_lshr_b32 s59, s59, 1\n\t"
      "v_mfma_f32_16x16x32_bf16 v[20:23], v[148:151], a[20:23], v[20:23]\n\t"
      "s_bitcmp1_b32 s59, 0\n\t"
      "s_cselect_b32 s84, 24576, 0\n\t"
      "s_add_u32 s84, s84, s58\n\t"
      "v_mfma_f32_16x16x32_bf16 v[24:27], v[72:75], a[0:3], 0\n\t"
      "s_add_u32 s58, s58, 786432\n\t"
      "s_lshr_b32 s59, s59, 1\n\t"
      "v_mfma_f32_16x16x32_bf16 v[24:27], v[88:91], a[4:7], v[24:27]\n\t"
      "s_bitcmp1_b32 s59, 0\n\t"
      "s_cselect_b32 s85, 24576, 0\n\t"
      "s_add_u32 s85, s85, s58\n\t"
      "v_mfma_f32_16x16x32_bf16 v[24:27], v[104:107], a[8:11], v[24:27]\n\t"
      "s_waitcnt vmcnt(12)\n\t"
      "s_waitcnt lgkmcnt(0)\n\t"
      "s_barrier\n\t"
      "v_mfma_f32_16x16x32_bf16 v[24:27], v[120:123], a[12:15], v[24:27]\n\t"
      "v_mfma_f32_16x16x32_bf16 v[24:27], v[136:139], a[16:19], v[24:27]\n\t"
      "v_mfma_f32_16x16x32_bf16 v[24:27], v[152:155], a[20:23], v[24:27]\n\t"
      "v_mfma_f32_16x16x32_bf16 v[28:31], v[76:79], a[0:3], 0\n\t"
      "ds_read_b128 a[188:191], v8 offset:12544\n\t"
      "v_mfma_f32_16x16x32_bf16 v[28:31], v[92:95], a[4:7], v[28:31]\n\t"
      "ds_read_b128 a[192:195], v60 offset:13568\n\t"
      "v_mfma_f32_16x16x32_bf16 v[28:31], v[108:111], a[8:11], v[28:31]\n\t"
      "ds_read_b128 a[196:199], v61 offset:14592\n\t"
      "v_mfma_f32_16x16x32_bf16 v[28:31], v[124:127], a[12:15], v[28:31]\n\t"
      "v_mfma_f32_16x16x32_bf16 v[28:31], v[140:143], a[16:19], v[28:31]\n\t"
      "v_mfma_f32_16x16x32_bf16 v[28:31], v[156:159], a[20:23], v[28:31]\n\t"
      "v_mfma_f32_16x16x32_bf16 v[32:35], a[60:63], a[0:3], 0\n\t"
      "v_mfma_f32_16x16x32_bf16 v[32:35], a[68:71], a[4:7], v[32:35]\n\t"
      "v_mfma_f32_16x16x32_bf16 v[32:35], a[76:79], a[8:11], v[32:35]\n\t"
      "v_mfma_f32_16x16x32_bf16 v[32:35], a[84:87], a[12:15], v[32:35]\n\t"
      "v_mfma_f32_16x16x32_bf16 v[32:35], a[92:95], a[16:19], v[32:35]\n\t"
      "v_mfma_f32_16x16x32_bf16 v[32:35], a[100:103], a[20:23], v[32:35]\n\t"
      "v_mfma_f32_16x16x32_bf16 v[36:39], a[64:67], a[0:3], 0\n\t"
      "v_mfma_f32_16x16x32_bf16 v[36:39], a[72:75], a[4:7], v[36:39]\n\t"
      "v_mfma_f32_16x16x32_bf16 v[36:39], a[80:83], a[8:11], v[36:39]\n\t"
      "v_mfma_f32_16x16x32_bf16 v[36:39], a[88:91], a[12:15], v[36:39]\n\t"
      "v_mfma_f32_16x16x32_bf16 v[36:39], a[96:99], a[16:19], v[36:39]\n\t"
      "v_mfma_f32_16x16x32_bf16 v[36:39], a[104:107], a[20:23], v[36:39]\n\t"
      "v_mfma_f32_16x16x32_bf16 v[16:19], v[160:163], a[24:27], v[16:19]\n\t"
      "ds_read_b128 a[0:3], v52 offset:0\n\t"
      "v_mfma_f32_16x16x32_bf16 v[16:19], v[176:179], a[28:31], v[16:19]\n\t"
      "ds_read_b128 a[4:7], v53 offset:0\n\t"
      "v_mfma_f32_16x16x32_bf16 v[16:19], v[192:195], a[32:35], v[16:19]\n\t"
      "ds_read_b128 a[8:11], v54 offset:0\n\t"
      "v_mfma_f32_16x16x32_bf16 v[16:19], v[208:211], a[36:39], v[16:19]\n\t"
      "ds_read_b128 a[12:15], v55 offset:0\n\t"
      "v_mfma_f32_16x16x32_bf16 v[16:19], v[224:227], a[40:43], v[16:19]\n\t"
      "ds_read_b128 a[16:19], v52 offset:256\n\t"
      "v_mfma_f32_16x16x32_bf16 v[16:19], v[240:243], a[44:47], v[16:19]\n\t"
      "ds_read_b128 a[20:23], v53 offset:256\n\t"
      "v_mfma_f32_16x16x32_bf16 v[20:23], v[164:167], a[24:27], v[20:23]\n\t"
      "s_add_u32 s51, s51, 12288\n\t"
      "s_cmp_eq_u32 s51, 160256\n\t"
      "s_cselect_b32 s51, 25088, s51\n\t"
      "v_mfma_f32_16x16x32_bf16 v[20:23], v[180:183], a[28:31], v[20:23]\n\t"
      "v_mfma_f32_16x16x32_bf16 v[20:23], v[196:199], a[32:35], v[20:23]\n\t"
      "v_mfma_f32_16x16x32_bf16 v[20:23], v[212:215], a[36:39], v[20:23]\n\t"
      "v_mfma_f32_16x16x32_bf16 v[20:23], v[228:231], a[40:43], v[20:23]\n\t"
      "s_waitcnt lgkmcnt(0)\n\t"
      "v_mfma_f32_16x16x32_bf16 v[20:23], v[244:247], a[44:47], v[20:23]\n\t"
      "v_mfma_f32_16x16x32_bf16 v[24:27], v[168:171], a[24:27], v[24:27]\n\t"
      "v_mfma_f32_16x16x32_bf16 v[24:27], v[184:187], a[28:31], v[24:27]\n\t"
      "v_cvt_pk_bf16_f32 v40, v16, v17\n\t"
      "v_cvt_pk_bf16_f32 v41, v18, v19\n\t"
      "v_mfma_f32_16x16x32_bf16 v[24:27], v[200:203], a[32:35], v[24:27]\n\t"
      "ds_write_b64 v0, v[40:41] offset:0\n\t"
      "v_mfma_f32_16x16x32_bf16 v[24:27], v[216:219], a[36:39], v[24:27]\n\t"
      "v_mfma_f32_16x16x32_bf16 v[24:27], v[232:235], a[40:43], v[24:27]\n\t"
      "v_mfma_f32_16x16x32_bf16 v[24:27], v[248:251], a[44:47], v[24:27]\n\t"
      "v_mfma_f32_16x16x32_bf16 v[28:31], v[172:175], a[24:27], v[28:31]\n\t"
      "v_mfma_f32_16x16x32_bf16 v[28:31], v[188:191], a[28:31], v[28:31]\n\t"
      "v_cvt_pk_bf16_f32 v42, v20, v21\n\t"
      "v_cvt_pk_bf16_f32 v43, v22, v23\n\t"
      "v_mfma_f32_16x16x32_bf16 v[28:31], v[204:207], a[32:35], v[28:31]\n\t"
      "ds_write_b64 v0, v[42:43] offset:32\n\t"
      "v_mfma_f32_16x16x32_bf16 v[28:31], v[220:223], a[36:39], v[28:31]\n\t"
      "v_mfma_f32_16x16x32_bf16 v[28:31], v[236:239], a[40:43], v[28:31]\n\t"
      "v_mfma_f32_16x16x32_bf16 v[28:31], v[252:255], a[44:47], v[28:31]\n\t"
      "v_mfma_f32_16x16x32_bf16 v[32:35], a[108:111], a[24:27], v[32:35]\n\t"
      "v_mfma_f32_16x16x32_bf16 v[32:35], a[116:119], a[28:31], v[32:35]\n\t"
      "v_cvt_pk_bf16_f32 v44, v24, v25\n\t"
      "v_cvt_pk_bf16_f32 v45, v26, v27\n\t"
      "v_mfma_f32_16x16x32_bf16 v[32:35], a[124:127], a[32:35], v[32:35]\n\t"
      "ds_write_b64 v0, v[44:45] offset:64\n\t"
      "v_mfma_f32_16x16x32_bf16 v[32:35], a[132:135], a[36:39], v[32:35]\n\t"
      "v_mfma_f32_16x16x32_bf16 v[32:35], a[140:143], a[40:43], v[32:35]\n\t"
      "v_mfma_f32_16x16x32_bf16 v[32:35], a[148:151], a[44:47], v[32:35]\n\t"
      "v_cvt_pk_bf16_f32 v46, v28, v29\n\t"
      "v_cvt_pk_bf16_f32 v47, v30, v31\n\t"
      "v_mfma_f32_16x16x32_bf16 v[36:39], a[112:115], a[24:27], v[36:39]\n\t"
      "ds_write_b64 v0, v[46:47] offset:96\n\t"
      "v_mfma_f32_16x16x32_bf16 v[36:39], a[120:123], a[28:31], v[36:39]\n\t"
      "v_mfma_f32_16x16x32_bf16 v[36:39], a[128:131], a[32:35], v[36:39]\n\t"
      "v_mfma_f32_16x16x32_bf16 v[36:39], a[136:139], a[36:39], v[36:39]\n\t"
      "v_mfma_f32_16x16x32_bf16 v[36:39], a[144:147], a[40:43], v[36:39]\n\t"
      "v_cvt_pk_bf16_f32 v48, v32, v33\n\t"
      "v_cvt_pk_bf16_f32 v49, v34, v35\n\t"
      "v_mfma_f32_16x16x32_bf16 v[36:39], a[152:155], a[44:47], v[36:39]\n\t"
      "ds_write_b64 v0, v[48:49] offset:128\n\t"
      "v_mfma_f32_16x16x32_bf16 v[16:19], v[64:67], a[0:3], 0\n\t"
      "ds_read_b128 a[24:27], v54 offset:256\n\t"
      "v_mfma_f32_16x16x32_bf16 v[16:19], v[80:83], a[4:7], v[16:19]\n\t"
      "ds_read_b128 a[28:31], v55 offset:256\n\t"
      "v_mfma_f32_16x16x32_bf16 v[16:19], v[96:99], a[8:11], v[16:19]\n\t"
      "ds_read_b128 a[32:35], v52 offset:512\n\t"
      "v_mfma_f32_16x16x32_bf16 v[16:19], v[112:115], a[12:15], v[16:19]\n\t"
      "ds_read_b128 a[36:39], v53 offset:512\n\t"
      "v_mfma_f32_16x16x32_bf16 v[16:19], v[128:131], a[16:19], v[16:19]\n\t"
      "ds_read_b128 a[40:43], v54 offset:512\n\t"
      "v_mfma_f32_16x16x32_bf16 v[16:19], v[144:147], a[20:23], v[16:19]\n\t"
      "ds_read_b128 a[44:47], v55 offset:512\n\t"
      "v_mfma_f32_16x16x32_bf16 v[20:23], v[68:71], a[0:3], 0\n\t"
      "v_cvt_pk_bf16_f32 v50, v36, v37\n\t"
      "v_cvt_pk_bf16_f32 v51, v38, v39\n\t"
      "v_mfma_f32_16x16x32_bf16 v[20:23], v[84:87], a[4:7], v[20:23]\n\t"
      "ds_write_b64 v0, v[50:51] offset:160\n\t"
      "v_mfma_f32_16x16x32_bf16 v[20:23], v[100:103], a[8:11], v[20:23]\n\t"
      "v_add_u32 v56, s51, v15\n\t"
      "v_xor_b32 v57, 64, v56\n\t"
      "v_mfma_f32_16x16x32_bf16 v[20:23], v[116:119], a[12:15], v[20:23]\n\t"
      "v_xor_b32 v58, 128, v56\n\t"
      "v_xor_b32 v59, 192, v56\n\t"
      "v_mfma_f32_16x16x32_bf16 v[20:23], v[132:135], a[16:19], v[20:23]\n\t"
      "buffer_store_dwordx4 a[188:191], v12, s[44:47], s49 offen sc1\n\t"
      "v_mfma_f32_16x16x32_bf16 v[20:23], v[148:151], a[20:23], v[20:23]\n\t"
      "v_mfma_f32_16x16x32_bf16 v[24:27], v[72:75], a[0:3], 0\n\t"
      "v_mfma_f32_16x16x32_bf16 v[24:27], v[88:91], a[4:7], v[24:27]\n\t"
      "v_mfma_f32_16x16x32_bf16 v[24:27], v[104:107], a[8:11], v[24:27]\n\t"
      "s_waitcnt vmcnt(7)\n\t"
      "s_waitcnt lgkmcnt(0)\n\t"
      "s_barrier\n\t"
      "v_mfma_f32_16x16x32_bf16 v[24:27], v[120:123], a[12:15], v[24:27]\n\t"
      "v_mfma_f32_16x16x32_bf16 v[24:27], v[136:139], a[16:19], v[24:27]\n\t"
      "v_mfma_f32_16x16x32_bf16 v[24:27], v[152:155], a[20:23], v[24:27]\n\t"
      "v_mfma_f32_16x16x32_bf16 v[28:31], v[76:79], a[0:3], 0\n\t"
      "ds_read_b128 a[200:203], v8 offset:0\n\t"
      "v_mfma_f32_16x16x32_bf16 v[28:31], v[92:95], a[4:7], v[28:31]\n\t"
      "ds_read_b128 a[204:207], v60 offset:1024\n\t"
      "v_mfma_f32_16x16x32_bf16 v[28:31], v[108:111], a[8:11], v[28:31]\n\t"
      "ds_read_b128 a[208:211], v61 offset:2048\n\t"
      "v_mfma_f32_16x16x32_bf16 v[28:31], v[124:127], a[12:15], v[28:31]\n\t"
      "v_mfma_f32_16x16x32_bf16 v[28:31], v[140:143], a[16:19], v[28:31]\n\t"
      "buffer_store_dwordx4 a[192:195], v13, s[44:47], s49 offen offset:1024 sc1\n\t"
      "v_mfma_f32_16x16x32_bf16 v[28:31], v[156:159], a[20:23], v[28:31]\n\t"
      "v_mfma_f32_16x16x32_bf16 v[32:35], a[60:63], a[0:3], 0\n\t"
      "v_mfma_f32_16x16x32_bf16 v[32:35], a[68:71], a[4:7], v[32:35]\n\t"
      "v_mfma_f32_16x16x32_bf16 v[32:35], a[76:79], a[8:11], v[32:35]\n\t"
      "v_mfma_f32_16x16x32_bf16 v[32:35], a[84:87], a[12:15], v[32:35]\n\t"
      "v_mfma_f32_16x16x32_bf16 v[32:35], a[92:95], a[16:19], v[32:35]\n\t"
      "v_mfma_f32_16x16x32_bf16 v[32:35], a[100:103], a[20:23], v[32:35]\n\t"
      "v_mfma_f32_16x16x32_bf16 v[36:39], a[64:67], a[0:3], 0\n\t"
      "buffer_store_dwordx4 a[196:199], v14, s[44:47], s49 offen offset:2048 sc1\n\t"
      "v_mfma_f32_16x16x32_bf16 v[36:39], a[72:75], a[4:7], v[36:39]\n\t"
      "v_mfma_f32_16x16x32_bf16 v[36:39], a[80:83], a[8:11], v[36:39]\n\t"
      "v_mfma_f32_16x16x32_bf16 v[36:39], a[88:91], a[12:15], v[36:39]\n\t"
      "v_mfma_f32_16x16x32_bf16 v[36:39], a[96:99], a[16:19], v[36:39]\n\t"
      "v_mfma_f32_16x16x32_bf16 v[36:39], a[104:107], a[20:23], v[36:39]\n\t"
      "v_mfma_f32_16x16x32_bf16 v[16:19], v[160:163], a[24:27], v[16:19]\n\t"
      "ds_read_b128 a[0:3], v56 offset:0\n\t"
      "v_mfma_f32_16x16x32_bf16 v[16:19], v[176:179], a[28:31], v[16:19]\n\t"
      "ds_read_b128 a[4:7], v57 offset:0\n\t"
      "v_mfma_f32_16x16x32_bf16 v[16:19], v[192:195], a[32:35], v[16:19]\n\t"
      "ds_read_b128 a[8:11], v58 offset:0\n\t"
      "v_mfma_f32_16x16x32_bf16 v[16:19], v[208:211], a[36:39], v[16:19]\n\t"
      "ds_read_b128 a[12:15], v59 offset:0\n\t"
      "v_mfma_f32_16x16x32_bf16 v[16:19], v[224:227], a[40:43], v[16:19]\n\t"
      "ds_read_b128 a[16:19], v56 offset:256\n\t"
      "v_mfma_f32_16x16x32_bf16 v[16:19], v[240:243], a[44:47], v[16:19]\n\t"
      "ds_read_b128 a[20:23], v57 offset:256\n\t"
      "v_mfma_f32_16x16x32_bf16 v[20:23], v[164:167], a[24:27], v[20:23]\n\t"
      "s_add_u32 s51, s51, 12288\n\t"
      "s_cmp_eq_u32 s51, 160256\n\t"
      "s_cselect_b32 s51, 25088, s51\n\t"
      "v_mfma_f32_16x16x32_bf16 v[20:23], v[180:183], a[28:31], v[20:23]\n\t"
      "s_waitcnt lgkmcnt(8)\n\t"
      "v_mfma_f32_16x16x32_bf16 v[20:23], v[196:199], a[32:35], v[20:23]\n\t"
      "buffer_store_dwordx4 a[200:203], v9, s[44:47], s84 offen sc1\n\t"
      "v_mfma_f32_16x16x32_bf16 v[20:23], v[212:215], a[36:39], v[20:23]\n\t"
      "v_mfma_f32_16x16x32_bf16 v[20:23], v[228:231], a[40:43], v[20:23]\n\t"
      "s_waitcnt lgkmcnt(0)\n\t"
      "v_mfma_f32_16x16x32_bf16 v[20:23], v[244:247], a[44:47], v[20:23]\n\t"
      "v_mfma_f32_16x16x32_bf16 v[24:27], v[168:171], a[24:27], v[24:27]\n\t"
      "v_mfma_f32_16x16x32_bf16 v[24:27], v[184:187], a[28:31], v[24:27]\n\t"
      "v_cvt_pk_bf16_f32 v40, v16, v17\n\t"
      "v_cvt_pk_bf16_f32 v41, v18, v19\n\t"
      "v_mfma_f32_16x16x32_bf16 v[24:27], v[200:203], a[32:35], v[24:27]\n\t"
      "ds_write_b64 v0, v[40:41] offset:12544\n\t"
      "v_mfma_f32_16x16x32_bf16 v[24:27], v[216:219], a[36:39], v[24:27]\n\t"
      "v_mfma_f32_16x16x32_bf16 v[24:27], v[232:235], a[40:43], v[24:27]\n\t"
      "buffer_store_dwordx4 a[204:207], v10, s[44:47], s84 offen offset:1024 sc1\n\t"
      "v_mfma_f32_16x16x32_bf16 v[24:27], v[248:251], a[44:47], v[24:27]\n\t"
      "v_mfma_f32_16x16x32_bf16 v[28:31], v[172:175], a[24:27], v[28:31]\n\t"
      "v_mfma_f32_16x16x32_bf16 v[28:31], v[188:191], a[28:31], v[28:31]\n\t"
      "v_cvt_pk_bf16_f32 v42, v20, v21\n\t"
      "v_cvt_pk_bf16_f32 v43, v22, v23\n\t"
      "v_mfma_f32_16x16x32_bf16 v[28:31], v[204:207], a[32:35], v[28:31]\n\t"
      "ds_write_b64 v0, v[42:43] offset:12576\n\t"
      "v_mfma_f32_16x16x32_bf16 v[28:31], v[220:223], a[36:39], v[28:31]\n\t"
      "v_mfma_f32_16x16x32_bf16 v[28:31], v[236:239], a[40:43], v[28:31]\n\t"
      "buffer_store_dwordx4 a[208:211], v11, s[44:47], s84 offen offset:2048 sc1\n\t"
      "v_mfma_f32_16x16x32_bf16 v[28:31], v[252:255], a[44:47], v[28:31]\n\t"
      "v_mfma_f32_16x16x32_bf16 v[32:35], a[108:111], a[24:27], v[32:35]\n\t"
      "v_mfma_f32_16x16x32_bf16 v[32:35], a[116:119], a[28:31], v[32:35]\n\t"
      "v_cvt_pk_bf16_f32 v44, v24, v25\n\t"
      "v_cvt_pk_bf16_f32 v45, v26, v27\n\t"
      "v_mfma_f32_16x16x32_bf16 v[32:35], a[124:127], a[32:35], v[32:35]\n\t"
      "ds_write_b64 v0, v[44:45] offset:12608\n\t"
      "v_mfma_f32_16x16x32_bf16 v[32:35], a[132:135], a[36:39], v[32:35]\n\t"
      "v_mfma_f32_16x16x32_bf16 v[32:35], a[140:143], a[40:43], v[32:35]\n\t"
      "v_mfma_f32_16x16x32_bf16 v[32:35], a[148:151], a[44:47], v[32:35]\n\t"
      "v_cvt_pk_bf16_f32 v46, v28, v29\n\t"
      "v_cvt_pk_bf16_f32 v47, v30, v31\n\t"
      "v_mfma_f32_16x16x32_bf16 v[36:39], a[112:115], a[24:27], v[36:39]\n\t"
      "ds_write_b64 v0, v[46:47] offset:12640\n\t"
      "v_mfma_f32_16x16x32_bf16 v[36:39], a[120:123], a[28:31], v[36:39]\n\t"
      "v_mfma_f32_16x16x32_bf16 v[36:39], a[128:131], a[32:35], v[36:39]\n\t"
      "v_mfma_f32_16x16x32_bf16 v[36:39], a[136:139], a[36:39], v[36:39]\n\t"
      "v_mfma_f32_16x16x32_bf16 v[36:39], a[144:147], a[40:43], v[36:39]\n\t"
      "v_cvt_pk_bf16_f32 v48, v32, v33\n\t"
      "v_cvt_pk_bf16_f32 v49, v34, v35\n\t"
      "v_mfma_f32_16x16x32_bf16 v[36:39], a[152:155], a[44:47], v[36:39]\n\t"
      "ds_write_b64 v0, v[48:49] offset:12672\n\t"
      "v_mfma_f32_16x16x32_bf16 v[16:19], v[64:67], a[0:3], 0\n\t"
      "ds_read_b128 a[24:27], v58 offset:256\n\t"
      "v_mfma_f32_16x16x32_bf16 v[16:19], v[80:83], a[4:7], v[16:19]\n\t"
      "ds_read_b128 a[28:31], v59 offset:256\n\t"
      "v_mfma_f32_16x16x32_bf16 v[16:19], v[96:99], a[8:11], v[16:19]\n\t"
      "ds_read_b128 a[32:35], v56 offset:512\n\t"
      "v_mfma_f32_16x16x32_bf16 v[16:19], v[112:115], a[12:15], v[16:19]\n\t"
      "ds_read_b128 a[36:39], v57 offset:512\n\t"
      "v_mfma_f32_16x16x32_bf16 v[16:19], v[128:131], a[16:19], v[16:19]\n\t"
      "ds_read_b128 a[40:43], v58 offset:512\n\t"
      "v_mfma_f32_16x16x32_bf16 v[16:19], v[144:147], a[20:23], v[16:19]\n\t"
      "ds_read_b128 a[44:47], v59 offset:512\n\t"
      "v_mfma_f32_16x16x32_bf16 v[20:23], v[68:71], a[0:3], 0\n\t"
      "v_cvt_pk_bf16_f32 v50, v36, v37\n\t"
      "v_cvt_pk_bf16_f32 v51, v38, v39\n\t"
      "v_mfma_f32_16x16x32_bf16 v[20:23], v[84:87], a[4:7], v[20:23]\n\t"
      "ds_write_b64 v0, v[50:51] offset:12704\n\t"
      "v_mfma_f32_16x16x32_bf16 v[20:23], v[100:103], a[8:11], v[20:23]\n\t"
      "v_add_u32 v52, s51, v15\n\t"
      "v_xor_b32 v53, 64, v52\n\t"
      "v_mfma_f32_16x16x32_bf16 v[20:23], v[116:119], a[12:15], v[20:23]\n\t"
      "v_xor_b32 v54, 128, v52\n\t"
      "v_xor_b32 v55, 192, v52\n\t"
      "v_mfma_f32_16x16x32_bf16 v[20:23], v[132:135], a[16:19], v[20:23]\n\t"
      "v_mfma_f32_16x16x32_bf16 v[20:23], v[148:151], a[20:23], v[20:23]\n\t"
      "v_mfma_f32_16x16x32_bf16 v[24:27], v[72:75], a[0:3], 0\n\t"
      "v_mfma_f32_16x16x32_bf16 v[24:27], v[88:91], a[4:7], v[24:27]\n\t"
      "v_mfma_f32_16x16x32_bf16 v[24:27], v[104:107], a[8:11], v[24:27]\n\t"
      "s_waitcnt vmcnt(6)\n\t"
      "s_waitcnt lgkmcnt(0)\n\t"
      "s_barrier\n\t"
      "v_mfma_f32_16x16x32_bf16 v[24:27], v[120:123], a[12:15], v[24:27]\n\t"
      "v_mfma_f32_16x16x32_bf16 v[24:27], v[136:139], a[16:19], v[24:27]\n\t"
      "v_mfma_f32_16x16x32_bf16 v[24:27], v[152:155], a[20:23], v[24:27]\n\t"
      "v_mfma_f32_16x16x32_bf16 v[28:31], v[76:79], a[0:3], 0\n\t"
      "ds_read_b128 a[212:215], v8 offset:12544\n\t"
      "v_mfma_f32_16x16x32_bf16 v[28:31], v[92:95], a[4:7], v[28:31]\n\t"
      "ds_read_b128 a[216:219], v60 offset:13568\n\t"
      "v_mfma_f32_16x16x32_bf16 v[28:31], v[108:111], a[8:11], v[28:31]\n\t"
      "ds_read_b128 a[220:223], v61 offset:14592\n\t"
      "v_mfma_f32_16x16x32_bf16 v[28:31], v[124:127], a[12:15], v[28:31]\n\t"
      "v_mfma_f32_16x16x32_bf16 v[28:31], v[140:143], a[16:19], v[28:31]\n\t"
      "v_mfma_f32_16x16x32_bf16 v[28:31], v[156:159], a[20:23], v[28:31]\n\t"
      "v_mfma_f32_16x16x32_bf16 v[32:35], a[60:63], a[0:3], 0\n\t"
      "v_mfma_f32_16x16x32_bf16 v[32:35], a[68:71], a[4:7], v[32:35]\n\t"
      "s_waitcnt lgkmcnt(2)\n\t"
      "v_mfma_f32_16x16x32_bf16 v[32:35], a[76:79], a[8:11], v[32:35]\n\t"
      "buffer_store_dwordx4 a[212:215], v12, s[44:47], s84 offen sc1\n\t"
      "v_mfma_f32_16x16x32_bf16 v[32:35], a[84:87], a[12:15], v[32:35]\n\t"
      "v_mfma_f32_16x16x32_bf16 v[32:35], a[92:95], a[16:19], v[32:35]\n\t"
      "v_mfma_f32_16x16x32_bf16 v[32:35], a[100:103], a[20:23], v[32:35]\n\t"
      "v_mfma_f32_16x16x32_bf16 v[36:39], a[64:67], a[0:3], 0\n\t"
      "v_mfma_f32_16x16x32_bf16 v[36:39], a[72:75], a[4:7], v[36:39]\n\t"
      "v_mfma_f32_16x16x32_bf16 v[36:39], a[80:83], a[8:11], v[36:39]\n\t"
      "v_mfma_f32_16x16x32_bf16 v[36:39], a[88:91], a[12:15], v[36:39]\n\t"
      "s_waitcnt lgkmcnt(1)\n\t"
      "v_mfma_f32_16x16x32_bf16 v[36:39], a[96:99], a[16:19], v[36:39]\n\t"
      "buffer_store_dwordx4 a[216:219], v13, s[44:47], s84 offen offset:1024 sc1\n\t"
      "v_mfma_f32_16x16x32_bf16 v[36:39], a[104:107], a[20:23], v[36:39]\n\t"
      "v_mfma_f32_16x16x32_bf16 v[16:19], v[160:163], a[24:27], v[16:19]\n\t"
      "ds_read_b128 a[0:3], v52 offset:0\n\t"
      "v_mfma_f32_16x16x32_bf16 v[16:19], v[176:179], a[28:31], v[16:19]\n\t"
      "ds_read_b128 a[4:7], v53 offset:0\n\t"
      "v_mfma_f32_16x16x32_bf16 v[16:19], v[192:195], a[32:35], v[16:19]\n\t"
      "ds_read_b128 a[8:11], v54 offset:0\n\t"
      "v_mfma_f32_16x16x32_bf16 v[16:19], v[208:211], a[36:39], v[16:19]\n\t"
      "ds_read_b128 a[12:15], v55 offset:0\n\t"
      "v_mfma_f32_16x16x32_bf16 v[16:19], v[224:227], a[40:43], v[16:19]\n\t"
      "ds_read_b128 a[16:19], v52 offset:256\n\t"
      "v_mfma_f32_16x16x32_bf16 v[16:19], v[240:243], a[44:47], v[16:19]\n\t"
      "ds_read_b128 a[20:23], v53 offset:256\n\t"
      "v_mfma_f32_16x16x32_bf16 v[20:23], v[164:167], a[24:27], v[20:23]\n\t"
      "s_add_u32 s51, s51, 12288\n\t"
      "s_cmp_eq_u32 s51, 160256\n\t"
      "s_cselect_b32 s51, 25088, s51\n\t"
      "v_mfma_f32_16x16x32_bf16 v[20:23], v[180:183], a[28:31], v[20:23]\n\t"
      "s_waitcnt lgkmcnt(6)\n\t"
      "v_mfma_f32_16x16x32_bf16 v[20:23], v[196:199], a[32:35], v[20:23]\n\t"
      "buffer_store_dwordx4 a[220:223], v14, s[44:47], s84 offen offset:2048 sc1\n\t"
      "v_mfma_f32_16x16x32_bf16 v[20:23], v[212:215], a[36:39], v[20:23]\n\t"
      "v_mfma_f32_16x16x32_bf16 v[20:23], v[228:231], a[40:43], v[20:23]\n\t"
      "s_waitcnt lgkmcnt(0)\n\t"
      "v_mfma_f32_16x16x32_bf16 v[20:23], v[244:247], a[44:47], v[20:23]\n\t"
      "v_mfma_f32_16x16x32_bf16 v[24:27], v[168:171], a[24:27], v[24:27]\n\t"
      "v_mfma_f32_16x16x32_bf16 v[24:27], v[184:187], a[28:31], v[24:27]\n\t"
      "v_cvt_pk_bf16_f32 v40, v16, v17\n\t"
      "v_cvt_pk_bf16_f32 v41, v18, v19\n\t"
      "v_mfma_f32_16x16x32_bf16 v[24:27], v[200:203], a[32:35], v[24:27]\n\t"
      "ds_write_b64 v0, v[40:41] offset:0\n\t"
      "v_mfma_f32_16x16x32_bf16 v[24:27], v[216:219], a[36:39], v[24:27]\n\t"
      "v_mfma_f32_16x16x32_bf16 v[24:27], v[232:235], a[40:43], v[24:27]\n\t"
      "v_mfma_f32_16x16x32_bf16 v[24:27], v[248:251], a[44:47], v[24:27]\n\t"
      "v_mfma_f32_16x16x32_bf16 v[28:31], v[172:175], a[24:27], v[28:31]\n\t"
      "v_mfma_f32_16x16x32_bf16 v[28:31], v[188:191], a[28:31], v[28:31]\n\t"
      "v_cvt_pk_bf16_f32 v42, v20, v21\n\t"
      "v_cvt_pk_bf16_f32 v43, v22, v23\n\t"
      "v_mfma_f32_16x16x32_bf16 v[28:31], v[204:207], a[32:35], v[28:31]\n\t"
      "ds_write_b64 v0, v[42:43] offset:32\n\t"
      "v_mfma_f32_16x16x32_bf16 v[28:31], v[220:223], a[36:39], v[28:31]\n\t"
      "v_mfma_f32_16x16x32_bf16 v[28:31], v[236:239], a[40:43], v[28:31]\n\t"
      "v_mfma_f32_16x16x32_bf16 v[28:31], v[252:255], a[44:47], v[28:31]\n\t"
      "v_mfma_f32_16x16x32_bf16 v[32:35], a[108:111], a[24:27], v[32:35]\n\t"
      "v_mfma_f32_16x16x32_bf16 v[32:35], a[116:119], a[28:31], v[32:35]\n\t"
      "v_cvt_pk_bf16_f32 v44, v24, v25\n\t"
      "v_cvt_pk_bf16_f32 v45, v26, v27\n\t"
      "v_mfma_f32_16x16x32_bf16 v[32:35], a[124:127], a[32:35], v[32:35]\n\t"
      "ds_write_b64 v0, v[44:45] offset:64\n\t"
      "v_mfma_f32_16x16x32_bf16 v[32:35], a[132:135], a[36:39], v[32:35]\n\t"
      "v_mfma_f32_16x16x32_bf16 v[32:35], a[140:143], a[40:43], v[32:35]\n\t"
      "v_mfma_f32_16x16x32_bf16 v[32:35], a[148:151], a[44:47], v[32:35]\n\t"
      "v_cvt_pk_bf16_f32 v46, v28, v29\n\t"
      "v_cvt_pk_bf16_f32 v47, v30, v31\n\t"
      "v_mfma_f32_16x16x32_bf16 v[36:39], a[112:115], a[24:27], v[36:39]\n\t"
      "ds_write_b64 v0, v[46:47] offset:96\n\t"
      "v_mfma_f32_16x16x32_bf16 v[36:39], a[120:123], a[28:31], v[36:39]\n\t"
      "v_mfma_f32_16x16x32_bf16 v[36:39], a[128:131], a[32:35], v[36:39]\n\t"
      "v_mfma_f32_16x16x32_bf16 v[36:39], a[136:139], a[36:39], v[36:39]\n\t"
      "v_mfma_f32_16x16x32_bf16 v[36:39], a[144:147], a[40:43], v[36:39]\n\t"
      "v_cvt_pk_bf16_f32 v48, v32, v33\n\t"
      "v_cvt_pk_bf16_f32 v49, v34, v35\n\t"
      "v_mfma_f32_16x16x32_bf16 v[36:39], a[152:155], a[44:47], v[36:39]\n\t"
      "ds_write_b64 v0, v[48:49] offset:128\n\t"
      "v_mfma_f32_16x16x32_bf16 v[16:19], v[64:67], a[0:3], 0\n\t"
      "ds_read_b128 a[24:27], v54 offset:256\n\t"
      "v_mfma_f32_16x16x32_bf16 v[16:19], v[80:83], a[4:7], v[16:19]\n\t"
      "ds_read_b128 a[28:31], v55 offset:256\n\t"
      "v_mfma_f32_16x16x32_bf16 v[16:19], v[96:99], a[8:11], v[16:19]\n\t"
      "ds_read_b128 a[32:35], v52 offset:512\n\t"
      "v_mfma_f32_16x16x32_bf16 v[16:19], v[112:115], a[12:15], v[16:19]\n\t"
      "ds_read_b128 a[36:39], v53 offset:512\n\t"
      "v_mfma_f32_16x16x32_bf16 v[16:19], v[128:131], a[16:19], v[16:19]\n\t"
      "ds_read_b128 a[40:43], v54 offset:512\n\t"
      "v_mfma_f32_16x16x32_bf16 v[16:19], v[144:147], a[20:23], v[16:19]\n\t"
      "ds_read_b128 a[44:47], v55 offset:512\n\t"
      "v_mfma_f32_16x16x32_bf16 v[20:23], v[68:71], a[0:3], 0\n\t"
      "v_cvt_pk_bf16_f32 v50, v36, v37\n\t"
      "v_cvt_pk_bf16_f32 v51, v38, v39\n\t"
      "v_mfma_f32_16x16x32_bf16 v[20:23], v[84:87], a[4:7], v[20:23]\n\t"
      "ds_write_b64 v0, v[50:51] offset:160\n\t"
      "v_mfma_f32_16x16x32_bf16 v[20:23], v[100:103], a[8:11], v[20:23]\n\t"
      "v_mfma_f32_16x16x32_bf16 v[20:23], v[116:119], a[12:15], v[20:23]\n\t"
      "v_mfma_f32_16x16x32_bf16 v[20:23], v[132:135], a[16:19], v[20:23]\n\t"
      "v_mfma_f32_16x16x32_bf16 v[20:23], v[148:151], a[20:23], v[20:23]\n\t"
      "v_mfma_f32_16x16x32_bf16 v[24:27], v[72:75], a[0:3], 0\n\t"
      "v_mfma_f32_16x16x32_bf16 v[24:27], v[88:91], a[4:7], v[24:27]\n\t"
      "v_mfma_f32_16x16x32_bf16 v[24:27], v[104:107], a[8:11], v[24:27]\n\t"
      "s_waitcnt lgkmcnt(0)\n\t"
      "s_barrier\n\t"
      "v_mfma_f32_16x16x32_bf16 v[24:27], v[120:123], a[12:15], v[24:27]\n\t"
      "v_mfma_f32_16x16x32_bf16 v[24:27], v[136:139], a[16:19], v[24:27]\n\t"
      "v_mfma_f32_16x16x32_bf16 v[24:27], v[152:155], a[20:23], v[24:27]\n\t"
      "v_mfma_f32_16x16x32_bf16 v[28:31], v[76:79], a[0:3], 0\n\t"
      "ds_read_b128 a[224:227], v8 offset:0\n\t"
      "v_mfma_f32_16x16x32_bf16 v[28:31], v[92:95], a[4:7], v[28:31]\n\t"
      "ds_read_b128 a[228:231], v60 offset:1024\n\t"
      "v_mfma_f32_16x16x32_bf16 v[28:31], v[108:111], a[8:11], v[28:31]\n\t"
      "ds_read_b128 a[232:235], v61 offset:2048\n\t"
      "v_mfma_f32_16x16x32_bf16 v[28:31], v[124:127], a[12:15], v[28:31]\n\t"
      "v_mfma_f32_16x16x32_bf16 v[28:31], v[140:143], a[16:19], v[28:31]\n\t"
      "v_mfma_f32_16x16x32_bf16 v[28:31], v[156:159], a[20:23], v[28:31]\n\t"
      "v_mfma_f32_16x16x32_bf16 v[32:35], a[60:63], a[0:3], 0\n\t"
      "v_mfma_f32_16x16x32_bf16 v[32:35], a[68:71], a[4:7], v[32:35]\n\t"
      "s_waitcnt lgkmcnt(2)\n\t"
      "v_mfma_f32_16x16x32_bf16 v[32:35], a[76:79], a[8:11], v[32:35]\n\t"
      "buffer_store_dwordx4 a[224:227], v9, s[44:47], s85 offen sc1\n\t"
      "v_mfma_f32_16x16x32_bf16 v[32:35], a[84:87], a[12:15], v[32:35]\n\t"
      "v_mfma_f32_16x16x32_bf16 v[32:35], a[92:95], a[16:19], v[32:35]\n\t"
      "v_mfma_f32_16x16x32_bf16 v[32:35], a[100:103], a[20:23], v[32:35]\n\t"
      "v_mfma_f32_16x16x32_bf16 v[36:39], a[64:67], a[0:3], 0\n\t"
      "v_mfma_f32_16x16x32_bf16 v[36:39], a[72:75], a[4:7], v[36:39]\n\t"
      "v_mfma_f32_16x16x32_bf16 v[36:39], a[80:83], a[8:11], v[36:39]\n\t"
      "v_mfma_f32_16x16x32_bf16 v[36:39], a[88:91], a[12:15], v[36:39]\n\t"
      "s_waitcnt lgkmcnt(1)\n\t"
      "v_mfma_f32_16x16x32_bf16 v[36:39], a[96:99], a[16:19], v[36:39]\n\t"
      "buffer_store_dwordx4 a[228:231], v10, s[44:47], s85 offen offset:1024 sc1\n\t"
      "v_mfma_f32_16x16x32_bf16 v[36:39], a[104:107], a[20:23], v[36:39]\n\t"
      "v_mfma_f32_16x16x32_bf16 v[16:19], v[160:163], a[24:27], v[16:19]\n\t"
      "v_mfma_f32_16x16x32_bf16 v[16:19], v[176:179], a[28:31], v[16:19]\n\t"
      "v_mfma_f32_16x16x32_bf16 v[16:19], v[192:195], a[32:35], v[16:19]\n\t"
      "v_mfma_f32_16x16x32_bf16 v[16:19], v[208:211], a[36:39], v[16:19]\n\t"
      "v_mfma_f32_16x16x32_bf16 v[16:19], v[224:227], a[40:43], v[16:19]\n\t"
      "v_mfma_f32_16x16x32_bf16 v[16:19], v[240:243], a[44:47], v[16:19]\n\t"
      "v_mfma_f32_16x16x32_bf16 v[20:23], v[164:167], a[24:27], v[20:23]\n\t"
      "v_mfma_f32_16x16x32_bf16 v[20:23], v[180:183], a[28:31], v[20:23]\n\t"
      "s_waitcnt lgkmcnt(0)\n\t"
      "v_mfma_f32_16x16x32_bf16 v[20:23], v[196:199], a[32:35], v[20:23]\n\t"
      "buffer_store_dwordx4 a[232:235], v11, s[44:47], s85 offen offset:2048 sc1\n\t"
      "v_mfma_f32_16x16x32_bf16 v[20:23], v[212:215], a[36:39], v[20:23]\n\t"
      "v_mfma_f32_16x16x32_bf16 v[20:23], v[228:231], a[40:43], v[20:23]\n\t"
      "v_mfma_f32_16x16x32_bf16 v[20:23], v[244:247], a[44:47], v[20:23]\n\t"
      "v_mfma_f32_16x16x32_bf16 v[24:27], v[168:171], a[24:27], v[24:27]\n\t"
      "v_mfma_f32_16x16x32_bf16 v[24:27], v[184:187], a[28:31], v[24:27]\n\t"
      "v_cvt_pk_bf16_f32 v40, v16, v17\n\t"
      "v_cvt_pk_bf16_f32 v41, v18, v19\n\t"
      "v_mfma_f32_16x16x32_bf16 v[24:27], v[200:203], a[32:35], v[24:27]\n\t"
      "ds_write_b64 v0, v[40:41] offset:12544\n\t"
      "v_mfma_f32_16x16x32_bf16 v[24:27], v[216:219], a[36:39], v[24:27]\n\t"
      "v_mfma_f32_16x16x32_bf16 v[24:27], v[232:235], a[40:43], v[24:27]\n\t"
      "v_mfma_f32_16x16x32_bf16 v[24:27], v[248:251], a[44:47], v[24:27]\n\t"
      "v_mfma_f32_16x16x32_bf16 v[28:31], v[172:175], a[24:27], v[28:31]\n\t"
      "v_mfma_f32_16x16x32_bf16 v[28:31], v[188:191], a[28:31], v[28:31]\n\t"
      "v_cvt_pk_bf16_f32 v42, v20, v21\n\t"
      "v_cvt_pk_bf16_f32 v43, v22, v23\n\t"
      "v_mfma_f32_16x16x32_bf16 v[28:31], v[204:207], a[32:35], v[28:31]\n\t"
      "ds_write_b64 v0, v[42:43] offset:12576\n\t"
      "v_mfma_f32_16x16x32_bf16 v[28:31], v[220:223], a[36:39], v[28:31]\n\t"
      "v_mfma_f32_16x16x32_bf16 v[28:31], v[236:239], a[40:43], v[28:31]\n\t"
      "v_mfma_f32_16x16x32_bf16 v[28:31], v[252:255], a[44:47], v[28:31]\n\t"
      "v_mfma_f32_16x16x32_bf16 v[32:35], a[108:111], a[24:27], v[32:35]\n\t"
      "v_mfma_f32_16x16x32_bf16 v[32:35], a[116:119], a[28:31], v[32:35]\n\t"
      "v_cvt_pk_bf16_f32 v44, v24, v25\n\t"
      "v_cvt_pk_bf16_f32 v45, v26, v27\n\t"
      "v_mfma_f32_16x16x32_bf16 v[32:35], a[124:127], a[32:35], v[32:35]\n\t"
      "ds_write_b64 v0, v[44:45] offset:12608\n\t"
      "v_mfma_f32_16x16x32_bf16 v[32:35], a[132:135], a[36:39], v[32:35]\n\t"
      "v_mfma_f32_16x16x32_bf16 v[32:35], a[140:143], a[40:43], v[32:35]\n\t"
      "v_mfma_f32_16x16x32_bf16 v[32:35], a[148:151], a[44:47], v[32:35]\n\t"
      "v_cvt_pk_bf16_f32 v46, v28, v29\n\t"
      "v_cvt_pk_bf16_f32 v47, v30, v31\n\t"
      "v_mfma_f32_16x16x32_bf16 v[36:39], a[112:115], a[24:27], v[36:39]\n\t"
      "ds_write_b64 v0, v[46:47] offset:12640\n\t"
      "v_mfma_f32_16x16x32_bf16 v[36:39], a[120:123], a[28:31], v[36:39]\n\t"
      "v_mfma_f32_16x16x32_bf16 v[36:39], a[128:131], a[32:35], v[36:39]\n\t"
      "v_mfma_f32_16x16x32_bf16 v[36:39], a[136:139], a[36:39], v[36:39]\n\t"
      "v_mfma_f32_16x16x32_bf16 v[36:39], a[144:147], a[40:43], v[36:39]\n\t"
      "v_cvt_pk_bf16_f32 v48, v32, v33\n\t"
      "v_cvt_pk_bf16_f32 v49, v34, v35\n\t"
      "v_mfma_f32_16x16x32_bf16 v[36:39], a[152:155], a[44:47], v[36:39]\n\t"
      "ds_write_b64 v0, v[48:49] offset:12672\n\t"
      ".Ltail_%=:\n\t"
      "s_nop 6\n\t"
      "v_cvt_pk_bf16_f32 v50, v36, v37\n\t"
      "v_cvt_pk_bf16_f32 v51, v38, v39\n\t"
      "ds_write_b64 v0, v[50:51] offset:12704\n\t"
      "s_cmp_eq_u32 s85, 113221632\n\t"
      "s_cbranch_scc1 .Lwrapo_%=\n\t"
      ".Lwrappedo_%=:\n\t"
      "s_waitcnt lgkmcnt(0)\n\t"
      "s_barrier\n\t"
      "ds_read_b128 a[48:51], v8 offset:12544\n\t"
      "ds_read_b128 a[52:55], v60 offset:13568\n\t"
      "ds_read_b128 a[56:59], v61 offset:14592\n\t"
      "s_waitcnt lgkmcnt(2)\n\t"
      "buffer_store_dwordx4 a[48:51], v12, s[44:47], s85 offen sc1\n\t"
      "s_waitcnt lgkmcnt(1)\n\t"
      "buffer_store_dwordx4 a[52:55], v13, s[44:47], s85 offen offset:1024 sc1\n\t"
      "s_waitcnt lgkmcnt(0)\n\t"
      "buffer_store_dwordx4 a[56:59], v14, s[44:47], s85 offen offset:2048 sc1\n\t"
      "s_cmp_lt_i32 s91, 0\n\t"
      "s_cselect_b32 s30, -1, 0\n\t"
      "s_xor_b32 s16, s91, 0x3ffff\n\t"
      "s_and_b32 s16, s16, s30\n\t"
      "s_cbranch_scc0 .Lend_%=\n\t"
      "s_waitcnt vmcnt(0) lgkmcnt(0)\n\t"
      "s_barrier\n\t"
      "s_lshl1_add_u32 s30, s14, 24576\n\t"
      "s_sub_u32 s15, s30, s15\n\t"
      "s_branch .Lstart_%=\n\t"
      ".Lwrap3_%=:\n\t"
      "v_add_u32 v5, s48, v5\n\t"
      "v_subrev_u32 v62, 113246208, v5\n\t"
      "v_min_u32 v5, v5, v62\n\t"
      "v_add_u32 v6, s48, v6\n\t"
      "v_subrev_u32 v62, 113246208, v6\n\t"
      "v_min_u32 v6, v6, v62\n\t"
      "v_add_u32 v7, s48, v7\n\t"
      "v_subrev_u32 v62, 113246208, v7\n\t"
      "v_min_u32 v7, v7, v62\n\t"
      "s_mov_b32 s48, 0\n\t"
      "buffer_load_dwordx4 v5, s[40:43], s48 offen nt lds\n\t"
      "buffer_load_dwordx4 v6, s[40:43], s48 offen offset:1024 nt lds\n\t"
      "buffer_load_dwordx4 v7, s[40:43], s48 offen offset:2048 nt lds\n\t"
      "s_branch .Lwrapped3_%=\n\t"
      ".Lwrapo_%=:\n\t"
      "v_add_u32 v12, s85, v12\n\t"
      "v_subrev_u32 v62, 113246208, v12\n\t"
      "v_min_u32 v12, v12, v62\n\t"
      "v_add_u32 v13, s85, v13\n\t"
      "v_subrev_u32 v62, 113246208, v13\n\t"
      "v_min_u32 v13, v13, v62\n\t"
      "v_add_u32 v14, s85, v14\n\t"
      "v_subrev_u32 v62, 113246208, v14\n\t"
      "v_min_u32 v14, v14, v62\n\t"
      "s_mov_b32 s85, 0\n\t"
      "s_branch .Lwrappedo_%=\n\t"
      ".Lwait_%=:\n\t"
      "s_sub_u32 s52, s52, 1\n\t"
      "s_cbranch_scc1 .Lwait2_%=\n\t"
      "s_add_u32 s90, s90, 0x20000\n\t"
      "v_mov_b32 v54, s90\n\t"
      "v_mov_b32 v55, s19\n\t"
      "global_store_dword v55, v54, s[12:13] sc1\n\t"
      "s_sleep 16\n\t"
      "global_load_dword v40, v62, s[12:13] sc1\n\t"
      "s_waitcnt vmcnt(0)\n\t"
      "s_branch .Lcheck_%=\n\t"
      ".Lwait2_%=:\n\t"
      "s_brev_b32 s91, 1\n\t"
      "s_branch .Lwaited_%=\n\t"
      ".Lspinf_%=:\n\t"
      "s_memrealtime s[36:37]\n\t"
      "s_waitcnt lgkmcnt(0)\n\t"
      "s_sub_u32 s53, s36, s38\n\t"
      "s_cmp_lt_i32 s53, 0\n\t"
      "s_cbranch_scc1 .Lspinf_%=\n\t"
      "s_branch .Lspunf_%=\n\t"
      ".Lspin0_%=:\n\t"
      "s_memrealtime s[36:37]\n\t"
      "s_waitcnt lgkmcnt(0)\n\t"
      "s_sub_u32 s53, s36, s38\n\t"
      "s_cmp_lt_i32 s53, 0\n\t"
      "s_cbranch_scc1 .Lspin0_%=\n\t"
      "s_branch .Lspun0_%=\n\t"
      ".Lspin1_%=:\n\t"
      "s_memrealtime s[36:37]\n\t"
      "s_waitcnt lgkmcnt(0)\n\t"
      "s_sub_u32 s53, s36, s38\n\t"
      "s_cmp_lt_i32 s53, 0\n\t"
      "s_cbranch_scc1 .Lspin1_%=\n\t"
      "s_branch .Lspun1_%=\n\t"
      ".Lend_%=:\n\t"
      : [tid] "+v"(tid)
      : [karg] "s"(__builtin_amdgcn_kernarg_segment_ptr()), [blk] "s"(blockIdx.x), [lds] "s"((unsigned)(size_t)lds)
      : "v1", "v2", "v3", "v4", "v5", "v6", "v7", "v8", "v9", "v10", "v11", "v12", "v13", "v14", "v15", "v16", "v17", "v18", "v19", "v20", "v21", "v22", "v23", "v24", "v25", "v26", "v27", "v28", "v29", "v30", "v31", "v32", "v33", "v34", "v35", "v36", "v37", "v38", "v39", "v40", "v41", "v42", "v43", "v44", "v45", "v46", "v47", "v48", "v49", "v50", "v51", "v52", "v53", "v54", "v55", "v56", "v57", "v58", "v59", "v60", "v61", "v62", "v63", "v64", "v65", "v66", "v67", "v68", "v69", "v70", "v71", "v72", "v73", "v74", "v75", "v76", "v77", "v78", "v79", "v80", "v81", "v82", "v83", "v84", "v85", "v86", "v87", "v88", "v89", "v90", "v91", "v92", "v93", "v94", "v95", "v96", "v97", "v98", "v99", "v100", "v101", "v102", "v103", "v104", "v105", "v106", "v107", "v108", "v109", "v110", "v111", "v112", "v113", "v114", "v115", "v116", "v117", "v118", "v119", "v120", "v121", "v122", "v123", "v124", "v125", "v126", "v127", "v128", "v129", "v130", "v131", "v132", "v133", "v134", "v135", "v136", "v137", "v138", "v139", "v140", "v141", "v142", "v143", "v144", "v145", "v146", "v147", "v148", "v149", "v150", "v151", "v152", "v153", "v154", "v155", "v156", "v157", "v158", "v159", "v160", "v161", "v162", "v163", "v164", "v165", "v166", "v167", "v168", "v169", "v170", "v171", "v172", "v173", "v174", "v175", "v176", "v177", "v178", "v179", "v180", "v181", "v182", "v183", "v184", "v185", "v186", "v187", "v188", "v189", "v190", "v191", "v192", "v193", "v194", "v195", "v196", "v197", "v198", "v199", "v200", "v201", "v202", "v203", "v204", "v205", "v206", "v207", "v208", "v209", "v210", "v211", "v212", "v213", "v214", "v215", "v216", "v217", "v218", "v219", "v220", "v221", "v222", "v223", "v224", "v225", "v226", "v227", "v228", "v229", "v230", "v231", "v232", "v233", "v234", "v235", "v236", "v237", "v238", "v239", "v240", "v241", "v242", "v243", "v244", "v245", "v246", "v247", "v248", "v249", "v250", "v251", "v252", "v253", "v254", "v255", "a0", "a1", "a2", "a3", "a4", "a5", "a6", "a7", "a8", "a9", "a10", "a11", "a12", "a13", "a14", "a15", "a16", "a17", "a18", "a19", "a20", "a21", "a22", "a23", "a24", "a25", "a26", "a27", "a28", "a29", "a30", "a31", "a32", "a33", "a34", "a35", "a36", "a37", "a38", "a39", "a40", "a41", "a42", "a43", "a44", "a45", "a46", "a47", "a48", "a49", "a50", "a51", "a52", "a53", "a54", "a55", "a56", "a57", "a58", "a59", "a60", "a61", "a62", "a63", "a64", "a65", "a66", "a67", "a68", "a69", "a70", "a71", "a72", "a73", "a74", "a75", "a76", "a77", "a78", "a79", "a80", "a81", "a82", "a83", "a84", "a85", "a86", "a87", "a88", "a89", "a90", "a91", "a92", "a93", "a94", "a95", "a96", "a97", "a98", "a99", "a100", "a101", "a102", "a103", "a104", "a105", "a106", "a107", "a108", "a109", "a110", "a111", "a112", "a113", "a114", "a115", "a116", "a117", "a118", "a119", "a120", "a121", "a122", "a123", "a124", "a125", "a126", "a127", "a128", "a129", "a130", "a131", "a132", "a133", "a134", "a135", "a136", "a137", "a138", "a139", "a140", "a141", "a142", "a143", "a144", "a145", "a146", "a147", "a148", "a149", "a150", "a151", "a152", "a153", "a154", "a155", "a156", "a157", "a158", "a159", "a160", "a161", "a162", "a163", "a164", "a165", "a166", "a167", "a168", "a169", "a170", "a171", "a172", "a173", "a174", "a175", "a176", "a177", "a178", "a179", "a180", "a181", "a182", "a183", "a184", "a185", "a186", "a187", "a188", "a189", "a190", "a191", "a192", "a193", "a194", "a195", "a196", "a197", "a198", "a199", "a200", "a201", "a202", "a203", "a204", "a205", "a206", "a207", "a208", "a209", "a210", "a211", "a212", "a213", "a214", "a215", "a216", "a217", "a218", "a219", "a220", "a221", "a222", "a223", "a224", "a225", "a226", "a227", "a228", "a229", "a230", "a231", "a232", "a233", "a234", "a235", "a236", "a237", "a238", "a239", "a240", "a241", "a242", "a243", "a244", "a245", "a246", "a247", "a248", "a249", "a250", "a251", "a252", "a253", "a254", "a255", "s4", "s5", "s6", "s7", "s8", "s9", "s10", "s11", "s12", "s13", "s14", "s15", "s16", "s17", "s18", "s19", "s20", "s21", "s22", "s23", "s24", "s25", "s26", "s27", "s28", "s29", "s30", "s31", "s32", "s33", "s34", "s35", "s36", "s37", "s38", "s39", "s40", "s41", "s42", "s43", "s44", "s45", "s46", "s47", "s48", "s49", "s50", "s51", "s52", "s53", "s54", "s55", "s56", "s57", "s58", "s59", "s60", "s61", "s62", "s63", "s64", "s65", "s66", "s67", "s68", "s69", "s70", "s71", "s72", "s73", "s74", "s75", "s76", "s77", "s78", "s79", "s80", "s81", "s82", "s83", "s84", "s85", "s86", "s87", "s88", "s89", "s90", "s91", "vcc", "scc", "memory");
}

__global__ __launch_bounds__(384) void dwsep_plain(const unsigned short* __restrict__ x, const unsigned short* __restrict__ dw,
                                                   const unsigned short* __restrict__ pw, unsigned short* __restrict__ out) {
  __shared__ float xs[384];
  const unsigned n = threadIdx.x;
  const size_t row = (size_t)blockIdx.x * 384u;
  xs[n] = __uint_as_float((unsigned)x[row + n] << 16) * __uint_as_float((unsigned)dw[n] << 16);
  __syncthreads();
  float acc = 0.f;
  for (int k = 0; k < 384; ++k) acc += xs[k] * __uint_as_float((unsigned)pw[k * 384 + n] << 16);
  const __bf16 o = (__bf16)acc;
  out[row + n] = __builtin_bit_cast(unsigned short, o);
}

extern "C" void kernel_launch(void* const* d_in, const int* in_sizes, int n_in, void* d_out, int out_size,
                              void* d_ws, size_t ws_size, hipStream_t stream) {
  const unsigned short* x = (const unsigned short*)d_in[0];
  const unsigned short* dw = (const unsigned short*)d_in[1];
  const unsigned short* pw = (const unsigned short*)d_in[2];
  unsigned short* out = (unsigned short*)d_out;
  if (in_sizes[0] == 56623104) dwsep_kernel<<<NWG, 256, 0, stream>>>(x, dw, pw, out, (unsigned*)d_ws);
  else dwsep_plain<<<in_sizes[0] / 384, 384, 0, stream>>>(x, dw, pw, out);
}
```

```cpp
#include <hip/hip_runtime.h>

constexpr unsigned ROWB = 768, TILEB = 12288, NWG = 256;

__global__ __launch_bounds__(256, 1) void dwsep_kernel(const unsigned short* __restrict__ x,
                                                       const unsigned short* __restrict__ dw,
                                                       const unsigned short* __restrict__ pw,
                                                       unsigned short* __restrict__ out, unsigned* ws) {
  __shared__ __attribute__((aligned(256))) char lds[163392];
  unsigned tid = threadIdx.x;
  asm volatile(
      "s_memtime s[32:33]\n\t"
      "s_load_dwordx8 s[4:11], %[karg], 0\n\t"
      "s_load_dwordx2 s[12:13], %[karg], 0x20\n\t"
      "v_readfirstlane_b32 s30, %[tid]\n\t"
      "s_lshr_b32 s31, %[blk], 1\n\t"
      "s_and_b32 s31, s31, 12\n\t"
      "s_and_b32 s34, %[blk], 3\n\t"
      "s_or_b32 s31, s31, s34\n\t"
      "s_lshr_b32 s30, s30, 6\n\t"
      "s_lshr_b32 s34, %[blk], 5\n\t"
      "s_bfe_u32 s35, %[blk], 0x10002\n\t"
      "s_mul_i32 s17, s30, 6144\n\t"
      "s_mul_i32 s18, s30, 3072\n\t"
      "s_mul_i32 s14, s34, 14155776\n\t"
      "s_mul_i32 s36, s31, 49152\n\t"
      "s_add_u32 s14, s14, s36\n\t"
      "s_mul_i32 s36, s35, 24576\n\t"
      "s_add_u32 s15, s14, s36\n\t"
      "s_mov_b32 s16, 0\n\t"
      "s_lshr_b32 s36, %[blk], 3\n\t"
      "s_lshl2_add_u32 s24, s36, s30\n\t"
      "s_sub_u32 s36, s24, s36\n\t"
      "s_sub_u32 s36, s36, 1\n\t"
      "s_mul_i32 s23, s36, 3072\n\t"
      "s_lshl_b32 s20, s34, 3\n\t"
      "s_mul_i32 s36, s34, 27\n\t"
      "s_lshr_b32 s36, s36, 2\n\t"
      "s_lshr_b32 s37, s31, 1\n\t"
      "s_add_u32 s19, s37, s20\n\t"
      "s_add_u32 s37, s37, s36\n\t"
      "s_min_u32 s37, s37, 53\n\t"
      "s_lshl_b32 s22, s37, 21\n\t"
      "s_lshl_b32 s36, s36, 21\n\t"
      "s_sub_u32 s21, s14, s36\n\t"
      "s_add_u32 s21, s21, 24576\n\t"
      "s_lshl_b32 s19, s19, 8\n\t"
      "s_and_b32 s31, s31, 1\n\t"
      "s_lshl1_add_u32 s31, s31, s35\n\t"
      "s_lshl2_add_u32 s19, s31, s19\n\t"
      ".Lstart_%=:\n\t"
      "v_mbcnt_lo_u32_b32 v233, -1, 0\n\t"
      "v_mbcnt_hi_u32_b32 v233, -1, v233\n\t"
      "s_mul_i32 s30, s17, 683\n\t"
      "s_lshr_b32 s30, s30, 16\n\t"
      "s_and_b32 s30, s30, 0xc0\n\t"
      "s_brev_b32 s34, 8\n\t"
      "s_mov_b32 s35, 0x20201010\n\t"
      "s_mov_b32 s36, 0x30303020\n\t"
      "v_lshl_add_u32 v1, v233, 4, s18\n\t"
      "v_lshrrev_b32 v16, 1, v233\n\t"
      "v_and_b32 v16, 24, v16\n\t"
      "v_bfe_u32 v17, s34, v16, 8\n\t"
      "v_or_b32 v17, s30, v17\n\t"
      "v_xor_b32 v18, v17, v1\n\t"
      "v_add_u32 v19, 80, v17\n\t"
      "v_add_u32 v17, 160, v17\n\t"
      "v_and_b32 v19, 256, v19\n\t"
      "v_and_b32 v17, 256, v17\n\t"
      "v_mad_u32_u24 v2, v19, 30, v18\n\t"
      "v_mad_u32_u24 v5, v17, 33, v18\n\t"
      "v_add_u32 v5, 19968, v5\n\t"
      "v_bfe_u32 v17, s35, v16, 8\n\t"
      "v_or_b32 v17, s30, v17\n\t"
      "v_xor_b32 v18, v17, v1\n\t"
      "v_add_u32 v19, 80, v17\n\t"
      "v_add_u32 v17, 160, v17\n\t"
      "v_and_b32 v19, 256, v19\n\t"
      "v_and_b32 v17, 256, v17\n\t"
      "v_mad_u32_u24 v3, v19, 30, v18\n\t"
      "v_mad_u32_u24 v6, v17, 33, v18\n\t"
      "v_add_u32 v6, 19968, v6\n\t"
      "v_bfe_u32 v17, s36, v16, 8\n\t"
      "v_or_b32 v17, s30, v17\n\t"
      "v_xor_b32 v18, v17, v1\n\t"
      "v_add_u32 v19, 80, v17\n\t"
      "v_add_u32 v17, 160, v17\n\t"
      "v_and_b32 v19, 256, v19\n\t"
      "v_and_b32 v17, 256, v17\n\t"
      "v_mad_u32_u24 v4, v19, 30, v18\n\t"
      "v_mad_u32_u24 v7, v17, 33, v18\n\t"
      "v_add_u32 v7, 19968, v7\n\t"
      "s_sub_u32 s31, s30, 64\n\t"
      "v_add_u32 v17, s31, v233\n\t"
      "v_min_u32 v17, 193, v17\n\t"
      "v_lshlrev_b32 v18, 2, v17\n\t"
      "v_lshlrev_b32 v17, 1, v17\n\t"
      "v_and_b32 v18, -16, v18\n\t"
      "v_and_or_b32 v8, v17, 6, v18\n\t"
      "s_mov_b32 s86, 294912\n\t"
      "s_mov_b32 s87, 0x00020000\n\t"
      "s_mov_b32 s42, 113246208\n\t"
      "s_mov_b32 s43, 0x00020000\n\t"
      "s_cmp_lg_u32 s17, 0\n\t"
      "s_cbranch_scc1 .Lno0_%=\n\t"
      "s_movk_i32 s31, -61\n\t"
      "v_lshrrev_b32 v16, 2, v233\n\t"
      "v_add_u32 v17, s20, v16\n\t"
      "v_lshl_add_u32 v18, v16, 3, s31\n\t"
      "v_cmp_gt_u32 vcc, 32, v233\n\t"
      "v_and_b32 v19, 3, v233\n\t"
      "v_lshlrev_b32 v19, 2, v19\n\t"
      "v_cndmask_b32 v17, v18, v17, vcc\n\t"
      "v_lshl_add_u32 v62, v17, 8, v19\n\t"
      "s_waitcnt lgkmcnt(2)\n\t"
      "s_add_u32 s32, s32, %[blk]\n\t"
      "s_mul_i32 s32, s32, 0x9e3779b1\n\t"
      "s_lshr_b32 s32, s32, 9\n\t"
      "s_and_b32 s32, s32, 0x1fdf80\n\t"
      "s_or_b32 s22, s22, s32\n\t"
      "v_mov_b32 v234, s22\n\t"
      "v_add_u32 v235, 8192, v234\n\t"
      "s_movk_i32 s64, 120\n\t"
      "s_mov_b64 vcc, 0\n\t"
      ".Lsweep_%=:\n\t"
      "s_getreg_b32 s30, hwreg(HW_REG_IB_STS, 0, 12)\n\t"
      "s_and_b32 s31, s30, 15\n\t"
      "s_lshr_b32 s30, s30, 8\n\t"
      "s_cmp_eq_u32 s31, 8\n\t"
      "s_addc_u32 s62, s62, 0\n\t"
      "s_cmp_eq_u32 s30, 1\n\t"
      "s_addc_u32 s63, s63, 0\n\t"
      "s_cmp_gt_u32 s31, 7\n\t"
      "s_addc_u32 s30, s30, 0\n\t"
      "s_sub_u32 s64, s64, 1\n\t"
      "s_min_u32 s30, s30, s64\n\t"
      "s_cmp_lg_u32 s30, 0\n\t"
      "s_cbranch_scc1 .Lsweep_%=\n\t"
      "s_cbranch_vccnz .Lswept_%=\n\t"
      "s_waitcnt lgkmcnt(0)\n\t"
      "global_load_dword v236, v234, s[4:5]\n\t"
      "global_load_dword v237, v235, s[4:5]\n\t"
      "s_memrealtime s[28:29]\n\t"
      "s_load_dword s60, s[10:11], s22\n\t"
      "s_load_dword s61, s[10:11], s22 offset:8192\n\t"
      "global_load_dword v63, v62, s[12:13] sc1\n\t"
      "s_mov_b64 s[40:41], s[4:5]\n\t"
      "s_add_u32 m0, s18, 25088\n\t"
      "s_add_u32 s48, s15, 0\n\t"
      "buffer_load_dwordx4 v2, s[40:43], s48 offen nt lds\n\t"
      "buffer_load_dwordx4 v3, s[40:43], s48 offen offset:1024 nt lds\n\t"
      "buffer_load_dwordx4 v4, s[40:43], s48 offen offset:2048 nt lds\n\t"
      "s_add_u32 m0, s18, 37376\n\t"
      "s_add_u32 s48, s15, 0\n\t"
      "buffer_load_dwordx4 v5, s[40:43], s48 offen nt lds\n\t"
      "buffer_load_dwordx4 v6, s[40:43], s48 offen offset:1024 nt lds\n\t"
      "buffer_load_dwordx4 v7, s[40:43], s48 offen offset:2048 nt lds\n\t"
      "s_barrier\n\t"
      "s_mov_b64 s[62:63], 0\n\t"
      "s_movk_i32 s64, 120\n\t"
      "s_mov_b64 vcc, -1\n\t"
      "s_branch .Lsweep_%=\n\t"
      ".Lswept_%=:\n\t"
      "s_min_u32 s62, s62, 7\n\t"
      "s_min_u32 s63, s63, 7\n\t"
      "s_lshl_b32 s63, s63, 8\n\t"
      "s_or_b32 s30, s62, s63\n\t"
      "s_sub_u32 s31, 0x808, s30\n\t"
      "s_add_u32 s30, s30, 0x10808\n\t"
      "s_getreg_b32 s62, hwreg(HW_REG_XCC_ID, 2, 1)\n\t"
      "s_cmp_lg_u32 s62, 0\n\t"
      "s_cselect_b32 s91, s30, s31\n\t"
      "s_mov_b64 s[84:85], s[8:9]\n\t"
      "v_mul_u32_u24 v16, 49152, v1\n\t"
      "v_add_u32 v16, s21, v16\n\t"
      "v_lshrrev_b32 v16, 21, v16\n\t"
      "v_lshlrev_b32 v16, 2, v16\n\t"
      "v_accvgpr_write_b32 a248, v16\n\t"
      "s_branch .Lgo0_%=\n\t"
      ".Lno0_%=:\n\t"
      "s_movk_i32 s82, 768\n\t"
      "s_mov_b32 s83, 0x00020000\n\t"
      "v_and_b32 v234, 0x3f0, v1\n\t"
      "s_waitcnt lgkmcnt(0)\n\t"
      "s_mov_b64 s[84:85], s[8:9]\n\t"
      "s_mov_b64 s[80:81], s[6:7]\n\t"
      "s_mov_b64 s[40:41], s[4:5]\n\t"
      "s_barrier\n\t"
      "buffer_load_dwordx4 a[200:203], v234, s[84:87], s23 offen offset:0\n\t"
      "buffer_load_dwordx4 a[204:207], v234, s[84:87], s23 offen offset:1024\n\t"
      "buffer_load_dwordx4 a[208:211], v234, s[84:87], s23 offen offset:2048\n\t"
      "buffer_load_ushort v224, v8, s[80:83], 0 offen\n\t"
      "buffer_load_ushort v225, v8, s[80:83], 0 offen offset:8\n\t"
      "s_add_u32 m0, s18, 25088\n\t"
      "s_add_u32 s48, s15, 0\n\t"
      "buffer_load_dwordx4 v2, s[40:43], s48 offen nt lds\n\t"
      "buffer_load_dwordx4 v3, s[40:43], s48 offen offset:1024 nt lds\n\t"
      "buffer_load_dwordx4 v4, s[40:43], s48 offen offset:2048 nt lds\n\t"
      "s_add_u32 m0, s18, 37376\n\t"
      "s_add_u32 s48, s15, 0\n\t"
      "buffer_load_dwordx4 v5, s[40:43], s48 offen nt lds\n\t"
      "buffer_load_dwordx4 v6, s[40:43], s48 offen offset:1024 nt lds\n\t"
      "buffer_load_dwordx4 v7, s[40:43], s48 offen offset:2048 nt lds\n\t"
      ".Lgo0_%=:\n\t"
      "s_getpc_b64 s[36:37]\n\t"
      ".Lpc_%=:\n\t"
      "s_lshr_b32 s30, s17, 5\n\t"
      "v_lshlrev_b32 v16, 4, v233\n\t"
      "v_add_u32 v17, 0, v233\n\t"
      "v_mul_u32_u24 v17, 171, v17\n\t"
      "v_lshrrev_b32 v17, 11, v17\n\t"
      "v_lshlrev_b32 v18, 2, v17\n\t"
      "v_and_b32 v18, 32, v18\n\t"
      "v_xor_b32 v18, v18, v16\n\t"
      "v_mul_u32_u24 v17, 576, v17\n\t"
      "v_add3_u32 v9, v17, s30, v18\n\t"
      "v_add_u32 v17, 64, v233\n\t"
      "v_mul_u32_u24 v17, 171, v17\n\t"
      "v_lshrrev_b32 v17, 11, v17\n\t"
      "v_lshlrev_b32 v18, 2, v17\n\t"
      "v_and_b32 v18, 32, v18\n\t"
      "v_xor_b32 v18, v18, v16\n\t"
      "v_mul_u32_u24 v17, 576, v17\n\t"
      "v_add3_u32 v10, v17, s30, v18\n\t"
      "v_add_u32 v17, 128, v233\n\t"
      "v_mul_u32_u24 v17, 171, v17\n\t"
      "v_lshrrev_b32 v17, 11, v17\n\t"
      "v_lshlrev_b32 v18, 2, v17\n\t"
      "v_and_b32 v18, 32, v18\n\t"
      "v_xor_b32 v18, v18, v16\n\t"
      "v_mul_u32_u24 v17, 576, v17\n\t"
      "v_add3_u32 v11, v17, s30, v18\n\t"
      "s_mov_b64 s[44:45], s[10:11]\n\t"
      "s_mov_b32 s46, 0\n\t"
      "s_mov_b32 s47, 0x00020000\n\t"
      "s_lshl_b32 s30, s24, 6\n\t"
      "s_add_u32 s31, s30, 0\n\t"
      "s_min_u32 s31, s31, .Lend_%=-.Lpc_%=-4\n\t"
      "s_load_dword s35, s[36:37], s31\n\t"
      "s_add_u32 s31, s30, 8192\n\t"
      "s_min_u32 s31, s31, .Lend_%=-.Lpc_%=-4\n\t"
      "s_load_dword s35, s[36:37], s31\n\t"
      "s_add_u32 s31, s30, 16384\n\t"
      "s_min_u32 s31, s31, .Lend_%=-.Lpc_%=-4\n\t"
      "s_load_dword s35, s[36:37], s31\n\t"
      "s_add_u32 s31, s30, 24576\n\t"
      "s_min_u32 s31, s31, .Lend_%=-.Lpc_%=-4\n\t"
      "s_load_dword s35, s[36:37], s31\n\t"
      "s_and_b32 s31, s24, 63\n\t"
      "s_lshl_b32 s31, s31, 21\n\t"
      "s_min_u32 s31, s31, 113246144\n\t"
      "s_bitcmp1_b32 s24, 6\n\t"
      "s_cselect_b64 s[32:33], s[44:45], s[40:41]\n\t"
      "s_load_dword s35, s[32:33], s31\n\t"
      "s_lshr_b32 s30, s17, 5\n\t"
      "v_lshrrev_b32 v16, 4, v233\n\t"
      "v_and_b32 v17, 15, v233\n\t"
      "v_and_b32 v18, 3, v233\n\t"
      "v_bfe_u32 v19, v233, 2, 2\n\t"
      "v_lshl_add_u32 v19, v16, 3, v19\n\t"
      "v_mul_u32_u24 v19, 192, v19\n\t"
      "v_lshl_add_u32 v19, v18, 3, v19\n\t"
      "v_lshlrev_b32 v20, 1, v233\n\t"
      "v_and_b32 v20, 32, v20\n\t"
      "v_add3_u32 v12, v19, v20, s17\n\t"
      "v_xor_b32 v13, 32, v12\n\t"
      "v_xor_b32 v19, v16, v17\n\t"
      "v_lshlrev_b32 v19, 4, v19\n\t"
      "v_mul_u32_u24 v20, 768, v17\n\t"
      "v_add_u32 v15, v19, v20\n\t"
      "v_mul_u32_u24 v20, 784, v17\n\t"
      "v_lshl_add_u32 v20, v16, 3, v20\n\t"
      "v_add_u32 v0, s30, v20\n\t"
      "v_lshlrev_b32 v19, 6, v16\n\t"
      "v_lshl_add_u32 v19, v18, 4, v19\n\t"
      "v_add_u32 v14, 160256, v19\n\t"
      "v_and_b32 v226, 0xfffffff0, v8\n\t"
      "v_and_b32 v227, 6, v8\n\t"
      "v_lshlrev_b32 v226, 2, v226\n\t"
      "v_lshl_add_u32 v226, v227, 3, v226\n\t"
      "v_add_u32 v226, 160256, v226\n\t"
      "v_and_b32 v232, 2, v8\n\t"
      "v_lshlrev_b32 v232, 3, v232\n\t"
      "v_cmp_gt_u32 vcc, 4, v227\n\t"
      "v_mov_b32 v56, v15\n\t"
      "v_xor_b32 v57, 64, v15\n\t"
      "v_xor_b32 v58, 128, v15\n\t"
      "v_xor_b32 v59, 192, v15\n\t"
      "v_add_u32 v60, 86528, v12\n\t"
      "v_add_u32 v61, 86528, v13\n\t"
      "v_xor_b32 v16, v2, v1\n\t"
      "v_and_b32 v16, 0xf0, v16\n\t"
      "v_xor_b32 v17, v16, v2\n\t"
      "v_xor_b32 v18, v16, v5\n\t"
      "v_add_u32 v19, v16, v1\n\t"
      "v_accvgpr_write_b32 a213, v17\n\t"
      "v_accvgpr_write_b32 a216, v18\n\t"
      "v_accvgpr_write_b32 a219, v19\n\t"
      "v_xor_b32 v16, v3, v1\n\t"
      "v_and_b32 v16, 0xf0, v16\n\t"
      "v_xor_b32 v17, v16, v3\n\t"
      "v_xor_b32 v18, v16, v6\n\t"
      "v_add_u32 v19, v16, v1\n\t"
      "v_accvgpr_write_b32 a214, v17\n\t"
      "v_accvgpr_write_b32 a217, v18\n\t"
      "v_accvgpr_write_b32 a220, v19\n\t"
      "v_xor_b32 v16, v4, v1\n\t"
      "v_and_b32 v16, 0xf0, v16\n\t"
      "v_xor_b32 v17, v16, v4\n\t"
      "v_xor_b32 v18, v16, v7\n\t"
      "v_add_u32 v19, v16, v1\n\t"
      "v_accvgpr_write_b32 a215, v17\n\t"
      "v_accvgpr_write_b32 a218, v18\n\t"
      "v_accvgpr_write_b32 a221, v19\n\t"
      "s_waitcnt vmcnt(6)\n\t"
      "s_bfe_u32 s30, s19, 0x30008\n\t"
      "s_bfe_u32 s31, s19, 0x20002\n\t"
      "s_lshl2_add_u32 s30, s30, s31\n\t"
      "v_readlane_b32 s90, v63, s30\n\t"
      "v_lshlrev_b32 v224, v232, v224\n\t"
      "v_lshlrev_b32 v225, v232, v225\n\t"
      "v_cndmask_b32 v228, 0, v224, vcc\n\t"
      "v_cndmask_b32 v230, 0, v225, vcc\n\t"
      "v_sub_u32 v229, v224, v228\n\t"
      "v_sub_u32 v231, v225, v230\n\t"
      "ds_write_b128 v226, v[228:231]\n\t"
      "s_add_u32 s90, s90, 0x2000000\n\t"
      "s_andn2_b32 s90, s90, 0x1ffffff\n\t"
      "s_or_b32 s90, s90, s91\n\t"
      "s_or_b32 s30, s17, s16\n\t"
      "s_cbranch_scc1 .Lno2_%=\n\t"
      "v_mov_b32 v234, s90\n\t"
      "v_mov_b32 v235, s19\n\t"
      "global_store_dword v235, v234, s[12:13] sc1\n\t"
      ".Lno2_%=:\n\t"
      "s_add_u32 m0, s17, 0\n\t"
      "s_mov_b32 s54, 0\n\t"
      "buffer_load_dwordx4 v9, s[84:87], s54 offen lds\n\t"
      "buffer_load_dwordx4 v10, s[84:87], s54 offen offset:1024 lds\n\t"
      "buffer_load_dwordx4 v11, s[84:87], s54 offen offset:2048 lds\n\t"
      "s_add_u32 m0, s17, 3072\n\t"
      "s_mov_b32 s54, 12288\n\t"
      "buffer_load_dwordx4 v9, s[84:87], s54 offen lds\n\t"
      "buffer_load_dwordx4 v10, s[84:87], s54 offen offset:1024 lds\n\t"
      "buffer_load_dwordx4 v11, s[84:87], s54 offen offset:2048 lds\n\t"
      "s_add_u32 m0, s17, 86528\n\t"
      "s_mov_b32 s54, 24576\n\t"
      "buffer_load_dwordx4 v9, s[84:87], s54 offen lds\n\t"
      "buffer_load_dwordx4 v10, s[84:87], s54 offen offset:1024 lds\n\t"
      "buffer_load_dwordx4 v11, s[84:87], s54 offen offset:2048 lds\n\t"
      "s_add_u32 m0, s17, 89600\n\t"
      "s_mov_b32 s54, 36864\n\t"
      "buffer_load_dwordx4 v9, s[84:87], s54 offen lds\n\t"
      "buffer_load_dwordx4 v10, s[84:87], s54 offen offset:1024 lds\n\t"
      "buffer_load_dwordx4 v11, s[84:87], s54 offen offset:2048 lds\n\t"
      "s_add_u32 m0, s17, 111104\n\t"
      "s_mov_b32 s54, 49152\n\t"
      "buffer_load_dwordx4 v9, s[84:87], s54 offen lds\n\t"
      "buffer_load_dwordx4 v10, s[84:87], s54 offen offset:1024 lds\n\t"
      "buffer_load_dwordx4 v11, s[84:87], s54 offen offset:2048 lds\n\t"
      "s_add_u32 m0, s17, 114176\n\t"
      "s_mov_b32 s54, 61440\n\t"
      "buffer_load_dwordx4 v9, s[84:87], s54 offen lds\n\t"
      "buffer_load_dwordx4 v10, s[84:87], s54 offen offset:1024 lds\n\t"
      "buffer_load_dwordx4 v11, s[84:87], s54 offen offset:2048 lds\n\t"
      "s_waitcnt lgkmcnt(0)\n\t"
      "s_waitcnt vmcnt(18)\n\t"
      "s_barrier\n\t"
      "s_waitcnt vmcnt(12)\n\t"
      "ds_read_b128 a[180:183], v14 offset:0\n\t"
      "ds_read_b64_tr_b16 v[64:65], v12 offset:0\n\t"
      "ds_read_b64_tr_b16 v[66:67], v12 offset:768\n\t"
      "ds_read_b64_tr_b16 v[68:69], v13 offset:0\n\t"
      "ds_read_b64_tr_b16 v[70:71], v13 offset:768\n\t"
      "ds_read_b64_tr_b16 v[72:73], v12 offset:64\n\t"
      "ds_read_b64_tr_b16 v[74:75], v12 offset:832\n\t"
      "ds_read_b64_tr_b16 v[76:77], v13 offset:64\n\t"
      "ds_read_b64_tr_b16 v[78:79], v13 offset:832\n\t"
      "ds_read_b64_tr_b16 a[60:61], v12 offset:128\n\t"
      "ds_read_b64_tr_b16 a[62:63], v12 offset:896\n\t"
      "ds_read_b64_tr_b16 a[64:65], v13 offset:128\n\t"
      "ds_read_b64_tr_b16 a[66:67], v13 offset:896\n\t"
      "s_add_u32 m0, s17, 135680\n\t"
      "s_mov_b32 s54, 73728\n\t"
      "buffer_load_dwordx4 v9, s[84:87], s54 offen lds\n\t"
      "buffer_load_dwordx4 v10, s[84:87], s54 offen offset:1024 lds\n\t"
      "buffer_load_dwordx4 v11, s[84:87], s54 offen offset:2048 lds\n\t"
      "s_add_u32 m0, s17, 138752\n\t"
      "s_mov_b32 s54, 86016\n\t"
      "buffer_load_dwordx4 v9, s[84:87], s54 offen lds\n\t"
      "buffer_load_dwordx4 v10, s[84:87], s54 offen offset:1024 lds\n\t"
      "buffer_load_dwordx4 v11, s[84:87], s54 offen offset:2048 lds\n\t"
      "s_waitcnt vmcnt(12)\n\t"
      "s_waitcnt lgkmcnt(0)\n\t"
      "; step\n\t"
      "v_mfma_f32_4x4x4_16b_bf16 v[40:43], a[180:181], v[64:65], 0\n\t"
      "ds_read_b128 a[184:187], v14 offset:256\n\t"
      "v_mfma_f32_4x4x4_16b_bf16 v[44:47], a[182:183], v[66:67], 0\n\t"
      "ds_read_b64_tr_b16 v[80:81], v60 offset:0\n\t"
      "v_mfma_f32_4x4x4_16b_bf16 v[48:51], a[180:181], v[68:69], 0\n\t"
      "ds_read_b64_tr_b16 v[82:83], v60 offset:768\n\t"
      "v_mfma_f32_4x4x4_16b_bf16 v[52:55], a[182:183], v[70:71], 0\n\t"
      "ds_read_b64_tr_b16 v[84:85], v61 offset:0\n\t"
      "ds_read_b64_tr_b16 v[86:87], v61 offset:768\n\t"
      "s_add_u32 m0, s17, 0\n\t"
      "s_mov_b32 s54, 98304\n\t"
      "buffer_load_dwordx4 v9, s[84:87], s54 offen lds\n\t"
      "buffer_load_dwordx4 v10, s[84:87], s54 offen offset:1024 lds\n\t"
      "v_cvt_pk_bf16_f32 v64, v40, v41\n\t"
      "v_cvt_pk_bf16_f32 v65, v42, v43\n\t"
      "v_cvt_pk_bf16_f32 v66, v44, v45\n\t"
      "v_cvt_pk_bf16_f32 v67, v46, v47\n\t"
      "v_cvt_pk_bf16_f32 v68, v48, v49\n\t"
      "v_cvt_pk_bf16_f32 v69, v50, v51\n\t"
      "v_cvt_pk_bf16_f32 v70, v52, v53\n\t"
      "v_cvt_pk_bf16_f32 v71, v54, v55\n\t"
      "v_mfma_f32_4x4x4_16b_bf16 v[40:43], a[180:181], v[72:73], 0\n\t"
      "ds_read_b64_tr_b16 v[88:89], v60 offset:64\n\t"
      "v_mfma_f32_4x4x4_16b_bf16 v[44:47], a[182:183], v[74:75], 0\n\t"
      "ds_read_b64_tr_b16 v[90:91], v60 offset:832\n\t"
      "v_mfma_f32_4x4x4_16b_bf16 v[48:51], a[180:181], v[76:77], 0\n\t"
      "ds_read_b64_tr_b16 v[92:93], v61 offset:64\n\t"
      "v_mfma_f32_4x4x4_16b_bf16 v[52:55], a[182:183], v[78:79], 0\n\t"
      "ds_read_b64_tr_b16 v[94:95], v61 offset:832\n\t"
      "buffer_load_dwordx4 v11, s[84:87], s54 offen offset:2048 lds\n\t"
      "s_add_u32 m0, s17, 3072\n\t"
      "s_mov_b32 s54, 110592\n\t"
      "buffer_load_dwordx4 v9, s[84:87], s54 offen lds\n\t"
      "v_cvt_pk_bf16_f32 v72, v40, v41\n\t"
      "v_cvt_pk_bf16_f32 v73, v42, v43\n\t"
      "v_cvt_pk_bf16_f32 v74, v44, v45\n\t"
      "v_cvt_pk_bf16_f32 v75, v46, v47\n\t"
      "v_cvt_pk_bf16_f32 v76, v48, v49\n\t"
      "v_cvt_pk_bf16_f32 v77, v50, v51\n\t"
      "v_cvt_pk_bf16_f32 v78, v52, v53\n\t"
      "v_cvt_pk_bf16_f32 v79, v54, v55\n\t"
      "ds_read_b128 a[224:227], v56 offset:25088\n\t"
      "v_mfma_f32_4x4x4_16b_bf16 v[40:43], a[180:181], a[60:61], 0\n\t"
      "ds_read_b64_tr_b16 a[68:69], v60 offset:128\n\t"
      "v_mfma_f32_4x4x4_16b_bf16 v[44:47], a[182:183], a[62:63], 0\n\t"
      "ds_read_b64_tr_b16 a[70:71], v60 offset:896\n\t"
      "v_mfma_f32_4x4x4_16b_bf16 v[48:51], a[180:181], a[64:65], 0\n\t"
      "ds_read_b64_tr_b16 a[72:73], v61 offset:128\n\t"
      "v_mfma_f32_4x4x4_16b_bf16 v[52:55], a[182:183], a[66:67], 0\n\t"
      "ds_read_b64_tr_b16 a[74:75], v61 offset:896\n\t"
      "buffer_load_dwordx4 v10, s[84:87], s54 offen offset:1024 lds\n\t"
      "buffer_load_dwordx4 v11, s[84:87], s54 offen offset:2048 lds\n\t"
      "ds_read_b128 a[228:231], v56 offset:37376\n\t"
      "v_cvt_pk_bf16_f32 v40, v40, v41\n\t"
      "v_cvt_pk_bf16_f32 v41, v42, v43\n\t"
      "v_cvt_pk_bf16_f32 v44, v44, v45\n\t"
      "v_cvt_pk_bf16_f32 v45, v46, v47\n\t"
      "v_cvt_pk_bf16_f32 v48, v48, v49\n\t"
      "v_cvt_pk_bf16_f32 v49, v50, v51\n\t"
      "v_cvt_pk_bf16_f32 v52, v52, v53\n\t"
      "v_cvt_pk_bf16_f32 v53, v54, v55\n\t"
      "v_accvgpr_write_b32 a60, v40\n\t"
      "v_accvgpr_write_b32 a61, v41\n\t"
      "v_accvgpr_write_b32 a62, v44\n\t"
      "v_accvgpr_write_b32 a63, v45\n\t"
      "v_accvgpr_write_b32 a64, v48\n\t"
      "v_accvgpr_write_b32 a65, v49\n\t"
      "v_accvgpr_write_b32 a66, v52\n\t"
      "v_accvgpr_write_b32 a67, v53\n\t"
      "s_waitcnt vmcnt(12)\n\t"
      "s_waitcnt lgkmcnt(1)\n\t"
      "; step\n\t"
      "s_memtime s[64:65]\n\t"
      "s_memrealtime s[66:67]\n\t"
      "v_mfma_f32_4x4x4_16b_bf16 v[40:43], a[184:185], v[80:81], 0\n\t"
      "ds_read_b128 a[180:183], v14 offset:512\n\t"
      "v_mfma_f32_4x4x4_16b_bf16 v[44:47], a[186:187], v[82:83], 0\n\t"
      "ds_read_b64_tr_b16 v[96:97], v60 offset:24576\n\t"
      "v_mfma_f32_4x4x4_16b_bf16 v[48:51], a[184:185], v[84:85], 0\n\t"
      "ds_read_b64_tr_b16 v[98:99], v60 offset:25344\n\t"
      "v_mfma_f32_4x4x4_16b_bf16 v[52:55], a[186:187], v[86:87], 0\n\t"
      "ds_read_b64_tr_b16 v[100:101], v61 offset:24576\n\t"
      "ds_read_b64_tr_b16 v[102:103], v61 offset:25344\n\t"
      "s_add_u32 m0, s17, 86528\n\t"
      "s_mov_b32 s54, 122880\n\t"
      "buffer_load_dwordx4 v9, s[84:87], s54 offen lds\n\t"
      "buffer_load_dwordx4 v10, s[84:87], s54 offen offset:1024 lds\n\t"
      "v_mfma_f32_16x16x32_bf16 v[16:19], v[64:67], a[224:227], 0\n\t"
      "v_cvt_pk_bf16_f32 v80, v40, v41\n\t"
      "v_cvt_pk_bf16_f32 v81, v42, v43\n\t"
      "v_mfma_f32_16x16x32_bf16 v[20:23], v[68:71], a[224:227], 0\n\t"
      "v_cvt_pk_bf16_f32 v82, v44, v45\n\t"
      "v_cvt_pk_bf16_f32 v83, v46, v47\n\t"
      "v_mfma_f32_16x16x32_bf16 v[24:27], v[72:75], a[224:227], 0\n\t"
      "v_cvt_pk_bf16_f32 v84, v48, v49\n\t"
      "v_cvt_pk_bf16_f32 v85, v50, v51\n\t"
      "v_mfma_f32_16x16x32_bf16 v[28:31], v[76:79], a[224:227], 0\n\t"
      "v_cvt_pk_bf16_f32 v86, v52, v53\n\t"
      "v_cvt_pk_bf16_f32 v87, v54, v55\n\t"
      "v_mfma_f32_4x4x4_16b_bf16 v[40:43], a[184:185], v[88:89], 0\n\t"
      "ds_read_b64_tr_b16 v[104:105], v60 offset:24640\n\t"
      "v_mfma_f32_4x4x4_16b_bf16 v[44:47], a[186:187], v[90:91], 0\n\t"
      "ds_read_b64_tr_b16 v[106:107], v60 offset:25408\n\t"
      "v_mfma_f32_4x4x4_16b_bf16 v[48:51], a[184:185], v[92:93], 0\n\t"
      "ds_read_b64_tr_b16 v[108:109], v61 offset:24640\n\t"
      "v_mfma_f32_4x4x4_16b_bf16 v[52:55], a[186:187], v[94:95], 0\n\t"
      "ds_read_b64_tr_b16 v[110:111], v61 offset:25408\n\t"
      "buffer_load_dwordx4 v11, s[84:87], s54 offen offset:2048 lds\n\t"
      "s_add_u32 m0, s17, 89600\n\t"
      "s_mov_b32 s54, 135168\n\t"
      "buffer_load_dwordx4 v9, s[84:87], s54 offen lds\n\t"
      "v_mfma_f32_16x16x32_bf16 v[32:35], a[60:63], a[224:227], 0\n\t"
      "v_cvt_pk_bf16_f32 v88, v40, v41\n\t"
      "v_cvt_pk_bf16_f32 v89, v42, v43\n\t"
      "v_mfma_f32_16x16x32_bf16 v[36:39], a[64:67], a[224:227], 0\n\t"
      "v_cvt_pk_bf16_f32 v90, v44, v45\n\t"
      "v_cvt_pk_bf16_f32 v91, v46, v47\n\t"
      "s_waitcnt lgkmcnt(9)\n\t"
      "v_mfma_f32_16x16x32_bf16 a[156:159], v[64:67], a[228:231], 0\n\t"
      "v_cvt_pk_bf16_f32 v92, v48, v49\n\t"
      "v_cvt_pk_bf16_f32 v93, v50, v51\n\t"
      "v_mfma_f32_16x16x32_bf16 a[160:163], v[68:71], a[228:231], 0\n\t"
      "v_cvt_pk_bf16_f32 v94, v52, v53\n\t"
      "v_cvt_pk_bf16_f32 v95, v54, v55\n\t"
      "ds_read_b128 a[232:235], v57 offset:25088\n\t"
      "v_mfma_f32_4x4x4_16b_bf16 v[40:43], a[184:185], a[68:69], 0\n\t"
      "ds_read_b64_tr_b16 a[76:77], v60 offset:24704\n\t"
      "v_mfma_f32_4x4x4_16b_bf16 v[44:47], a[186:187], a[70:71], 0\n\t"
      "ds_read_b64_tr_b16 a[78:79], v60 offset:25472\n\t"
      "v_mfma_f32_4x4x4_16b_bf16 v[48:51], a[184:185], a[72:73], 0\n\t"
      "ds_read_b64_tr_b16 a[80:81], v61 offset:24704\n\t"
      "v_mfma_f32_4x4x4_16b_bf16 v[52:55], a[186:187], a[74:75], 0\n\t"
      "ds_read_b64_tr_b16 a[82:83], v61 offset:25472\n\t"
      "buffer_load_dwordx4 v10, s[84:87], s54 offen offset:1024 lds\n\t"
      "buffer_load_dwordx4 v11, s[84:87], s54 offen offset:2048 lds\n\t"
      "ds_read_b128 a[236:239], v57 offset:37376\n\t"
      "v_mfma_f32_16x16x32_bf16 a[164:167], v[72:75], a[228:231], 0\n\t"
      "v_cvt_pk_bf16_f32 v40, v40, v41\n\t"
      "v_cvt_pk_bf16_f32 v41, v42, v43\n\t"
      "v_mfma_f32_16x16x32_bf16 a[168:171], v[76:79], a[228:231], 0\n\t"
      "v_cvt_pk_bf16_f32 v44, v44, v45\n\t"
      "v_cvt_pk_bf16_f32 v45, v46, v47\n\t"
      "v_mfma_f32_16x16x32_bf16 a[172:175], a[60:63], a[228:231], 0\n\t"
      "v_cvt_pk_bf16_f32 v48, v48, v49\n\t"
      "v_cvt_pk_bf16_f32 v49, v50, v51\n\t"
      "v_mfma_f32_16x16x32_bf16 a[176:179], a[64:67], a[228:231], 0\n\t"
      "v_cvt_pk_bf16_f32 v52, v52, v53\n\t"
      "v_cvt_pk_bf16_f32 v53, v54, v55\n\t"
      "v_accvgpr_write_b32 a68, v40\n\t"
      "v_accvgpr_write_b32 a69, v41\n\t"
      "v_accvgpr_write_b32 a70, v44\n\t"
      "v_accvgpr_write_b32 a71, v45\n\t"
      "v_accvgpr_write_b32 a72, v48\n\t"
      "v_accvgpr_write_b32 a73, v49\n\t"
      "v_accvgpr_write_b32 a74, v52\n\t"
      "v_accvgpr_write_b32 a75, v53\n\t"
      "s_waitcnt vmcnt(12)\n\t"
      "s_waitcnt lgkmcnt(1)\n\t"
      "; step\n\t"
      "v_mfma_f32_4x4x4_16b_bf16 v[40:43], a[180:181], v[96:97], 0\n\t"
      "ds_read_b128 a[184:187], v14 offset:768\n\t"
      "v_mfma_f32_4x4x4_16b_bf16 v[44:47], a[182:183], v[98:99], 0\n\t"
      "ds_read_b64_tr_b16 v[112:113], v60 offset:49152\n\t"
      "v_mfma_f32_4x4x4_16b_bf16 v[48:51], a[180:181], v[100:101], 0\n\t"
      "ds_read_b64_tr_b16 v[114:115], v60 offset:49920\n\t"
      "v_mfma_f32_4x4x4_16b_bf16 v[52:55], a[182:183], v[102:103], 0\n\t"
      "ds_read_b64_tr_b16 v[116:117], v61 offset:49152\n\t"
      "ds_read_b64_tr_b16 v[118:119], v61 offset:49920\n\t"
      "s_add_u32 m0, s17, 111104\n\t"
      "s_mov_b32 s54, 147456\n\t"
      "buffer_load_dwordx4 v9, s[84:87], s54 offen lds\n\t"
      "buffer_load_dwordx4 v10, s[84:87], s54 offen offset:1024 lds\n\t"
      "v_mfma_f32_16x16x32_bf16 v[16:19], v[80:83], a[232:235], v[16:19]\n\t"
      "v_cvt_pk_bf16_f32 v96, v40, v41\n\t"
      "v_cvt_pk_bf16_f32 v97, v42, v43\n\t"
      "v_mfma_f32_16x16x32_bf16 v[20:23], v[84:87], a[232:235], v[20:23]\n\t"
      "v_cvt_pk_bf16_f32 v98, v44, v45\n\t"
      "v_cvt_pk_bf16_f32 v99, v46, v47\n\t"
      "v_mfma_f32_16x16x32_bf16 v[24:27], v[88:91], a[232:235], v[24:27]\n\t"
      "v_cvt_pk_bf16_f32 v100, v48, v49\n\t"
      "v_cvt_pk_bf16_f32 v101, v50, v51\n\t"
      "v_mfma_f32_16x16x32_bf16 v[28:31], v[92:95], a[232:235], v[28:31]\n\t"
      "v_cvt_pk_bf16_f32 v102, v52, v53\n\t"
      "v_cvt_pk_bf16_f32 v103, v54, v55\n\t"
      "v_mfma_f32_4x4x4_16b_bf16 v[40:43], a[180:181], v[104:105], 0\n\t"
      "ds_read_b64_tr_b16 v[120:121], v60 offset:49216\n\t"
      "v_mfma_f32_4x4x4_16b_bf16 v[44:47], a[182:183], v[106:107], 0\n\t"
      "ds_read_b64_tr_b16 v[122:123], v60 offset:49984\n\t"
      "v_mfma_f32_4x4x4_16b_bf16 v[48:51], a[180:181], v[108:109], 0\n\t"
      "ds_read_b64_tr_b16 v[124:125], v61 offset:49216\n\t"
      "v_mfma_f32_4x4x4_16b_bf16 v[52:55], a[182:183], v[110:111], 0\n\t"
      "ds_read_b64_tr_b16 v[126:127], v61 offset:49984\n\t"
      "buffer_load_dwordx4 v11, s[84:87], s54 offen offset:2048 lds\n\t"
      "s_add_u32 m0, s17, 114176\n\t"
      "s_mov_b32 s54, 159744\n\t"
      "buffer_load_dwordx4 v9, s[84:87], s54 offen lds\n\t"
      "v_mfma_f32_16x16x32_bf16 v[32:35], a[68:71], a[232:235], v[32:35]\n\t"
      "v_cvt_pk_bf16_f32 v104, v40, v41\n\t"
      "v_cvt_pk_bf16_f32 v105, v42, v43\n\t"
      "v_mfma_f32_16x16x32_bf16 v[36:39], a[72:75], a[232:235], v[36:39]\n\t"
      "v_cvt_pk_bf16_f32 v106, v44, v45\n\t"
      "v_cvt_pk_bf16_f32 v107, v46, v47\n\t"
      "s_waitcnt lgkmcnt(9)\n\t"
      "v_mfma_f32_16x16x32_bf16 a[156:159], v[80:83], a[236:239], a[156:159]\n\t"
      "v_cvt_pk_bf16_f32 v108, v48, v49\n\t"
      "v_cvt_pk_bf16_f32 v109, v50, v51\n\t"
      "v_mfma_f32_16x16x32_bf16 a[160:163], v[84:87], a[236:239], a[160:163]\n\t"
      "v_cvt_pk_bf16_f32 v110, v52, v53\n\t"
      "v_cvt_pk_bf16_f32 v111, v54, v55\n\t"
      "ds_read_b128 a[240:243], v58 offset:25088\n\t"
      "v_mfma_f32_4x4x4_16b_bf16 v[40:43], a[180:181], a[76:77], 0\n\t"
      "ds_read_b64_tr_b16 a[84:85], v60 offset:49280\n\t"
      "v_mfma_f32_4x4x4_16b_bf16 v[44:47], a[182:183], a[78:79], 0\n\t"
      "ds_read_b64_tr_b16 a[86:87], v60 offset:50048\n\t"
      "v_mfma_f32_4x4x4_16b_bf16 v[48:51], a[180:181], a[80:81], 0\n\t"
      "ds_read_b64_tr_b16 a[88:89], v61 offset:49280\n\t"
      "v_mfma_f32_4x4x4_16b_bf16 v[52:55], a[182:183], a[82:83], 0\n\t"
      "ds_read_b64_tr_b16 a[90:91], v61 offset:50048\n\t"
      "buffer_load_dwordx4 v10, s[84:87], s54 offen offset:1024 lds\n\t"
      "buffer_load_dwordx4 v11, s[84:87], s54 offen offset:2048 lds\n\t"
      "ds_read_b128 a[244:247], v58 offset:37376\n\t"
      "v_mfma_f32_16x16x32_bf16 a[164:167], v[88:91], a[236:239], a[164:167]\n\t"
      "v_cvt_pk_bf16_f32 v40, v40, v41\n\t"
      "v_cvt_pk_bf16_f32 v41, v42, v43\n\t"
      "v_mfma_f32_16x16x32_bf16 a[168:171], v[92:95], a[236:239], a[168:171]\n\t"
      "v_cvt_pk_bf16_f32 v44, v44, v45\n\t"
      "v_cvt_pk_bf16_f32 v45, v46, v47\n\t"
      "v_mfma_f32_16x16x32_bf16 a[172:175], a[68:71], a[236:239], a[172:175]\n\t"
      "v_cvt_pk_bf16_f32 v48, v48, v49\n\t"
      "v_cvt_pk_bf16_f32 v49, v50, v51\n\t"
      "v_mfma_f32_16x16x32_bf16 a[176:179], a[72:75], a[236:239], a[176:179]\n\t"
      "v_cvt_pk_bf16_f32 v52, v52, v53\n\t"
      "v_cvt_pk_bf16_f32 v53, v54, v55\n\t"
      "v_accvgpr_write_b32 a76, v40\n\t"
      "v_accvgpr_write_b32 a77, v41\n\t"
      "v_accvgpr_write_b32 a78, v44\n\t"
      "v_accvgpr_write_b32 a79, v45\n\t"
      "v_accvgpr_write_b32 a80, v48\n\t"
      "v_accvgpr_write_b32 a81, v49\n\t"
      "v_accvgpr_write_b32 a82, v52\n\t"
      "v_accvgpr_write_b32 a83, v53\n\t"
      "s_waitcnt vmcnt(12)\n\t"
      "s_waitcnt lgkmcnt(1)\n\t"
      "; step\n\t"
      "v_mfma_f32_4x4x4_16b_bf16 v[40:43], a[184:185], v[112:113], 0\n\t"
      "ds_read_b128 a[180:183], v14 offset:1024\n\t"
      "v_mfma_f32_4x4x4_16b_bf16 v[44:47], a[186:187], v[114:115], 0\n\t"
      "ds_read_b64_tr_b16 v[128:129], v12 offset:0\n\t"
      "v_mfma_f32_4x4x4_16b_bf16 v[48:51], a[184:185], v[116:117], 0\n\t"
      "ds_read_b64_tr_b16 v[130:131], v12 offset:768\n\t"
      "v_mfma_f32_4x4x4_16b_bf16 v[52:55], a[186:187], v[118:119], 0\n\t"
      "ds_read_b64_tr_b16 v[132:133], v13 offset:0\n\t"
      "ds_read_b64_tr_b16 v[134:135], v13 offset:768\n\t"
      "s_add_u32 m0, s17, 135680\n\t"
      "s_mov_b32 s54, 172032\n\t"
      "buffer_load_dwordx4 v9, s[84:87], s54 offen lds\n\t"
      "buffer_load_dwordx4 v10, s[84:87], s54 offen offset:1024 lds\n\t"
      "v_mfma_f32_16x16x32_bf16 v[16:19], v[96:99], a[240:243], v[16:19]\n\t"
      "v_cvt_pk_bf16_f32 v112, v40, v41\n\t"
      "v_cvt_pk_bf16_f32 v113, v42, v43\n\t"
      "v_mfma_f32_16x16x32_bf16 v[20:23], v[100:103], a[240:243], v[20:23]\n\t"
      "v_cvt_pk_bf16_f32 v114, v44, v45\n\t"
      "v_cvt_pk_bf16_f32 v115, v46, v47\n\t"
      "v_mfma_f32_16x16x32_bf16 v[24:27], v[104:107], a[240:243], v[24:27]\n\t"
      "v_cvt_pk_bf16_f32 v116, v48, v49\n\t"
      "v_cvt_pk_bf16_f32 v117, v50, v51\n\t"
      "v_mfma_f32_16x16x32_bf16 v[28:31], v[108:111], a[240:243], v[28:31]\n\t"
      "v_cvt_pk_bf16_f32 v118, v52, v53\n\t"
      "v_cvt_pk_bf16_f32 v119, v54, v55\n\t"
      "v_mfma_f32_4x4x4_16b_bf16 v[40:43], a[184:185], v[120:121], 0\n\t"
      "ds_read_b64_tr_b16 v[136:137], v12 offset:64\n\t"
      "v_mfma_f32_4x4x4_16b_bf16 v[44:47], a[186:187], v[122:123], 0\n\t"
      "ds_read_b64_tr_b16 v[138:139], v12 offset:832\n\t"
      "v_mfma_f32_4x4x4_16b_bf16 v[48:51], a[184:185], v[124:125], 0\n\t"
      "ds_read_b64_tr_b16 v[140:141], v13 offset:64\n\t"
      "v_mfma_f32_4x4x4_16b_bf16 v[52:55], a[186:187], v[126:127], 0\n\t"
      "ds_read_b64_tr_b16 v[142:143], v13 offset:832\n\t"
      "buffer_load_dwordx4 v11, s[84:87], s54 offen offset:2048 lds\n\t"
      "s_add_u32 m0, s17, 138752\n\t"
      "s_mov_b32 s54, 184320\n\t"
      "buffer_load_dwordx4 v9, s[84:87], s54 offen lds\n\t"
      "v_mfma_f32_16x16x32_bf16 v[32:35], a[76:79], a[240:243], v[32:35]\n\t"
      "v_cvt_pk_bf16_f32 v120, v40, v41\n\t"
      "v_cvt_pk_bf16_f32 v121, v42, v43\n\t"
      "v_mfma_f32_16x16x32_bf16 v[36:39], a[80:83], a[240:243], v[36:39]\n\t"
      "v_cvt_pk_bf16_f32 v122, v44, v45\n\t"
      "v_cvt_pk_bf16_f32 v123, v46, v47\n\t"
      "s_waitcnt lgkmcnt(9)\n\t"
      "v_mfma_f32_16x16x32_bf16 a[156:159], v[96:99], a[244:247], a[156:159]\n\t"
      "v_cvt_pk_bf16_f32 v124, v48, v49\n\t"
      "v_cvt_pk_bf16_f32 v125, v50, v51\n\t"
      "v_mfma_f32_16x16x32_bf16 a[160:163], v[100:103], a[244:247], a[160:163]\n\t"
      "v_cvt_pk_bf16_f32 v126, v52, v53\n\t"
      "v_cvt_pk_bf16_f32 v127, v54, v55\n\t"
      "ds_read_b128 a[224:227], v59 offset:25088\n\t"
      "v_mfma_f32_4x4x4_16b_bf16 v[40:43], a[184:185], a[84:85], 0\n\t"
      "ds_read_b64_tr_b16 a[92:93], v12 offset:128\n\t"
      "v_mfma_f32_4x4x4_16b_bf16 v[44:47], a[186:187], a[86:87], 0\n\t"
      "ds_read_b64_tr_b16 a[94:95], v12 offset:896\n\t"
      "v_mfma_f32_4x4x4_16b_bf16 v[48:51], a[184:185], a[88:89], 0\n\t"
      "ds_read_b64_tr_b16 a[96:97], v13 offset:128\n\t"
      "v_mfma_f32_4x4x4_16b_bf16 v[52:55], a[186:187], a[90:91], 0\n\t"
      "ds_read_b64_tr_b16 a[98:99], v13 offset:896\n\t"
      "buffer_load_dwordx4 v10, s[84:87], s54 offen offset:1024 lds\n\t"
      "buffer_load_dwordx4 v11, s[84:87], s54 offen offset:2048 lds\n\t"
      "ds_read_b128 a[228:231], v59 offset:37376\n\t"
      "v_mfma_f32_16x16x32_bf16 a[164:167], v[104:107], a[244:247], a[164:167]\n\t"
      "v_cvt_pk_bf16_f32 v40, v40, v41\n\t"
      "v_cvt_pk_bf16_f32 v41, v42, v43\n\t"
      "v_mfma_f32_16x16x32_bf16 a[168:171], v[108:111], a[244:247], a[168:171]\n\t"
      "v_cvt_pk_bf16_f32 v44, v44, v45\n\t"
      "v_cvt_pk_bf16_f32 v45, v46, v47\n\t"
      "v_mfma_f32_16x16x32_bf16 a[172:175], a[76:79], a[244:247], a[172:175]\n\t"
      "v_cvt_pk_bf16_f32 v48, v48, v49\n\t"
      "v_cvt_pk_bf16_f32 v49, v50, v51\n\t"
      "v_mfma_f32_16x16x32_bf16 a[176:179], a[80:83], a[244:247], a[176:179]\n\t"
      "v_cvt_pk_bf16_f32 v52, v52, v53\n\t"
      "v_cvt_pk_bf16_f32 v53, v54, v55\n\t"
      "v_accvgpr_write_b32 a84, v40\n\t"
      "v_accvgpr_write_b32 a85, v41\n\t"
      "v_accvgpr_write_b32 a86, v44\n\t"
      "v_accvgpr_write_b32 a87, v45\n\t"
      "v_accvgpr_write_b32 a88, v48\n\t"
      "v_accvgpr_write_b32 a89, v49\n\t"
      "v_accvgpr_write_b32 a90, v52\n\t"
      "v_accvgpr_write_b32 a91, v53\n\t"
      "s_waitcnt vmcnt(12)\n\t"
      "s_waitcnt lgkmcnt(1)\n\t"
      "; step\n\t"
      "v_mfma_f32_4x4x4_16b_bf16 v[40:43], a[180:181], v[128:129], 0\n\t"
      "ds_read_b128 a[184:187], v14 offset:1280\n\t"
      "v_mfma_f32_4x4x4_16b_bf16 v[44:47], a[182:183], v[130:131], 0\n\t"
      "ds_read_b64_tr_b16 v[144:145], v60 offset:0\n\t"
      "v_mfma_f32_4x4x4_16b_bf16 v[48:51], a[180:181], v[132:133], 0\n\t"
      "ds_read_b64_tr_b16 v[146:147], v60 offset:768\n\t"
      "v_mfma_f32_4x4x4_16b_bf16 v[52:55], a[182:183], v[134:135], 0\n\t"
      "ds_read_b64_tr_b16 v[148:149], v61 offset:0\n\t"
      "ds_read_b64_tr_b16 v[150:151], v61 offset:768\n\t"
      "s_add_u32 m0, s17, 0\n\t"
      "s_mov_b32 s54, 196608\n\t"
      "buffer_load_dwordx4 v9, s[84:87], s54 offen lds\n\t"
      "buffer_load_dwordx4 v10, s[84:87], s54 offen offset:1024 lds\n\t"
      "v_mfma_f32_16x16x32_bf16 v[16:19], v[112:115], a[224:227], v[16:19]\n\t"
      "v_cvt_pk_bf16_f32 v128, v40, v41\n\t"
      "v_cvt_pk_bf16_f32 v129, v42, v43\n\t"
      "v_mfma_f32_16x16x32_bf16 v[20:23], v[116:119], a[224:227], v[20:23]\n\t"
      "v_cvt_pk_bf16_f32 v130, v44, v45\n\t"
      "v_cvt_pk_bf16_f32 v131, v46, v47\n\t"
      "v_mfma_f32_16x16x32_bf16 v[24:27], v[120:123], a[224:227], v[24:27]\n\t"
      "v_cvt_pk_bf16_f32 v132, v48, v49\n\t"
      "v_cvt_pk_bf16_f32 v133, v50, v51\n\t"
      "v_mfma_f32_16x16x32_bf16 v[28:31], v[124:127], a[224:227], v[28:31]\n\t"
      "v_cvt_pk_bf16_f32 v134, v52, v53\n\t"
      "v_cvt_pk_bf16_f32 v135, v54, v55\n\t"
      "v_mfma_f32_4x4x4_16b_bf16 v[40:43], a[180:181], v[136:137], 0\n\t"
      "ds_read_b64_tr_b16 v[152:153], v60 offset:64\n\t"
      "v_mfma_f32_4x4x4_16b_bf16 v[44:47], a[182:183], v[138:139], 0\n\t"
      "ds_read_b64_tr_b16 v[154:155], v60 offset:832\n\t"
      "v_mfma_f32_4x4x4_16b_bf16 v[48:51], a[180:181], v[140:141], 0\n\t"
      "ds_read_b64_tr_b16 v[156:157], v61 offset:64\n\t"
      "v_mfma_f32_4x4x4_16b_bf16 v[52:55], a[182:183], v[142:143], 0\n\t"
      "ds_read_b64_tr_b16 v[158:159], v61 offset:832\n\t"
      "buffer_load_dwordx4 v11, s[84:87], s54 offen offset:2048 lds\n\t"
      "s_add_u32 m0, s17, 3072\n\t"
      "s_mov_b32 s54, 208896\n\t"
      "buffer_load_dwordx4 v9, s[84:87], s54 offen lds\n\t"
      "v_mfma_f32_16x16x32_bf16 v[32:35], a[84:87], a[224:227], v[32:35]\n\t"
      "v_cvt_pk_bf16_f32 v136, v40, v41\n\t"
      "v_cvt_pk_bf16_f32 v137, v42, v43\n\t"
      "v_mfma_f32_16x16x32_bf16 v[36:39], a[88:91], a[224:227], v[36:39]\n\t"
      "v_cvt_pk_bf16_f32 v138, v44, v45\n\t"
      "v_cvt_pk_bf16_f32 v139, v46, v47\n\t"
      "s_waitcnt lgkmcnt(9)\n\t"
      "v_mfma_f32_16x16x32_bf16 a[156:159], v[112:115], a[228:231], a[156:159]\n\t"
      "v_cvt_pk_bf16_f32 v140, v48, v49\n\t"
      "v_cvt_pk_bf16_f32 v141, v50, v51\n\t"
      "v_mfma_f32_16x16x32_bf16 a[160:163], v[116:119], a[228:231], a[160:163]\n\t"
      "v_cvt_pk_bf16_f32 v142, v52, v53\n\t"
      "v_cvt_pk_bf16_f32 v143, v54, v55\n\t"
      "ds_read_b128 a[232:235], v56 offset:25344\n\t"
      "v_mfma_f32_4x4x4_16b_bf16 v[40:43], a[180:181], a[92:93], 0\n\t"
      "ds_read_b64_tr_b16 a[100:101], v60 offset:128\n\t"
      "v_mfma_f32_4x4x4_16b_bf16 v[44:47], a[182:183], a[94:95], 0\n\t"
      "ds_read_b64_tr_b16 a[102:103], v60 offset:896\n\t"
      "v_mfma_f32_4x4x4_16b_bf16 v[48:51], a[180:181], a[96:97], 0\n\t"
      "ds_read_b64_tr_b16 a[104:105], v61 offset:128\n\t"
      "v_mfma_f32_4x4x4_16b_bf16 v[52:55], a[182:183], a[98:99], 0\n\t"
      "ds_read_b64_tr_b16 a[106:107], v61 offset:896\n\t"
      "buffer_load_dwordx4 v10, s[84:87], s54 offen offset:1024 lds\n\t"
      "buffer_load_dwordx4 v11, s[84:87], s54 offen offset:2048 lds\n\t"
      "ds_read_b128 a[236:239], v56 offset:37632\n\t"
      "v_mfma_f32_16x16x32_bf16 a[164:167], v[120:123], a[228:231], a[164:167]\n\t"
      "v_cvt_pk_bf16_f32 v40, v40, v41\n\t"
      "v_cvt_pk_bf16_f32 v41, v42, v43\n\t"
      "v_mfma_f32_16x16x32_bf16 a[168:171], v[124:127], a[228:231], a[168:171]\n\t"
      "v_cvt_pk_bf16_f32 v44, v44, v45\n\t"
      "v_cvt_pk_bf16_f32 v45, v46, v47\n\t"
      "v_mfma_f32_16x16x32_bf16 a[172:175], a[84:87], a[228:231], a[172:175]\n\t"
      "v_cvt_pk_bf16_f32 v48, v48, v49\n\t"
      "v_cvt_pk_bf16_f32 v49, v50, v51\n\t"
      "v_mfma_f32_16x16x32_bf16 a[176:179], a[88:91], a[228:231], a[176:179]\n\t"
      "v_cvt_pk_bf16_f32 v52, v52, v53\n\t"
      "v_cvt_pk_bf16_f32 v53, v54, v55\n\t"
      "v_accvgpr_write_b32 a92, v40\n\t"
      "v_accvgpr_write_b32 a93, v41\n\t"
      "v_accvgpr_write_b32 a94, v44\n\t"
      "v_accvgpr_write_b32 a95, v45\n\t"
      "v_accvgpr_write_b32 a96, v48\n\t"
      "v_accvgpr_write_b32 a97, v49\n\t"
      "v_accvgpr_write_b32 a98, v52\n\t"
      "v_accvgpr_write_b32 a99, v53\n\t"
      "s_waitcnt vmcnt(12)\n\t"
      "s_waitcnt lgkmcnt(1)\n\t"
      "; step\n\t"
      "v_mfma_f32_4x4x4_16b_bf16 v[40:43], a[184:185], v[144:145], 0\n\t"
      "ds_read_b128 a[180:183], v14 offset:1536\n\t"
      "v_mfma_f32_4x4x4_16b_bf16 v[44:47], a[186:187], v[146:147], 0\n\t"
      "ds_read_b64_tr_b16 v[160:161], v60 offset:24576\n\t"
      "v_mfma_f32_4x4x4_16b_bf16 v[48:51], a[184:185], v[148:149], 0\n\t"
      "ds_read_b64_tr_b16 v[162:163], v60 offset:25344\n\t"
      "v_mfma_f32_4x4x4_16b_bf16 v[52:55], a[186:187], v[150:151], 0\n\t"
      "ds_read_b64_tr_b16 v[164:165], v61 offset:24576\n\t"
      "ds_read_b64_tr_b16 v[166:167], v61 offset:25344\n\t"
      "s_add_u32 m0, s17, 86528\n\t"
      "s_mov_b32 s54, 221184\n\t"
      "buffer_load_dwordx4 v9, s[84:87], s54 offen lds\n\t"
      "buffer_load_dwordx4 v10, s[84:87], s54 offen offset:1024 lds\n\t"
      "v_mfma_f32_16x16x32_bf16 v[16:19], v[128:131], a[232:235], v[16:19]\n\t"
      "v_cvt_pk_bf16_f32 v144, v40, v41\n\t"
      "v_cvt_pk_bf16_f32 v145, v42, v43\n\t"
      "v_mfma_f32_16x16x32_bf16 v[20:23], v[132:135], a[232:235], v[20:23]\n\t"
      "v_cvt_pk_bf16_f32 v146, v44, v45\n\t"
      "v_cvt_pk_bf16_f32 v147, v46, v47\n\t"
      "v_mfma_f32_16x16x32_bf16 v[24:27], v[136:139], a[232:235], v[24:27]\n\t"
      "v_cvt_pk_bf16_f32 v148, v48, v49\n\t"
      "v_cvt_pk_bf16_f32 v149, v50, v51\n\t"
      "v_mfma_f32_16x16x32_bf16 v[28:31], v[140:143], a[232:235], v[28:31]\n\t"
      "v_cvt_pk_bf16_f32 v150, v52, v53\n\t"
      "v_cvt_pk_bf16_f32 v151, v54, v55\n\t"
      "v_mfma_f32_4x4x4_16b_bf16 v[40:43], a[184:185], v[152:153], 0\n\t"
      "ds_read_b64_tr_b16 v[168:169], v60 offset:24640\n\t"
      "v_mfma_f32_4x4x4_16b_bf16 v[44:47], a[186:187], v[154:155], 0\n\t"
      "ds_read_b64_tr_b16 v[170:171], v60 offset:25408\n\t"
      "v_mfma_f32_4x4x4_16b_bf16 v[48:51], a[184:185], v[156:157], 0\n\t"
      "ds_read_b64_tr_b16 v[172:173], v61 offset:24640\n\t"
      "v_mfma_f32_4x4x4_16b_bf16 v[52:55], a[186:187], v[158:159], 0\n\t"
      "ds_read_b64_tr_b16 v[174:175], v61 offset:25408\n\t"
      "buffer_load_dwordx4 v11, s[84:87], s54 offen offset:2048 lds\n\t"
      "s_add_u32 m0, s17, 89600\n\t"
      "s_mov_b32 s54, 233472\n\t"
      "buffer_load_dwordx4 v9, s[84:87], s54 offen lds\n\t"
      "v_mfma_f32_16x16x32_bf16 v[32:35], a[92:95], a[232:235], v[32:35]\n\t"
      "v_cvt_pk_bf16_f32 v152, v40, v41\n\t"
      "v_cvt_pk_bf16_f32 v153, v42, v43\n\t"
      "v_mfma_f32_16x16x32_bf16 v[36:39], a[96:99], a[232:235], v[36:39]\n\t"
      "v_cvt_pk_bf16_f32 v154, v44, v45\n\t"
      "v_cvt_pk_bf16_f32 v155, v46, v47\n\t"
      "s_waitcnt lgkmcnt(9)\n\t"
      "v_mfma_f32_16x16x32_bf16 a[156:159], v[128:131], a[236:239], a[156:159]\n\t"
      "v_cvt_pk_bf16_f32 v156, v48, v49\n\t"
      "v_cvt_pk_bf16_f32 v157, v50, v51\n\t"
      "v_mfma_f32_16x16x32_bf16 a[160:163], v[132:135], a[236:239], a[160:163]\n\t"
      "v_cvt_pk_bf16_f32 v158, v52, v53\n\t"
      "v_cvt_pk_bf16_f32 v159, v54, v55\n\t"
      "ds_read_b128 a[240:243], v57 offset:25344\n\t"
      "v_mfma_f32_4x4x4_16b_bf16 v[40:43], a[184:185], a[100:101], 0\n\t"
      "ds_read_b64_tr_b16 a[108:109], v60 offset:24704\n\t"
      "v_mfma_f32_4x4x4_16b_bf16 v[44:47], a[186:187], a[102:103], 0\n\t"
      "ds_read_b64_tr_b16 a[110:111], v60 offset:25472\n\t"
      "v_mfma_f32_4x4x4_16b_bf16 v[48:51], a[184:185], a[104:105], 0\n\t"
      "ds_read_b64_tr_b16 a[112:113], v61 offset:24704\n\t"
      "v_mfma_f32_4x4x4_16b_bf16 v[52:55], a[186:187], a[106:107], 0\n\t"
      "ds_read_b64_tr_b16 a[114:115], v61 offset:25472\n\t"
      "buffer_load_dwordx4 v10, s[84:87], s54 offen offset:1024 lds\n\t"
      "buffer_load_dwordx4 v11, s[84:87], s54 offen offset:2048 lds\n\t"
      "ds_read_b128 a[244:247], v57 offset:37632\n\t"
      "v_mfma_f32_16x16x32_bf16 a[164:167], v[136:139], a[236:239], a[164:167]\n\t"
      "v_cvt_pk_bf16_f32 v40, v40, v41\n\t"
      "v_cvt_pk_bf16_f32 v41, v42, v43\n\t"
      "v_mfma_f32_16x16x32_bf16 a[168:171], v[140:143], a[236:239], a[168:171]\n\t"
      "v_cvt_pk_bf16_f32 v44, v44, v45\n\t"
      "v_cvt_pk_bf16_f32 v45, v46, v47\n\t"
      "v_mfma_f32_16x16x32_bf16 a[172:175], a[92:95], a[236:239], a[172:175]\n\t"
      "v_cvt_pk_bf16_f32 v48, v48, v49\n\t"
      "v_cvt_pk_bf16_f32 v49, v50, v51\n\t"
      "v_mfma_f32_16x16x32_bf16 a[176:179], a[96:99], a[236:239], a[176:179]\n\t"
      "v_cvt_pk_bf16_f32 v52, v52, v53\n\t"
      "v_cvt_pk_bf16_f32 v53, v54, v55\n\t"
      "v_accvgpr_write_b32 a100, v40\n\t"
      "v_accvgpr_write_b32 a101, v41\n\t"
      "v_accvgpr_write_b32 a102, v44\n\t"
      "v_accvgpr_write_b32 a103, v45\n\t"
      "v_accvgpr_write_b32 a104, v48\n\t"
      "v_accvgpr_write_b32 a105, v49\n\t"
      "v_accvgpr_write_b32 a106, v52\n\t"
      "v_accvgpr_write_b32 a107, v53\n\t"
      "s_waitcnt vmcnt(12)\n\t"
      "s_waitcnt lgkmcnt(1)\n\t"
      "; step\n\t"
      "v_mfma_f32_4x4x4_16b_bf16 v[40:43], a[180:181], v[160:161], 0\n\t"
      "ds_read_b128 a[184:187], v14 offset:1792\n\t"
      "v_mfma_f32_4x4x4_16b_bf16 v[44:47], a[182:183], v[162:163], 0\n\t"
      "ds_read_b64_tr_b16 v[176:177], v60 offset:49152\n\t"
      "v_mfma_f32_4x4x4_16b_bf16 v[48:51], a[180:181], v[164:165], 0\n\t"
      "ds_read_b64_tr_b16 v[178:179], v60 offset:49920\n\t"
      "v_mfma_f32_4x4x4_16b_bf16 v[52:55], a[182:183], v[166:167], 0\n\t"
      "ds_read_b64_tr_b16 v[180:181], v61 offset:49152\n\t"
      "ds_read_b64_tr_b16 v[182:183], v61 offset:49920\n\t"
      "s_add_u32 m0, s17, 111104\n\t"
      "s_mov_b32 s54, 245760\n\t"
      "buffer_load_dwordx4 v9, s[84:87], s54 offen lds\n\t"
      "buffer_load_dwordx4 v10, s[84:87], s54 offen offset:1024 lds\n\t"
      "v_mfma_f32_16x16x32_bf16 v[16:19], v[144:147], a[240:243], v[16:19]\n\t"
      "v_cvt_pk_bf16_f32 v160, v40, v41\n\t"
      "v_cvt_pk_bf16_f32 v161, v42, v43\n\t"
      "v_mfma_f32_16x16x32_bf16 v[20:23], v[148:151], a[240:243], v[20:23]\n\t"
      "v_cvt_pk_bf16_f32 v162, v44, v45\n\t"
      "v_cvt_pk_bf16_f32 v163, v46, v47\n\t"
      "v_mfma_f32_16x16x32_bf16 v[24:27], v[152:155], a[240:243], v[24:27]\n\t"
      "v_cvt_pk_bf16_f32 v164, v48, v49\n\t"
      "v_cvt_pk_bf16_f32 v165, v50, v51\n\t"
      "v_mfma_f32_16x16x32_bf16 v[28:31], v[156:159], a[240:243], v[28:31]\n\t"
      "v_cvt_pk_bf16_f32 v166, v52, v53\n\t"
      "v_cvt_pk_bf16_f32 v167, v54, v55\n\t"
      "v_mfma_f32_4x4x4_16b_bf16 v[40:43], a[180:181], v[168:169], 0\n\t"
      "ds_read_b64_tr_b16 v[184:185], v60 offset:49216\n\t"
      "v_mfma_f32_4x4x4_16b_bf16 v[44:47], a[182:183], v[170:171], 0\n\t"
      "ds_read_b64_tr_b16 v[186:187], v60 offset:49984\n\t"
      "v_mfma_f32_4x4x4_16b_bf16 v[48:51], a[180:181], v[172:173], 0\n\t"
      "ds_read_b64_tr_b16 v[188:189], v61 offset:49216\n\t"
      "v_mfma_f32_4x4x4_16b_bf16 v[52:55], a[182:183], v[174:175], 0\n\t"
      "ds_read_b64_tr_b16 v[190:191], v61 offset:49984\n\t"
      "buffer_load_dwordx4 v11, s[84:87], s54 offen offset:2048 lds\n\t"
      "s_add_u32 m0, s17, 114176\n\t"
      "s_mov_b32 s54, 258048\n\t"
      "buffer_load_dwordx4 v9, s[84:87], s54 offen lds\n\t"
      "v_mfma_f32_16x16x32_bf16 v[32:35], a[100:103], a[240:243], v[32:35]\n\t"
      "v_cvt_pk_bf16_f32 v168, v40, v41\n\t"
      "v_cvt_pk_bf16_f32 v169, v42, v43\n\t"
      "v_mfma_f32_16x16x32_bf16 v[36:39], a[104:107], a[240:243], v[36:39]\n\t"
      "v_cvt_pk_bf16_f32 v170, v44, v45\n\t"
      "v_cvt_pk_bf16_f32 v171, v46, v47\n\t"
      "s_waitcnt lgkmcnt(9)\n\t"
      "v_mfma_f32_16x16x32_bf16 a[156:159], v[144:147], a[244:247], a[156:159]\n\t"
      "v_cvt_pk_bf16_f32 v172, v48, v49\n\t"
      "v_cvt_pk_bf16_f32 v173, v50, v51\n\t"
      "v_mfma_f32_16x16x32_bf16 a[160:163], v[148:151], a[244:247], a[160:163]\n\t"
      "v_cvt_pk_bf16_f32 v174, v52, v53\n\t"
      "v_cvt_pk_bf16_f32 v175, v54, v55\n\t"
      "ds_read_b128 a[224:227], v58 offset:25344\n\t"
      "v_mfma_f32_4x4x4_16b_bf16 v[40:43], a[180:181], a[108:109], 0\n\t"
      "ds_read_b64_tr_b16 a[116:117], v60 offset:49280\n\t"
      "v_mfma_f32_4x4x4_16b_bf16 v[44:47], a[182:183], a[110:111], 0\n\t"
      "ds_read_b64_tr_b16 a[118:119], v60 offset:50048\n\t"
      "v_mfma_f32_4x4x4_16b_bf16 v[48:51], a[180:181], a[112:113], 0\n\t"
      "ds_read_b64_tr_b16 a[120:121], v61 offset:49280\n\t"
      "v_mfma_f32_4x4x4_16b_bf16 v[52:55], a[182:183], a[114:115], 0\n\t"
      "ds_read_b64_tr_b16 a[122:123], v61 offset:50048\n\t"
      "buffer_load_dwordx4 v10, s[84:87], s54 offen offset:1024 lds\n\t"
      "buffer_load_dwordx4 v11, s[84:87], s54 offen offset:2048 lds\n\t"
      "ds_read_b128 a[228:231], v58 offset:37632\n\t"
      "v_mfma_f32_16x16x32_bf16 a[164:167], v[152:155], a[244:247], a[164:167]\n\t"
      "v_cvt_pk_bf16_f32 v40, v40, v41\n\t"
      "v_cvt_pk_bf16_f32 v41, v42, v43\n\t"
      "v_mfma_f32_16x16x32_bf16 a[168:171], v[156:159], a[244:247], a[168:171]\n\t"
      "v_cvt_pk_bf16_f32 v44, v44, v45\n\t"
      "v_cvt_pk_bf16_f32 v45, v46, v47\n\t"
      "v_mfma_f32_16x16x32_bf16 a[172:175], a[100:103], a[244:247], a[172:175]\n\t"
      "v_cvt_pk_bf16_f32 v48, v48, v49\n\t"
      "v_cvt_pk_bf16_f32 v49, v50, v51\n\t"
      "v_mfma_f32_16x16x32_bf16 a[176:179], a[104:107], a[244:247], a[176:179]\n\t"
      "v_cvt_pk_bf16_f32 v52, v52, v53\n\t"
      "v_cvt_pk_bf16_f32 v53, v54, v55\n\t"
      "v_accvgpr_write_b32 a108, v40\n\t"
      "v_accvgpr_write_b32 a109, v41\n\t"
      "v_accvgpr_write_b32 a110, v44\n\t"
      "v_accvgpr_write_b32 a111, v45\n\t"
      "v_accvgpr_write_b32 a112, v48\n\t"
      "v_accvgpr_write_b32 a113, v49\n\t"
      "v_accvgpr_write_b32 a114, v52\n\t"
      "v_accvgpr_write_b32 a115, v53\n\t"
      "s_waitcnt vmcnt(12)\n\t"
      "s_waitcnt lgkmcnt(1)\n\t"
      "; step\n\t"
      "v_mfma_f32_4x4x4_16b_bf16 v[40:43], a[184:185], v[176:177], 0\n\t"
      "ds_read_b128 a[180:183], v14 offset:2048\n\t"
      "v_mfma_f32_4x4x4_16b_bf16 v[44:47], a[186:187], v[178:179], 0\n\t"
      "ds_read_b64_tr_b16 v[192:193], v12 offset:0\n\t"
      "v_mfma_f32_4x4x4_16b_bf16 v[48:51], a[184:185], v[180:181], 0\n\t"
      "ds_read_b64_tr_b16 v[194:195], v12 offset:768\n\t"
      "v_mfma_f32_4x4x4_16b_bf16 v[52:55], a[186:187], v[182:183], 0\n\t"
      "ds_read_b64_tr_b16 v[196:197], v13 offset:0\n\t"
      "ds_read_b64_tr_b16 v[198:199], v13 offset:768\n\t"
      "s_add_u32 m0, s17, 135680\n\t"
      "s_mov_b32 s54, 270336\n\t"
      "buffer_load_dwordx4 v9, s[84:87], s54 offen lds\n\t"
      "buffer_load_dwordx4 v10, s[84:87], s54 offen offset:1024 lds\n\t"
      "v_mfma_f32_16x16x32_bf16 v[16:19], v[160:163], a[224:227], v[16:19]\n\t"
      "v_cvt_pk_bf16_f32 v176, v40, v41\n\t"
      "v_cvt_pk_bf16_f32 v177, v42, v43\n\t"
      "v_mfma_f32_16x16x32_bf16 v[20:23], v[164:167], a[224:227], v[20:23]\n\t"
      "v_cvt_pk_bf16_f32 v178, v44, v45\n\t"
      "v_cvt_pk_bf16_f32 v179, v46, v47\n\t"
      "v_mfma_f32_16x16x32_bf16 v[24:27], v[168:171], a[224:227], v[24:27]\n\t"
      "v_cvt_pk_bf16_f32 v180, v48, v49\n\t"
      "v_cvt_pk_bf16_f32 v181, v50, v51\n\t"
      "v_mfma_f32_16x16x32_bf16 v[28:31], v[172:175], a[224:227], v[28:31]\n\t"
      "v_cvt_pk_bf16_f32 v182, v52, v53\n\t"
      "v_cvt_pk_bf16_f32 v183, v54, v55\n\t"
      "v_mfma_f32_4x4x4_16b_bf16 v[40:43], a[184:185], v[184:185], 0\n\t"
      "ds_read_b64_tr_b16 v[200:201], v12 offset:64\n\t"
      "v_mfma_f32_4x4x4_16b_bf16 v[44:47], a[186:187], v[186:187], 0\n\t"
      "ds_read_b64_tr_b16 v[202:203], v12 offset:832\n\t"
      "v_mfma_f32_4x4x4_16b_bf16 v[48:51], a[184:185], v[188:189], 0\n\t"
      "ds_read_b64_tr_b16 v[204:205], v13 offset:64\n\t"
      "v_mfma_f32_4x4x4_16b_bf16 v[52:55], a[186:187], v[190:191], 0\n\t"
      "ds_read_b64_tr_b16 v[206:207], v13 offset:832\n\t"
      "buffer_load_dwordx4 v11, s[84:87], s54 offen offset:2048 lds\n\t"
      "s_add_u32 m0, s17, 138752\n\t"
      "s_mov_b32 s54, 282624\n\t"
      "buffer_load_dwordx4 v9, s[84:87], s54 offen lds\n\t"
      "v_mfma_f32_16x16x32_bf16 v[32:35], a[108:111], a[224:227], v[32:35]\n\t"
      "v_cvt_pk_bf16_f32 v184, v40, v41\n\t"
      "v_cvt_pk_bf16_f32 v185, v42, v43\n\t"
      "v_mfma_f32_16x16x32_bf16 v[36:39], a[112:115], a[224:227], v[36:39]\n\t"
      "v_cvt_pk_bf16_f32 v186, v44, v45\n\t"
      "v_cvt_pk_bf16_f32 v187, v46, v47\n\t"
      "s_waitcnt lgkmcnt(9)\n\t"
      "v_mfma_f32_16x16x32_bf16 a[156:159], v[160:163], a[228:231], a[156:159]\n\t"
      "v_cvt_pk_bf16_f32 v188, v48, v49\n\t"
      "v_cvt_pk_bf16_f32 v189, v50, v51\n\t"
      "v_mfma_f32_16x16x32_bf16 a[160:163], v[164:167], a[228:231], a[160:163]\n\t"
      "v_cvt_pk_bf16_f32 v190, v52, v53\n\t"
      "v_cvt_pk_bf16_f32 v191, v54, v55\n\t"
      "ds_read_b128 a[232:235], v59 offset:25344\n\t"
      "v_mfma_f32_4x4x4_16b_bf16 v[40:43], a[184:185], a[116:117], 0\n\t"
      "ds_read_b64_tr_b16 a[124:125], v12 offset:128\n\t"
      "v_mfma_f32_4x4x4_16b_bf16 v[44:47], a[186:187], a[118:119], 0\n\t"
      "ds_read_b64_tr_b16 a[126:127], v12 offset:896\n\t"
      "v_mfma_f32_4x4x4_16b_bf16 v[48:51], a[184:185], a[120:121], 0\n\t"
      "ds_read_b64_tr_b16 a[128:129], v13 offset:128\n\t"
      "v_mfma_f32_4x4x4_16b_bf16 v[52:55], a[186:187], a[122:123], 0\n\t"
      "ds_read_b64_tr_b16 a[130:131], v13 offset:896\n\t"
      "buffer_load_dwordx4 v10, s[84:87], s54 offen offset:1024 lds\n\t"
      "buffer_load_dwordx4 v11, s[84:87], s54 offen offset:2048 lds\n\t"
      "ds_read_b128 a[236:239], v59 offset:37632\n\t"
      "s_or_b32 s30, s17, s16\n\t"
      "s_cbranch_scc1 .Lno3_%=\n\t"
      "global_load_dword a252, v62, s[12:13] sc1\n\t"
      ".Lno3_%=:\n\t"
      "s_add_u32 m0, s18, 49664\n\t"
      "s_add_u32 s48, s15, 786432\n\t"
      "buffer_load_dwordx4 v2, s[40:43], s48 offen nt lds\n\t"
      "buffer_load_dwordx4 v3, s[40:43], s48 offen offset:1024 nt lds\n\t"
      "buffer_load_dwordx4 v4, s[40:43], s48 offen offset:2048 nt lds\n\t"
      "v_mfma_f32_16x16x32_bf16 a[164:167], v[168:171], a[228:231], a[164:167]\n\t"
      "v_cvt_pk_bf16_f32 v40, v40, v41\n\t"
      "v_cvt_pk_bf16_f32 v41, v42, v43\n\t"
      "v_mfma_f32_16x16x32_bf16 a[168:171], v[172:175], a[228:231], a[168:171]\n\t"
      "v_cvt_pk_bf16_f32 v44, v44, v45\n\t"
      "v_cvt_pk_bf16_f32 v45, v46, v47\n\t"
      "v_mfma_f32_16x16x32_bf16 a[172:175], a[108:111], a[228:231], a[172:175]\n\t"
      "v_cvt_pk_bf16_f32 v48, v48, v49\n\t"
      "v_cvt_pk_bf16_f32 v49, v50, v51\n\t"
      "v_mfma_f32_16x16x32_bf16 a[176:179], a[112:115], a[228:231], a[176:179]\n\t"
      "v_cvt_pk_bf16_f32 v52, v52, v53\n\t"
      "v_cvt_pk_bf16_f32 v53, v54, v55\n\t"
      "v_accvgpr_write_b32 a116, v40\n\t"
      "v_accvgpr_write_b32 a117, v41\n\t"
      "v_accvgpr_write_b32 a118, v44\n\t"
      "v_accvgpr_write_b32 a119, v45\n\t"
      "v_accvgpr_write_b32 a120, v48\n\t"
      "v_accvgpr_write_b32 a121, v49\n\t"
      "v_accvgpr_write_b32 a122, v52\n\t"
      "v_accvgpr_write_b32 a123, v53\n\t"
      "s_waitcnt vmcnt(15)\n\t"
      "s_waitcnt lgkmcnt(1)\n\t"
      "; step\n\t"
      "v_mfma_f32_4x4x4_16b_bf16 v[40:43], a[180:181], v[192:193], 0\n\t"
      "ds_read_b128 a[184:187], v14 offset:2304\n\t"
      "v_mfma_f32_4x4x4_16b_bf16 v[44:47], a[182:183], v[194:195], 0\n\t"
      "ds_read_b64_tr_b16 v[208:209], v60 offset:0\n\t"
      "v_mfma_f32_4x4x4_16b_bf16 v[48:51], a[180:181], v[196:197], 0\n\t"
      "ds_read_b64_tr_b16 v[210:211], v60 offset:768\n\t"
      "v_mfma_f32_4x4x4_16b_bf16 v[52:55], a[182:183], v[198:199], 0\n\t"
      "ds_read_b64_tr_b16 v[212:213], v61 offset:0\n\t"
      "ds_read_b64_tr_b16 v[214:215], v61 offset:768\n\t"
      "s_add_u32 m0, s18, 61952\n\t"
      "s_add_u32 s48, s15, 786432\n\t"
      "buffer_load_dwordx4 v5, s[40:43], s48 offen nt lds\n\t"
      "buffer_load_dwordx4 v6, s[40:43], s48 offen offset:1024 nt lds\n\t"
      "v_mfma_f32_16x16x32_bf16 v[16:19], v[176:179], a[232:235], v[16:19]\n\t"
      "v_cvt_pk_bf16_f32 v192, v40, v41\n\t"
      "v_cvt_pk_bf16_f32 v193, v42, v43\n\t"
      "v_mfma_f32_16x16x32_bf16 v[20:23], v[180:183], a[232:235], v[20:23]\n\t"
      "v_cvt_pk_bf16_f32 v194, v44, v45\n\t"
      "v_cvt_pk_bf16_f32 v195, v46, v47\n\t"
      "v_mfma_f32_16x16x32_bf16 v[24:27], v[184:187], a[232:235], v[24:27]\n\t"
      "v_cvt_pk_bf16_f32 v196, v48, v49\n\t"
      "v_cvt_pk_bf16_f32 v197, v50, v51\n\t"
      "v_mfma_f32_16x16x32_bf16 v[28:31], v[188:191], a[232:235], v[28:31]\n\t"
      "v_cvt_pk_bf16_f32 v198, v52, v53\n\t"
      "v_cvt_pk_bf16_f32 v199, v54, v55\n\t"
      "v_mfma_f32_4x4x4_16b_bf16 v[40:43], a[180:181], v[200:201], 0\n\t"
      "ds_read_b64_tr_b16 v[216:217], v60 offset:64\n\t"
      "v_mfma_f32_4x4x4_16b_bf16 v[44:47], a[182:183], v[202:203], 0\n\t"
      "ds_read_b64_tr_b16 v[218:219], v60 offset:832\n\t"
      "v_mfma_f32_4x4x4_16b_bf16 v[48:51], a[180:181], v[204:205], 0\n\t"
      "ds_read_b64_tr_b16 v[220:221], v61 offset:64\n\t"
      "v_mfma_f32_4x4x4_16b_bf16 v[52:55], a[182:183], v[206:207], 0\n\t"
      "ds_read_b64_tr_b16 v[222:223], v61 offset:832\n\t"
      "buffer_load_dwordx4 v7, s[40:43], s48 offen offset:2048 nt lds\n\t"
      "v_mfma_f32_16x16x32_bf16 v[32:35], a[116:119], a[232:235], v[32:35]\n\t"
      "v_cvt_pk_bf16_f32 v200, v40, v41\n\t"
      "v_cvt_pk_bf16_f32 v201, v42, v43\n\t"
      "v_mfma_f32_16x16x32_bf16 v[36:39], a[120:123], a[232:235], v[36:39]\n\t"
      "v_cvt_pk_bf16_f32 v202, v44, v45\n\t"
      "v_cvt_pk_bf16_f32 v203, v46, v47\n\t"
      "s_waitcnt lgkmcnt(9)\n\t"
      "v_mfma_f32_16x16x32_bf16 a[156:159], v[176:179], a[236:239], a[156:159]\n\t"
      "v_cvt_pk_bf16_f32 v204, v48, v49\n\t"
      "v_cvt_pk_bf16_f32 v205, v50, v51\n\t"
      "v_mfma_f32_16x16x32_bf16 a[160:163], v[180:183], a[236:239], a[160:163]\n\t"
      "v_cvt_pk_bf16_f32 v206, v52, v53\n\t"
      "v_cvt_pk_bf16_f32 v207, v54, v55\n\t"
      "ds_read_b128 a[240:243], v56 offset:25600\n\t"
      "v_mfma_f32_4x4x4_16b_bf16 v[40:43], a[180:181], a[124:125], 0\n\t"
      "ds_read_b64_tr_b16 a[132:133], v60 offset:128\n\t"
      "v_mfma_f32_4x4x4_16b_bf16 v[44:47], a[182:183], a[126:127], 0\n\t"
      "ds_read_b64_tr_b16 a[134:135], v60 offset:896\n\t"
      "v_mfma_f32_4x4x4_16b_bf16 v[48:51], a[180:181], a[128:129], 0\n\t"
      "ds_read_b64_tr_b16 a[136:137], v61 offset:128\n\t"
      "v_mfma_f32_4x4x4_16b_bf16 v[52:55], a[182:183], a[130:131], 0\n\t"
      "ds_read_b64_tr_b16 a[138:139], v61 offset:896\n\t"
      "ds_read_b128 a[244:247], v56 offset:37888\n\t"
      "v_mfma_f32_16x16x32_bf16 a[164:167], v[184:187], a[236:239], a[164:167]\n\t"
      "v_cvt_pk_bf16_f32 v40, v40, v41\n\t"
      "v_cvt_pk_bf16_f32 v41, v42, v43\n\t"
      "v_mfma_f32_16x16x32_bf16 a[168:171], v[188:191], a[236:239], a[168:171]\n\t"
      "v_cvt_pk_bf16_f32 v44, v44, v45\n\t"
      "v_cvt_pk_bf16_f32 v45, v46, v47\n\t"
      "v_mfma_f32_16x16x32_bf16 a[172:175], a[116:119], a[236:239], a[172:175]\n\t"
      "v_cvt_pk_bf16_f32 v48, v48, v49\n\t"
      "v_cvt_pk_bf16_f32 v49, v50, v51\n\t"
      "v_mfma_f32_16x16x32_bf16 a[176:179], a[120:123], a[236:239], a[176:179]\n\t"
      "v_cvt_pk_bf16_f32 v52, v52, v53\n\t"
      "v_cvt_pk_bf16_f32 v53, v54, v55\n\t"
      "v_accvgpr_write_b32 a124, v40\n\t"
      "v_accvgpr_write_b32 a125, v41\n\t"
      "v_accvgpr_write_b32 a126, v44\n\t"
      "v_accvgpr_write_b32 a127, v45\n\t"
      "v_accvgpr_write_b32 a128, v48\n\t"
      "v_accvgpr_write_b32 a129, v49\n\t"
      "v_accvgpr_write_b32 a130, v52\n\t"
      "v_accvgpr_write_b32 a131, v53\n\t"
      "s_waitcnt vmcnt(12)\n\t"
      "s_waitcnt lgkmcnt(1)\n\t"
      "; step\n\t"
      "v_mfma_f32_4x4x4_16b_bf16 v[40:43], a[184:185], v[208:209], 0\n\t"
      "ds_read_b128 a[180:183], v14 offset:2560\n\t"
      "v_mfma_f32_4x4x4_16b_bf16 v[44:47], a[186:187], v[210:211], 0\n\t"
      "ds_read_b64_tr_b16 v[224:225], v60 offset:24576\n\t"
      "v_mfma_f32_4x4x4_16b_bf16 v[48:51], a[184:185], v[212:213], 0\n\t"
      "ds_read_b64_tr_b16 v[226:227], v60 offset:25344\n\t"
      "v_mfma_f32_4x4x4_16b_bf16 v[52:55], a[186:187], v[214:215], 0\n\t"
      "ds_read_b64_tr_b16 v[228:229], v61 offset:24576\n\t"
      "ds_read_b64_tr_b16 v[230:231], v61 offset:25344\n\t"
      "v_mfma_f32_16x16x32_bf16 v[16:19], v[192:195], a[240:243], v[16:19]\n\t"
      "v_cvt_pk_bf16_f32 v208, v40, v41\n\t"
      "v_cvt_pk_bf16_f32 v209, v42, v43\n\t"
      "v_mfma_f32_16x16x32_bf16 v[20:23], v[196:199], a[240:243], v[20:23]\n\t"
      "v_cvt_pk_bf16_f32 v210, v44, v45\n\t"
      "v_cvt_pk_bf16_f32 v211, v46, v47\n\t"
      "v_mfma_f32_16x16x32_bf16 v[24:27], v[200:203], a[240:243], v[24:27]\n\t"
      "v_cvt_pk_bf16_f32 v212, v48, v49\n\t"
      "v_cvt_pk_bf16_f32 v213, v50, v51\n\t"
      "v_mfma_f32_16x16x32_bf16 v[28:31], v[204:207], a[240:243], v[28:31]\n\t"
      "v_cvt_pk_bf16_f32 v214, v52, v53\n\t"
      "v_cvt_pk_bf16_f32 v215, v54, v55\n\t"
      "v_mfma_f32_4x4x4_16b_bf16 v[40:43], a[184:185], v[216:217], 0\n\t"
      "ds_read_b64_tr_b16 v[232:233], v60 offset:24640\n\t"
      "v_mfma_f32_4x4x4_16b_bf16 v[44:47], a[186:187], v[218:219], 0\n\t"
      "ds_read_b64_tr_b16 v[234:235], v60 offset:25408\n\t"
      "v_mfma_f32_4x4x4_16b_bf16 v[48:51], a[184:185], v[220:221], 0\n\t"
      "ds_read_b64_tr_b16 v[236:237], v61 offset:24640\n\t"
      "v_mfma_f32_4x4x4_16b_bf16 v[52:55], a[186:187], v[222:223], 0\n\t"
      "ds_read_b64_tr_b16 v[238:239], v61 offset:25408\n\t"
      "v_mfma_f32_16x16x32_bf16 v[32:35], a[124:127], a[240:243], v[32:35]\n\t"
      "v_cvt_pk_bf16_f32 v216, v40, v41\n\t"
      "v_cvt_pk_bf16_f32 v217, v42, v43\n\t"
      "v_mfma_f32_16x16x32_bf16 v[36:39], a[128:131], a[240:243], v[36:39]\n\t"
      "v_cvt_pk_bf16_f32 v218, v44, v45\n\t"
      "v_cvt_pk_bf16_f32 v219, v46, v47\n\t"
      "s_waitcnt lgkmcnt(9)\n\t"
      "v_mfma_f32_16x16x32_bf16 a[156:159], v[192:195], a[244:247], a[156:159]\n\t"
      "v_cvt_pk_bf16_f32 v220, v48, v49\n\t"
      "v_cvt_pk_bf16_f32 v221, v50, v51\n\t"
      "v_mfma_f32_16x16x32_bf16 a[160:163], v[196:199], a[244:247], a[160:163]\n\t"
      "v_cvt_pk_bf16_f32 v222, v52, v53\n\t"
      "v_cvt_pk_bf16_f32 v223, v54, v55\n\t"
      "ds_read_b128 a[224:227], v57 offset:25600\n\t"
      "v_mfma_f32_4x4x4_16b_bf16 v[40:43], a[184:185], a[132:133], 0\n\t"
      "ds_read_b64_tr_b16 a[140:141], v60 offset:24704\n\t"
      "v_mfma_f32_4x4x4_16b_bf16 v[44:47], a[186:187], a[134:135], 0\n\t"
      "ds_read_b64_tr_b16 a[142:143], v60 offset:25472\n\t"
      "v_mfma_f32_4x4x4_16b_bf16 v[48:51], a[184:185], a[136:137], 0\n\t"
      "ds_read_b64_tr_b16 a[144:145], v61 offset:24704\n\t"
      "v_mfma_f32_4x4x4_16b_bf16 v[52:55], a[186:187], a[138:139], 0\n\t"
      "ds_read_b64_tr_b16 a[146:147], v61 offset:25472\n\t"
      "ds_read_b128 a[228:231], v57 offset:37888\n\t"
      "v_mfma_f32_16x16x32_bf16 a[164:167], v[200:203], a[244:247], a[164:167]\n\t"
      "v_cvt_pk_bf16_f32 v40, v40, v41\n\t"
      "v_cvt_pk_bf16_f32 v41, v42, v43\n\t"
      "v_mfma_f32_16x16x32_bf16 a[168:171], v[204:207], a[244:247], a[168:171]\n\t"
      "v_cvt_pk_bf16_f32 v44, v44, v45\n\t"
      "v_cvt_pk_bf16_f32 v45, v46, v47\n\t"
      "v_mfma_f32_16x16x32_bf16 a[172:175], a[124:127], a[244:247], a[172:175]\n\t"
      "v_cvt_pk_bf16_f32 v48, v48, v49\n\t"
      "v_cvt_pk_bf16_f32 v49, v50, v51\n\t"
      "v_mfma_f32_16x16x32_bf16 a[176:179], a[128:131], a[244:247], a[176:179]\n\t"
      "v_cvt_pk_bf16_f32 v52, v52, v53\n\t"
      "v_cvt_pk_bf16_f32 v53, v54, v55\n\t"
      "v_accvgpr_write_b32 a132, v40\n\t"
      "v_accvgpr_write_b32 a133, v41\n\t"
      "v_accvgpr_write_b32 a134, v44\n\t"
      "v_accvgpr_write_b32 a135, v45\n\t"
      "v_accvgpr_write_b32 a136, v48\n\t"
      "v_accvgpr_write_b32 a137, v49\n\t"
      "v_accvgpr_write_b32 a138, v52\n\t"
      "v_accvgpr_write_b32 a139, v53\n\t"
      "s_waitcnt vmcnt(6)\n\t"
      "s_waitcnt lgkmcnt(1)\n\t"
      "; step\n\t"
      "s_memtime s[68:69]\n\t"
      "s_memrealtime s[70:71]\n\t"
      "v_mfma_f32_4x4x4_16b_bf16 v[40:43], a[180:181], v[224:225], 0\n\t"
      "ds_read_b128 a[184:187], v14 offset:2816\n\t"
      "v_mfma_f32_4x4x4_16b_bf16 v[44:47], a[182:183], v[226:227], 0\n\t"
      "ds_read_b64_tr_b16 v[240:241], v60 offset:49152\n\t"
      "v_mfma_f32_4x4x4_16b_bf16 v[48:51], a[180:181], v[228:229], 0\n\t"
      "ds_read_b64_tr_b16 v[242:243], v60 offset:49920\n\t"
      "v_mfma_f32_4x4x4_16b_bf16 v[52:55], a[182:183], v[230:231], 0\n\t"
      "ds_read_b64_tr_b16 v[244:245], v61 offset:49152\n\t"
      "ds_read_b64_tr_b16 v[246:247], v61 offset:49920\n\t"
      "v_mfma_f32_16x16x32_bf16 v[16:19], v[208:211], a[224:227], v[16:19]\n\t"
      "v_cvt_pk_bf16_f32 v224, v40, v41\n\t"
      "v_cvt_pk_bf16_f32 v225, v42, v43\n\t"
      "v_mfma_f32_16x16x32_bf16 v[20:23], v[212:215], a[224:227], v[20:23]\n\t"
      "v_cvt_pk_bf16_f32 v226, v44, v45\n\t"
      "v_cvt_pk_bf16_f32 v227, v46, v47\n\t"
      "v_mfma_f32_16x16x32_bf16 v[24:27], v[216:219], a[224:227], v[24:27]\n\t"
      "v_cvt_pk_bf16_f32 v228, v48, v49\n\t"
      "v_cvt_pk_bf16_f32 v229, v50, v51\n\t"
      "v_mfma_f32_16x16x32_bf16 v[28:31], v[220:223], a[224:227], v[28:31]\n\t"
      "v_cvt_pk_bf16_f32 v230, v52, v53\n\t"
      "v_cvt_pk_bf16_f32 v231, v54, v55\n\t"
      "v_mfma_f32_4x4x4_16b_bf16 v[40:43], a[180:181], v[232:233], 0\n\t"
      "ds_read_b64_tr_b16 v[248:249], v60 offset:49216\n\t"
      "v_mfma_f32_4x4x4_16b_bf16 v[44:47], a[182:183], v[234:235], 0\n\t"
      "ds_read_b64_tr_b16 v[250:251], v60 offset:49984\n\t"
      "v_mfma_f32_4x4x4_16b_bf16 v[48:51], a[180:181], v[236:237], 0\n\t"
      "ds_read_b64_tr_b16 v[252:253], v61 offset:49216\n\t"
      "v_mfma_f32_4x4x4_16b_bf16 v[52:55], a[182:183], v[238:239], 0\n\t"
      "ds_read_b64_tr_b16 v[254:255], v61 offset:49984\n\t"
      "v_mfma_f32_16x16x32_bf16 v[32:35], a[132:135], a[224:227], v[32:35]\n\t"
      "v_cvt_pk_bf16_f32 v232, v40, v41\n\t"
      "v_cvt_pk_bf16_f32 v233, v42, v43\n\t"
      "v_mfma_f32_16x16x32_bf16 v[36:39], a[136:139], a[224:227], v[36:39]\n\t"
      "v_cvt_pk_bf16_f32 v234, v44, v45\n\t"
      "v_cvt_pk_bf16_f32 v235, v46, v47\n\t"
      "s_waitcnt lgkmcnt(9)\n\t"
      "v_mfma_f32_16x16x32_bf16 a[156:159], v[208:211], a[228:231], a[156:159]\n\t"
      "v_cvt_pk_bf16_f32 v236, v48, v49\n\t"
      "v_cvt_pk_bf16_f32 v237, v50, v51\n\t"
      "v_mfma_f32_16x16x32_bf16 a[160:163], v[212:215], a[228:231], a[160:163]\n\t"
      "v_cvt_pk_bf16_f32 v238, v52, v53\n\t"
      "v_cvt_pk_bf16_f32 v239, v54, v55\n\t"
      "ds_read_b128 a[232:235], v58 offset:25600\n\t"
      "v_mfma_f32_4x4x4_16b_bf16 v[40:43], a[180:181], a[140:141], 0\n\t"
      "ds_read_b64_tr_b16 a[148:149], v60 offset:49280\n\t"
      "v_mfma_f32_4x4x4_16b_bf16 v[44:47], a[182:183], a[142:143], 0\n\t"
      "ds_read_b64_tr_b16 a[150:151], v60 offset:50048\n\t"
      "v_mfma_f32_4x4x4_16b_bf16 v[48:51], a[180:181], a[144:145], 0\n\t"
      "ds_read_b64_tr_b16 a[152:153], v61 offset:49280\n\t"
      "v_mfma_f32_4x4x4_16b_bf16 v[52:55], a[182:183], a[146:147], 0\n\t"
      "ds_read_b64_tr_b16 a[154:155], v61 offset:50048\n\t"
      "v_mfma_f32_16x16x32_bf16 a[164:167], v[216:219], a[228:231], a[164:167]\n\t"
      "v_cvt_pk_bf16_f32 v40, v40, v41\n\t"
      "v_cvt_pk_bf16_f32 v41, v42, v43\n\t"
      "v_mfma_f32_16x16x32_bf16 a[168:171], v[220:223], a[228:231], a[168:171]\n\t"
      "v_cvt_pk_bf16_f32 v44, v44, v45\n\t"
      "v_cvt_pk_bf16_f32 v45, v46, v47\n\t"
      "v_mfma_f32_16x16x32_bf16 a[172:175], a[132:135], a[228:231], a[172:175]\n\t"
      "v_cvt_pk_bf16_f32 v48, v48, v49\n\t"
      "v_cvt_pk_bf16_f32 v49, v50, v51\n\t"
      "v_mfma_f32_16x16x32_bf16 a[176:179], a[136:139], a[228:231], a[176:179]\n\t"
      "v_cvt_pk_bf16_f32 v52, v52, v53\n\t"
      "v_cvt_pk_bf16_f32 v53, v54, v55\n\t"
      "v_accvgpr_write_b32 a140, v40\n\t"
      "v_accvgpr_write_b32 a141, v41\n\t"
      "v_accvgpr_write_b32 a142, v44\n\t"
      "v_accvgpr_write_b32 a143, v45\n\t"
      "v_accvgpr_write_b32 a144, v48\n\t"
      "v_accvgpr_write_b32 a145, v49\n\t"
      "v_accvgpr_write_b32 a146, v52\n\t"
      "v_accvgpr_write_b32 a147, v53\n\t"
      "s_waitcnt lgkmcnt(0)\n\t"
      "; step\n\t"
      "v_mfma_f32_4x4x4_16b_bf16 v[40:43], a[184:185], v[240:241], 0\n\t"
      "v_mfma_f32_4x4x4_16b_bf16 v[44:47], a[186:187], v[242:243], 0\n\t"
      "v_mfma_f32_4x4x4_16b_bf16 v[48:51], a[184:185], v[244:245], 0\n\t"
      "v_mfma_f32_4x4x4_16b_bf16 v[52:55], a[186:187], v[246:247], 0\n\t"
      "v_mfma_f32_16x16x32_bf16 v[16:19], v[224:227], a[232:235], v[16:19]\n\t"
      "s_nop 0\n\t"
      "v_cvt_pk_bf16_f32 v240, v40, v41\n\t"
      "v_cvt_pk_bf16_f32 v241, v42, v43\n\t"
      "v_mfma_f32_16x16x32_bf16 v[20:23], v[228:231], a[232:235], v[20:23]\n\t"
      "v_cvt_pk_bf16_f32 v242, v44, v45\n\t"
      "v_cvt_pk_bf16_f32 v243, v46, v47\n\t"
      "v_cvt_pk_bf16_f32 v244, v48, v49\n\t"
      "v_cvt_pk_bf16_f32 v245, v50, v51\n\t"
      "v_cvt_pk_bf16_f32 v246, v52, v53\n\t"
      "v_cvt_pk_bf16_f32 v247, v54, v55\n\t"
      "v_mfma_f32_4x4x4_16b_bf16 v[40:43], a[184:185], v[248:249], 0\n\t"
      "v_mfma_f32_4x4x4_16b_bf16 v[44:47], a[186:187], v[250:251], 0\n\t"
      "v_mfma_f32_4x4x4_16b_bf16 v[48:51], a[184:185], v[252:253], 0\n\t"
      "v_mfma_f32_4x4x4_16b_bf16 v[52:55], a[186:187], v[254:255], 0\n\t"
      "v_mfma_f32_16x16x32_bf16 v[24:27], v[232:235], a[232:235], v[24:27]\n\t"
      "s_nop 0\n\t"
      "v_cvt_pk_bf16_f32 v248, v40, v41\n\t"
      "v_cvt_pk_bf16_f32 v249, v42, v43\n\t"
      "v_mfma_f32_16x16x32_bf16 v[28:31], v[236:239], a[232:235], v[28:31]\n\t"
      "v_cvt_pk_bf16_f32 v250, v44, v45\n\t"
      "v_cvt_pk_bf16_f32 v251, v46, v47\n\t"
      "v_cvt_pk_bf16_f32 v252, v48, v49\n\t"
      "v_cvt_pk_bf16_f32 v253, v50, v51\n\t"
      "v_cvt_pk_bf16_f32 v254, v52, v53\n\t"
      "v_cvt_pk_bf16_f32 v255, v54, v55\n\t"
      "ds_read_b128 a[236:239], v59 offset:25600\n\t"
      "v_mfma_f32_4x4x4_16b_bf16 v[40:43], a[184:185], a[148:149], 0\n\t"
      "v_mfma_f32_4x4x4_16b_bf16 v[44:47], a[186:187], a[150:151], 0\n\t"
      "v_mfma_f32_4x4x4_16b_bf16 v[48:51], a[184:185], a[152:153], 0\n\t"
      "v_mfma_f32_4x4x4_16b_bf16 v[52:55], a[186:187], a[154:155], 0\n\t"
      "ds_read_b128 a[240:243], v58 offset:37888\n\t"
      "ds_read_b128 a[244:247], v59 offset:37888\n\t"
      "v_mfma_f32_16x16x32_bf16 v[32:35], a[140:143], a[232:235], v[32:35]\n\t"
      "v_cvt_pk_bf16_f32 v40, v40, v41\n\t"
      "v_cvt_pk_bf16_f32 v41, v42, v43\n\t"
      "v_mfma_f32_16x16x32_bf16 v[36:39], a[144:147], a[232:235], v[36:39]\n\t"
      "v_cvt_pk_bf16_f32 v44, v44, v45\n\t"
      "v_cvt_pk_bf16_f32 v45, v46, v47\n\t"
      "v_cvt_pk_bf16_f32 v48, v48, v49\n\t"
      "v_cvt_pk_bf16_f32 v49, v50, v51\n\t"
      "v_cvt_pk_bf16_f32 v52, v52, v53\n\t"
      "v_cvt_pk_bf16_f32 v53, v54, v55\n\t"
      "v_accvgpr_write_b32 a148, v40\n\t"
      "v_accvgpr_write_b32 a149, v41\n\t"
      "v_accvgpr_write_b32 a150, v44\n\t"
      "v_accvgpr_write_b32 a151, v45\n\t"
      "v_accvgpr_write_b32 a152, v48\n\t"
      "v_accvgpr_write_b32 a153, v49\n\t"
      "v_accvgpr_write_b32 a154, v52\n\t"
      "v_accvgpr_write_b32 a155, v53\n\t"
      "s_waitcnt lgkmcnt(2)\n\t"
      "v_mfma_f32_16x16x32_bf16 v[16:19], v[240:243], a[236:239], v[16:19]\n\t"
      "s_add_u32 s49, s15, 0\n\t"
      "s_mov_b32 s50, 98816\n\t"
      "s_mov_b32 s51, 61952\n\t"
      "v_mfma_f32_16x16x32_bf16 v[20:23], v[244:247], a[236:239], v[20:23]\n\t"
      "s_mov_b32 s58, s14\n\t"
      "s_add_u32 s56, s14, 2359296\n\t"
      "v_mfma_f32_16x16x32_bf16 v[24:27], v[248:251], a[236:239], v[24:27]\n\t"
      "s_movk_i32 s39, 92\n\t"
      "s_movk_i32 s29, 71\n\t"
      "s_mov_b32 s32, 1947275679\n\t"
      "v_mfma_f32_16x16x32_bf16 v[28:31], v[252:255], a[236:239], v[28:31]\n\t"
      "s_mov_b32 s33, 143165559\n\t"
      "s_mov_b32 s34, 1950421356\n\t"
      "s_mov_b32 s35, 8947847\n\t"
      "v_mfma_f32_16x16x32_bf16 v[32:35], a[148:151], a[236:239], v[32:35]\n\t"
      "v_mov_b32 v52, 163328\n\t"
      "v_mfma_f32_16x16x32_bf16 v[36:39], a[152:155], a[236:239], v[36:39]\n\t"
      "s_cmp_lg_u32 s17, 0\n\t"
      "s_cselect_b32 s39, 0, s39\n\t"
      "s_cbranch_scc1 .Lno4_%=\n\t"
      "v_accvgpr_read_b32 v41, a248\n\t"
      "v_mov_b32 v51, 0x4040\n\t"
      "s_movk_i32 s52, 2000\n\t"
      "s_mov_b32 s91, 0\n\t"
      "s_waitcnt vmcnt(6) \n\t"
      "v_accvgpr_read_b32 v40, a252\n\t"
      ".Lcheck_%=:\n\t"
      "v_cmp_ne_u32 s[54:55], v40, v63\n\t"
      "s_cmp_lg_u32 s16, 0\n\t"
      "v_add_u32_dpp v42, v40, v40 quad_perm:[2,3,0,1] row_mask:0xf bank_mask:0xf\n\t"
      "s_cselect_b32 s30, -1, s54\n\t"
      "s_cmp_lg_u32 s30, -1\n\t"
      "s_cbranch_scc1 .Lwait_%=\n\t"
      ".Lwaited_%=:\n\t"
      "v_add_u32_dpp v42, v42, v42 quad_perm:[1,0,3,2] row_mask:0xf bank_mask:0xf\n\t"
      "s_bfe_u32 s30, s19, 0x30008\n\t"
      "s_bfe_u32 s31, s19, 0x10003\n\t"
      "s_lshl1_add_u32 s30, s30, s31\n\t"
      "s_lshl_b32 s30, s30, 1\n\t"
      "v_mov_b32 v43, v42\n\t"
      "v_subrev_u32_dpp v43, v42, v51 row_half_mirror row_mask:0x3 bank_mask:0xf\n\t"
      "v_lshl_add_u32 v42, v42, 1, v43\n\t"
      "v_readlane_b32 s31, v40, s30\n\t"
      "v_add_u32 v42, 0x1f1f, v42\n\t"
      "v_and_b32 v43, 128, v42\n\t"
      "v_and_b32 v44, 32768, v42\n\t"
      "v_cmp_ne_u32 s[88:89], 0, v43\n\t"
      "v_cmp_ne_u32 s[36:37], 0, v44\n\t"
      "s_bitcmp1_b32 s19, 2\n\t"
      "s_cselect_b32 s30, 0x3ffff, 0\n\t"
      "s_bitcmp1_b32 s31, 16\n\t"
      "s_cselect_b32 s31, 262140, 0\n\t"
      "s_xor_b32 s30, s30, s31\n\t"
      "v_lshrrev_b32 v41, v41, s88\n\t"
      "v_and_b32 v41, 1, v41\n\t"
      "v_cmp_ne_u32 vcc, 0, v41\n\t"
      "s_xor_b32 s36, s37, s89\n\t"
      "s_and_b32 s36, s36, 0x11111111\n\t"
      "s_bcnt1_i32_b32 s36, s36\n\t"
      "s_and_b32 s31, vcc_lo, 262140\n\t"
      "s_xor_b32 s31, s31, s30\n\t"
      "s_or_b32 s31, s31, s91\n\t"
      "s_and_b32 s30, s16, 0x3ffff\n\t"
      "s_cmp_lg_u32 s16, 0\n\t"
      "s_cselect_b32 s31, s30, s31\n\t"
      "v_mov_b32 v53, s31\n\t"
      "ds_write_b32 v52, v53\n\t"
      "s_lshl_b32 s30, s36, 2\n\t"
      "s_mov_b32 s54, 1412636928\n\t"
      "s_mov_b32 s55, 5\n\t"
      "s_lshr_b64 s[54:55], s[54:55], s30\n\t"
      "s_and_b32 s30, s54, 15\n\t"
      "s_add_u32 s29, s29, s30\n\t"
      ".Lno4_%=:\n\t"
      "v_accvgpr_read_b32 v9, a213\n\t"
      "v_accvgpr_read_b32 v10, a214\n\t"
      "v_accvgpr_read_b32 v11, a215\n\t"
      "v_accvgpr_read_b32 v12, a216\n\t"
      "v_accvgpr_read_b32 v13, a217\n\t"
      "v_accvgpr_read_b32 v14, a218\n\t"
      "v_accvgpr_read_b32 v8, a219\n\t"
      "v_accvgpr_read_b32 v60, a220\n\t"
      "v_accvgpr_read_b32 v61, a221\n\t"
      "v_add_u32 v56, 49664, v56\n\t"
      "v_add_u32 v57, 49664, v57\n\t"
      "v_add_u32 v58, 49664, v58\n\t"
      "v_add_u32 v59, 49664, v59\n\t"
      "v_cvt_pk_bf16_f32 v40, v16, v17\n\t"
      "v_cvt_pk_bf16_f32 v41, v18, v19\n\t"
      "v_cvt_pk_bf16_f32 v42, v20, v21\n\t"
      "v_cvt_pk_bf16_f32 v43, v22, v23\n\t"
      "v_cvt_pk_bf16_f32 v44, v24, v25\n\t"
      "v_cvt_pk_bf16_f32 v45, v26, v27\n\t"
      "v_cvt_pk_bf16_f32 v46, v28, v29\n\t"
      "v_cvt_pk_bf16_f32 v47, v30, v31\n\t"
      "v_cvt_pk_bf16_f32 v48, v32, v33\n\t"
      "v_cvt_pk_bf16_f32 v49, v34, v35\n\t"
      "v_cvt_pk_bf16_f32 v50, v36, v37\n\t"
      "v_cvt_pk_bf16_f32 v51, v38, v39\n\t"
      "s_mov_b32 s46, 113246208\n\t"
      "s_mov_b32 s52, 15\n\t"
      "s_add_u32 s90, s90, 0x2000000\n\t"
      "s_waitcnt vmcnt(3)\n\t"
      "s_waitcnt lgkmcnt(0)\n\t"
      "s_barrier\n\t"
      "v_mfma_f32_16x16x32_bf16 a[156:159], v[224:227], a[240:243], a[156:159]\n\t"
      "ds_read_b32 v63, v52\n\t"
      "ds_read_b128 a[0:3], v56 offset:0\n\t"
      "s_memrealtime s[36:37]\n\t"
      "v_mfma_f32_16x16x32_bf16 a[160:163], v[228:231], a[240:243], a[160:163]\n\t"
      "ds_read_b128 a[4:7], v57 offset:0\n\t"
      "ds_read_b128 a[8:11], v58 offset:0\n\t"
      "v_mfma_f32_16x16x32_bf16 a[164:167], v[232:235], a[240:243], a[164:167]\n\t"
      "ds_read_b128 a[12:15], v59 offset:0\n\t"
      "ds_read_b128 a[16:19], v56 offset:256\n\t"
      "v_mfma_f32_16x16x32_bf16 a[168:171], v[236:239], a[240:243], a[168:171]\n\t"
      "ds_read_b128 a[20:23], v57 offset:256\n\t"
      "ds_write_b64 v0, v[40:41] offset:0\n\t"
      "v_mfma_f32_16x16x32_bf16 a[172:175], a[140:143], a[240:243], a[172:175]\n\t"
      "ds_write_b64 v0, v[42:43] offset:32\n\t"
      "v_mfma_f32_16x16x32_bf16 a[176:179], a[144:147], a[240:243], a[176:179]\n\t"
      "s_waitcnt lgkmcnt(8)\n\t"
      "v_readfirstlane_b32 s91, v63\n\t"
      "v_mfma_f32_16x16x32_bf16 a[156:159], v[240:243], a[244:247], a[156:159]\n\t"
      "ds_write_b64 v0, v[44:45] offset:64\n\t"
      "s_add_u32 m0, s18, 74240\n\t"
      "v_mfma_f32_16x16x32_bf16 a[160:163], v[244:247], a[244:247], a[160:163]\n\t"
      "s_bitcmp1_b32 s91, 2\n\t"
      "s_cselect_b32 s48, 24576, 0\n\t"
      "s_add_u32 s48, s48, s14\n\t"
      "v_mfma_f32_16x16x32_bf16 a[164:167], v[248:251], a[244:247], a[164:167]\n\t"
      "s_add_u32 s48, s48, 1572864\n\t"
      "buffer_load_dwordx4 v2, s[40:43], s48 offen nt lds\n\t"
      "s_mov_b32 s59, s91\n\t"
      "v_mfma_f32_16x16x32_bf16 a[168:171], v[252:255], a[244:247], a[168:171]\n\t"
      "ds_write_b64 v0, v[46:47] offset:96\n\t"
      "buffer_load_dwordx4 v3, s[40:43], s48 offen offset:1024 nt lds\n\t"
      "v_mfma_f32_16x16x32_bf16 a[172:175], a[148:151], a[244:247], a[172:175]\n\t"
      "ds_write_b64 v0, v[48:49] offset:128\n\t"
      "buffer_load_dwordx4 v4, s[40:43], s48 offen offset:2048 nt lds\n\t"
      "v_mfma_f32_16x16x32_bf16 a[176:179], a[152:155], a[244:247], a[176:179]\n\t"
      "ds_write_b64 v0, v[50:51] offset:160\n\t"
      "s_lshr_b32 s57, s91, 3\n\t"
      "s_bitcmp1_b32 s91, 3\n\t"
      "s_cselect_b32 s48, 24576, 0\n\t"
      "s_add_u32 s48, s48, s14\n\t"
      "s_add_u32 s48, s48, 2359296\n\t"
      "s_or_b32 s30, s17, s16\n\t"
      "s_cbranch_scc1 .Lno5_%=\n\t"
      "s_sub_u32 s68, s68, s64\n\t"
      "s_sub_u32 s70, s70, s66\n\t"
      "s_mul_i32 s64, s68, 100\n\t"
      "s_mul_i32 s66, s70, 1750\n\t"
      "s_sub_i32 s66, s64, s66\n\t"
      "s_ashr_i32 s66, s66, 9\n\t"
      "s_add_i32 s66, s66, 128\n\t"
      "s_max_i32 s66, s66, 1\n\t"
      "s_min_i32 s66, s66, 255\n\t"
      "s_lshl_b32 s66, s66, 17\n\t"
      "s_or_b32 s90, s90, s66\n\t"
      "v_mov_b32 v54, s90\n\t"
      "v_mov_b32 v55, s19\n\t"
      "global_store_dword v55, v54, s[12:13] sc1\n\t"
      ".Lno5_%=:\n\t"
      "s_waitcnt lgkmcnt(11)\n\t"
      "v_mfma_f32_16x16x32_bf16 v[16:19], v[64:67], a[0:3], 0\n\t"
      "ds_read_b128 a[24:27], v58 offset:256\n\t"
      "ds_read_b128 a[28:31], v59 offset:256\n\t"
      "s_waitcnt lgkmcnt(12)\n\t"
      "v_mfma_f32_16x16x32_bf16 v[16:19], v[80:83], a[4:7], v[16:19]\n\t"
      "ds_read_b128 a[32:35], v56 offset:512\n\t"
      "ds_read_b128 a[36:39], v57 offset:512\n\t"
      "s_waitcnt lgkmcnt(13)\n\t"
      "v_mfma_f32_16x16x32_bf16 v[16:19], v[96:99], a[8:11], v[16:19]\n\t"
      "ds_read_b128 a[40:43], v58 offset:512\n\t"
      "ds_read_b128 a[44:47], v59 offset:512\n\t"
      "s_waitcnt lgkmcnt(14)\n\t"
      "v_mfma_f32_16x16x32_bf16 v[16:19], v[112:115], a[12:15], v[16:19]\n\t"
      "v_accvgpr_read_b32 v52, a156\n\t"
      "v_accvgpr_read_b32 v53, a157\n\t"
      "s_waitcnt lgkmcnt(13)\n\t"
      "v_mfma_f32_16x16x32_bf16 v[16:19], v[128:131], a[16:19], v[16:19]\n\t"
      "v_accvgpr_read_b32 v54, a158\n\t"
      "v_accvgpr_read_b32 v55, a159\n\t"
      "s_waitcnt lgkmcnt(12)\n\t"
      "v_mfma_f32_16x16x32_bf16 v[16:19], v[144:147], a[20:23], v[16:19]\n\t"
      "v_cvt_pk_bf16_f32 v40, v52, v53\n\t"
      "v_cvt_pk_bf16_f32 v41, v54, v55\n\t"
      "v_mfma_f32_16x16x32_bf16 v[20:23], v[68:71], a[0:3], 0\n\t"
      "ds_write_b64 v0, v[40:41] offset:12544\n\t"
      "v_mfma_f32_16x16x32_bf16 v[20:23], v[84:87], a[4:7], v[20:23]\n\t"
      "s_waitcnt lgkmcnt(7)\n\t"
      "s_barrier\n\t"
      "v_mfma_f32_16x16x32_bf16 v[20:23], v[100:103], a[8:11], v[20:23]\n\t"
      "ds_read_b128 a[48:51], v8 offset:0\n\t"
      "ds_read_b128 a[52:55], v60 offset:1024\n\t"
      "ds_read_b128 a[56:59], v61 offset:2048\n\t"
      "v_mfma_f32_16x16x32_bf16 v[20:23], v[116:119], a[12:15], v[20:23]\n\t"
      "v_accvgpr_read_b32 v52, a160\n\t"
      "v_accvgpr_read_b32 v53, a161\n\t"
      "v_mfma_f32_16x16x32_bf16 v[20:23], v[132:135], a[16:19], v[20:23]\n\t"
      "v_accvgpr_read_b32 v54, a162\n\t"
      "v_accvgpr_read_b32 v55, a163\n\t"
      "v_mfma_f32_16x16x32_bf16 v[20:23], v[148:151], a[20:23], v[20:23]\n\t"
      "v_cvt_pk_bf16_f32 v42, v52, v53\n\t"
      "v_cvt_pk_bf16_f32 v43, v54, v55\n\t"
      "v_mfma_f32_16x16x32_bf16 v[24:27], v[72:75], a[0:3], 0\n\t"
      "ds_write_b64 v0, v[42:43] offset:12576\n\t"
      "s_waitcnt lgkmcnt(3)\n\t"
      "v_mfma_f32_16x16x32_bf16 v[24:27], v[88:91], a[4:7], v[24:27]\n\t"
      "buffer_store_dwordx4 a[48:51], v9, s[44:47], s49 offen sc1\n\t"
      "v_mfma_f32_16x16x32_bf16 v[24:27], v[104:107], a[8:11], v[24:27]\n\t"
      "v_accvgpr_read_b32 v52, a164\n\t"
      "v_accvgpr_read_b32 v53, a165\n\t"
      "v_mfma_f32_16x16x32_bf16 v[24:27], v[120:123], a[12:15], v[24:27]\n\t"
      "v_accvgpr_read_b32 v54, a166\n\t"
      "v_accvgpr_read_b32 v55, a167\n\t"
      "v_mfma_f32_16x16x32_bf16 v[24:27], v[136:139], a[16:19], v[24:27]\n\t"
      "v_cvt_pk_bf16_f32 v44, v52, v53\n\t"
      "v_cvt_pk_bf16_f32 v45, v54, v55\n\t"
      "v_mfma_f32_16x16x32_bf16 v[24:27], v[152:155], a[20:23], v[24:27]\n\t"
      "ds_write_b64 v0, v[44:45] offset:12608\n\t"
      "s_waitcnt lgkmcnt(3)\n\t"
      "v_mfma_f32_16x16x32_bf16 v[28:31], v[76:79], a[0:3], 0\n\t"
      "buffer_store_dwordx4 a[52:55], v10, s[44:47], s49 offen offset:1024 sc1\n\t"
      "v_mfma_f32_16x16x32_bf16 v[28:31], v[92:95], a[4:7], v[28:31]\n\t"
      "v_accvgpr_read_b32 v52, a168\n\t"
      "v_accvgpr_read_b32 v53, a169\n\t"
      "v_mfma_f32_16x16x32_bf16 v[28:31], v[108:111], a[8:11], v[28:31]\n\t"
      "v_accvgpr_read_b32 v54, a170\n\t"
      "v_accvgpr_read_b32 v55, a171\n\t"
      "v_mfma_f32_16x16x32_bf16 v[28:31], v[124:127], a[12:15], v[28:31]\n\t"
      "v_cvt_pk_bf16_f32 v46, v52, v53\n\t"
      "v_cvt_pk_bf16_f32 v47, v54, v55\n\t"
      "v_mfma_f32_16x16x32_bf16 v[28:31], v[140:143], a[16:19], v[28:31]\n\t"
      "ds_write_b64 v0, v[46:47] offset:12640\n\t"
      "s_waitcnt lgkmcnt(3)\n\t"
      "v_mfma_f32_16x16x32_bf16 v[28:31], v[156:159], a[20:23], v[28:31]\n\t"
      "buffer_store_dwordx4 a[56:59], v11, s[44:47], s49 offen offset:2048 sc1\n\t"
      "v_mfma_f32_16x16x32_bf16 v[32:35], a[60:63], a[0:3], 0\n\t"
      "v_accvgpr_read_b32 v52, a172\n\t"
      "v_accvgpr_read_b32 v53, a173\n\t"
      "v_mfma_f32_16x16x32_bf16 v[32:35], a[68:71], a[4:7], v[32:35]\n\t"
      "v_accvgpr_read_b32 v54, a174\n\t"
      "v_accvgpr_read_b32 v55, a175\n\t"
      "v_mfma_f32_16x16x32_bf16 v[32:35], a[76:79], a[8:11], v[32:35]\n\t"
      "v_cvt_pk_bf16_f32 v48, v52, v53\n\t"
      "v_cvt_pk_bf16_f32 v49, v54, v55\n\t"
      "v_mfma_f32_16x16x32_bf16 v[32:35], a[84:87], a[12:15], v[32:35]\n\t"
      "ds_write_b64 v0, v[48:49] offset:12672\n\t"
      "v_mfma_f32_16x16x32_bf16 v[32:35], a[92:95], a[16:19], v[32:35]\n\t"
      "v_accvgpr_read_b32 v52, a176\n\t"
      "v_accvgpr_read_b32 v53, a177\n\t"
      "v_mfma_f32_16x16x32_bf16 v[32:35], a[100:103], a[20:23], v[32:35]\n\t"
      "v_accvgpr_read_b32 v54, a178\n\t"
      "v_accvgpr_read_b32 v55, a179\n\t"
      "v_mfma_f32_16x16x32_bf16 v[36:39], a[64:67], a[0:3], 0\n\t"
      "v_cvt_pk_bf16_f32 v50, v52, v53\n\t"
      "v_cvt_pk_bf16_f32 v51, v54, v55\n\t"
      "v_mfma_f32_16x16x32_bf16 v[36:39], a[72:75], a[4:7], v[36:39]\n\t"
      "ds_write_b64 v0, v[50:51] offset:12704\n\t"
      "v_mfma_f32_16x16x32_bf16 v[36:39], a[80:83], a[8:11], v[36:39]\n\t"
      "v_add_u32 v52, s51, v15\n\t"
      "v_xor_b32 v53, 64, v52\n\t"
      "v_mfma_f32_16x16x32_bf16 v[36:39], a[88:91], a[12:15], v[36:39]\n\t"
      "v_xor_b32 v54, 128, v52\n\t"
      "v_xor_b32 v55, 192, v52\n\t"
      "v_mfma_f32_16x16x32_bf16 v[36:39], a[96:99], a[16:19], v[36:39]\n\t"
      "s_waitcnt lgkmcnt(0)\n\t"
      "s_add_u32 s38, s36, s39\n\t"
      "s_sub_u32 s28, s36, s28\n\t"
      "s_sub_i32 s28, s28, 515\n\t"
      "s_max_i32 s28, s28, 0\n\t"
      "s_min_i32 s28, s28, 60\n\t"
      "s_and_b32 s39, s32, 15\n\t"
      "s_cselect_b32 s53, s29, 0\n\t"
      "s_memrealtime s[36:37]\n\t"
      "v_mfma_f32_16x16x32_bf16 v[36:39], a[104:107], a[20:23], v[36:39]\n\t"
      "s_add_u32 s39, s39, s53\n\t"
      "s_lshr_b64 s[32:33], s[32:33], 4\n\t"
      "s_add_u32 m0, s18, 86528\n\t"
      "v_mfma_f32_16x16x32_bf16 v[16:19], v[160:163], a[24:27], v[16:19]\n\t"
      "s_bitcmp1_b32 s91, 2\n\t"
      "s_cselect_b32 s54, 24576, 0\n\t"
      "s_add_u32 s54, s54, s14\n\t"
      "s_add_u32 s54, s54, 1572864\n\t"
      "v_mfma_f32_16x16x32_bf16 v[16:19], v[176:179], a[28:31], v[16:19]\n\t"
      "s_waitcnt vmcnt(6)\n\t"
      "s_barrier\n\t"
      "v_mfma_f32_16x16x32_bf16 v[16:19], v[192:195], a[32:35], v[16:19]\n\t"
      "ds_read_b128 a[0:3], v52 offset:0\n\t"
      "ds_read_b128 a[188:191], v8 offset:12544\n\t"
      "buffer_load_dwordx4 v5, s[40:43], s54 offen nt lds\n\t"
      "v_mfma_f32_16x16x32_bf16 v[16:19], v[208:211], a[36:39], v[16:19]\n\t"
      "ds_read_b128 a[4:7], v53 offset:0\n\t"
      "ds_read_b128 a[192:195], v60 offset:13568\n\t"
      "v_mfma_f32_16x16x32_bf16 v[16:19], v[224:227], a[40:43], v[16:19]\n\t"
      "ds_read_b128 a[8:11], v54 offset:0\n\t"
      "ds_read_b128 a[196:199], v61 offset:14592\n\t"
      "buffer_load_dwordx4 v6, s[40:43], s54 offen offset:1024 nt lds\n\t"
      "v_mfma_f32_16x16x32_bf16 v[16:19], v[240:243], a[44:47], v[16:19]\n\t"
      "ds_read_b128 a[12:15], v55 offset:0\n\t"
      "v_mfma_f32_16x16x32_bf16 v[20:23], v[164:167], a[24:27], v[20:23]\n\t"
      "ds_read_b128 a[16:19], v52 offset:256\n\t"
      "buffer_load_dwordx4 v7, s[40:43], s54 offen offset:2048 nt lds\n\t"
      "v_mfma_f32_16x16x32_bf16 v[20:23], v[180:183], a[28:31], v[20:23]\n\t"
      "ds_read_b128 a[20:23], v53 offset:256\n\t"
      "s_add_u32 m0, s18, s50\n\t"
      "v_mfma_f32_16x16x32_bf16 v[20:23], v[196:199], a[32:35], v[20:23]\n\t"
      "s_add_u32 s51, s51, 12288\n\t"
      "s_cmp_eq_u32 s51, 160256\n\t"
      "s_cselect_b32 s51, 25088, s51\n\t"
      "s_waitcnt lgkmcnt(7)\n\t"
      "v_mfma_f32_16x16x32_bf16 v[20:23], v[212:215], a[36:39], v[20:23]\n\t"
      "buffer_store_dwordx4 a[188:191], v12, s[44:47], s49 offen sc1\n\t"
      "v_mfma_f32_16x16x32_bf16 v[20:23], v[228:231], a[40:43], v[20:23]\n\t"
      "v_mfma_f32_16x16x32_bf16 v[20:23], v[244:247], a[44:47], v[20:23]\n\t"
      "s_waitcnt lgkmcnt(0)\n\t"
      "s_sub_u32 s53, s36, s38\n\t"
      "s_cmp_lt_i32 s53, 0\n\t"
      "v_mfma_f32_16x16x32_bf16 v[24:27], v[168:171], a[24:27], v[24:27]\n\t"
      "s_cbranch_scc1 .Lspinf_%=\n\t"
      ".Lspunf_%=:\n\t"
      "s_sub_i32 s53, s53, 30\n\t"
      "s_max_i32 s53, s53, 0\n\t"
      "v_mfma_f32_16x16x32_bf16 v[24:27], v[184:187], a[28:31], v[24:27]\n\t"
      "s_add_u32 s38, s38, s53\n\t"
      "s_add_u32 s38, s38, s39\n\t"
      "s_add_u32 s28, s28, s53\n\t"
      "v_mfma_f32_16x16x32_bf16 v[24:27], v[200:203], a[32:35], v[24:27]\n\t"
      "v_cvt_pk_bf16_f32 v40, v16, v17\n\t"
      "v_cvt_pk_bf16_f32 v41, v18, v19\n\t"
      "v_mfma_f32_16x16x32_bf16 v[24:27], v[216:219], a[36:39], v[24:27]\n\t"
      "ds_write_b64 v0, v[40:41] offset:0\n\t"
      "v_mfma_f32_16x16x32_bf16 v[24:27], v[232:235], a[40:43], v[24:27]\n\t"
      "buffer_load_dwordx4 v2, s[40:43], s48 offen nt lds\n\t"
      "v_mfma_f32_16x16x32_bf16 v[24:27], v[248:251], a[44:47], v[24:27]\n\t"
      "buffer_store_dwordx4 a[192:195], v13, s[44:47], s49 offen offset:1024 sc1\n\t"
      "v_mfma_f32_16x16x32_bf16 v[28:31], v[172:175], a[24:27], v[28:31]\n\t"
      "v_mfma_f32_16x16x32_bf16 v[28:31], v[188:191], a[28:31], v[28:31]\n\t"
      "v_cvt_pk_bf16_f32 v42, v20, v21\n\t"
      "v_cvt_pk_bf16_f32 v43, v22, v23\n\t"
      "v_mfma_f32_16x16x32_bf16 v[28:31], v[204:207], a[32:35], v[28:31]\n\t"
      "ds_write_b64 v0, v[42:43] offset:32\n\t"
      "v_mfma_f32_16x16x32_bf16 v[28:31], v[220:223], a[36:39], v[28:31]\n\t"
      "buffer_load_dwordx4 v3, s[40:43], s48 offen offset:1024 nt lds\n\t"
      "v_mfma_f32_16x16x32_bf16 v[28:31], v[236:239], a[40:43], v[28:31]\n\t"
      "buffer_store_dwordx4 a[196:199], v14, s[44:47], s49 offen offset:2048 sc1\n\t"
      "v_mfma_f32_16x16x32_bf16 v[28:31], v[252:255], a[44:47], v[28:31]\n\t"
      "s_add_u32 s58, s58, 786432\n\t"
      "s_lshr_b32 s59, s59, 1\n\t"
      "v_mfma_f32_16x16x32_bf16 v[32:35], a[108:111], a[24:27], v[32:35]\n\t"
      "s_bitcmp1_b32 s59, 0\n\t"
      "s_cselect_b32 s49, 24576, 0\n\t"
      "s_add_u32 s49, s49, s58\n\t"
      "v_mfma_f32_16x16x32_bf16 v[32:35], a[116:119], a[28:31], v[32:35]\n\t"
      "v_cvt_pk_bf16_f32 v44, v24, v25\n\t"
      "v_cvt_pk_bf16_f32 v45, v26, v27\n\t"
      "v_mfma_f32_16x16x32_bf16 v[32:35], a[124:127], a[32:35], v[32:35]\n\t"
      "ds_write_b64 v0, v[44:45] offset:64\n\t"
      "v_mfma_f32_16x16x32_bf16 v[32:35], a[132:135], a[36:39], v[32:35]\n\t"
      "buffer_load_dwordx4 v4, s[40:43], s48 offen offset:2048 nt lds\n\t"
      "v_mfma_f32_16x16x32_bf16 v[32:35], a[140:143], a[40:43], v[32:35]\n\t"
      "s_add_u32 s50, s50, 12288\n\t"
      "v_mfma_f32_16x16x32_bf16 v[32:35], a[148:151], a[44:47], v[32:35]\n\t"
      "v_cvt_pk_bf16_f32 v46, v28, v29\n\t"
      "v_cvt_pk_bf16_f32 v47, v30, v31\n\t"
      "v_mfma_f32_16x16x32_bf16 v[36:39], a[112:115], a[24:27], v[36:39]\n\t"
      "ds_write_b64 v0, v[46:47] offset:96\n\t"
      "v_mfma_f32_16x16x32_bf16 v[36:39], a[120:123], a[28:31], v[36:39]\n\t"
      "s_cmp_eq_u32 s50, 160256\n\t"
      "s_cselect_b32 s50, 25088, s50\n\t"
      "v_mfma_f32_16x16x32_bf16 v[36:39], a[128:131], a[32:35], v[36:39]\n\t"
      "v_mfma_f32_16x16x32_bf16 v[36:39], a[136:139], a[36:39], v[36:39]\n\t"
      "v_mfma_f32_16x16x32_bf16 v[36:39], a[144:147], a[40:43], v[36:39]\n\t"
      "v_cvt_pk_bf16_f32 v48, v32, v33\n\t"
      "v_cvt_pk_bf16_f32 v49, v34, v35\n\t"
      "v_mfma_f32_16x16x32_bf16 v[36:39], a[152:155], a[44:47], v[36:39]\n\t"
      "ds_write_b64 v0, v[48:49] offset:128\n\t"
      "s_cmp_eq_u32 s17, 6144\n\t"
      "s_cbranch_scc1 .Lmid1_%=\n\t"
      "s_cmp_eq_u32 s17, 12288\n\t"
      "s_cbranch_scc1 .Lmid2_%=\n\t"
      "s_cmp_eq_u32 s17, 18432\n\t"
      "s_cbranch_scc1 .Lmid3_%=\n\t"
      "global_load_dword v63, v62, s[12:13] sc1\n\t"
      "s_branch .Lmid0_%=\n\t"
      ".Lloop0_%=:\n\t"
      "v_mfma_f32_16x16x32_bf16 v[16:19], v[64:67], a[0:3], 0\n\t"
      "ds_read_b128 a[24:27], v58 offset:256\n\t"
      "v_mfma_f32_16x16x32_bf16 v[16:19], v[80:83], a[4:7], v[16:19]\n\t"
      "ds_read_b128 a[28:31], v59 offset:256\n\t"
      "v_mfma_f32_16x16x32_bf16 v[16:19], v[96:99], a[8:11], v[16:19]\n\t"
      "ds_read_b128 a[32:35], v56 offset:512\n\t"
      "v_mfma_f32_16x16x32_bf16 v[16:19], v[112:115], a[12:15], v[16:19]\n\t"
      "ds_read_b128 a[36:39], v57 offset:512\n\t"
      "v_mfma_f32_16x16x32_bf16 v[16:19], v[128:131], a[16:19], v[16:19]\n\t"
      "ds_read_b128 a[40:43], v58 offset:512\n\t"
      "v_mfma_f32_16x16x32_bf16 v[16:19], v[144:147], a[20:23], v[16:19]\n\t"
      "ds_read_b128 a[44:47], v59 offset:512\n\t"
      "v_mfma_f32_16x16x32_bf16 v[20:23], v[68:71], a[0:3], 0\n\t"
      "v_cvt_pk_bf16_f32 v50, v36, v37\n\t"
      "v_cvt_pk_bf16_f32 v51, v38, v39\n\t"
      "v_mfma_f32_16x16x32_bf16 v[20:23], v[84:87], a[4:7], v[20:23]\n\t"
      "ds_write_b64 v0, v[50:51] offset:12704\n\t"
      "v_mfma_f32_16x16x32_bf16 v[20:23], v[100:103], a[8:11], v[20:23]\n\t"
      "v_add_u32 v52, s51, v15\n\t"
      "v_xor_b32 v53, 64, v52\n\t"
      "v_mfma_f32_16x16x32_bf16 v[20:23], v[116:119], a[12:15], v[20:23]\n\t"
      "v_xor_b32 v54, 128, v52\n\t"
      "v_xor_b32 v55, 192, v52\n\t"
      "v_mfma_f32_16x16x32_bf16 v[20:23], v[132:135], a[16:19], v[20:23]\n\t"
      "s_add_u32 s56, s56, 786432\n\t"
      "s_lshr_b32 s57, s57, 1\n\t"
      "v_mfma_f32_16x16x32_bf16 v[20:23], v[148:151], a[20:23], v[20:23]\n\t"
      "s_bitcmp1_b32 s57, 0\n\t"
      "s_cselect_b32 s48, 24576, 0\n\t"
      "s_add_u32 s48, s48, s56\n\t"
      "v_mfma_f32_16x16x32_bf16 v[24:27], v[72:75], a[0:3], 0\n\t"
      "v_mfma_f32_16x16x32_bf16 v[24:27], v[88:91], a[4:7], v[24:27]\n\t"
      "v_mfma_f32_16x16x32_bf16 v[24:27], v[104:107], a[8:11], v[24:27]\n\t"
      "s_waitcnt vmcnt(12)\n\t"
      "s_waitcnt lgkmcnt(0)\n\t"
      "s_barrier\n\t"
      "v_mfma_f32_16x16x32_bf16 v[24:27], v[120:123], a[12:15], v[24:27]\n\t"
      "v_mfma_f32_16x16x32_bf16 v[24:27], v[136:139], a[16:19], v[24:27]\n\t"
      "v_mfma_f32_16x16x32_bf16 v[24:27], v[152:155], a[20:23], v[24:27]\n\t"
      "v_mfma_f32_16x16x32_bf16 v[28:31], v[76:79], a[0:3], 0\n\t"
      "ds_read_b128 a[48:51], v8 offset:12544\n\t"
      "v_mfma_f32_16x16x32_bf16 v[28:31], v[92:95], a[4:7], v[28:31]\n\t"
      "ds_read_b128 a[52:55], v60 offset:13568\n\t"
      "v_mfma_f32_16x16x32_bf16 v[28:31], v[108:111], a[8:11], v[28:31]\n\t"
      "ds_read_b128 a[56:59], v61 offset:14592\n\t"
      "v_mfma_f32_16x16x32_bf16 v[28:31], v[124:127], a[12:15], v[28:31]\n\t"
      "s_and_b32 s39, s32, 15\n\t"
      "s_cselect_b32 s53, s29, 0\n\t"
      "v_mfma_f32_16x16x32_bf16 v[28:31], v[140:143], a[16:19], v[28:31]\n\t"
      "s_add_u32 s39, s39, s53\n\t"
      "s_lshr_b64 s[32:33], s[32:33], 4\n\t"
      "v_mfma_f32_16x16x32_bf16 v[28:31], v[156:159], a[20:23], v[28:31]\n\t"
      "s_cmp_le_u32 s52, 12\n\t"
      "s_cselect_b32 s53, 3, 0\n\t"
      "s_min_u32 s53, s28, s53\n\t"
      "v_mfma_f32_16x16x32_bf16 v[32:35], a[60:63], a[0:3], 0\n\t"
      "s_sub_u32 s28, s28, s53\n\t"
      "s_sub_u32 s39, s39, s53\n\t"
      "v_mfma_f32_16x16x32_bf16 v[32:35], a[68:71], a[4:7], v[32:35]\n\t"
      "s_waitcnt lgkmcnt(2)\n\t"
      "v_mfma_f32_16x16x32_bf16 v[32:35], a[76:79], a[8:11], v[32:35]\n\t"
      "buffer_store_dwordx4 a[48:51], v12, s[44:47], s49 offen sc1\n\t"
      "v_mfma_f32_16x16x32_bf16 v[32:35], a[84:87], a[12:15], v[32:35]\n\t"
      "v_mfma_f32_16x16x32_bf16 v[32:35], a[92:95], a[16:19], v[32:35]\n\t"
      "v_mfma_f32_16x16x32_bf16 v[32:35], a[100:103], a[20:23], v[32:35]\n\t"
      "s_add_u32 m0, s18, s50\n\t"
      "v_mfma_f32_16x16x32_bf16 v[36:39], a[64:67], a[0:3], 0\n\t"
      "buffer_load_dwordx4 v2, s[40:43], s48 offen nt lds\n\t"
      "v_mfma_f32_16x16x32_bf16 v[36:39], a[72:75], a[4:7], v[36:39]\n\t"
      "s_memrealtime s[36:37]\n\t"
      "v_mfma_f32_16x16x32_bf16 v[36:39], a[80:83], a[8:11], v[36:39]\n\t"
      "v_mfma_f32_16x16x32_bf16 v[36:39], a[88:91], a[12:15], v[36:39]\n\t"
      "s_waitcnt lgkmcnt(1)\n\t"
      "v_mfma_f32_16x16x32_bf16 v[36:39], a[96:99], a[16:19], v[36:39]\n\t"
      "buffer_store_dwordx4 a[52:55], v13, s[44:47], s49 offen offset:1024 sc1\n\t"
      "v_mfma_f32_16x16x32_bf16 v[36:39], a[104:107], a[20:23], v[36:39]\n\t"
      "v_mfma_f32_16x16x32_bf16 v[16:19], v[160:163], a[24:27], v[16:19]\n\t"
      "ds_read_b128 a[0:3], v52 offset:0\n\t"
      "v_mfma_f32_16x16x32_bf16 v[16:19], v[176:179], a[28:31], v[16:19]\n\t"
      "ds_read_b128 a[4:7], v53 offset:0\n\t"
      "v_mfma_f32_16x16x32_bf16 v[16:19], v[192:195], a[32:35], v[16:19]\n\t"
      "ds_read_b128 a[8:11], v54 offset:0\n\t"
      "v_mfma_f32_16x16x32_bf16 v[16:19], v[208:211], a[36:39], v[16:19]\n\t"
      "ds_read_b128 a[12:15], v55 offset:0\n\t"
      "v_mfma_f32_16x16x32_bf16 v[16:19], v[224:227], a[40:43], v[16:19]\n\t"
      "ds_read_b128 a[16:19], v52 offset:256\n\t"
      "v_mfma_f32_16x16x32_bf16 v[16:19], v[240:243], a[44:47], v[16:19]\n\t"
      "ds_read_b128 a[20:23], v53 offset:256\n\t"
      "v_mfma_f32_16x16x32_bf16 v[20:23], v[164:167], a[24:27], v[20:23]\n\t"
      "s_add_u32 s51, s51, 12288\n\t"
      "s_cmp_eq_u32 s51, 160256\n\t"
      "s_cselect_b32 s51, 25088, s51\n\t"
      "v_mfma_f32_16x16x32_bf16 v[20:23], v[180:183], a[28:31], v[20:23]\n\t"
      "s_waitcnt lgkmcnt(6)\n\t"
      "v_mfma_f32_16x16x32_bf16 v[20:23], v[196:199], a[32:35], v[20:23]\n\t"
      "buffer_store_dwordx4 a[56:59], v14, s[44:47], s49 offen offset:2048 sc1\n\t"
      "v_mfma_f32_16x16x32_bf16 v[20:23], v[212:215], a[36:39], v[20:23]\n\t"
      "v_mfma_f32_16x16x32_bf16 v[20:23], v[228:231], a[40:43], v[20:23]\n\t"
      "s_waitcnt lgkmcnt(0)\n\t"
      "s_sub_u32 s53, s36, s38\n\t"
      "s_cmp_lt_i32 s53, 0\n\t"
      "v_mfma_f32_16x16x32_bf16 v[20:23], v[244:247], a[44:47], v[20:23]\n\t"
      "s_cbranch_scc1 .Lspin0_%=\n\t"
      ".Lspun0_%=:\n\t"
      "s_sub_i32 s53, s53, 30\n\t"
      "s_max_i32 s53, s53, 0\n\t"
      "v_mfma_f32_16x16x32_bf16 v[24:27], v[168:171], a[24:27], v[24:27]\n\t"
      "s_add_u32 s38, s38, s53\n\t"
      "s_add_u32 s38, s38, s39\n\t"
      "s_add_u32 s28, s28, s53\n\t"
      "v_mfma_f32_16x16x32_bf16 v[24:27], v[184:187], a[28:31], v[24:27]\n\t"
      "v_cvt_pk_bf16_f32 v40, v16, v17\n\t"
      "v_cvt_pk_bf16_f32 v41, v18, v19\n\t"
      "v_mfma_f32_16x16x32_bf16 v[24:27], v[200:203], a[32:35], v[24:27]\n\t"
      "ds_write_b64 v0, v[40:41] offset:0\n\t"
      "v_mfma_f32_16x16x32_bf16 v[24:27], v[216:219], a[36:39], v[24:27]\n\t"
      "buffer_load_dwordx4 v3, s[40:43], s48 offen offset:1024 nt lds\n\t"
      "v_mfma_f32_16x16x32_bf16 v[24:27], v[232:235], a[40:43], v[24:27]\n\t"
      "s_add_u32 s58, s58, 786432\n\t"
      "s_lshr_b32 s59, s59, 1\n\t"
      "v_mfma_f32_16x16x32_bf16 v[24:27], v[248:251], a[44:47], v[24:27]\n\t"
      "s_bitcmp1_b32 s59, 0\n\t"
      "s_cselect_b32 s49, 24576, 0\n\t"
      "s_add_u32 s49, s49, s58\n\t"
      "v_mfma_f32_16x16x32_bf16 v[28:31], v[172:175], a[24:27], v[28:31]\n\t"
      "v_mfma_f32_16x16x32_bf16 v[28:31], v[188:191], a[28:31], v[28:31]\n\t"
      "v_cvt_pk_bf16_f32 v42, v20, v21\n\t"
      "v_cvt_pk_bf16_f32 v43, v22, v23\n\t"
      "v_mfma_f32_16x16x32_bf16 v[28:31], v[204:207], a[32:35], v[28:31]\n\t"
      "ds_write_b64 v0, v[42:43] offset:32\n\t"
      "v_mfma_f32_16x16x32_bf16 v[28:31], v[220:223], a[36:39], v[28:31]\n\t"
      "v_mfma_f32_16x16x32_bf16 v[28:31], v[236:239], a[40:43], v[28:31]\n\t"
      "v_mfma_f32_16x16x32_bf16 v[28:31], v[252:255], a[44:47], v[28:31]\n\t"
      "s_cmp_eq_u32 s52, 14\n\t"
      "s_cbranch_scc1 .Lshb_%=\n\t"
      ".Lshbr_%=:\n\t"
      "v_mfma_f32_16x16x32_bf16 v[32:35], a[108:111], a[24:27], v[32:35]\n\t"
      "v_mfma_f32_16x16x32_bf16 v[32:35], a[116:119], a[28:31], v[32:35]\n\t"
      "v_cvt_pk_bf16_f32 v44, v24, v25\n\t"
      "v_cvt_pk_bf16_f32 v45, v26, v27\n\t"
      "v_mfma_f32_16x16x32_bf16 v[32:35], a[124:127], a[32:35], v[32:35]\n\t"
      "ds_write_b64 v0, v[44:45] offset:64\n\t"
      "v_mfma_f32_16x16x32_bf16 v[32:35], a[132:135], a[36:39], v[32:35]\n\t"
      "buffer_load_dwordx4 v4, s[40:43], s48 offen offset:2048 nt lds\n\t"
      "v_mfma_f32_16x16x32_bf16 v[32:35], a[140:143], a[40:43], v[32:35]\n\t"
      "s_add_u32 s50, s50, 12288\n\t"
      "v_mfma_f32_16x16x32_bf16 v[32:35], a[148:151], a[44:47], v[32:35]\n\t"
      "v_cvt_pk_bf16_f32 v46, v28, v29\n\t"
      "v_cvt_pk_bf16_f32 v47, v30, v31\n\t"
      "v_mfma_f32_16x16x32_bf16 v[36:39], a[112:115], a[24:27], v[36:39]\n\t"
      "ds_write_b64 v0, v[46:47] offset:96\n\t"
      "v_mfma_f32_16x16x32_bf16 v[36:39], a[120:123], a[28:31], v[36:39]\n\t"
      "s_cmp_eq_u32 s50, 160256\n\t"
      "s_cselect_b32 s50, 25088, s50\n\t"
      "v_mfma_f32_16x16x32_bf16 v[36:39], a[128:131], a[32:35], v[36:39]\n\t"
      "v_mfma_f32_16x16x32_bf16 v[36:39], a[136:139], a[36:39], v[36:39]\n\t"
      "v_mfma_f32_16x16x32_bf16 v[36:39], a[144:147], a[40:43], v[36:39]\n\t"
      "v_cvt_pk_bf16_f32 v48, v32, v33\n\t"
      "v_cvt_pk_bf16_f32 v49, v34, v35\n\t"
      "v_mfma_f32_16x16x32_bf16 v[36:39], a[152:155], a[44:47], v[36:39]\n\t"
      "ds_write_b64 v0, v[48:49] offset:128\n\t"
      ".Lmid0_%=:\n\t"
      "v_mfma_f32_16x16x32_bf16 v[16:19], v[64:67], a[0:3], 0\n\t"
      "ds_read_b128 a[24:27], v54 offset:256\n\t"
      "v_mfma_f32_16x16x32_bf16 v[16:19], v[80:83], a[4:7], v[16:19]\n\t"
      "ds_read_b128 a[28:31], v55 offset:256\n\t"
      "v_mfma_f32_16x16x32_bf16 v[16:19], v[96:99], a[8:11], v[16:19]\n\t"
      "ds_read_b128 a[32:35], v52 offset:512\n\t"
      "v_mfma_f32_16x16x32_bf16 v[16:19], v[112:115], a[12:15], v[16:19]\n\t"
      "ds_read_b128 a[36:39], v53 offset:512\n\t"
      "v_mfma_f32_16x16x32_bf16 v[16:19], v[128:131], a[16:19], v[16:19]\n\t"
      "ds_read_b128 a[40:43], v54 offset:512\n\t"
      "v_mfma_f32_16x16x32_bf16 v[16:19], v[144:147], a[20:23], v[16:19]\n\t"
      "ds_read_b128 a[44:47], v55 offset:512\n\t"
      "v_mfma_f32_16x16x32_bf16 v[20:23], v[68:71], a[0:3], 0\n\t"
      "v_cvt_pk_bf16_f32 v50, v36, v37\n\t"
      "v_cvt_pk_bf16_f32 v51, v38, v39\n\t"
      "v_mfma_f32_16x16x32_bf16 v[20:23], v[84:87], a[4:7], v[20:23]\n\t"
      "ds_write_b64 v0, v[50:51] offset:160\n\t"
      "v_mfma_f32_16x16x32_bf16 v[20:23], v[100:103], a[8:11], v[20:23]\n\t"
      "v_add_u32 v56, s51, v15\n\t"
      "v_xor_b32 v57, 64, v56\n\t"
      "v_mfma_f32_16x16x32_bf16 v[20:23], v[116:119], a[12:15], v[20:23]\n\t"
      "v_xor_b32 v58, 128, v56\n\t"
      "v_xor_b32 v59, 192, v56\n\t"
      "v_mfma_f32_16x16x32_bf16 v[20:23], v[132:135], a[16:19], v[20:23]\n\t"
      "v_mfma_f32_16x16x32_bf16 v[20:23], v[148:151], a[20:23], v[20:23]\n\t"
      "v_mfma_f32_16x16x32_bf16 v[24:27], v[72:75], a[0:3], 0\n\t"
      "v_mfma_f32_16x16x32_bf16 v[24:27], v[88:91], a[4:7], v[24:27]\n\t"
      "v_mfma_f32_16x16x32_bf16 v[24:27], v[104:107], a[8:11], v[24:27]\n\t"
      "s_waitcnt vmcnt(12)\n\t"
      "s_waitcnt lgkmcnt(0)\n\t"
      "s_barrier\n\t"
      "v_mfma_f32_16x16x32_bf16 v[24:27], v[120:123], a[12:15], v[24:27]\n\t"
      "v_mfma_f32_16x16x32_bf16 v[24:27], v[136:139], a[16:19], v[24:27]\n\t"
      "v_mfma_f32_16x16x32_bf16 v[24:27], v[152:155], a[20:23], v[24:27]\n\t"
      "v_mfma_f32_16x16x32_bf16 v[28:31], v[76:79], a[0:3], 0\n\t"
      "ds_read_b128 a[48:51], v8 offset:0\n\t"
      "v_mfma_f32_16x16x32_bf16 v[28:31], v[92:95], a[4:7], v[28:31]\n\t"
      "ds_read_b128 a[52:55], v60 offset:1024\n\t"
      "v_mfma_f32_16x16x32_bf16 v[28:31], v[108:111], a[8:11], v[28:31]\n\t"
      "ds_read_b128 a[56:59], v61 offset:2048\n\t"
      "v_mfma_f32_16x16x32_bf16 v[28:31], v[124:127], a[12:15], v[28:31]\n\t"
      "s_and_b32 s39, s34, 15\n\t"
      "s_cselect_b32 s53, s29, 0\n\t"
      "v_mfma_f32_16x16x32_bf16 v[28:31], v[140:143], a[16:19], v[28:31]\n\t"
      "s_add_u32 s39, s39, s53\n\t"
      "s_lshr_b64 s[34:35], s[34:35], 4\n\t"
      "v_mfma_f32_16x16x32_bf16 v[28:31], v[156:159], a[20:23], v[28:31]\n\t"
      "s_cmp_le_u32 s52, 12\n\t"
      "s_cselect_b32 s53, 3, 0\n\t"
      "s_min_u32 s53, s28, s53\n\t"
      "v_mfma_f32_16x16x32_bf16 v[32:35], a[60:63], a[0:3], 0\n\t"
      "s_sub_u32 s28, s28, s53\n\t"
      "s_sub_u32 s39, s39, s53\n\t"
      "v_mfma_f32_16x16x32_bf16 v[32:35], a[68:71], a[4:7], v[32:35]\n\t"
      "s_waitcnt lgkmcnt(2)\n\t"
      "v_mfma_f32_16x16x32_bf16 v[32:35], a[76:79], a[8:11], v[32:35]\n\t"
      "buffer_store_dwordx4 a[48:51], v9, s[44:47], s49 offen sc1\n\t"
      "v_mfma_f32_16x16x32_bf16 v[32:35], a[84:87], a[12:15], v[32:35]\n\t"
      "v_mfma_f32_16x16x32_bf16 v[32:35], a[92:95], a[16:19], v[32:35]\n\t"
      "v_mfma_f32_16x16x32_bf16 v[32:35], a[100:103], a[20:23], v[32:35]\n\t"
      "s_add_u32 m0, s18, s50\n\t"
      "v_mfma_f32_16x16x32_bf16 v[36:39], a[64:67], a[0:3], 0\n\t"
      "buffer_load_dwordx4 v5, s[40:43], s48 offen nt lds\n\t"
      "v_mfma_f32_16x16x32_bf16 v[36:39], a[72:75], a[4:7], v[36:39]\n\t"
      "s_memrealtime s[36:37]\n\t"
      "v_mfma_f32_16x16x32_bf16 v[36:39], a[80:83], a[8:11], v[36:39]\n\t"
      "v_mfma_f32_16x16x32_bf16 v[36:39], a[88:91], a[12:15], v[36:39]\n\t"
      "s_waitcnt lgkmcnt(1)\n\t"
      "v_mfma_f32_16x16x32_bf16 v[36:39], a[96:99], a[16:19], v[36:39]\n\t"
      "buffer_store_dwordx4 a[52:55], v10, s[44:47], s49 offen offset:1024 sc1\n\t"
      "v_mfma_f32_16x16x32_bf16 v[36:39], a[104:107], a[20:23], v[36:39]\n\t"
      "v_mfma_f32_16x16x32_bf16 v[16:19], v[160:163], a[24:27], v[16:19]\n\t"
      "ds_read_b128 a[0:3], v56 offset:0\n\t"
      "v_mfma_f32_16x16x32_bf16 v[16:19], v[176:179], a[28:31], v[16:19]\n\t"
      "ds_read_b128 a[4:7], v57 offset:0\n\t"
      "v_mfma_f32_16x16x32_bf16 v[16:19], v[192:195], a[32:35], v[16:19]\n\t"
      "ds_read_b128 a[8:11], v58 offset:0\n\t"
      "v_mfma_f32_16x16x32_bf16 v[16:19], v[208:211], a[36:39], v[16:19]\n\t"
      "ds_read_b128 a[12:15], v59 offset:0\n\t"
      "v_mfma_f32_16x16x32_bf16 v[16:19], v[224:227], a[40:43], v[16:19]\n\t"
      "ds_read_b128 a[16:19], v56 offset:256\n\t"
      "v_mfma_f32_16x16x32_bf16 v[16:19], v[240:243], a[44:47], v[16:19]\n\t"
      "ds_read_b128 a[20:23], v57 offset:256\n\t"
      "v_mfma_f32_16x16x32_bf16 v[20:23], v[164:167], a[24:27], v[20:23]\n\t"
      "s_add_u32 s51, s51, 12288\n\t"
      "s_cmp_eq_u32 s51, 160256\n\t"
      "s_cselect_b32 s51, 25088, s51\n\t"
      "v_mfma_f32_16x16x32_bf16 v[20:23], v[180:183], a[28:31], v[20:23]\n\t"
      "s_waitcnt lgkmcnt(6)\n\t"
      "v_mfma_f32_16x16x32_bf16 v[20:23], v[196:199], a[32:35], v[20:23]\n\t"
      "buffer_store_dwordx4 a[56:59], v11, s[44:47], s49 offen offset:2048 sc1\n\t"
      "v_mfma_f32_16x16x32_bf16 v[20:23], v[212:215], a[36:39], v[20:23]\n\t"
      "v_mfma_f32_16x16x32_bf16 v[20:23], v[228:231], a[40:43], v[20:23]\n\t"
      "s_waitcnt lgkmcnt(0)\n\t"
      "s_sub_u32 s53, s36, s38\n\t"
      "s_cmp_lt_i32 s53, 0\n\t"
      "v_mfma_f32_16x16x32_bf16 v[20:23], v[244:247], a[44:47], v[20:23]\n\t"
      "s_cbranch_scc1 .Lspin1_%=\n\t"
      ".Lspun1_%=:\n\t"
      "s_sub_i32 s53, s53, 30\n\t"
      "s_max_i32 s53, s53, 0\n\t"
      "v_mfma_f32_16x16x32_bf16 v[24:27], v[168:171], a[24:27], v[24:27]\n\t"
      "s_add_u32 s38, s38, s53\n\t"
      "s_add_u32 s38, s38, s39\n\t"
      "s_add_u32 s28, s28, s53\n\t"
      "v_mfma_f32_16x16x32_bf16 v[24:27], v[184:187], a[28:31], v[24:27]\n\t"
      "v_cvt_pk_bf16_f32 v40, v16, v17\n\t"
      "v_cvt_pk_bf16_f32 v41, v18, v19\n\t"
      "v_mfma_f32_16x16x32_bf16 v[24:27], v[200:203], a[32:35], v[24:27]\n\t"
      "ds_write_b64 v0, v[40:41] offset:12544\n\t"
      "v_mfma_f32_16x16x32_bf16 v[24:27], v[216:219], a[36:39], v[24:27]\n\t"
      "buffer_load_dwordx4 v6, s[40:43], s48 offen offset:1024 nt lds\n\t"
      "v_mfma_f32_16x16x32_bf16 v[24:27], v[232:235], a[40:43], v[24:27]\n\t"
      "v_mfma_f32_16x16x32_bf16 v[24:27], v[248:251], a[44:47], v[24:27]\n\t"
      "v_mfma_f32_16x16x32_bf16 v[28:31], v[172:175], a[24:27], v[28:31]\n\t"
      "v_mfma_f32_16x16x32_bf16 v[28:31], v[188:191], a[28:31], v[28:31]\n\t"
      "v_cvt_pk_bf16_f32 v42, v20, v21\n\t"
      "v_cvt_pk_bf16_f32 v43, v22, v23\n\t"
      "v_mfma_f32_16x16x32_bf16 v[28:31], v[204:207], a[32:35], v[28:31]\n\t"
      "ds_write_b64 v0, v[42:43] offset:12576\n\t"
      "v_mfma_f32_16x16x32_bf16 v[28:31], v[220:223], a[36:39], v[28:31]\n\t"
      "v_mfma_f32_16x16x32_bf16 v[28:31], v[236:239], a[40:43], v[28:31]\n\t"
      "v_mfma_f32_16x16x32_bf16 v[28:31], v[252:255], a[44:47], v[28:31]\n\t"
      "v_mfma_f32_16x16x32_bf16 v[32:35], a[108:111], a[24:27], v[32:35]\n\t"
      "v_mfma_f32_16x16x32_bf16 v[32:35], a[116:119], a[28:31], v[32:35]\n\t"
      "v_cvt_pk_bf16_f32 v44, v24, v25\n\t"
      "v_cvt_pk_bf16_f32 v45, v26, v27\n\t"
      "v_mfma_f32_16x16x32_bf16 v[32:35], a[124:127], a[32:35], v[32:35]\n\t"
      "ds_write_b64 v0, v[44:45] offset:12608\n\t"
      "v_mfma_f32_16x16x32_bf16 v[32:35], a[132:135], a[36:39], v[32:35]\n\t"
      "buffer_load_dwordx4 v7, s[40:43], s48 offen offset:2048 nt lds\n\t"
      "v_mfma_f32_16x16x32_bf16 v[32:35], a[140:143], a[40:43], v[32:35]\n\t"
      "s_add_u32 s50, s50, 12288\n\t"
      "v_mfma_f32_16x16x32_bf16 v[32:35], a[148:151], a[44:47], v[32:35]\n\t"
      "v_cvt_pk_bf16_f32 v46, v28, v29\n\t"
      "v_cvt_pk_bf16_f32 v47, v30, v31\n\t"
      "v_mfma_f32_16x16x32_bf16 v[36:39], a[112:115], a[24:27], v[36:39]\n\t"
      "ds_write_b64 v0, v[46:47] offset:12640\n\t"
      "v_mfma_f32_16x16x32_bf16 v[36:39], a[120:123], a[28:31], v[36:39]\n\t"
      "s_cmp_eq_u32 s50, 160256\n\t"
      "s_cselect_b32 s50, 25088, s50\n\t"
      "v_mfma_f32_16x16x32_bf16 v[36:39], a[128:131], a[32:35], v[36:39]\n\t"
      "s_sub_u32 s52, s52, 1\n\t"
      "s_cmp_lg_u32 s52, 0\n\t"
      "v_mfma_f32_16x16x32_bf16 v[36:39], a[136:139], a[36:39], v[36:39]\n\t"
      "v_mfma_f32_16x16x32_bf16 v[36:39], a[144:147], a[40:43], v[36:39]\n\t"
      "v_cvt_pk_bf16_f32 v48, v32, v33\n\t"
      "v_cvt_pk_bf16_f32 v49, v34, v35\n\t"
      "v_mfma_f32_16x16x32_bf16 v[36:39], a[152:155], a[44:47], v[36:39]\n\t"
      "ds_write_b64 v0, v[48:49] offset:12672\n\t"
      "s_cbranch_scc1 .Lloop0_%=\n\t"
      "s_branch .Lendc_%=\n\t"
      ".Lshb_%=:\n\t"
      "s_waitcnt vmcnt(10)\n\t"
      "v_bfe_u32 v63, v63, 17, 8\n\t"
      "v_min_u32 v62, 1, v63\n\t"
      "v_lshl_add_u32 v63, v62, 16, v63\n\t"
      "s_nop 1\n\t"
      "v_add_u32_dpp v63, v63, v63 row_shr:1 row_mask:0xf bank_mask:0xf bound_ctrl:0\n\t"
      "s_nop 1\n\t"
      "v_add_u32_dpp v63, v63, v63 row_shr:2 row_mask:0xf bank_mask:0xf bound_ctrl:0\n\t"
      "s_nop 1\n\t"
      "v_add_u32_dpp v63, v63, v63 row_shr:4 row_mask:0xf bank_mask:0xf bound_ctrl:0\n\t"
      "s_nop 1\n\t"
      "v_add_u32_dpp v63, v63, v63 row_shr:8 row_mask:0xf bank_mask:0xf bound_ctrl:0\n\t"
      "s_nop 0\n\t"
      "v_readlane_b32 s64, v63, 15\n\t"
      "v_readlane_b32 s65, v63, 31\n\t"
      "s_nop 1\n\t"
      "s_add_u32 s64, s64, s65\n\t"
      "s_lshr_b32 s65, s64, 16\n\t"
      "s_and_b32 s64, s64, 0xffff\n\t"
      "s_mov_b32 s66, 0\n\t"
      "s_mul_i32 s67, s65, 989\n\t"
      "s_lshl_b32 s69, s64, 3\n\t"
      "s_cmp_lt_u32 s69, s67\n\t"
      "s_cselect_b32 s66, 1, s66\n\t"
      "s_mul_i32 s67, s65, 957\n\t"
      "s_lshl_b32 s69, s64, 3\n\t"
      "s_cmp_lt_u32 s69, s67\n\t"
      "s_cselect_b32 s66, 3, s66\n\t"
      "s_add_u32 s29, s29, s66\n\t"
      "s_mul_i32 s67, s65, 737\n\t"
      "s_cmp_lt_u32 s69, s67\n\t"
      "s_cbranch_scc0 .Lshbr_%=\n\t"
      "s_movk_i32 s29, 79\n\t"
      "s_mov_b32 s32, 0x11111111\n\t"
      "s_mov_b32 s33, 0x11111111\n\t"
      "s_mov_b32 s34, 0x11111111\n\t"
      "s_mov_b32 s35, 0x11111111\n\t"
      "s_sub_i32 s28, s28, 30\n\t"
      "s_min_i32 s66, s28, 0\n\t"
      "s_sub_u32 s38, s38, s66\n\t"
      "s_max_i32 s28, s28, 0\n\t"
      "s_branch .Lshbr_%=\n\t"
      ".Lloop1_%=:\n\t"
      "v_mfma_f32_16x16x32_bf16 v[16:19], v[64:67], a[0:3], 0\n\t"
      "ds_read_b128 a[24:27], v58 offset:256\n\t"
      "v_mfma_f32_16x16x32_bf16 v[16:19], v[80:83], a[4:7], v[16:19]\n\t"
      "ds_read_b128 a[28:31], v59 offset:256\n\t"
      "v_mfma_f32_16x16x32_bf16 v[16:19], v[96:99], a[8:11], v[16:19]\n\t"
      "ds_read_b128 a[32:35], v56 offset:512\n\t"
      "v_mfma_f32_16x16x32_bf16 v[16:19], v[112:115], a[12:15], v[16:19]\n\t"
      "ds_read_b128 a[36:39], v57 offset:512\n\t"
      "v_mfma_f32_16x16x32_bf16 v[16:19], v[128:131], a[16:19], v[16:19]\n\t"
      "ds_read_b128 a[40:43], v58 offset:512\n\t"
      "v_mfma_f32_16x16x32_bf16 v[16:19], v[144:147], a[20:23], v[16:19]\n\t"
      "ds_read_b128 a[44:47], v59 offset:512\n\t"
      "v_mfma_f32_16x16x32_bf16 v[20:23], v[68:71], a[0:3], 0\n\t"
      "v_cvt_pk_bf16_f32 v50, v36, v37\n\t"
      "v_cvt_pk_bf16_f32 v51, v38, v39\n\t"
      "v_mfma_f32_16x16x32_bf16 v[20:23], v[84:87], a[4:7], v[20:23]\n\t"
      "ds_write_b64 v0, v[50:51] offset:12704\n\t"
      "v_mfma_f32_16x16x32_bf16 v[20:23], v[100:103], a[8:11], v[20:23]\n\t"
      "v_add_u32 v52, s51, v15\n\t"
      "v_xor_b32 v53, 64, v52\n\t"
      "v_mfma_f32_16x16x32_bf16 v[20:23], v[116:119], a[12:15], v[20:23]\n\t"
      "v_xor_b32 v54, 128, v52\n\t"
      "v_xor_b32 v55, 192, v52\n\t"
      "v_mfma_f32_16x16x32_bf16 v[20:23], v[132:135], a[16:19], v[20:23]\n\t"
      "s_add_u32 s56, s56, 786432\n\t"
      "s_lshr_b32 s57, s57, 1\n\t"
      "v_mfma_f32_16x16x32_bf16 v[20:23], v[148:151], a[20:23], v[20:23]\n\t"
      "s_bitcmp1_b32 s57, 0\n\t"
      "s_cselect_b32 s48, 24576, 0\n\t"
      "s_add_u32 s48, s48, s56\n\t"
      "v_mfma_f32_16x16x32_bf16 v[24:27], v[72:75], a[0:3], 0\n\t"
      "v_mfma_f32_16x16x32_bf16 v[24:27], v[88:91], a[4:7], v[24:27]\n\t"
      "v_mfma_f32_16x16x32_bf16 v[24:27], v[104:107], a[8:11], v[24:27]\n\t"
      "v_mfma_f32_16x16x32_bf16 v[24:27], v[120:123], a[12:15], v[24:27]\n\t"
      "s_waitcnt vmcnt(12)\n\t"
      "s_waitcnt lgkmcnt(0)\n\t"
      "s_barrier\n\t"
      "v_mfma_f32_16x16x32_bf16 v[24:27], v[136:139], a[16:19], v[24:27]\n\t"
      "v_mfma_f32_16x16x32_bf16 v[24:27], v[152:155], a[20:23], v[24:27]\n\t"
      "v_mfma_f32_16x16x32_bf16 v[28:31], v[76:79], a[0:3], 0\n\t"
      "ds_read_b128 a[48:51], v8 offset:12544\n\t"
      "v_mfma_f32_16x16x32_bf16 v[28:31], v[92:95], a[4:7], v[28:31]\n\t"
      "ds_read_b128 a[52:55], v60 offset:13568\n\t"
      "v_mfma_f32_16x16x32_bf16 v[28:31], v[108:111], a[8:11], v[28:31]\n\t"
      "ds_read_b128 a[56:59], v61 offset:14592\n\t"
      "v_mfma_f32_16x16x32_bf16 v[28:31], v[124:127], a[12:15], v[28:31]\n\t"
      "v_mfma_f32_16x16x32_bf16 v[28:31], v[140:143], a[16:19], v[28:31]\n\t"
      "v_mfma_f32_16x16x32_bf16 v[28:31], v[156:159], a[20:23], v[28:31]\n\t"
      "v_mfma_f32_16x16x32_bf16 v[32:35], a[60:63], a[0:3], 0\n\t"
      "v_mfma_f32_16x16x32_bf16 v[32:35], a[68:71], a[4:7], v[32:35]\n\t"
      "s_waitcnt lgkmcnt(2)\n\t"
      "v_mfma_f32_16x16x32_bf16 v[32:35], a[76:79], a[8:11], v[32:35]\n\t"
      "buffer_store_dwordx4 a[48:51], v12, s[44:47], s49 offen sc1\n\t"
      "v_mfma_f32_16x16x32_bf16 v[32:35], a[84:87], a[12:15], v[32:35]\n\t"
      "v_mfma_f32_16x16x32_bf16 v[32:35], a[92:95], a[16:19], v[32:35]\n\t"
      "v_mfma_f32_16x16x32_bf16 v[32:35], a[100:103], a[20:23], v[32:35]\n\t"
      "s_add_u32 m0, s18, s50\n\t"
      "v_mfma_f32_16x16x32_bf16 v[36:39], a[64:67], a[0:3], 0\n\t"
      "buffer_load_dwordx4 v2, s[40:43], s48 offen nt lds\n\t"
      "v_mfma_f32_16x16x32_bf16 v[36:39], a[72:75], a[4:7], v[36:39]\n\t"
      "v_mfma_f32_16x16x32_bf16 v[36:39], a[80:83], a[8:11], v[36:39]\n\t"
      "v_mfma_f32_16x16x32_bf16 v[36:39], a[88:91], a[12:15], v[36:39]\n\t"
      "s_waitcnt lgkmcnt(1)\n\t"
      "v_mfma_f32_16x16x32_bf16 v[36:39], a[96:99], a[16:19], v[36:39]\n\t"
      "buffer_store_dwordx4 a[52:55], v13, s[44:47], s49 offen offset:1024 sc1\n\t"
      "v_mfma_f32_16x16x32_bf16 v[36:39], a[104:107], a[20:23], v[36:39]\n\t"
      "v_mfma_f32_16x16x32_bf16 v[16:19], v[160:163], a[24:27], v[16:19]\n\t"
      "ds_read_b128 a[0:3], v52 offset:0\n\t"
      "v_mfma_f32_16x16x32_bf16 v[16:19], v[176:179], a[28:31], v[16:19]\n\t"
      "ds_read_b128 a[4:7], v53 offset:0\n\t"
      "v_mfma_f32_16x16x32_bf16 v[16:19], v[192:195], a[32:35], v[16:19]\n\t"
      "ds_read_b128 a[8:11], v54 offset:0\n\t"
      "v_mfma_f32_16x16x32_bf16 v[16:19], v[208:211], a[36:39], v[16:19]\n\t"
      "ds_read_b128 a[12:15], v55 offset:0\n\t"
      "v_mfma_f32_16x16x32_bf16 v[16:19], v[224:227], a[40:43], v[16:19]\n\t"
      "ds_read_b128 a[16:19], v52 offset:256\n\t"
      "v_mfma_f32_16x16x32_bf16 v[16:19], v[240:243], a[44:47], v[16:19]\n\t"
      "ds_read_b128 a[20:23], v53 offset:256\n\t"
      "v_mfma_f32_16x16x32_bf16 v[20:23], v[164:167], a[24:27], v[20:23]\n\t"
      "s_add_u32 s51, s51, 12288\n\t"
      "s_cmp_eq_u32 s51, 160256\n\t"
      "s_cselect_b32 s51, 25088, s51\n\t"
      "v_mfma_f32_16x16x32_bf16 v[20:23], v[180:183], a[28:31], v[20:23]\n\t"
      "s_waitcnt lgkmcnt(6)\n\t"
      "v_mfma_f32_16x16x32_bf16 v[20:23], v[196:199], a[32:35], v[20:23]\n\t"
      "buffer_store_dwordx4 a[56:59], v14, s[44:47], s49 offen offset:2048 sc1\n\t"
      "v_mfma_f32_16x16x32_bf16 v[20:23], v[212:215], a[36:39], v[20:23]\n\t"
      "v_mfma_f32_16x16x32_bf16 v[20:23], v[228:231], a[40:43], v[20:23]\n\t"
      "s_waitcnt lgkmcnt(0)\n\t"
      "v_mfma_f32_16x16x32_bf16 v[20:23], v[244:247], a[44:47], v[20:23]\n\t"
      "v_mfma_f32_16x16x32_bf16 v[24:27], v[168:171], a[24:27], v[24:27]\n\t"
      "v_mfma_f32_16x16x32_bf16 v[24:27], v[184:187], a[28:31], v[24:27]\n\t"
      "v_cvt_pk_bf16_f32 v40, v16, v17\n\t"
      "v_cvt_pk_bf16_f32 v41, v18, v19\n\t"
      "v_mfma_f32_16x16x32_bf16 v[24:27], v[200:203], a[32:35], v[24:27]\n\t"
      "ds_write_b64 v0, v[40:41] offset:0\n\t"
      "v_mfma_f32_16x16x32_bf16 v[24:27], v[216:219], a[36:39], v[24:27]\n\t"
      "buffer_load_dwordx4 v3, s[40:43], s48 offen offset:1024 nt lds\n\t"
      "v_mfma_f32_16x16x32_bf16 v[24:27], v[232:235], a[40:43], v[24:27]\n\t"
      "s_add_u32 s58, s58, 786432\n\t"
      "s_lshr_b32 s59, s59, 1\n\t"
      "v_mfma_f32_16x16x32_bf16 v[24:27], v[248:251], a[44:47], v[24:27]\n\t"
      "s_bitcmp1_b32 s59, 0\n\t"
      "s_cselect_b32 s49, 24576, 0\n\t"
      "s_add_u32 s49, s49, s58\n\t"
      "v_mfma_f32_16x16x32_bf16 v[28:31], v[172:175], a[24:27], v[28:31]\n\t"
      "v_mfma_f32_16x16x32_bf16 v[28:31], v[188:191], a[28:31], v[28:31]\n\t"
      "v_cvt_pk_bf16_f32 v42, v20, v21\n\t"
      "v_cvt_pk_bf16_f32 v43, v22, v23\n\t"
      "v_mfma_f32_16x16x32_bf16 v[28:31], v[204:207], a[32:35], v[28:31]\n\t"
      "ds_write_b64 v0, v[42:43] offset:32\n\t"
      "v_mfma_f32_16x16x32_bf16 v[28:31], v[220:223], a[36:39], v[28:31]\n\t"
      "v_mfma_f32_16x16x32_bf16 v[28:31], v[236:239], a[40:43], v[28:31]\n\t"
      "v_mfma_f32_16x16x32_bf16 v[28:31], v[252:255], a[44:47], v[28:31]\n\t"
      "v_mfma_f32_16x16x32_bf16 v[32:35], a[108:111], a[24:27], v[32:35]\n\t"
      "v_mfma_f32_16x16x32_bf16 v[32:35], a[116:119], a[28:31], v[32:35]\n\t"
      "v_cvt_pk_bf16_f32 v44, v24, v25\n\t"
      "v_cvt_pk_bf16_f32 v45, v26, v27\n\t"
      "v_mfma_f32_16x16x32_bf16 v[32:35], a[124:127], a[32:35], v[32:35]\n\t"
      "ds_write_b64 v0, v[44:45] offset:64\n\t"
      "v_mfma_f32_16x16x32_bf16 v[32:35], a[132:135], a[36:39], v[32:35]\n\t"
      "buffer_load_dwordx4 v4, s[40:43], s48 offen offset:2048 nt lds\n\t"
      "v_mfma_f32_16x16x32_bf16 v[32:35], a[140:143], a[40:43], v[32:35]\n\t"
      "s_add_u32 s50, s50, 12288\n\t"
      "v_mfma_f32_16x16x32_bf16 v[32:35], a[148:151], a[44:47], v[32:35]\n\t"
      "v_cvt_pk_bf16_f32 v46, v28, v29\n\t"
      "v_cvt_pk_bf16_f32 v47, v30, v31\n\t"
      "v_mfma_f32_16x16x32_bf16 v[36:39], a[112:115], a[24:27], v[36:39]\n\t"
      "ds_write_b64 v0, v[46:47] offset:96\n\t"
      "v_mfma_f32_16x16x32_bf16 v[36:39], a[120:123], a[28:31], v[36:39]\n\t"
      "s_cmp_eq_u32 s50, 160256\n\t"
      "s_cselect_b32 s50, 25088, s50\n\t"
      "v_mfma_f32_16x16x32_bf16 v[36:39], a[128:131], a[32:35], v[36:39]\n\t"
      "v_mfma_f32_16x16x32_bf16 v[36:39], a[136:139], a[36:39], v[36:39]\n\t"
      "v_mfma_f32_16x16x32_bf16 v[36:39], a[144:147], a[40:43], v[36:39]\n\t"
      "v_cvt_pk_bf16_f32 v48, v32, v33\n\t"
      "v_cvt_pk_bf16_f32 v49, v34, v35\n\t"
      "v_mfma_f32_16x16x32_bf16 v[36:39], a[152:155], a[44:47], v[36:39]\n\t"
      "ds_write_b64 v0, v[48:49] offset:128\n\t"
      ".Lmid1_%=:\n\t"
      "v_mfma_f32_16x16x32_bf16 v[16:19], v[64:67], a[0:3], 0\n\t"
      "ds_read_b128 a[24:27], v54 offset:256\n\t"
      "v_mfma_f32_16x16x32_bf16 v[16:19], v[80:83], a[4:7], v[16:19]\n\t"
      "ds_read_b128 a[28:31], v55 offset:256\n\t"
      "v_mfma_f32_16x16x32_bf16 v[16:19], v[96:99], a[8:11], v[16:19]\n\t"
      "ds_read_b128 a[32:35], v52 offset:512\n\t"
      "v_mfma_f32_16x16x32_bf16 v[16:19], v[112:115], a[12:15], v[16:19]\n\t"
      "ds_read_b128 a[36:39], v53 offset:512\n\t"
      "v_mfma_f32_16x16x32_bf16 v[16:19], v[128:131], a[16:19], v[16:19]\n\t"
      "ds_read_b128 a[40:43], v54 offset:512\n\t"
      "v_mfma_f32_16x16x32_bf16 v[16:19], v[144:147], a[20:23], v[16:19]\n\t"
      "ds_read_b128 a[44:47], v55 offset:512\n\t"
      "v_mfma_f32_16x16x32_bf16 v[20:23], v[68:71], a[0:3], 0\n\t"
      "v_cvt_pk_bf16_f32 v50, v36, v37\n\t"
      "v_cvt_pk_bf16_f32 v51, v38, v39\n\t"
      "v_mfma_f32_16x16x32_bf16 v[20:23], v[84:87], a[4:7], v[20:23]\n\t"
      "ds_write_b64 v0, v[50:51] offset:160\n\t"
      "v_mfma_f32_16x16x32_bf16 v[20:23], v[100:103], a[8:11], v[20:23]\n\t"
      "v_add_u32 v56, s51, v15\n\t"
      "v_xor_b32 v57, 64, v56\n\t"
      "v_mfma_f32_16x16x32_bf16 v[20:23], v[116:119], a[12:15], v[20:23]\n\t"
      "v_xor_b32 v58, 128, v56\n\t"
      "v_xor_b32 v59, 192, v56\n\t"
      "v_mfma_f32_16x16x32_bf16 v[20:23], v[132:135], a[16:19], v[20:23]\n\t"
      "v_mfma_f32_16x16x32_bf16 v[20:23], v[148:151], a[20:23], v[20:23]\n\t"
      "v_mfma_f32_16x16x32_bf16 v[24:27], v[72:75], a[0:3], 0\n\t"
      "v_mfma_f32_16x16x32_bf16 v[24:27], v[88:91], a[4:7], v[24:27]\n\t"
      "v_mfma_f32_16x16x32_bf16 v[24:27], v[104:107], a[8:11], v[24:27]\n\t"
      "v_mfma_f32_16x16x32_bf16 v[24:27], v[120:123], a[12:15], v[24:27]\n\t"
      "s_waitcnt vmcnt(12)\n\t"
      "s_waitcnt lgkmcnt(0)\n\t"
      "s_barrier\n\t"
      "v_mfma_f32_16x16x32_bf16 v[24:27], v[136:139], a[16:19], v[24:27]\n\t"
      "v_mfma_f32_16x16x32_bf16 v[24:27], v[152:155], a[20:23], v[24:27]\n\t"
      "v_mfma_f32_16x16x32_bf16 v[28:31], v[76:79], a[0:3], 0\n\t"
      "ds_read_b128 a[48:51], v8 offset:0\n\t"
      "v_mfma_f32_16x16x32_bf16 v[28:31], v[92:95], a[4:7], v[28:31]\n\t"
      "ds_read_b128 a[52:55], v60 offset:1024\n\t"
      "v_mfma_f32_16x16x32_bf16 v[28:31], v[108:111], a[8:11], v[28:31]\n\t"
      "ds_read_b128 a[56:59], v61 offset:2048\n\t"
      "v_mfma_f32_16x16x32_bf16 v[28:31], v[124:127], a[12:15], v[28:31]\n\t"
      "v_mfma_f32_16x16x32_bf16 v[28:31], v[140:143], a[16:19], v[28:31]\n\t"
      "v_mfma_f32_16x16x32_bf16 v[28:31], v[156:159], a[20:23], v[28:31]\n\t"
      "v_mfma_f32_16x16x32_bf16 v[32:35], a[60:63], a[0:3], 0\n\t"
      "v_mfma_f32_16x16x32_bf16 v[32:35], a[68:71], a[4:7], v[32:35]\n\t"
      "s_waitcnt lgkmcnt(2)\n\t"
      "v_mfma_f32_16x16x32_bf16 v[32:35], a[76:79], a[8:11], v[32:35]\n\t"
      "buffer_store_dwordx4 a[48:51], v9, s[44:47], s49 offen sc1\n\t"
      "v_mfma_f32_16x16x32_bf16 v[32:35], a[84:87], a[12:15], v[32:35]\n\t"
      "v_mfma_f32_16x16x32_bf16 v[32:35], a[92:95], a[16:19], v[32:35]\n\t"
      "v_mfma_f32_16x16x32_bf16 v[32:35], a[100:103], a[20:23], v[32:35]\n\t"
      "s_add_u32 m0, s18, s50\n\t"
      "v_mfma_f32_16x16x32_bf16 v[36:39], a[64:67], a[0:3], 0\n\t"
      "buffer_load_dwordx4 v5, s[40:43], s48 offen nt lds\n\t"
      "v_mfma_f32_16x16x32_bf16 v[36:39], a[72:75], a[4:7], v[36:39]\n\t"
      "v_mfma_f32_16x16x32_bf16 v[36:39], a[80:83], a[8:11], v[36:39]\n\t"
      "v_mfma_f32_16x16x32_bf16 v[36:39], a[88:91], a[12:15], v[36:39]\n\t"
      "s_waitcnt lgkmcnt(1)\n\t"
      "v_mfma_f32_16x16x32_bf16 v[36:39], a[96:99], a[16:19], v[36:39]\n\t"
      "buffer_store_dwordx4 a[52:55], v10, s[44:47], s49 offen offset:1024 sc1\n\t"
      "v_mfma_f32_16x16x32_bf16 v[36:39], a[104:107], a[20:23], v[36:39]\n\t"
      "v_mfma_f32_16x16x32_bf16 v[16:19], v[160:163], a[24:27], v[16:19]\n\t"
      "ds_read_b128 a[0:3], v56 offset:0\n\t"
      "v_mfma_f32_16x16x32_bf16 v[16:19], v[176:179], a[28:31], v[16:19]\n\t"
      "ds_read_b128 a[4:7], v57 offset:0\n\t"
      "v_mfma_f32_16x16x32_bf16 v[16:19], v[192:195], a[32:35], v[16:19]\n\t"
      "ds_read_b128 a[8:11], v58 offset:0\n\t"
      "v_mfma_f32_16x16x32_bf16 v[16:19], v[208:211], a[36:39], v[16:19]\n\t"
      "ds_read_b128 a[12:15], v59 offset:0\n\t"
      "v_mfma_f32_16x16x32_bf16 v[16:19], v[224:227], a[40:43], v[16:19]\n\t"
      "ds_read_b128 a[16:19], v56 offset:256\n\t"
      "v_mfma_f32_16x16x32_bf16 v[16:19], v[240:243], a[44:47], v[16:19]\n\t"
      "ds_read_b128 a[20:23], v57 offset:256\n\t"
      "v_mfma_f32_16x16x32_bf16 v[20:23], v[164:167], a[24:27], v[20:23]\n\t"
      "s_add_u32 s51, s51, 12288\n\t"
      "s_cmp_eq_u32 s51, 160256\n\t"
      "s_cselect_b32 s51, 25088, s51\n\t"
      "v_mfma_f32_16x16x32_bf16 v[20:23], v[180:183], a[28:31], v[20:23]\n\t"
      "s_waitcnt lgkmcnt(6)\n\t"
      "v_mfma_f32_16x16x32_bf16 v[20:23], v[196:199], a[32:35], v[20:23]\n\t"
      "buffer_store_dwordx4 a[56:59], v11, s[44:47], s49 offen offset:2048 sc1\n\t"
      "v_mfma_f32_16x16x32_bf16 v[20:23], v[212:215], a[36:39], v[20:23]\n\t"
      "v_mfma_f32_16x16x32_bf16 v[20:23], v[228:231], a[40:43], v[20:23]\n\t"
      "s_waitcnt lgkmcnt(0)\n\t"
      "v_mfma_f32_16x16x32_bf16 v[20:23], v[244:247], a[44:47], v[20:23]\n\t"
      "v_mfma_f32_16x16x32_bf16 v[24:27], v[168:171], a[24:27], v[24:27]\n\t"
      "v_mfma_f32_16x16x32_bf16 v[24:27], v[184:187], a[28:31], v[24:27]\n\t"
      "v_cvt_pk_bf16_f32 v40, v16, v17\n\t"
      "v_cvt_pk_bf16_f32 v41, v18, v19\n\t"
      "v_mfma_f32_16x16x32_bf16 v[24:27], v[200:203], a[32:35], v[24:27]\n\t"
      "ds_write_b64 v0, v[40:41] offset:12544\n\t"
      "v_mfma_f32_16x16x32_bf16 v[24:27], v[216:219], a[36:39], v[24:27]\n\t"
      "buffer_load_dwordx4 v6, s[40:43], s48 offen offset:1024 nt lds\n\t"
      "v_mfma_f32_16x16x32_bf16 v[24:27], v[232:235], a[40:43], v[24:27]\n\t"
      "v_mfma_f32_16x16x32_bf16 v[24:27], v[248:251], a[44:47], v[24:27]\n\t"
      "v_mfma_f32_16x16x32_bf16 v[28:31], v[172:175], a[24:27], v[28:31]\n\t"
      "v_mfma_f32_16x16x32_bf16 v[28:31], v[188:191], a[28:31], v[28:31]\n\t"
      "v_cvt_pk_bf16_f32 v42, v20, v21\n\t"
      "v_cvt_pk_bf16_f32 v43, v22, v23\n\t"
      "v_mfma_f32_16x16x32_bf16 v[28:31], v[204:207], a[32:35], v[28:31]\n\t"
      "ds_write_b64 v0, v[42:43] offset:12576\n\t"
      "v_mfma_f32_16x16x32_bf16 v[28:31], v[220:223], a[36:39], v[28:31]\n\t"
      "v_mfma_f32_16x16x32_bf16 v[28:31], v[236:239], a[40:43], v[28:31]\n\t"
      "v_mfma_f32_16x16x32_bf16 v[28:31], v[252:255], a[44:47], v[28:31]\n\t"
      "v_mfma_f32_16x16x32_bf16 v[32:35], a[108:111], a[24:27], v[32:35]\n\t"
      "v_mfma_f32_16x16x32_bf16 v[32:35], a[116:119], a[28:31], v[32:35]\n\t"
      "v_cvt_pk_bf16_f32 v44, v24, v25\n\t"
      "v_cvt_pk_bf16_f32 v45, v26, v27\n\t"
      "v_mfma_f32_16x16x32_bf16 v[32:35], a[124:127], a[32:35], v[32:35]\n\t"
      "ds_write_b64 v0, v[44:45] offset:12608\n\t"
      "v_mfma_f32_16x16x32_bf16 v[32:35], a[132:135], a[36:39], v[32:35]\n\t"
      "buffer_load_dwordx4 v7, s[40:43], s48 offen offset:2048 nt lds\n\t"
      "v_mfma_f32_16x16x32_bf16 v[32:35], a[140:143], a[40:43], v[32:35]\n\t"
      "s_add_u32 s50, s50, 12288\n\t"
      "v_mfma_f32_16x16x32_bf16 v[32:35], a[148:151], a[44:47], v[32:35]\n\t"
      "v_cvt_pk_bf16_f32 v46, v28, v29\n\t"
      "v_cvt_pk_bf16_f32 v47, v30, v31\n\t"
      "v_mfma_f32_16x16x32_bf16 v[36:39], a[112:115], a[24:27], v[36:39]\n\t"
      "ds_write_b64 v0, v[46:47] offset:12640\n\t"
      "v_mfma_f32_16x16x32_bf16 v[36:39], a[120:123], a[28:31], v[36:39]\n\t"
      "s_cmp_eq_u32 s50, 160256\n\t"
      "s_cselect_b32 s50, 25088, s50\n\t"
      "v_mfma_f32_16x16x32_bf16 v[36:39], a[128:131], a[32:35], v[36:39]\n\t"
      "s_sub_u32 s52, s52, 1\n\t"
      "s_cmp_lg_u32 s52, 0\n\t"
      "v_mfma_f32_16x16x32_bf16 v[36:39], a[136:139], a[36:39], v[36:39]\n\t"
      "v_mfma_f32_16x16x32_bf16 v[36:39], a[144:147], a[40:43], v[36:39]\n\t"
      "v_cvt_pk_bf16_f32 v48, v32, v33\n\t"
      "v_cvt_pk_bf16_f32 v49, v34, v35\n\t"
      "v_mfma_f32_16x16x32_bf16 v[36:39], a[152:155], a[44:47], v[36:39]\n\t"
      "ds_write_b64 v0, v[48:49] offset:12672\n\t"
      "s_cbranch_scc1 .Lloop1_%=\n\t"
      "s_branch .Lendc_%=\n\t"
      ".Lloop2_%=:\n\t"
      "v_mfma_f32_16x16x32_bf16 v[16:19], v[64:67], a[0:3], 0\n\t"
      "ds_read_b128 a[24:27], v58 offset:256\n\t"
      "v_mfma_f32_16x16x32_bf16 v[16:19], v[80:83], a[4:7], v[16:19]\n\t"
      "ds_read_b128 a[28:31], v59 offset:256\n\t"
      "v_mfma_f32_16x16x32_bf16 v[16:19], v[96:99], a[8:11], v[16:19]\n\t"
      "ds_read_b128 a[32:35], v56 offset:512\n\t"
      "v_mfma_f32_16x16x32_bf16 v[16:19], v[112:115], a[12:15], v[16:19]\n\t"
      "ds_read_b128 a[36:39], v57 offset:512\n\t"
      "v_mfma_f32_16x16x32_bf16 v[16:19], v[128:131], a[16:19], v[16:19]\n\t"
      "ds_read_b128 a[40:43], v58 offset:512\n\t"
      "v_mfma_f32_16x16x32_bf16 v[16:19], v[144:147], a[20:23], v[16:19]\n\t"
      "ds_read_b128 a[44:47], v59 offset:512\n\t"
      "v_mfma_f32_16x16x32_bf16 v[20:23], v[68:71], a[0:3], 0\n\t"
      "v_cvt_pk_bf16_f32 v50, v36, v37\n\t"
      "v_cvt_pk_bf16_f32 v51, v38, v39\n\t"
      "v_mfma_f32_16x16x32_bf16 v[20:23], v[84:87], a[4:7], v[20:23]\n\t"
      "ds_write_b64 v0, v[50:51] offset:12704\n\t"
      "v_mfma_f32_16x16x32_bf16 v[20:23], v[100:103], a[8:11], v[20:23]\n\t"
      "v_add_u32 v52, s51, v15\n\t"
      "v_xor_b32 v53, 64, v52\n\t"
      "v_mfma_f32_16x16x32_bf16 v[20:23], v[116:119], a[12:15], v[20:23]\n\t"
      "v_xor_b32 v54, 128, v52\n\t"
      "v_xor_b32 v55, 192, v52\n\t"
      "v_mfma_f32_16x16x32_bf16 v[20:23], v[132:135], a[16:19], v[20:23]\n\t"
      "s_add_u32 s56, s56, 786432\n\t"
      "s_lshr_b32 s57, s57, 1\n\t"
      "v_mfma_f32_16x16x32_bf16 v[20:23], v[148:151], a[20:23], v[20:23]\n\t"
      "s_bitcmp1_b32 s57, 0\n\t"
      "s_cselect_b32 s48, 24576, 0\n\t"
      "s_add_u32 s48, s48, s56\n\t"
      "v_mfma_f32_16x16x32_bf16 v[24:27], v[72:75], a[0:3], 0\n\t"
      "v_mfma_f32_16x16x32_bf16 v[24:27], v[88:91], a[4:7], v[24:27]\n\t"
      "v_mfma_f32_16x16x32_bf16 v[24:27], v[104:107], a[8:11], v[24:27]\n\t"
      "v_mfma_f32_16x16x32_bf16 v[24:27], v[120:123], a[12:15], v[24:27]\n\t"
      "v_mfma_f32_16x16x32_bf16 v[24:27], v[136:139], a[16:19], v[24:27]\n\t"
      "s_waitcnt vmcnt(12)\n\t"
      "s_waitcnt lgkmcnt(0)\n\t"
      "s_barrier\n\t"
      "v_mfma_f32_16x16x32_bf16 v[24:27], v[152:155], a[20:23], v[24:27]\n\t"
      "v_mfma_f32_16x16x32_bf16 v[28:31], v[76:79], a[0:3], 0\n\t"
      "ds_read_b128 a[48:51], v8 offset:12544\n\t"
      "v_mfma_f32_16x16x32_bf16 v[28:31], v[92:95], a[4:7], v[28:31]\n\t"
      "ds_read_b128 a[52:55], v60 offset:13568\n\t"
      "v_mfma_f32_16x16x32_bf16 v[28:31], v[108:111], a[8:11], v[28:31]\n\t"
      "ds_read_b128 a[56:59], v61 offset:14592\n\t"
      "v_mfma_f32_16x16x32_bf16 v[28:31], v[124:127], a[12:15], v[28:31]\n\t"
      "v_mfma_f32_16x16x32_bf16 v[28:31], v[140:143], a[16:19], v[28:31]\n\t"
      "v_mfma_f32_16x16x32_bf16 v[28:31], v[156:159], a[20:23], v[28:31]\n\t"
      "v_mfma_f32_16x16x32_bf16 v[32:35], a[60:63], a[0:3], 0\n\t"
      "v_mfma_f32_16x16x32_bf16 v[32:35], a[68:71], a[4:7], v[32:35]\n\t"
      "s_waitcnt lgkmcnt(2)\n\t"
      "v_mfma_f32_16x16x32_bf16 v[32:35], a[76:79], a[8:11], v[32:35]\n\t"
      "buffer_store_dwordx4 a[48:51], v12, s[44:47], s49 offen sc1\n\t"
      "v_mfma_f32_16x16x32_bf16 v[32:35], a[84:87], a[12:15], v[32:35]\n\t"
      "v_mfma_f32_16x16x32_bf16 v[32:35], a[92:95], a[16:19], v[32:35]\n\t"
      "v_mfma_f32_16x16x32_bf16 v[32:35], a[100:103], a[20:23], v[32:35]\n\t"
      "s_add_u32 m0, s18, s50\n\t"
      "v_mfma_f32_16x16x32_bf16 v[36:39], a[64:67], a[0:3], 0\n\t"
      "buffer_load_dwordx4 v2, s[40:43], s48 offen nt lds\n\t"
      "v_mfma_f32_16x16x32_bf16 v[36:39], a[72:75], a[4:7], v[36:39]\n\t"
      "v_mfma_f32_16x16x32_bf16 v[36:39], a[80:83], a[8:11], v[36:39]\n\t"
      "v_mfma_f32_16x16x32_bf16 v[36:39], a[88:91], a[12:15], v[36:39]\n\t"
      "s_waitcnt lgkmcnt(1)\n\t"
      "v_mfma_f32_16x16x32_bf16 v[36:39], a[96:99], a[16:19], v[36:39]\n\t"
      "buffer_store_dwordx4 a[52:55], v13, s[44:47], s49 offen offset:1024 sc1\n\t"
      "v_mfma_f32_16x16x32_bf16 v[36:39], a[104:107], a[20:23], v[36:39]\n\t"
      "v_mfma_f32_16x16x32_bf16 v[16:19], v[160:163], a[24:27], v[16:19]\n\t"
      "ds_read_b128 a[0:3], v52 offset:0\n\t"
      "v_mfma_f32_16x16x32_bf16 v[16:19], v[176:179], a[28:31], v[16:19]\n\t"
      "ds_read_b128 a[4:7], v53 offset:0\n\t"
      "v_mfma_f32_16x16x32_bf16 v[16:19], v[192:195], a[32:35], v[16:19]\n\t"
      "ds_read_b128 a[8:11], v54 offset:0\n\t"
      "v_mfma_f32_16x16x32_bf16 v[16:19], v[208:211], a[36:39], v[16:19]\n\t"
      "ds_read_b128 a[12:15], v55 offset:0\n\t"
      "v_mfma_f32_16x16x32_bf16 v[16:19], v[224:227], a[40:43], v[16:19]\n\t"
      "ds_read_b128 a[16:19], v52 offset:256\n\t"
      "v_mfma_f32_16x16x32_bf16 v[16:19], v[240:243], a[44:47], v[16:19]\n\t"
      "ds_read_b128 a[20:23], v53 offset:256\n\t"
      "v_mfma_f32_16x16x32_bf16 v[20:23], v[164:167], a[24:27], v[20:23]\n\t"
      "s_add_u32 s51, s51, 12288\n\t"
      "s_cmp_eq_u32 s51, 160256\n\t"
      "s_cselect_b32 s51, 25088, s51\n\t"
      "v_mfma_f32_16x16x32_bf16 v[20:23], v[180:183], a[28:31], v[20:23]\n\t"
      "s_waitcnt lgkmcnt(6)\n\t"
      "v_mfma_f32_16x16x32_bf16 v[20:23], v[196:199], a[32:35], v[20:23]\n\t"
      "buffer_store_dwordx4 a[56:59], v14, s[44:47], s49 offen offset:2048 sc1\n\t"
      "v_mfma_f32_16x16x32_bf16 v[20:23], v[212:215], a[36:39], v[20:23]\n\t"
      "v_mfma_f32_16x16x32_bf16 v[20:23], v[228:231], a[40:43], v[20:23]\n\t"
      "s_waitcnt lgkmcnt(0)\n\t"
      "v_mfma_f32_16x16x32_bf16 v[20:23], v[244:247], a[44:47], v[20:23]\n\t"
      "v_mfma_f32_16x16x32_bf16 v[24:27], v[168:171], a[24:27], v[24:27]\n\t"
      "v_mfma_f32_16x16x32_bf16 v[24:27], v[184:187], a[28:31], v[24:27]\n\t"
      "v_cvt_pk_bf16_f32 v40, v16, v17\n\t"
      "v_cvt_pk_bf16_f32 v41, v18, v19\n\t"
      "v_mfma_f32_16x16x32_bf16 v[24:27], v[200:203], a[32:35], v[24:27]\n\t"
      "ds_write_b64 v0, v[40:41] offset:0\n\t"
      "v_mfma_f32_16x16x32_bf16 v[24:27], v[216:219], a[36:39], v[24:27]\n\t"
      "buffer_load_dwordx4 v3, s[40:43], s48 offen offset:1024 nt lds\n\t"
      "v_mfma_f32_16x16x32_bf16 v[24:27], v[232:235], a[40:43], v[24:27]\n\t"
      "s_add_u32 s58, s58, 786432\n\t"
      "s_lshr_b32 s59, s59, 1\n\t"
      "v_mfma_f32_16x16x32_bf16 v[24:27], v[248:251], a[44:47], v[24:27]\n\t"
      "s_bitcmp1_b32 s59, 0\n\t"
      "s_cselect_b32 s49, 24576, 0\n\t"
      "s_add_u32 s49, s49, s58\n\t"
      "v_mfma_f32_16x16x32_bf16 v[28:31], v[172:175], a[24:27], v[28:31]\n\t"
      "v_mfma_f32_16x16x32_bf16 v[28:31], v[188:191], a[28:31], v[28:31]\n\t"
      "v_cvt_pk_bf16_f32 v42, v20, v21\n\t"
      "v_cvt_pk_bf16_f32 v43, v22, v23\n\t"
      "v_mfma_f32_16x16x32_bf16 v[28:31], v[204:207], a[32:35], v[28:31]\n\t"
      "ds_write_b64 v0, v[42:43] offset:32\n\t"
      "v_mfma_f32_16x16x32_bf16 v[28:31], v[220:223], a[36:39], v[28:31]\n\t"
      "v_mfma_f32_16x16x32_bf16 v[28:31], v[236:239], a[40:43], v[28:31]\n\t"
      "v_mfma_f32_16x16x32_bf16 v[28:31], v[252:255], a[44:47], v[28:31]\n\t"
      "v_mfma_f32_16x16x32_bf16 v[32:35], a[108:111], a[24:27], v[32:35]\n\t"
      "v_mfma_f32_16x16x32_bf16 v[32:35], a[116:119], a[28:31], v[32:35]\n\t"
      "v_cvt_pk_bf16_f32 v44, v24, v25\n\t"
      "v_cvt_pk_bf16_f32 v45, v26, v27\n\t"
      "v_mfma_f32_16x16x32_bf16 v[32:35], a[124:127], a[32:35], v[32:35]\n\t"
      "ds_write_b64 v0, v[44:45] offset:64\n\t"
      "v_mfma_f32_16x16x32_bf16 v[32:35], a[132:135], a[36:39], v[32:35]\n\t"
      "buffer_load_dwordx4 v4, s[40:43], s48 offen offset:2048 nt lds\n\t"
      "v_mfma_f32_16x16x32_bf16 v[32:35], a[140:143], a[40:43], v[32:35]\n\t"
      "s_add_u32 s50, s50, 12288\n\t"
      "v_mfma_f32_16x16x32_bf16 v[32:35], a[148:151], a[44:47], v[32:35]\n\t"
      "v_cvt_pk_bf16_f32 v46, v28, v29\n\t"
      "v_cvt_pk_bf16_f32 v47, v30, v31\n\t"
      "v_mfma_f32_16x16x32_bf16 v[36:39], a[112:115], a[24:27], v[36:39]\n\t"
      "ds_write_b64 v0, v[46:47] offset:96\n\t"
      "v_mfma_f32_16x16x32_bf16 v[36:39], a[120:123], a[28:31], v[36:39]\n\t"
      "s_cmp_eq_u32 s50, 160256\n\t"
      "s_cselect_b32 s50, 25088, s50\n\t"
      "v_mfma_f32_16x16x32_bf16 v[36:39], a[128:131], a[32:35], v[36:39]\n\t"
      "v_mfma_f32_16x16x32_bf16 v[36:39], a[136:139], a[36:39], v[36:39]\n\t"
      "v_mfma_f32_16x16x32_bf16 v[36:39], a[144:147], a[40:43], v[36:39]\n\t"
      "v_cvt_pk_bf16_f32 v48, v32, v33\n\t"
      "v_cvt_pk_bf16_f32 v49, v34, v35\n\t"
      "v_mfma_f32_16x16x32_bf16 v[36:39], a[152:155], a[44:47], v[36:39]\n\t"
      "ds_write_b64 v0, v[48:49] offset:128\n\t"
      ".Lmid2_%=:\n\t"
      "v_mfma_f32_16x16x32_bf16 v[16:19], v[64:67], a[0:3], 0\n\t"
      "ds_read_b128 a[24:27], v54 offset:256\n\t"
      "v_mfma_f32_16x16x32_bf16 v[16:19], v[80:83], a[4:7], v[16:19]\n\t"
      "ds_read_b128 a[28:31], v55 offset:256\n\t"
      "v_mfma_f32_16x16x32_bf16 v[16:19], v[96:99], a[8:11], v[16:19]\n\t"
      "ds_read_b128 a[32:35], v52 offset:512\n\t"
      "v_mfma_f32_16x16x32_bf16 v[16:19], v[112:115], a[12:15], v[16:19]\n\t"
      "ds_read_b128 a[36:39], v53 offset:512\n\t"
      "v_mfma_f32_16x16x32_bf16 v[16:19], v[128:131], a[16:19], v[16:19]\n\t"
      "ds_read_b128 a[40:43], v54 offset:512\n\t"
      "v_mfma_f32_16x16x32_bf16 v[16:19], v[144:147], a[20:23], v[16:19]\n\t"
      "ds_read_b128 a[44:47], v55 offset:512\n\t"
      "v_mfma_f32_16x16x32_bf16 v[20:23], v[68:71], a[0:3], 0\n\t"
      "v_cvt_pk_bf16_f32 v50, v36, v37\n\t"
      "v_cvt_pk_bf16_f32 v51, v38, v39\n\t"
      "v_mfma_f32_16x16x32_bf16 v[20:23], v[84:87], a[4:7], v[20:23]\n\t"
      "ds_write_b64 v0, v[50:51] offset:160\n\t"
      "v_mfma_f32_16x16x32_bf16 v[20:23], v[100:103], a[8:11], v[20:23]\n\t"
      "v_add_u32 v56, s51, v15\n\t"
      "v_xor_b32 v57, 64, v56\n\t"
      "v_mfma_f32_16x16x32_bf16 v[20:23], v[116:119], a[12:15], v[20:23]\n\t"
      "v_xor_b32 v58, 128, v56\n\t"
      "v_xor_b32 v59, 192, v56\n\t"
      "v_mfma_f32_16x16x32_bf16 v[20:23], v[132:135], a[16:19], v[20:23]\n\t"
      "v_mfma_f32_16x16x32_bf16 v[20:23], v[148:151], a[20:23], v[20:23]\n\t"
      "v_mfma_f32_16x16x32_bf16 v[24:27], v[72:75], a[0:3], 0\n\t"
      "v_mfma_f32_16x16x32_bf16 v[24:27], v[88:91], a[4:7], v[24:27]\n\t"
      "v_mfma_f32_16x16x32_bf16 v[24:27], v[104:107], a[8:11], v[24:27]\n\t"
      "v_mfma_f32_16x16x32_bf16 v[24:27], v[120:123], a[12:15], v[24:27]\n\t"
      "v_mfma_f32_16x16x32_bf16 v[24:27], v[136:139], a[16:19], v[24:27]\n\t"
      "s_waitcnt vmcnt(12)\n\t"
      "s_waitcnt lgkmcnt(0)\n\t"
      "s_barrier\n\t"
      "v_mfma_f32_16x16x32_bf16 v[24:27], v[152:155], a[20:23], v[24:27]\n\t"
      "v_mfma_f32_16x16x32_bf16 v[28:31], v[76:79], a[0:3], 0\n\t"
      "ds_read_b128 a[48:51], v8 offset:0\n\t"
      "v_mfma_f32_16x16x32_bf16 v[28:31], v[92:95], a[4:7], v[28:31]\n\t"
      "ds_read_b128 a[52:55], v60 offset:1024\n\t"
      "v_mfma_f32_16x16x32_bf16 v[28:31], v[108:111], a[8:11], v[28:31]\n\t"
      "ds_read_b128 a[56:59], v61 offset:2048\n\t"
      "v_mfma_f32_16x16x32_bf16 v[28:31], v[124:127], a[12:15], v[28:31]\n\t"
      "v_mfma_f32_16x16x32_bf16 v[28:31], v[140:143], a[16:19], v[28:31]\n\t"
      "v_mfma_f32_16x16x32_bf16 v[28:31], v[156:159], a[20:23], v[28:31]\n\t"
      "v_mfma_f32_16x16x32_bf16 v[32:35], a[60:63], a[0:3], 0\n\t"
      "v_mfma_f32_16x16x32_bf16 v[32:35], a[68:71], a[4:7], v[32:35]\n\t"
      "s_waitcnt lgkmcnt(2)\n\t"
      "v_mfma_f32_16x16x32_bf16 v[32:35], a[76:79], a[8:11], v[32:35]\n\t"
      "buffer_store_dwordx4 a[48:51], v9, s[44:47], s49 offen sc1\n\t"
      "v_mfma_f32_16x16x32_bf16 v[32:35], a[84:87], a[12:15], v[32:35]\n\t"
      "v_mfma_f32_16x16x32_bf16 v[32:35], a[92:95], a[16:19], v[32:35]\n\t"
      "v_mfma_f32_16x16x32_bf16 v[32:35], a[100:103], a[20:23], v[32:35]\n\t"
      "s_add_u32 m0, s18, s50\n\t"
      "v_mfma_f32_16x16x32_bf16 v[36:39], a[64:67], a[0:3], 0\n\t"
      "buffer_load_dwordx4 v5, s[40:43], s48 offen nt lds\n\t"
      "v_mfma_f32_16x16x32_bf16 v[36:39], a[72:75], a[4:7], v[36:39]\n\t"
      "v_mfma_f32_16x16x32_bf16 v[36:39], a[80:83], a[8:11], v[36:39]\n\t"
      "v_mfma_f32_16x16x32_bf16 v[36:39], a[88:91], a[12:15], v[36:39]\n\t"
      "s_waitcnt lgkmcnt(1)\n\t"
      "v_mfma_f32_16x16x32_bf16 v[36:39], a[96:99], a[16:19], v[36:39]\n\t"
      "buffer_store_dwordx4 a[52:55], v10, s[44:47], s49 offen offset:1024 sc1\n\t"
      "v_mfma_f32_16x16x32_bf16 v[36:39], a[104:107], a[20:23], v[36:39]\n\t"
      "v_mfma_f32_16x16x32_bf16 v[16:19], v[160:163], a[24:27], v[16:19]\n\t"
      "ds_read_b128 a[0:3], v56 offset:0\n\t"
      "v_mfma_f32_16x16x32_bf16 v[16:19], v[176:179], a[28:31], v[16:19]\n\t"
      "ds_read_b128 a[4:7], v57 offset:0\n\t"
      "v_mfma_f32_16x16x32_bf16 v[16:19], v[192:195], a[32:35], v[16:19]\n\t"
      "ds_read_b128 a[8:11], v58 offset:0\n\t"
      "v_mfma_f32_16x16x32_bf16 v[16:19], v[208:211], a[36:39], v[16:19]\n\t"
      "ds_read_b128 a[12:15], v59 offset:0\n\t"
      "v_mfma_f32_16x16x32_bf16 v[16:19], v[224:227], a[40:43], v[16:19]\n\t"
      "ds_read_b128 a[16:19], v56 offset:256\n\t"
      "v_mfma_f32_16x16x32_bf16 v[16:19], v[240:243], a[44:47], v[16:19]\n\t"
      "ds_read_b128 a[20:23], v57 offset:256\n\t"
      "v_mfma_f32_16x16x32_bf16 v[20:23], v[164:167], a[24:27], v[20:23]\n\t"
      "s_add_u32 s51, s51, 12288\n\t"
      "s_cmp_eq_u32 s51, 160256\n\t"
      "s_cselect_b32 s51, 25088, s51\n\t"
      "v_mfma_f32_16x16x32_bf16 v[20:23], v[180:183], a[28:31], v[20:23]\n\t"
      "s_waitcnt lgkmcnt(6)\n\t"
      "v_mfma_f32_16x16x32_bf16 v[20:23], v[196:199], a[32:35], v[20:23]\n\t"
      "buffer_store_dwordx4 a[56:59], v11, s[44:47], s49 offen offset:2048 sc1\n\t"
      "v_mfma_f32_16x16x32_bf16 v[20:23], v[212:215], a[36:39], v[20:23]\n\t"
      "v_mfma_f32_16x16x32_bf16 v[20:23], v[228:231], a[40:43], v[20:23]\n\t"
      "s_waitcnt lgkmcnt(0)\n\t"
      "v_mfma_f32_16x16x32_bf16 v[20:23], v[244:247], a[44:47], v[20:23]\n\t"
      "v_mfma_f32_16x16x32_bf16 v[24:27], v[168:171], a[24:27], v[24:27]\n\t"
      "v_mfma_f32_16x16x32_bf16 v[24:27], v[184:187], a[28:31], v[24:27]\n\t"
      "v_cvt_pk_bf16_f32 v40, v16, v17\n\t"
      "v_cvt_pk_bf16_f32 v41, v18, v19\n\t"
      "v_mfma_f32_16x16x32_bf16 v[24:27], v[200:203], a[32:35], v[24:27]\n\t"
      "ds_write_b64 v0, v[40:41] offset:12544\n\t"
      "v_mfma_f32_16x16x32_bf16 v[24:27], v[216:219], a[36:39], v[24:27]\n\t"
      "buffer_load_dwordx4 v6, s[40:43], s48 offen offset:1024 nt lds\n\t"
      "v_mfma_f32_16x16x32_bf16 v[24:27], v[232:235], a[40:43], v[24:27]\n\t"
      "v_mfma_f32_16x16x32_bf16 v[24:27], v[248:251], a[44:47], v[24:27]\n\t"
      "v_mfma_f32_16x16x32_bf16 v[28:31], v[172:175], a[24:27], v[28:31]\n\t"
      "v_mfma_f32_16x16x32_bf16 v[28:31], v[188:191], a[28:31], v[28:31]\n\t"
      "v_cvt_pk_bf16_f32 v42, v20, v21\n\t"
      "v_cvt_pk_bf16_f32 v43, v22, v23\n\t"
      "v_mfma_f32_16x16x32_bf16 v[28:31], v[204:207], a[32:35], v[28:31]\n\t"
      "ds_write_b64 v0, v[42:43] offset:12576\n\t"
      "v_mfma_f32_16x16x32_bf16 v[28:31], v[220:223], a[36:39], v[28:31]\n\t"
      "v_mfma_f32_16x16x32_bf16 v[28:31], v[236:239], a[40:43], v[28:31]\n\t"
      "v_mfma_f32_16x16x32_bf16 v[28:31], v[252:255], a[44:47], v[28:31]\n\t"
      "v_mfma_f32_16x16x32_bf16 v[32:35], a[108:111], a[24:27], v[32:35]\n\t"
      "v_mfma_f32_16x16x32_bf16 v[32:35], a[116:119], a[28:31], v[32:35]\n\t"
      "v_cvt_pk_bf16_f32 v44, v24, v25\n\t"
      "v_cvt_pk_bf16_f32 v45, v26, v27\n\t"
      "v_mfma_f32_16x16x32_bf16 v[32:35], a[124:127], a[32:35], v[32:35]\n\t"
      "ds_write_b64 v0, v[44:45] offset:12608\n\t"
      "v_mfma_f32_16x16x32_bf16 v[32:35], a[132:135], a[36:39], v[32:35]\n\t"
      "buffer_load_dwordx4 v7, s[40:43], s48 offen offset:2048 nt lds\n\t"
      "v_mfma_f32_16x16x32_bf16 v[32:35], a[140:143], a[40:43], v[32:35]\n\t"
      "s_add_u32 s50, s50, 12288\n\t"
      "v_mfma_f32_16x16x32_bf16 v[32:35], a[148:151], a[44:47], v[32:35]\n\t"
      "v_cvt_pk_bf16_f32 v46, v28, v29\n\t"
      "v_cvt_pk_bf16_f32 v47, v30, v31\n\t"
      "v_mfma_f32_16x16x32_bf16 v[36:39], a[112:115], a[24:27], v[36:39]\n\t"
      "ds_write_b64 v0, v[46:47] offset:12640\n\t"
      "v_mfma_f32_16x16x32_bf16 v[36:39], a[120:123], a[28:31], v[36:39]\n\t"
      "s_cmp_eq_u32 s50, 160256\n\t"
      "s_cselect_b32 s50, 25088, s50\n\t"
      "v_mfma_f32_16x16x32_bf16 v[36:39], a[128:131], a[32:35], v[36:39]\n\t"
      "s_sub_u32 s52, s52, 1\n\t"
      "s_cmp_lg_u32 s52, 0\n\t"
      "v_mfma_f32_16x16x32_bf16 v[36:39], a[136:139], a[36:39], v[36:39]\n\t"
      "v_mfma_f32_16x16x32_bf16 v[36:39], a[144:147], a[40:43], v[36:39]\n\t"
      "v_cvt_pk_bf16_f32 v48, v32, v33\n\t"
      "v_cvt_pk_bf16_f32 v49, v34, v35\n\t"
      "v_mfma_f32_16x16x32_bf16 v[36:39], a[152:155], a[44:47], v[36:39]\n\t"
      "ds_write_b64 v0, v[48:49] offset:12672\n\t"
      "s_cbranch_scc1 .Lloop2_%=\n\t"
      "s_branch .Lendc_%=\n\t"
      ".Lloop3_%=:\n\t"
      "v_mfma_f32_16x16x32_bf16 v[16:19], v[64:67], a[0:3], 0\n\t"
      "ds_read_b128 a[24:27], v58 offset:256\n\t"
      "v_mfma_f32_16x16x32_bf16 v[16:19], v[80:83], a[4:7], v[16:19]\n\t"
      "ds_read_b128 a[28:31], v59 offset:256\n\t"
      "v_mfma_f32_16x16x32_bf16 v[16:19], v[96:99], a[8:11], v[16:19]\n\t"
      "ds_read_b128 a[32:35], v56 offset:512\n\t"
      "v_mfma_f32_16x16x32_bf16 v[16:19], v[112:115], a[12:15], v[16:19]\n\t"
      "ds_read_b128 a[36:39], v57 offset:512\n\t"
      "v_mfma_f32_16x16x32_bf16 v[16:19], v[128:131], a[16:19], v[16:19]\n\t"
      "ds_read_b128 a[40:43], v58 offset:512\n\t"
      "v_mfma_f32_16x16x32_bf16 v[16:19], v[144:147], a[20:23], v[16:19]\n\t"
      "ds_read_b128 a[44:47], v59 offset:512\n\t"
      "v_mfma_f32_16x16x32_bf16 v[20:23], v[68:71], a[0:3], 0\n\t"
      "v_cvt_pk_bf16_f32 v50, v36, v37\n\t"
      "v_cvt_pk_bf16_f32 v51, v38, v39\n\t"
      "v_mfma_f32_16x16x32_bf16 v[20:23], v[84:87], a[4:7], v[20:23]\n\t"
      "ds_write_b64 v0, v[50:51] offset:12704\n\t"
      "v_mfma_f32_16x16x32_bf16 v[20:23], v[100:103], a[8:11], v[20:23]\n\t"
      "v_add_u32 v52, s51, v15\n\t"
      "v_xor_b32 v53, 64, v52\n\t"
      "v_mfma_f32_16x16x32_bf16 v[20:23], v[116:119], a[12:15], v[20:23]\n\t"
      "v_xor_b32 v54, 128, v52\n\t"
      "v_xor_b32 v55, 192, v52\n\t"
      "v_mfma_f32_16x16x32_bf16 v[20:23], v[132:135], a[16:19], v[20:23]\n\t"
      "s_add_u32 s56, s56, 786432\n\t"
      "s_lshr_b32 s57, s57, 1\n\t"
      "v_mfma_f32_16x16x32_bf16 v[20:23], v[148:151], a[20:23], v[20:23]\n\t"
      "s_bitcmp1_b32 s57, 0\n\t"
      "s_cselect_b32 s48, 24576, 0\n\t"
      "s_add_u32 s48, s48, s56\n\t"
      "v_mfma_f32_16x16x32_bf16 v[24:27], v[72:75], a[0:3], 0\n\t"
      "v_mfma_f32_16x16x32_bf16 v[24:27], v[88:91], a[4:7], v[24:27]\n\t"
      "v_mfma_f32_16x16x32_bf16 v[24:27], v[104:107], a[8:11], v[24:27]\n\t"
      "v_mfma_f32_16x16x32_bf16 v[24:27], v[120:123], a[12:15], v[24:27]\n\t"
      "v_mfma_f32_16x16x32_bf16 v[24:27], v[136:139], a[16:19], v[24:27]\n\t"
      "v_mfma_f32_16x16x32_bf16 v[24:27], v[152:155], a[20:23], v[24:27]\n\t"
      "s_waitcnt vmcnt(12)\n\t"
      "s_waitcnt lgkmcnt(0)\n\t"
      "s_barrier\n\t"
      "v_mfma_f32_16x16x32_bf16 v[28:31], v[76:79], a[0:3], 0\n\t"
      "ds_read_b128 a[48:51], v8 offset:12544\n\t"
      "v_mfma_f32_16x16x32_bf16 v[28:31], v[92:95], a[4:7], v[28:31]\n\t"
      "ds_read_b128 a[52:55], v60 offset:13568\n\t"
      "v_mfma_f32_16x16x32_bf16 v[28:31], v[108:111], a[8:11], v[28:31]\n\t"
      "ds_read_b128 a[56:59], v61 offset:14592\n\t"
      "v_mfma_f32_16x16x32_bf16 v[28:31], v[124:127], a[12:15], v[28:31]\n\t"
      "v_mfma_f32_16x16x32_bf16 v[28:31], v[140:143], a[16:19], v[28:31]\n\t"
      "v_mfma_f32_16x16x32_bf16 v[28:31], v[156:159], a[20:23], v[28:31]\n\t"
      "v_mfma_f32_16x16x32_bf16 v[32:35], a[60:63], a[0:3], 0\n\t"
      "v_mfma_f32_16x16x32_bf16 v[32:35], a[68:71], a[4:7], v[32:35]\n\t"
      "s_waitcnt lgkmcnt(2)\n\t"
      "v_mfma_f32_16x16x32_bf16 v[32:35], a[76:79], a[8:11], v[32:35]\n\t"
      "buffer_store_dwordx4 a[48:51], v12, s[44:47], s49 offen sc1\n\t"
      "v_mfma_f32_16x16x32_bf16 v[32:35], a[84:87], a[12:15], v[32:35]\n\t"
      "v_mfma_f32_16x16x32_bf16 v[32:35], a[92:95], a[16:19], v[32:35]\n\t"
      "v_mfma_f32_16x16x32_bf16 v[32:35], a[100:103], a[20:23], v[32:35]\n\t"
      "s_add_u32 m0, s18, s50\n\t"
      "v_mfma_f32_16x16x32_bf16 v[36:39], a[64:67], a[0:3], 0\n\t"
      "buffer_load_dwordx4 v2, s[40:43], s48 offen nt lds\n\t"
      "v_mfma_f32_16x16x32_bf16 v[36:39], a[72:75], a[4:7], v[36:39]\n\t"
      "v_mfma_f32_16x16x32_bf16 v[36:39], a[80:83], a[8:11], v[36:39]\n\t"
      "v_mfma_f32_16x16x32_bf16 v[36:39], a[88:91], a[12:15], v[36:39]\n\t"
      "s_waitcnt lgkmcnt(1)\n\t"
      "v_mfma_f32_16x16x32_bf16 v[36:39], a[96:99], a[16:19], v[36:39]\n\t"
      "buffer_store_dwordx4 a[52:55], v13, s[44:47], s49 offen offset:1024 sc1\n\t"
      "v_mfma_f32_16x16x32_bf16 v[36:39], a[104:107], a[20:23], v[36:39]\n\t"
      "v_mfma_f32_16x16x32_bf16 v[16:19], v[160:163], a[24:27], v[16:19]\n\t"
      "ds_read_b128 a[0:3], v52 offset:0\n\t"
      "v_mfma_f32_16x16x32_bf16 v[16:19], v[176:179], a[28:31], v[16:19]\n\t"
      "ds_read_b128 a[4:7], v53 offset:0\n\t"
      "v_mfma_f32_16x16x32_bf16 v[16:19], v[192:195], a[32:35], v[16:19]\n\t"
      "ds_read_b128 a[8:11], v54 offset:0\n\t"
      "v_mfma_f32_16x16x32_bf16 v[16:19], v[208:211], a[36:39], v[16:19]\n\t"
      "ds_read_b128 a[12:15], v55 offset:0\n\t"
      "v_mfma_f32_16x16x32_bf16 v[16:19], v[224:227], a[40:43], v[16:19]\n\t"
      "ds_read_b128 a[16:19], v52 offset:256\n\t"
      "v_mfma_f32_16x16x32_bf16 v[16:19], v[240:243], a[44:47], v[16:19]\n\t"
      "ds_read_b128 a[20:23], v53 offset:256\n\t"
      "v_mfma_f32_16x16x32_bf16 v[20:23], v[164:167], a[24:27], v[20:23]\n\t"
      "s_add_u32 s51, s51, 12288\n\t"
      "s_cmp_eq_u32 s51, 160256\n\t"
      "s_cselect_b32 s51, 25088, s51\n\t"
      "v_mfma_f32_16x16x32_bf16 v[20:23], v[180:183], a[28:31], v[20:23]\n\t"
      "s_waitcnt lgkmcnt(6)\n\t"
      "v_mfma_f32_16x16x32_bf16 v[20:23], v[196:199], a[32:35], v[20:23]\n\t"
      "buffer_store_dwordx4 a[56:59], v14, s[44:47], s49 offen offset:2048 sc1\n\t"
      "v_mfma_f32_16x16x32_bf16 v[20:23], v[212:215], a[36:39], v[20:23]\n\t"
      "v_mfma_f32_16x16x32_bf16 v[20:23], v[228:231], a[40:43], v[20:23]\n\t"
      "s_waitcnt lgkmcnt(0)\n\t"
      "v_mfma_f32_16x16x32_bf16 v[20:23], v[244:247], a[44:47], v[20:23]\n\t"
      "v_mfma_f32_16x16x32_bf16 v[24:27], v[168:171], a[24:27], v[24:27]\n\t"
      "v_mfma_f32_16x16x32_bf16 v[24:27], v[184:187], a[28:31], v[24:27]\n\t"
      "v_cvt_pk_bf16_f32 v40, v16, v17\n\t"
      "v_cvt_pk_bf16_f32 v41, v18, v19\n\t"
      "v_mfma_f32_16x16x32_bf16 v[24:27], v[200:203], a[32:35], v[24:27]\n\t"
      "ds_write_b64 v0, v[40:41] offset:0\n\t"
      "v_mfma_f32_16x16x32_bf16 v[24:27], v[216:219], a[36:39], v[24:27]\n\t"
      "buffer_load_dwordx4 v3, s[40:43], s48 offen offset:1024 nt lds\n\t"
      "v_mfma_f32_16x16x32_bf16 v[24:27], v[232:235], a[40:43], v[24:27]\n\t"
      "s_add_u32 s58, s58, 786432\n\t"
      "s_lshr_b32 s59, s59, 1\n\t"
      "v_mfma_f32_16x16x32_bf16 v[24:27], v[248:251], a[44:47], v[24:27]\n\t"
      "s_bitcmp1_b32 s59, 0\n\t"
      "s_cselect_b32 s49, 24576, 0\n\t"
      "s_add_u32 s49, s49, s58\n\t"
      "v_mfma_f32_16x16x32_bf16 v[28:31], v[172:175], a[24:27], v[28:31]\n\t"
      "v_mfma_f32_16x16x32_bf16 v[28:31], v[188:191], a[28:31], v[28:31]\n\t"
      "v_cvt_pk_bf16_f32 v42, v20, v21\n\t"
      "v_cvt_pk_bf16_f32 v43, v22, v23\n\t"
      "v_mfma_f32_16x16x32_bf16 v[28:31], v[204:207], a[32:35], v[28:31]\n\t"
      "ds_write_b64 v0, v[42:43] offset:32\n\t"
      "v_mfma_f32_16x16x32_bf16 v[28:31], v[220:223], a[36:39], v[28:31]\n\t"
      "v_mfma_f32_16x16x32_bf16 v[28:31], v[236:239], a[40:43], v[28:31]\n\t"
      "v_mfma_f32_16x16x32_bf16 v[28:31], v[252:255], a[44:47], v[28:31]\n\t"
      "v_mfma_f32_16x16x32_bf16 v[32:35], a[108:111], a[24:27], v[32:35]\n\t"
      "v_mfma_f32_16x16x32_bf16 v[32:35], a[116:119], a[28:31], v[32:35]\n\t"
      "v_cvt_pk_bf16_f32 v44, v24, v25\n\t"
      "v_cvt_pk_bf16_f32 v45, v26, v27\n\t"
      "v_mfma_f32_16x16x32_bf16 v[32:35], a[124:127], a[32:35], v[32:35]\n\t"
      "ds_write_b64 v0, v[44:45] offset:64\n\t"
      "v_mfma_f32_16x16x32_bf16 v[32:35], a[132:135], a[36:39], v[32:35]\n\t"
      "buffer_load_dwordx4 v4, s[40:43], s48 offen offset:2048 nt lds\n\t"
      "v_mfma_f32_16x16x32_bf16 v[32:35], a[140:143], a[40:43], v[32:35]\n\t"
      "s_add_u32 s50, s50, 12288\n\t"
      "v_mfma_f32_16x16x32_bf16 v[32:35], a[148:151], a[44:47], v[32:35]\n\t"
      "v_cvt_pk_bf16_f32 v46, v28, v29\n\t"
      "v_cvt_pk_bf16_f32 v47, v30, v31\n\t"
      "v_mfma_f32_16x16x32_bf16 v[36:39], a[112:115], a[24:27], v[36:39]\n\t"
      "ds_write_b64 v0, v[46:47] offset:96\n\t"
      "v_mfma_f32_16x16x32_bf16 v[36:39], a[120:123], a[28:31], v[36:39]\n\t"
      "s_cmp_eq_u32 s50, 160256\n\t"
      "s_cselect_b32 s50, 25088, s50\n\t"
      "v_mfma_f32_16x16x32_bf16 v[36:39], a[128:131], a[32:35], v[36:39]\n\t"
      "v_mfma_f32_16x16x32_bf16 v[36:39], a[136:139], a[36:39], v[36:39]\n\t"
      "v_mfma_f32_16x16x32_bf16 v[36:39], a[144:147], a[40:43], v[36:39]\n\t"
      "v_cvt_pk_bf16_f32 v48, v32, v33\n\t"
      "v_cvt_pk_bf16_f32 v49, v34, v35\n\t"
      "v_mfma_f32_16x16x32_bf16 v[36:39], a[152:155], a[44:47], v[36:39]\n\t"
      "ds_write_b64 v0, v[48:49] offset:128\n\t"
      ".Lmid3_%=:\n\t"
      "v_mfma_f32_16x16x32_bf16 v[16:19], v[64:67], a[0:3], 0\n\t"
      "ds_read_b128 a[24:27], v54 offset:256\n\t"
      "v_mfma_f32_16x16x32_bf16 v[16:19], v[80:83], a[4:7], v[16:19]\n\t"
      "ds_read_b128 a[28:31], v55 offset:256\n\t"
      "v_mfma_f32_16x16x32_bf16 v[16:19], v[96:99], a[8:11], v[16:19]\n\t"
      "ds_read_b128 a[32:35], v52 offset:512\n\t"
      "v_mfma_f32_16x16x32_bf16 v[16:19], v[112:115], a[12:15], v[16:19]\n\t"
      "ds_read_b128 a[36:39], v53 offset:512\n\t"
      "v_mfma_f32_16x16x32_bf16 v[16:19], v[128:131], a[16:19], v[16:19]\n\t"
      "ds_read_b128 a[40:43], v54 offset:512\n\t"
      "v_mfma_f32_16x16x32_bf16 v[16:19], v[144:147], a[20:23], v[16:19]\n\t"
      "ds_read_b128 a[44:47], v55 offset:512\n\t"
      "v_mfma_f32_16x16x32_bf16 v[20:23], v[68:71], a[0:3], 0\n\t"
      "v_cvt_pk_bf16_f32 v50, v36, v37\n\t"
      "v_cvt_pk_bf16_f32 v51, v38, v39\n\t"
      "v_mfma_f32_16x16x32_bf16 v[20:23], v[84:87], a[4:7], v[20:23]\n\t"
      "ds_write_b64 v0, v[50:51] offset:160\n\t"
      "v_mfma_f32_16x16x32_bf16 v[20:23], v[100:103], a[8:11], v[20:23]\n\t"
      "v_add_u32 v56, s51, v15\n\t"
      "v_xor_b32 v57, 64, v56\n\t"
      "v_mfma_f32_16x16x32_bf16 v[20:23], v[116:119], a[12:15], v[20:23]\n\t"
      "v_xor_b32 v58, 128, v56\n\t"
      "v_xor_b32 v59, 192, v56\n\t"
      "v_mfma_f32_16x16x32_bf16 v[20:23], v[132:135], a[16:19], v[20:23]\n\t"
      "v_mfma_f32_16x16x32_bf16 v[20:23], v[148:151], a[20:23], v[20:23]\n\t"
      "v_mfma_f32_16x16x32_bf16 v[24:27], v[72:75], a[0:3], 0\n\t"
      "v_mfma_f32_16x16x32_bf16 v[24:27], v[88:91], a[4:7], v[24:27]\n\t"
      "v_mfma_f32_16x16x32_bf16 v[24:27], v[104:107], a[8:11], v[24:27]\n\t"
      "v_mfma_f32_16x16x32_bf16 v[24:27], v[120:123], a[12:15], v[24:27]\n\t"
      "v_mfma_f32_16x16x32_bf16 v[24:27], v[136:139], a[16:19], v[24:27]\n\t"
      "v_mfma_f32_16x16x32_bf16 v[24:27], v[152:155], a[20:23], v[24:27]\n\t"
      "s_waitcnt vmcnt(12)\n\t"
      "s_waitcnt lgkmcnt(0)\n\t"
      "s_barrier\n\t"
      "v_mfma_f32_16x16x32_bf16 v[28:31], v[76:79], a[0:3], 0\n\t"
      "ds_read_b128 a[48:51], v8 offset:0\n\t"
      "v_mfma_f32_16x16x32_bf16 v[28:31], v[92:95], a[4:7], v[28:31]\n\t"
      "ds_read_b128 a[52:55], v60 offset:1024\n\t"
      "v_mfma_f32_16x16x32_bf16 v[28:31], v[108:111], a[8:11], v[28:31]\n\t"
      "ds_read_b128 a[56:59], v61 offset:2048\n\t"
      "v_mfma_f32_16x16x32_bf16 v[28:31], v[124:127], a[12:15], v[28:31]\n\t"
      "v_mfma_f32_16x16x32_bf16 v[28:31], v[140:143], a[16:19], v[28:31]\n\t"
      "v_mfma_f32_16x16x32_bf16 v[28:31], v[156:159], a[20:23], v[28:31]\n\t"
      "v_mfma_f32_16x16x32_bf16 v[32:35], a[60:63], a[0:3], 0\n\t"
      "v_mfma_f32_16x16x32_bf16 v[32:35], a[68:71], a[4:7], v[32:35]\n\t"
      "s_waitcnt lgkmcnt(2)\n\t"
      "v_mfma_f32_16x16x32_bf16 v[32:35], a[76:79], a[8:11], v[32:35]\n\t"
      "buffer_store_dwordx4 a[48:51], v9, s[44:47], s49 offen sc1\n\t"
      "v_mfma_f32_16x16x32_bf16 v[32:35], a[84:87], a[12:15], v[32:35]\n\t"
      "v_mfma_f32_16x16x32_bf16 v[32:35], a[92:95], a[16:19], v[32:35]\n\t"
      "v_mfma_f32_16x16x32_bf16 v[32:35], a[100:103], a[20:23], v[32:35]\n\t"
      "s_add_u32 m0, s18, s50\n\t"
      "v_mfma_f32_16x16x32_bf16 v[36:39], a[64:67], a[0:3], 0\n\t"
      "buffer_load_dwordx4 v5, s[40:43], s48 offen nt lds\n\t"
      "v_mfma_f32_16x16x32_bf16 v[36:39], a[72:75], a[4:7], v[36:39]\n\t"
      "v_mfma_f32_16x16x32_bf16 v[36:39], a[80:83], a[8:11], v[36:39]\n\t"
      "v_mfma_f32_16x16x32_bf16 v[36:39], a[88:91], a[12:15], v[36:39]\n\t"
      "s_waitcnt lgkmcnt(1)\n\t"
      "v_mfma_f32_16x16x32_bf16 v[36:39], a[96:99], a[16:19], v[36:39]\n\t"
      "buffer_store_dwordx4 a[52:55], v10, s[44:47], s49 offen offset:1024 sc1\n\t"
      "v_mfma_f32_16x16x32_bf16 v[36:39], a[104:107], a[20:23], v[36:39]\n\t"
      "v_mfma_f32_16x16x32_bf16 v[16:19], v[160:163], a[24:27], v[16:19]\n\t"
      "ds_read_b128 a[0:3], v56 offset:0\n\t"
      "v_mfma_f32_16x16x32_bf16 v[16:19], v[176:179], a[28:31], v[16:19]\n\t"
      "ds_read_b128 a[4:7], v57 offset:0\n\t"
      "v_mfma_f32_16x16x32_bf16 v[16:19], v[192:195], a[32:35], v[16:19]\n\t"
      "ds_read_b128 a[8:11], v58 offset:0\n\t"
      "v_mfma_f32_16x16x32_bf16 v[16:19], v[208:211], a[36:39], v[16:19]\n\t"
      "ds_read_b128 a[12:15], v59 offset:0\n\t"
      "v_mfma_f32_16x16x32_bf16 v[16:19], v[224:227], a[40:43], v[16:19]\n\t"
      "ds_read_b128 a[16:19], v56 offset:256\n\t"
      "v_mfma_f32_16x16x32_bf16 v[16:19], v[240:243], a[44:47], v[16:19]\n\t"
      "ds_read_b128 a[20:23], v57 offset:256\n\t"
      "v_mfma_f32_16x16x32_bf16 v[20:23], v[164:167], a[24:27], v[20:23]\n\t"
      "s_add_u32 s51, s51, 12288\n\t"
      "s_cmp_eq_u32 s51, 160256\n\t"
      "s_cselect_b32 s51, 25088, s51\n\t"
      "v_mfma_f32_16x16x32_bf16 v[20:23], v[180:183], a[28:31], v[20:23]\n\t"
      "s_waitcnt lgkmcnt(6)\n\t"
      "v_mfma_f32_16x16x32_bf16 v[20:23], v[196:199], a[32:35], v[20:23]\n\t"
      "buffer_store_dwordx4 a[56:59], v11, s[44:47], s49 offen offset:2048 sc1\n\t"
      "v_mfma_f32_16x16x32_bf16 v[20:23], v[212:215], a[36:39], v[20:23]\n\t"
      "v_mfma_f32_16x16x32_bf16 v[20:23], v[228:231], a[40:43], v[20:23]\n\t"
      "s_waitcnt lgkmcnt(0)\n\t"
      "v_mfma_f32_16x16x32_bf16 v[20:23], v[244:247], a[44:47], v[20:23]\n\t"
      "v_mfma_f32_16x16x32_bf16 v[24:27], v[168:171], a[24:27], v[24:27]\n\t"
      "v_mfma_f32_16x16x32_bf16 v[24:27], v[184:187], a[28:31], v[24:27]\n\t"
      "v_cvt_pk_bf16_f32 v40, v16, v17\n\t"
      "v_cvt_pk_bf16_f32 v41, v18, v19\n\t"
      "v_mfma_f32_16x16x32_bf16 v[24:27], v[200:203], a[32:35], v[24:27]\n\t"
      "ds_write_b64 v0, v[40:41] offset:12544\n\t"
      "v_mfma_f32_16x16x32_bf16 v[24:27], v[216:219], a[36:39], v[24:27]\n\t"
      "buffer_load_dwordx4 v6, s[40:43], s48 offen offset:1024 nt lds\n\t"
      "v_mfma_f32_16x16x32_bf16 v[24:27], v[232:235], a[40:43], v[24:27]\n\t"
      "v_mfma_f32_16x16x32_bf16 v[24:27], v[248:251], a[44:47], v[24:27]\n\t"
      "v_mfma_f32_16x16x32_bf16 v[28:31], v[172:175], a[24:27], v[28:31]\n\t"
      "v_mfma_f32_16x16x32_bf16 v[28:31], v[188:191], a[28:31], v[28:31]\n\t"
      "v_cvt_pk_bf16_f32 v42, v20, v21\n\t"
      "v_cvt_pk_bf16_f32 v43, v22, v23\n\t"
      "v_mfma_f32_16x16x32_bf16 v[28:31], v[204:207], a[32:35], v[28:31]\n\t"
      "ds_write_b64 v0, v[42:43] offset:12576\n\t"
      "v_mfma_f32_16x16x32_bf16 v[28:31], v[220:223], a[36:39], v[28:31]\n\t"
      "v_mfma_f32_16x16x32_bf16 v[28:31], v[236:239], a[40:43], v[28:31]\n\t"
      "v_mfma_f32_16x16x32_bf16 v[28:31], v[252:255], a[44:47], v[28:31]\n\t"
      "v_mfma_f32_16x16x32_bf16 v[32:35], a[108:111], a[24:27], v[32:35]\n\t"
      "v_mfma_f32_16x16x32_bf16 v[32:35], a[116:119], a[28:31], v[32:35]\n\t"
      "v_cvt_pk_bf16_f32 v44, v24, v25\n\t"
      "v_cvt_pk_bf16_f32 v45, v26, v27\n\t"
      "v_mfma_f32_16x16x32_bf16 v[32:35], a[124:127], a[32:35], v[32:35]\n\t"
      "ds_write_b64 v0, v[44:45] offset:12608\n\t"
      "v_mfma_f32_16x16x32_bf16 v[32:35], a[132:135], a[36:39], v[32:35]\n\t"
      "buffer_load_dwordx4 v7, s[40:43], s48 offen offset:2048 nt lds\n\t"
      "v_mfma_f32_16x16x32_bf16 v[32:35], a[140:143], a[40:43], v[32:35]\n\t"
      "s_add_u32 s50, s50, 12288\n\t"
      "v_mfma_f32_16x16x32_bf16 v[32:35], a[148:151], a[44:47], v[32:35]\n\t"
      "v_cvt_pk_bf16_f32 v46, v28, v29\n\t"
      "v_cvt_pk_bf16_f32 v47, v30, v31\n\t"
      "v_mfma_f32_16x16x32_bf16 v[36:39], a[112:115], a[24:27], v[36:39]\n\t"
      "ds_write_b64 v0, v[46:47] offset:12640\n\t"
      "v_mfma_f32_16x16x32_bf16 v[36:39], a[120:123], a[28:31], v[36:39]\n\t"
      "s_cmp_eq_u32 s50, 160256\n\t"
      "s_cselect_b32 s50, 25088, s50\n\t"
      "v_mfma_f32_16x16x32_bf16 v[36:39], a[128:131], a[32:35], v[36:39]\n\t"
      "s_sub_u32 s52, s52, 1\n\t"
      "s_cmp_lg_u32 s52, 0\n\t"
      "v_mfma_f32_16x16x32_bf16 v[36:39], a[136:139], a[36:39], v[36:39]\n\t"
      "v_mfma_f32_16x16x32_bf16 v[36:39], a[144:147], a[40:43], v[36:39]\n\t"
      "v_cvt_pk_bf16_f32 v48, v32, v33\n\t"
      "v_cvt_pk_bf16_f32 v49, v34, v35\n\t"
      "v_mfma_f32_16x16x32_bf16 v[36:39], a[152:155], a[44:47], v[36:39]\n\t"
      "ds_write_b64 v0, v[48:49] offset:12672\n\t"
      "s_cbranch_scc1 .Lloop3_%=\n\t"
      ".Lendc_%=:\n\t"
      "s_cmp_eq_u32 s48, 113221632\n\t"
      "s_cbranch_scc1 .Lwrap3_%=\n\t"
      ".Lwrapped3_%=:\n\t"
      "v_mfma_f32_16x16x32_bf16 v[16:19], v[64:67], a[0:3], 0\n\t"
      "ds_read_b128 a[24:27], v58 offset:256\n\t"
      "v_mfma_f32_16x16x32_bf16 v[16:19], v[80:83], a[4:7], v[16:19]\n\t"
      "ds_read_b128 a[28:31], v59 offset:256\n\t"
      "v_mfma_f32_16x16x32_bf16 v[16:19], v[96:99], a[8:11], v[16:19]\n\t"
      "ds_read_b128 a[32:35], v56 offset:512\n\t"
      "v_mfma_f32_16x16x32_bf16 v[16:19], v[112:115], a[12:15], v[16:19]\n\t"
      "ds_read_b128 a[36:39], v57 offset:512\n\t"
      "v_mfma_f32_16x16x32_bf16 v[16:19], v[128:131], a[16:19], v[16:19]\n\t"
      "ds_read_b128 a[40:43], v58 offset:512\n\t"
      "v_mfma_f32_16x16x32_bf16 v[16:19], v[144:147], a[20:23], v[16:19]\n\t"
      "ds_read_b128 a[44:47], v59 offset:512\n\t"
      "v_mfma_f32_16x16x32_bf16 v[20:23], v[68:71], a[0:3], 0\n\t"
      "v_cvt_pk_bf16_f32 v50, v36, v37\n\t"
      "v_cvt_pk_bf16_f32 v51, v38, v39\n\t"
      "v_mfma_f32_16x16x32_bf16 v[20:23], v[84:87], a[4:7], v[20:23]\n\t"
      "ds_write_b64 v0, v[50:51] offset:12704\n\t"
      "v_mfma_f32_16x16x32_bf16 v[20:23], v[100:103], a[8:11], v[20:23]\n\t"
      "v_add_u32 v52, s51, v15\n\t"
      "v_xor_b32 v53, 64, v52\n\t"
      "v_mfma_f32_16x16x32_bf16 v[20:23], v[116:119], a[12:15], v[20:23]\n\t"
      "v_xor_b32 v54, 128, v52\n\t"
      "v_xor_b32 v55, 192, v52\n\t"
      "v_mfma_f32_16x16x32_bf16 v[20:23], v[132:135], a[16:19], v[20:23]\n\t"
      "s_add_u32 s58, s58, 786432\n\t"
      "s_lshr_b32 s59, s59, 1\n\t"
      "v_mfma_f32_16x16x32_bf16 v[20:23], v[148:151], a[20:23], v[20:23]\n\t"
      "s_bitcmp1_b32 s59, 0\n\t"
      "s_cselect_b32 s84, 24576, 0\n\t"
      "s_add_u32 s84, s84, s58\n\t"
      "v_mfma_f32_16x16x32_bf16 v[24:27], v[72:75], a[0:3], 0\n\t"
      "s_add_u32 s58, s58, 786432\n\t"
      "s_lshr_b32 s59, s59, 1\n\t"
      "v_mfma_f32_16x16x32_bf16 v[24:27], v[88:91], a[4:7], v[24:27]\n\t"
      "s_bitcmp1_b32 s59, 0\n\t"
      "s_cselect_b32 s85, 24576, 0\n\t"
      "s_add_u32 s85, s85, s58\n\t"
      "v_mfma_f32_16x16x32_bf16 v[24:27], v[104:107], a[8:11], v[24:27]\n\t"
      "s_waitcnt vmcnt(12)\n\t"
      "s_waitcnt lgkmcnt(0)\n\t"
      "s_barrier\n\t"
      "v_mfma_f32_16x16x32_bf16 v[24:27], v[120:123], a[12:15], v[24:27]\n\t"
      "v_mfma_f32_16x16x32_bf16 v[24:27], v[136:139], a[16:19], v[24:27]\n\t"
      "v_mfma_f32_16x16x32_bf16 v[24:27], v[152:155], a[20:23], v[24:27]\n\t"
      "v_mfma_f32_16x16x32_bf16 v[28:31], v[76:79], a[0:3], 0\n\t"
      "ds_read_b128 a[188:191], v8 offset:12544\n\t"
      "v_mfma_f32_16x16x32_bf16 v[28:31], v[92:95], a[4:7], v[28:31]\n\t"
      "ds_read_b128 a[192:195], v60 offset:13568\n\t"
      "v_mfma_f32_16x16x32_bf16 v[28:31], v[108:111], a[8:11], v[28:31]\n\t"
      "ds_read_b128 a[196:199], v61 offset:14592\n\t"
      "v_mfma_f32_16x16x32_bf16 v[28:31], v[124:127], a[12:15], v[28:31]\n\t"
      "v_mfma_f32_16x16x32_bf16 v[28:31], v[140:143], a[16:19], v[28:31]\n\t"
      "v_mfma_f32_16x16x32_bf16 v[28:31], v[156:159], a[20:23], v[28:31]\n\t"
      "v_mfma_f32_16x16x32_bf16 v[32:35], a[60:63], a[0:3], 0\n\t"
      "v_mfma_f32_16x16x32_bf16 v[32:35], a[68:71], a[4:7], v[32:35]\n\t"
      "v_mfma_f32_16x16x32_bf16 v[32:35], a[76:79], a[8:11], v[32:35]\n\t"
      "v_mfma_f32_16x16x32_bf16 v[32:35], a[84:87], a[12:15], v[32:35]\n\t"
      "v_mfma_f32_16x16x32_bf16 v[32:35], a[92:95], a[16:19], v[32:35]\n\t"
      "v_mfma_f32_16x16x32_bf16 v[32:35], a[100:103], a[20:23], v[32:35]\n\t"
      "v_mfma_f32_16x16x32_bf16 v[36:39], a[64:67], a[0:3], 0\n\t"
      "v_mfma_f32_16x16x32_bf16 v[36:39], a[72:75], a[4:7], v[36:39]\n\t"
      "v_mfma_f32_16x16x32_bf16 v[36:39], a[80:83], a[8:11], v[36:39]\n\t"
      "v_mfma_f32_16x16x32_bf16 v[36:39], a[88:91], a[12:15], v[36:39]\n\t"
      "v_mfma_f32_16x16x32_bf16 v[36:39], a[96:99], a[16:19], v[36:39]\n\t"
      "v_mfma_f32_16x16x32_bf16 v[36:39], a[104:107], a[20:23], v[36:39]\n\t"
      "v_mfma_f32_16x16x32_bf16 v[16:19], v[160:163], a[24:27], v[16:19]\n\t"
      "ds_read_b128 a[0:3], v52 offset:0\n\t"
      "v_mfma_f32_16x16x32_bf16 v[16:19], v[176:179], a[28:31], v[16:19]\n\t"
      "ds_read_b128 a[4:7], v53 offset:0\n\t"
      "v_mfma_f32_16x16x32_bf16 v[16:19], v[192:195], a[32:35], v[16:19]\n\t"
      "ds_read_b128 a[8:11], v54 offset:0\n\t"
      "v_mfma_f32_16x16x32_bf16 v[16:19], v[208:211], a[36:39], v[16:19]\n\t"
      "ds_read_b128 a[12:15], v55 offset:0\n\t"
      "v_mfma_f32_16x16x32_bf16 v[16:19], v[224:227], a[40:43], v[16:19]\n\t"
      "ds_read_b128 a[16:19], v52 offset:256\n\t"
      "v_mfma_f32_16x16x32_bf16 v[16:19], v[240:243], a[44:47], v[16:19]\n\t"
      "ds_read_b128 a[20:23], v53 offset:256\n\t"
      "v_mfma_f32_16x16x32_bf16 v[20:23], v[164:167], a[24:27], v[20:23]\n\t"
      "s_add_u32 s51, s51, 12288\n\t"
      "s_cmp_eq_u32 s51, 160256\n\t"
      "s_cselect_b32 s51, 25088, s51\n\t"
      "v_mfma_f32_16x16x32_bf16 v[20:23], v[180:183], a[28:31], v[20:23]\n\t"
      "v_mfma_f32_16x16x32_bf16 v[20:23], v[196:199], a[32:35], v[20:23]\n\t"
      "v_mfma_f32_16x16x32_bf16 v[20:23], v[212:215], a[36:39], v[20:23]\n\t"
      "v_mfma_f32_16x16x32_bf16 v[20:23], v[228:231], a[40:43], v[20:23]\n\t"
      "s_waitcnt lgkmcnt(0)\n\t"
      "v_mfma_f32_16x16x32_bf16 v[20:23], v[244:247], a[44:47], v[20:23]\n\t"
      "v_mfma_f32_16x16x32_bf16 v[24:27], v[168:171], a[24:27], v[24:27]\n\t"
      "v_mfma_f32_16x16x32_bf16 v[24:27], v[184:187], a[28:31], v[24:27]\n\t"
      "v_cvt_pk_bf16_f32 v40, v16, v17\n\t"
      "v_cvt_pk_bf16_f32 v41, v18, v19\n\t"
      "v_mfma_f32_16x16x32_bf16 v[24:27], v[200:203], a[32:35], v[24:27]\n\t"
      "ds_write_b64 v0, v[40:41] offset:0\n\t"
      "v_mfma_f32_16x16x32_bf16 v[24:27], v[216:219], a[36:39], v[24:27]\n\t"
      "v_mfma_f32_16x16x32_bf16 v[24:27], v[232:235], a[40:43], v[24:27]\n\t"
      "v_mfma_f32_16x16x32_bf16 v[24:27], v[248:251], a[44:47], v[24:27]\n\t"
      "v_mfma_f32_16x16x32_bf16 v[28:31], v[172:175], a[24:27], v[28:31]\n\t"
      "v_mfma_f32_16x16x32_bf16 v[28:31], v[188:191], a[28:31], v[28:31]\n\t"
      "v_cvt_pk_bf16_f32 v42, v20, v21\n\t"
      "v_cvt_pk_bf16_f32 v43, v22, v23\n\t"
      "v_mfma_f32_16x16x32_bf16 v[28:31], v[204:207], a[32:35], v[28:31]\n\t"
      "ds_write_b64 v0, v[42:43] offset:32\n\t"
      "v_mfma_f32_16x16x32_bf16 v[28:31], v[220:223], a[36:39], v[28:31]\n\t"
      "v_mfma_f32_16x16x32_bf16 v[28:31], v[236:239], a[40:43], v[28:31]\n\t"
      "v_mfma_f32_16x16x32_bf16 v[28:31], v[252:255], a[44:47], v[28:31]\n\t"
      "v_mfma_f32_16x16x32_bf16 v[32:35], a[108:111], a[24:27], v[32:35]\n\t"
      "v_mfma_f32_16x16x32_bf16 v[32:35], a[116:119], a[28:31], v[32:35]\n\t"
      "v_cvt_pk_bf16_f32 v44, v24, v25\n\t"
      "v_cvt_pk_bf16_f32 v45, v26, v27\n\t"
      "v_mfma_f32_16x16x32_bf16 v[32:35], a[124:127], a[32:35], v[32:35]\n\t"
      "ds_write_b64 v0, v[44:45] offset:64\n\t"
      "v_mfma_f32_16x16x32_bf16 v[32:35], a[132:135], a[36:39], v[32:35]\n\t"
      "v_mfma_f32_16x16x32_bf16 v[32:35], a[140:143], a[40:43], v[32:35]\n\t"
      "v_mfma_f32_16x16x32_bf16 v[32:35], a[148:151], a[44:47], v[32:35]\n\t"
      "v_cvt_pk_bf16_f32 v46, v28, v29\n\t"
      "v_cvt_pk_bf16_f32 v47, v30, v31\n\t"
      "v_mfma_f32_16x16x32_bf16 v[36:39], a[112:115], a[24:27], v[36:39]\n\t"
      "ds_write_b64 v0, v[46:47] offset:96\n\t"
      "v_mfma_f32_16x16x32_bf16 v[36:39], a[120:123], a[28:31], v[36:39]\n\t"
      "v_mfma_f32_16x16x32_bf16 v[36:39], a[128:131], a[32:35], v[36:39]\n\t"
      "v_mfma_f32_16x16x32_bf16 v[36:39], a[136:139], a[36:39], v[36:39]\n\t"
      "v_mfma_f32_16x16x32_bf16 v[36:39], a[144:147], a[40:43], v[36:39]\n\t"
      "v_cvt_pk_bf16_f32 v48, v32, v33\n\t"
      "v_cvt_pk_bf16_f32 v49, v34, v35\n\t"
      "v_mfma_f32_16x16x32_bf16 v[36:39], a[152:155], a[44:47], v[36:39]\n\t"
      "ds_write_b64 v0, v[48:49] offset:128\n\t"
      "v_mfma_f32_16x16x32_bf16 v[16:19], v[64:67], a[0:3], 0\n\t"
      "ds_read_b128 a[24:27], v54 offset:256\n\t"
      "v_mfma_f32_16x16x32_bf16 v[16:19], v[80:83], a[4:7], v[16:19]\n\t"
      "ds_read_b128 a[28:31], v55 offset:256\n\t"
      "v_mfma_f32_16x16x32_bf16 v[16:19], v[96:99], a[8:11], v[16:19]\n\t"
      "ds_read_b128 a[32:35], v52 offset:512\n\t"
      "v_mfma_f32_16x16x32_bf16 v[16:19], v[112:115], a[12:15], v[16:19]\n\t"
      "ds_read_b128 a[36:39], v53 offset:512\n\t"
      "v_mfma_f32_16x16x32_bf16 v[16:19], v[128:131], a[16:19], v[16:19]\n\t"
      "ds_read_b128 a[40:43], v54 offset:512\n\t"
      "v_mfma_f32_16x16x32_bf16 v[16:19], v[144:147], a[20:23], v[16:19]\n\t"
      "ds_read_b128 a[44:47], v55 offset:512\n\t"
      "v_mfma_f32_16x16x32_bf16 v[20:23], v[68:71], a[0:3], 0\n\t"
      "v_cvt_pk_bf16_f32 v50, v36, v37\n\t"
      "v_cvt_pk_bf16_f32 v51, v38, v39\n\t"
      "v_mfma_f32_16x16x32_bf16 v[20:23], v[84:87], a[4:7], v[20:23]\n\t"
      "ds_write_b64 v0, v[50:51] offset:160\n\t"
      "v_mfma_f32_16x16x32_bf16 v[20:23], v[100:103], a[8:11], v[20:23]\n\t"
      "v_add_u32 v56, s51, v15\n\t"
      "v_xor_b32 v57, 64, v56\n\t"
      "v_mfma_f32_16x16x32_bf16 v[20:23], v[116:119], a[12:15], v[20:23]\n\t"
      "v_xor_b32 v58, 128, v56\n\t"
      "v_xor_b32 v59, 192, v56\n\t"
      "v_mfma_f32_16x16x32_bf16 v[20:23], v[132:135], a[16:19], v[20:23]\n\t"
      "buffer_store_dwordx4 a[188:191], v12, s[44:47], s49 offen sc1\n\t"
      "v_mfma_f32_16x16x32_bf16 v[20:23], v[148:151], a[20:23], v[20:23]\n\t"
      "v_mfma_f32_16x16x32_bf16 v[24:27], v[72:75], a[0:3], 0\n\t"
      "v_mfma_f32_16x16x32_bf16 v[24:27], v[88:91], a[4:7], v[24:27]\n\t"
      "v_mfma_f32_16x16x32_bf16 v[24:27], v[104:107], a[8:11], v[24:27]\n\t"
      "s_waitcnt vmcnt(7)\n\t"
      "s_waitcnt lgkmcnt(0)\n\t"
      "s_barrier\n\t"
      "v_mfma_f32_16x16x32_bf16 v[24:27], v[120:123], a[12:15], v[24:27]\n\t"
      "v_mfma_f32_16x16x32_bf16 v[24:27], v[136:139], a[16:19], v[24:27]\n\t"
      "v_mfma_f32_16x16x32_bf16 v[24:27], v[152:155], a[20:23], v[24:27]\n\t"
      "v_mfma_f32_16x16x32_bf16 v[28:31], v[76:79], a[0:3], 0\n\t"
      "ds_read_b128 a[200:203], v8 offset:0\n\t"
      "v_mfma_f32_16x16x32_bf16 v[28:31], v[92:95], a[4:7], v[28:31]\n\t"
      "ds_read_b128 a[204:207], v60 offset:1024\n\t"
      "v_mfma_f32_16x16x32_bf16 v[28:31], v[108:111], a[8:11], v[28:31]\n\t"
      "ds_read_b128 a[208:211], v61 offset:2048\n\t"
      "v_mfma_f32_16x16x32_bf16 v[28:31], v[124:127], a[12:15], v[28:31]\n\t"
      "v_mfma_f32_16x16x32_bf16 v[28:31], v[140:143], a[16:19], v[28:31]\n\t"
      "buffer_store_dwordx4 a[192:195], v13, s[44:47], s49 offen offset:1024 sc1\n\t"
      "v_mfma_f32_16x16x32_bf16 v[28:31], v[156:159], a[20:23], v[28:31]\n\t"
      "v_mfma_f32_16x16x32_bf16 v[32:35], a[60:63], a[0:3], 0\n\t"
      "v_mfma_f32_16x16x32_bf16 v[32:35], a[68:71], a[4:7], v[32:35]\n\t"
      "v_mfma_f32_16x16x32_bf16 v[32:35], a[76:79], a[8:11], v[32:35]\n\t"
      "v_mfma_f32_16x16x32_bf16 v[32:35], a[84:87], a[12:15], v[32:35]\n\t"
      "v_mfma_f32_16x16x32_bf16 v[32:35], a[92:95], a[16:19], v[32:35]\n\t"
      "v_mfma_f32_16x16x32_bf16 v[32:35], a[100:103], a[20:23], v[32:35]\n\t"
      "v_mfma_f32_16x16x32_bf16 v[36:39], a[64:67], a[0:3], 0\n\t"
      "buffer_store_dwordx4 a[196:199], v14, s[44:47], s49 offen offset:2048 sc1\n\t"
      "v_mfma_f32_16x16x32_bf16 v[36:39], a[72:75], a[4:7], v[36:39]\n\t"
      "v_mfma_f32_16x16x32_bf16 v[36:39], a[80:83], a[8:11], v[36:39]\n\t"
      "v_mfma_f32_16x16x32_bf16 v[36:39], a[88:91], a[12:15], v[36:39]\n\t"
      "v_mfma_f32_16x16x32_bf16 v[36:39], a[96:99], a[16:19], v[36:39]\n\t"
      "v_mfma_f32_16x16x32_bf16 v[36:39], a[104:107], a[20:23], v[36:39]\n\t"
      "v_mfma_f32_16x16x32_bf16 v[16:19], v[160:163], a[24:27], v[16:19]\n\t"
      "ds_read_b128 a[0:3], v56 offset:0\n\t"
      "v_mfma_f32_16x16x32_bf16 v[16:19], v[176:179], a[28:31], v[16:19]\n\t"
      "ds_read_b128 a[4:7], v57 offset:0\n\t"
      "v_mfma_f32_16x16x32_bf16 v[16:19], v[192:195], a[32:35], v[16:19]\n\t"
      "ds_read_b128 a[8:11], v58 offset:0\n\t"
      "v_mfma_f32_16x16x32_bf16 v[16:19], v[208:211], a[36:39], v[16:19]\n\t"
      "ds_read_b128 a[12:15], v59 offset:0\n\t"
      "v_mfma_f32_16x16x32_bf16 v[16:19], v[224:227], a[40:43], v[16:19]\n\t"
      "ds_read_b128 a[16:19], v56 offset:256\n\t"
      "v_mfma_f32_16x16x32_bf16 v[16:19], v[240:243], a[44:47], v[16:19]\n\t"
      "ds_read_b128 a[20:23], v57 offset:256\n\t"
      "v_mfma_f32_16x16x32_bf16 v[20:23], v[164:167], a[24:27], v[20:23]\n\t"
      "s_add_u32 s51, s51, 12288\n\t"
      "s_cmp_eq_u32 s51, 160256\n\t"
      "s_cselect_b32 s51, 25088, s51\n\t"
      "v_mfma_f32_16x16x32_bf16 v[20:23], v[180:183], a[28:31], v[20:23]\n\t"
      "s_waitcnt lgkmcnt(8)\n\t"
      "v_mfma_f32_16x16x32_bf16 v[20:23], v[196:199], a[32:35], v[20:23]\n\t"
      "buffer_store_dwordx4 a[200:203], v9, s[44:47], s84 offen sc1\n\t"
      "v_mfma_f32_16x16x32_bf16 v[20:23], v[212:215], a[36:39], v[20:23]\n\t"
      "v_mfma_f32_16x16x32_bf16 v[20:23], v[228:231], a[40:43], v[20:23]\n\t"
      "s_waitcnt lgkmcnt(0)\n\t"
      "v_mfma_f32_16x16x32_bf16 v[20:23], v[244:247], a[44:47], v[20:23]\n\t"
      "v_mfma_f32_16x16x32_bf16 v[24:27], v[168:171], a[24:27], v[24:27]\n\t"
      "v_mfma_f32_16x16x32_bf16 v[24:27], v[184:187], a[28:31], v[24:27]\n\t"
      "v_cvt_pk_bf16_f32 v40, v16, v17\n\t"
      "v_cvt_pk_bf16_f32 v41, v18, v19\n\t"
      "v_mfma_f32_16x16x32_bf16 v[24:27], v[200:203], a[32:35], v[24:27]\n\t"
      "ds_write_b64 v0, v[40:41] offset:12544\n\t"
      "v_mfma_f32_16x16x32_bf16 v[24:27], v[216:219], a[36:39], v[24:27]\n\t"
      "v_mfma_f32_16x16x32_bf16 v[24:27], v[232:235], a[40:43], v[24:27]\n\t"
      "buffer_store_dwordx4 a[204:207], v10, s[44:47], s84 offen offset:1024 sc1\n\t"
      "v_mfma_f32_16x16x32_bf16 v[24:27], v[248:251], a[44:47], v[24:27]\n\t"
      "v_mfma_f32_16x16x32_bf16 v[28:31], v[172:175], a[24:27], v[28:31]\n\t"
      "v_mfma_f32_16x16x32_bf16 v[28:31], v[188:191], a[28:31], v[28:31]\n\t"
      "v_cvt_pk_bf16_f32 v42, v20, v21\n\t"
      "v_cvt_pk_bf16_f32 v43, v22, v23\n\t"
      "v_mfma_f32_16x16x32_bf16 v[28:31], v[204:207], a[32:35], v[28:31]\n\t"
      "ds_write_b64 v0, v[42:43] offset:12576\n\t"
      "v_mfma_f32_16x16x32_bf16 v[28:31], v[220:223], a[36:39], v[28:31]\n\t"
      "v_mfma_f32_16x16x32_bf16 v[28:31], v[236:239], a[40:43], v[28:31]\n\t"
      "buffer_store_dwordx4 a[208:211], v11, s[44:47], s84 offen offset:2048 sc1\n\t"
      "v_mfma_f32_16x16x32_bf16 v[28:31], v[252:255], a[44:47], v[28:31]\n\t"
      "v_mfma_f32_16x16x32_bf16 v[32:35], a[108:111], a[24:27], v[32:35]\n\t"
      "v_mfma_f32_16x16x32_bf16 v[32:35], a[116:119], a[28:31], v[32:35]\n\t"
      "v_cvt_pk_bf16_f32 v44, v24, v25\n\t"
      "v_cvt_pk_bf16_f32 v45, v26, v27\n\t"
      "v_mfma_f32_16x16x32_bf16 v[32:35], a[124:127], a[32:35], v[32:35]\n\t"
      "ds_write_b64 v0, v[44:45] offset:12608\n\t"
      "v_mfma_f32_16x16x32_bf16 v[32:35], a[132:135], a[36:39], v[32:35]\n\t"
      "v_mfma_f32_16x16x32_bf16 v[32:35], a[140:143], a[40:43], v[32:35]\n\t"
      "v_mfma_f32_16x16x32_bf16 v[32:35], a[148:151], a[44:47], v[32:35]\n\t"
      "v_cvt_pk_bf16_f32 v46, v28, v29\n\t"
      "v_cvt_pk_bf16_f32 v47, v30, v31\n\t"
      "v_mfma_f32_16x16x32_bf16 v[36:39], a[112:115], a[24:27], v[36:39]\n\t"
      "ds_write_b64 v0, v[46:47] offset:12640\n\t"
      "v_mfma_f32_16x16x32_bf16 v[36:39], a[120:123], a[28:31], v[36:39]\n\t"
      "v_mfma_f32_16x16x32_bf16 v[36:39], a[128:131], a[32:35], v[36:39]\n\t"
      "v_mfma_f32_16x16x32_bf16 v[36:39], a[136:139], a[36:39], v[36:39]\n\t"
      "v_mfma_f32_16x16x32_bf16 v[36:39], a[144:147], a[40:43], v[36:39]\n\t"
      "v_cvt_pk_bf16_f32 v48, v32, v33\n\t"
      "v_cvt_pk_bf16_f32 v49, v34, v35\n\t"
      "v_mfma_f32_16x16x32_bf16 v[36:39], a[152:155], a[44:47], v[36:39]\n\t"
      "ds_write_b64 v0, v[48:49] offset:12672\n\t"
      "v_mfma_f32_16x16x32_bf16 v[16:19], v[64:67], a[0:3], 0\n\t"
      "ds_read_b128 a[24:27], v58 offset:256\n\t"
      "v_mfma_f32_16x16x32_bf16 v[16:19], v[80:83], a[4:7], v[16:19]\n\t"
      "ds_read_b128 a[28:31], v59 offset:256\n\t"
      "v_mfma_f32_16x16x32_bf16 v[16:19], v[96:99], a[8:11], v[16:19]\n\t"
      "ds_read_b128 a[32:35], v56 offset:512\n\t"
      "v_mfma_f32_16x16x32_bf16 v[16:19], v[112:115], a[12:15], v[16:19]\n\t"
      "ds_read_b128 a[36:39], v57 offset:512\n\t"
      "v_mfma_f32_16x16x32_bf16 v[16:19], v[128:131], a[16:19], v[16:19]\n\t"
      "ds_read_b128 a[40:43], v58 offset:512\n\t"
      "v_mfma_f32_16x16x32_bf16 v[16:19], v[144:147], a[20:23], v[16:19]\n\t"
      "ds_read_b128 a[44:47], v59 offset:512\n\t"
      "v_mfma_f32_16x16x32_bf16 v[20:23], v[68:71], a[0:3], 0\n\t"
      "v_cvt_pk_bf16_f32 v50, v36, v37\n\t"
      "v_cvt_pk_bf16_f32 v51, v38, v39\n\t"
      "v_mfma_f32_16x16x32_bf16 v[20:23], v[84:87], a[4:7], v[20:23]\n\t"
      "ds_write_b64 v0, v[50:51] offset:12704\n\t"
      "v_mfma_f32_16x16x32_bf16 v[20:23], v[100:103], a[8:11], v[20:23]\n\t"
      "v_add_u32 v52, s51, v15\n\t"
      "v_xor_b32 v53, 64, v52\n\t"
      "v_mfma_f32_16x16x32_bf16 v[20:23], v[116:119], a[12:15], v[20:23]\n\t"
      "v_xor_b32 v54, 128, v52\n\t"
      "v_xor_b32 v55, 192, v52\n\t"
      "v_mfma_f32_16x16x32_bf16 v[20:23], v[132:135], a[16:19], v[20:23]\n\t"
      "v_mfma_f32_16x16x32_bf16 v[20:23], v[148:151], a[20:23], v[20:23]\n\t"
      "v_mfma_f32_16x16x32_bf16 v[24:27], v[72:75], a[0:3], 0\n\t"
      "v_mfma_f32_16x16x32_bf16 v[24:27], v[88:91], a[4:7], v[24:27]\n\t"
      "v_mfma_f32_16x16x32_bf16 v[24:27], v[104:107], a[8:11], v[24:27]\n\t"
      "s_waitcnt vmcnt(6)\n\t"
      "s_waitcnt lgkmcnt(0)\n\t"
      "s_barrier\n\t"
      "v_mfma_f32_16x16x32_bf16 v[24:27], v[120:123], a[12:15], v[24:27]\n\t"
      "v_mfma_f32_16x16x32_bf16 v[24:27], v[136:139], a[16:19], v[24:27]\n\t"
      "v_mfma_f32_16x16x32_bf16 v[24:27], v[152:155], a[20:23], v[24:27]\n\t"
      "v_mfma_f32_16x16x32_bf16 v[28:31], v[76:79], a[0:3], 0\n\t"
      "ds_read_b128 a[212:215], v8 offset:12544\n\t"
      "v_mfma_f32_16x16x32_bf16 v[28:31], v[92:95], a[4:7], v[28:31]\n\t"
      "ds_read_b128 a[216:219], v60 offset:13568\n\t"
      "v_mfma_f32_16x16x32_bf16 v[28:31], v[108:111], a[8:11], v[28:31]\n\t"
      "ds_read_b128 a[220:223], v61 offset:14592\n\t"
      "v_mfma_f32_16x16x32_bf16 v[28:31], v[124:127], a[12:15], v[28:31]\n\t"
      "v_mfma_f32_16x16x32_bf16 v[28:31], v[140:143], a[16:19], v[28:31]\n\t"
      "v_mfma_f32_16x16x32_bf16 v[28:31], v[156:159], a[20:23], v[28:31]\n\t"
      "v_mfma_f32_16x16x32_bf16 v[32:35], a[60:63], a[0:3], 0\n\t"
      "v_mfma_f32_16x16x32_bf16 v[32:35], a[68:71], a[4:7], v[32:35]\n\t"
      "s_waitcnt lgkmcnt(2)\n\t"
      "v_mfma_f32_16x16x32_bf16 v[32:35], a[76:79], a[8:11], v[32:35]\n\t"
      "buffer_store_dwordx4 a[212:215], v12, s[44:47], s84 offen sc1\n\t"
      "v_mfma_f32_16x16x32_bf16 v[32:35], a[84:87], a[12:15], v[32:35]\n\t"
      "v_mfma_f32_16x16x32_bf16 v[32:35], a[92:95], a[16:19], v[32:35]\n\t"
      "v_mfma_f32_16x16x32_bf16 v[32:35], a[100:103], a[20:23], v[32:35]\n\t"
      "v_mfma_f32_16x16x32_bf16 v[36:39], a[64:67], a[0:3], 0\n\t"
      "v_mfma_f32_16x16x32_bf16 v[36:39], a[72:75], a[4:7], v[36:39]\n\t"
      "v_mfma_f32_16x16x32_bf16 v[36:39], a[80:83], a[8:11], v[36:39]\n\t"
      "v_mfma_f32_16x16x32_bf16 v[36:39], a[88:91], a[12:15], v[36:39]\n\t"
      "s_waitcnt lgkmcnt(1)\n\t"
      "v_mfma_f32_16x16x32_bf16 v[36:39], a[96:99], a[16:19], v[36:39]\n\t"
      "buffer_store_dwordx4 a[216:219], v13, s[44:47], s84 offen offset:1024 sc1\n\t"
      "v_mfma_f32_16x16x32_bf16 v[36:39], a[104:107], a[20:23], v[36:39]\n\t"
      "v_mfma_f32_16x16x32_bf16 v[16:19], v[160:163], a[24:27], v[16:19]\n\t"
      "ds_read_b128 a[0:3], v52 offset:0\n\t"
      "v_mfma_f32_16x16x32_bf16 v[16:19], v[176:179], a[28:31], v[16:19]\n\t"
      "ds_read_b128 a[4:7], v53 offset:0\n\t"
      "v_mfma_f32_16x16x32_bf16 v[16:19], v[192:195], a[32:35], v[16:19]\n\t"
      "ds_read_b128 a[8:11], v54 offset:0\n\t"
      "v_mfma_f32_16x16x32_bf16 v[16:19], v[208:211], a[36:39], v[16:19]\n\t"
      "ds_read_b128 a[12:15], v55 offset:0\n\t"
      "v_mfma_f32_16x16x32_bf16 v[16:19], v[224:227], a[40:43], v[16:19]\n\t"
      "ds_read_b128 a[16:19], v52 offset:256\n\t"
      "v_mfma_f32_16x16x32_bf16 v[16:19], v[240:243], a[44:47], v[16:19]\n\t"
      "ds_read_b128 a[20:23], v53 offset:256\n\t"
      "v_mfma_f32_16x16x32_bf16 v[20:23], v[164:167], a[24:27], v[20:23]\n\t"
      "s_add_u32 s51, s51, 12288\n\t"
      "s_cmp_eq_u32 s51, 160256\n\t"
      "s_cselect_b32 s51, 25088, s51\n\t"
      "v_mfma_f32_16x16x32_bf16 v[20:23], v[180:183], a[28:31], v[20:23]\n\t"
      "s_waitcnt lgkmcnt(6)\n\t"
      "v_mfma_f32_16x16x32_bf16 v[20:23], v[196:199], a[32:35], v[20:23]\n\t"
      "buffer_store_dwordx4 a[220:223], v14, s[44:47], s84 offen offset:2048 sc1\n\t"
      "v_mfma_f32_16x16x32_bf16 v[20:23], v[212:215], a[36:39], v[20:23]\n\t"
      "v_mfma_f32_16x16x32_bf16 v[20:23], v[228:231], a[40:43], v[20:23]\n\t"
      "s_waitcnt lgkmcnt(0)\n\t"
      "v_mfma_f32_16x16x32_bf16 v[20:23], v[244:247], a[44:47], v[20:23]\n\t"
      "v_mfma_f32_16x16x32_bf16 v[24:27], v[168:171], a[24:27], v[24:27]\n\t"
      "v_mfma_f32_16x16x32_bf16 v[24:27], v[184:187], a[28:31], v[24:27]\n\t"
      "v_cvt_pk_bf16_f32 v40, v16, v17\n\t"
      "v_cvt_pk_bf16_f32 v41, v18, v19\n\t"
      "v_mfma_f32_16x16x32_bf16 v[24:27], v[200:203], a[32:35], v[24:27]\n\t"
      "ds_write_b64 v0, v[40:41] offset:0\n\t"
      "v_mfma_f32_16x16x32_bf16 v[24:27], v[216:219], a[36:39], v[24:27]\n\t"
      "v_mfma_f32_16x16x32_bf16 v[24:27], v[232:235], a[40:43], v[24:27]\n\t"
      "v_mfma_f32_16x16x32_bf16 v[24:27], v[248:251], a[44:47], v[24:27]\n\t"
      "v_mfma_f32_16x16x32_bf16 v[28:31], v[172:175], a[24:27], v[28:31]\n\t"
      "v_mfma_f32_16x16x32_bf16 v[28:31], v[188:191], a[28:31], v[28:31]\n\t"
      "v_cvt_pk_bf16_f32 v42, v20, v21\n\t"
      "v_cvt_pk_bf16_f32 v43, v22, v23\n\t"
      "v_mfma_f32_16x16x32_bf16 v[28:31], v[204:207], a[32:35], v[28:31]\n\t"
      "ds_write_b64 v0, v[42:43] offset:32\n\t"
      "v_mfma_f32_16x16x32_bf16 v[28:31], v[220:223], a[36:39], v[28:31]\n\t"
      "v_mfma_f32_16x16x32_bf16 v[28:31], v[236:239], a[40:43], v[28:31]\n\t"
      "v_mfma_f32_16x16x32_bf16 v[28:31], v[252:255], a[44:47], v[28:31]\n\t"
      "v_mfma_f32_16x16x32_bf16 v[32:35], a[108:111], a[24:27], v[32:35]\n\t"
      "v_mfma_f32_16x16x32_bf16 v[32:35], a[116:119], a[28:31], v[32:35]\n\t"
      "v_cvt_pk_bf16_f32 v44, v24, v25\n\t"
      "v_cvt_pk_bf16_f32 v45, v26, v27\n\t"
      "v_mfma_f32_16x16x32_bf16 v[32:35], a[124:127], a[32:35], v[32:35]\n\t"
      "ds_write_b64 v0, v[44:45] offset:64\n\t"
      "v_mfma_f32_16x16x32_bf16 v[32:35], a[132:135], a[36:39], v[32:35]\n\t"
      "v_mfma_f32_16x16x32_bf16 v[32:35], a[140:143], a[40:43], v[32:35]\n\t"
      "v_mfma_f32_16x16x32_bf16 v[32:35], a[148:151], a[44:47], v[32:35]\n\t"
      "v_cvt_pk_bf16_f32 v46, v28, v29\n\t"
      "v_cvt_pk_bf16_f32 v47, v30, v31\n\t"
      "v_mfma_f32_16x16x32_bf16 v[36:39], a[112:115], a[24:27], v[36:39]\n\t"
      "ds_write_b64 v0, v[46:47] offset:96\n\t"
      "v_mfma_f32_16x16x32_bf16 v[36:39], a[120:123], a[28:31], v[36:39]\n\t"
      "v_mfma_f32_16x16x32_bf16 v[36:39], a[128:131], a[32:35], v[36:39]\n\t"
      "v_mfma_f32_16x16x32_bf16 v[36:39], a[136:139], a[36:39], v[36:39]\n\t"
      "v_mfma_f32_16x16x32_bf16 v[36:39], a[144:147], a[40:43], v[36:39]\n\t"
      "v_cvt_pk_bf16_f32 v48, v32, v33\n\t"
      "v_cvt_pk_bf16_f32 v49, v34, v35\n\t"
      "v_mfma_f32_16x16x32_bf16 v[36:39], a[152:155], a[44:47], v[36:39]\n\t"
      "ds_write_b64 v0, v[48:49] offset:128\n\t"
      "v_mfma_f32_16x16x32_bf16 v[16:19], v[64:67], a[0:3], 0\n\t"
      "ds_read_b128 a[24:27], v54 offset:256\n\t"
      "v_mfma_f32_16x16x32_bf16 v[16:19], v[80:83], a[4:7], v[16:19]\n\t"
      "ds_read_b128 a[28:31], v55 offset:256\n\t"
      "v_mfma_f32_16x16x32_bf16 v[16:19], v[96:99], a[8:11], v[16:19]\n\t"
      "ds_read_b128 a[32:35], v52 offset:512\n\t"
      "v_mfma_f32_16x16x32_bf16 v[16:19], v[112:115], a[12:15], v[16:19]\n\t"
      "ds_read_b128 a[36:39], v53 offset:512\n\t"
      "v_mfma_f32_16x16x32_bf16 v[16:19], v[128:131], a[16:19], v[16:19]\n\t"
      "ds_read_b128 a[40:43], v54 offset:512\n\t"
      "v_mfma_f32_16x16x32_bf16 v[16:19], v[144:147], a[20:23], v[16:19]\n\t"
      "ds_read_b128 a[44:47], v55 offset:512\n\t"
      "v_mfma_f32_16x16x32_bf16 v[20:23], v[68:71], a[0:3], 0\n\t"
      "v_cvt_pk_bf16_f32 v50, v36, v37\n\t"
      "v_cvt_pk_bf16_f32 v51, v38, v39\n\t"
      "v_mfma_f32_16x16x32_bf16 v[20:23], v[84:87], a[4:7], v[20:23]\n\t"
      "ds_write_b64 v0, v[50:51] offset:160\n\t"
      "v_mfma_f32_16x16x32_bf16 v[20:23], v[100:103], a[8:11], v[20:23]\n\t"
      "v_mfma_f32_16x16x32_bf16 v[20:23], v[116:119], a[12:15], v[20:23]\n\t"
      "v_mfma_f32_16x16x32_bf16 v[20:23], v[132:135], a[16:19], v[20:23]\n\t"
      "v_mfma_f32_16x16x32_bf16 v[20:23], v[148:151], a[20:23], v[20:23]\n\t"
      "v_mfma_f32_16x16x32_bf16 v[24:27], v[72:75], a[0:3], 0\n\t"
      "v_mfma_f32_16x16x32_bf16 v[24:27], v[88:91], a[4:7], v[24:27]\n\t"
      "v_mfma_f32_16x16x32_bf16 v[24:27], v[104:107], a[8:11], v[24:27]\n\t"
      "s_waitcnt lgkmcnt(0)\n\t"
      "s_barrier\n\t"
      "v_mfma_f32_16x16x32_bf16 v[24:27], v[120:123], a[12:15], v[24:27]\n\t"
      "v_mfma_f32_16x16x32_bf16 v[24:27], v[136:139], a[16:19], v[24:27]\n\t"
      "v_mfma_f32_16x16x32_bf16 v[24:27], v[152:155], a[20:23], v[24:27]\n\t"
      "v_mfma_f32_16x16x32_bf16 v[28:31], v[76:79], a[0:3], 0\n\t"
      "ds_read_b128 a[224:227], v8 offset:0\n\t"
      "v_mfma_f32_16x16x32_bf16 v[28:31], v[92:95], a[4:7], v[28:31]\n\t"
      "ds_read_b128 a[228:231], v60 offset:1024\n\t"
      "v_mfma_f32_16x16x32_bf16 v[28:31], v[108:111], a[8:11], v[28:31]\n\t"
      "ds_read_b128 a[232:235], v61 offset:2048\n\t"
      "v_mfma_f32_16x16x32_bf16 v[28:31], v[124:127], a[12:15], v[28:31]\n\t"
      "v_mfma_f32_16x16x32_bf16 v[28:31], v[140:143], a[16:19], v[28:31]\n\t"
      "v_mfma_f32_16x16x32_bf16 v[28:31], v[156:159], a[20:23], v[28:31]\n\t"
      "v_mfma_f32_16x16x32_bf16 v[32:35], a[60:63], a[0:3], 0\n\t"
      "v_mfma_f32_16x16x32_bf16 v[32:35], a[68:71], a[4:7], v[32:35]\n\t"
      "s_waitcnt lgkmcnt(2)\n\t"
      "v_mfma_f32_16x16x32_bf16 v[32:35], a[76:79], a[8:11], v[32:35]\n\t"
      "buffer_store_dwordx4 a[224:227], v9, s[44:47], s85 offen sc1\n\t"
      "v_mfma_f32_16x16x32_bf16 v[32:35], a[84:87], a[12:15], v[32:35]\n\t"
      "v_mfma_f32_16x16x32_bf16 v[32:35], a[92:95], a[16:19], v[32:35]\n\t"
      "v_mfma_f32_16x16x32_bf16 v[32:35], a[100:103], a[20:23], v[32:35]\n\t"
      "v_mfma_f32_16x16x32_bf16 v[36:39], a[64:67], a[0:3], 0\n\t"
      "v_mfma_f32_16x16x32_bf16 v[36:39], a[72:75], a[4:7], v[36:39]\n\t"
      "v_mfma_f32_16x16x32_bf16 v[36:39], a[80:83], a[8:11], v[36:39]\n\t"
      "v_mfma_f32_16x16x32_bf16 v[36:39], a[88:91], a[12:15], v[36:39]\n\t"
      "s_waitcnt lgkmcnt(1)\n\t"
      "v_mfma_f32_16x16x32_bf16 v[36:39], a[96:99], a[16:19], v[36:39]\n\t"
      "buffer_store_dwordx4 a[228:231], v10, s[44:47], s85 offen offset:1024 sc1\n\t"
      "v_mfma_f32_16x16x32_bf16 v[36:39], a[104:107], a[20:23], v[36:39]\n\t"
      "v_mfma_f32_16x16x32_bf16 v[16:19], v[160:163], a[24:27], v[16:19]\n\t"
      "v_mfma_f32_16x16x32_bf16 v[16:19], v[176:179], a[28:31], v[16:19]\n\t"
      "v_mfma_f32_16x16x32_bf16 v[16:19], v[192:195], a[32:35], v[16:19]\n\t"
      "v_mfma_f32_16x16x32_bf16 v[16:19], v[208:211], a[36:39], v[16:19]\n\t"
      "v_mfma_f32_16x16x32_bf16 v[16:19], v[224:227], a[40:43], v[16:19]\n\t"
      "v_mfma_f32_16x16x32_bf16 v[16:19], v[240:243], a[44:47], v[16:19]\n\t"
      "v_mfma_f32_16x16x32_bf16 v[20:23], v[164:167], a[24:27], v[20:23]\n\t"
      "v_mfma_f32_16x16x32_bf16 v[20:23], v[180:183], a[28:31], v[20:23]\n\t"
      "s_waitcnt lgkmcnt(0)\n\t"
      "v_mfma_f32_16x16x32_bf16 v[20:23], v[196:199], a[32:35], v[20:23]\n\t"
      "buffer_store_dwordx4 a[232:235], v11, s[44:47], s85 offen offset:2048 sc1\n\t"
      "v_mfma_f32_16x16x32_bf16 v[20:23], v[212:215], a[36:39], v[20:23]\n\t"
      "v_mfma_f32_16x16x32_bf16 v[20:23], v[228:231], a[40:43], v[20:23]\n\t"
      "v_mfma_f32_16x16x32_bf16 v[20:23], v[244:247], a[44:47], v[20:23]\n\t"
      "v_mfma_f32_16x16x32_bf16 v[24:27], v[168:171], a[24:27], v[24:27]\n\t"
      "v_mfma_f32_16x16x32_bf16 v[24:27], v[184:187], a[28:31], v[24:27]\n\t"
      "v_cvt_pk_bf16_f32 v40, v16, v17\n\t"
      "v_cvt_pk_bf16_f32 v41, v18, v19\n\t"
      "v_mfma_f32_16x16x32_bf16 v[24:27], v[200:203], a[32:35], v[24:27]\n\t"
      "ds_write_b64 v0, v[40:41] offset:12544\n\t"
      "v_mfma_f32_16x16x32_bf16 v[24:27], v[216:219], a[36:39], v[24:27]\n\t"
      "v_mfma_f32_16x16x32_bf16 v[24:27], v[232:235], a[40:43], v[24:27]\n\t"
      "v_mfma_f32_16x16x32_bf16 v[24:27], v[248:251], a[44:47], v[24:27]\n\t"
      "v_mfma_f32_16x16x32_bf16 v[28:31], v[172:175], a[24:27], v[28:31]\n\t"
      "v_mfma_f32_16x16x32_bf16 v[28:31], v[188:191], a[28:31], v[28:31]\n\t"
      "v_cvt_pk_bf16_f32 v42, v20, v21\n\t"
      "v_cvt_pk_bf16_f32 v43, v22, v23\n\t"
      "v_mfma_f32_16x16x32_bf16 v[28:31], v[204:207], a[32:35], v[28:31]\n\t"
      "ds_write_b64 v0, v[42:43] offset:12576\n\t"
      "v_mfma_f32_16x16x32_bf16 v[28:31], v[220:223], a[36:39], v[28:31]\n\t"
      "v_mfma_f32_16x16x32_bf16 v[28:31], v[236:239], a[40:43], v[28:31]\n\t"
      "v_mfma_f32_16x16x32_bf16 v[28:31], v[252:255], a[44:47], v[28:31]\n\t"
      "v_mfma_f32_16x16x32_bf16 v[32:35], a[108:111], a[24:27], v[32:35]\n\t"
      "v_mfma_f32_16x16x32_bf16 v[32:35], a[116:119], a[28:31], v[32:35]\n\t"
      "v_cvt_pk_bf16_f32 v44, v24, v25\n\t"
      "v_cvt_pk_bf16_f32 v45, v26, v27\n\t"
      "v_mfma_f32_16x16x32_bf16 v[32:35], a[124:127], a[32:35], v[32:35]\n\t"
      "ds_write_b64 v0, v[44:45] offset:12608\n\t"
      "v_mfma_f32_16x16x32_bf16 v[32:35], a[132:135], a[36:39], v[32:35]\n\t"
      "v_mfma_f32_16x16x32_bf16 v[32:35], a[140:143], a[40:43], v[32:35]\n\t"
      "v_mfma_f32_16x16x32_bf16 v[32:35], a[148:151], a[44:47], v[32:35]\n\t"
      "v_cvt_pk_bf16_f32 v46, v28, v29\n\t"
      "v_cvt_pk_bf16_f32 v47, v30, v31\n\t"
      "v_mfma_f32_16x16x32_bf16 v[36:39], a[112:115], a[24:27], v[36:39]\n\t"
      "ds_write_b64 v0, v[46:47] offset:12640\n\t"
      "v_mfma_f32_16x16x32_bf16 v[36:39], a[120:123], a[28:31], v[36:39]\n\t"
      "v_mfma_f32_16x16x32_bf16 v[36:39], a[128:131], a[32:35], v[36:39]\n\t"
      "v_mfma_f32_16x16x32_bf16 v[36:39], a[136:139], a[36:39], v[36:39]\n\t"
      "v_mfma_f32_16x16x32_bf16 v[36:39], a[144:147], a[40:43], v[36:39]\n\t"
      "v_cvt_pk_bf16_f32 v48, v32, v33\n\t"
      "v_cvt_pk_bf16_f32 v49, v34, v35\n\t"
      "v_mfma_f32_16x16x32_bf16 v[36:39], a[152:155], a[44:47], v[36:39]\n\t"
      "ds_write_b64 v0, v[48:49] offset:12672\n\t"
      ".Ltail_%=:\n\t"
      "s_nop 6\n\t"
      "v_cvt_pk_bf16_f32 v50, v36, v37\n\t"
      "v_cvt_pk_bf16_f32 v51, v38, v39\n\t"
      "ds_write_b64 v0, v[50:51] offset:12704\n\t"
      "s_cmp_eq_u32 s85, 113221632\n\t"
      "s_cbranch_scc1 .Lwrapo_%=\n\t"
      ".Lwrappedo_%=:\n\t"
      "s_waitcnt lgkmcnt(0)\n\t"
      "s_barrier\n\t"
      "ds_read_b128 a[48:51], v8 offset:12544\n\t"
      "ds_read_b128 a[52:55], v60 offset:13568\n\t"
      "ds_read_b128 a[56:59], v61 offset:14592\n\t"
      "s_waitcnt lgkmcnt(2)\n\t"
      "buffer_store_dwordx4 a[48:51], v12, s[44:47], s85 offen sc1\n\t"
      "s_waitcnt lgkmcnt(1)\n\t"
      "buffer_store_dwordx4 a[52:55], v13, s[44:47], s85 offen offset:1024 sc1\n\t"
      "s_waitcnt lgkmcnt(0)\n\t"
      "buffer_store_dwordx4 a[56:59], v14, s[44:47], s85 offen offset:2048 sc1\n\t"
      "s_cmp_lt_i32 s91, 0\n\t"
      "s_cselect_b32 s30, -1, 0\n\t"
      "s_xor_b32 s16, s91, 0x3ffff\n\t"
      "s_and_b32 s16, s16, s30\n\t"
      "s_cbranch_scc0 .Lend_%=\n\t"
      "s_waitcnt vmcnt(0) lgkmcnt(0)\n\t"
      "s_barrier\n\t"
      "s_lshl1_add_u32 s30, s14, 24576\n\t"
      "s_sub_u32 s15, s30, s15\n\t"
      "s_branch .Lstart_%=\n\t"
      ".Lwrap3_%=:\n\t"
      "v_add_u32 v5, s48, v5\n\t"
      "v_subrev_u32 v62, 113246208, v5\n\t"
      "v_min_u32 v5, v5, v62\n\t"
      "v_add_u32 v6, s48, v6\n\t"
      "v_subrev_u32 v62, 113246208, v6\n\t"
      "v_min_u32 v6, v6, v62\n\t"
      "v_add_u32 v7, s48, v7\n\t"
      "v_subrev_u32 v62, 113246208, v7\n\t"
      "v_min_u32 v7, v7, v62\n\t"
      "s_mov_b32 s48, 0\n\t"
      "buffer_load_dwordx4 v5, s[40:43], s48 offen nt lds\n\t"
      "buffer_load_dwordx4 v6, s[40:43], s48 offen offset:1024 nt lds\n\t"
      "buffer_load_dwordx4 v7, s[40:43], s48 offen offset:2048 nt lds\n\t"
      "s_branch .Lwrapped3_%=\n\t"
      ".Lwrapo_%=:\n\t"
      "v_add_u32 v12, s85, v12\n\t"
      "v_subrev_u32 v62, 113246208, v12\n\t"
      "v_min_u32 v12, v12, v62\n\t"
      "v_add_u32 v13, s85, v13\n\t"
      "v_subrev_u32 v62, 113246208, v13\n\t"
      "v_min_u32 v13, v13, v62\n\t"
      "v_add_u32 v14, s85, v14\n\t"
      "v_subrev_u32 v62, 113246208, v14\n\t"
      "v_min_u32 v14, v14, v62\n\t"
      "s_mov_b32 s85, 0\n\t"
      "s_branch .Lwrappedo_%=\n\t"
      ".Lwait_%=:\n\t"
      "s_sub_u32 s52, s52, 1\n\t"
      "s_cbranch_scc1 .Lwait2_%=\n\t"
      "s_add_u32 s90, s90, 0x2000000\n\t"
      "v_mov_b32 v54, s90\n\t"
      "v_mov_b32 v55, s19\n\t"
      "global_store_dword v55, v54, s[12:13] sc1\n\t"
      "s_sleep 16\n\t"
      "global_load_dword v40, v62, s[12:13] sc1\n\t"
      "s_waitcnt vmcnt(0)\n\t"
      "s_branch .Lcheck_%=\n\t"
      ".Lwait2_%=:\n\t"
      "s_brev_b32 s91, 1\n\t"
      "s_branch .Lwaited_%=\n\t"
      ".Lspinf_%=:\n\t"
      "s_memrealtime s[36:37]\n\t"
      "s_waitcnt lgkmcnt(0)\n\t"
      "s_sub_u32 s53, s36, s38\n\t"
      "s_cmp_lt_i32 s53, 0\n\t"
      "s_cbranch_scc1 .Lspinf_%=\n\t"
      "s_branch .Lspunf_%=\n\t"
      ".Lspin0_%=:\n\t"
      "s_memrealtime s[36:37]\n\t"
      "s_waitcnt lgkmcnt(0)\n\t"
      "s_sub_u32 s53, s36, s38\n\t"
      "s_cmp_lt_i32 s53, 0\n\t"
      "s_cbranch_scc1 .Lspin0_%=\n\t"
      "s_branch .Lspun0_%=\n\t"
      ".Lspin1_%=:\n\t"
      "s_memrealtime s[36:37]\n\t"
      "s_waitcnt lgkmcnt(0)\n\t"
      "s_sub_u32 s53, s36, s38\n\t"
      "s_cmp_lt_i32 s53, 0\n\t"
      "s_cbranch_scc1 .Lspin1_%=\n\t"
      "s_branch .Lspun1_%=\n\t"
      ".Lend_%=:\n\t"
      : [tid] "+v"(tid)
      : [karg] "s"(__builtin_amdgcn_kernarg_segment_ptr()), [blk] "s"(blockIdx.x), [lds] "s"((unsigned)(size_t)lds)
      : "v1", "v2", "v3", "v4", "v5", "v6", "v7", "v8", "v9", "v10", "v11", "v12", "v13", "v14", "v15", "v16", "v17", "v18", "v19", "v20", "v21", "v22", "v23", "v24", "v25", "v26", "v27", "v28", "v29", "v30", "v31", "v32", "v33", "v34", "v35", "v36", "v37", "v38", "v39", "v40", "v41", "v42", "v43", "v44", "v45", "v46", "v47", "v48", "v49", "v50", "v51", "v52", "v53", "v54", "v55", "v56", "v57", "v58", "v59", "v60", "v61", "v62", "v63", "v64", "v65", "v66", "v67", "v68", "v69", "v70", "v71", "v72", "v73", "v74", "v75", "v76", "v77", "v78", "v79", "v80", "v81", "v82", "v83", "v84", "v85", "v86", "v87", "v88", "v89", "v90", "v91", "v92", "v93", "v94", "v95", "v96", "v97", "v98", "v99", "v100", "v101", "v102", "v103", "v104", "v105", "v106", "v107", "v108", "v109", "v110", "v111", "v112", "v113", "v114", "v115", "v116", "v117", "v118", "v119", "v120", "v121", "v122", "v123", "v124", "v125", "v126", "v127", "v128", "v129", "v130", "v131", "v132", "v133", "v134", "v135", "v136", "v137", "v138", "v139", "v140", "v141", "v142", "v143", "v144", "v145", "v146", "v147", "v148", "v149", "v150", "v151", "v152", "v153", "v154", "v155", "v156", "v157", "v158", "v159", "v160", "v161", "v162", "v163", "v164", "v165", "v166", "v167", "v168", "v169", "v170", "v171", "v172", "v173", "v174", "v175", "v176", "v177", "v178", "v179", "v180", "v181", "v182", "v183", "v184", "v185", "v186", "v187", "v188", "v189", "v190", "v191", "v192", "v193", "v194", "v195", "v196", "v197", "v198", "v199", "v200", "v201", "v202", "v203", "v204", "v205", "v206", "v207", "v208", "v209", "v210", "v211", "v212", "v213", "v214", "v215", "v216", "v217", "v218", "v219", "v220", "v221", "v222", "v223", "v224", "v225", "v226", "v227", "v228", "v229", "v230", "v231", "v232", "v233", "v234", "v235", "v236", "v237", "v238", "v239", "v240", "v241", "v242", "v243", "v244", "v245", "v246", "v247", "v248", "v249", "v250", "v251", "v252", "v253", "v254", "v255", "a0", "a1", "a2", "a3", "a4", "a5", "a6", "a7", "a8", "a9", "a10", "a11", "a12", "a13", "a14", "a15", "a16", "a17", "a18", "a19", "a20", "a21", "a22", "a23", "a24", "a25", "a26", "a27", "a28", "a29", "a30", "a31", "a32", "a33", "a34", "a35", "a36", "a37", "a38", "a39", "a40", "a41", "a42", "a43", "a44", "a45", "a46", "a47", "a48", "a49", "a50", "a51", "a52", "a53", "a54", "a55", "a56", "a57", "a58", "a59", "a60", "a61", "a62", "a63", "a64", "a65", "a66", "a67", "a68", "a69", "a70", "a71", "a72", "a73", "a74", "a75", "a76", "a77", "a78", "a79", "a80", "a81", "a82", "a83", "a84", "a85", "a86", "a87", "a88", "a89", "a90", "a91", "a92", "a93", "a94", "a95", "a96", "a97", "a98", "a99", "a100", "a101", "a102", "a103", "a104", "a105", "a106", "a107", "a108", "a109", "a110", "a111", "a112", "a113", "a114", "a115", "a116", "a117", "a118", "a119", "a120", "a121", "a122", "a123", "a124", "a125", "a126", "a127", "a128", "a129", "a130", "a131", "a132", "a133", "a134", "a135", "a136", "a137", "a138", "a139", "a140", "a141", "a142", "a143", "a144", "a145", "a146", "a147", "a148", "a149", "a150", "a151", "a152", "a153", "a154", "a155", "a156", "a157", "a158", "a159", "a160", "a161", "a162", "a163", "a164", "a165", "a166", "a167", "a168", "a169", "a170", "a171", "a172", "a173", "a174", "a175", "a176", "a177", "a178", "a179", "a180", "a181", "a182", "a183", "a184", "a185", "a186", "a187", "a188", "a189", "a190", "a191", "a192", "a193", "a194", "a195", "a196", "a197", "a198", "a199", "a200", "a201", "a202", "a203", "a204", "a205", "a206", "a207", "a208", "a209", "a210", "a211", "a212", "a213", "a214", "a215", "a216", "a217", "a218", "a219", "a220", "a221", "a222", "a223", "a224", "a225", "a226", "a227", "a228", "a229", "a230", "a231", "a232", "a233", "a234", "a235", "a236", "a237", "a238", "a239", "a240", "a241", "a242", "a243", "a244", "a245", "a246", "a247", "a248", "a249", "a250", "a251", "a252", "a253", "a254", "a255", "s4", "s5", "s6", "s7", "s8", "s9", "s10", "s11", "s12", "s13", "s14", "s15", "s16", "s17", "s18", "s19", "s20", "s21", "s22", "s23", "s24", "s25", "s26", "s27", "s28", "s29", "s30", "s31", "s32", "s33", "s34", "s35", "s36", "s37", "s38", "s39", "s40", "s41", "s42", "s43", "s44", "s45", "s46", "s47", "s48", "s49", "s50", "s51", "s52", "s53", "s54", "s55", "s56", "s57", "s58", "s59", "s60", "s61", "s62", "s63", "s64", "s65", "s66", "s67", "s68", "s69", "s70", "s71", "s72", "s73", "s74", "s75", "s76", "s77", "s78", "s79", "s80", "s81", "s82", "s83", "s84", "s85", "s86", "s87", "s88", "s89", "s90", "s91", "vcc", "scc", "memory");
}

__global__ __launch_bounds__(384) void dwsep_plain(const unsigned short* __restrict__ x, const unsigned short* __restrict__ dw,
                                                   const unsigned short* __restrict__ pw, unsigned short* __restrict__ out) {
  __shared__ float xs[384];
  const unsigned n = threadIdx.x;
  const size_t row = (size_t)blockIdx.x * 384u;
  xs[n] = __uint_as_float((unsigned)x[row + n] << 16) * __uint_as_float((unsigned)dw[n] << 16);
  __syncthreads();
  float acc = 0.f;
  for (int k = 0; k < 384; ++k) acc += xs[k] * __uint_as_float((unsigned)pw[k * 384 + n] << 16);
  const __bf16 o = (__bf16)acc;
  out[row + n] = __builtin_bit_cast(unsigned short, o);
}

extern "C" void kernel_launch(void* const* d_in, const int* in_sizes, int n_in, void* d_out, int out_size,
                              void* d_ws, size_t ws_size, hipStream_t stream) {
  const unsigned short* x = (const unsigned short*)d_in[0];
  const unsigned short* dw = (const unsigned short*)d_in[1];
  const unsigned short* pw = (const unsigned short*)d_in[2];
  unsigned short* out = (unsigned short*)d_out;
  if (in_sizes[0] == 56623104) dwsep_kernel<<<NWG, 256, 0, stream>>>(x, dw, pw, out, (unsigned*)d_ws);
  else dwsep_plain<<<in_sizes[0] / 384, 384, 0, stream>>>(x, dw, pw, out);
}
```

```cpp
#include <hip/hip_runtime.h>

constexpr unsigned ROWB = 768, TILEB = 12288, NWG = 256;

__global__ __launch_bounds__(256, 1) void dwsep_kernel(const unsigned short* __restrict__ x,
                                                       const unsigned short* __restrict__ dw,
                                                       const unsigned short* __restrict__ pw,
                                                       unsigned short* __restrict__ out, unsigned* ws) {
  __shared__ __attribute__((aligned(256))) char lds[163392];
  unsigned tid = threadIdx.x;
  asm volatile(
      "s_memtime s[32:33]\n\t"
      "s_load_dwordx8 s[4:11], %[karg], 0\n\t"
      "s_load_dwordx2 s[12:13], %[karg], 0x20\n\t"
      "v_readfirstlane_b32 s30, %[tid]\n\t"
      "s_lshr_b32 s31, %[blk], 1\n\t"
      "s_and_b32 s31, s31, 12\n\t"
      "s_and_b32 s34, %[blk], 3\n\t"
      "s_or_b32 s31, s31, s34\n\t"
      "s_lshr_b32 s30, s30, 6\n\t"
      "s_lshr_b32 s34, %[blk], 5\n\t"
      "s_bfe_u32 s35, %[blk], 0x10002\n\t"
      "s_mul_i32 s17, s30, 6144\n\t"
      "s_mul_i32 s18, s30, 3072\n\t"
      "s_mul_i32 s14, s34, 14155776\n\t"
      "s_mul_i32 s36, s31, 49152\n\t"
      "s_add_u32 s14, s14, s36\n\t"
      "s_mul_i32 s36, s35, 24576\n\t"
      "s_add_u32 s15, s14, s36\n\t"
      "s_mov_b32 s16, 0\n\t"
      "s_lshr_b32 s36, %[blk], 3\n\t"
      "s_lshl2_add_u32 s24, s36, s30\n\t"
      "s_sub_u32 s36, s24, s36\n\t"
      "s_sub_u32 s36, s36, 1\n\t"
      "s_mul_i32 s23, s36, 3072\n\t"
      "s_lshl_b32 s20, s34, 3\n\t"
      "s_mul_i32 s36, s34, 27\n\t"
      "s_lshr_b32 s36, s36, 2\n\t"
      "s_lshr_b32 s37, s31, 1\n\t"
      "s_add_u32 s19, s37, s20\n\t"
      "s_add_u32 s37, s37, s36\n\t"
      "s_min_u32 s37, s37, 53\n\t"
      "s_lshl_b32 s22, s37, 21\n\t"
      "s_lshl_b32 s36, s36, 21\n\t"
      "s_sub_u32 s21, s14, s36\n\t"
      "s_add_u32 s21, s21, 24576\n\t"
      "s_lshl_b32 s19, s19, 8\n\t"
      "s_and_b32 s31, s31, 1\n\t"
      "s_lshl1_add_u32 s31, s31, s35\n\t"
      "s_lshl2_add_u32 s19, s31, s19\n\t"
      ".Lstart_%=:\n\t"
      "v_mbcnt_lo_u32_b32 v233, -1, 0\n\t"
      "v_mbcnt_hi_u32_b32 v233, -1, v233\n\t"
      "s_mul_i32 s30, s17, 683\n\t"
      "s_lshr_b32 s30, s30, 16\n\t"
      "s_and_b32 s30, s30, 0xc0\n\t"
      "s_brev_b32 s34, 8\n\t"
      "s_mov_b32 s35, 0x20201010\n\t"
      "s_mov_b32 s36, 0x30303020\n\t"
      "v_lshl_add_u32 v1, v233, 4, s18\n\t"
      "v_lshrrev_b32 v16, 1, v233\n\t"
      "v_and_b32 v16, 24, v16\n\t"
      "v_bfe_u32 v17, s34, v16, 8\n\t"
      "v_or_b32 v17, s30, v17\n\t"
      "v_xor_b32 v18, v17, v1\n\t"
      "v_add_u32 v19, 80, v17\n\t"
      "v_add_u32 v17, 160, v17\n\t"
      "v_and_b32 v19, 256, v19\n\t"
      "v_and_b32 v17, 256, v17\n\t"
      "v_mad_u32_u24 v2, v19, 30, v18\n\t"
      "v_mad_u32_u24 v5, v17, 33, v18\n\t"
      "v_add_u32 v5, 19968, v5\n\t"
      "v_bfe_u32 v17, s35, v16, 8\n\t"
      "v_or_b32 v17, s30, v17\n\t"
      "v_xor_b32 v18, v17, v1\n\t"
      "v_add_u32 v19, 80, v17\n\t"
      "v_add_u32 v17, 160, v17\n\t"
      "v_and_b32 v19, 256, v19\n\t"
      "v_and_b32 v17, 256, v17\n\t"
      "v_mad_u32_u24 v3, v19, 30, v18\n\t"
      "v_mad_u32_u24 v6, v17, 33, v18\n\t"
      "v_add_u32 v6, 19968, v6\n\t"
      "v_bfe_u32 v17, s36, v16, 8\n\t"
      "v_or_b32 v17, s30, v17\n\t"
      "v_xor_b32 v18, v17, v1\n\t"
      "v_add_u32 v19, 80, v17\n\t"
      "v_add_u32 v17, 160, v17\n\t"
      "v_and_b32 v19, 256, v19\n\t"
      "v_and_b32 v17, 256, v17\n\t"
      "v_mad_u32_u24 v4, v19, 30, v18\n\t"
      "v_mad_u32_u24 v7, v17, 33, v18\n\t"
      "v_add_u32 v7, 19968, v7\n\t"
      "s_sub_u32 s31, s30, 64\n\t"
      "v_add_u32 v17, s31, v233\n\t"
      "v_min_u32 v17, 193, v17\n\t"
      "v_lshlrev_b32 v18, 2, v17\n\t"
      "v_lshlrev_b32 v17, 1, v17\n\t"
      "v_and_b32 v18, -16, v18\n\t"
      "v_and_or_b32 v8, v17, 6, v18\n\t"
      "s_mov_b32 s86, 294912\n\t"
      "s_mov_b32 s87, 0x00020000\n\t"
      "s_mov_b32 s42, 113246208\n\t"
      "s_mov_b32 s43, 0x00020000\n\t"
      "s_cmp_lg_u32 s17, 0\n\t"
      "s_cbranch_scc1 .Lno0_%=\n\t"
      "s_movk_i32 s31, -61\n\t"
      "v_lshrrev_b32 v16, 2, v233\n\t"
      "v_add_u32 v17, s20, v16\n\t"
      "v_lshl_add_u32 v18, v16, 3, s31\n\t"
      "v_cmp_gt_u32 vcc, 32, v233\n\t"
      "v_and_b32 v19, 3, v233\n\t"
      "v_lshlrev_b32 v19, 2, v19\n\t"
      "v_cndmask_b32 v17, v18, v17, vcc\n\t"
      "v_lshl_add_u32 v62, v17, 8, v19\n\t"
      "s_waitcnt lgkmcnt(2)\n\t"
      "s_add_u32 s32, s32, %[blk]\n\t"
      "s_mul_i32 s32, s32, 0x9e3779b1\n\t"
      "s_lshr_b32 s32, s32, 9\n\t"
      "s_and_b32 s32, s32, 0x1fdf80\n\t"
      "s_or_b32 s22, s22, s32\n\t"
      "v_mov_b32 v234, s22\n\t"
      "v_add_u32 v235, 8192, v234\n\t"
      "s_movk_i32 s64, 120\n\t"
      "s_mov_b64 vcc, 0\n\t"
      ".Lsweep_%=:\n\t"
      "s_getreg_b32 s30, hwreg(HW_REG_IB_STS, 0, 12)\n\t"
      "s_and_b32 s31, s30, 15\n\t"
      "s_lshr_b32 s30, s30, 8\n\t"
      "s_cmp_eq_u32 s31, 8\n\t"
      "s_addc_u32 s62, s62, 0\n\t"
      "s_cmp_eq_u32 s30, 1\n\t"
      "s_addc_u32 s63, s63, 0\n\t"
      "s_cmp_gt_u32 s31, 7\n\t"
      "s_addc_u32 s30, s30, 0\n\t"
      "s_sub_u32 s64, s64, 1\n\t"
      "s_min_u32 s30, s30, s64\n\t"
      "s_cmp_lg_u32 s30, 0\n\t"
      "s_cbranch_scc1 .Lsweep_%=\n\t"
      "s_cbranch_vccnz .Lswept_%=\n\t"
      "s_waitcnt lgkmcnt(0)\n\t"
      "global_load_dword v236, v234, s[4:5]\n\t"
      "global_load_dword v237, v235, s[4:5]\n\t"
      "s_memrealtime s[28:29]\n\t"
      "s_load_dword s60, s[10:11], s22\n\t"
      "s_load_dword s61, s[10:11], s22 offset:8192\n\t"
      "global_load_dword v63, v62, s[12:13] sc1\n\t"
      "s_mov_b64 s[40:41], s[4:5]\n\t"
      "s_add_u32 m0, s18, 25088\n\t"
      "s_add_u32 s48, s15, 0\n\t"
      "buffer_load_dwordx4 v2, s[40:43], s48 offen nt lds\n\t"
      "buffer_load_dwordx4 v3, s[40:43], s48 offen offset:1024 nt lds\n\t"
      "buffer_load_dwordx4 v4, s[40:43], s48 offen offset:2048 nt lds\n\t"
      "s_add_u32 m0, s18, 37376\n\t"
      "s_add_u32 s48, s15, 0\n\t"
      "buffer_load_dwordx4 v5, s[40:43], s48 offen nt lds\n\t"
      "buffer_load_dwordx4 v6, s[40:43], s48 offen offset:1024 nt lds\n\t"
      "buffer_load_dwordx4 v7, s[40:43], s48 offen offset:2048 nt lds\n\t"
      "s_barrier\n\t"
      "s_mov_b64 s[62:63], 0\n\t"
      "s_movk_i32 s64, 120\n\t"
      "s_mov_b64 vcc, -1\n\t"
      "s_branch .Lsweep_%=\n\t"
      ".Lswept_%=:\n\t"
      "s_min_u32 s62, s62, 7\n\t"
      "s_min_u32 s63, s63, 7\n\t"
      "s_lshl_b32 s63, s63, 8\n\t"
      "s_or_b32 s30, s62, s63\n\t"
      "s_sub_u32 s31, 0x808, s30\n\t"
      "s_add_u32 s30, s30, 0x10808\n\t"
      "s_getreg_b32 s62, hwreg(HW_REG_XCC_ID, 2, 1)\n\t"
      "s_cmp_lg_u32 s62, 0\n\t"
      "s_cselect_b32 s91, s30, s31\n\t"
      "s_mov_b64 s[84:85], s[8:9]\n\t"
      "v_mul_u32_u24 v16, 49152, v1\n\t"
      "v_add_u32 v16, s21, v16\n\t"
      "v_lshrrev_b32 v16, 21, v16\n\t"
      "v_lshlrev_b32 v16, 2, v16\n\t"
      "v_accvgpr_write_b32 a248, v16\n\t"
      "s_branch .Lgo0_%=\n\t"
      ".Lno0_%=:\n\t"
      "s_movk_i32 s82, 768\n\t"
      "s_mov_b32 s83, 0x00020000\n\t"
      "v_and_b32 v234, 0x3f0, v1\n\t"
      "s_waitcnt lgkmcnt(0)\n\t"
      "s_mov_b64 s[84:85], s[8:9]\n\t"
      "s_mov_b64 s[80:81], s[6:7]\n\t"
      "s_mov_b64 s[40:41], s[4:5]\n\t"
      "s_barrier\n\t"
      "buffer_load_dwordx4 a[200:203], v234, s[84:87], s23 offen offset:0\n\t"
      "buffer_load_dwordx4 a[204:207], v234, s[84:87], s23 offen offset:1024\n\t"
      "buffer_load_dwordx4 a[208:211], v234, s[84:87], s23 offen offset:2048\n\t"
      "buffer_load_ushort v224, v8, s[80:83], 0 offen\n\t"
      "buffer_load_ushort v225, v8, s[80:83], 0 offen offset:8\n\t"
      "s_add_u32 m0, s18, 25088\n\t"
      "s_add_u32 s48, s15, 0\n\t"
      "buffer_load_dwordx4 v2, s[40:43], s48 offen nt lds\n\t"
      "buffer_load_dwordx4 v3, s[40:43], s48 offen offset:1024 nt lds\n\t"
      "buffer_load_dwordx4 v4, s[40:43], s48 offen offset:2048 nt lds\n\t"
      "s_add_u32 m0, s18, 37376\n\t"
      "s_add_u32 s48, s15, 0\n\t"
      "buffer_load_dwordx4 v5, s[40:43], s48 offen nt lds\n\t"
      "buffer_load_dwordx4 v6, s[40:43], s48 offen offset:1024 nt lds\n\t"
      "buffer_load_dwordx4 v7, s[40:43], s48 offen offset:2048 nt lds\n\t"
      ".Lgo0_%=:\n\t"
      "s_getpc_b64 s[36:37]\n\t"
      ".Lpc_%=:\n\t"
      "s_lshr_b32 s30, s17, 5\n\t"
      "v_lshlrev_b32 v16, 4, v233\n\t"
      "v_add_u32 v17, 0, v233\n\t"
      "v_mul_u32_u24 v17, 171, v17\n\t"
      "v_lshrrev_b32 v17, 11, v17\n\t"
      "v_lshlrev_b32 v18, 2, v17\n\t"
      "v_and_b32 v18, 32, v18\n\t"
      "v_xor_b32 v18, v18, v16\n\t"
      "v_mul_u32_u24 v17, 576, v17\n\t"
      "v_add3_u32 v9, v17, s30, v18\n\t"
      "v_add_u32 v17, 64, v233\n\t"
      "v_mul_u32_u24 v17, 171, v17\n\t"
      "v_lshrrev_b32 v17, 11, v17\n\t"
      "v_lshlrev_b32 v18, 2, v17\n\t"
      "v_and_b32 v18, 32, v18\n\t"
      "v_xor_b32 v18, v18, v16\n\t"
      "v_mul_u32_u24 v17, 576, v17\n\t"
      "v_add3_u32 v10, v17, s30, v18\n\t"
      "v_add_u32 v17, 128, v233\n\t"
      "v_mul_u32_u24 v17, 171, v17\n\t"
      "v_lshrrev_b32 v17, 11, v17\n\t"
      "v_lshlrev_b32 v18, 2, v17\n\t"
      "v_and_b32 v18, 32, v18\n\t"
      "v_xor_b32 v18, v18, v16\n\t"
      "v_mul_u32_u24 v17, 576, v17\n\t"
      "v_add3_u32 v11, v17, s30, v18\n\t"
      "s_mov_b64 s[44:45], s[10:11]\n\t"
      "s_mov_b32 s46, 0\n\t"
      "s_mov_b32 s47, 0x00020000\n\t"
      "s_lshl_b32 s30, s24, 6\n\t"
      "s_add_u32 s31, s30, 0\n\t"
      "s_min_u32 s31, s31, .Lend_%=-.Lpc_%=-4\n\t"
      "s_load_dword s35, s[36:37], s31\n\t"
      "s_add_u32 s31, s30, 8192\n\t"
      "s_min_u32 s31, s31, .Lend_%=-.Lpc_%=-4\n\t"
      "s_load_dword s35, s[36:37], s31\n\t"
      "s_add_u32 s31, s30, 16384\n\t"
      "s_min_u32 s31, s31, .Lend_%=-.Lpc_%=-4\n\t"
      "s_load_dword s35, s[36:37], s31\n\t"
      "s_add_u32 s31, s30, 24576\n\t"
      "s_min_u32 s31, s31, .Lend_%=-.Lpc_%=-4\n\t"
      "s_load_dword s35, s[36:37], s31\n\t"
      "s_and_b32 s31, s24, 63\n\t"
      "s_lshl_b32 s31, s31, 21\n\t"
      "s_min_u32 s31, s31, 113246144\n\t"
      "s_bitcmp1_b32 s24, 6\n\t"
      "s_cselect_b64 s[32:33], s[44:45], s[40:41]\n\t"
      "s_load_dword s35, s[32:33], s31\n\t"
      "s_lshr_b32 s30, s17, 5\n\t"
      "v_lshrrev_b32 v16, 4, v233\n\t"
      "v_and_b32 v17, 15, v233\n\t"
      "v_and_b32 v18, 3, v233\n\t"
      "v_bfe_u32 v19, v233, 2, 2\n\t"
      "v_lshl_add_u32 v19, v16, 3, v19\n\t"
      "v_mul_u32_u24 v19, 192, v19\n\t"
      "v_lshl_add_u32 v19, v18, 3, v19\n\t"
      "v_lshlrev_b32 v20, 1, v233\n\t"
      "v_and_b32 v20, 32, v20\n\t"
      "v_add3_u32 v12, v19, v20, s17\n\t"
      "v_xor_b32 v13, 32, v12\n\t"
      "v_xor_b32 v19, v16, v17\n\t"
      "v_lshlrev_b32 v19, 4, v19\n\t"
      "v_mul_u32_u24 v20, 768, v17\n\t"
      "v_add_u32 v15, v19, v20\n\t"
      "v_mul_u32_u24 v20, 784, v17\n\t"
      "v_lshl_add_u32 v20, v16, 3, v20\n\t"
      "v_add_u32 v0, s30, v20\n\t"
      "v_lshlrev_b32 v19, 6, v16\n\t"
      "v_lshl_add_u32 v19, v18, 4, v19\n\t"
      "v_add_u32 v14, 160256, v19\n\t"
      "v_and_b32 v226, 0xfffffff0, v8\n\t"
      "v_and_b32 v227, 6, v8\n\t"
      "v_lshlrev_b32 v226, 2, v226\n\t"
      "v_lshl_add_u32 v226, v227, 3, v226\n\t"
      "v_add_u32 v226, 160256, v226\n\t"
      "v_and_b32 v232, 2, v8\n\t"
      "v_lshlrev_b32 v232, 3, v232\n\t"
      "v_cmp_gt_u32 vcc, 4, v227\n\t"
      "v_mov_b32 v56, v15\n\t"
      "v_xor_b32 v57, 64, v15\n\t"
      "v_xor_b32 v58, 128, v15\n\t"
      "v_xor_b32 v59, 192, v15\n\t"
      "v_add_u32 v60, 86528, v12\n\t"
      "v_add_u32 v61, 86528, v13\n\t"
      "v_xor_b32 v16, v2, v1\n\t"
      "v_and_b32 v16, 0xf0, v16\n\t"
      "v_xor_b32 v17, v16, v2\n\t"
      "v_xor_b32 v18, v16, v5\n\t"
      "v_add_u32 v19, v16, v1\n\t"
      "v_accvgpr_write_b32 a213, v17\n\t"
      "v_accvgpr_write_b32 a216, v18\n\t"
      "v_accvgpr_write_b32 a219, v19\n\t"
      "v_xor_b32 v16, v3, v1\n\t"
      "v_and_b32 v16, 0xf0, v16\n\t"
      "v_xor_b32 v17, v16, v3\n\t"
      "v_xor_b32 v18, v16, v6\n\t"
      "v_add_u32 v19, v16, v1\n\t"
      "v_accvgpr_write_b32 a214, v17\n\t"
      "v_accvgpr_write_b32 a217, v18\n\t"
      "v_accvgpr_write_b32 a220, v19\n\t"
      "v_xor_b32 v16, v4, v1\n\t"
      "v_and_b32 v16, 0xf0, v16\n\t"
      "v_xor_b32 v17, v16, v4\n\t"
      "v_xor_b32 v18, v16, v7\n\t"
      "v_add_u32 v19, v16, v1\n\t"
      "v_accvgpr_write_b32 a215, v17\n\t"
      "v_accvgpr_write_b32 a218, v18\n\t"
      "v_accvgpr_write_b32 a221, v19\n\t"
      "s_waitcnt vmcnt(6)\n\t"
      "s_bfe_u32 s30, s19, 0x30008\n\t"
      "s_bfe_u32 s31, s19, 0x20002\n\t"
      "s_lshl2_add_u32 s30, s30, s31\n\t"
      "v_readlane_b32 s90, v63, s30\n\t"
      "v_lshlrev_b32 v224, v232, v224\n\t"
      "v_lshlrev_b32 v225, v232, v225\n\t"
      "v_cndmask_b32 v228, 0, v224, vcc\n\t"
      "v_cndmask_b32 v230, 0, v225, vcc\n\t"
      "v_sub_u32 v229, v224, v228\n\t"
      "v_sub_u32 v231, v225, v230\n\t"
      "ds_write_b128 v226, v[228:231]\n\t"
      "s_add_u32 s90, s90, 0x2000000\n\t"
      "s_andn2_b32 s90, s90, 0x1ffffff\n\t"
      "s_or_b32 s90, s90, s91\n\t"
      "s_or_b32 s30, s17, s16\n\t"
      "s_cbranch_scc1 .Lno2_%=\n\t"
      "v_mov_b32 v234, s90\n\t"
      "v_mov_b32 v235, s19\n\t"
      "global_store_dword v235, v234, s[12:13] sc1\n\t"
      ".Lno2_%=:\n\t"
      "s_add_u32 m0, s17, 0\n\t"
      "s_mov_b32 s54, 0\n\t"
      "buffer_load_dwordx4 v9, s[84:87], s54 offen lds\n\t"
      "buffer_load_dwordx4 v10, s[84:87], s54 offen offset:1024 lds\n\t"
      "buffer_load_dwordx4 v11, s[84:87], s54 offen offset:2048 lds\n\t"
      "s_add_u32 m0, s17, 3072\n\t"
      "s_mov_b32 s54, 12288\n\t"
      "buffer_load_dwordx4 v9, s[84:87], s54 offen lds\n\t"
      "buffer_load_dwordx4 v10, s[84:87], s54 offen offset:1024 lds\n\t"
      "buffer_load_dwordx4 v11, s[84:87], s54 offen offset:2048 lds\n\t"
      "s_add_u32 m0, s17, 86528\n\t"
      "s_mov_b32 s54, 24576\n\t"
      "buffer_load_dwordx4 v9, s[84:87], s54 offen lds\n\t"
      "buffer_load_dwordx4 v10, s[84:87], s54 offen offset:1024 lds\n\t"
      "buffer_load_dwordx4 v11, s[84:87], s54 offen offset:2048 lds\n\t"
      "s_add_u32 m0, s17, 89600\n\t"
      "s_mov_b32 s54, 36864\n\t"
      "buffer_load_dwordx4 v9, s[84:87], s54 offen lds\n\t"
      "buffer_load_dwordx4 v10, s[84:87], s54 offen offset:1024 lds\n\t"
      "buffer_load_dwordx4 v11, s[84:87], s54 offen offset:2048 lds\n\t"
      "s_add_u32 m0, s17, 111104\n\t"
      "s_mov_b32 s54, 49152\n\t"
      "buffer_load_dwordx4 v9, s[84:87], s54 offen lds\n\t"
      "buffer_load_dwordx4 v10, s[84:87], s54 offen offset:1024 lds\n\t"
      "buffer_load_dwordx4 v11, s[84:87], s54 offen offset:2048 lds\n\t"
      "s_add_u32 m0, s17, 114176\n\t"
      "s_mov_b32 s54, 61440\n\t"
      "buffer_load_dwordx4 v9, s[84:87], s54 offen lds\n\t"
      "buffer_load_dwordx4 v10, s[84:87], s54 offen offset:1024 lds\n\t"
      "buffer_load_dwordx4 v11, s[84:87], s54 offen offset:2048 lds\n\t"
      "s_waitcnt lgkmcnt(0)\n\t"
      "s_waitcnt vmcnt(18)\n\t"
      "s_barrier\n\t"
      "s_waitcnt vmcnt(12)\n\t"
      "ds_read_b128 a[180:183], v14 offset:0\n\t"
      "ds_read_b64_tr_b16 v[64:65], v12 offset:0\n\t"
      "ds_read_b64_tr_b16 v[66:67], v12 offset:768\n\t"
      "ds_read_b64_tr_b16 v[68:69], v13 offset:0\n\t"
      "ds_read_b64_tr_b16 v[70:71], v13 offset:768\n\t"
      "ds_read_b64_tr_b16 v[72:73], v12 offset:64\n\t"
      "ds_read_b64_tr_b16 v[74:75], v12 offset:832\n\t"
      "ds_read_b64_tr_b16 v[76:77], v13 offset:64\n\t"
      "ds_read_b64_tr_b16 v[78:79], v13 offset:832\n\t"
      "ds_read_b64_tr_b16 a[60:61], v12 offset:128\n\t"
      "ds_read_b64_tr_b16 a[62:63], v12 offset:896\n\t"
      "ds_read_b64_tr_b16 a[64:65], v13 offset:128\n\t"
      "ds_read_b64_tr_b16 a[66:67], v13 offset:896\n\t"
      "s_add_u32 m0, s17, 135680\n\t"
      "s_mov_b32 s54, 73728\n\t"
      "buffer_load_dwordx4 v9, s[84:87], s54 offen lds\n\t"
      "buffer_load_dwordx4 v10, s[84:87], s54 offen offset:1024 lds\n\t"
      "buffer_load_dwordx4 v11, s[84:87], s54 offen offset:2048 lds\n\t"
      "s_add_u32 m0, s17, 138752\n\t"
      "s_mov_b32 s54, 86016\n\t"
      "buffer_load_dwordx4 v9, s[84:87], s54 offen lds\n\t"
      "buffer_load_dwordx4 v10, s[84:87], s54 offen offset:1024 lds\n\t"
      "buffer_load_dwordx4 v11, s[84:87], s54 offen offset:2048 lds\n\t"
      "s_waitcnt vmcnt(12)\n\t"
      "s_waitcnt lgkmcnt(0)\n\t"
      "; step\n\t"
      "v_mfma_f32_4x4x4_16b_bf16 v[40:43], a[180:181], v[64:65], 0\n\t"
      "ds_read_b128 a[184:187], v14 offset:256\n\t"
      "v_mfma_f32_4x4x4_16b_bf16 v[44:47], a[182:183], v[66:67], 0\n\t"
      "ds_read_b64_tr_b16 v[80:81], v60 offset:0\n\t"
      "v_mfma_f32_4x4x4_16b_bf16 v[48:51], a[180:181], v[68:69], 0\n\t"
      "ds_read_b64_tr_b16 v[82:83], v60 offset:768\n\t"
      "v_mfma_f32_4x4x4_16b_bf16 v[52:55], a[182:183], v[70:71], 0\n\t"
      "ds_read_b64_tr_b16 v[84:85], v61 offset:0\n\t"
      "ds_read_b64_tr_b16 v[86:87], v61 offset:768\n\t"
      "s_add_u32 m0, s17, 0\n\t"
      "s_mov_b32 s54, 98304\n\t"
      "buffer_load_dwordx4 v9, s[84:87], s54 offen lds\n\t"
      "buffer_load_dwordx4 v10, s[84:87], s54 offen offset:1024 lds\n\t"
      "v_cvt_pk_bf16_f32 v64, v40, v41\n\t"
      "v_cvt_pk_bf16_f32 v65, v42, v43\n\t"
      "v_cvt_pk_bf16_f32 v66, v44, v45\n\t"
      "v_cvt_pk_bf16_f32 v67, v46, v47\n\t"
      "v_cvt_pk_bf16_f32 v68, v48, v49\n\t"
      "v_cvt_pk_bf16_f32 v69, v50, v51\n\t"
      "v_cvt_pk_bf16_f32 v70, v52, v53\n\t"
      "v_cvt_pk_bf16_f32 v71, v54, v55\n\t"
      "v_mfma_f32_4x4x4_16b_bf16 v[40:43], a[180:181], v[72:73], 0\n\t"
      "ds_read_b64_tr_b16 v[88:89], v60 offset:64\n\t"
      "v_mfma_f32_4x4x4_16b_bf16 v[44:47], a[182:183], v[74:75], 0\n\t"
      "ds_read_b64_tr_b16 v[90:91], v60 offset:832\n\t"
      "v_mfma_f32_4x4x4_16b_bf16 v[48:51], a[180:181], v[76:77], 0\n\t"
      "ds_read_b64_tr_b16 v[92:93], v61 offset:64\n\t"
      "v_mfma_f32_4x4x4_16b_bf16 v[52:55], a[182:183], v[78:79], 0\n\t"
      "ds_read_b64_tr_b16 v[94:95], v61 offset:832\n\t"
      "buffer_load_dwordx4 v11, s[84:87], s54 offen offset:2048 lds\n\t"
      "s_add_u32 m0, s17, 3072\n\t"
      "s_mov_b32 s54, 110592\n\t"
      "buffer_load_dwordx4 v9, s[84:87], s54 offen lds\n\t"
      "v_cvt_pk_bf16_f32 v72, v40, v41\n\t"
      "v_cvt_pk_bf16_f32 v73, v42, v43\n\t"
      "v_cvt_pk_bf16_f32 v74, v44, v45\n\t"
      "v_cvt_pk_bf16_f32 v75, v46, v47\n\t"
      "v_cvt_pk_bf16_f32 v76, v48, v49\n\t"
      "v_cvt_pk_bf16_f32 v77, v50, v51\n\t"
      "v_cvt_pk_bf16_f32 v78, v52, v53\n\t"
      "v_cvt_pk_bf16_f32 v79, v54, v55\n\t"
      "ds_read_b128 a[224:227], v56 offset:25088\n\t"
      "v_mfma_f32_4x4x4_16b_bf16 v[40:43], a[180:181], a[60:61], 0\n\t"
      "ds_read_b64_tr_b16 a[68:69], v60 offset:128\n\t"
      "v_mfma_f32_4x4x4_16b_bf16 v[44:47], a[182:183], a[62:63], 0\n\t"
      "ds_read_b64_tr_b16 a[70:71], v60 offset:896\n\t"
      "v_mfma_f32_4x4x4_16b_bf16 v[48:51], a[180:181], a[64:65], 0\n\t"
      "ds_read_b64_tr_b16 a[72:73], v61 offset:128\n\t"
      "v_mfma_f32_4x4x4_16b_bf16 v[52:55], a[182:183], a[66:67], 0\n\t"
      "ds_read_b64_tr_b16 a[74:75], v61 offset:896\n\t"
      "buffer_load_dwordx4 v10, s[84:87], s54 offen offset:1024 lds\n\t"
      "buffer_load_dwordx4 v11, s[84:87], s54 offen offset:2048 lds\n\t"
      "ds_read_b128 a[228:231], v56 offset:37376\n\t"
      "v_cvt_pk_bf16_f32 v40, v40, v41\n\t"
      "v_cvt_pk_bf16_f32 v41, v42, v43\n\t"
      "v_cvt_pk_bf16_f32 v44, v44, v45\n\t"
      "v_cvt_pk_bf16_f32 v45, v46, v47\n\t"
      "v_cvt_pk_bf16_f32 v48, v48, v49\n\t"
      "v_cvt_pk_bf16_f32 v49, v50, v51\n\t"
      "v_cvt_pk_bf16_f32 v52, v52, v53\n\t"
      "v_cvt_pk_bf16_f32 v53, v54, v55\n\t"
      "v_accvgpr_write_b32 a60, v40\n\t"
      "v_accvgpr_write_b32 a61, v41\n\t"
      "v_accvgpr_write_b32 a62, v44\n\t"
      "v_accvgpr_write_b32 a63, v45\n\t"
      "v_accvgpr_write_b32 a64, v48\n\t"
      "v_accvgpr_write_b32 a65, v49\n\t"
      "v_accvgpr_write_b32 a66, v52\n\t"
      "v_accvgpr_write_b32 a67, v53\n\t"
      "s_waitcnt vmcnt(12)\n\t"
      "s_waitcnt lgkmcnt(1)\n\t"
      "; step\n\t"
      "s_memtime s[64:65]\n\t"
      "s_memrealtime s[66:67]\n\t"
      "v_mfma_f32_4x4x4_16b_bf16 v[40:43], a[184:185], v[80:81], 0\n\t"
      "ds_read_b128 a[180:183], v14 offset:512\n\t"
      "v_mfma_f32_4x4x4_16b_bf16 v[44:47], a[186:187], v[82:83], 0\n\t"
      "ds_read_b64_tr_b16 v[96:97], v60 offset:24576\n\t"
      "v_mfma_f32_4x4x4_16b_bf16 v[48:51], a[184:185], v[84:85], 0\n\t"
      "ds_read_b64_tr_b16 v[98:99], v60 offset:25344\n\t"
      "v_mfma_f32_4x4x4_16b_bf16 v[52:55], a[186:187], v[86:87], 0\n\t"
      "ds_read_b64_tr_b16 v[100:101], v61 offset:24576\n\t"
      "ds_read_b64_tr_b16 v[102:103], v61 offset:25344\n\t"
      "s_add_u32 m0, s17, 86528\n\t"
      "s_mov_b32 s54, 122880\n\t"
      "buffer_load_dwordx4 v9, s[84:87], s54 offen lds\n\t"
      "buffer_load_dwordx4 v10, s[84:87], s54 offen offset:1024 lds\n\t"
      "v_mfma_f32_16x16x32_bf16 v[16:19], v[64:67], a[224:227], 0\n\t"
      "v_cvt_pk_bf16_f32 v80, v40, v41\n\t"
      "v_cvt_pk_bf16_f32 v81, v42, v43\n\t"
      "v_mfma_f32_16x16x32_bf16 v[20:23], v[68:71], a[224:227], 0\n\t"
      "v_cvt_pk_bf16_f32 v82, v44, v45\n\t"
      "v_cvt_pk_bf16_f32 v83, v46, v47\n\t"
      "v_mfma_f32_16x16x32_bf16 v[24:27], v[72:75], a[224:227], 0\n\t"
      "v_cvt_pk_bf16_f32 v84, v48, v49\n\t"
      "v_cvt_pk_bf16_f32 v85, v50, v51\n\t"
      "v_mfma_f32_16x16x32_bf16 v[28:31], v[76:79], a[224:227], 0\n\t"
      "v_cvt_pk_bf16_f32 v86, v52, v53\n\t"
      "v_cvt_pk_bf16_f32 v87, v54, v55\n\t"
      "v_mfma_f32_4x4x4_16b_bf16 v[40:43], a[184:185], v[88:89], 0\n\t"
      "ds_read_b64_tr_b16 v[104:105], v60 offset:24640\n\t"
      "v_mfma_f32_4x4x4_16b_bf16 v[44:47], a[186:187], v[90:91], 0\n\t"
      "ds_read_b64_tr_b16 v[106:107], v60 offset:25408\n\t"
      "v_mfma_f32_4x4x4_16b_bf16 v[48:51], a[184:185], v[92:93], 0\n\t"
      "ds_read_b64_tr_b16 v[108:109], v61 offset:24640\n\t"
      "v_mfma_f32_4x4x4_16b_bf16 v[52:55], a[186:187], v[94:95], 0\n\t"
      "ds_read_b64_tr_b16 v[110:111], v61 offset:25408\n\t"
      "buffer_load_dwordx4 v11, s[84:87], s54 offen offset:2048 lds\n\t"
      "s_add_u32 m0, s17, 89600\n\t"
      "s_mov_b32 s54, 135168\n\t"
      "buffer_load_dwordx4 v9, s[84:87], s54 offen lds\n\t"
      "v_mfma_f32_16x16x32_bf16 v[32:35], a[60:63], a[224:227], 0\n\t"
      "v_cvt_pk_bf16_f32 v88, v40, v41\n\t"
      "v_cvt_pk_bf16_f32 v89, v42, v43\n\t"
      "v_mfma_f32_16x16x32_bf16 v[36:39], a[64:67], a[224:227], 0\n\t"
      "v_cvt_pk_bf16_f32 v90, v44, v45\n\t"
      "v_cvt_pk_bf16_f32 v91, v46, v47\n\t"
      "s_waitcnt lgkmcnt(9)\n\t"
      "v_mfma_f32_16x16x32_bf16 a[156:159], v[64:67], a[228:231], 0\n\t"
      "v_cvt_pk_bf16_f32 v92, v48, v49\n\t"
      "v_cvt_pk_bf16_f32 v93, v50, v51\n\t"
      "v_mfma_f32_16x16x32_bf16 a[160:163], v[68:71], a[228:231], 0\n\t"
      "v_cvt_pk_bf16_f32 v94, v52, v53\n\t"
      "v_cvt_pk_bf16_f32 v95, v54, v55\n\t"
      "ds_read_b128 a[232:235], v57 offset:25088\n\t"
      "v_mfma_f32_4x4x4_16b_bf16 v[40:43], a[184:185], a[68:69], 0\n\t"
      "ds_read_b64_tr_b16 a[76:77], v60 offset:24704\n\t"
      "v_mfma_f32_4x4x4_16b_bf16 v[44:47], a[186:187], a[70:71], 0\n\t"
      "ds_read_b64_tr_b16 a[78:79], v60 offset:25472\n\t"
      "v_mfma_f32_4x4x4_16b_bf16 v[48:51], a[184:185], a[72:73], 0\n\t"
      "ds_read_b64_tr_b16 a[80:81], v61 offset:24704\n\t"
      "v_mfma_f32_4x4x4_16b_bf16 v[52:55], a[186:187], a[74:75], 0\n\t"
      "ds_read_b64_tr_b16 a[82:83], v61 offset:25472\n\t"
      "buffer_load_dwordx4 v10, s[84:87], s54 offen offset:1024 lds\n\t"
      "buffer_load_dwordx4 v11, s[84:87], s54 offen offset:2048 lds\n\t"
      "ds_read_b128 a[236:239], v57 offset:37376\n\t"
      "v_mfma_f32_16x16x32_bf16 a[164:167], v[72:75], a[228:231], 0\n\t"
      "v_cvt_pk_bf16_f32 v40, v40, v41\n\t"
      "v_cvt_pk_bf16_f32 v41, v42, v43\n\t"
      "v_mfma_f32_16x16x32_bf16 a[168:171], v[76:79], a[228:231], 0\n\t"
      "v_cvt_pk_bf16_f32 v44, v44, v45\n\t"
      "v_cvt_pk_bf16_f32 v45, v46, v47\n\t"
      "v_mfma_f32_16x16x32_bf16 a[172:175], a[60:63], a[228:231], 0\n\t"
      "v_cvt_pk_bf16_f32 v48, v48, v49\n\t"
      "v_cvt_pk_bf16_f32 v49, v50, v51\n\t"
      "v_mfma_f32_16x16x32_bf16 a[176:179], a[64:67], a[228:231], 0\n\t"
      "v_cvt_pk_bf16_f32 v52, v52, v53\n\t"
      "v_cvt_pk_bf16_f32 v53, v54, v55\n\t"
      "v_accvgpr_write_b32 a68, v40\n\t"
      "v_accvgpr_write_b32 a69, v41\n\t"
      "v_accvgpr_write_b32 a70, v44\n\t"
      "v_accvgpr_write_b32 a71, v45\n\t"
      "v_accvgpr_write_b32 a72, v48\n\t"
      "v_accvgpr_write_b32 a73, v49\n\t"
      "v_accvgpr_write_b32 a74, v52\n\t"
      "v_accvgpr_write_b32 a75, v53\n\t"
      "s_waitcnt vmcnt(12)\n\t"
      "s_waitcnt lgkmcnt(1)\n\t"
      "; step\n\t"
      "v_mfma_f32_4x4x4_16b_bf16 v[40:43], a[180:181], v[96:97], 0\n\t"
      "ds_read_b128 a[184:187], v14 offset:768\n\t"
      "v_mfma_f32_4x4x4_16b_bf16 v[44:47], a[182:183], v[98:99], 0\n\t"
      "ds_read_b64_tr_b16 v[112:113], v60 offset:49152\n\t"
      "v_mfma_f32_4x4x4_16b_bf16 v[48:51], a[180:181], v[100:101], 0\n\t"
      "ds_read_b64_tr_b16 v[114:115], v60 offset:49920\n\t"
      "v_mfma_f32_4x4x4_16b_bf16 v[52:55], a[182:183], v[102:103], 0\n\t"
      "ds_read_b64_tr_b16 v[116:117], v61 offset:49152\n\t"
      "ds_read_b64_tr_b16 v[118:119], v61 offset:49920\n\t"
      "s_add_u32 m0, s17, 111104\n\t"
      "s_mov_b32 s54, 147456\n\t"
      "buffer_load_dwordx4 v9, s[84:87], s54 offen lds\n\t"
      "buffer_load_dwordx4 v10, s[84:87], s54 offen offset:1024 lds\n\t"
      "v_mfma_f32_16x16x32_bf16 v[16:19], v[80:83], a[232:235], v[16:19]\n\t"
      "v_cvt_pk_bf16_f32 v96, v40, v41\n\t"
      "v_cvt_pk_bf16_f32 v97, v42, v43\n\t"
      "v_mfma_f32_16x16x32_bf16 v[20:23], v[84:87], a[232:235], v[20:23]\n\t"
      "v_cvt_pk_bf16_f32 v98, v44, v45\n\t"
      "v_cvt_pk_bf16_f32 v99, v46, v47\n\t"
      "v_mfma_f32_16x16x32_bf16 v[24:27], v[88:91], a[232:235], v[24:27]\n\t"
      "v_cvt_pk_bf16_f32 v100, v48, v49\n\t"
      "v_cvt_pk_bf16_f32 v101, v50, v51\n\t"
      "v_mfma_f32_16x16x32_bf16 v[28:31], v[92:95], a[232:235], v[28:31]\n\t"
      "v_cvt_pk_bf16_f32 v102, v52, v53\n\t"
      "v_cvt_pk_bf16_f32 v103, v54, v55\n\t"
      "v_mfma_f32_4x4x4_16b_bf16 v[40:43], a[180:181], v[104:105], 0\n\t"
      "ds_read_b64_tr_b16 v[120:121], v60 offset:49216\n\t"
      "v_mfma_f32_4x4x4_16b_bf16 v[44:47], a[182:183], v[106:107], 0\n\t"
      "ds_read_b64_tr_b16 v[122:123], v60 offset:49984\n\t"
      "v_mfma_f32_4x4x4_16b_bf16 v[48:51], a[180:181], v[108:109], 0\n\t"
      "ds_read_b64_tr_b16 v[124:125], v61 offset:49216\n\t"
      "v_mfma_f32_4x4x4_16b_bf16 v[52:55], a[182:183], v[110:111], 0\n\t"
      "ds_read_b64_tr_b16 v[126:127], v61 offset:49984\n\t"
      "buffer_load_dwordx4 v11, s[84:87], s54 offen offset:2048 lds\n\t"
      "s_add_u32 m0, s17, 114176\n\t"
      "s_mov_b32 s54, 159744\n\t"
      "buffer_load_dwordx4 v9, s[84:87], s54 offen lds\n\t"
      "v_mfma_f32_16x16x32_bf16 v[32:35], a[68:71], a[232:235], v[32:35]\n\t"
      "v_cvt_pk_bf16_f32 v104, v40, v41\n\t"
      "v_cvt_pk_bf16_f32 v105, v42, v43\n\t"
      "v_mfma_f32_16x16x32_bf16 v[36:39], a[72:75], a[232:235], v[36:39]\n\t"
      "v_cvt_pk_bf16_f32 v106, v44, v45\n\t"
      "v_cvt_pk_bf16_f32 v107, v46, v47\n\t"
      "s_waitcnt lgkmcnt(9)\n\t"
      "v_mfma_f32_16x16x32_bf16 a[156:159], v[80:83], a[236:239], a[156:159]\n\t"
      "v_cvt_pk_bf16_f32 v108, v48, v49\n\t"
      "v_cvt_pk_bf16_f32 v109, v50, v51\n\t"
      "v_mfma_f32_16x16x32_bf16 a[160:163], v[84:87], a[236:239], a[160:163]\n\t"
      "v_cvt_pk_bf16_f32 v110, v52, v53\n\t"
      "v_cvt_pk_bf16_f32 v111, v54, v55\n\t"
      "ds_read_b128 a[240:243], v58 offset:25088\n\t"
      "v_mfma_f32_4x4x4_16b_bf16 v[40:43], a[180:181], a[76:77], 0\n\t"
      "ds_read_b64_tr_b16 a[84:85], v60 offset:49280\n\t"
      "v_mfma_f32_4x4x4_16b_bf16 v[44:47], a[182:183], a[78:79], 0\n\t"
      "ds_read_b64_tr_b16 a[86:87], v60 offset:50048\n\t"
      "v_mfma_f32_4x4x4_16b_bf16 v[48:51], a[180:181], a[80:81], 0\n\t"
      "ds_read_b64_tr_b16 a[88:89], v61 offset:49280\n\t"
      "v_mfma_f32_4x4x4_16b_bf16 v[52:55], a[182:183], a[82:83], 0\n\t"
      "ds_read_b64_tr_b16 a[90:91], v61 offset:50048\n\t"
      "buffer_load_dwordx4 v10, s[84:87], s54 offen offset:1024 lds\n\t"
      "buffer_load_dwordx4 v11, s[84:87], s54 offen offset:2048 lds\n\t"
      "ds_read_b128 a[244:247], v58 offset:37376\n\t"
      "v_mfma_f32_16x16x32_bf16 a[164:167], v[88:91], a[236:239], a[164:167]\n\t"
      "v_cvt_pk_bf16_f32 v40, v40, v41\n\t"
      "v_cvt_pk_bf16_f32 v41, v42, v43\n\t"
      "v_mfma_f32_16x16x32_bf16 a[168:171], v[92:95], a[236:239], a[168:171]\n\t"
      "v_cvt_pk_bf16_f32 v44, v44, v45\n\t"
      "v_cvt_pk_bf16_f32 v45, v46, v47\n\t"
      "v_mfma_f32_16x16x32_bf16 a[172:175], a[68:71], a[236:239], a[172:175]\n\t"
      "v_cvt_pk_bf16_f32 v48, v48, v49\n\t"
      "v_cvt_pk_bf16_f32 v49, v50, v51\n\t"
      "v_mfma_f32_16x16x32_bf16 a[176:179], a[72:75], a[236:239], a[176:179]\n\t"
      "v_cvt_pk_bf16_f32 v52, v52, v53\n\t"
      "v_cvt_pk_bf16_f32 v53, v54, v55\n\t"
      "v_accvgpr_write_b32 a76, v40\n\t"
      "v_accvgpr_write_b32 a77, v41\n\t"
      "v_accvgpr_write_b32 a78, v44\n\t"
      "v_accvgpr_write_b32 a79, v45\n\t"
      "v_accvgpr_write_b32 a80, v48\n\t"
      "v_accvgpr_write_b32 a81, v49\n\t"
      "v_accvgpr_write_b32 a82, v52\n\t"
      "v_accvgpr_write_b32 a83, v53\n\t"
      "s_waitcnt vmcnt(12)\n\t"
      "s_waitcnt lgkmcnt(1)\n\t"
      "; step\n\t"
      "v_mfma_f32_4x4x4_16b_bf16 v[40:43], a[184:185], v[112:113], 0\n\t"
      "ds_read_b128 a[180:183], v14 offset:1024\n\t"
      "v_mfma_f32_4x4x4_16b_bf16 v[44:47], a[186:187], v[114:115], 0\n\t"
      "ds_read_b64_tr_b16 v[128:129], v12 offset:0\n\t"
      "v_mfma_f32_4x4x4_16b_bf16 v[48:51], a[184:185], v[116:117], 0\n\t"
      "ds_read_b64_tr_b16 v[130:131], v12 offset:768\n\t"
      "v_mfma_f32_4x4x4_16b_bf16 v[52:55], a[186:187], v[118:119], 0\n\t"
      "ds_read_b64_tr_b16 v[132:133], v13 offset:0\n\t"
      "ds_read_b64_tr_b16 v[134:135], v13 offset:768\n\t"
      "s_add_u32 m0, s17, 135680\n\t"
      "s_mov_b32 s54, 172032\n\t"
      "buffer_load_dwordx4 v9, s[84:87], s54 offen lds\n\t"
      "buffer_load_dwordx4 v10, s[84:87], s54 offen offset:1024 lds\n\t"
      "v_mfma_f32_16x16x32_bf16 v[16:19], v[96:99], a[240:243], v[16:19]\n\t"
      "v_cvt_pk_bf16_f32 v112, v40, v41\n\t"
      "v_cvt_pk_bf16_f32 v113, v42, v43\n\t"
      "v_mfma_f32_16x16x32_bf16 v[20:23], v[100:103], a[240:243], v[20:23]\n\t"
      "v_cvt_pk_bf16_f32 v114, v44, v45\n\t"
      "v_cvt_pk_bf16_f32 v115, v46, v47\n\t"
      "v_mfma_f32_16x16x32_bf16 v[24:27], v[104:107], a[240:243], v[24:27]\n\t"
      "v_cvt_pk_bf16_f32 v116, v48, v49\n\t"
      "v_cvt_pk_bf16_f32 v117, v50, v51\n\t"
      "v_mfma_f32_16x16x32_bf16 v[28:31], v[108:111], a[240:243], v[28:31]\n\t"
      "v_cvt_pk_bf16_f32 v118, v52, v53\n\t"
      "v_cvt_pk_bf16_f32 v119, v54, v55\n\t"
      "v_mfma_f32_4x4x4_16b_bf16 v[40:43], a[184:185], v[120:121], 0\n\t"
      "ds_read_b64_tr_b16 v[136:137], v12 offset:64\n\t"
      "v_mfma_f32_4x4x4_16b_bf16 v[44:47], a[186:187], v[122:123], 0\n\t"
      "ds_read_b64_tr_b16 v[138:139], v12 offset:832\n\t"
      "v_mfma_f32_4x4x4_16b_bf16 v[48:51], a[184:185], v[124:125], 0\n\t"
      "ds_read_b64_tr_b16 v[140:141], v13 offset:64\n\t"
      "v_mfma_f32_4x4x4_16b_bf16 v[52:55], a[186:187], v[126:127], 0\n\t"
      "ds_read_b64_tr_b16 v[142:143], v13 offset:832\n\t"
      "buffer_load_dwordx4 v11, s[84:87], s54 offen offset:2048 lds\n\t"
      "s_add_u32 m0, s17, 138752\n\t"
      "s_mov_b32 s54, 184320\n\t"
      "buffer_load_dwordx4 v9, s[84:87], s54 offen lds\n\t"
      "v_mfma_f32_16x16x32_bf16 v[32:35], a[76:79], a[240:243], v[32:35]\n\t"
      "v_cvt_pk_bf16_f32 v120, v40, v41\n\t"
      "v_cvt_pk_bf16_f32 v121, v42, v43\n\t"
      "v_mfma_f32_16x16x32_bf16 v[36:39], a[80:83], a[240:243], v[36:39]\n\t"
      "v_cvt_pk_bf16_f32 v122, v44, v45\n\t"
      "v_cvt_pk_bf16_f32 v123, v46, v47\n\t"
      "s_waitcnt lgkmcnt(9)\n\t"
      "v_mfma_f32_16x16x32_bf16 a[156:159], v[96:99], a[244:247], a[156:159]\n\t"
      "v_cvt_pk_bf16_f32 v124, v48, v49\n\t"
      "v_cvt_pk_bf16_f32 v125, v50, v51\n\t"
      "v_mfma_f32_16x16x32_bf16 a[160:163], v[100:103], a[244:247], a[160:163]\n\t"
      "v_cvt_pk_bf16_f32 v126, v52, v53\n\t"
      "v_cvt_pk_bf16_f32 v127, v54, v55\n\t"
      "ds_read_b128 a[224:227], v59 offset:25088\n\t"
      "v_mfma_f32_4x4x4_16b_bf16 v[40:43], a[184:185], a[84:85], 0\n\t"
      "ds_read_b64_tr_b16 a[92:93], v12 offset:128\n\t"
      "v_mfma_f32_4x4x4_16b_bf16 v[44:47], a[186:187], a[86:87], 0\n\t"
      "ds_read_b64_tr_b16 a[94:95], v12 offset:896\n\t"
      "v_mfma_f32_4x4x4_16b_bf16 v[48:51], a[184:185], a[88:89], 0\n\t"
      "ds_read_b64_tr_b16 a[96:97], v13 offset:128\n\t"
      "v_mfma_f32_4x4x4_16b_bf16 v[52:55], a[186:187], a[90:91], 0\n\t"
      "ds_read_b64_tr_b16 a[98:99], v13 offset:896\n\t"
      "buffer_load_dwordx4 v10, s[84:87], s54 offen offset:1024 lds\n\t"
      "buffer_load_dwordx4 v11, s[84:87], s54 offen offset:2048 lds\n\t"
      "ds_read_b128 a[228:231], v59 offset:37376\n\t"
      "v_mfma_f32_16x16x32_bf16 a[164:167], v[104:107], a[244:247], a[164:167]\n\t"
      "v_cvt_pk_bf16_f32 v40, v40, v41\n\t"
      "v_cvt_pk_bf16_f32 v41, v42, v43\n\t"
      "v_mfma_f32_16x16x32_bf16 a[168:171], v[108:111], a[244:247], a[168:171]\n\t"
      "v_cvt_pk_bf16_f32 v44, v44, v45\n\t"
      "v_cvt_pk_bf16_f32 v45, v46, v47\n\t"
      "v_mfma_f32_16x16x32_bf16 a[172:175], a[76:79], a[244:247], a[172:175]\n\t"
      "v_cvt_pk_bf16_f32 v48, v48, v49\n\t"
      "v_cvt_pk_bf16_f32 v49, v50, v51\n\t"
      "v_mfma_f32_16x16x32_bf16 a[176:179], a[80:83], a[244:247], a[176:179]\n\t"
      "v_cvt_pk_bf16_f32 v52, v52, v53\n\t"
      "v_cvt_pk_bf16_f32 v53, v54, v55\n\t"
      "v_accvgpr_write_b32 a84, v40\n\t"
      "v_accvgpr_write_b32 a85, v41\n\t"
      "v_accvgpr_write_b32 a86, v44\n\t"
      "v_accvgpr_write_b32 a87, v45\n\t"
      "v_accvgpr_write_b32 a88, v48\n\t"
      "v_accvgpr_write_b32 a89, v49\n\t"
      "v_accvgpr_write_b32 a90, v52\n\t"
      "v_accvgpr_write_b32 a91, v53\n\t"
      "s_waitcnt vmcnt(12)\n\t"
      "s_waitcnt lgkmcnt(1)\n\t"
      "; step\n\t"
      "v_mfma_f32_4x4x4_16b_bf16 v[40:43], a[180:181], v[128:129], 0\n\t"
      "ds_read_b128 a[184:187], v14 offset:1280\n\t"
      "v_mfma_f32_4x4x4_16b_bf16 v[44:47], a[182:183], v[130:131], 0\n\t"
      "ds_read_b64_tr_b16 v[144:145], v60 offset:0\n\t"
      "v_mfma_f32_4x4x4_16b_bf16 v[48:51], a[180:181], v[132:133], 0\n\t"
      "ds_read_b64_tr_b16 v[146:147], v60 offset:768\n\t"
      "v_mfma_f32_4x4x4_16b_bf16 v[52:55], a[182:183], v[134:135], 0\n\t"
      "ds_read_b64_tr_b16 v[148:149], v61 offset:0\n\t"
      "ds_read_b64_tr_b16 v[150:151], v61 offset:768\n\t"
      "s_add_u32 m0, s17, 0\n\t"
      "s_mov_b32 s54, 196608\n\t"
      "buffer_load_dwordx4 v9, s[84:87], s54 offen lds\n\t"
      "buffer_load_dwordx4 v10, s[84:87], s54 offen offset:1024 lds\n\t"
      "v_mfma_f32_16x16x32_bf16 v[16:19], v[112:115], a[224:227], v[16:19]\n\t"
      "v_cvt_pk_bf16_f32 v128, v40, v41\n\t"
      "v_cvt_pk_bf16_f32 v129, v42, v43\n\t"
      "v_mfma_f32_16x16x32_bf16 v[20:23], v[116:119], a[224:227], v[20:23]\n\t"
      "v_cvt_pk_bf16_f32 v130, v44, v45\n\t"
      "v_cvt_pk_bf16_f32 v131, v46, v47\n\t"
      "v_mfma_f32_16x16x32_bf16 v[24:27], v[120:123], a[224:227], v[24:27]\n\t"
      "v_cvt_pk_bf16_f32 v132, v48, v49\n\t"
      "v_cvt_pk_bf16_f32 v133, v50, v51\n\t"
      "v_mfma_f32_16x16x32_bf16 v[28:31], v[124:127], a[224:227], v[28:31]\n\t"
      "v_cvt_pk_bf16_f32 v134, v52, v53\n\t"
      "v_cvt_pk_bf16_f32 v135, v54, v55\n\t"
      "v_mfma_f32_4x4x4_16b_bf16 v[40:43], a[180:181], v[136:137], 0\n\t"
      "ds_read_b64_tr_b16 v[152:153], v60 offset:64\n\t"
      "v_mfma_f32_4x4x4_16b_bf16 v[44:47], a[182:183], v[138:139], 0\n\t"
      "ds_read_b64_tr_b16 v[154:155], v60 offset:832\n\t"
      "v_mfma_f32_4x4x4_16b_bf16 v[48:51], a[180:181], v[140:141], 0\n\t"
      "ds_read_b64_tr_b16 v[156:157], v61 offset:64\n\t"
      "v_mfma_f32_4x4x4_16b_bf16 v[52:55], a[182:183], v[142:143], 0\n\t"
      "ds_read_b64_tr_b16 v[158:159], v61 offset:832\n\t"
      "buffer_load_dwordx4 v11, s[84:87], s54 offen offset:2048 lds\n\t"
      "s_add_u32 m0, s17, 3072\n\t"
      "s_mov_b32 s54, 208896\n\t"
      "buffer_load_dwordx4 v9, s[84:87], s54 offen lds\n\t"
      "v_mfma_f32_16x16x32_bf16 v[32:35], a[84:87], a[224:227], v[32:35]\n\t"
      "v_cvt_pk_bf16_f32 v136, v40, v41\n\t"
      "v_cvt_pk_bf16_f32 v137, v42, v43\n\t"
      "v_mfma_f32_16x16x32_bf16 v[36:39], a[88:91], a[224:227], v[36:39]\n\t"
      "v_cvt_pk_bf16_f32 v138, v44, v45\n\t"
      "v_cvt_pk_bf16_f32 v139, v46, v47\n\t"
      "s_waitcnt lgkmcnt(9)\n\t"
      "v_mfma_f32_16x16x32_bf16 a[156:159], v[112:115], a[228:231], a[156:159]\n\t"
      "v_cvt_pk_bf16_f32 v140, v48, v49\n\t"
      "v_cvt_pk_bf16_f32 v141, v50, v51\n\t"
      "v_mfma_f32_16x16x32_bf16 a[160:163], v[116:119], a[228:231], a[160:163]\n\t"
      "v_cvt_pk_bf16_f32 v142, v52, v53\n\t"
      "v_cvt_pk_bf16_f32 v143, v54, v55\n\t"
      "ds_read_b128 a[232:235], v56 offset:25344\n\t"
      "v_mfma_f32_4x4x4_16b_bf16 v[40:43], a[180:181], a[92:93], 0\n\t"
      "ds_read_b64_tr_b16 a[100:101], v60 offset:128\n\t"
      "v_mfma_f32_4x4x4_16b_bf16 v[44:47], a[182:183], a[94:95], 0\n\t"
      "ds_read_b64_tr_b16 a[102:103], v60 offset:896\n\t"
      "v_mfma_f32_4x4x4_16b_bf16 v[48:51], a[180:181], a[96:97], 0\n\t"
      "ds_read_b64_tr_b16 a[104:105], v61 offset:128\n\t"
      "v_mfma_f32_4x4x4_16b_bf16 v[52:55], a[182:183], a[98:99], 0\n\t"
      "ds_read_b64_tr_b16 a[106:107], v61 offset:896\n\t"
      "buffer_load_dwordx4 v10, s[84:87], s54 offen offset:1024 lds\n\t"
      "buffer_load_dwordx4 v11, s[84:87], s54 offen offset:2048 lds\n\t"
      "ds_read_b128 a[236:239], v56 offset:37632\n\t"
      "v_mfma_f32_16x16x32_bf16 a[164:167], v[120:123], a[228:231], a[164:167]\n\t"
      "v_cvt_pk_bf16_f32 v40, v40, v41\n\t"
      "v_cvt_pk_bf16_f32 v41, v42, v43\n\t"
      "v_mfma_f32_16x16x32_bf16 a[168:171], v[124:127], a[228:231], a[168:171]\n\t"
      "v_cvt_pk_bf16_f32 v44, v44, v45\n\t"
      "v_cvt_pk_bf16_f32 v45, v46, v47\n\t"
      "v_mfma_f32_16x16x32_bf16 a[172:175], a[84:87], a[228:231], a[172:175]\n\t"
      "v_cvt_pk_bf16_f32 v48, v48, v49\n\t"
      "v_cvt_pk_bf16_f32 v49, v50, v51\n\t"
      "v_mfma_f32_16x16x32_bf16 a[176:179], a[88:91], a[228:231], a[176:179]\n\t"
      "v_cvt_pk_bf16_f32 v52, v52, v53\n\t"
      "v_cvt_pk_bf16_f32 v53, v54, v55\n\t"
      "v_accvgpr_write_b32 a92, v40\n\t"
      "v_accvgpr_write_b32 a93, v41\n\t"
      "v_accvgpr_write_b32 a94, v44\n\t"
      "v_accvgpr_write_b32 a95, v45\n\t"
      "v_accvgpr_write_b32 a96, v48\n\t"
      "v_accvgpr_write_b32 a97, v49\n\t"
      "v_accvgpr_write_b32 a98, v52\n\t"
      "v_accvgpr_write_b32 a99, v53\n\t"
      "s_waitcnt vmcnt(12)\n\t"
      "s_waitcnt lgkmcnt(1)\n\t"
      "; step\n\t"
      "v_mfma_f32_4x4x4_16b_bf16 v[40:43], a[184:185], v[144:145], 0\n\t"
      "ds_read_b128 a[180:183], v14 offset:1536\n\t"
      "v_mfma_f32_4x4x4_16b_bf16 v[44:47], a[186:187], v[146:147], 0\n\t"
      "ds_read_b64_tr_b16 v[160:161], v60 offset:24576\n\t"
      "v_mfma_f32_4x4x4_16b_bf16 v[48:51], a[184:185], v[148:149], 0\n\t"
      "ds_read_b64_tr_b16 v[162:163], v60 offset:25344\n\t"
      "v_mfma_f32_4x4x4_16b_bf16 v[52:55], a[186:187], v[150:151], 0\n\t"
      "ds_read_b64_tr_b16 v[164:165], v61 offset:24576\n\t"
      "ds_read_b64_tr_b16 v[166:167], v61 offset:25344\n\t"
      "s_add_u32 m0, s17, 86528\n\t"
      "s_mov_b32 s54, 221184\n\t"
      "buffer_load_dwordx4 v9, s[84:87], s54 offen lds\n\t"
      "buffer_load_dwordx4 v10, s[84:87], s54 offen offset:1024 lds\n\t"
      "v_mfma_f32_16x16x32_bf16 v[16:19], v[128:131], a[232:235], v[16:19]\n\t"
      "v_cvt_pk_bf16_f32 v144, v40, v41\n\t"
      "v_cvt_pk_bf16_f32 v145, v42, v43\n\t"
      "v_mfma_f32_16x16x32_bf16 v[20:23], v[132:135], a[232:235], v[20:23]\n\t"
      "v_cvt_pk_bf16_f32 v146, v44, v45\n\t"
      "v_cvt_pk_bf16_f32 v147, v46, v47\n\t"
      "v_mfma_f32_16x16x32_bf16 v[24:27], v[136:139], a[232:235], v[24:27]\n\t"
      "v_cvt_pk_bf16_f32 v148, v48, v49\n\t"
      "v_cvt_pk_bf16_f32 v149, v50, v51\n\t"
      "v_mfma_f32_16x16x32_bf16 v[28:31], v[140:143], a[232:235], v[28:31]\n\t"
      "v_cvt_pk_bf16_f32 v150, v52, v53\n\t"
      "v_cvt_pk_bf16_f32 v151, v54, v55\n\t"
      "v_mfma_f32_4x4x4_16b_bf16 v[40:43], a[184:185], v[152:153], 0\n\t"
      "ds_read_b64_tr_b16 v[168:169], v60 offset:24640\n\t"
      "v_mfma_f32_4x4x4_16b_bf16 v[44:47], a[186:187], v[154:155], 0\n\t"
      "ds_read_b64_tr_b16 v[170:171], v60 offset:25408\n\t"
      "v_mfma_f32_4x4x4_16b_bf16 v[48:51], a[184:185], v[156:157], 0\n\t"
      "ds_read_b64_tr_b16 v[172:173], v61 offset:24640\n\t"
      "v_mfma_f32_4x4x4_16b_bf16 v[52:55], a[186:187], v[158:159], 0\n\t"
      "ds_read_b64_tr_b16 v[174:175], v61 offset:25408\n\t"
      "buffer_load_dwordx4 v11, s[84:87], s54 offen offset:2048 lds\n\t"
      "s_add_u32 m0, s17, 89600\n\t"
      "s_mov_b32 s54, 233472\n\t"
      "buffer_load_dwordx4 v9, s[84:87], s54 offen lds\n\t"
      "v_mfma_f32_16x16x32_bf16 v[32:35], a[92:95], a[232:235], v[32:35]\n\t"
      "v_cvt_pk_bf16_f32 v152, v40, v41\n\t"
      "v_cvt_pk_bf16_f32 v153, v42, v43\n\t"
      "v_mfma_f32_16x16x32_bf16 v[36:39], a[96:99], a[232:235], v[36:39]\n\t"
      "v_cvt_pk_bf16_f32 v154, v44, v45\n\t"
      "v_cvt_pk_bf16_f32 v155, v46, v47\n\t"
      "s_waitcnt lgkmcnt(9)\n\t"
      "v_mfma_f32_16x16x32_bf16 a[156:159], v[128:131], a[236:239], a[156:159]\n\t"
      "v_cvt_pk_bf16_f32 v156, v48, v49\n\t"
      "v_cvt_pk_bf16_f32 v157, v50, v51\n\t"
      "v_mfma_f32_16x16x32_bf16 a[160:163], v[132:135], a[236:239], a[160:163]\n\t"
      "v_cvt_pk_bf16_f32 v158, v52, v53\n\t"
      "v_cvt_pk_bf16_f32 v159, v54, v55\n\t"
      "ds_read_b128 a[240:243], v57 offset:25344\n\t"
      "v_mfma_f32_4x4x4_16b_bf16 v[40:43], a[184:185], a[100:101], 0\n\t"
      "ds_read_b64_tr_b16 a[108:109], v60 offset:24704\n\t"
      "v_mfma_f32_4x4x4_16b_bf16 v[44:47], a[186:187], a[102:103], 0\n\t"
      "ds_read_b64_tr_b16 a[110:111], v60 offset:25472\n\t"
      "v_mfma_f32_4x4x4_16b_bf16 v[48:51], a[184:185], a[104:105], 0\n\t"
      "ds_read_b64_tr_b16 a[112:113], v61 offset:24704\n\t"
      "v_mfma_f32_4x4x4_16b_bf16 v[52:55], a[186:187], a[106:107], 0\n\t"
      "ds_read_b64_tr_b16 a[114:115], v61 offset:25472\n\t"
      "buffer_load_dwordx4 v10, s[84:87], s54 offen offset:1024 lds\n\t"
      "buffer_load_dwordx4 v11, s[84:87], s54 offen offset:2048 lds\n\t"
      "ds_read_b128 a[244:247], v57 offset:37632\n\t"
      "v_mfma_f32_16x16x32_bf16 a[164:167], v[136:139], a[236:239], a[164:167]\n\t"
      "v_cvt_pk_bf16_f32 v40, v40, v41\n\t"
      "v_cvt_pk_bf16_f32 v41, v42, v43\n\t"
      "v_mfma_f32_16x16x32_bf16 a[168:171], v[140:143], a[236:239], a[168:171]\n\t"
      "v_cvt_pk_bf16_f32 v44, v44, v45\n\t"
      "v_cvt_pk_bf16_f32 v45, v46, v47\n\t"
      "v_mfma_f32_16x16x32_bf16 a[172:175], a[92:95], a[236:239], a[172:175]\n\t"
      "v_cvt_pk_bf16_f32 v48, v48, v49\n\t"
      "v_cvt_pk_bf16_f32 v49, v50, v51\n\t"
      "v_mfma_f32_16x16x32_bf16 a[176:179], a[96:99], a[236:239], a[176:179]\n\t"
      "v_cvt_pk_bf16_f32 v52, v52, v53\n\t"
      "v_cvt_pk_bf16_f32 v53, v54, v55\n\t"
      "v_accvgpr_write_b32 a100, v40\n\t"
      "v_accvgpr_write_b32 a101, v41\n\t"
      "v_accvgpr_write_b32 a102, v44\n\t"
      "v_accvgpr_write_b32 a103, v45\n\t"
      "v_accvgpr_write_b32 a104, v48\n\t"
      "v_accvgpr_write_b32 a105, v49\n\t"
      "v_accvgpr_write_b32 a106, v52\n\t"
      "v_accvgpr_write_b32 a107, v53\n\t"
      "s_waitcnt vmcnt(12)\n\t"
      "s_waitcnt lgkmcnt(1)\n\t"
      "; step\n\t"
      "v_mfma_f32_4x4x4_16b_bf16 v[40:43], a[180:181], v[160:161], 0\n\t"
      "ds_read_b128 a[184:187], v14 offset:1792\n\t"
      "v_mfma_f32_4x4x4_16b_bf16 v[44:47], a[182:183], v[162:163], 0\n\t"
      "ds_read_b64_tr_b16 v[176:177], v60 offset:49152\n\t"
      "v_mfma_f32_4x4x4_16b_bf16 v[48:51], a[180:181], v[164:165], 0\n\t"
      "ds_read_b64_tr_b16 v[178:179], v60 offset:49920\n\t"
      "v_mfma_f32_4x4x4_16b_bf16 v[52:55], a[182:183], v[166:167], 0\n\t"
      "ds_read_b64_tr_b16 v[180:181], v61 offset:49152\n\t"
      "ds_read_b64_tr_b16 v[182:183], v61 offset:49920\n\t"
      "s_add_u32 m0, s17, 111104\n\t"
      "s_mov_b32 s54, 245760\n\t"
      "buffer_load_dwordx4 v9, s[84:87], s54 offen lds\n\t"
      "buffer_load_dwordx4 v10, s[84:87], s54 offen offset:1024 lds\n\t"
      "v_mfma_f32_16x16x32_bf16 v[16:19], v[144:147], a[240:243], v[16:19]\n\t"
      "v_cvt_pk_bf16_f32 v160, v40, v41\n\t"
      "v_cvt_pk_bf16_f32 v161, v42, v43\n\t"
      "v_mfma_f32_16x16x32_bf16 v[20:23], v[148:151], a[240:243], v[20:23]\n\t"
      "v_cvt_pk_bf16_f32 v162, v44, v45\n\t"
      "v_cvt_pk_bf16_f32 v163, v46, v47\n\t"
      "v_mfma_f32_16x16x32_bf16 v[24:27], v[152:155], a[240:243], v[24:27]\n\t"
      "v_cvt_pk_bf16_f32 v164, v48, v49\n\t"
      "v_cvt_pk_bf16_f32 v165, v50, v51\n\t"
      "v_mfma_f32_16x16x32_bf16 v[28:31], v[156:159], a[240:243], v[28:31]\n\t"
      "v_cvt_pk_bf16_f32 v166, v52, v53\n\t"
      "v_cvt_pk_bf16_f32 v167, v54, v55\n\t"
      "v_mfma_f32_4x4x4_16b_bf16 v[40:43], a[180:181], v[168:169], 0\n\t"
      "ds_read_b64_tr_b16 v[184:185], v60 offset:49216\n\t"
      "v_mfma_f32_4x4x4_16b_bf16 v[44:47], a[182:183], v[170:171], 0\n\t"
      "ds_read_b64_tr_b16 v[186:187], v60 offset:49984\n\t"
      "v_mfma_f32_4x4x4_16b_bf16 v[48:51], a[180:181], v[172:173], 0\n\t"
      "ds_read_b64_tr_b16 v[188:189], v61 offset:49216\n\t"
      "v_mfma_f32_4x4x4_16b_bf16 v[52:55], a[182:183], v[174:175], 0\n\t"
      "ds_read_b64_tr_b16 v[190:191], v61 offset:49984\n\t"
      "buffer_load_dwordx4 v11, s[84:87], s54 offen offset:2048 lds\n\t"
      "s_add_u32 m0, s17, 114176\n\t"
      "s_mov_b32 s54, 258048\n\t"
      "buffer_load_dwordx4 v9, s[84:87], s54 offen lds\n\t"
      "v_mfma_f32_16x16x32_bf16 v[32:35], a[100:103], a[240:243], v[32:35]\n\t"
      "v_cvt_pk_bf16_f32 v168, v40, v41\n\t"
      "v_cvt_pk_bf16_f32 v169, v42, v43\n\t"
      "v_mfma_f32_16x16x32_bf16 v[36:39], a[104:107], a[240:243], v[36:39]\n\t"
      "v_cvt_pk_bf16_f32 v170, v44, v45\n\t"
      "v_cvt_pk_bf16_f32 v171, v46, v47\n\t"
      "s_waitcnt lgkmcnt(9)\n\t"
      "v_mfma_f32_16x16x32_bf16 a[156:159], v[144:147], a[244:247], a[156:159]\n\t"
      "v_cvt_pk_bf16_f32 v172, v48, v49\n\t"
      "v_cvt_pk_bf16_f32 v173, v50, v51\n\t"
      "v_mfma_f32_16x16x32_bf16 a[160:163], v[148:151], a[244:247], a[160:163]\n\t"
      "v_cvt_pk_bf16_f32 v174, v52, v53\n\t"
      "v_cvt_pk_bf16_f32 v175, v54, v55\n\t"
      "ds_read_b128 a[224:227], v58 offset:25344\n\t"
      "v_mfma_f32_4x4x4_16b_bf16 v[40:43], a[180:181], a[108:109], 0\n\t"
      "ds_read_b64_tr_b16 a[116:117], v60 offset:49280\n\t"
      "v_mfma_f32_4x4x4_16b_bf16 v[44:47], a[182:183], a[110:111], 0\n\t"
      "ds_read_b64_tr_b16 a[118:119], v60 offset:50048\n\t"
      "v_mfma_f32_4x4x4_16b_bf16 v[48:51], a[180:181], a[112:113], 0\n\t"
      "ds_read_b64_tr_b16 a[120:121], v61 offset:49280\n\t"
      "v_mfma_f32_4x4x4_16b_bf16 v[52:55], a[182:183], a[114:115], 0\n\t"
      "ds_read_b64_tr_b16 a[122:123], v61 offset:50048\n\t"
      "buffer_load_dwordx4 v10, s[84:87], s54 offen offset:1024 lds\n\t"
      "buffer_load_dwordx4 v11, s[84:87], s54 offen offset:2048 lds\n\t"
      "ds_read_b128 a[228:231], v58 offset:37632\n\t"
      "v_mfma_f32_16x16x32_bf16 a[164:167], v[152:155], a[244:247], a[164:167]\n\t"
      "v_cvt_pk_bf16_f32 v40, v40, v41\n\t"
      "v_cvt_pk_bf16_f32 v41, v42, v43\n\t"
      "v_mfma_f32_16x16x32_bf16 a[168:171], v[156:159], a[244:247], a[168:171]\n\t"
      "v_cvt_pk_bf16_f32 v44, v44, v45\n\t"
      "v_cvt_pk_bf16_f32 v45, v46, v47\n\t"
      "v_mfma_f32_16x16x32_bf16 a[172:175], a[100:103], a[244:247], a[172:175]\n\t"
      "v_cvt_pk_bf16_f32 v48, v48, v49\n\t"
      "v_cvt_pk_bf16_f32 v49, v50, v51\n\t"
      "v_mfma_f32_16x16x32_bf16 a[176:179], a[104:107], a[244:247], a[176:179]\n\t"
      "v_cvt_pk_bf16_f32 v52, v52, v53\n\t"
      "v_cvt_pk_bf16_f32 v53, v54, v55\n\t"
      "v_accvgpr_write_b32 a108, v40\n\t"
      "v_accvgpr_write_b32 a109, v41\n\t"
      "v_accvgpr_write_b32 a110, v44\n\t"
      "v_accvgpr_write_b32 a111, v45\n\t"
      "v_accvgpr_write_b32 a112, v48\n\t"
      "v_accvgpr_write_b32 a113, v49\n\t"
      "v_accvgpr_write_b32 a114, v52\n\t"
      "v_accvgpr_write_b32 a115, v53\n\t"
      "s_waitcnt vmcnt(12)\n\t"
      "s_waitcnt lgkmcnt(1)\n\t"
      "; step\n\t"
      "v_mfma_f32_4x4x4_16b_bf16 v[40:43], a[184:185], v[176:177], 0\n\t"
      "ds_read_b128 a[180:183], v14 offset:2048\n\t"
      "v_mfma_f32_4x4x4_16b_bf16 v[44:47], a[186:187], v[178:179], 0\n\t"
      "ds_read_b64_tr_b16 v[192:193], v12 offset:0\n\t"
      "v_mfma_f32_4x4x4_16b_bf16 v[48:51], a[184:185], v[180:181], 0\n\t"
      "ds_read_b64_tr_b16 v[194:195], v12 offset:768\n\t"
      "v_mfma_f32_4x4x4_16b_bf16 v[52:55], a[186:187], v[182:183], 0\n\t"
      "ds_read_b64_tr_b16 v[196:197], v13 offset:0\n\t"
      "ds_read_b64_tr_b16 v[198:199], v13 offset:768\n\t"
      "s_add_u32 m0, s17, 135680\n\t"
      "s_mov_b32 s54, 270336\n\t"
      "buffer_load_dwordx4 v9, s[84:87], s54 offen lds\n\t"
      "buffer_load_dwordx4 v10, s[84:87], s54 offen offset:1024 lds\n\t"
      "v_mfma_f32_16x16x32_bf16 v[16:19], v[160:163], a[224:227], v[16:19]\n\t"
      "v_cvt_pk_bf16_f32 v176, v40, v41\n\t"
      "v_cvt_pk_bf16_f32 v177, v42, v43\n\t"
      "v_mfma_f32_16x16x32_bf16 v[20:23], v[164:167], a[224:227], v[20:23]\n\t"
      "v_cvt_pk_bf16_f32 v178, v44, v45\n\t"
      "v_cvt_pk_bf16_f32 v179, v46, v47\n\t"
      "v_mfma_f32_16x16x32_bf16 v[24:27], v[168:171], a[224:227], v[24:27]\n\t"
      "v_cvt_pk_bf16_f32 v180, v48, v49\n\t"
      "v_cvt_pk_bf16_f32 v181, v50, v51\n\t"
      "v_mfma_f32_16x16x32_bf16 v[28:31], v[172:175], a[224:227], v[28:31]\n\t"
      "v_cvt_pk_bf16_f32 v182, v52, v53\n\t"
      "v_cvt_pk_bf16_f32 v183, v54, v55\n\t"
      "v_mfma_f32_4x4x4_16b_bf16 v[40:43], a[184:185], v[184:185], 0\n\t"
      "ds_read_b64_tr_b16 v[200:201], v12 offset:64\n\t"
      "v_mfma_f32_4x4x4_16b_bf16 v[44:47], a[186:187], v[186:187], 0\n\t"
      "ds_read_b64_tr_b16 v[202:203], v12 offset:832\n\t"
      "v_mfma_f32_4x4x4_16b_bf16 v[48:51], a[184:185], v[188:189], 0\n\t"
      "ds_read_b64_tr_b16 v[204:205], v13 offset:64\n\t"
      "v_mfma_f32_4x4x4_16b_bf16 v[52:55], a[186:187], v[190:191], 0\n\t"
      "ds_read_b64_tr_b16 v[206:207], v13 offset:832\n\t"
      "buffer_load_dwordx4 v11, s[84:87], s54 offen offset:2048 lds\n\t"
      "s_add_u32 m0, s17, 138752\n\t"
      "s_mov_b32 s54, 282624\n\t"
      "buffer_load_dwordx4 v9, s[84:87], s54 offen lds\n\t"
      "v_mfma_f32_16x16x32_bf16 v[32:35], a[108:111], a[224:227], v[32:35]\n\t"
      "v_cvt_pk_bf16_f32 v184, v40, v41\n\t"
      "v_cvt_pk_bf16_f32 v185, v42, v43\n\t"
      "v_mfma_f32_16x16x32_bf16 v[36:39], a[112:115], a[224:227], v[36:39]\n\t"
      "v_cvt_pk_bf16_f32 v186, v44, v45\n\t"
      "v_cvt_pk_bf16_f32 v187, v46, v47\n\t"
      "s_waitcnt lgkmcnt(9)\n\t"
      "v_mfma_f32_16x16x32_bf16 a[156:159], v[160:163], a[228:231], a[156:159]\n\t"
      "v_cvt_pk_bf16_f32 v188, v48, v49\n\t"
      "v_cvt_pk_bf16_f32 v189, v50, v51\n\t"
      "v_mfma_f32_16x16x32_bf16 a[160:163], v[164:167], a[228:231], a[160:163]\n\t"
      "v_cvt_pk_bf16_f32 v190, v52, v53\n\t"
      "v_cvt_pk_bf16_f32 v191, v54, v55\n\t"
      "ds_read_b128 a[232:235], v59 offset:25344\n\t"
      "v_mfma_f32_4x4x4_16b_bf16 v[40:43], a[184:185], a[116:117], 0\n\t"
      "ds_read_b64_tr_b16 a[124:125], v12 offset:128\n\t"
      "v_mfma_f32_4x4x4_16b_bf16 v[44:47], a[186:187], a[118:119], 0\n\t"
      "ds_read_b64_tr_b16 a[126:127], v12 offset:896\n\t"
      "v_mfma_f32_4x4x4_16b_bf16 v[48:51], a[184:185], a[120:121], 0\n\t"
      "ds_read_b64_tr_b16 a[128:129], v13 offset:128\n\t"
      "v_mfma_f32_4x4x4_16b_bf16 v[52:55], a[186:187], a[122:123], 0\n\t"
      "ds_read_b64_tr_b16 a[130:131], v13 offset:896\n\t"
      "buffer_load_dwordx4 v10, s[84:87], s54 offen offset:1024 lds\n\t"
      "buffer_load_dwordx4 v11, s[84:87], s54 offen offset:2048 lds\n\t"
      "ds_read_b128 a[236:239], v59 offset:37632\n\t"
      "s_or_b32 s30, s17, s16\n\t"
      "s_cbranch_scc1 .Lno3_%=\n\t"
      "global_load_dword a252, v62, s[12:13] sc1\n\t"
      ".Lno3_%=:\n\t"
      "s_add_u32 m0, s18, 49664\n\t"
      "s_add_u32 s48, s15, 786432\n\t"
      "buffer_load_dwordx4 v2, s[40:43], s48 offen nt lds\n\t"
      "buffer_load_dwordx4 v3, s[40:43], s48 offen offset:1024 nt lds\n\t"
      "buffer_load_dwordx4 v4, s[40:43], s48 offen offset:2048 nt lds\n\t"
      "v_mfma_f32_16x16x32_bf16 a[164:167], v[168:171], a[228:231], a[164:167]\n\t"
      "v_cvt_pk_bf16_f32 v40, v40, v41\n\t"
      "v_cvt_pk_bf16_f32 v41, v42, v43\n\t"
      "v_mfma_f32_16x16x32_bf16 a[168:171], v[172:175], a[228:231], a[168:171]\n\t"
      "v_cvt_pk_bf16_f32 v44, v44, v45\n\t"
      "v_cvt_pk_bf16_f32 v45, v46, v47\n\t"
      "v_mfma_f32_16x16x32_bf16 a[172:175], a[108:111], a[228:231], a[172:175]\n\t"
      "v_cvt_pk_bf16_f32 v48, v48, v49\n\t"
      "v_cvt_pk_bf16_f32 v49, v50, v51\n\t"
      "v_mfma_f32_16x16x32_bf16 a[176:179], a[112:115], a[228:231], a[176:179]\n\t"
      "v_cvt_pk_bf16_f32 v52, v52, v53\n\t"
      "v_cvt_pk_bf16_f32 v53, v54, v55\n\t"
      "v_accvgpr_write_b32 a116, v40\n\t"
      "v_accvgpr_write_b32 a117, v41\n\t"
      "v_accvgpr_write_b32 a118, v44\n\t"
      "v_accvgpr_write_b32 a119, v45\n\t"
      "v_accvgpr_write_b32 a120, v48\n\t"
      "v_accvgpr_write_b32 a121, v49\n\t"
      "v_accvgpr_write_b32 a122, v52\n\t"
      "v_accvgpr_write_b32 a123, v53\n\t"
      "s_waitcnt vmcnt(15)\n\t"
      "s_waitcnt lgkmcnt(1)\n\t"
      "; step\n\t"
      "v_mfma_f32_4x4x4_16b_bf16 v[40:43], a[180:181], v[192:193], 0\n\t"
      "ds_read_b128 a[184:187], v14 offset:2304\n\t"
      "v_mfma_f32_4x4x4_16b_bf16 v[44:47], a[182:183], v[194:195], 0\n\t"
      "ds_read_b64_tr_b16 v[208:209], v60 offset:0\n\t"
      "v_mfma_f32_4x4x4_16b_bf16 v[48:51], a[180:181], v[196:197], 0\n\t"
      "ds_read_b64_tr_b16 v[210:211], v60 offset:768\n\t"
      "v_mfma_f32_4x4x4_16b_bf16 v[52:55], a[182:183], v[198:199], 0\n\t"
      "ds_read_b64_tr_b16 v[212:213], v61 offset:0\n\t"
      "ds_read_b64_tr_b16 v[214:215], v61 offset:768\n\t"
      "s_add_u32 m0, s18, 61952\n\t"
      "s_add_u32 s48, s15, 786432\n\t"
      "buffer_load_dwordx4 v5, s[40:43], s48 offen nt lds\n\t"
      "buffer_load_dwordx4 v6, s[40:43], s48 offen offset:1024 nt lds\n\t"
      "v_mfma_f32_16x16x32_bf16 v[16:19], v[176:179], a[232:235], v[16:19]\n\t"
      "v_cvt_pk_bf16_f32 v192, v40, v41\n\t"
      "v_cvt_pk_bf16_f32 v193, v42, v43\n\t"
      "v_mfma_f32_16x16x32_bf16 v[20:23], v[180:183], a[232:235], v[20:23]\n\t"
      "v_cvt_pk_bf16_f32 v194, v44, v45\n\t"
      "v_cvt_pk_bf16_f32 v195, v46, v47\n\t"
      "v_mfma_f32_16x16x32_bf16 v[24:27], v[184:187], a[232:235], v[24:27]\n\t"
      "v_cvt_pk_bf16_f32 v196, v48, v49\n\t"
      "v_cvt_pk_bf16_f32 v197, v50, v51\n\t"
      "v_mfma_f32_16x16x32_bf16 v[28:31], v[188:191], a[232:235], v[28:31]\n\t"
      "v_cvt_pk_bf16_f32 v198, v52, v53\n\t"
      "v_cvt_pk_bf16_f32 v199, v54, v55\n\t"
      "v_mfma_f32_4x4x4_16b_bf16 v[40:43], a[180:181], v[200:201], 0\n\t"
      "ds_read_b64_tr_b16 v[216:217], v60 offset:64\n\t"
      "v_mfma_f32_4x4x4_16b_bf16 v[44:47], a[182:183], v[202:203], 0\n\t"
      "ds_read_b64_tr_b16 v[218:219], v60 offset:832\n\t"
      "v_mfma_f32_4x4x4_16b_bf16 v[48:51], a[180:181], v[204:205], 0\n\t"
      "ds_read_b64_tr_b16 v[220:221], v61 offset:64\n\t"
      "v_mfma_f32_4x4x4_16b_bf16 v[52:55], a[182:183], v[206:207], 0\n\t"
      "ds_read_b64_tr_b16 v[222:223], v61 offset:832\n\t"
      "buffer_load_dwordx4 v7, s[40:43], s48 offen offset:2048 nt lds\n\t"
      "v_mfma_f32_16x16x32_bf16 v[32:35], a[116:119], a[232:235], v[32:35]\n\t"
      "v_cvt_pk_bf16_f32 v200, v40, v41\n\t"
      "v_cvt_pk_bf16_f32 v201, v42, v43\n\t"
      "v_mfma_f32_16x16x32_bf16 v[36:39], a[120:123], a[232:235], v[36:39]\n\t"
      "v_cvt_pk_bf16_f32 v202, v44, v45\n\t"
      "v_cvt_pk_bf16_f32 v203, v46, v47\n\t"
      "s_waitcnt lgkmcnt(9)\n\t"
      "v_mfma_f32_16x16x32_bf16 a[156:159], v[176:179], a[236:239], a[156:159]\n\t"
      "v_cvt_pk_bf16_f32 v204, v48, v49\n\t"
      "v_cvt_pk_bf16_f32 v205, v50, v51\n\t"
      "v_mfma_f32_16x16x32_bf16 a[160:163], v[180:183], a[236:239], a[160:163]\n\t"
      "v_cvt_pk_bf16_f32 v206, v52, v53\n\t"
      "v_cvt_pk_bf16_f32 v207, v54, v55\n\t"
      "ds_read_b128 a[240:243], v56 offset:25600\n\t"
      "v_mfma_f32_4x4x4_16b_bf16 v[40:43], a[180:181], a[124:125], 0\n\t"
      "ds_read_b64_tr_b16 a[132:133], v60 offset:128\n\t"
      "v_mfma_f32_4x4x4_16b_bf16 v[44:47], a[182:183], a[126:127], 0\n\t"
      "ds_read_b64_tr_b16 a[134:135], v60 offset:896\n\t"
      "v_mfma_f32_4x4x4_16b_bf16 v[48:51], a[180:181], a[128:129], 0\n\t"
      "ds_read_b64_tr_b16 a[136:137], v61 offset:128\n\t"
      "v_mfma_f32_4x4x4_16b_bf16 v[52:55], a[182:183], a[130:131], 0\n\t"
      "ds_read_b64_tr_b16 a[138:139], v61 offset:896\n\t"
      "ds_read_b128 a[244:247], v56 offset:37888\n\t"
      "v_mfma_f32_16x16x32_bf16 a[164:167], v[184:187], a[236:239], a[164:167]\n\t"
      "v_cvt_pk_bf16_f32 v40, v40, v41\n\t"
      "v_cvt_pk_bf16_f32 v41, v42, v43\n\t"
      "v_mfma_f32_16x16x32_bf16 a[168:171], v[188:191], a[236:239], a[168:171]\n\t"
      "v_cvt_pk_bf16_f32 v44, v44, v45\n\t"
      "v_cvt_pk_bf16_f32 v45, v46, v47\n\t"
      "v_mfma_f32_16x16x32_bf16 a[172:175], a[116:119], a[236:239], a[172:175]\n\t"
      "v_cvt_pk_bf16_f32 v48, v48, v49\n\t"
      "v_cvt_pk_bf16_f32 v49, v50, v51\n\t"
      "v_mfma_f32_16x16x32_bf16 a[176:179], a[120:123], a[236:239], a[176:179]\n\t"
      "v_cvt_pk_bf16_f32 v52, v52, v53\n\t"
      "v_cvt_pk_bf16_f32 v53, v54, v55\n\t"
      "v_accvgpr_write_b32 a124, v40\n\t"
      "v_accvgpr_write_b32 a125, v41\n\t"
      "v_accvgpr_write_b32 a126, v44\n\t"
      "v_accvgpr_write_b32 a127, v45\n\t"
      "v_accvgpr_write_b32 a128, v48\n\t"
      "v_accvgpr_write_b32 a129, v49\n\t"
      "v_accvgpr_write_b32 a130, v52\n\t"
      "v_accvgpr_write_b32 a131, v53\n\t"
      "s_waitcnt vmcnt(12)\n\t"
      "s_waitcnt lgkmcnt(1)\n\t"
      "; step\n\t"
      "v_mfma_f32_4x4x4_16b_bf16 v[40:43], a[184:185], v[208:209], 0\n\t"
      "ds_read_b128 a[180:183], v14 offset:2560\n\t"
      "v_mfma_f32_4x4x4_16b_bf16 v[44:47], a[186:187], v[210:211], 0\n\t"
      "ds_read_b64_tr_b16 v[224:225], v60 offset:24576\n\t"
      "v_mfma_f32_4x4x4_16b_bf16 v[48:51], a[184:185], v[212:213], 0\n\t"
      "ds_read_b64_tr_b16 v[226:227], v60 offset:25344\n\t"
      "v_mfma_f32_4x4x4_16b_bf16 v[52:55], a[186:187], v[214:215], 0\n\t"
      "ds_read_b64_tr_b16 v[228:229], v61 offset:24576\n\t"
      "ds_read_b64_tr_b16 v[230:231], v61 offset:25344\n\t"
      "v_mfma_f32_16x16x32_bf16 v[16:19], v[192:195], a[240:243], v[16:19]\n\t"
      "v_cvt_pk_bf16_f32 v208, v40, v41\n\t"
      "v_cvt_pk_bf16_f32 v209, v42, v43\n\t"
      "v_mfma_f32_16x16x32_bf16 v[20:23], v[196:199], a[240:243], v[20:23]\n\t"
      "v_cvt_pk_bf16_f32 v210, v44, v45\n\t"
      "v_cvt_pk_bf16_f32 v211, v46, v47\n\t"
      "v_mfma_f32_16x16x32_bf16 v[24:27], v[200:203], a[240:243], v[24:27]\n\t"
      "v_cvt_pk_bf16_f32 v212, v48, v49\n\t"
      "v_cvt_pk_bf16_f32 v213, v50, v51\n\t"
      "v_mfma_f32_16x16x32_bf16 v[28:31], v[204:207], a[240:243], v[28:31]\n\t"
      "v_cvt_pk_bf16_f32 v214, v52, v53\n\t"
      "v_cvt_pk_bf16_f32 v215, v54, v55\n\t"
      "v_mfma_f32_4x4x4_16b_bf16 v[40:43], a[184:185], v[216:217], 0\n\t"
      "ds_read_b64_tr_b16 v[232:233], v60 offset:24640\n\t"
      "v_mfma_f32_4x4x4_16b_bf16 v[44:47], a[186:187], v[218:219], 0\n\t"
      "ds_read_b64_tr_b16 v[234:235], v60 offset:25408\n\t"
      "v_mfma_f32_4x4x4_16b_bf16 v[48:51], a[184:185], v[220:221], 0\n\t"
      "ds_read_b64_tr_b16 v[236:237], v61 offset:24640\n\t"
      "v_mfma_f32_4x4x4_16b_bf16 v[52:55], a[186:187], v[222:223], 0\n\t"
      "ds_read_b64_tr_b16 v[238:239], v61 offset:25408\n\t"
      "v_mfma_f32_16x16x32_bf16 v[32:35], a[124:127], a[240:243], v[32:35]\n\t"
      "v_cvt_pk_bf16_f32 v216, v40, v41\n\t"
      "v_cvt_pk_bf16_f32 v217, v42, v43\n\t"
      "v_mfma_f32_16x16x32_bf16 v[36:39], a[128:131], a[240:243], v[36:39]\n\t"
      "v_cvt_pk_bf16_f32 v218, v44, v45\n\t"
      "v_cvt_pk_bf16_f32 v219, v46, v47\n\t"
      "s_waitcnt lgkmcnt(9)\n\t"
      "v_mfma_f32_16x16x32_bf16 a[156:159], v[192:195], a[244:247], a[156:159]\n\t"
      "v_cvt_pk_bf16_f32 v220, v48, v49\n\t"
      "v_cvt_pk_bf16_f32 v221, v50, v51\n\t"
      "v_mfma_f32_16x16x32_bf16 a[160:163], v[196:199], a[244:247], a[160:163]\n\t"
      "v_cvt_pk_bf16_f32 v222, v52, v53\n\t"
      "v_cvt_pk_bf16_f32 v223, v54, v55\n\t"
      "ds_read_b128 a[224:227], v57 offset:25600\n\t"
      "v_mfma_f32_4x4x4_16b_bf16 v[40:43], a[184:185], a[132:133], 0\n\t"
      "ds_read_b64_tr_b16 a[140:141], v60 offset:24704\n\t"
      "v_mfma_f32_4x4x4_16b_bf16 v[44:47], a[186:187], a[134:135], 0\n\t"
      "ds_read_b64_tr_b16 a[142:143], v60 offset:25472\n\t"
      "v_mfma_f32_4x4x4_16b_bf16 v[48:51], a[184:185], a[136:137], 0\n\t"
      "ds_read_b64_tr_b16 a[144:145], v61 offset:24704\n\t"
      "v_mfma_f32_4x4x4_16b_bf16 v[52:55], a[186:187], a[138:139], 0\n\t"
      "ds_read_b64_tr_b16 a[146:147], v61 offset:25472\n\t"
      "ds_read_b128 a[228:231], v57 offset:37888\n\t"
      "v_mfma_f32_16x16x32_bf16 a[164:167], v[200:203], a[244:247], a[164:167]\n\t"
      "v_cvt_pk_bf16_f32 v40, v40, v41\n\t"
      "v_cvt_pk_bf16_f32 v41, v42, v43\n\t"
      "v_mfma_f32_16x16x32_bf16 a[168:171], v[204:207], a[244:247], a[168:171]\n\t"
      "v_cvt_pk_bf16_f32 v44, v44, v45\n\t"
      "v_cvt_pk_bf16_f32 v45, v46, v47\n\t"
      "v_mfma_f32_16x16x32_bf16 a[172:175], a[124:127], a[244:247], a[172:175]\n\t"
      "v_cvt_pk_bf16_f32 v48, v48, v49\n\t"
      "v_cvt_pk_bf16_f32 v49, v50, v51\n\t"
      "v_mfma_f32_16x16x32_bf16 a[176:179], a[128:131], a[244:247], a[176:179]\n\t"
      "v_cvt_pk_bf16_f32 v52, v52, v53\n\t"
      "v_cvt_pk_bf16_f32 v53, v54, v55\n\t"
      "v_accvgpr_write_b32 a132, v40\n\t"
      "v_accvgpr_write_b32 a133, v41\n\t"
      "v_accvgpr_write_b32 a134, v44\n\t"
      "v_accvgpr_write_b32 a135, v45\n\t"
      "v_accvgpr_write_b32 a136, v48\n\t"
      "v_accvgpr_write_b32 a137, v49\n\t"
      "v_accvgpr_write_b32 a138, v52\n\t"
      "v_accvgpr_write_b32 a139, v53\n\t"
      "s_waitcnt vmcnt(6)\n\t"
      "s_waitcnt lgkmcnt(1)\n\t"
      "; step\n\t"
      "s_memtime s[68:69]\n\t"
      "s_memrealtime s[70:71]\n\t"
      "v_mfma_f32_4x4x4_16b_bf16 v[40:43], a[180:181], v[224:225], 0\n\t"
      "ds_read_b128 a[184:187], v14 offset:2816\n\t"
      "v_mfma_f32_4x4x4_16b_bf16 v[44:47], a[182:183], v[226:227], 0\n\t"
      "ds_read_b64_tr_b16 v[240:241], v60 offset:49152\n\t"
      "v_mfma_f32_4x4x4_16b_bf16 v[48:51], a[180:181], v[228:229], 0\n\t"
      "ds_read_b64_tr_b16 v[242:243], v60 offset:49920\n\t"
      "v_mfma_f32_4x4x4_16b_bf16 v[52:55], a[182:183], v[230:231], 0\n\t"
      "ds_read_b64_tr_b16 v[244:245], v61 offset:49152\n\t"
      "ds_read_b64_tr_b16 v[246:247], v61 offset:49920\n\t"
      "v_mfma_f32_16x16x32_bf16 v[16:19], v[208:211], a[224:227], v[16:19]\n\t"
      "v_cvt_pk_bf16_f32 v224, v40, v41\n\t"
      "v_cvt_pk_bf16_f32 v225, v42, v43\n\t"
      "v_mfma_f32_16x16x32_bf16 v[20:23], v[212:215], a[224:227], v[20:23]\n\t"
      "v_cvt_pk_bf16_f32 v226, v44, v45\n\t"
      "v_cvt_pk_bf16_f32 v227, v46, v47\n\t"
      "v_mfma_f32_16x16x32_bf16 v[24:27], v[216:219], a[224:227], v[24:27]\n\t"
      "v_cvt_pk_bf16_f32 v228, v48, v49\n\t"
      "v_cvt_pk_bf16_f32 v229, v50, v51\n\t"
      "v_mfma_f32_16x16x32_bf16 v[28:31], v[220:223], a[224:227], v[28:31]\n\t"
      "v_cvt_pk_bf16_f32 v230, v52, v53\n\t"
      "v_cvt_pk_bf16_f32 v231, v54, v55\n\t"
      "v_mfma_f32_4x4x4_16b_bf16 v[40:43], a[180:181], v[232:233], 0\n\t"
      "ds_read_b64_tr_b16 v[248:249], v60 offset:49216\n\t"
      "v_mfma_f32_4x4x4_16b_bf16 v[44:47], a[182:183], v[234:235], 0\n\t"
      "ds_read_b64_tr_b16 v[250:251], v60 offset:49984\n\t"
      "v_mfma_f32_4x4x4_16b_bf16 v[48:51], a[180:181], v[236:237], 0\n\t"
      "ds_read_b64_tr_b16 v[252:253], v61 offset:49216\n\t"
      "v_mfma_f32_4x4x4_16b_bf16 v[52:55], a[182:183], v[238:239], 0\n\t"
      "ds_read_b64_tr_b16 v[254:255], v61 offset:49984\n\t"
      "v_mfma_f32_16x16x32_bf16 v[32:35], a[132:135], a[224:227], v[32:35]\n\t"
      "v_cvt_pk_bf16_f32 v232, v40, v41\n\t"
      "v_cvt_pk_bf16_f32 v233, v42, v43\n\t"
      "v_mfma_f32_16x16x32_bf16 v[36:39], a[136:139], a[224:227], v[36:39]\n\t"
      "v_cvt_pk_bf16_f32 v234, v44, v45\n\t"
      "v_cvt_pk_bf16_f32 v235, v46, v47\n\t"
      "s_waitcnt lgkmcnt(9)\n\t"
      "v_mfma_f32_16x16x32_bf16 a[156:159], v[208:211], a[228:231], a[156:159]\n\t"
      "v_cvt_pk_bf16_f32 v236, v48, v49\n\t"
      "v_cvt_pk_bf16_f32 v237, v50, v51\n\t"
      "v_mfma_f32_16x16x32_bf16 a[160:163], v[212:215], a[228:231], a[160:163]\n\t"
      "v_cvt_pk_bf16_f32 v238, v52, v53\n\t"
      "v_cvt_pk_bf16_f32 v239, v54, v55\n\t"
      "ds_read_b128 a[232:235], v58 offset:25600\n\t"
      "v_mfma_f32_4x4x4_16b_bf16 v[40:43], a[180:181], a[140:141], 0\n\t"
      "ds_read_b64_tr_b16 a[148:149], v60 offset:49280\n\t"
      "v_mfma_f32_4x4x4_16b_bf16 v[44:47], a[182:183], a[142:143], 0\n\t"
      "ds_read_b64_tr_b16 a[150:151], v60 offset:50048\n\t"
      "v_mfma_f32_4x4x4_16b_bf16 v[48:51], a[180:181], a[144:145], 0\n\t"
      "ds_read_b64_tr_b16 a[152:153], v61 offset:49280\n\t"
      "v_mfma_f32_4x4x4_16b_bf16 v[52:55], a[182:183], a[146:147], 0\n\t"
      "ds_read_b64_tr_b16 a[154:155], v61 offset:50048\n\t"
      "v_mfma_f32_16x16x32_bf16 a[164:167], v[216:219], a[228:231], a[164:167]\n\t"
      "v_cvt_pk_bf16_f32 v40, v40, v41\n\t"
      "v_cvt_pk_bf16_f32 v41, v42, v43\n\t"
      "v_mfma_f32_16x16x32_bf16 a[168:171], v[220:223], a[228:231], a[168:171]\n\t"
      "v_cvt_pk_bf16_f32 v44, v44, v45\n\t"
      "v_cvt_pk_bf16_f32 v45, v46, v47\n\t"
      "v_mfma_f32_16x16x32_bf16 a[172:175], a[132:135], a[228:231], a[172:175]\n\t"
      "v_cvt_pk_bf16_f32 v48, v48, v49\n\t"
      "v_cvt_pk_bf16_f32 v49, v50, v51\n\t"
      "v_mfma_f32_16x16x32_bf16 a[176:179], a[136:139], a[228:231], a[176:179]\n\t"
      "v_cvt_pk_bf16_f32 v52, v52, v53\n\t"
      "v_cvt_pk_bf16_f32 v53, v54, v55\n\t"
      "v_accvgpr_write_b32 a140, v40\n\t"
      "v_accvgpr_write_b32 a141, v41\n\t"
      "v_accvgpr_write_b32 a142, v44\n\t"
      "v_accvgpr_write_b32 a143, v45\n\t"
      "v_accvgpr_write_b32 a144, v48\n\t"
      "v_accvgpr_write_b32 a145, v49\n\t"
      "v_accvgpr_write_b32 a146, v52\n\t"
      "v_accvgpr_write_b32 a147, v53\n\t"
      "s_waitcnt lgkmcnt(0)\n\t"
      "; step\n\t"
      "v_mfma_f32_4x4x4_16b_bf16 v[40:43], a[184:185], v[240:241], 0\n\t"
      "v_mfma_f32_4x4x4_16b_bf16 v[44:47], a[186:187], v[242:243], 0\n\t"
      "v_mfma_f32_4x4x4_16b_bf16 v[48:51], a[184:185], v[244:245], 0\n\t"
      "v_mfma_f32_4x4x4_16b_bf16 v[52:55], a[186:187], v[246:247], 0\n\t"
      "v_mfma_f32_16x16x32_bf16 v[16:19], v[224:227], a[232:235], v[16:19]\n\t"
      "s_nop 0\n\t"
      "v_cvt_pk_bf16_f32 v240, v40, v41\n\t"
      "v_cvt_pk_bf16_f32 v241, v42, v43\n\t"
      "v_mfma_f32_16x16x32_bf16 v[20:23], v[228:231], a[232:235], v[20:23]\n\t"
      "v_cvt_pk_bf16_f32 v242, v44, v45\n\t"
      "v_cvt_pk_bf16_f32 v243, v46, v47\n\t"
      "v_cvt_pk_bf16_f32 v244, v48, v49\n\t"
      "v_cvt_pk_bf16_f32 v245, v50, v51\n\t"
      "v_cvt_pk_bf16_f32 v246, v52, v53\n\t"
      "v_cvt_pk_bf16_f32 v247, v54, v55\n\t"
      "v_mfma_f32_4x4x4_16b_bf16 v[40:43], a[184:185], v[248:249], 0\n\t"
      "v_mfma_f32_4x4x4_16b_bf16 v[44:47], a[186:187], v[250:251], 0\n\t"
      "v_mfma_f32_4x4x4_16b_bf16 v[48:51], a[184:185], v[252:253], 0\n\t"
      "v_mfma_f32_4x4x4_16b_bf16 v[52:55], a[186:187], v[254:255], 0\n\t"
      "v_mfma_f32_16x16x32_bf16 v[24:27], v[232:235], a[232:235], v[24:27]\n\t"
      "s_nop 0\n\t"
      "v_cvt_pk_bf16_f32 v248, v40, v41\n\t"
      "v_cvt_pk_bf16_f32 v249, v42, v43\n\t"
      "v_mfma_f32_16x16x32_bf16 v[28:31], v[236:239], a[232:235], v[28:31]\n\t"
      "v_cvt_pk_bf16_f32 v250, v44, v45\n\t"
      "v_cvt_pk_bf16_f32 v251, v46, v47\n\t"
      "v_cvt_pk_bf16_f32 v252, v48, v49\n\t"
      "v_cvt_pk_bf16_f32 v253, v50, v51\n\t"
      "v_cvt_pk_bf16_f32 v254, v52, v53\n\t"
      "v_cvt_pk_bf16_f32 v255, v54, v55\n\t"
      "ds_read_b128 a[236:239], v59 offset:25600\n\t"
      "v_mfma_f32_4x4x4_16b_bf16 v[40:43], a[184:185], a[148:149], 0\n\t"
      "v_mfma_f32_4x4x4_16b_bf16 v[44:47], a[186:187], a[150:151], 0\n\t"
      "v_mfma_f32_4x4x4_16b_bf16 v[48:51], a[184:185], a[152:153], 0\n\t"
      "v_mfma_f32_4x4x4_16b_bf16 v[52:55], a[186:187], a[154:155], 0\n\t"
      "ds_read_b128 a[240:243], v58 offset:37888\n\t"
      "ds_read_b128 a[244:247], v59 offset:37888\n\t"
      "v_mfma_f32_16x16x32_bf16 v[32:35], a[140:143], a[232:235], v[32:35]\n\t"
      "v_cvt_pk_bf16_f32 v40, v40, v41\n\t"
      "v_cvt_pk_bf16_f32 v41, v42, v43\n\t"
      "v_mfma_f32_16x16x32_bf16 v[36:39], a[144:147], a[232:235], v[36:39]\n\t"
      "v_cvt_pk_bf16_f32 v44, v44, v45\n\t"
      "v_cvt_pk_bf16_f32 v45, v46, v47\n\t"
      "v_cvt_pk_bf16_f32 v48, v48, v49\n\t"
      "v_cvt_pk_bf16_f32 v49, v50, v51\n\t"
      "v_cvt_pk_bf16_f32 v52, v52, v53\n\t"
      "v_cvt_pk_bf16_f32 v53, v54, v55\n\t"
      "v_accvgpr_write_b32 a148, v40\n\t"
      "v_accvgpr_write_b32 a149, v41\n\t"
      "v_accvgpr_write_b32 a150, v44\n\t"
      "v_accvgpr_write_b32 a151, v45\n\t"
      "v_accvgpr_write_b32 a152, v48\n\t"
      "v_accvgpr_write_b32 a153, v49\n\t"
      "v_accvgpr_write_b32 a154, v52\n\t"
      "v_accvgpr_write_b32 a155, v53\n\t"
      "s_waitcnt lgkmcnt(2)\n\t"
      "v_mfma_f32_16x16x32_bf16 v[16:19], v[240:243], a[236:239], v[16:19]\n\t"
      "s_add_u32 s49, s15, 0\n\t"
      "s_mov_b32 s50, 98816\n\t"
      "s_mov_b32 s51, 61952\n\t"
      "v_mfma_f32_16x16x32_bf16 v[20:23], v[244:247], a[236:239], v[20:23]\n\t"
      "s_mov_b32 s58, s14\n\t"
      "s_add_u32 s56, s14, 2359296\n\t"
      "v_mfma_f32_16x16x32_bf16 v[24:27], v[248:251], a[236:239], v[24:27]\n\t"
      "s_movk_i32 s39, 96\n\t"
      "s_movk_i32 s29, 71\n\t"
      "s_mov_b32 s32, 1947275679\n\t"
      "v_mfma_f32_16x16x32_bf16 v[28:31], v[252:255], a[236:239], v[28:31]\n\t"
      "s_mov_b32 s33, 143165559\n\t"
      "s_mov_b32 s34, 1950421356\n\t"
      "s_mov_b32 s35, 8947847\n\t"
      "v_mfma_f32_16x16x32_bf16 v[32:35], a[148:151], a[236:239], v[32:35]\n\t"
      "v_mov_b32 v52, 163328\n\t"
      "v_mfma_f32_16x16x32_bf16 v[36:39], a[152:155], a[236:239], v[36:39]\n\t"
      "s_cmp_lg_u32 s17, 0\n\t"
      "s_cselect_b32 s39, 0, s39\n\t"
      "s_cbranch_scc1 .Lno4_%=\n\t"
      "v_accvgpr_read_b32 v41, a248\n\t"
      "v_mov_b32 v51, 0x4040\n\t"
      "s_movk_i32 s52, 2000\n\t"
      "s_mov_b32 s91, 0\n\t"
      "s_waitcnt vmcnt(6) \n\t"
      "v_accvgpr_read_b32 v40, a252\n\t"
      ".Lcheck_%=:\n\t"
      "v_cmp_ne_u32 s[54:55], v40, v63\n\t"
      "s_cmp_lg_u32 s16, 0\n\t"
      "v_add_u32_dpp v42, v40, v40 quad_perm:[2,3,0,1] row_mask:0xf bank_mask:0xf\n\t"
      "s_cselect_b32 s30, -1, s54\n\t"
      "s_cmp_lg_u32 s30, -1\n\t"
      "s_cbranch_scc1 .Lwait_%=\n\t"
      ".Lwaited_%=:\n\t"
      "v_add_u32_dpp v42, v42, v42 quad_perm:[1,0,3,2] row_mask:0xf bank_mask:0xf\n\t"
      "s_bfe_u32 s30, s19, 0x30008\n\t"
      "s_bfe_u32 s31, s19, 0x10003\n\t"
      "s_lshl1_add_u32 s30, s30, s31\n\t"
      "s_lshl_b32 s30, s30, 1\n\t"
      "v_mov_b32 v43, v42\n\t"
      "v_subrev_u32_dpp v43, v42, v51 row_half_mirror row_mask:0x3 bank_mask:0xf\n\t"
      "v_lshl_add_u32 v42, v42, 1, v43\n\t"
      "v_readlane_b32 s31, v40, s30\n\t"
      "v_add_u32 v42, 0x1f1f, v42\n\t"
      "v_and_b32 v43, 128, v42\n\t"
      "v_and_b32 v44, 32768, v42\n\t"
      "v_cmp_ne_u32 s[88:89], 0, v43\n\t"
      "v_cmp_ne_u32 s[36:37], 0, v44\n\t"
      "s_bitcmp1_b32 s19, 2\n\t"
      "s_cselect_b32 s30, 0x3ffff, 0\n\t"
      "s_bitcmp1_b32 s31, 16\n\t"
      "s_cselect_b32 s31, 262140, 0\n\t"
      "s_xor_b32 s30, s30, s31\n\t"
      "v_lshrrev_b32 v41, v41, s88\n\t"
      "v_and_b32 v41, 1, v41\n\t"
      "v_cmp_ne_u32 vcc, 0, v41\n\t"
      "s_nop 0\n\t"
      "s_and_b32 s31, vcc_lo, 262140\n\t"
      "s_xor_b32 s31, s31, s30\n\t"
      "s_or_b32 s31, s31, s91\n\t"
      "s_and_b32 s30, s16, 0x3ffff\n\t"
      "s_cmp_lg_u32 s16, 0\n\t"
      "s_cselect_b32 s31, s30, s31\n\t"
      "v_mov_b32 v53, s31\n\t"
      "ds_write_b32 v52, v53\n\t"
      "s_xor_b32 s36, s37, s89\n\t"
      "s_lshr_b32 s30, s55, 1\n\t"
      "s_and_b32 s55, s55, s30\n\t"
      "s_lshr_b32 s30, s55, 2\n\t"
      "s_and_b32 s55, s55, s30\n\t"
      "s_and_b32 s36, s36, s55\n\t"
      "s_and_b32 s36, s36, 0x11111111\n\t"
      "s_bcnt1_i32_b32 s36, s36\n\t"
      "s_lshl_b32 s30, s36, 2\n\t"
      "s_mov_b32 s54, 1412636928\n\t"
      "s_mov_b32 s55, 5\n\t"
      "s_lshr_b64 s[54:55], s[54:55], s30\n\t"
      "s_and_b32 s30, s54, 15\n\t"
      "s_add_u32 s29, s29, s30\n\t"
      ".Lno4_%=:\n\t"
      "v_accvgpr_read_b32 v9, a213\n\t"
      "v_accvgpr_read_b32 v10, a214\n\t"
      "v_accvgpr_read_b32 v11, a215\n\t"
      "v_accvgpr_read_b32 v12, a216\n\t"
      "v_accvgpr_read_b32 v13, a217\n\t"
      "v_accvgpr_read_b32 v14, a218\n\t"
      "v_accvgpr_read_b32 v8, a219\n\t"
      "v_accvgpr_read_b32 v60, a220\n\t"
      "v_accvgpr_read_b32 v61, a221\n\t"
      "v_add_u32 v56, 49664, v56\n\t"
      "v_add_u32 v57, 49664, v57\n\t"
      "v_add_u32 v58, 49664, v58\n\t"
      "v_add_u32 v59, 49664, v59\n\t"
      "v_cvt_pk_bf16_f32 v40, v16, v17\n\t"
      "v_cvt_pk_bf16_f32 v41, v18, v19\n\t"
      "v_cvt_pk_bf16_f32 v42, v20, v21\n\t"
      "v_cvt_pk_bf16_f32 v43, v22, v23\n\t"
      "v_cvt_pk_bf16_f32 v44, v24, v25\n\t"
      "v_cvt_pk_bf16_f32 v45, v26, v27\n\t"
      "v_cvt_pk_bf16_f32 v46, v28, v29\n\t"
      "v_cvt_pk_bf16_f32 v47, v30, v31\n\t"
      "v_cvt_pk_bf16_f32 v48, v32, v33\n\t"
      "v_cvt_pk_bf16_f32 v49, v34, v35\n\t"
      "v_cvt_pk_bf16_f32 v50, v36, v37\n\t"
      "v_cvt_pk_bf16_f32 v51, v38, v39\n\t"
      "s_mov_b32 s46, 113246208\n\t"
      "s_mov_b32 s52, 15\n\t"
      "s_add_u32 s90, s90, 0x2000000\n\t"
      "s_waitcnt vmcnt(3)\n\t"
      "s_waitcnt lgkmcnt(0)\n\t"
      "s_barrier\n\t"
      "v_mfma_f32_16x16x32_bf16 a[156:159], v[224:227], a[240:243], a[156:159]\n\t"
      "ds_read_b32 v63, v52\n\t"
      "ds_read_b128 a[0:3], v56 offset:0\n\t"
      "s_memrealtime s[36:37]\n\t"
      "v_mfma_f32_16x16x32_bf16 a[160:163], v[228:231], a[240:243], a[160:163]\n\t"
      "ds_read_b128 a[4:7], v57 offset:0\n\t"
      "ds_read_b128 a[8:11], v58 offset:0\n\t"
      "v_mfma_f32_16x16x32_bf16 a[164:167], v[232:235], a[240:243], a[164:167]\n\t"
      "ds_read_b128 a[12:15], v59 offset:0\n\t"
      "ds_read_b128 a[16:19], v56 offset:256\n\t"
      "v_mfma_f32_16x16x32_bf16 a[168:171], v[236:239], a[240:243], a[168:171]\n\t"
      "ds_read_b128 a[20:23], v57 offset:256\n\t"
      "ds_write_b64 v0, v[40:41] offset:0\n\t"
      "v_mfma_f32_16x16x32_bf16 a[172:175], a[140:143], a[240:243], a[172:175]\n\t"
      "ds_write_b64 v0, v[42:43] offset:32\n\t"
      "v_mfma_f32_16x16x32_bf16 a[176:179], a[144:147], a[240:243], a[176:179]\n\t"
      "s_waitcnt lgkmcnt(8)\n\t"
      "v_readfirstlane_b32 s91, v63\n\t"
      "v_mfma_f32_16x16x32_bf16 a[156:159], v[240:243], a[244:247], a[156:159]\n\t"
      "ds_write_b64 v0, v[44:45] offset:64\n\t"
      "s_add_u32 m0, s18, 74240\n\t"
      "v_mfma_f32_16x16x32_bf16 a[160:163], v[244:247], a[244:247], a[160:163]\n\t"
      "s_bitcmp1_b32 s91, 2\n\t"
      "s_cselect_b32 s48, 24576, 0\n\t"
      "s_add_u32 s48, s48, s14\n\t"
      "v_mfma_f32_16x16x32_bf16 a[164:167], v[248:251], a[244:247], a[164:167]\n\t"
      "s_add_u32 s48, s48, 1572864\n\t"
      "buffer_load_dwordx4 v2, s[40:43], s48 offen nt lds\n\t"
      "s_mov_b32 s59, s91\n\t"
      "v_mfma_f32_16x16x32_bf16 a[168:171], v[252:255], a[244:247], a[168:171]\n\t"
      "ds_write_b64 v0, v[46:47] offset:96\n\t"
      "buffer_load_dwordx4 v3, s[40:43], s48 offen offset:1024 nt lds\n\t"
      "v_mfma_f32_16x16x32_bf16 a[172:175], a[148:151], a[244:247], a[172:175]\n\t"
      "ds_write_b64 v0, v[48:49] offset:128\n\t"
      "buffer_load_dwordx4 v4, s[40:43], s48 offen offset:2048 nt lds\n\t"
      "v_mfma_f32_16x16x32_bf16 a[176:179], a[152:155], a[244:247], a[176:179]\n\t"
      "ds_write_b64 v0, v[50:51] offset:160\n\t"
      "s_lshr_b32 s57, s91, 3\n\t"
      "s_bitcmp1_b32 s91, 3\n\t"
      "s_cselect_b32 s48, 24576, 0\n\t"
      "s_add_u32 s48, s48, s14\n\t"
      "s_add_u32 s48, s48, 2359296\n\t"
      "s_or_b32 s30, s17, s16\n\t"
      "s_cbranch_scc1 .Lno5_%=\n\t"
      "s_sub_u32 s68, s68, s64\n\t"
      "s_sub_u32 s70, s70, s66\n\t"
      "s_mul_i32 s64, s68, 100\n\t"
      "s_mul_i32 s66, s70, 1750\n\t"
      "s_sub_i32 s66, s64, s66\n\t"
      "s_ashr_i32 s66, s66, 9\n\t"
      "s_add_i32 s66, s66, 128\n\t"
      "s_max_i32 s66, s66, 1\n\t"
      "s_min_i32 s66, s66, 255\n\t"
      "s_lshl_b32 s66, s66, 17\n\t"
      "s_or_b32 s90, s90, s66\n\t"
      "v_mov_b32 v54, s90\n\t"
      "v_mov_b32 v55, s19\n\t"
      "global_store_dword v55, v54, s[12:13] sc1\n\t"
      ".Lno5_%=:\n\t"
      "s_waitcnt lgkmcnt(11)\n\t"
      "v_mfma_f32_16x16x32_bf16 v[16:19], v[64:67], a[0:3], 0\n\t"
      "ds_read_b128 a[24:27], v58 offset:256\n\t"
      "ds_read_b128 a[28:31], v59 offset:256\n\t"
      "s_waitcnt lgkmcnt(12)\n\t"
      "v_mfma_f32_16x16x32_bf16 v[16:19], v[80:83], a[4:7], v[16:19]\n\t"
      "ds_read_b128 a[32:35], v56 offset:512\n\t"
      "ds_read_b128 a[36:39], v57 offset:512\n\t"
      "s_waitcnt lgkmcnt(13)\n\t"
      "v_mfma_f32_16x16x32_bf16 v[16:19], v[96:99], a[8:11], v[16:19]\n\t"
      "ds_read_b128 a[40:43], v58 offset:512\n\t"
      "ds_read_b128 a[44:47], v59 offset:512\n\t"
      "s_waitcnt lgkmcnt(14)\n\t"
      "v_mfma_f32_16x16x32_bf16 v[16:19], v[112:115], a[12:15], v[16:19]\n\t"
      "v_accvgpr_read_b32 v52, a156\n\t"
      "v_accvgpr_read_b32 v53, a157\n\t"
      "s_waitcnt lgkmcnt(13)\n\t"
      "v_mfma_f32_16x16x32_bf16 v[16:19], v[128:131], a[16:19], v[16:19]\n\t"
      "v_accvgpr_read_b32 v54, a158\n\t"
      "v_accvgpr_read_b32 v55, a159\n\t"
      "s_waitcnt lgkmcnt(12)\n\t"
      "v_mfma_f32_16x16x32_bf16 v[16:19], v[144:147], a[20:23], v[16:19]\n\t"
      "v_cvt_pk_bf16_f32 v40, v52, v53\n\t"
      "v_cvt_pk_bf16_f32 v41, v54, v55\n\t"
      "v_mfma_f32_16x16x32_bf16 v[20:23], v[68:71], a[0:3], 0\n\t"
      "ds_write_b64 v0, v[40:41] offset:12544\n\t"
      "v_mfma_f32_16x16x32_bf16 v[20:23], v[84:87], a[4:7], v[20:23]\n\t"
      "s_waitcnt lgkmcnt(7)\n\t"
      "s_barrier\n\t"
      "v_mfma_f32_16x16x32_bf16 v[20:23], v[100:103], a[8:11], v[20:23]\n\t"
      "ds_read_b128 a[48:51], v8 offset:0\n\t"
      "ds_read_b128 a[52:55], v60 offset:1024\n\t"
      "ds_read_b128 a[56:59], v61 offset:2048\n\t"
      "v_mfma_f32_16x16x32_bf16 v[20:23], v[116:119], a[12:15], v[20:23]\n\t"
      "v_accvgpr_read_b32 v52, a160\n\t"
      "v_accvgpr_read_b32 v53, a161\n\t"
      "v_mfma_f32_16x16x32_bf16 v[20:23], v[132:135], a[16:19], v[20:23]\n\t"
      "v_accvgpr_read_b32 v54, a162\n\t"
      "v_accvgpr_read_b32 v55, a163\n\t"
      "v_mfma_f32_16x16x32_bf16 v[20:23], v[148:151], a[20:23], v[20:23]\n\t"
      "v_cvt_pk_bf16_f32 v42, v52, v53\n\t"
      "v_cvt_pk_bf16_f32 v43, v54, v55\n\t"
      "v_mfma_f32_16x16x32_bf16 v[24:27], v[72:75], a[0:3], 0\n\t"
      "ds_write_b64 v0, v[42:43] offset:12576\n\t"
      "s_waitcnt lgkmcnt(3)\n\t"
      "v_mfma_f32_16x16x32_bf16 v[24:27], v[88:91], a[4:7], v[24:27]\n\t"
      "buffer_store_dwordx4 a[48:51], v9, s[44:47], s49 offen sc1\n\t"
      "v_mfma_f32_16x16x32_bf16 v[24:27], v[104:107], a[8:11], v[24:27]\n\t"
      "v_accvgpr_read_b32 v52, a164\n\t"
      "v_accvgpr_read_b32 v53, a165\n\t"
      "v_mfma_f32_16x16x32_bf16 v[24:27], v[120:123], a[12:15], v[24:27]\n\t"
      "v_accvgpr_read_b32 v54, a166\n\t"
      "v_accvgpr_read_b32 v55, a167\n\t"
      "v_mfma_f32_16x16x32_bf16 v[24:27], v[136:139], a[16:19], v[24:27]\n\t"
      "v_cvt_pk_bf16_f32 v44, v52, v53\n\t"
      "v_cvt_pk_bf16_f32 v45, v54, v55\n\t"
      "v_mfma_f32_16x16x32_bf16 v[24:27], v[152:155], a[20:23], v[24:27]\n\t"
      "ds_write_b64 v0, v[44:45] offset:12608\n\t"
      "s_waitcnt lgkmcnt(3)\n\t"
      "v_mfma_f32_16x16x32_bf16 v[28:31], v[76:79], a[0:3], 0\n\t"
      "buffer_store_dwordx4 a[52:55], v10, s[44:47], s49 offen offset:1024 sc1\n\t"
      "v_mfma_f32_16x16x32_bf16 v[28:31], v[92:95], a[4:7], v[28:31]\n\t"
      "v_accvgpr_read_b32 v52, a168\n\t"
      "v_accvgpr_read_b32 v53, a169\n\t"
      "v_mfma_f32_16x16x32_bf16 v[28:31], v[108:111], a[8:11], v[28:31]\n\t"
      "v_accvgpr_read_b32 v54, a170\n\t"
      "v_accvgpr_read_b32 v55, a171\n\t"
      "v_mfma_f32_16x16x32_bf16 v[28:31], v[124:127], a[12:15], v[28:31]\n\t"
      "v_cvt_pk_bf16_f32 v46, v52, v53\n\t"
      "v_cvt_pk_bf16_f32 v47, v54, v55\n\t"
      "v_mfma_f32_16x16x32_bf16 v[28:31], v[140:143], a[16:19], v[28:31]\n\t"
      "ds_write_b64 v0, v[46:47] offset:12640\n\t"
      "s_waitcnt lgkmcnt(3)\n\t"
      "v_mfma_f32_16x16x32_bf16 v[28:31], v[156:159], a[20:23], v[28:31]\n\t"
      "buffer_store_dwordx4 a[56:59], v11, s[44:47], s49 offen offset:2048 sc1\n\t"
      "v_mfma_f32_16x16x32_bf16 v[32:35], a[60:63], a[0:3], 0\n\t"
      "v_accvgpr_read_b32 v52, a172\n\t"
      "v_accvgpr_read_b32 v53, a173\n\t"
      "v_mfma_f32_16x16x32_bf16 v[32:35], a[68:71], a[4:7], v[32:35]\n\t"
      "v_accvgpr_read_b32 v54, a174\n\t"
      "v_accvgpr_read_b32 v55, a175\n\t"
      "v_mfma_f32_16x16x32_bf16 v[32:35], a[76:79], a[8:11], v[32:35]\n\t"
      "v_cvt_pk_bf16_f32 v48, v52, v53\n\t"
      "v_cvt_pk_bf16_f32 v49, v54, v55\n\t"
      "v_mfma_f32_16x16x32_bf16 v[32:35], a[84:87], a[12:15], v[32:35]\n\t"
      "ds_write_b64 v0, v[48:49] offset:12672\n\t"
      "v_mfma_f32_16x16x32_bf16 v[32:35], a[92:95], a[16:19], v[32:35]\n\t"
      "v_accvgpr_read_b32 v52, a176\n\t"
      "v_accvgpr_read_b32 v53, a177\n\t"
      "v_mfma_f32_16x16x32_bf16 v[32:35], a[100:103], a[20:23], v[32:35]\n\t"
      "v_accvgpr_read_b32 v54, a178\n\t"
      "v_accvgpr_read_b32 v55, a179\n\t"
      "v_mfma_f32_16x16x32_bf16 v[36:39], a[64:67], a[0:3], 0\n\t"
      "v_cvt_pk_bf16_f32 v50, v52, v53\n\t"
      "v_cvt_pk_bf16_f32 v51, v54, v55\n\t"
      "v_mfma_f32_16x16x32_bf16 v[36:39], a[72:75], a[4:7], v[36:39]\n\t"
      "ds_write_b64 v0, v[50:51] offset:12704\n\t"
      "v_mfma_f32_16x16x32_bf16 v[36:39], a[80:83], a[8:11], v[36:39]\n\t"
      "v_add_u32 v52, s51, v15\n\t"
      "v_xor_b32 v53, 64, v52\n\t"
      "v_mfma_f32_16x16x32_bf16 v[36:39], a[88:91], a[12:15], v[36:39]\n\t"
      "v_xor_b32 v54, 128, v52\n\t"
      "v_xor_b32 v55, 192, v52\n\t"
      "v_mfma_f32_16x16x32_bf16 v[36:39], a[96:99], a[16:19], v[36:39]\n\t"
      "s_waitcnt lgkmcnt(0)\n\t"
      "s_add_u32 s38, s36, s39\n\t"
      "s_sub_u32 s28, s36, s28\n\t"
      "s_sub_i32 s28, s28, 515\n\t"
      "s_max_i32 s28, s28, 0\n\t"
      "s_min_i32 s28, s28, 60\n\t"
      "s_and_b32 s39, s32, 15\n\t"
      "s_cselect_b32 s53, s29, 0\n\t"
      "s_memrealtime s[36:37]\n\t"
      "v_mfma_f32_16x16x32_bf16 v[36:39], a[104:107], a[20:23], v[36:39]\n\t"
      "s_add_u32 s39, s39, s53\n\t"
      "s_lshr_b64 s[32:33], s[32:33], 4\n\t"
      "s_add_u32 m0, s18, 86528\n\t"
      "v_mfma_f32_16x16x32_bf16 v[16:19], v[160:163], a[24:27], v[16:19]\n\t"
      "s_bitcmp1_b32 s91, 2\n\t"
      "s_cselect_b32 s54, 24576, 0\n\t"
      "s_add_u32 s54, s54, s14\n\t"
      "s_add_u32 s54, s54, 1572864\n\t"
      "v_mfma_f32_16x16x32_bf16 v[16:19], v[176:179], a[28:31], v[16:19]\n\t"
      "s_waitcnt vmcnt(6)\n\t"
      "s_barrier\n\t"
      "v_mfma_f32_16x16x32_bf16 v[16:19], v[192:195], a[32:35], v[16:19]\n\t"
      "ds_read_b128 a[0:3], v52 offset:0\n\t"
      "ds_read_b128 a[188:191], v8 offset:12544\n\t"
      "buffer_load_dwordx4 v5, s[40:43], s54 offen nt lds\n\t"
      "v_mfma_f32_16x16x32_bf16 v[16:19], v[208:211], a[36:39], v[16:19]\n\t"
      "ds_read_b128 a[4:7], v53 offset:0\n\t"
      "ds_read_b128 a[192:195], v60 offset:13568\n\t"
      "v_mfma_f32_16x16x32_bf16 v[16:19], v[224:227], a[40:43], v[16:19]\n\t"
      "ds_read_b128 a[8:11], v54 offset:0\n\t"
      "ds_read_b128 a[196:199], v61 offset:14592\n\t"
      "buffer_load_dwordx4 v6, s[40:43], s54 offen offset:1024 nt lds\n\t"
      "v_mfma_f32_16x16x32_bf16 v[16:19], v[240:243], a[44:47], v[16:19]\n\t"
      "ds_read_b128 a[12:15], v55 offset:0\n\t"
      "v_mfma_f32_16x16x32_bf16 v[20:23], v[164:167], a[24:27], v[20:23]\n\t"
      "ds_read_b128 a[16:19], v52 offset:256\n\t"
      "buffer_load_dwordx4 v7, s[40:43], s54 offen offset:2048 nt lds\n\t"
      "v_mfma_f32_16x16x32_bf16 v[20:23], v[180:183], a[28:31], v[20:23]\n\t"
      "ds_read_b128 a[20:23], v53 offset:256\n\t"
      "s_add_u32 m0, s18, s50\n\t"
      "v_mfma_f32_16x16x32_bf16 v[20:23], v[196:199], a[32:35], v[20:23]\n\t"
      "s_add_u32 s51, s51, 12288\n\t"
      "s_cmp_eq_u32 s51, 160256\n\t"
      "s_cselect_b32 s51, 25088, s51\n\t"
      "s_waitcnt lgkmcnt(7)\n\t"
      "v_mfma_f32_16x16x32_bf16 v[20:23], v[212:215], a[36:39], v[20:23]\n\t"
      "buffer_store_dwordx4 a[188:191], v12, s[44:47], s49 offen sc1\n\t"
      "v_mfma_f32_16x16x32_bf16 v[20:23], v[228:231], a[40:43], v[20:23]\n\t"
      "v_mfma_f32_16x16x32_bf16 v[20:23], v[244:247], a[44:47], v[20:23]\n\t"
      "s_waitcnt lgkmcnt(0)\n\t"
      "s_sub_u32 s53, s36, s38\n\t"
      "s_cmp_lt_i32 s53, 0\n\t"
      "v_mfma_f32_16x16x32_bf16 v[24:27], v[168:171], a[24:27], v[24:27]\n\t"
      "s_cbranch_scc1 .Lspinf_%=\n\t"
      ".Lspunf_%=:\n\t"
      "s_sub_i32 s53, s53, 30\n\t"
      "s_max_i32 s53, s53, 0\n\t"
      "v_mfma_f32_16x16x32_bf16 v[24:27], v[184:187], a[28:31], v[24:27]\n\t"
      "s_add_u32 s38, s38, s53\n\t"
      "s_add_u32 s38, s38, s39\n\t"
      "s_add_u32 s28, s28, s53\n\t"
      "v_mfma_f32_16x16x32_bf16 v[24:27], v[200:203], a[32:35], v[24:27]\n\t"
      "v_cvt_pk_bf16_f32 v40, v16, v17\n\t"
      "v_cvt_pk_bf16_f32 v41, v18, v19\n\t"
      "v_mfma_f32_16x16x32_bf16 v[24:27], v[216:219], a[36:39], v[24:27]\n\t"
      "ds_write_b64 v0, v[40:41] offset:0\n\t"
      "v_mfma_f32_16x16x32_bf16 v[24:27], v[232:235], a[40:43], v[24:27]\n\t"
      "buffer_load_dwordx4 v2, s[40:43], s48 offen nt lds\n\t"
      "v_mfma_f32_16x16x32_bf16 v[24:27], v[248:251], a[44:47], v[24:27]\n\t"
      "buffer_store_dwordx4 a[192:195], v13, s[44:47], s49 offen offset:1024 sc1\n\t"
      "v_mfma_f32_16x16x32_bf16 v[28:31], v[172:175], a[24:27], v[28:31]\n\t"
      "v_mfma_f32_16x16x32_bf16 v[28:31], v[188:191], a[28:31], v[28:31]\n\t"
      "v_cvt_pk_bf16_f32 v42, v20, v21\n\t"
      "v_cvt_pk_bf16_f32 v43, v22, v23\n\t"
      "v_mfma_f32_16x16x32_bf16 v[28:31], v[204:207], a[32:35], v[28:31]\n\t"
      "ds_write_b64 v0, v[42:43] offset:32\n\t"
      "v_mfma_f32_16x16x32_bf16 v[28:31], v[220:223], a[36:39], v[28:31]\n\t"
      "buffer_load_dwordx4 v3, s[40:43], s48 offen offset:1024 nt lds\n\t"
      "v_mfma_f32_16x16x32_bf16 v[28:31], v[236:239], a[40:43], v[28:31]\n\t"
      "buffer_store_dwordx4 a[196:199], v14, s[44:47], s49 offen offset:2048 sc1\n\t"
      "v_mfma_f32_16x16x32_bf16 v[28:31], v[252:255], a[44:47], v[28:31]\n\t"
      "s_add_u32 s58, s58, 786432\n\t"
      "s_lshr_b32 s59, s59, 1\n\t"
      "v_mfma_f32_16x16x32_bf16 v[32:35], a[108:111], a[24:27], v[32:35]\n\t"
      "s_bitcmp1_b32 s59, 0\n\t"
      "s_cselect_b32 s49, 24576, 0\n\t"
      "s_add_u32 s49, s49, s58\n\t"
      "v_mfma_f32_16x16x32_bf16 v[32:35], a[116:119], a[28:31], v[32:35]\n\t"
      "v_cvt_pk_bf16_f32 v44, v24, v25\n\t"
      "v_cvt_pk_bf16_f32 v45, v26, v27\n\t"
      "v_mfma_f32_16x16x32_bf16 v[32:35], a[124:127], a[32:35], v[32:35]\n\t"
      "ds_write_b64 v0, v[44:45] offset:64\n\t"
      "v_mfma_f32_16x16x32_bf16 v[32:35], a[132:135], a[36:39], v[32:35]\n\t"
      "buffer_load_dwordx4 v4, s[40:43], s48 offen offset:2048 nt lds\n\t"
      "v_mfma_f32_16x16x32_bf16 v[32:35], a[140:143], a[40:43], v[32:35]\n\t"
      "s_add_u32 s50, s50, 12288\n\t"
      "v_mfma_f32_16x16x32_bf16 v[32:35], a[148:151], a[44:47], v[32:35]\n\t"
      "v_cvt_pk_bf16_f32 v46, v28, v29\n\t"
      "v_cvt_pk_bf16_f32 v47, v30, v31\n\t"
      "v_mfma_f32_16x16x32_bf16 v[36:39], a[112:115], a[24:27], v[36:39]\n\t"
      "ds_write_b64 v0, v[46:47] offset:96\n\t"
      "v_mfma_f32_16x16x32_bf16 v[36:39], a[120:123], a[28:31], v[36:39]\n\t"
      "s_cmp_eq_u32 s50, 160256\n\t"
      "s_cselect_b32 s50, 25088, s50\n\t"
      "v_mfma_f32_16x16x32_bf16 v[36:39], a[128:131], a[32:35], v[36:39]\n\t"
      "v_mfma_f32_16x16x32_bf16 v[36:39], a[136:139], a[36:39], v[36:39]\n\t"
      "v_mfma_f32_16x16x32_bf16 v[36:39], a[144:147], a[40:43], v[36:39]\n\t"
      "v_cvt_pk_bf16_f32 v48, v32, v33\n\t"
      "v_cvt_pk_bf16_f32 v49, v34, v35\n\t"
      "v_mfma_f32_16x16x32_bf16 v[36:39], a[152:155], a[44:47], v[36:39]\n\t"
      "ds_write_b64 v0, v[48:49] offset:128\n\t"
      "s_cmp_eq_u32 s17, 6144\n\t"
      "s_cbranch_scc1 .Lmid1_%=\n\t"
      "s_cmp_eq_u32 s17, 12288\n\t"
      "s_cbranch_scc1 .Lmid2_%=\n\t"
      "s_cmp_eq_u32 s17, 18432\n\t"
      "s_cbranch_scc1 .Lmid3_%=\n\t"
      "global_load_dword v63, v62, s[12:13] sc1\n\t"
      "s_branch .Lmid0_%=\n\t"
      ".Lloop0_%=:\n\t"
      "v_mfma_f32_16x16x32_bf16 v[16:19], v[64:67], a[0:3], 0\n\t"
      "ds_read_b128 a[24:27], v58 offset:256\n\t"
      "v_mfma_f32_16x16x32_bf16 v[16:19], v[80:83], a[4:7], v[16:19]\n\t"
      "ds_read_b128 a[28:31], v59 offset:256\n\t"
      "v_mfma_f32_16x16x32_bf16 v[16:19], v[96:99], a[8:11], v[16:19]\n\t"
      "ds_read_b128 a[32:35], v56 offset:512\n\t"
      "v_mfma_f32_16x16x32_bf16 v[16:19], v[112:115], a[12:15], v[16:19]\n\t"
      "ds_read_b128 a[36:39], v57 offset:512\n\t"
      "v_mfma_f32_16x16x32_bf16 v[16:19], v[128:131], a[16:19], v[16:19]\n\t"
      "ds_read_b128 a[40:43], v58 offset:512\n\t"
      "v_mfma_f32_16x16x32_bf16 v[16:19], v[144:147], a[20:23], v[16:19]\n\t"
      "ds_read_b128 a[44:47], v59 offset:512\n\t"
      "v_mfma_f32_16x16x32_bf16 v[20:23], v[68:71], a[0:3], 0\n\t"
      "v_cvt_pk_bf16_f32 v50, v36, v37\n\t"
      "v_cvt_pk_bf16_f32 v51, v38, v39\n\t"
      "v_mfma_f32_16x16x32_bf16 v[20:23], v[84:87], a[4:7], v[20:23]\n\t"
      "ds_write_b64 v0, v[50:51] offset:12704\n\t"
      "v_mfma_f32_16x16x32_bf16 v[20:23], v[100:103], a[8:11], v[20:23]\n\t"
      "v_add_u32 v52, s51, v15\n\t"
      "v_xor_b32 v53, 64, v52\n\t"
      "v_mfma_f32_16x16x32_bf16 v[20:23], v[116:119], a[12:15], v[20:23]\n\t"
      "v_xor_b32 v54, 128, v52\n\t"
      "v_xor_b32 v55, 192, v52\n\t"
      "v_mfma_f32_16x16x32_bf16 v[20:23], v[132:135], a[16:19], v[20:23]\n\t"
      "s_add_u32 s56, s56, 786432\n\t"
      "s_lshr_b32 s57, s57, 1\n\t"
      "v_mfma_f32_16x16x32_bf16 v[20:23], v[148:151], a[20:23], v[20:23]\n\t"
      "s_bitcmp1_b32 s57, 0\n\t"
      "s_cselect_b32 s48, 24576, 0\n\t"
      "s_add_u32 s48, s48, s56\n\t"
      "v_mfma_f32_16x16x32_bf16 v[24:27], v[72:75], a[0:3], 0\n\t"
      "v_mfma_f32_16x16x32_bf16 v[24:27], v[88:91], a[4:7], v[24:27]\n\t"
      "v_mfma_f32_16x16x32_bf16 v[24:27], v[104:107], a[8:11], v[24:27]\n\t"
      "s_waitcnt vmcnt(12)\n\t"
      "s_waitcnt lgkmcnt(0)\n\t"
      "s_barrier\n\t"
      "v_mfma_f32_16x16x32_bf16 v[24:27], v[120:123], a[12:15], v[24:27]\n\t"
      "v_mfma_f32_16x16x32_bf16 v[24:27], v[136:139], a[16:19], v[24:27]\n\t"
      "v_mfma_f32_16x16x32_bf16 v[24:27], v[152:155], a[20:23], v[24:27]\n\t"
      "v_mfma_f32_16x16x32_bf16 v[28:31], v[76:79], a[0:3], 0\n\t"
      "ds_read_b128 a[48:51], v8 offset:12544\n\t"
      "v_mfma_f32_16x16x32_bf16 v[28:31], v[92:95], a[4:7], v[28:31]\n\t"
      "ds_read_b128 a[52:55], v60 offset:13568\n\t"
      "v_mfma_f32_16x16x32_bf16 v[28:31], v[108:111], a[8:11], v[28:31]\n\t"
      "ds_read_b128 a[56:59], v61 offset:14592\n\t"
      "v_mfma_f32_16x16x32_bf16 v[28:31], v[124:127], a[12:15], v[28:31]\n\t"
      "s_and_b32 s39, s32, 15\n\t"
      "s_cselect_b32 s53, s29, 0\n\t"
      "v_mfma_f32_16x16x32_bf16 v[28:31], v[140:143], a[16:19], v[28:31]\n\t"
      "s_add_u32 s39, s39, s53\n\t"
      "s_lshr_b64 s[32:33], s[32:33], 4\n\t"
      "v_mfma_f32_16x16x32_bf16 v[28:31], v[156:159], a[20:23], v[28:31]\n\t"
      "s_cmp_le_u32 s52, 12\n\t"
      "s_cselect_b32 s53, 3, 0\n\t"
      "s_min_u32 s53, s28, s53\n\t"
      "v_mfma_f32_16x16x32_bf16 v[32:35], a[60:63], a[0:3], 0\n\t"
      "s_sub_u32 s28, s28, s53\n\t"
      "s_sub_u32 s39, s39, s53\n\t"
      "v_mfma_f32_16x16x32_bf16 v[32:35], a[68:71], a[4:7], v[32:35]\n\t"
      "s_waitcnt lgkmcnt(2)\n\t"
      "v_mfma_f32_16x16x32_bf16 v[32:35], a[76:79], a[8:11], v[32:35]\n\t"
      "buffer_store_dwordx4 a[48:51], v12, s[44:47], s49 offen sc1\n\t"
      "v_mfma_f32_16x16x32_bf16 v[32:35], a[84:87], a[12:15], v[32:35]\n\t"
      "v_mfma_f32_16x16x32_bf16 v[32:35], a[92:95], a[16:19], v[32:35]\n\t"
      "v_mfma_f32_16x16x32_bf16 v[32:35], a[100:103], a[20:23], v[32:35]\n\t"
      "s_add_u32 m0, s18, s50\n\t"
      "v_mfma_f32_16x16x32_bf16 v[36:39], a[64:67], a[0:3], 0\n\t"
      "buffer_load_dwordx4 v2, s[40:43], s48 offen nt lds\n\t"
      "v_mfma_f32_16x16x32_bf16 v[36:39], a[72:75], a[4:7], v[36:39]\n\t"
      "s_memrealtime s[36:37]\n\t"
      "v_mfma_f32_16x16x32_bf16 v[36:39], a[80:83], a[8:11], v[36:39]\n\t"
      "v_mfma_f32_16x16x32_bf16 v[36:39], a[88:91], a[12:15], v[36:39]\n\t"
      "s_waitcnt lgkmcnt(1)\n\t"
      "v_mfma_f32_16x16x32_bf16 v[36:39], a[96:99], a[16:19], v[36:39]\n\t"
      "buffer_store_dwordx4 a[52:55], v13, s[44:47], s49 offen offset:1024 sc1\n\t"
      "v_mfma_f32_16x16x32_bf16 v[36:39], a[104:107], a[20:23], v[36:39]\n\t"
      "v_mfma_f32_16x16x32_bf16 v[16:19], v[160:163], a[24:27], v[16:19]\n\t"
      "ds_read_b128 a[0:3], v52 offset:0\n\t"
      "v_mfma_f32_16x16x32_bf16 v[16:19], v[176:179], a[28:31], v[16:19]\n\t"
      "ds_read_b128 a[4:7], v53 offset:0\n\t"
      "v_mfma_f32_16x16x32_bf16 v[16:19], v[192:195], a[32:35], v[16:19]\n\t"
      "ds_read_b128 a[8:11], v54 offset:0\n\t"
      "v_mfma_f32_16x16x32_bf16 v[16:19], v[208:211], a[36:39], v[16:19]\n\t"
      "ds_read_b128 a[12:15], v55 offset:0\n\t"
      "v_mfma_f32_16x16x32_bf16 v[16:19], v[224:227], a[40:43], v[16:19]\n\t"
      "ds_read_b128 a[16:19], v52 offset:256\n\t"
      "v_mfma_f32_16x16x32_bf16 v[16:19], v[240:243], a[44:47], v[16:19]\n\t"
      "ds_read_b128 a[20:23], v53 offset:256\n\t"
      "v_mfma_f32_16x16x32_bf16 v[20:23], v[164:167], a[24:27], v[20:23]\n\t"
      "s_add_u32 s51, s51, 12288\n\t"
      "s_cmp_eq_u32 s51, 160256\n\t"
      "s_cselect_b32 s51, 25088, s51\n\t"
      "v_mfma_f32_16x16x32_bf16 v[20:23], v[180:183], a[28:31], v[20:23]\n\t"
      "s_waitcnt lgkmcnt(6)\n\t"
      "v_mfma_f32_16x16x32_bf16 v[20:23], v[196:199], a[32:35], v[20:23]\n\t"
      "buffer_store_dwordx4 a[56:59], v14, s[44:47], s49 offen offset:2048 sc1\n\t"
      "v_mfma_f32_16x16x32_bf16 v[20:23], v[212:215], a[36:39], v[20:23]\n\t"
      "v_mfma_f32_16x16x32_bf16 v[20:23], v[228:231], a[40:43], v[20:23]\n\t"
      "s_waitcnt lgkmcnt(0)\n\t"
      "s_sub_u32 s53, s36, s38\n\t"
      "s_cmp_lt_i32 s53, 0\n\t"
      "v_mfma_f32_16x16x32_bf16 v[20:23], v[244:247], a[44:47], v[20:23]\n\t"
      "s_cbranch_scc1 .Lspin0_%=\n\t"
      ".Lspun0_%=:\n\t"
      "s_sub_i32 s53, s53, 30\n\t"
      "s_max_i32 s53, s53, 0\n\t"
      "v_mfma_f32_16x16x32_bf16 v[24:27], v[168:171], a[24:27], v[24:27]\n\t"
      "s_add_u32 s38, s38, s53\n\t"
      "s_add_u32 s38, s38, s39\n\t"
      "s_add_u32 s28, s28, s53\n\t"
      "v_mfma_f32_16x16x32_bf16 v[24:27], v[184:187], a[28:31], v[24:27]\n\t"
      "v_cvt_pk_bf16_f32 v40, v16, v17\n\t"
      "v_cvt_pk_bf16_f32 v41, v18, v19\n\t"
      "v_mfma_f32_16x16x32_bf16 v[24:27], v[200:203], a[32:35], v[24:27]\n\t"
      "ds_write_b64 v0, v[40:41] offset:0\n\t"
      "v_mfma_f32_16x16x32_bf16 v[24:27], v[216:219], a[36:39], v[24:27]\n\t"
      "buffer_load_dwordx4 v3, s[40:43], s48 offen offset:1024 nt lds\n\t"
      "v_mfma_f32_16x16x32_bf16 v[24:27], v[232:235], a[40:43], v[24:27]\n\t"
      "s_add_u32 s58, s58, 786432\n\t"
      "s_lshr_b32 s59, s59, 1\n\t"
      "v_mfma_f32_16x16x32_bf16 v[24:27], v[248:251], a[44:47], v[24:27]\n\t"
      "s_bitcmp1_b32 s59, 0\n\t"
      "s_cselect_b32 s49, 24576, 0\n\t"
      "s_add_u32 s49, s49, s58\n\t"
      "v_mfma_f32_16x16x32_bf16 v[28:31], v[172:175], a[24:27], v[28:31]\n\t"
      "v_mfma_f32_16x16x32_bf16 v[28:31], v[188:191], a[28:31], v[28:31]\n\t"
      "v_cvt_pk_bf16_f32 v42, v20, v21\n\t"
      "v_cvt_pk_bf16_f32 v43, v22, v23\n\t"
      "v_mfma_f32_16x16x32_bf16 v[28:31], v[204:207], a[32:35], v[28:31]\n\t"
      "ds_write_b64 v0, v[42:43] offset:32\n\t"
      "v_mfma_f32_16x16x32_bf16 v[28:31], v[220:223], a[36:39], v[28:31]\n\t"
      "v_mfma_f32_16x16x32_bf16 v[28:31], v[236:239], a[40:43], v[28:31]\n\t"
      "v_mfma_f32_16x16x32_bf16 v[28:31], v[252:255], a[44:47], v[28:31]\n\t"
      "s_cmp_eq_u32 s52, 14\n\t"
      "s_cbranch_scc1 .Lshb_%=\n\t"
      ".Lshbr_%=:\n\t"
      "v_mfma_f32_16x16x32_bf16 v[32:35], a[108:111], a[24:27], v[32:35]\n\t"
      "v_mfma_f32_16x16x32_bf16 v[32:35], a[116:119], a[28:31], v[32:35]\n\t"
      "v_cvt_pk_bf16_f32 v44, v24, v25\n\t"
      "v_cvt_pk_bf16_f32 v45, v26, v27\n\t"
      "v_mfma_f32_16x16x32_bf16 v[32:35], a[124:127], a[32:35], v[32:35]\n\t"
      "ds_write_b64 v0, v[44:45] offset:64\n\t"
      "v_mfma_f32_16x16x32_bf16 v[32:35], a[132:135], a[36:39], v[32:35]\n\t"
      "buffer_load_dwordx4 v4, s[40:43], s48 offen offset:2048 nt lds\n\t"
      "v_mfma_f32_16x16x32_bf16 v[32:35], a[140:143], a[40:43], v[32:35]\n\t"
      "s_add_u32 s50, s50, 12288\n\t"
      "v_mfma_f32_16x16x32_bf16 v[32:35], a[148:151], a[44:47], v[32:35]\n\t"
      "v_cvt_pk_bf16_f32 v46, v28, v29\n\t"
      "v_cvt_pk_bf16_f32 v47, v30, v31\n\t"
      "v_mfma_f32_16x16x32_bf16 v[36:39], a[112:115], a[24:27], v[36:39]\n\t"
      "ds_write_b64 v0, v[46:47] offset:96\n\t"
      "v_mfma_f32_16x16x32_bf16 v[36:39], a[120:123], a[28:31], v[36:39]\n\t"
      "s_cmp_eq_u32 s50, 160256\n\t"
      "s_cselect_b32 s50, 25088, s50\n\t"
      "v_mfma_f32_16x16x32_bf16 v[36:39], a[128:131], a[32:35], v[36:39]\n\t"
      "v_mfma_f32_16x16x32_bf16 v[36:39], a[136:139], a[36:39], v[36:39]\n\t"
      "v_mfma_f32_16x16x32_bf16 v[36:39], a[144:147], a[40:43], v[36:39]\n\t"
      "v_cvt_pk_bf16_f32 v48, v32, v33\n\t"
      "v_cvt_pk_bf16_f32 v49, v34, v35\n\t"
      "v_mfma_f32_16x16x32_bf16 v[36:39], a[152:155], a[44:47], v[36:39]\n\t"
      "ds_write_b64 v0, v[48:49] offset:128\n\t"
      ".Lmid0_%=:\n\t"
      "v_mfma_f32_16x16x32_bf16 v[16:19], v[64:67], a[0:3], 0\n\t"
      "ds_read_b128 a[24:27], v54 offset:256\n\t"
      "v_mfma_f32_16x16x32_bf16 v[16:19], v[80:83], a[4:7], v[16:19]\n\t"
      "ds_read_b128 a[28:31], v55 offset:256\n\t"
      "v_mfma_f32_16x16x32_bf16 v[16:19], v[96:99], a[8:11], v[16:19]\n\t"
      "ds_read_b128 a[32:35], v52 offset:512\n\t"
      "v_mfma_f32_16x16x32_bf16 v[16:19], v[112:115], a[12:15], v[16:19]\n\t"
      "ds_read_b128 a[36:39], v53 offset:512\n\t"
      "v_mfma_f32_16x16x32_bf16 v[16:19], v[128:131], a[16:19], v[16:19]\n\t"
      "ds_read_b128 a[40:43], v54 offset:512\n\t"
      "v_mfma_f32_16x16x32_bf16 v[16:19], v[144:147], a[20:23], v[16:19]\n\t"
      "ds_read_b128 a[44:47], v55 offset:512\n\t"
      "v_mfma_f32_16x16x32_bf16 v[20:23], v[68:71], a[0:3], 0\n\t"
      "v_cvt_pk_bf16_f32 v50, v36, v37\n\t"
      "v_cvt_pk_bf16_f32 v51, v38, v39\n\t"
      "v_mfma_f32_16x16x32_bf16 v[20:23], v[84:87], a[4:7], v[20:23]\n\t"
      "ds_write_b64 v0, v[50:51] offset:160\n\t"
      "v_mfma_f32_16x16x32_bf16 v[20:23], v[100:103], a[8:11], v[20:23]\n\t"
      "v_add_u32 v56, s51, v15\n\t"
      "v_xor_b32 v57, 64, v56\n\t"
      "v_mfma_f32_16x16x32_bf16 v[20:23], v[116:119], a[12:15], v[20:23]\n\t"
      "v_xor_b32 v58, 128, v56\n\t"
      "v_xor_b32 v59, 192, v56\n\t"
      "v_mfma_f32_16x16x32_bf16 v[20:23], v[132:135], a[16:19], v[20:23]\n\t"
      "v_mfma_f32_16x16x32_bf16 v[20:23], v[148:151], a[20:23], v[20:23]\n\t"
      "v_mfma_f32_16x16x32_bf16 v[24:27], v[72:75], a[0:3], 0\n\t"
      "v_mfma_f32_16x16x32_bf16 v[24:27], v[88:91], a[4:7], v[24:27]\n\t"
      "v_mfma_f32_16x16x32_bf16 v[24:27], v[104:107], a[8:11], v[24:27]\n\t"
      "s_waitcnt vmcnt(12)\n\t"
      "s_waitcnt lgkmcnt(0)\n\t"
      "s_barrier\n\t"
      "v_mfma_f32_16x16x32_bf16 v[24:27], v[120:123], a[12:15], v[24:27]\n\t"
      "v_mfma_f32_16x16x32_bf16 v[24:27], v[136:139], a[16:19], v[24:27]\n\t"
      "v_mfma_f32_16x16x32_bf16 v[24:27], v[152:155], a[20:23], v[24:27]\n\t"
      "v_mfma_f32_16x16x32_bf16 v[28:31], v[76:79], a[0:3], 0\n\t"
      "ds_read_b128 a[48:51], v8 offset:0\n\t"
      "v_mfma_f32_16x16x32_bf16 v[28:31], v[92:95], a[4:7], v[28:31]\n\t"
      "ds_read_b128 a[52:55], v60 offset:1024\n\t"
      "v_mfma_f32_16x16x32_bf16 v[28:31], v[108:111], a[8:11], v[28:31]\n\t"
      "ds_read_b128 a[56:59], v61 offset:2048\n\t"
      "v_mfma_f32_16x16x32_bf16 v[28:31], v[124:127], a[12:15], v[28:31]\n\t"
      "s_and_b32 s39, s34, 15\n\t"
      "s_cselect_b32 s53, s29, 0\n\t"
      "v_mfma_f32_16x16x32_bf16 v[28:31], v[140:143], a[16:19], v[28:31]\n\t"
      "s_add_u32 s39, s39, s53\n\t"
      "s_lshr_b64 s[34:35], s[34:35], 4\n\t"
      "v_mfma_f32_16x16x32_bf16 v[28:31], v[156:159], a[20:23], v[28:31]\n\t"
      "s_cmp_le_u32 s52, 12\n\t"
      "s_cselect_b32 s53, 3, 0\n\t"
      "s_min_u32 s53, s28, s53\n\t"
      "v_mfma_f32_16x16x32_bf16 v[32:35], a[60:63], a[0:3], 0\n\t"
      "s_sub_u32 s28, s28, s53\n\t"
      "s_sub_u32 s39, s39, s53\n\t"
      "v_mfma_f32_16x16x32_bf16 v[32:35], a[68:71], a[4:7], v[32:35]\n\t"
      "s_waitcnt lgkmcnt(2)\n\t"
      "v_mfma_f32_16x16x32_bf16 v[32:35], a[76:79], a[8:11], v[32:35]\n\t"
      "buffer_store_dwordx4 a[48:51], v9, s[44:47], s49 offen sc1\n\t"
      "v_mfma_f32_16x16x32_bf16 v[32:35], a[84:87], a[12:15], v[32:35]\n\t"
      "v_mfma_f32_16x16x32_bf16 v[32:35], a[92:95], a[16:19], v[32:35]\n\t"
      "v_mfma_f32_16x16x32_bf16 v[32:35], a[100:103], a[20:23], v[32:35]\n\t"
      "s_add_u32 m0, s18, s50\n\t"
      "v_mfma_f32_16x16x32_bf16 v[36:39], a[64:67], a[0:3], 0\n\t"
      "buffer_load_dwordx4 v5, s[40:43], s48 offen nt lds\n\t"
      "v_mfma_f32_16x16x32_bf16 v[36:39], a[72:75], a[4:7], v[36:39]\n\t"
      "s_memrealtime s[36:37]\n\t"
      "v_mfma_f32_16x16x32_bf16 v[36:39], a[80:83], a[8:11], v[36:39]\n\t"
      "v_mfma_f32_16x16x32_bf16 v[36:39], a[88:91], a[12:15], v[36:39]\n\t"
      "s_waitcnt lgkmcnt(1)\n\t"
      "v_mfma_f32_16x16x32_bf16 v[36:39], a[96:99], a[16:19], v[36:39]\n\t"
      "buffer_store_dwordx4 a[52:55], v10, s[44:47], s49 offen offset:1024 sc1\n\t"
      "v_mfma_f32_16x16x32_bf16 v[36:39], a[104:107], a[20:23], v[36:39]\n\t"
      "v_mfma_f32_16x16x32_bf16 v[16:19], v[160:163], a[24:27], v[16:19]\n\t"
      "ds_read_b128 a[0:3], v56 offset:0\n\t"
      "v_mfma_f32_16x16x32_bf16 v[16:19], v[176:179], a[28:31], v[16:19]\n\t"
      "ds_read_b128 a[4:7], v57 offset:0\n\t"
      "v_mfma_f32_16x16x32_bf16 v[16:19], v[192:195], a[32:35], v[16:19]\n\t"
      "ds_read_b128 a[8:11], v58 offset:0\n\t"
      "v_mfma_f32_16x16x32_bf16 v[16:19], v[208:211], a[36:39], v[16:19]\n\t"
      "ds_read_b128 a[12:15], v59 offset:0\n\t"
      "v_mfma_f32_16x16x32_bf16 v[16:19], v[224:227], a[40:43], v[16:19]\n\t"
      "ds_read_b128 a[16:19], v56 offset:256\n\t"
      "v_mfma_f32_16x16x32_bf16 v[16:19], v[240:243], a[44:47], v[16:19]\n\t"
      "ds_read_b128 a[20:23], v57 offset:256\n\t"
      "v_mfma_f32_16x16x32_bf16 v[20:23], v[164:167], a[24:27], v[20:23]\n\t"
      "s_add_u32 s51, s51, 12288\n\t"
      "s_cmp_eq_u32 s51, 160256\n\t"
      "s_cselect_b32 s51, 25088, s51\n\t"
      "v_mfma_f32_16x16x32_bf16 v[20:23], v[180:183], a[28:31], v[20:23]\n\t"
      "s_waitcnt lgkmcnt(6)\n\t"
      "v_mfma_f32_16x16x32_bf16 v[20:23], v[196:199], a[32:35], v[20:23]\n\t"
      "buffer_store_dwordx4 a[56:59], v11, s[44:47], s49 offen offset:2048 sc1\n\t"
      "v_mfma_f32_16x16x32_bf16 v[20:23], v[212:215], a[36:39], v[20:23]\n\t"
      "v_mfma_f32_16x16x32_bf16 v[20:23], v[228:231], a[40:43], v[20:23]\n\t"
      "s_waitcnt lgkmcnt(0)\n\t"
      "s_sub_u32 s53, s36, s38\n\t"
      "s_cmp_lt_i32 s53, 0\n\t"
      "v_mfma_f32_16x16x32_bf16 v[20:23], v[244:247], a[44:47], v[20:23]\n\t"
      "s_cbranch_scc1 .Lspin1_%=\n\t"
      ".Lspun1_%=:\n\t"
      "s_sub_i32 s53, s53, 30\n\t"
      "s_max_i32 s53, s53, 0\n\t"
      "v_mfma_f32_16x16x32_bf16 v[24:27], v[168:171], a[24:27], v[24:27]\n\t"
      "s_add_u32 s38, s38, s53\n\t"
      "s_add_u32 s38, s38, s39\n\t"
      "s_add_u32 s28, s28, s53\n\t"
      "v_mfma_f32_16x16x32_bf16 v[24:27], v[184:187], a[28:31], v[24:27]\n\t"
      "v_cvt_pk_bf16_f32 v40, v16, v17\n\t"
      "v_cvt_pk_bf16_f32 v41, v18, v19\n\t"
      "v_mfma_f32_16x16x32_bf16 v[24:27], v[200:203], a[32:35], v[24:27]\n\t"
      "ds_write_b64 v0, v[40:41] offset:12544\n\t"
      "v_mfma_f32_16x16x32_bf16 v[24:27], v[216:219], a[36:39], v[24:27]\n\t"
      "buffer_load_dwordx4 v6, s[40:43], s48 offen offset:1024 nt lds\n\t"
      "v_mfma_f32_16x16x32_bf16 v[24:27], v[232:235], a[40:43], v[24:27]\n\t"
      "v_mfma_f32_16x16x32_bf16 v[24:27], v[248:251], a[44:47], v[24:27]\n\t"
      "v_mfma_f32_16x16x32_bf16 v[28:31], v[172:175], a[24:27], v[28:31]\n\t"
      "v_mfma_f32_16x16x32_bf16 v[28:31], v[188:191], a[28:31], v[28:31]\n\t"
      "v_cvt_pk_bf16_f32 v42, v20, v21\n\t"
      "v_cvt_pk_bf16_f32 v43, v22, v23\n\t"
      "v_mfma_f32_16x16x32_bf16 v[28:31], v[204:207], a[32:35], v[28:31]\n\t"
      "ds_write_b64 v0, v[42:43] offset:12576\n\t"
      "v_mfma_f32_16x16x32_bf16 v[28:31], v[220:223], a[36:39], v[28:31]\n\t"
      "v_mfma_f32_16x16x32_bf16 v[28:31], v[236:239], a[40:43], v[28:31]\n\t"
      "v_mfma_f32_16x16x32_bf16 v[28:31], v[252:255], a[44:47], v[28:31]\n\t"
      "v_mfma_f32_16x16x32_bf16 v[32:35], a[108:111], a[24:27], v[32:35]\n\t"
      "v_mfma_f32_16x16x32_bf16 v[32:35], a[116:119], a[28:31], v[32:35]\n\t"
      "v_cvt_pk_bf16_f32 v44, v24, v25\n\t"
      "v_cvt_pk_bf16_f32 v45, v26, v27\n\t"
      "v_mfma_f32_16x16x32_bf16 v[32:35], a[124:127], a[32:35], v[32:35]\n\t"
      "ds_write_b64 v0, v[44:45] offset:12608\n\t"
      "v_mfma_f32_16x16x32_bf16 v[32:35], a[132:135], a[36:39], v[32:35]\n\t"
      "buffer_load_dwordx4 v7, s[40:43], s48 offen offset:2048 nt lds\n\t"
      "v_mfma_f32_16x16x32_bf16 v[32:35], a[140:143], a[40:43], v[32:35]\n\t"
      "s_add_u32 s50, s50, 12288\n\t"
      "v_mfma_f32_16x16x32_bf16 v[32:35], a[148:151], a[44:47], v[32:35]\n\t"
      "v_cvt_pk_bf16_f32 v46, v28, v29\n\t"
      "v_cvt_pk_bf16_f32 v47, v30, v31\n\t"
      "v_mfma_f32_16x16x32_bf16 v[36:39], a[112:115], a[24:27], v[36:39]\n\t"
      "ds_write_b64 v0, v[46:47] offset:12640\n\t"
      "v_mfma_f32_16x16x32_bf16 v[36:39], a[120:123], a[28:31], v[36:39]\n\t"
      "s_cmp_eq_u32 s50, 160256\n\t"
      "s_cselect_b32 s50, 25088, s50\n\t"
      "v_mfma_f32_16x16x32_bf16 v[36:39], a[128:131], a[32:35], v[36:39]\n\t"
      "s_sub_u32 s52, s52, 1\n\t"
      "s_cmp_lg_u32 s52, 0\n\t"
      "v_mfma_f32_16x16x32_bf16 v[36:39], a[136:139], a[36:39], v[36:39]\n\t"
      "v_mfma_f32_16x16x32_bf16 v[36:39], a[144:147], a[40:43], v[36:39]\n\t"
      "v_cvt_pk_bf16_f32 v48, v32, v33\n\t"
      "v_cvt_pk_bf16_f32 v49, v34, v35\n\t"
      "v_mfma_f32_16x16x32_bf16 v[36:39], a[152:155], a[44:47], v[36:39]\n\t"
      "ds_write_b64 v0, v[48:49] offset:12672\n\t"
      "s_cbranch_scc1 .Lloop0_%=\n\t"
      "s_branch .Lendc_%=\n\t"
      ".Lshb_%=:\n\t"
      "s_waitcnt vmcnt(10)\n\t"
      "v_bfe_u32 v63, v63, 17, 8\n\t"
      "v_min_u32 v62, 1, v63\n\t"
      "v_lshl_add_u32 v63, v62, 16, v63\n\t"
      "s_nop 1\n\t"
      "v_add_u32_dpp v63, v63, v63 row_shr:1 row_mask:0xf bank_mask:0xf bound_ctrl:0\n\t"
      "s_nop 1\n\t"
      "v_add_u32_dpp v63, v63, v63 row_shr:2 row_mask:0xf bank_mask:0xf bound_ctrl:0\n\t"
      "s_nop 1\n\t"
      "v_add_u32_dpp v63, v63, v63 row_shr:4 row_mask:0xf bank_mask:0xf bound_ctrl:0\n\t"
      "s_nop 1\n\t"
      "v_add_u32_dpp v63, v63, v63 row_shr:8 row_mask:0xf bank_mask:0xf bound_ctrl:0\n\t"
      "s_nop 0\n\t"
      "v_readlane_b32 s64, v63, 15\n\t"
      "v_readlane_b32 s65, v63, 31\n\t"
      "s_nop 1\n\t"
      "s_add_u32 s64, s64, s65\n\t"
      "s_lshr_b32 s65, s64, 16\n\t"
      "s_and_b32 s64, s64, 0xffff\n\t"
      "s_mov_b32 s66, 0\n\t"
      "s_mul_i32 s67, s65, 989\n\t"
      "s_lshl_b32 s69, s64, 3\n\t"
      "s_cmp_lt_u32 s69, s67\n\t"
      "s_cselect_b32 s66, 1, s66\n\t"
      "s_mul_i32 s67, s65, 957\n\t"
      "s_lshl_b32 s69, s64, 3\n\t"
      "s_cmp_lt_u32 s69, s67\n\t"
      "s_cselect_b32 s66, 3, s66\n\t"
      "s_add_u32 s29, s29, s66\n\t"
      "s_mul_i32 s67, s65, 737\n\t"
      "s_cmp_lt_u32 s69, s67\n\t"
      "s_cbranch_scc0 .Lshbr_%=\n\t"
      "s_movk_i32 s29, 79\n\t"
      "s_mov_b32 s32, 0x11111111\n\t"
      "s_mov_b32 s33, 0x11111111\n\t"
      "s_mov_b32 s34, 0x11111111\n\t"
      "s_mov_b32 s35, 0x11111111\n\t"
      "s_sub_i32 s28, s28, 30\n\t"
      "s_min_i32 s66, s28, 0\n\t"
      "s_sub_u32 s38, s38, s66\n\t"
      "s_max_i32 s28, s28, 0\n\t"
      "s_branch .Lshbr_%=\n\t"
      ".Lloop1_%=:\n\t"
      "v_mfma_f32_16x16x32_bf16 v[16:19], v[64:67], a[0:3], 0\n\t"
      "ds_read_b128 a[24:27], v58 offset:256\n\t"
      "v_mfma_f32_16x16x32_bf16 v[16:19], v[80:83], a[4:7], v[16:19]\n\t"
      "ds_read_b128 a[28:31], v59 offset:256\n\t"
      "v_mfma_f32_16x16x32_bf16 v[16:19], v[96:99], a[8:11], v[16:19]\n\t"
      "ds_read_b128 a[32:35], v56 offset:512\n\t"
      "v_mfma_f32_16x16x32_bf16 v[16:19], v[112:115], a[12:15], v[16:19]\n\t"
      "ds_read_b128 a[36:39], v57 offset:512\n\t"
      "v_mfma_f32_16x16x32_bf16 v[16:19], v[128:131], a[16:19], v[16:19]\n\t"
      "ds_read_b128 a[40:43], v58 offset:512\n\t"
      "v_mfma_f32_16x16x32_bf16 v[16:19], v[144:147], a[20:23], v[16:19]\n\t"
      "ds_read_b128 a[44:47], v59 offset:512\n\t"
      "v_mfma_f32_16x16x32_bf16 v[20:23], v[68:71], a[0:3], 0\n\t"
      "v_cvt_pk_bf16_f32 v50, v36, v37\n\t"
      "v_cvt_pk_bf16_f32 v51, v38, v39\n\t"
      "v_mfma_f32_16x16x32_bf16 v[20:23], v[84:87], a[4:7], v[20:23]\n\t"
      "ds_write_b64 v0, v[50:51] offset:12704\n\t"
      "v_mfma_f32_16x16x32_bf16 v[20:23], v[100:103], a[8:11], v[20:23]\n\t"
      "v_add_u32 v52, s51, v15\n\t"
      "v_xor_b32 v53, 64, v52\n\t"
      "v_mfma_f32_16x16x32_bf16 v[20:23], v[116:119], a[12:15], v[20:23]\n\t"
      "v_xor_b32 v54, 128, v52\n\t"
      "v_xor_b32 v55, 192, v52\n\t"
      "v_mfma_f32_16x16x32_bf16 v[20:23], v[132:135], a[16:19], v[20:23]\n\t"
      "s_add_u32 s56, s56, 786432\n\t"
      "s_lshr_b32 s57, s57, 1\n\t"
      "v_mfma_f32_16x16x32_bf16 v[20:23], v[148:151], a[20:23], v[20:23]\n\t"
      "s_bitcmp1_b32 s57, 0\n\t"
      "s_cselect_b32 s48, 24576, 0\n\t"
      "s_add_u32 s48, s48, s56\n\t"
      "v_mfma_f32_16x16x32_bf16 v[24:27], v[72:75], a[0:3], 0\n\t"
      "v_mfma_f32_16x16x32_bf16 v[24:27], v[88:91], a[4:7], v[24:27]\n\t"
      "v_mfma_f32_16x16x32_bf16 v[24:27], v[104:107], a[8:11], v[24:27]\n\t"
      "v_mfma_f32_16x16x32_bf16 v[24:27], v[120:123], a[12:15], v[24:27]\n\t"
      "s_waitcnt vmcnt(12)\n\t"
      "s_waitcnt lgkmcnt(0)\n\t"
      "s_barrier\n\t"
      "v_mfma_f32_16x16x32_bf16 v[24:27], v[136:139], a[16:19], v[24:27]\n\t"
      "v_mfma_f32_16x16x32_bf16 v[24:27], v[152:155], a[20:23], v[24:27]\n\t"
      "v_mfma_f32_16x16x32_bf16 v[28:31], v[76:79], a[0:3], 0\n\t"
      "ds_read_b128 a[48:51], v8 offset:12544\n\t"
      "v_mfma_f32_16x16x32_bf16 v[28:31], v[92:95], a[4:7], v[28:31]\n\t"
      "ds_read_b128 a[52:55], v60 offset:13568\n\t"
      "v_mfma_f32_16x16x32_bf16 v[28:31], v[108:111], a[8:11], v[28:31]\n\t"
      "ds_read_b128 a[56:59], v61 offset:14592\n\t"
      "v_mfma_f32_16x16x32_bf16 v[28:31], v[124:127], a[12:15], v[28:31]\n\t"
      "v_mfma_f32_16x16x32_bf16 v[28:31], v[140:143], a[16:19], v[28:31]\n\t"
      "v_mfma_f32_16x16x32_bf16 v[28:31], v[156:159], a[20:23], v[28:31]\n\t"
      "v_mfma_f32_16x16x32_bf16 v[32:35], a[60:63], a[0:3], 0\n\t"
      "v_mfma_f32_16x16x32_bf16 v[32:35], a[68:71], a[4:7], v[32:35]\n\t"
      "s_waitcnt lgkmcnt(2)\n\t"
      "v_mfma_f32_16x16x32_bf16 v[32:35], a[76:79], a[8:11], v[32:35]\n\t"
      "buffer_store_dwordx4 a[48:51], v12, s[44:47], s49 offen sc1\n\t"
      "v_mfma_f32_16x16x32_bf16 v[32:35], a[84:87], a[12:15], v[32:35]\n\t"
      "v_mfma_f32_16x16x32_bf16 v[32:35], a[92:95], a[16:19], v[32:35]\n\t"
      "v_mfma_f32_16x16x32_bf16 v[32:35], a[100:103], a[20:23], v[32:35]\n\t"
      "s_add_u32 m0, s18, s50\n\t"
      "v_mfma_f32_16x16x32_bf16 v[36:39], a[64:67], a[0:3], 0\n\t"
      "buffer_load_dwordx4 v2, s[40:43], s48 offen nt lds\n\t"
      "v_mfma_f32_16x16x32_bf16 v[36:39], a[72:75], a[4:7], v[36:39]\n\t"
      "v_mfma_f32_16x16x32_bf16 v[36:39], a[80:83], a[8:11], v[36:39]\n\t"
      "v_mfma_f32_16x16x32_bf16 v[36:39], a[88:91], a[12:15], v[36:39]\n\t"
      "s_waitcnt lgkmcnt(1)\n\t"
      "v_mfma_f32_16x16x32_bf16 v[36:39], a[96:99], a[16:19], v[36:39]\n\t"
      "buffer_store_dwordx4 a[52:55], v13, s[44:47], s49 offen offset:1024 sc1\n\t"
      "v_mfma_f32_16x16x32_bf16 v[36:39], a[104:107], a[20:23], v[36:39]\n\t"
      "v_mfma_f32_16x16x32_bf16 v[16:19], v[160:163], a[24:27], v[16:19]\n\t"
      "ds_read_b128 a[0:3], v52 offset:0\n\t"
      "v_mfma_f32_16x16x32_bf16 v[16:19], v[176:179], a[28:31], v[16:19]\n\t"
      "ds_read_b128 a[4:7], v53 offset:0\n\t"
      "v_mfma_f32_16x16x32_bf16 v[16:19], v[192:195], a[32:35], v[16:19]\n\t"
      "ds_read_b128 a[8:11], v54 offset:0\n\t"
      "v_mfma_f32_16x16x32_bf16 v[16:19], v[208:211], a[36:39], v[16:19]\n\t"
      "ds_read_b128 a[12:15], v55 offset:0\n\t"
      "v_mfma_f32_16x16x32_bf16 v[16:19], v[224:227], a[40:43], v[16:19]\n\t"
      "ds_read_b128 a[16:19], v52 offset:256\n\t"
      "v_mfma_f32_16x16x32_bf16 v[16:19], v[240:243], a[44:47], v[16:19]\n\t"
      "ds_read_b128 a[20:23], v53 offset:256\n\t"
      "v_mfma_f32_16x16x32_bf16 v[20:23], v[164:167], a[24:27], v[20:23]\n\t"
      "s_add_u32 s51, s51, 12288\n\t"
      "s_cmp_eq_u32 s51, 160256\n\t"
      "s_cselect_b32 s51, 25088, s51\n\t"
      "v_mfma_f32_16x16x32_bf16 v[20:23], v[180:183], a[28:31], v[20:23]\n\t"
      "s_waitcnt lgkmcnt(6)\n\t"
      "v_mfma_f32_16x16x32_bf16 v[20:23], v[196:199], a[32:35], v[20:23]\n\t"
      "buffer_store_dwordx4 a[56:59], v14, s[44:47], s49 offen offset:2048 sc1\n\t"
      "v_mfma_f32_16x16x32_bf16 v[20:23], v[212:215], a[36:39], v[20:23]\n\t"
      "v_mfma_f32_16x16x32_bf16 v[20:23], v[228:231], a[40:43], v[20:23]\n\t"
      "s_waitcnt lgkmcnt(0)\n\t"
      "v_mfma_f32_16x16x32_bf16 v[20:23], v[244:247], a[44:47], v[20:23]\n\t"
      "v_mfma_f32_16x16x32_bf16 v[24:27], v[168:171], a[24:27], v[24:27]\n\t"
      "v_mfma_f32_16x16x32_bf16 v[24:27], v[184:187], a[28:31], v[24:27]\n\t"
      "v_cvt_pk_bf16_f32 v40, v16, v17\n\t"
      "v_cvt_pk_bf16_f32 v41, v18, v19\n\t"
      "v_mfma_f32_16x16x32_bf16 v[24:27], v[200:203], a[32:35], v[24:27]\n\t"
      "ds_write_b64 v0, v[40:41] offset:0\n\t"
      "v_mfma_f32_16x16x32_bf16 v[24:27], v[216:219], a[36:39], v[24:27]\n\t"
      "buffer_load_dwordx4 v3, s[40:43], s48 offen offset:1024 nt lds\n\t"
      "v_mfma_f32_16x16x32_bf16 v[24:27], v[232:235], a[40:43], v[24:27]\n\t"
      "s_add_u32 s58, s58, 786432\n\t"
      "s_lshr_b32 s59, s59, 1\n\t"
      "v_mfma_f32_16x16x32_bf16 v[24:27], v[248:251], a[44:47], v[24:27]\n\t"
      "s_bitcmp1_b32 s59, 0\n\t"
      "s_cselect_b32 s49, 24576, 0\n\t"
      "s_add_u32 s49, s49, s58\n\t"
      "v_mfma_f32_16x16x32_bf16 v[28:31], v[172:175], a[24:27], v[28:31]\n\t"
      "v_mfma_f32_16x16x32_bf16 v[28:31], v[188:191], a[28:31], v[28:31]\n\t"
      "v_cvt_pk_bf16_f32 v42, v20, v21\n\t"
      "v_cvt_pk_bf16_f32 v43, v22, v23\n\t"
      "v_mfma_f32_16x16x32_bf16 v[28:31], v[204:207], a[32:35], v[28:31]\n\t"
      "ds_write_b64 v0, v[42:43] offset:32\n\t"
      "v_mfma_f32_16x16x32_bf16 v[28:31], v[220:223], a[36:39], v[28:31]\n\t"
      "v_mfma_f32_16x16x32_bf16 v[28:31], v[236:239], a[40:43], v[28:31]\n\t"
      "v_mfma_f32_16x16x32_bf16 v[28:31], v[252:255], a[44:47], v[28:31]\n\t"
      "v_mfma_f32_16x16x32_bf16 v[32:35], a[108:111], a[24:27], v[32:35]\n\t"
      "v_mfma_f32_16x16x32_bf16 v[32:35], a[116:119], a[28:31], v[32:35]\n\t"
      "v_cvt_pk_bf16_f32 v44, v24, v25\n\t"
      "v_cvt_pk_bf16_f32 v45, v26, v27\n\t"
      "v_mfma_f32_16x16x32_bf16 v[32:35], a[124:127], a[32:35], v[32:35]\n\t"
      "ds_write_b64 v0, v[44:45] offset:64\n\t"
      "v_mfma_f32_16x16x32_bf16 v[32:35], a[132:135], a[36:39], v[32:35]\n\t"
      "buffer_load_dwordx4 v4, s[40:43], s48 offen offset:2048 nt lds\n\t"
      "v_mfma_f32_16x16x32_bf16 v[32:35], a[140:143], a[40:43], v[32:35]\n\t"
      "s_add_u32 s50, s50, 12288\n\t"
      "v_mfma_f32_16x16x32_bf16 v[32:35], a[148:151], a[44:47], v[32:35]\n\t"
      "v_cvt_pk_bf16_f32 v46, v28, v29\n\t"
      "v_cvt_pk_bf16_f32 v47, v30, v31\n\t"
      "v_mfma_f32_16x16x32_bf16 v[36:39], a[112:115], a[24:27], v[36:39]\n\t"
      "ds_write_b64 v0, v[46:47] offset:96\n\t"
      "v_mfma_f32_16x16x32_bf16 v[36:39], a[120:123], a[28:31], v[36:39]\n\t"
      "s_cmp_eq_u32 s50, 160256\n\t"
      "s_cselect_b32 s50, 25088, s50\n\t"
      "v_mfma_f32_16x16x32_bf16 v[36:39], a[128:131], a[32:35], v[36:39]\n\t"
      "v_mfma_f32_16x16x32_bf16 v[36:39], a[136:139], a[36:39], v[36:39]\n\t"
      "v_mfma_f32_16x16x32_bf16 v[36:39], a[144:147], a[40:43], v[36:39]\n\t"
      "v_cvt_pk_bf16_f32 v48, v32, v33\n\t"
      "v_cvt_pk_bf16_f32 v49, v34, v35\n\t"
      "v_mfma_f32_16x16x32_bf16 v[36:39], a[152:155], a[44:47], v[36:39]\n\t"
      "ds_write_b64 v0, v[48:49] offset:128\n\t"
      ".Lmid1_%=:\n\t"
      "v_mfma_f32_16x16x32_bf16 v[16:19], v[64:67], a[0:3], 0\n\t"
      "ds_read_b128 a[24:27], v54 offset:256\n\t"
      "v_mfma_f32_16x16x32_bf16 v[16:19], v[80:83], a[4:7], v[16:19]\n\t"
      "ds_read_b128 a[28:31], v55 offset:256\n\t"
      "v_mfma_f32_16x16x32_bf16 v[16:19], v[96:99], a[8:11], v[16:19]\n\t"
      "ds_read_b128 a[32:35], v52 offset:512\n\t"
      "v_mfma_f32_16x16x32_bf16 v[16:19], v[112:115], a[12:15], v[16:19]\n\t"
      "ds_read_b128 a[36:39], v53 offset:512\n\t"
      "v_mfma_f32_16x16x32_bf16 v[16:19], v[128:131], a[16:19], v[16:19]\n\t"
      "ds_read_b128 a[40:43], v54 offset:512\n\t"
      "v_mfma_f32_16x16x32_bf16 v[16:19], v[144:147], a[20:23], v[16:19]\n\t"
      "ds_read_b128 a[44:47], v55 offset:512\n\t"
      "v_mfma_f32_16x16x32_bf16 v[20:23], v[68:71], a[0:3], 0\n\t"
      "v_cvt_pk_bf16_f32 v50, v36, v37\n\t"
      "v_cvt_pk_bf16_f32 v51, v38, v39\n\t"
      "v_mfma_f32_16x16x32_bf16 v[20:23], v[84:87], a[4:7], v[20:23]\n\t"
      "ds_write_b64 v0, v[50:51] offset:160\n\t"
      "v_mfma_f32_16x16x32_bf16 v[20:23], v[100:103], a[8:11], v[20:23]\n\t"
      "v_add_u32 v56, s51, v15\n\t"
      "v_xor_b32 v57, 64, v56\n\t"
      "v_mfma_f32_16x16x32_bf16 v[20:23], v[116:119], a[12:15], v[20:23]\n\t"
      "v_xor_b32 v58, 128, v56\n\t"
      "v_xor_b32 v59, 192, v56\n\t"
      "v_mfma_f32_16x16x32_bf16 v[20:23], v[132:135], a[16:19], v[20:23]\n\t"
      "v_mfma_f32_16x16x32_bf16 v[20:23], v[148:151], a[20:23], v[20:23]\n\t"
      "v_mfma_f32_16x16x32_bf16 v[24:27], v[72:75], a[0:3], 0\n\t"
      "v_mfma_f32_16x16x32_bf16 v[24:27], v[88:91], a[4:7], v[24:27]\n\t"
      "v_mfma_f32_16x16x32_bf16 v[24:27], v[104:107], a[8:11], v[24:27]\n\t"
      "v_mfma_f32_16x16x32_bf16 v[24:27], v[120:123], a[12:15], v[24:27]\n\t"
      "s_waitcnt vmcnt(12)\n\t"
      "s_waitcnt lgkmcnt(0)\n\t"
      "s_barrier\n\t"
      "v_mfma_f32_16x16x32_bf16 v[24:27], v[136:139], a[16:19], v[24:27]\n\t"
      "v_mfma_f32_16x16x32_bf16 v[24:27], v[152:155], a[20:23], v[24:27]\n\t"
      "v_mfma_f32_16x16x32_bf16 v[28:31], v[76:79], a[0:3], 0\n\t"
      "ds_read_b128 a[48:51], v8 offset:0\n\t"
      "v_mfma_f32_16x16x32_bf16 v[28:31], v[92:95], a[4:7], v[28:31]\n\t"
      "ds_read_b128 a[52:55], v60 offset:1024\n\t"
      "v_mfma_f32_16x16x32_bf16 v[28:31], v[108:111], a[8:11], v[28:31]\n\t"
      "ds_read_b128 a[56:59], v61 offset:2048\n\t"
      "v_mfma_f32_16x16x32_bf16 v[28:31], v[124:127], a[12:15], v[28:31]\n\t"
      "v_mfma_f32_16x16x32_bf16 v[28:31], v[140:143], a[16:19], v[28:31]\n\t"
      "v_mfma_f32_16x16x32_bf16 v[28:31], v[156:159], a[20:23], v[28:31]\n\t"
      "v_mfma_f32_16x16x32_bf16 v[32:35], a[60:63], a[0:3], 0\n\t"
      "v_mfma_f32_16x16x32_bf16 v[32:35], a[68:71], a[4:7], v[32:35]\n\t"
      "s_waitcnt lgkmcnt(2)\n\t"
      "v_mfma_f32_16x16x32_bf16 v[32:35], a[76:79], a[8:11], v[32:35]\n\t"
      "buffer_store_dwordx4 a[48:51], v9, s[44:47], s49 offen sc1\n\t"
      "v_mfma_f32_16x16x32_bf16 v[32:35], a[84:87], a[12:15], v[32:35]\n\t"
      "v_mfma_f32_16x16x32_bf16 v[32:35], a[92:95], a[16:19], v[32:35]\n\t"
      "v_mfma_f32_16x16x32_bf16 v[32:35], a[100:103], a[20:23], v[32:35]\n\t"
      "s_add_u32 m0, s18, s50\n\t"
      "v_mfma_f32_16x16x32_bf16 v[36:39], a[64:67], a[0:3], 0\n\t"
      "buffer_load_dwordx4 v5, s[40:43], s48 offen nt lds\n\t"
      "v_mfma_f32_16x16x32_bf16 v[36:39], a[72:75], a[4:7], v[36:39]\n\t"
      "v_mfma_f32_16x16x32_bf16 v[36:39], a[80:83], a[8:11], v[36:39]\n\t"
      "v_mfma_f32_16x16x32_bf16 v[36:39], a[88:91], a[12:15], v[36:39]\n\t"
      "s_waitcnt lgkmcnt(1)\n\t"
      "v_mfma_f32_16x16x32_bf16 v[36:39], a[96:99], a[16:19], v[36:39]\n\t"
      "buffer_store_dwordx4 a[52:55], v10, s[44:47], s49 offen offset:1024 sc1\n\t"
      "v_mfma_f32_16x16x32_bf16 v[36:39], a[104:107], a[20:23], v[36:39]\n\t"
      "v_mfma_f32_16x16x32_bf16 v[16:19], v[160:163], a[24:27], v[16:19]\n\t"
      "ds_read_b128 a[0:3], v56 offset:0\n\t"
      "v_mfma_f32_16x16x32_bf16 v[16:19], v[176:179], a[28:31], v[16:19]\n\t"
      "ds_read_b128 a[4:7], v57 offset:0\n\t"
      "v_mfma_f32_16x16x32_bf16 v[16:19], v[192:195], a[32:35], v[16:19]\n\t"
      "ds_read_b128 a[8:11], v58 offset:0\n\t"
      "v_mfma_f32_16x16x32_bf16 v[16:19], v[208:211], a[36:39], v[16:19]\n\t"
      "ds_read_b128 a[12:15], v59 offset:0\n\t"
      "v_mfma_f32_16x16x32_bf16 v[16:19], v[224:227], a[40:43], v[16:19]\n\t"
      "ds_read_b128 a[16:19], v56 offset:256\n\t"
      "v_mfma_f32_16x16x32_bf16 v[16:19], v[240:243], a[44:47], v[16:19]\n\t"
      "ds_read_b128 a[20:23], v57 offset:256\n\t"
      "v_mfma_f32_16x16x32_bf16 v[20:23], v[164:167], a[24:27], v[20:23]\n\t"
      "s_add_u32 s51, s51, 12288\n\t"
      "s_cmp_eq_u32 s51, 160256\n\t"
      "s_cselect_b32 s51, 25088, s51\n\t"
      "v_mfma_f32_16x16x32_bf16 v[20:23], v[180:183], a[28:31], v[20:23]\n\t"
      "s_waitcnt lgkmcnt(6)\n\t"
      "v_mfma_f32_16x16x32_bf16 v[20:23], v[196:199], a[32:35], v[20:23]\n\t"
      "buffer_store_dwordx4 a[56:59], v11, s[44:47], s49 offen offset:2048 sc1\n\t"
      "v_mfma_f32_16x16x32_bf16 v[20:23], v[212:215], a[36:39], v[20:23]\n\t"
      "v_mfma_f32_16x16x32_bf16 v[20:23], v[228:231], a[40:43], v[20:23]\n\t"
      "s_waitcnt lgkmcnt(0)\n\t"
      "v_mfma_f32_16x16x32_bf16 v[20:23], v[244:247], a[44:47], v[20:23]\n\t"
      "v_mfma_f32_16x16x32_bf16 v[24:27], v[168:171], a[24:27], v[24:27]\n\t"
      "v_mfma_f32_16x16x32_bf16 v[24:27], v[184:187], a[28:31], v[24:27]\n\t"
      "v_cvt_pk_bf16_f32 v40, v16, v17\n\t"
      "v_cvt_pk_bf16_f32 v41, v18, v19\n\t"
      "v_mfma_f32_16x16x32_bf16 v[24:27], v[200:203], a[32:35], v[24:27]\n\t"
      "ds_write_b64 v0, v[40:41] offset:12544\n\t"
      "v_mfma_f32_16x16x32_bf16 v[24:27], v[216:219], a[36:39], v[24:27]\n\t"
      "buffer_load_dwordx4 v6, s[40:43], s48 offen offset:1024 nt lds\n\t"
      "v_mfma_f32_16x16x32_bf16 v[24:27], v[232:235], a[40:43], v[24:27]\n\t"
      "v_mfma_f32_16x16x32_bf16 v[24:27], v[248:251], a[44:47], v[24:27]\n\t"
      "v_mfma_f32_16x16x32_bf16 v[28:31], v[172:175], a[24:27], v[28:31]\n\t"
      "v_mfma_f32_16x16x32_bf16 v[28:31], v[188:191], a[28:31], v[28:31]\n\t"
      "v_cvt_pk_bf16_f32 v42, v20, v21\n\t"
      "v_cvt_pk_bf16_f32 v43, v22, v23\n\t"
      "v_mfma_f32_16x16x32_bf16 v[28:31], v[204:207], a[32:35], v[28:31]\n\t"
      "ds_write_b64 v0, v[42:43] offset:12576\n\t"
      "v_mfma_f32_16x16x32_bf16 v[28:31], v[220:223], a[36:39], v[28:31]\n\t"
      "v_mfma_f32_16x16x32_bf16 v[28:31], v[236:239], a[40:43], v[28:31]\n\t"
      "v_mfma_f32_16x16x32_bf16 v[28:31], v[252:255], a[44:47], v[28:31]\n\t"
      "v_mfma_f32_16x16x32_bf16 v[32:35], a[108:111], a[24:27], v[32:35]\n\t"
      "v_mfma_f32_16x16x32_bf16 v[32:35], a[116:119], a[28:31], v[32:35]\n\t"
      "v_cvt_pk_bf16_f32 v44, v24, v25\n\t"
      "v_cvt_pk_bf16_f32 v45, v26, v27\n\t"
      "v_mfma_f32_16x16x32_bf16 v[32:35], a[124:127], a[32:35], v[32:35]\n\t"
      "ds_write_b64 v0, v[44:45] offset:12608\n\t"
      "v_mfma_f32_16x16x32_bf16 v[32:35], a[132:135], a[36:39], v[32:35]\n\t"
      "buffer_load_dwordx4 v7, s[40:43], s48 offen offset:2048 nt lds\n\t"
      "v_mfma_f32_16x16x32_bf16 v[32:35], a[140:143], a[40:43], v[32:35]\n\t"
      "s_add_u32 s50, s50, 12288\n\t"
      "v_mfma_f32_16x16x32_bf16 v[32:35], a[148:151], a[44:47], v[32:35]\n\t"
      "v_cvt_pk_bf16_f32 v46, v28, v29\n\t"
      "v_cvt_pk_bf16_f32 v47, v30, v31\n\t"
      "v_mfma_f32_16x16x32_bf16 v[36:39], a[112:115], a[24:27], v[36:39]\n\t"
      "ds_write_b64 v0, v[46:47] offset:12640\n\t"
      "v_mfma_f32_16x16x32_bf16 v[36:39], a[120:123], a[28:31], v[36:39]\n\t"
      "s_cmp_eq_u32 s50, 160256\n\t"
      "s_cselect_b32 s50, 25088, s50\n\t"
      "v_mfma_f32_16x16x32_bf16 v[36:39], a[128:131], a[32:35], v[36:39]\n\t"
      "s_sub_u32 s52, s52, 1\n\t"
      "s_cmp_lg_u32 s52, 0\n\t"
      "v_mfma_f32_16x16x32_bf16 v[36:39], a[136:139], a[36:39], v[36:39]\n\t"
      "v_mfma_f32_16x16x32_bf16 v[36:39], a[144:147], a[40:43], v[36:39]\n\t"
      "v_cvt_pk_bf16_f32 v48, v32, v33\n\t"
      "v_cvt_pk_bf16_f32 v49, v34, v35\n\t"
      "v_mfma_f32_16x16x32_bf16 v[36:39], a[152:155], a[44:47], v[36:39]\n\t"
      "ds_write_b64 v0, v[48:49] offset:12672\n\t"
      "s_cbranch_scc1 .Lloop1_%=\n\t"
      "s_branch .Lendc_%=\n\t"
      ".Lloop2_%=:\n\t"
      "v_mfma_f32_16x16x32_bf16 v[16:19], v[64:67], a[0:3], 0\n\t"
      "ds_read_b128 a[24:27], v58 offset:256\n\t"
      "v_mfma_f32_16x16x32_bf16 v[16:19], v[80:83], a[4:7], v[16:19]\n\t"
      "ds_read_b128 a[28:31], v59 offset:256\n\t"
      "v_mfma_f32_16x16x32_bf16 v[16:19], v[96:99], a[8:11], v[16:19]\n\t"
      "ds_read_b128 a[32:35], v56 offset:512\n\t"
      "v_mfma_f32_16x16x32_bf16 v[16:19], v[112:115], a[12:15], v[16:19]\n\t"
      "ds_read_b128 a[36:39], v57 offset:512\n\t"
      "v_mfma_f32_16x16x32_bf16 v[16:19], v[128:131], a[16:19], v[16:19]\n\t"
      "ds_read_b128 a[40:43], v58 offset:512\n\t"
      "v_mfma_f32_16x16x32_bf16 v[16:19], v[144:147], a[20:23], v[16:19]\n\t"
      "ds_read_b128 a[44:47], v59 offset:512\n\t"
      "v_mfma_f32_16x16x32_bf16 v[20:23], v[68:71], a[0:3], 0\n\t"
      "v_cvt_pk_bf16_f32 v50, v36, v37\n\t"
      "v_cvt_pk_bf16_f32 v51, v38, v39\n\t"
      "v_mfma_f32_16x16x32_bf16 v[20:23], v[84:87], a[4:7], v[20:23]\n\t"
      "ds_write_b64 v0, v[50:51] offset:12704\n\t"
      "v_mfma_f32_16x16x32_bf16 v[20:23], v[100:103], a[8:11], v[20:23]\n\t"
      "v_add_u32 v52, s51, v15\n\t"
      "v_xor_b32 v53, 64, v52\n\t"
      "v_mfma_f32_16x16x32_bf16 v[20:23], v[116:119], a[12:15], v[20:23]\n\t"
      "v_xor_b32 v54, 128, v52\n\t"
      "v_xor_b32 v55, 192, v52\n\t"
      "v_mfma_f32_16x16x32_bf16 v[20:23], v[132:135], a[16:19], v[20:23]\n\t"
      "s_add_u32 s56, s56, 786432\n\t"
      "s_lshr_b32 s57, s57, 1\n\t"
      "v_mfma_f32_16x16x32_bf16 v[20:23], v[148:151], a[20:23], v[20:23]\n\t"
      "s_bitcmp1_b32 s57, 0\n\t"
      "s_cselect_b32 s48, 24576, 0\n\t"
      "s_add_u32 s48, s48, s56\n\t"
      "v_mfma_f32_16x16x32_bf16 v[24:27], v[72:75], a[0:3], 0\n\t"
      "v_mfma_f32_16x16x32_bf16 v[24:27], v[88:91], a[4:7], v[24:27]\n\t"
      "v_mfma_f32_16x16x32_bf16 v[24:27], v[104:107], a[8:11], v[24:27]\n\t"
      "v_mfma_f32_16x16x32_bf16 v[24:27], v[120:123], a[12:15], v[24:27]\n\t"
      "v_mfma_f32_16x16x32_bf16 v[24:27], v[136:139], a[16:19], v[24:27]\n\t"
      "s_waitcnt vmcnt(12)\n\t"
      "s_waitcnt lgkmcnt(0)\n\t"
      "s_barrier\n\t"
      "v_mfma_f32_16x16x32_bf16 v[24:27], v[152:155], a[20:23], v[24:27]\n\t"
      "v_mfma_f32_16x16x32_bf16 v[28:31], v[76:79], a[0:3], 0\n\t"
      "ds_read_b128 a[48:51], v8 offset:12544\n\t"
      "v_mfma_f32_16x16x32_bf16 v[28:31], v[92:95], a[4:7], v[28:31]\n\t"
      "ds_read_b128 a[52:55], v60 offset:13568\n\t"
      "v_mfma_f32_16x16x32_bf16 v[28:31], v[108:111], a[8:11], v[28:31]\n\t"
      "ds_read_b128 a[56:59], v61 offset:14592\n\t"
      "v_mfma_f32_16x16x32_bf16 v[28:31], v[124:127], a[12:15], v[28:31]\n\t"
      "v_mfma_f32_16x16x32_bf16 v[28:31], v[140:143], a[16:19], v[28:31]\n\t"
      "v_mfma_f32_16x16x32_bf16 v[28:31], v[156:159], a[20:23], v[28:31]\n\t"
      "v_mfma_f32_16x16x32_bf16 v[32:35], a[60:63], a[0:3], 0\n\t"
      "v_mfma_f32_16x16x32_bf16 v[32:35], a[68:71], a[4:7], v[32:35]\n\t"
      "s_waitcnt lgkmcnt(2)\n\t"
      "v_mfma_f32_16x16x32_bf16 v[32:35], a[76:79], a[8:11], v[32:35]\n\t"
      "buffer_store_dwordx4 a[48:51], v12, s[44:47], s49 offen sc1\n\t"
      "v_mfma_f32_16x16x32_bf16 v[32:35], a[84:87], a[12:15], v[32:35]\n\t"
      "v_mfma_f32_16x16x32_bf16 v[32:35], a[92:95], a[16:19], v[32:35]\n\t"
      "v_mfma_f32_16x16x32_bf16 v[32:35], a[100:103], a[20:23], v[32:35]\n\t"
      "s_add_u32 m0, s18, s50\n\t"
      "v_mfma_f32_16x16x32_bf16 v[36:39], a[64:67], a[0:3], 0\n\t"
      "buffer_load_dwordx4 v2, s[40:43], s48 offen nt lds\n\t"
      "v_mfma_f32_16x16x32_bf16 v[36:39], a[72:75], a[4:7], v[36:39]\n\t"
      "v_mfma_f32_16x16x32_bf16 v[36:39], a[80:83], a[8:11], v[36:39]\n\t"
      "v_mfma_f32_16x16x32_bf16 v[36:39], a[88:91], a[12:15], v[36:39]\n\t"
      "s_waitcnt lgkmcnt(1)\n\t"
      "v_mfma_f32_16x16x32_bf16 v[36:39], a[96:99], a[16:19], v[36:39]\n\t"
      "buffer_store_dwordx4 a[52:55], v13, s[44:47], s49 offen offset:1024 sc1\n\t"
      "v_mfma_f32_16x16x32_bf16 v[36:39], a[104:107], a[20:23], v[36:39]\n\t"
      "v_mfma_f32_16x16x32_bf16 v[16:19], v[160:163], a[24:27], v[16:19]\n\t"
      "ds_read_b128 a[0:3], v52 offset:0\n\t"
      "v_mfma_f32_16x16x32_bf16 v[16:19], v[176:179], a[28:31], v[16:19]\n\t"
      "ds_read_b128 a[4:7], v53 offset:0\n\t"
      "v_mfma_f32_16x16x32_bf16 v[16:19], v[192:195], a[32:35], v[16:19]\n\t"
      "ds_read_b128 a[8:11], v54 offset:0\n\t"
      "v_mfma_f32_16x16x32_bf16 v[16:19], v[208:211], a[36:39], v[16:19]\n\t"
      "ds_read_b128 a[12:15], v55 offset:0\n\t"
      "v_mfma_f32_16x16x32_bf16 v[16:19], v[224:227], a[40:43], v[16:19]\n\t"
      "ds_read_b128 a[16:19], v52 offset:256\n\t"
      "v_mfma_f32_16x16x32_bf16 v[16:19], v[240:243], a[44:47], v[16:19]\n\t"
      "ds_read_b128 a[20:23], v53 offset:256\n\t"
      "v_mfma_f32_16x16x32_bf16 v[20:23], v[164:167], a[24:27], v[20:23]\n\t"
      "s_add_u32 s51, s51, 12288\n\t"
      "s_cmp_eq_u32 s51, 160256\n\t"
      "s_cselect_b32 s51, 25088, s51\n\t"
      "v_mfma_f32_16x16x32_bf16 v[20:23], v[180:183], a[28:31], v[20:23]\n\t"
      "s_waitcnt lgkmcnt(6)\n\t"
      "v_mfma_f32_16x16x32_bf16 v[20:23], v[196:199], a[32:35], v[20:23]\n\t"
      "buffer_store_dwordx4 a[56:59], v14, s[44:47], s49 offen offset:2048 sc1\n\t"
      "v_mfma_f32_16x16x32_bf16 v[20:23], v[212:215], a[36:39], v[20:23]\n\t"
      "v_mfma_f32_16x16x32_bf16 v[20:23], v[228:231], a[40:43], v[20:23]\n\t"
      "s_waitcnt lgkmcnt(0)\n\t"
      "v_mfma_f32_16x16x32_bf16 v[20:23], v[244:247], a[44:47], v[20:23]\n\t"
      "v_mfma_f32_16x16x32_bf16 v[24:27], v[168:171], a[24:27], v[24:27]\n\t"
      "v_mfma_f32_16x16x32_bf16 v[24:27], v[184:187], a[28:31], v[24:27]\n\t"
      "v_cvt_pk_bf16_f32 v40, v16, v17\n\t"
      "v_cvt_pk_bf16_f32 v41, v18, v19\n\t"
      "v_mfma_f32_16x16x32_bf16 v[24:27], v[200:203], a[32:35], v[24:27]\n\t"
      "ds_write_b64 v0, v[40:41] offset:0\n\t"
      "v_mfma_f32_16x16x32_bf16 v[24:27], v[216:219], a[36:39], v[24:27]\n\t"
      "buffer_load_dwordx4 v3, s[40:43], s48 offen offset:1024 nt lds\n\t"
      "v_mfma_f32_16x16x32_bf16 v[24:27], v[232:235], a[40:43], v[24:27]\n\t"
      "s_add_u32 s58, s58, 786432\n\t"
      "s_lshr_b32 s59, s59, 1\n\t"
      "v_mfma_f32_16x16x32_bf16 v[24:27], v[248:251], a[44:47], v[24:27]\n\t"
      "s_bitcmp1_b32 s59, 0\n\t"
      "s_cselect_b32 s49, 24576, 0\n\t"
      "s_add_u32 s49, s49, s58\n\t"
      "v_mfma_f32_16x16x32_bf16 v[28:31], v[172:175], a[24:27], v[28:31]\n\t"
      "v_mfma_f32_16x16x32_bf16 v[28:31], v[188:191], a[28:31], v[28:31]\n\t"
      "v_cvt_pk_bf16_f32 v42, v20, v21\n\t"
      "v_cvt_pk_bf16_f32 v43, v22, v23\n\t"
      "v_mfma_f32_16x16x32_bf16 v[28:31], v[204:207], a[32:35], v[28:31]\n\t"
      "ds_write_b64 v0, v[42:43] offset:32\n\t"
      "v_mfma_f32_16x16x32_bf16 v[28:31], v[220:223], a[36:39], v[28:31]\n\t"
      "v_mfma_f32_16x16x32_bf16 v[28:31], v[236:239], a[40:43], v[28:31]\n\t"
      "v_mfma_f32_16x16x32_bf16 v[28:31], v[252:255], a[44:47], v[28:31]\n\t"
      "v_mfma_f32_16x16x32_bf16 v[32:35], a[108:111], a[24:27], v[32:35]\n\t"
      "v_mfma_f32_16x16x32_bf16 v[32:35], a[116:119], a[28:31], v[32:35]\n\t"
      "v_cvt_pk_bf16_f32 v44, v24, v25\n\t"
      "v_cvt_pk_bf16_f32 v45, v26, v27\n\t"
      "v_mfma_f32_16x16x32_bf16 v[32:35], a[124:127], a[32:35], v[32:35]\n\t"
      "ds_write_b64 v0, v[44:45] offset:64\n\t"
      "v_mfma_f32_16x16x32_bf16 v[32:35], a[132:135], a[36:39], v[32:35]\n\t"
      "buffer_load_dwordx4 v4, s[40:43], s48 offen offset:2048 nt lds\n\t"
      "v_mfma_f32_16x16x32_bf16 v[32:35], a[140:143], a[40:43], v[32:35]\n\t"
      "s_add_u32 s50, s50, 12288\n\t"
      "v_mfma_f32_16x16x32_bf16 v[32:35], a[148:151], a[44:47], v[32:35]\n\t"
      "v_cvt_pk_bf16_f32 v46, v28, v29\n\t"
      "v_cvt_pk_bf16_f32 v47, v30, v31\n\t"
      "v_mfma_f32_16x16x32_bf16 v[36:39], a[112:115], a[24:27], v[36:39]\n\t"
      "ds_write_b64 v0, v[46:47] offset:96\n\t"
      "v_mfma_f32_16x16x32_bf16 v[36:39], a[120:123], a[28:31], v[36:39]\n\t"
      "s_cmp_eq_u32 s50, 160256\n\t"
      "s_cselect_b32 s50, 25088, s50\n\t"
      "v_mfma_f32_16x16x32_bf16 v[36:39], a[128:131], a[32:35], v[36:39]\n\t"
      "v_mfma_f32_16x16x32_bf16 v[36:39], a[136:139], a[36:39], v[36:39]\n\t"
      "v_mfma_f32_16x16x32_bf16 v[36:39], a[144:147], a[40:43], v[36:39]\n\t"
      "v_cvt_pk_bf16_f32 v48, v32, v33\n\t"
      "v_cvt_pk_bf16_f32 v49, v34, v35\n\t"
      "v_mfma_f32_16x16x32_bf16 v[36:39], a[152:155], a[44:47], v[36:39]\n\t"
      "ds_write_b64 v0, v[48:49] offset:128\n\t"
      ".Lmid2_%=:\n\t"
      "v_mfma_f32_16x16x32_bf16 v[16:19], v[64:67], a[0:3], 0\n\t"
      "ds_read_b128 a[24:27], v54 offset:256\n\t"
      "v_mfma_f32_16x16x32_bf16 v[16:19], v[80:83], a[4:7], v[16:19]\n\t"
      "ds_read_b128 a[28:31], v55 offset:256\n\t"
      "v_mfma_f32_16x16x32_bf16 v[16:19], v[96:99], a[8:11], v[16:19]\n\t"
      "ds_read_b128 a[32:35], v52 offset:512\n\t"
      "v_mfma_f32_16x16x32_bf16 v[16:19], v[112:115], a[12:15], v[16:19]\n\t"
      "ds_read_b128 a[36:39], v53 offset:512\n\t"
      "v_mfma_f32_16x16x32_bf16 v[16:19], v[128:131], a[16:19], v[16:19]\n\t"
      "ds_read_b128 a[40:43], v54 offset:512\n\t"
      "v_mfma_f32_16x16x32_bf16 v[16:19], v[144:147], a[20:23], v[16:19]\n\t"
      "ds_read_b128 a[44:47], v55 offset:512\n\t"
      "v_mfma_f32_16x16x32_bf16 v[20:23], v[68:71], a[0:3], 0\n\t"
      "v_cvt_pk_bf16_f32 v50, v36, v37\n\t"
      "v_cvt_pk_bf16_f32 v51, v38, v39\n\t"
      "v_mfma_f32_16x16x32_bf16 v[20:23], v[84:87], a[4:7], v[20:23]\n\t"
      "ds_write_b64 v0, v[50:51] offset:160\n\t"
      "v_mfma_f32_16x16x32_bf16 v[20:23], v[100:103], a[8:11], v[20:23]\n\t"
      "v_add_u32 v56, s51, v15\n\t"
      "v_xor_b32 v57, 64, v56\n\t"
      "v_mfma_f32_16x16x32_bf16 v[20:23], v[116:119], a[12:15], v[20:23]\n\t"
      "v_xor_b32 v58, 128, v56\n\t"
      "v_xor_b32 v59, 192, v56\n\t"
      "v_mfma_f32_16x16x32_bf16 v[20:23], v[132:135], a[16:19], v[20:23]\n\t"
      "v_mfma_f32_16x16x32_bf16 v[20:23], v[148:151], a[20:23], v[20:23]\n\t"
      "v_mfma_f32_16x16x32_bf16 v[24:27], v[72:75], a[0:3], 0\n\t"
      "v_mfma_f32_16x16x32_bf16 v[24:27], v[88:91], a[4:7], v[24:27]\n\t"
      "v_mfma_f32_16x16x32_bf16 v[24:27], v[104:107], a[8:11], v[24:27]\n\t"
      "v_mfma_f32_16x16x32_bf16 v[24:27], v[120:123], a[12:15], v[24:27]\n\t"
      "v_mfma_f32_16x16x32_bf16 v[24:27], v[136:139], a[16:19], v[24:27]\n\t"
      "s_waitcnt vmcnt(12)\n\t"
      "s_waitcnt lgkmcnt(0)\n\t"
      "s_barrier\n\t"
      "v_mfma_f32_16x16x32_bf16 v[24:27], v[152:155], a[20:23], v[24:27]\n\t"
      "v_mfma_f32_16x16x32_bf16 v[28:31], v[76:79], a[0:3], 0\n\t"
      "ds_read_b128 a[48:51], v8 offset:0\n\t"
      "v_mfma_f32_16x16x32_bf16 v[28:31], v[92:95], a[4:7], v[28:31]\n\t"
      "ds_read_b128 a[52:55], v60 offset:1024\n\t"
      "v_mfma_f32_16x16x32_bf16 v[28:31], v[108:111], a[8:11], v[28:31]\n\t"
      "ds_read_b128 a[56:59], v61 offset:2048\n\t"
      "v_mfma_f32_16x16x32_bf16 v[28:31], v[124:127], a[12:15], v[28:31]\n\t"
      "v_mfma_f32_16x16x32_bf16 v[28:31], v[140:143], a[16:19], v[28:31]\n\t"
      "v_mfma_f32_16x16x32_bf16 v[28:31], v[156:159], a[20:23], v[28:31]\n\t"
      "v_mfma_f32_16x16x32_bf16 v[32:35], a[60:63], a[0:3], 0\n\t"
      "v_mfma_f32_16x16x32_bf16 v[32:35], a[68:71], a[4:7], v[32:35]\n\t"
      "s_waitcnt lgkmcnt(2)\n\t"
      "v_mfma_f32_16x16x32_bf16 v[32:35], a[76:79], a[8:11], v[32:35]\n\t"
      "buffer_store_dwordx4 a[48:51], v9, s[44:47], s49 offen sc1\n\t"
      "v_mfma_f32_16x16x32_bf16 v[32:35], a[84:87], a[12:15], v[32:35]\n\t"
      "v_mfma_f32_16x16x32_bf16 v[32:35], a[92:95], a[16:19], v[32:35]\n\t"
      "v_mfma_f32_16x16x32_bf16 v[32:35], a[100:103], a[20:23], v[32:35]\n\t"
      "s_add_u32 m0, s18, s50\n\t"
      "v_mfma_f32_16x16x32_bf16 v[36:39], a[64:67], a[0:3], 0\n\t"
      "buffer_load_dwordx4 v5, s[40:43], s48 offen nt lds\n\t"
      "v_mfma_f32_16x16x32_bf16 v[36:39], a[72:75], a[4:7], v[36:39]\n\t"
      "v_mfma_f32_16x16x32_bf16 v[36:39], a[80:83], a[8:11], v[36:39]\n\t"
      "v_mfma_f32_16x16x32_bf16 v[36:39], a[88:91], a[12:15], v[36:39]\n\t"
      "s_waitcnt lgkmcnt(1)\n\t"
      "v_mfma_f32_16x16x32_bf16 v[36:39], a[96:99], a[16:19], v[36:39]\n\t"
      "buffer_store_dwordx4 a[52:55], v10, s[44:47], s49 offen offset:1024 sc1\n\t"
      "v_mfma_f32_16x16x32_bf16 v[36:39], a[104:107], a[20:23], v[36:39]\n\t"
      "v_mfma_f32_16x16x32_bf16 v[16:19], v[160:163], a[24:27], v[16:19]\n\t"
      "ds_read_b128 a[0:3], v56 offset:0\n\t"
      "v_mfma_f32_16x16x32_bf16 v[16:19], v[176:179], a[28:31], v[16:19]\n\t"
      "ds_read_b128 a[4:7], v57 offset:0\n\t"
      "v_mfma_f32_16x16x32_bf16 v[16:19], v[192:195], a[32:35], v[16:19]\n\t"
      "ds_read_b128 a[8:11], v58 offset:0\n\t"
      "v_mfma_f32_16x16x32_bf16 v[16:19], v[208:211], a[36:39], v[16:19]\n\t"
      "ds_read_b128 a[12:15], v59 offset:0\n\t"
      "v_mfma_f32_16x16x32_bf16 v[16:19], v[224:227], a[40:43], v[16:19]\n\t"
      "ds_read_b128 a[16:19], v56 offset:256\n\t"
      "v_mfma_f32_16x16x32_bf16 v[16:19], v[240:243], a[44:47], v[16:19]\n\t"
      "ds_read_b128 a[20:23], v57 offset:256\n\t"
      "v_mfma_f32_16x16x32_bf16 v[20:23], v[164:167], a[24:27], v[20:23]\n\t"
      "s_add_u32 s51, s51, 12288\n\t"
      "s_cmp_eq_u32 s51, 160256\n\t"
      "s_cselect_b32 s51, 25088, s51\n\t"
      "v_mfma_f32_16x16x32_bf16 v[20:23], v[180:183], a[28:31], v[20:23]\n\t"
      "s_waitcnt lgkmcnt(6)\n\t"
      "v_mfma_f32_16x16x32_bf16 v[20:23], v[196:199], a[32:35], v[20:23]\n\t"
      "buffer_store_dwordx4 a[56:59], v11, s[44:47], s49 offen offset:2048 sc1\n\t"
      "v_mfma_f32_16x16x32_bf16 v[20:23], v[212:215], a[36:39], v[20:23]\n\t"
      "v_mfma_f32_16x16x32_bf16 v[20:23], v[228:231], a[40:43], v[20:23]\n\t"
      "s_waitcnt lgkmcnt(0)\n\t"
      "v_mfma_f32_16x16x32_bf16 v[20:23], v[244:247], a[44:47], v[20:23]\n\t"
      "v_mfma_f32_16x16x32_bf16 v[24:27], v[168:171], a[24:27], v[24:27]\n\t"
      "v_mfma_f32_16x16x32_bf16 v[24:27], v[184:187], a[28:31], v[24:27]\n\t"
      "v_cvt_pk_bf16_f32 v40, v16, v17\n\t"
      "v_cvt_pk_bf16_f32 v41, v18, v19\n\t"
      "v_mfma_f32_16x16x32_bf16 v[24:27], v[200:203], a[32:35], v[24:27]\n\t"
      "ds_write_b64 v0, v[40:41] offset:12544\n\t"
      "v_mfma_f32_16x16x32_bf16 v[24:27], v[216:219], a[36:39], v[24:27]\n\t"
      "buffer_load_dwordx4 v6, s[40:43], s48 offen offset:1024 nt lds\n\t"
      "v_mfma_f32_16x16x32_bf16 v[24:27], v[232:235], a[40:43], v[24:27]\n\t"
      "v_mfma_f32_16x16x32_bf16 v[24:27], v[248:251], a[44:47], v[24:27]\n\t"
      "v_mfma_f32_16x16x32_bf16 v[28:31], v[172:175], a[24:27], v[28:31]\n\t"
      "v_mfma_f32_16x16x32_bf16 v[28:31], v[188:191], a[28:31], v[28:31]\n\t"
      "v_cvt_pk_bf16_f32 v42, v20, v21\n\t"
      "v_cvt_pk_bf16_f32 v43, v22, v23\n\t"
      "v_mfma_f32_16x16x32_bf16 v[28:31], v[204:207], a[32:35], v[28:31]\n\t"
      "ds_write_b64 v0, v[42:43] offset:12576\n\t"
      "v_mfma_f32_16x16x32_bf16 v[28:31], v[220:223], a[36:39], v[28:31]\n\t"
      "v_mfma_f32_16x16x32_bf16 v[28:31], v[236:239], a[40:43], v[28:31]\n\t"
      "v_mfma_f32_16x16x32_bf16 v[28:31], v[252:255], a[44:47], v[28:31]\n\t"
      "v_mfma_f32_16x16x32_bf16 v[32:35], a[108:111], a[24:27], v[32:35]\n\t"
      "v_mfma_f32_16x16x32_bf16 v[32:35], a[116:119], a[28:31], v[32:35]\n\t"
      "v_cvt_pk_bf16_f32 v44, v24, v25\n\t"
      "v_cvt_pk_bf16_f32 v45, v26, v27\n\t"
      "v_mfma_f32_16x16x32_bf16 v[32:35], a[124:127], a[32:35], v[32:35]\n\t"
      "ds_write_b64 v0, v[44:45] offset:12608\n\t"
      "v_mfma_f32_16x16x32_bf16 v[32:35], a[132:135], a[36:39], v[32:35]\n\t"
      "buffer_load_dwordx4 v7, s[40:43], s48 offen offset:2048 nt lds\n\t"
      "v_mfma_f32_16x16x32_bf16 v[32:35], a[140:143], a[40:43], v[32:35]\n\t"
      "s_add_u32 s50, s50, 12288\n\t"
      "v_mfma_f32_16x16x32_bf16 v[32:35], a[148:151], a[44:47], v[32:35]\n\t"
      "v_cvt_pk_bf16_f32 v46, v28, v29\n\t"
      "v_cvt_pk_bf16_f32 v47, v30, v31\n\t"
      "v_mfma_f32_16x16x32_bf16 v[36:39], a[112:115], a[24:27], v[36:39]\n\t"
      "ds_write_b64 v0, v[46:47] offset:12640\n\t"
      "v_mfma_f32_16x16x32_bf16 v[36:39], a[120:123], a[28:31], v[36:39]\n\t"
      "s_cmp_eq_u32 s50, 160256\n\t"
      "s_cselect_b32 s50, 25088, s50\n\t"
      "v_mfma_f32_16x16x32_bf16 v[36:39], a[128:131], a[32:35], v[36:39]\n\t"
      "s_sub_u32 s52, s52, 1\n\t"
      "s_cmp_lg_u32 s52, 0\n\t"
      "v_mfma_f32_16x16x32_bf16 v[36:39], a[136:139], a[36:39], v[36:39]\n\t"
      "v_mfma_f32_16x16x32_bf16 v[36:39], a[144:147], a[40:43], v[36:39]\n\t"
      "v_cvt_pk_bf16_f32 v48, v32, v33\n\t"
      "v_cvt_pk_bf16_f32 v49, v34, v35\n\t"
      "v_mfma_f32_16x16x32_bf16 v[36:39], a[152:155], a[44:47], v[36:39]\n\t"
      "ds_write_b64 v0, v[48:49] offset:12672\n\t"
      "s_cbranch_scc1 .Lloop2_%=\n\t"
      "s_branch .Lendc_%=\n\t"
      ".Lloop3_%=:\n\t"
      "v_mfma_f32_16x16x32_bf16 v[16:19], v[64:67], a[0:3], 0\n\t"
      "ds_read_b128 a[24:27], v58 offset:256\n\t"
      "v_mfma_f32_16x16x32_bf16 v[16:19], v[80:83], a[4:7], v[16:19]\n\t"
      "ds_read_b128 a[28:31], v59 offset:256\n\t"
      "v_mfma_f32_16x16x32_bf16 v[16:19], v[96:99], a[8:11], v[16:19]\n\t"
      "ds_read_b128 a[32:35], v56 offset:512\n\t"
      "v_mfma_f32_16x16x32_bf16 v[16:19], v[112:115], a[12:15], v[16:19]\n\t"
      "ds_read_b128 a[36:39], v57 offset:512\n\t"
      "v_mfma_f32_16x16x32_bf16 v[16:19], v[128:131], a[16:19], v[16:19]\n\t"
      "ds_read_b128 a[40:43], v58 offset:512\n\t"
      "v_mfma_f32_16x16x32_bf16 v[16:19], v[144:147], a[20:23], v[16:19]\n\t"
      "ds_read_b128 a[44:47], v59 offset:512\n\t"
      "v_mfma_f32_16x16x32_bf16 v[20:23], v[68:71], a[0:3], 0\n\t"
      "v_cvt_pk_bf16_f32 v50, v36, v37\n\t"
      "v_cvt_pk_bf16_f32 v51, v38, v39\n\t"
      "v_mfma_f32_16x16x32_bf16 v[20:23], v[84:87], a[4:7], v[20:23]\n\t"
      "ds_write_b64 v0, v[50:51] offset:12704\n\t"
      "v_mfma_f32_16x16x32_bf16 v[20:23], v[100:103], a[8:11], v[20:23]\n\t"
      "v_add_u32 v52, s51, v15\n\t"
      "v_xor_b32 v53, 64, v52\n\t"
      "v_mfma_f32_16x16x32_bf16 v[20:23], v[116:119], a[12:15], v[20:23]\n\t"
      "v_xor_b32 v54, 128, v52\n\t"
      "v_xor_b32 v55, 192, v52\n\t"
      "v_mfma_f32_16x16x32_bf16 v[20:23], v[132:135], a[16:19], v[20:23]\n\t"
      "s_add_u32 s56, s56, 786432\n\t"
      "s_lshr_b32 s57, s57, 1\n\t"
      "v_mfma_f32_16x16x32_bf16 v[20:23], v[148:151], a[20:23], v[20:23]\n\t"
      "s_bitcmp1_b32 s57, 0\n\t"
      "s_cselect_b32 s48, 24576, 0\n\t"
      "s_add_u32 s48, s48, s56\n\t"
      "v_mfma_f32_16x16x32_bf16 v[24:27], v[72:75], a[0:3], 0\n\t"
      "v_mfma_f32_16x16x32_bf16 v[24:27], v[88:91], a[4:7], v[24:27]\n\t"
      "v_mfma_f32_16x16x32_bf16 v[24:27], v[104:107], a[8:11], v[24:27]\n\t"
      "v_mfma_f32_16x16x32_bf16 v[24:27], v[120:123], a[12:15], v[24:27]\n\t"
      "v_mfma_f32_16x16x32_bf16 v[24:27], v[136:139], a[16:19], v[24:27]\n\t"
      "v_mfma_f32_16x16x32_bf16 v[24:27], v[152:155], a[20:23], v[24:27]\n\t"
      "s_waitcnt vmcnt(12)\n\t"
      "s_waitcnt lgkmcnt(0)\n\t"
      "s_barrier\n\t"
      "v_mfma_f32_16x16x32_bf16 v[28:31], v[76:79], a[0:3], 0\n\t"
      "ds_read_b128 a[48:51], v8 offset:12544\n\t"
      "v_mfma_f32_16x16x32_bf16 v[28:31], v[92:95], a[4:7], v[28:31]\n\t"
      "ds_read_b128 a[52:55], v60 offset:13568\n\t"
      "v_mfma_f32_16x16x32_bf16 v[28:31], v[108:111], a[8:11], v[28:31]\n\t"
      "ds_read_b128 a[56:59], v61 offset:14592\n\t"
      "v_mfma_f32_16x16x32_bf16 v[28:31], v[124:127], a[12:15], v[28:31]\n\t"
      "v_mfma_f32_16x16x32_bf16 v[28:31], v[140:143], a[16:19], v[28:31]\n\t"
      "v_mfma_f32_16x16x32_bf16 v[28:31], v[156:159], a[20:23], v[28:31]\n\t"
      "v_mfma_f32_16x16x32_bf16 v[32:35], a[60:63], a[0:3], 0\n\t"
      "v_mfma_f32_16x16x32_bf16 v[32:35], a[68:71], a[4:7], v[32:35]\n\t"
      "s_waitcnt lgkmcnt(2)\n\t"
      "v_mfma_f32_16x16x32_bf16 v[32:35], a[76:79], a[8:11], v[32:35]\n\t"
      "buffer_store_dwordx4 a[48:51], v12, s[44:47], s49 offen sc1\n\t"
      "v_mfma_f32_16x16x32_bf16 v[32:35], a[84:87], a[12:15], v[32:35]\n\t"
      "v_mfma_f32_16x16x32_bf16 v[32:35], a[92:95], a[16:19], v[32:35]\n\t"
      "v_mfma_f32_16x16x32_bf16 v[32:35], a[100:103], a[20:23], v[32:35]\n\t"
      "s_add_u32 m0, s18, s50\n\t"
      "v_mfma_f32_16x16x32_bf16 v[36:39], a[64:67], a[0:3], 0\n\t"
      "buffer_load_dwordx4 v2, s[40:43], s48 offen nt lds\n\t"
      "v_mfma_f32_16x16x32_bf16 v[36:39], a[72:75], a[4:7], v[36:39]\n\t"
      "v_mfma_f32_16x16x32_bf16 v[36:39], a[80:83], a[8:11], v[36:39]\n\t"
      "v_mfma_f32_16x16x32_bf16 v[36:39], a[88:91], a[12:15], v[36:39]\n\t"
      "s_waitcnt lgkmcnt(1)\n\t"
      "v_mfma_f32_16x16x32_bf16 v[36:39], a[96:99], a[16:19], v[36:39]\n\t"
      "buffer_store_dwordx4 a[52:55], v13, s[44:47], s49 offen offset:1024 sc1\n\t"
      "v_mfma_f32_16x16x32_bf16 v[36:39], a[104:107], a[20:23], v[36:39]\n\t"
      "v_mfma_f32_16x16x32_bf16 v[16:19], v[160:163], a[24:27], v[16:19]\n\t"
      "ds_read_b128 a[0:3], v52 offset:0\n\t"
      "v_mfma_f32_16x16x32_bf16 v[16:19], v[176:179], a[28:31], v[16:19]\n\t"
      "ds_read_b128 a[4:7], v53 offset:0\n\t"
      "v_mfma_f32_16x16x32_bf16 v[16:19], v[192:195], a[32:35], v[16:19]\n\t"
      "ds_read_b128 a[8:11], v54 offset:0\n\t"
      "v_mfma_f32_16x16x32_bf16 v[16:19], v[208:211], a[36:39], v[16:19]\n\t"
      "ds_read_b128 a[12:15], v55 offset:0\n\t"
      "v_mfma_f32_16x16x32_bf16 v[16:19], v[224:227], a[40:43], v[16:19]\n\t"
      "ds_read_b128 a[16:19], v52 offset:256\n\t"
      "v_mfma_f32_16x16x32_bf16 v[16:19], v[240:243], a[44:47], v[16:19]\n\t"
      "ds_read_b128 a[20:23], v53 offset:256\n\t"
      "v_mfma_f32_16x16x32_bf16 v[20:23], v[164:167], a[24:27], v[20:23]\n\t"
      "s_add_u32 s51, s51, 12288\n\t"
      "s_cmp_eq_u32 s51, 160256\n\t"
      "s_cselect_b32 s51, 25088, s51\n\t"
      "v_mfma_f32_16x16x32_bf16 v[20:23], v[180:183], a[28:31], v[20:23]\n\t"
      "s_waitcnt lgkmcnt(6)\n\t"
      "v_mfma_f32_16x16x32_bf16 v[20:23], v[196:199], a[32:35], v[20:23]\n\t"
      "buffer_store_dwordx4 a[56:59], v14, s[44:47], s49 offen offset:2048 sc1\n\t"
      "v_mfma_f32_16x16x32_bf16 v[20:23], v[212:215], a[36:39], v[20:23]\n\t"
      "v_mfma_f32_16x16x32_bf16 v[20:23], v[228:231], a[40:43], v[20:23]\n\t"
      "s_waitcnt lgkmcnt(0)\n\t"
      "v_mfma_f32_16x16x32_bf16 v[20:23], v[244:247], a[44:47], v[20:23]\n\t"
      "v_mfma_f32_16x16x32_bf16 v[24:27], v[168:171], a[24:27], v[24:27]\n\t"
      "v_mfma_f32_16x16x32_bf16 v[24:27], v[184:187], a[28:31], v[24:27]\n\t"
      "v_cvt_pk_bf16_f32 v40, v16, v17\n\t"
      "v_cvt_pk_bf16_f32 v41, v18, v19\n\t"
      "v_mfma_f32_16x16x32_bf16 v[24:27], v[200:203], a[32:35], v[24:27]\n\t"
      "ds_write_b64 v0, v[40:41] offset:0\n\t"
      "v_mfma_f32_16x16x32_bf16 v[24:27], v[216:219], a[36:39], v[24:27]\n\t"
      "buffer_load_dwordx4 v3, s[40:43], s48 offen offset:1024 nt lds\n\t"
      "v_mfma_f32_16x16x32_bf16 v[24:27], v[232:235], a[40:43], v[24:27]\n\t"
      "s_add_u32 s58, s58, 786432\n\t"
      "s_lshr_b32 s59, s59, 1\n\t"
      "v_mfma_f32_16x16x32_bf16 v[24:27], v[248:251], a[44:47], v[24:27]\n\t"
      "s_bitcmp1_b32 s59, 0\n\t"
      "s_cselect_b32 s49, 24576, 0\n\t"
      "s_add_u32 s49, s49, s58\n\t"
      "v_mfma_f32_16x16x32_bf16 v[28:31], v[172:175], a[24:27], v[28:31]\n\t"
      "v_mfma_f32_16x16x32_bf16 v[28:31], v[188:191], a[28:31], v[28:31]\n\t"
      "v_cvt_pk_bf16_f32 v42, v20, v21\n\t"
      "v_cvt_pk_bf16_f32 v43, v22, v23\n\t"
      "v_mfma_f32_16x16x32_bf16 v[28:31], v[204:207], a[32:35], v[28:31]\n\t"
      "ds_write_b64 v0, v[42:43] offset:32\n\t"
      "v_mfma_f32_16x16x32_bf16 v[28:31], v[220:223], a[36:39], v[28:31]\n\t"
      "v_mfma_f32_16x16x32_bf16 v[28:31], v[236:239], a[40:43], v[28:31]\n\t"
      "v_mfma_f32_16x16x32_bf16 v[28:31], v[252:255], a[44:47], v[28:31]\n\t"
      "v_mfma_f32_16x16x32_bf16 v[32:35], a[108:111], a[24:27], v[32:35]\n\t"
      "v_mfma_f32_16x16x32_bf16 v[32:35], a[116:119], a[28:31], v[32:35]\n\t"
      "v_cvt_pk_bf16_f32 v44, v24, v25\n\t"
      "v_cvt_pk_bf16_f32 v45, v26, v27\n\t"
      "v_mfma_f32_16x16x32_bf16 v[32:35], a[124:127], a[32:35], v[32:35]\n\t"
      "ds_write_b64 v0, v[44:45] offset:64\n\t"
      "v_mfma_f32_16x16x32_bf16 v[32:35], a[132:135], a[36:39], v[32:35]\n\t"
      "buffer_load_dwordx4 v4, s[40:43], s48 offen offset:2048 nt lds\n\t"
      "v_mfma_f32_16x16x32_bf16 v[32:35], a[140:143], a[40:43], v[32:35]\n\t"
      "s_add_u32 s50, s50, 12288\n\t"
      "v_mfma_f32_16x16x32_bf16 v[32:35], a[148:151], a[44:47], v[32:35]\n\t"
      "v_cvt_pk_bf16_f32 v46, v28, v29\n\t"
      "v_cvt_pk_bf16_f32 v47, v30, v31\n\t"
      "v_mfma_f32_16x16x32_bf16 v[36:39], a[112:115], a[24:27], v[36:39]\n\t"
      "ds_write_b64 v0, v[46:47] offset:96\n\t"
      "v_mfma_f32_16x16x32_bf16 v[36:39], a[120:123], a[28:31], v[36:39]\n\t"
      "s_cmp_eq_u32 s50, 160256\n\t"
      "s_cselect_b32 s50, 25088, s50\n\t"
      "v_mfma_f32_16x16x32_bf16 v[36:39], a[128:131], a[32:35], v[36:39]\n\t"
      "v_mfma_f32_16x16x32_bf16 v[36:39], a[136:139], a[36:39], v[36:39]\n\t"
      "v_mfma_f32_16x16x32_bf16 v[36:39], a[144:147], a[40:43], v[36:39]\n\t"
      "v_cvt_pk_bf16_f32 v48, v32, v33\n\t"
      "v_cvt_pk_bf16_f32 v49, v34, v35\n\t"
      "v_mfma_f32_16x16x32_bf16 v[36:39], a[152:155], a[44:47], v[36:39]\n\t"
      "ds_write_b64 v0, v[48:49] offset:128\n\t"
      ".Lmid3_%=:\n\t"
      "v_mfma_f32_16x16x32_bf16 v[16:19], v[64:67], a[0:3], 0\n\t"
      "ds_read_b128 a[24:27], v54 offset:256\n\t"
      "v_mfma_f32_16x16x32_bf16 v[16:19], v[80:83], a[4:7], v[16:19]\n\t"
      "ds_read_b128 a[28:31], v55 offset:256\n\t"
      "v_mfma_f32_16x16x32_bf16 v[16:19], v[96:99], a[8:11], v[16:19]\n\t"
      "ds_read_b128 a[32:35], v52 offset:512\n\t"
      "v_mfma_f32_16x16x32_bf16 v[16:19], v[112:115], a[12:15], v[16:19]\n\t"
      "ds_read_b128 a[36:39], v53 offset:512\n\t"
      "v_mfma_f32_16x16x32_bf16 v[16:19], v[128:131], a[16:19], v[16:19]\n\t"
      "ds_read_b128 a[40:43], v54 offset:512\n\t"
      "v_mfma_f32_16x16x32_bf16 v[16:19], v[144:147], a[20:23], v[16:19]\n\t"
      "ds_read_b128 a[44:47], v55 offset:512\n\t"
      "v_mfma_f32_16x16x32_bf16 v[20:23], v[68:71], a[0:3], 0\n\t"
      "v_cvt_pk_bf16_f32 v50, v36, v37\n\t"
      "v_cvt_pk_bf16_f32 v51, v38, v39\n\t"
      "v_mfma_f32_16x16x32_bf16 v[20:23], v[84:87], a[4:7], v[20:23]\n\t"
      "ds_write_b64 v0, v[50:51] offset:160\n\t"
      "v_mfma_f32_16x16x32_bf16 v[20:23], v[100:103], a[8:11], v[20:23]\n\t"
      "v_add_u32 v56, s51, v15\n\t"
      "v_xor_b32 v57, 64, v56\n\t"
      "v_mfma_f32_16x16x32_bf16 v[20:23], v[116:119], a[12:15], v[20:23]\n\t"
      "v_xor_b32 v58, 128, v56\n\t"
      "v_xor_b32 v59, 192, v56\n\t"
      "v_mfma_f32_16x16x32_bf16 v[20:23], v[132:135], a[16:19], v[20:23]\n\t"
      "v_mfma_f32_16x16x32_bf16 v[20:23], v[148:151], a[20:23], v[20:23]\n\t"
      "v_mfma_f32_16x16x32_bf16 v[24:27], v[72:75], a[0:3], 0\n\t"
      "v_mfma_f32_16x16x32_bf16 v[24:27], v[88:91], a[4:7], v[24:27]\n\t"
      "v_mfma_f32_16x16x32_bf16 v[24:27], v[104:107], a[8:11], v[24:27]\n\t"
      "v_mfma_f32_16x16x32_bf16 v[24:27], v[120:123], a[12:15], v[24:27]\n\t"
      "v_mfma_f32_16x16x32_bf16 v[24:27], v[136:139], a[16:19], v[24:27]\n\t"
      "v_mfma_f32_16x16x32_bf16 v[24:27], v[152:155], a[20:23], v[24:27]\n\t"
      "s_waitcnt vmcnt(12)\n\t"
      "s_waitcnt lgkmcnt(0)\n\t"
      "s_barrier\n\t"
      "v_mfma_f32_16x16x32_bf16 v[28:31], v[76:79], a[0:3], 0\n\t"
      "ds_read_b128 a[48:51], v8 offset:0\n\t"
      "v_mfma_f32_16x16x32_bf16 v[28:31], v[92:95], a[4:7], v[28:31]\n\t"
      "ds_read_b128 a[52:55], v60 offset:1024\n\t"
      "v_mfma_f32_16x16x32_bf16 v[28:31], v[108:111], a[8:11], v[28:31]\n\t"
      "ds_read_b128 a[56:59], v61 offset:2048\n\t"
      "v_mfma_f32_16x16x32_bf16 v[28:31], v[124:127], a[12:15], v[28:31]\n\t"
      "v_mfma_f32_16x16x32_bf16 v[28:31], v[140:143], a[16:19], v[28:31]\n\t"
      "v_mfma_f32_16x16x32_bf16 v[28:31], v[156:159], a[20:23], v[28:31]\n\t"
      "v_mfma_f32_16x16x32_bf16 v[32:35], a[60:63], a[0:3], 0\n\t"
      "v_mfma_f32_16x16x32_bf16 v[32:35], a[68:71], a[4:7], v[32:35]\n\t"
      "s_waitcnt lgkmcnt(2)\n\t"
      "v_mfma_f32_16x16x32_bf16 v[32:35], a[76:79], a[8:11], v[32:35]\n\t"
      "buffer_store_dwordx4 a[48:51], v9, s[44:47], s49 offen sc1\n\t"
      "v_mfma_f32_16x16x32_bf16 v[32:35], a[84:87], a[12:15], v[32:35]\n\t"
      "v_mfma_f32_16x16x32_bf16 v[32:35], a[92:95], a[16:19], v[32:35]\n\t"
      "v_mfma_f32_16x16x32_bf16 v[32:35], a[100:103], a[20:23], v[32:35]\n\t"
      "s_add_u32 m0, s18, s50\n\t"
      "v_mfma_f32_16x16x32_bf16 v[36:39], a[64:67], a[0:3], 0\n\t"
      "buffer_load_dwordx4 v5, s[40:43], s48 offen nt lds\n\t"
      "v_mfma_f32_16x16x32_bf16 v[36:39], a[72:75], a[4:7], v[36:39]\n\t"
      "v_mfma_f32_16x16x32_bf16 v[36:39], a[80:83], a[8:11], v[36:39]\n\t"
      "v_mfma_f32_16x16x32_bf16 v[36:39], a[88:91], a[12:15], v[36:39]\n\t"
      "s_waitcnt lgkmcnt(1)\n\t"
      "v_mfma_f32_16x16x32_bf16 v[36:39], a[96:99], a[16:19], v[36:39]\n\t"
      "buffer_store_dwordx4 a[52:55], v10, s[44:47], s49 offen offset:1024 sc1\n\t"
      "v_mfma_f32_16x16x32_bf16 v[36:39], a[104:107], a[20:23], v[36:39]\n\t"
      "v_mfma_f32_16x16x32_bf16 v[16:19], v[160:163], a[24:27], v[16:19]\n\t"
      "ds_read_b128 a[0:3], v56 offset:0\n\t"
      "v_mfma_f32_16x16x32_bf16 v[16:19], v[176:179], a[28:31], v[16:19]\n\t"
      "ds_read_b128 a[4:7], v57 offset:0\n\t"
      "v_mfma_f32_16x16x32_bf16 v[16:19], v[192:195], a[32:35], v[16:19]\n\t"
      "ds_read_b128 a[8:11], v58 offset:0\n\t"
      "v_mfma_f32_16x16x32_bf16 v[16:19], v[208:211], a[36:39], v[16:19]\n\t"
      "ds_read_b128 a[12:15], v59 offset:0\n\t"
      "v_mfma_f32_16x16x32_bf16 v[16:19], v[224:227], a[40:43], v[16:19]\n\t"
      "ds_read_b128 a[16:19], v56 offset:256\n\t"
      "v_mfma_f32_16x16x32_bf16 v[16:19], v[240:243], a[44:47], v[16:19]\n\t"
      "ds_read_b128 a[20:23], v57 offset:256\n\t"
      "v_mfma_f32_16x16x32_bf16 v[20:23], v[164:167], a[24:27], v[20:23]\n\t"
      "s_add_u32 s51, s51, 12288\n\t"
      "s_cmp_eq_u32 s51, 160256\n\t"
      "s_cselect_b32 s51, 25088, s51\n\t"
      "v_mfma_f32_16x16x32_bf16 v[20:23], v[180:183], a[28:31], v[20:23]\n\t"
      "s_waitcnt lgkmcnt(6)\n\t"
      "v_mfma_f32_16x16x32_bf16 v[20:23], v[196:199], a[32:35], v[20:23]\n\t"
      "buffer_store_dwordx4 a[56:59], v11, s[44:47], s49 offen offset:2048 sc1\n\t"
      "v_mfma_f32_16x16x32_bf16 v[20:23], v[212:215], a[36:39], v[20:23]\n\t"
      "v_mfma_f32_16x16x32_bf16 v[20:23], v[228:231], a[40:43], v[20:23]\n\t"
      "s_waitcnt lgkmcnt(0)\n\t"
      "v_mfma_f32_16x16x32_bf16 v[20:23], v[244:247], a[44:47], v[20:23]\n\t"
      "v_mfma_f32_16x16x32_bf16 v[24:27], v[168:171], a[24:27], v[24:27]\n\t"
      "v_mfma_f32_16x16x32_bf16 v[24:27], v[184:187], a[28:31], v[24:27]\n\t"
      "v_cvt_pk_bf16_f32 v40, v16, v17\n\t"
      "v_cvt_pk_bf16_f32 v41, v18, v19\n\t"
      "v_mfma_f32_16x16x32_bf16 v[24:27], v[200:203], a[32:35], v[24:27]\n\t"
      "ds_write_b64 v0, v[40:41] offset:12544\n\t"
      "v_mfma_f32_16x16x32_bf16 v[24:27], v[216:219], a[36:39], v[24:27]\n\t"
      "buffer_load_dwordx4 v6, s[40:43], s48 offen offset:1024 nt lds\n\t"
      "v_mfma_f32_16x16x32_bf16 v[24:27], v[232:235], a[40:43], v[24:27]\n\t"
      "v_mfma_f32_16x16x32_bf16 v[24:27], v[248:251], a[44:47], v[24:27]\n\t"
      "v_mfma_f32_16x16x32_bf16 v[28:31], v[172:175], a[24:27], v[28:31]\n\t"
      "v_mfma_f32_16x16x32_bf16 v[28:31], v[188:191], a[28:31], v[28:31]\n\t"
      "v_cvt_pk_bf16_f32 v42, v20, v21\n\t"
      "v_cvt_pk_bf16_f32 v43, v22, v23\n\t"
      "v_mfma_f32_16x16x32_bf16 v[28:31], v[204:207], a[32:35], v[28:31]\n\t"
      "ds_write_b64 v0, v[42:43] offset:12576\n\t"
      "v_mfma_f32_16x16x32_bf16 v[28:31], v[220:223], a[36:39], v[28:31]\n\t"
      "v_mfma_f32_16x16x32_bf16 v[28:31], v[236:239], a[40:43], v[28:31]\n\t"
      "v_mfma_f32_16x16x32_bf16 v[28:31], v[252:255], a[44:47], v[28:31]\n\t"
      "v_mfma_f32_16x16x32_bf16 v[32:35], a[108:111], a[24:27], v[32:35]\n\t"
      "v_mfma_f32_16x16x32_bf16 v[32:35], a[116:119], a[28:31], v[32:35]\n\t"
      "v_cvt_pk_bf16_f32 v44, v24, v25\n\t"
      "v_cvt_pk_bf16_f32 v45, v26, v27\n\t"
      "v_mfma_f32_16x16x32_bf16 v[32:35], a[124:127], a[32:35], v[32:35]\n\t"
      "ds_write_b64 v0, v[44:45] offset:12608\n\t"
      "v_mfma_f32_16x16x32_bf16 v[32:35], a[132:135], a[36:39], v[32:35]\n\t"
      "buffer_load_dwordx4 v7, s[40:43], s48 offen offset:2048 nt lds\n\t"
      "v_mfma_f32_16x16x32_bf16 v[32:35], a[140:143], a[40:43], v[32:35]\n\t"
      "s_add_u32 s50, s50, 12288\n\t"
      "v_mfma_f32_16x16x32_bf16 v[32:35], a[148:151], a[44:47], v[32:35]\n\t"
      "v_cvt_pk_bf16_f32 v46, v28, v29\n\t"
      "v_cvt_pk_bf16_f32 v47, v30, v31\n\t"
      "v_mfma_f32_16x16x32_bf16 v[36:39], a[112:115], a[24:27], v[36:39]\n\t"
      "ds_write_b64 v0, v[46:47] offset:12640\n\t"
      "v_mfma_f32_16x16x32_bf16 v[36:39], a[120:123], a[28:31], v[36:39]\n\t"
      "s_cmp_eq_u32 s50, 160256\n\t"
      "s_cselect_b32 s50, 25088, s50\n\t"
      "v_mfma_f32_16x16x32_bf16 v[36:39], a[128:131], a[32:35], v[36:39]\n\t"
      "s_sub_u32 s52, s52, 1\n\t"
      "s_cmp_lg_u32 s52, 0\n\t"
      "v_mfma_f32_16x16x32_bf16 v[36:39], a[136:139], a[36:39], v[36:39]\n\t"
      "v_mfma_f32_16x16x32_bf16 v[36:39], a[144:147], a[40:43], v[36:39]\n\t"
      "v_cvt_pk_bf16_f32 v48, v32, v33\n\t"
      "v_cvt_pk_bf16_f32 v49, v34, v35\n\t"
      "v_mfma_f32_16x16x32_bf16 v[36:39], a[152:155], a[44:47], v[36:39]\n\t"
      "ds_write_b64 v0, v[48:49] offset:12672\n\t"
      "s_cbranch_scc1 .Lloop3_%=\n\t"
      ".Lendc_%=:\n\t"
      "s_cmp_eq_u32 s48, 113221632\n\t"
      "s_cbranch_scc1 .Lwrap3_%=\n\t"
      ".Lwrapped3_%=:\n\t"
      "v_mfma_f32_16x16x32_bf16 v[16:19], v[64:67], a[0:3], 0\n\t"
      "ds_read_b128 a[24:27], v58 offset:256\n\t"
      "v_mfma_f32_16x16x32_bf16 v[16:19], v[80:83], a[4:7], v[16:19]\n\t"
      "ds_read_b128 a[28:31], v59 offset:256\n\t"
      "v_mfma_f32_16x16x32_bf16 v[16:19], v[96:99], a[8:11], v[16:19]\n\t"
      "ds_read_b128 a[32:35], v56 offset:512\n\t"
      "v_mfma_f32_16x16x32_bf16 v[16:19], v[112:115], a[12:15], v[16:19]\n\t"
      "ds_read_b128 a[36:39], v57 offset:512\n\t"
      "v_mfma_f32_16x16x32_bf16 v[16:19], v[128:131], a[16:19], v[16:19]\n\t"
      "ds_read_b128 a[40:43], v58 offset:512\n\t"
      "v_mfma_f32_16x16x32_bf16 v[16:19], v[144:147], a[20:23], v[16:19]\n\t"
      "ds_read_b128 a[44:47], v59 offset:512\n\t"
      "v_mfma_f32_16x16x32_bf16 v[20:23], v[68:71], a[0:3], 0\n\t"
      "v_cvt_pk_bf16_f32 v50, v36, v37\n\t"
      "v_cvt_pk_bf16_f32 v51, v38, v39\n\t"
      "v_mfma_f32_16x16x32_bf16 v[20:23], v[84:87], a[4:7], v[20:23]\n\t"
      "ds_write_b64 v0, v[50:51] offset:12704\n\t"
      "v_mfma_f32_16x16x32_bf16 v[20:23], v[100:103], a[8:11], v[20:23]\n\t"
      "v_add_u32 v52, s51, v15\n\t"
      "v_xor_b32 v53, 64, v52\n\t"
      "v_mfma_f32_16x16x32_bf16 v[20:23], v[116:119], a[12:15], v[20:23]\n\t"
      "v_xor_b32 v54, 128, v52\n\t"
      "v_xor_b32 v55, 192, v52\n\t"
      "v_mfma_f32_16x16x32_bf16 v[20:23], v[132:135], a[16:19], v[20:23]\n\t"
      "s_add_u32 s58, s58, 786432\n\t"
      "s_lshr_b32 s59, s59, 1\n\t"
      "v_mfma_f32_16x16x32_bf16 v[20:23], v[148:151], a[20:23], v[20:23]\n\t"
      "s_bitcmp1_b32 s59, 0\n\t"
      "s_cselect_b32 s84, 24576, 0\n\t"
      "s_add_u32 s84, s84, s58\n\t"
      "v_mfma_f32_16x16x32_bf16 v[24:27], v[72:75], a[0:3], 0\n\t"
      "s_add_u32 s58, s58, 786432\n\t"
      "s_lshr_b32 s59, s59, 1\n\t"
      "v_mfma_f32_16x16x32_bf16 v[24:27], v[88:91], a[4:7], v[24:27]\n\t"
      "s_bitcmp1_b32 s59, 0\n\t"
      "s_cselect_b32 s85, 24576, 0\n\t"
      "s_add_u32 s85, s85, s58\n\t"
      "v_mfma_f32_16x16x32_bf16 v[24:27], v[104:107], a[8:11], v[24:27]\n\t"
      "s_waitcnt vmcnt(12)\n\t"
      "s_waitcnt lgkmcnt(0)\n\t"
      "s_barrier\n\t"
      "v_mfma_f32_16x16x32_bf16 v[24:27], v[120:123], a[12:15], v[24:27]\n\t"
      "v_mfma_f32_16x16x32_bf16 v[24:27], v[136:139], a[16:19], v[24:27]\n\t"
      "v_mfma_f32_16x16x32_bf16 v[24:27], v[152:155], a[20:23], v[24:27]\n\t"
      "v_mfma_f32_16x16x32_bf16 v[28:31], v[76:79], a[0:3], 0\n\t"
      "ds_read_b128 a[188:191], v8 offset:12544\n\t"
      "v_mfma_f32_16x16x32_bf16 v[28:31], v[92:95], a[4:7], v[28:31]\n\t"
      "ds_read_b128 a[192:195], v60 offset:13568\n\t"
      "v_mfma_f32_16x16x32_bf16 v[28:31], v[108:111], a[8:11], v[28:31]\n\t"
      "ds_read_b128 a[196:199], v61 offset:14592\n\t"
      "v_mfma_f32_16x16x32_bf16 v[28:31], v[124:127], a[12:15], v[28:31]\n\t"
      "v_mfma_f32_16x16x32_bf16 v[28:31], v[140:143], a[16:19], v[28:31]\n\t"
      "v_mfma_f32_16x16x32_bf16 v[28:31], v[156:159], a[20:23], v[28:31]\n\t"
      "v_mfma_f32_16x16x32_bf16 v[32:35], a[60:63], a[0:3], 0\n\t"
      "v_mfma_f32_16x16x32_bf16 v[32:35], a[68:71], a[4:7], v[32:35]\n\t"
      "v_mfma_f32_16x16x32_bf16 v[32:35], a[76:79], a[8:11], v[32:35]\n\t"
      "v_mfma_f32_16x16x32_bf16 v[32:35], a[84:87], a[12:15], v[32:35]\n\t"
      "v_mfma_f32_16x16x32_bf16 v[32:35], a[92:95], a[16:19], v[32:35]\n\t"
      "v_mfma_f32_16x16x32_bf16 v[32:35], a[100:103], a[20:23], v[32:35]\n\t"
      "v_mfma_f32_16x16x32_bf16 v[36:39], a[64:67], a[0:3], 0\n\t"
      "v_mfma_f32_16x16x32_bf16 v[36:39], a[72:75], a[4:7], v[36:39]\n\t"
      "v_mfma_f32_16x16x32_bf16 v[36:39], a[80:83], a[8:11], v[36:39]\n\t"
      "v_mfma_f32_16x16x32_bf16 v[36:39], a[88:91], a[12:15], v[36:39]\n\t"
      "v_mfma_f32_16x16x32_bf16 v[36:39], a[96:99], a[16:19], v[36:39]\n\t"
      "v_mfma_f32_16x16x32_bf16 v[36:39], a[104:107], a[20:23], v[36:39]\n\t"
      "v_mfma_f32_16x16x32_bf16 v[16:19], v[160:163], a[24:27], v[16:19]\n\t"
      "ds_read_b128 a[0:3], v52 offset:0\n\t"
      "v_mfma_f32_16x16x32_bf16 v[16:19], v[176:179], a[28:31], v[16:19]\n\t"
      "ds_read_b128 a[4:7], v53 offset:0\n\t"
      "v_mfma_f32_16x16x32_bf16 v[16:19], v[192:195], a[32:35], v[16:19]\n\t"
      "ds_read_b128 a[8:11], v54 offset:0\n\t"
      "v_mfma_f32_16x16x32_bf16 v[16:19], v[208:211], a[36:39], v[16:19]\n\t"
      "ds_read_b128 a[12:15], v55 offset:0\n\t"
      "v_mfma_f32_16x16x32_bf16 v[16:19], v[224:227], a[40:43], v[16:19]\n\t"
      "ds_read_b128 a[16:19], v52 offset:256\n\t"
      "v_mfma_f32_16x16x32_bf16 v[16:19], v[240:243], a[44:47], v[16:19]\n\t"
      "ds_read_b128 a[20:23], v53 offset:256\n\t"
      "v_mfma_f32_16x16x32_bf16 v[20:23], v[164:167], a[24:27], v[20:23]\n\t"
      "s_add_u32 s51, s51, 12288\n\t"
      "s_cmp_eq_u32 s51, 160256\n\t"
      "s_cselect_b32 s51, 25088, s51\n\t"
      "v_mfma_f32_16x16x32_bf16 v[20:23], v[180:183], a[28:31], v[20:23]\n\t"
      "v_mfma_f32_16x16x32_bf16 v[20:23], v[196:199], a[32:35], v[20:23]\n\t"
      "v_mfma_f32_16x16x32_bf16 v[20:23], v[212:215], a[36:39], v[20:23]\n\t"
      "v_mfma_f32_16x16x32_bf16 v[20:23], v[228:231], a[40:43], v[20:23]\n\t"
      "s_waitcnt lgkmcnt(0)\n\t"
      "v_mfma_f32_16x16x32_bf16 v[20:23], v[244:247], a[44:47], v[20:23]\n\t"
      "v_mfma_f32_16x16x32_bf16 v[24:27], v[168:171], a[24:27], v[24:27]\n\t"
      "v_mfma_f32_16x16x32_bf16 v[24:27], v[184:187], a[28:31], v[24:27]\n\t"
      "v_cvt_pk_bf16_f32 v40, v16, v17\n\t"
      "v_cvt_pk_bf16_f32 v41, v18, v19\n\t"
      "v_mfma_f32_16x16x32_bf16 v[24:27], v[200:203], a[32:35], v[24:27]\n\t"
      "ds_write_b64 v0, v[40:41] offset:0\n\t"
      "v_mfma_f32_16x16x32_bf16 v[24:27], v[216:219], a[36:39], v[24:27]\n\t"
      "v_mfma_f32_16x16x32_bf16 v[24:27], v[232:235], a[40:43], v[24:27]\n\t"
      "v_mfma_f32_16x16x32_bf16 v[24:27], v[248:251], a[44:47], v[24:27]\n\t"
      "v_mfma_f32_16x16x32_bf16 v[28:31], v[172:175], a[24:27], v[28:31]\n\t"
      "v_mfma_f32_16x16x32_bf16 v[28:31], v[188:191], a[28:31], v[28:31]\n\t"
      "v_cvt_pk_bf16_f32 v42, v20, v21\n\t"
      "v_cvt_pk_bf16_f32 v43, v22, v23\n\t"
      "v_mfma_f32_16x16x32_bf16 v[28:31], v[204:207], a[32:35], v[28:31]\n\t"
      "ds_write_b64 v0, v[42:43] offset:32\n\t"
      "v_mfma_f32_16x16x32_bf16 v[28:31], v[220:223], a[36:39], v[28:31]\n\t"
      "v_mfma_f32_16x16x32_bf16 v[28:31], v[236:239], a[40:43], v[28:31]\n\t"
      "v_mfma_f32_16x16x32_bf16 v[28:31], v[252:255], a[44:47], v[28:31]\n\t"
      "v_mfma_f32_16x16x32_bf16 v[32:35], a[108:111], a[24:27], v[32:35]\n\t"
      "v_mfma_f32_16x16x32_bf16 v[32:35], a[116:119], a[28:31], v[32:35]\n\t"
      "v_cvt_pk_bf16_f32 v44, v24, v25\n\t"
      "v_cvt_pk_bf16_f32 v45, v26, v27\n\t"
      "v_mfma_f32_16x16x32_bf16 v[32:35], a[124:127], a[32:35], v[32:35]\n\t"
      "ds_write_b64 v0, v[44:45] offset:64\n\t"
      "v_mfma_f32_16x16x32_bf16 v[32:35], a[132:135], a[36:39], v[32:35]\n\t"
      "v_mfma_f32_16x16x32_bf16 v[32:35], a[140:143], a[40:43], v[32:35]\n\t"
      "v_mfma_f32_16x16x32_bf16 v[32:35], a[148:151], a[44:47], v[32:35]\n\t"
      "v_cvt_pk_bf16_f32 v46, v28, v29\n\t"
      "v_cvt_pk_bf16_f32 v47, v30, v31\n\t"
      "v_mfma_f32_16x16x32_bf16 v[36:39], a[112:115], a[24:27], v[36:39]\n\t"
      "ds_write_b64 v0, v[46:47] offset:96\n\t"
      "v_mfma_f32_16x16x32_bf16 v[36:39], a[120:123], a[28:31], v[36:39]\n\t"
      "v_mfma_f32_16x16x32_bf16 v[36:39], a[128:131], a[32:35], v[36:39]\n\t"
      "v_mfma_f32_16x16x32_bf16 v[36:39], a[136:139], a[36:39], v[36:39]\n\t"
      "v_mfma_f32_16x16x32_bf16 v[36:39], a[144:147], a[40:43], v[36:39]\n\t"
      "v_cvt_pk_bf16_f32 v48, v32, v33\n\t"
      "v_cvt_pk_bf16_f32 v49, v34, v35\n\t"
      "v_mfma_f32_16x16x32_bf16 v[36:39], a[152:155], a[44:47], v[36:39]\n\t"
      "ds_write_b64 v0, v[48:49] offset:128\n\t"
      "v_mfma_f32_16x16x32_bf16 v[16:19], v[64:67], a[0:3], 0\n\t"
      "ds_read_b128 a[24:27], v54 offset:256\n\t"
      "v_mfma_f32_16x16x32_bf16 v[16:19], v[80:83], a[4:7], v[16:19]\n\t"
      "ds_read_b128 a[28:31], v55 offset:256\n\t"
      "v_mfma_f32_16x16x32_bf16 v[16:19], v[96:99], a[8:11], v[16:19]\n\t"
      "ds_read_b128 a[32:35], v52 offset:512\n\t"
      "v_mfma_f32_16x16x32_bf16 v[16:19], v[112:115], a[12:15], v[16:19]\n\t"
      "ds_read_b128 a[36:39], v53 offset:512\n\t"
      "v_mfma_f32_16x16x32_bf16 v[16:19], v[128:131], a[16:19], v[16:19]\n\t"
      "ds_read_b128 a[40:43], v54 offset:512\n\t"
      "v_mfma_f32_16x16x32_bf16 v[16:19], v[144:147], a[20:23], v[16:19]\n\t"
      "ds_read_b128 a[44:47], v55 offset:512\n\t"
      "v_mfma_f32_16x16x32_bf16 v[20:23], v[68:71], a[0:3], 0\n\t"
      "v_cvt_pk_bf16_f32 v50, v36, v37\n\t"
      "v_cvt_pk_bf16_f32 v51, v38, v39\n\t"
      "v_mfma_f32_16x16x32_bf16 v[20:23], v[84:87], a[4:7], v[20:23]\n\t"
      "ds_write_b64 v0, v[50:51] offset:160\n\t"
      "v_mfma_f32_16x16x32_bf16 v[20:23], v[100:103], a[8:11], v[20:23]\n\t"
      "v_add_u32 v56, s51, v15\n\t"
      "v_xor_b32 v57, 64, v56\n\t"
      "v_mfma_f32_16x16x32_bf16 v[20:23], v[116:119], a[12:15], v[20:23]\n\t"
      "v_xor_b32 v58, 128, v56\n\t"
      "v_xor_b32 v59, 192, v56\n\t"
      "v_mfma_f32_16x16x32_bf16 v[20:23], v[132:135], a[16:19], v[20:23]\n\t"
      "v_mfma_f32_16x16x32_bf16 v[20:23], v[148:151], a[20:23], v[20:23]\n\t"
      "v_mfma_f32_16x16x32_bf16 v[24:27], v[72:75], a[0:3], 0\n\t"
      "v_mfma_f32_16x16x32_bf16 v[24:27], v[88:91], a[4:7], v[24:27]\n\t"
      "v_mfma_f32_16x16x32_bf16 v[24:27], v[104:107], a[8:11], v[24:27]\n\t"
      "s_waitcnt vmcnt(6)\n\t"
      "s_waitcnt lgkmcnt(0)\n\t"
      "s_barrier\n\t"
      "v_mfma_f32_16x16x32_bf16 v[24:27], v[120:123], a[12:15], v[24:27]\n\t"
      "v_mfma_f32_16x16x32_bf16 v[24:27], v[136:139], a[16:19], v[24:27]\n\t"
      "v_mfma_f32_16x16x32_bf16 v[24:27], v[152:155], a[20:23], v[24:27]\n\t"
      "v_mfma_f32_16x16x32_bf16 v[28:31], v[76:79], a[0:3], 0\n\t"
      "ds_read_b128 a[200:203], v8 offset:0\n\t"
      "v_mfma_f32_16x16x32_bf16 v[28:31], v[92:95], a[4:7], v[28:31]\n\t"
      "ds_read_b128 a[204:207], v60 offset:1024\n\t"
      "v_mfma_f32_16x16x32_bf16 v[28:31], v[108:111], a[8:11], v[28:31]\n\t"
      "ds_read_b128 a[208:211], v61 offset:2048\n\t"
      "v_mfma_f32_16x16x32_bf16 v[28:31], v[124:127], a[12:15], v[28:31]\n\t"
      "v_mfma_f32_16x16x32_bf16 v[28:31], v[140:143], a[16:19], v[28:31]\n\t"
      "v_mfma_f32_16x16x32_bf16 v[28:31], v[156:159], a[20:23], v[28:31]\n\t"
      "v_mfma_f32_16x16x32_bf16 v[32:35], a[60:63], a[0:3], 0\n\t"
      "v_mfma_f32_16x16x32_bf16 v[32:35], a[68:71], a[4:7], v[32:35]\n\t"
      "v_mfma_f32_16x16x32_bf16 v[32:35], a[76:79], a[8:11], v[32:35]\n\t"
      "v_mfma_f32_16x16x32_bf16 v[32:35], a[84:87], a[12:15], v[32:35]\n\t"
      "v_mfma_f32_16x16x32_bf16 v[32:35], a[92:95], a[16:19], v[32:35]\n\t"
      "v_mfma_f32_16x16x32_bf16 v[32:35], a[100:103], a[20:23], v[32:35]\n\t"
      "v_mfma_f32_16x16x32_bf16 v[36:39], a[64:67], a[0:3], 0\n\t"
      "v_mfma_f32_16x16x32_bf16 v[36:39], a[72:75], a[4:7], v[36:39]\n\t"
      "v_mfma_f32_16x16x32_bf16 v[36:39], a[80:83], a[8:11], v[36:39]\n\t"
      "v_mfma_f32_16x16x32_bf16 v[36:39], a[88:91], a[12:15], v[36:39]\n\t"
      "v_mfma_f32_16x16x32_bf16 v[36:39], a[96:99], a[16:19], v[36:39]\n\t"
      "v_mfma_f32_16x16x32_bf16 v[36:39], a[104:107], a[20:23], v[36:39]\n\t"
      "v_mfma_f32_16x16x32_bf16 v[16:19], v[160:163], a[24:27], v[16:19]\n\t"
      "ds_read_b128 a[0:3], v56 offset:0\n\t"
      "v_mfma_f32_16x16x32_bf16 v[16:19], v[176:179], a[28:31], v[16:19]\n\t"
      "ds_read_b128 a[4:7], v57 offset:0\n\t"
      "v_mfma_f32_16x16x32_bf16 v[16:19], v[192:195], a[32:35], v[16:19]\n\t"
      "ds_read_b128 a[8:11], v58 offset:0\n\t"
      "v_mfma_f32_16x16x32_bf16 v[16:19], v[208:211], a[36:39], v[16:19]\n\t"
      "ds_read_b128 a[12:15], v59 offset:0\n\t"
      "v_mfma_f32_16x16x32_bf16 v[16:19], v[224:227], a[40:43], v[16:19]\n\t"
      "ds_read_b128 a[16:19], v56 offset:256\n\t"
      "v_mfma_f32_16x16x32_bf16 v[16:19], v[240:243], a[44:47], v[16:19]\n\t"
      "ds_read_b128 a[20:23], v57 offset:256\n\t"
      "v_mfma_f32_16x16x32_bf16 v[20:23], v[164:167], a[24:27], v[20:23]\n\t"
      "s_add_u32 s51, s51, 12288\n\t"
      "s_cmp_eq_u32 s51, 160256\n\t"
      "s_cselect_b32 s51, 25088, s51\n\t"
      "v_mfma_f32_16x16x32_bf16 v[20:23], v[180:183], a[28:31], v[20:23]\n\t"
      "v_mfma_f32_16x16x32_bf16 v[20:23], v[196:199], a[32:35], v[20:23]\n\t"
      "buffer_store_dwordx4 a[188:191], v12, s[44:47], s49 offen sc1\n\t"
      "v_mfma_f32_16x16x32_bf16 v[20:23], v[212:215], a[36:39], v[20:23]\n\t"
      "v_mfma_f32_16x16x32_bf16 v[20:23], v[228:231], a[40:43], v[20:23]\n\t"
      "s_waitcnt lgkmcnt(0)\n\t"
      "v_mfma_f32_16x16x32_bf16 v[20:23], v[244:247], a[44:47], v[20:23]\n\t"
      "v_mfma_f32_16x16x32_bf16 v[24:27], v[168:171], a[24:27], v[24:27]\n\t"
      "v_mfma_f32_16x16x32_bf16 v[24:27], v[184:187], a[28:31], v[24:27]\n\t"
      "v_cvt_pk_bf16_f32 v40, v16, v17\n\t"
      "v_cvt_pk_bf16_f32 v41, v18, v19\n\t"
      "v_mfma_f32_16x16x32_bf16 v[24:27], v[200:203], a[32:35], v[24:27]\n\t"
      "ds_write_b64 v0, v[40:41] offset:12544\n\t"
      "v_mfma_f32_16x16x32_bf16 v[24:27], v[216:219], a[36:39], v[24:27]\n\t"
      "v_mfma_f32_16x16x32_bf16 v[24:27], v[232:235], a[40:43], v[24:27]\n\t"
      "v_mfma_f32_16x16x32_bf16 v[24:27], v[248:251], a[44:47], v[24:27]\n\t"
      "v_mfma_f32_16x16x32_bf16 v[28:31], v[172:175], a[24:27], v[28:31]\n\t"
      "v_mfma_f32_16x16x32_bf16 v[28:31], v[188:191], a[28:31], v[28:31]\n\t"
      "v_cvt_pk_bf16_f32 v42, v20, v21\n\t"
      "v_cvt_pk_bf16_f32 v43, v22, v23\n\t"
      "v_mfma_f32_16x16x32_bf16 v[28:31], v[204:207], a[32:35], v[28:31]\n\t"
      "ds_write_b64 v0, v[42:43] offset:12576\n\t"
      "v_mfma_f32_16x16x32_bf16 v[28:31], v[220:223], a[36:39], v[28:31]\n\t"
      "v_mfma_f32_16x16x32_bf16 v[28:31], v[236:239], a[40:43], v[28:31]\n\t"
      "buffer_store_dwordx4 a[192:195], v13, s[44:47], s49 offen offset:1024 sc1\n\t"
      "v_mfma_f32_16x16x32_bf16 v[28:31], v[252:255], a[44:47], v[28:31]\n\t"
      "v_mfma_f32_16x16x32_bf16 v[32:35], a[108:111], a[24:27], v[32:35]\n\t"
      "v_mfma_f32_16x16x32_bf16 v[32:35], a[116:119], a[28:31], v[32:35]\n\t"
      "v_cvt_pk_bf16_f32 v44, v24, v25\n\t"
      "v_cvt_pk_bf16_f32 v45, v26, v27\n\t"
      "v_mfma_f32_16x16x32_bf16 v[32:35], a[124:127], a[32:35], v[32:35]\n\t"
      "ds_write_b64 v0, v[44:45] offset:12608\n\t"
      "v_mfma_f32_16x16x32_bf16 v[32:35], a[132:135], a[36:39], v[32:35]\n\t"
      "v_mfma_f32_16x16x32_bf16 v[32:35], a[140:143], a[40:43], v[32:35]\n\t"
      "v_mfma_f32_16x16x32_bf16 v[32:35], a[148:151], a[44:47], v[32:35]\n\t"
      "v_cvt_pk_bf16_f32 v46, v28, v29\n\t"
      "v_cvt_pk_bf16_f32 v47, v30, v31\n\t"
      "v_mfma_f32_16x16x32_bf16 v[36:39], a[112:115], a[24:27], v[36:39]\n\t"
      "ds_write_b64 v0, v[46:47] offset:12640\n\t"
      "v_mfma_f32_16x16x32_bf16 v[36:39], a[120:123], a[28:31], v[36:39]\n\t"
      "v_mfma_f32_16x16x32_bf16 v[36:39], a[128:131], a[32:35], v[36:39]\n\t"
      "v_mfma_f32_16x16x32_bf16 v[36:39], a[136:139], a[36:39], v[36:39]\n\t"
      "v_mfma_f32_16x16x32_bf16 v[36:39], a[144:147], a[40:43], v[36:39]\n\t"
      "v_cvt_pk_bf16_f32 v48, v32, v33\n\t"
      "v_cvt_pk_bf16_f32 v49, v34, v35\n\t"
      "v_mfma_f32_16x16x32_bf16 v[36:39], a[152:155], a[44:47], v[36:39]\n\t"
      "ds_write_b64 v0, v[48:49] offset:12672\n\t"
      "v_mfma_f32_16x16x32_bf16 v[16:19], v[64:67], a[0:3], 0\n\t"
      "ds_read_b128 a[24:27], v58 offset:256\n\t"
      "v_mfma_f32_16x16x32_bf16 v[16:19], v[80:83], a[4:7], v[16:19]\n\t"
      "ds_read_b128 a[28:31], v59 offset:256\n\t"
      "v_mfma_f32_16x16x32_bf16 v[16:19], v[96:99], a[8:11], v[16:19]\n\t"
      "ds_read_b128 a[32:35], v56 offset:512\n\t"
      "v_mfma_f32_16x16x32_bf16 v[16:19], v[112:115], a[12:15], v[16:19]\n\t"
      "ds_read_b128 a[36:39], v57 offset:512\n\t"
      "buffer_store_dwordx4 a[196:199], v14, s[44:47], s49 offen offset:2048 sc1\n\t"
      "v_mfma_f32_16x16x32_bf16 v[16:19], v[128:131], a[16:19], v[16:19]\n\t"
      "ds_read_b128 a[40:43], v58 offset:512\n\t"
      "v_mfma_f32_16x16x32_bf16 v[16:19], v[144:147], a[20:23], v[16:19]\n\t"
      "ds_read_b128 a[44:47], v59 offset:512\n\t"
      "v_mfma_f32_16x16x32_bf16 v[20:23], v[68:71], a[0:3], 0\n\t"
      "v_cvt_pk_bf16_f32 v50, v36, v37\n\t"
      "v_cvt_pk_bf16_f32 v51, v38, v39\n\t"
      "v_mfma_f32_16x16x32_bf16 v[20:23], v[84:87], a[4:7], v[20:23]\n\t"
      "ds_write_b64 v0, v[50:51] offset:12704\n\t"
      "v_mfma_f32_16x16x32_bf16 v[20:23], v[100:103], a[8:11], v[20:23]\n\t"
      "v_add_u32 v52, s51, v15\n\t"
      "v_xor_b32 v53, 64, v52\n\t"
      "v_mfma_f32_16x16x32_bf16 v[20:23], v[116:119], a[12:15], v[20:23]\n\t"
      "v_xor_b32 v54, 128, v52\n\t"
      "v_xor_b32 v55, 192, v52\n\t"
      "v_mfma_f32_16x16x32_bf16 v[20:23], v[132:135], a[16:19], v[20:23]\n\t"
      "v_mfma_f32_16x16x32_bf16 v[20:23], v[148:151], a[20:23], v[20:23]\n\t"
      "v_mfma_f32_16x16x32_bf16 v[24:27], v[72:75], a[0:3], 0\n\t"
      "buffer_store_dwordx4 a[200:203], v9, s[44:47], s84 offen sc1\n\t"
      "v_mfma_f32_16x16x32_bf16 v[24:27], v[88:91], a[4:7], v[24:27]\n\t"
      "v_mfma_f32_16x16x32_bf16 v[24:27], v[104:107], a[8:11], v[24:27]\n\t"
      "s_waitcnt vmcnt(4)\n\t"
      "s_waitcnt lgkmcnt(0)\n\t"
      "s_barrier\n\t"
      "v_mfma_f32_16x16x32_bf16 v[24:27], v[120:123], a[12:15], v[24:27]\n\t"
      "v_mfma_f32_16x16x32_bf16 v[24:27], v[136:139], a[16:19], v[24:27]\n\t"
      "v_mfma_f32_16x16x32_bf16 v[24:27], v[152:155], a[20:23], v[24:27]\n\t"
      "v_mfma_f32_16x16x32_bf16 v[28:31], v[76:79], a[0:3], 0\n\t"
      "ds_read_b128 a[212:215], v8 offset:12544\n\t"
      "v_mfma_f32_16x16x32_bf16 v[28:31], v[92:95], a[4:7], v[28:31]\n\t"
      "ds_read_b128 a[216:219], v60 offset:13568\n\t"
      "v_mfma_f32_16x16x32_bf16 v[28:31], v[108:111], a[8:11], v[28:31]\n\t"
      "ds_read_b128 a[220:223], v61 offset:14592\n\t"
      "v_mfma_f32_16x16x32_bf16 v[28:31], v[124:127], a[12:15], v[28:31]\n\t"
      "v_mfma_f32_16x16x32_bf16 v[28:31], v[140:143], a[16:19], v[28:31]\n\t"
      "v_mfma_f32_16x16x32_bf16 v[28:31], v[156:159], a[20:23], v[28:31]\n\t"
      "v_mfma_f32_16x16x32_bf16 v[32:35], a[60:63], a[0:3], 0\n\t"
      "buffer_store_dwordx4 a[204:207], v10, s[44:47], s84 offen offset:1024 sc1\n\t"
      "v_mfma_f32_16x16x32_bf16 v[32:35], a[68:71], a[4:7], v[32:35]\n\t"
      "v_mfma_f32_16x16x32_bf16 v[32:35], a[76:79], a[8:11], v[32:35]\n\t"
      "v_mfma_f32_16x16x32_bf16 v[32:35], a[84:87], a[12:15], v[32:35]\n\t"
      "v_mfma_f32_16x16x32_bf16 v[32:35], a[92:95], a[16:19], v[32:35]\n\t"
      "v_mfma_f32_16x16x32_bf16 v[32:35], a[100:103], a[20:23], v[32:35]\n\t"
      "v_mfma_f32_16x16x32_bf16 v[36:39], a[64:67], a[0:3], 0\n\t"
      "v_mfma_f32_16x16x32_bf16 v[36:39], a[72:75], a[4:7], v[36:39]\n\t"
      "v_mfma_f32_16x16x32_bf16 v[36:39], a[80:83], a[8:11], v[36:39]\n\t"
      "v_mfma_f32_16x16x32_bf16 v[36:39], a[88:91], a[12:15], v[36:39]\n\t"
      "v_mfma_f32_16x16x32_bf16 v[36:39], a[96:99], a[16:19], v[36:39]\n\t"
      "buffer_store_dwordx4 a[208:211], v11, s[44:47], s84 offen offset:2048 sc1\n\t"
      "v_mfma_f32_16x16x32_bf16 v[36:39], a[104:107], a[20:23], v[36:39]\n\t"
      "v_mfma_f32_16x16x32_bf16 v[16:19], v[160:163], a[24:27], v[16:19]\n\t"
      "ds_read_b128 a[0:3], v52 offset:0\n\t"
      "v_mfma_f32_16x16x32_bf16 v[16:19], v[176:179], a[28:31], v[16:19]\n\t"
      "ds_read_b128 a[4:7], v53 offset:0\n\t"
      "v_mfma_f32_16x16x32_bf16 v[16:19], v[192:195], a[32:35], v[16:19]\n\t"
      "ds_read_b128 a[8:11], v54 offset:0\n\t"
      "v_mfma_f32_16x16x32_bf16 v[16:19], v[208:211], a[36:39], v[16:19]\n\t"
      "ds_read_b128 a[12:15], v55 offset:0\n\t"
      "v_mfma_f32_16x16x32_bf16 v[16:19], v[224:227], a[40:43], v[16:19]\n\t"
      "ds_read_b128 a[16:19], v52 offset:256\n\t"
      "v_mfma_f32_16x16x32_bf16 v[16:19], v[240:243], a[44:47], v[16:19]\n\t"
      "ds_read_b128 a[20:23], v53 offset:256\n\t"
      "v_mfma_f32_16x16x32_bf16 v[20:23], v[164:167], a[24:27], v[20:23]\n\t"
      "s_add_u32 s51, s51, 12288\n\t"
      "s_cmp_eq_u32 s51, 160256\n\t"
      "s_cselect_b32 s51, 25088, s51\n\t"
      "v_mfma_f32_16x16x32_bf16 v[20:23], v[180:183], a[28:31], v[20:23]\n\t"
      "s_waitcnt lgkmcnt(8)\n\t"
      "v_mfma_f32_16x16x32_bf16 v[20:23], v[196:199], a[32:35], v[20:23]\n\t"
      "buffer_store_dwordx4 a[212:215], v12, s[44:47], s84 offen sc1\n\t"
      "v_mfma_f32_16x16x32_bf16 v[20:23], v[212:215], a[36:39], v[20:23]\n\t"
      "v_mfma_f32_16x16x32_bf16 v[20:23], v[228:231], a[40:43], v[20:23]\n\t"
      "s_waitcnt lgkmcnt(0)\n\t"
      "v_mfma_f32_16x16x32_bf16 v[20:23], v[244:247], a[44:47], v[20:23]\n\t"
      "v_mfma_f32_16x16x32_bf16 v[24:27], v[168:171], a[24:27], v[24:27]\n\t"
      "v_mfma_f32_16x16x32_bf16 v[24:27], v[184:187], a[28:31], v[24:27]\n\t"
      "v_cvt_pk_bf16_f32 v40, v16, v17\n\t"
      "v_cvt_pk_bf16_f32 v41, v18, v19\n\t"
      "v_mfma_f32_16x16x32_bf16 v[24:27], v[200:203], a[32:35], v[24:27]\n\t"
      "ds_write_b64 v0, v[40:41] offset:0\n\t"
      "v_mfma_f32_16x16x32_bf16 v[24:27], v[216:219], a[36:39], v[24:27]\n\t"
      "v_mfma_f32_16x16x32_bf16 v[24:27], v[232:235], a[40:43], v[24:27]\n\t"
      "v_mfma_f32_16x16x32_bf16 v[24:27], v[248:251], a[44:47], v[24:27]\n\t"
      "v_mfma_f32_16x16x32_bf16 v[28:31], v[172:175], a[24:27], v[28:31]\n\t"
      "v_mfma_f32_16x16x32_bf16 v[28:31], v[188:191], a[28:31], v[28:31]\n\t"
      "v_cvt_pk_bf16_f32 v42, v20, v21\n\t"
      "v_cvt_pk_bf16_f32 v43, v22, v23\n\t"
      "v_mfma_f32_16x16x32_bf16 v[28:31], v[204:207], a[32:35], v[28:31]\n\t"
      "ds_write_b64 v0, v[42:43] offset:32\n\t"
      "v_mfma_f32_16x16x32_bf16 v[28:31], v[220:223], a[36:39], v[28:31]\n\t"
      "v_mfma_f32_16x16x32_bf16 v[28:31], v[236:239], a[40:43], v[28:31]\n\t"
      "buffer_store_dwordx4 a[216:219], v13, s[44:47], s84 offen offset:1024 sc1\n\t"
      "v_mfma_f32_16x16x32_bf16 v[28:31], v[252:255], a[44:47], v[28:31]\n\t"
      "v_mfma_f32_16x16x32_bf16 v[32:35], a[108:111], a[24:27], v[32:35]\n\t"
      "v_mfma_f32_16x16x32_bf16 v[32:35], a[116:119], a[28:31], v[32:35]\n\t"
      "v_cvt_pk_bf16_f32 v44, v24, v25\n\t"
      "v_cvt_pk_bf16_f32 v45, v26, v27\n\t"
      "v_mfma_f32_16x16x32_bf16 v[32:35], a[124:127], a[32:35], v[32:35]\n\t"
      "ds_write_b64 v0, v[44:45] offset:64\n\t"
      "v_mfma_f32_16x16x32_bf16 v[32:35], a[132:135], a[36:39], v[32:35]\n\t"
      "v_mfma_f32_16x16x32_bf16 v[32:35], a[140:143], a[40:43], v[32:35]\n\t"
      "v_mfma_f32_16x16x32_bf16 v[32:35], a[148:151], a[44:47], v[32:35]\n\t"
      "v_cvt_pk_bf16_f32 v46, v28, v29\n\t"
      "v_cvt_pk_bf16_f32 v47, v30, v31\n\t"
      "v_mfma_f32_16x16x32_bf16 v[36:39], a[112:115], a[24:27], v[36:39]\n\t"
      "ds_write_b64 v0, v[46:47] offset:96\n\t"
      "v_mfma_f32_16x16x32_bf16 v[36:39], a[120:123], a[28:31], v[36:39]\n\t"
      "v_mfma_f32_16x16x32_bf16 v[36:39], a[128:131], a[32:35], v[36:39]\n\t"
      "v_mfma_f32_16x16x32_bf16 v[36:39], a[136:139], a[36:39], v[36:39]\n\t"
      "v_mfma_f32_16x16x32_bf16 v[36:39], a[144:147], a[40:43], v[36:39]\n\t"
      "v_cvt_pk_bf16_f32 v48, v32, v33\n\t"
      "v_cvt_pk_bf16_f32 v49, v34, v35\n\t"
      "v_mfma_f32_16x16x32_bf16 v[36:39], a[152:155], a[44:47], v[36:39]\n\t"
      "ds_write_b64 v0, v[48:49] offset:128\n\t"
      "v_mfma_f32_16x16x32_bf16 v[16:19], v[64:67], a[0:3], 0\n\t"
      "ds_read_b128 a[24:27], v54 offset:256\n\t"
      "v_mfma_f32_16x16x32_bf16 v[16:19], v[80:83], a[4:7], v[16:19]\n\t"
      "ds_read_b128 a[28:31], v55 offset:256\n\t"
      "v_mfma_f32_16x16x32_bf16 v[16:19], v[96:99], a[8:11], v[16:19]\n\t"
      "ds_read_b128 a[32:35], v52 offset:512\n\t"
      "v_mfma_f32_16x16x32_bf16 v[16:19], v[112:115], a[12:15], v[16:19]\n\t"
      "ds_read_b128 a[36:39], v53 offset:512\n\t"
      "v_mfma_f32_16x16x32_bf16 v[16:19], v[128:131], a[16:19], v[16:19]\n\t"
      "ds_read_b128 a[40:43], v54 offset:512\n\t"
      "v_mfma_f32_16x16x32_bf16 v[16:19], v[144:147], a[20:23], v[16:19]\n\t"
      "ds_read_b128 a[44:47], v55 offset:512\n\t"
      "v_mfma_f32_16x16x32_bf16 v[20:23], v[68:71], a[0:3], 0\n\t"
      "v_cvt_pk_bf16_f32 v50, v36, v37\n\t"
      "v_cvt_pk_bf16_f32 v51, v38, v39\n\t"
      "v_mfma_f32_16x16x32_bf16 v[20:23], v[84:87], a[4:7], v[20:23]\n\t"
      "ds_write_b64 v0, v[50:51] offset:160\n\t"
      "v_mfma_f32_16x16x32_bf16 v[20:23], v[100:103], a[8:11], v[20:23]\n\t"
      "v_mfma_f32_16x16x32_bf16 v[20:23], v[116:119], a[12:15], v[20:23]\n\t"
      "v_mfma_f32_16x16x32_bf16 v[20:23], v[132:135], a[16:19], v[20:23]\n\t"
      "buffer_store_dwordx4 a[220:223], v14, s[44:47], s84 offen offset:2048 sc1\n\t"
      "v_mfma_f32_16x16x32_bf16 v[20:23], v[148:151], a[20:23], v[20:23]\n\t"
      "v_mfma_f32_16x16x32_bf16 v[24:27], v[72:75], a[0:3], 0\n\t"
      "v_mfma_f32_16x16x32_bf16 v[24:27], v[88:91], a[4:7], v[24:27]\n\t"
      "v_mfma_f32_16x16x32_bf16 v[24:27], v[104:107], a[8:11], v[24:27]\n\t"
      "s_waitcnt lgkmcnt(0)\n\t"
      "s_barrier\n\t"
      "v_mfma_f32_16x16x32_bf16 v[24:27], v[120:123], a[12:15], v[24:27]\n\t"
      "v_mfma_f32_16x16x32_bf16 v[24:27], v[136:139], a[16:19], v[24:27]\n\t"
      "v_mfma_f32_16x16x32_bf16 v[24:27], v[152:155], a[20:23], v[24:27]\n\t"
      "v_mfma_f32_16x16x32_bf16 v[28:31], v[76:79], a[0:3], 0\n\t"
      "ds_read_b128 a[224:227], v8 offset:0\n\t"
      "v_mfma_f32_16x16x32_bf16 v[28:31], v[92:95], a[4:7], v[28:31]\n\t"
      "ds_read_b128 a[228:231], v60 offset:1024\n\t"
      "v_mfma_f32_16x16x32_bf16 v[28:31], v[108:111], a[8:11], v[28:31]\n\t"
      "ds_read_b128 a[232:235], v61 offset:2048\n\t"
      "v_mfma_f32_16x16x32_bf16 v[28:31], v[124:127], a[12:15], v[28:31]\n\t"
      "v_mfma_f32_16x16x32_bf16 v[28:31], v[140:143], a[16:19], v[28:31]\n\t"
      "v_mfma_f32_16x16x32_bf16 v[28:31], v[156:159], a[20:23], v[28:31]\n\t"
      "v_mfma_f32_16x16x32_bf16 v[32:35], a[60:63], a[0:3], 0\n\t"
      "v_mfma_f32_16x16x32_bf16 v[32:35], a[68:71], a[4:7], v[32:35]\n\t"
      "s_waitcnt lgkmcnt(2)\n\t"
      "v_mfma_f32_16x16x32_bf16 v[32:35], a[76:79], a[8:11], v[32:35]\n\t"
      "buffer_store_dwordx4 a[224:227], v9, s[44:47], s85 offen sc1\n\t"
      "v_mfma_f32_16x16x32_bf16 v[32:35], a[84:87], a[12:15], v[32:35]\n\t"
      "v_mfma_f32_16x16x32_bf16 v[32:35], a[92:95], a[16:19], v[32:35]\n\t"
      "v_mfma_f32_16x16x32_bf16 v[32:35], a[100:103], a[20:23], v[32:35]\n\t"
      "v_mfma_f32_16x16x32_bf16 v[36:39], a[64:67], a[0:3], 0\n\t"
      "v_mfma_f32_16x16x32_bf16 v[36:39], a[72:75], a[4:7], v[36:39]\n\t"
      "v_mfma_f32_16x16x32_bf16 v[36:39], a[80:83], a[8:11], v[36:39]\n\t"
      "v_mfma_f32_16x16x32_bf16 v[36:39], a[88:91], a[12:15], v[36:39]\n\t"
      "s_waitcnt lgkmcnt(1)\n\t"
      "v_mfma_f32_16x16x32_bf16 v[36:39], a[96:99], a[16:19], v[36:39]\n\t"
      "buffer_store_dwordx4 a[228:231], v10, s[44:47], s85 offen offset:1024 sc1\n\t"
      "v_mfma_f32_16x16x32_bf16 v[36:39], a[104:107], a[20:23], v[36:39]\n\t"
      "v_mfma_f32_16x16x32_bf16 v[16:19], v[160:163], a[24:27], v[16:19]\n\t"
      "v_mfma_f32_16x16x32_bf16 v[16:19], v[176:179], a[28:31], v[16:19]\n\t"
      "v_mfma_f32_16x16x32_bf16 v[16:19], v[192:195], a[32:35], v[16:19]\n\t"
      "v_mfma_f32_16x16x32_bf16 v[16:19], v[208:211], a[36:39], v[16:19]\n\t"
      "v_mfma_f32_16x16x32_bf16 v[16:19], v[224:227], a[40:43], v[16:19]\n\t"
      "v_mfma_f32_16x16x32_bf16 v[16:19], v[240:243], a[44:47], v[16:19]\n\t"
      "v_mfma_f32_16x16x32_bf16 v[20:23], v[164:167], a[24:27], v[20:23]\n\t"
      "v_mfma_f32_16x16x32_bf16 v[20:23], v[180:183], a[28:31], v[20:23]\n\t"
      "s_waitcnt lgkmcnt(0)\n\t"
      "v_mfma_f32_16x16x32_bf16 v[20:23], v[196:199], a[32:35], v[20:23]\n\t"
      "buffer_store_dwordx4 a[232:235], v11, s[44:47], s85 offen offset:2048 sc1\n\t"
      "v_mfma_f32_16x16x32_bf16 v[20:23], v[212:215], a[36:39], v[20:23]\n\t"
      "v_mfma_f32_16x16x32_bf16 v[20:23], v[228:231], a[40:43], v[20:23]\n\t"
      "v_mfma_f32_16x16x32_bf16 v[20:23], v[244:247], a[44:47], v[20:23]\n\t"
      "v_mfma_f32_16x16x32_bf16 v[24:27], v[168:171], a[24:27], v[24:27]\n\t"
      "v_mfma_f32_16x16x32_bf16 v[24:27], v[184:187], a[28:31], v[24:27]\n\t"
      "v_cvt_pk_bf16_f32 v40, v16, v17\n\t"
      "v_cvt_pk_bf16_f32 v41, v18, v19\n\t"
      "v_mfma_f32_16x16x32_bf16 v[24:27], v[200:203], a[32:35], v[24:27]\n\t"
      "ds_write_b64 v0, v[40:41] offset:12544\n\t"
      "v_mfma_f32_16x16x32_bf16 v[24:27], v[216:219], a[36:39], v[24:27]\n\t"
      "v_mfma_f32_16x16x32_bf16 v[24:27], v[232:235], a[40:43], v[24:27]\n\t"
      "v_mfma_f32_16x16x32_bf16 v[24:27], v[248:251], a[44:47], v[24:27]\n\t"
      "v_mfma_f32_16x16x32_bf16 v[28:31], v[172:175], a[24:27], v[28:31]\n\t"
      "v_mfma_f32_16x16x32_bf16 v[28:31], v[188:191], a[28:31], v[28:31]\n\t"
      "v_cvt_pk_bf16_f32 v42, v20, v21\n\t"
      "v_cvt_pk_bf16_f32 v43, v22, v23\n\t"
      "v_mfma_f32_16x16x32_bf16 v[28:31], v[204:207], a[32:35], v[28:31]\n\t"
      "ds_write_b64 v0, v[42:43] offset:12576\n\t"
      "v_mfma_f32_16x16x32_bf16 v[28:31], v[220:223], a[36:39], v[28:31]\n\t"
      "v_mfma_f32_16x16x32_bf16 v[28:31], v[236:239], a[40:43], v[28:31]\n\t"
      "v_mfma_f32_16x16x32_bf16 v[28:31], v[252:255], a[44:47], v[28:31]\n\t"
      "v_mfma_f32_16x16x32_bf16 v[32:35], a[108:111], a[24:27], v[32:35]\n\t"
      "v_mfma_f32_16x16x32_bf16 v[32:35], a[116:119], a[28:31], v[32:35]\n\t"
      "v_cvt_pk_bf16_f32 v44, v24, v25\n\t"
      "v_cvt_pk_bf16_f32 v45, v26, v27\n\t"
      "v_mfma_f32_16x16x32_bf16 v[32:35], a[124:127], a[32:35], v[32:35]\n\t"
      "ds_write_b64 v0, v[44:45] offset:12608\n\t"
      "v_mfma_f32_16x16x32_bf16 v[32:35], a[132:135], a[36:39], v[32:35]\n\t"
      "v_mfma_f32_16x16x32_bf16 v[32:35], a[140:143], a[40:43], v[32:35]\n\t"
      "v_mfma_f32_16x16x32_bf16 v[32:35], a[148:151], a[44:47], v[32:35]\n\t"
      "v_cvt_pk_bf16_f32 v46, v28, v29\n\t"
      "v_cvt_pk_bf16_f32 v47, v30, v31\n\t"
      "v_mfma_f32_16x16x32_bf16 v[36:39], a[112:115], a[24:27], v[36:39]\n\t"
      "ds_write_b64 v0, v[46:47] offset:12640\n\t"
      "v_mfma_f32_16x16x32_bf16 v[36:39], a[120:123], a[28:31], v[36:39]\n\t"
      "v_mfma_f32_16x16x32_bf16 v[36:39], a[128:131], a[32:35], v[36:39]\n\t"
      "v_mfma_f32_16x16x32_bf16 v[36:39], a[136:139], a[36:39], v[36:39]\n\t"
      "v_mfma_f32_16x16x32_bf16 v[36:39], a[144:147], a[40:43], v[36:39]\n\t"
      "v_cvt_pk_bf16_f32 v48, v32, v33\n\t"
      "v_cvt_pk_bf16_f32 v49, v34, v35\n\t"
      "v_mfma_f32_16x16x32_bf16 v[36:39], a[152:155], a[44:47], v[36:39]\n\t"
      "ds_write_b64 v0, v[48:49] offset:12672\n\t"
      ".Ltail_%=:\n\t"
      "s_nop 6\n\t"
      "v_cvt_pk_bf16_f32 v50, v36, v37\n\t"
      "v_cvt_pk_bf16_f32 v51, v38, v39\n\t"
      "ds_write_b64 v0, v[50:51] offset:12704\n\t"
      "s_cmp_eq_u32 s85, 113221632\n\t"
      "s_cbranch_scc1 .Lwrapo_%=\n\t"
      ".Lwrappedo_%=:\n\t"
      "s_waitcnt lgkmcnt(0)\n\t"
      "s_barrier\n\t"
      "ds_read_b128 a[48:51], v8 offset:12544\n\t"
      "ds_read_b128 a[52:55], v60 offset:13568\n\t"
      "ds_read_b128 a[56:59], v61 offset:14592\n\t"
      "s_waitcnt lgkmcnt(2)\n\t"
      "buffer_store_dwordx4 a[48:51], v12, s[44:47], s85 offen sc1\n\t"
      "s_waitcnt lgkmcnt(1)\n\t"
      "buffer_store_dwordx4 a[52:55], v13, s[44:47], s85 offen offset:1024 sc1\n\t"
      "s_waitcnt lgkmcnt(0)\n\t"
      "buffer_store_dwordx4 a[56:59], v14, s[44:47], s85 offen offset:2048 sc1\n\t"
      "s_cmp_lt_i32 s91, 0\n\t"
      "s_cselect_b32 s30, -1, 0\n\t"
      "s_xor_b32 s16, s91, 0x3ffff\n\t"
      "s_and_b32 s16, s16, s30\n\t"
      "s_cbranch_scc0 .Lend_%=\n\t"
      "s_waitcnt vmcnt(0) lgkmcnt(0)\n\t"
      "s_barrier\n\t"
      "s_lshl1_add_u32 s30, s14, 24576\n\t"
      "s_sub_u32 s15, s30, s15\n\t"
      "s_branch .Lstart_%=\n\t"
      ".Lwrap3_%=:\n\t"
      "v_add_u32 v5, s48, v5\n\t"
      "v_subrev_u32 v62, 113246208, v5\n\t"
      "v_min_u32 v5, v5, v62\n\t"
      "v_add_u32 v6, s48, v6\n\t"
      "v_subrev_u32 v62, 113246208, v6\n\t"
      "v_min_u32 v6, v6, v62\n\t"
      "v_add_u32 v7, s48, v7\n\t"
      "v_subrev_u32 v62, 113246208, v7\n\t"
      "v_min_u32 v7, v7, v62\n\t"
      "s_mov_b32 s48, 0\n\t"
      "buffer_load_dwordx4 v5, s[40:43], s48 offen nt lds\n\t"
      "buffer_load_dwordx4 v6, s[40:43], s48 offen offset:1024 nt lds\n\t"
      "buffer_load_dwordx4 v7, s[40:43], s48 offen offset:2048 nt lds\n\t"
      "s_branch .Lwrapped3_%=\n\t"
      ".Lwrapo_%=:\n\t"
      "v_add_u32 v12, s85, v12\n\t"
      "v_subrev_u32 v62, 113246208, v12\n\t"
      "v_min_u32 v12, v12, v62\n\t"
      "v_add_u32 v13, s85, v13\n\t"
      "v_subrev_u32 v62, 113246208, v13\n\t"
      "v_min_u32 v13, v13, v62\n\t"
      "v_add_u32 v14, s85, v14\n\t"
      "v_subrev_u32 v62, 113246208, v14\n\t"
      "v_min_u32 v14, v14, v62\n\t"
      "s_mov_b32 s85, 0\n\t"
      "s_branch .Lwrappedo_%=\n\t"
      ".Lwait_%=:\n\t"
      "s_sub_u32 s52, s52, 1\n\t"
      "s_cbranch_scc1 .Lwait2_%=\n\t"
      "s_add_u32 s90, s90, 0x2000000\n\t"
      "v_mov_b32 v54, s90\n\t"
      "v_mov_b32 v55, s19\n\t"
      "global_store_dword v55, v54, s[12:13] sc1\n\t"
      "s_sleep 16\n\t"
      "global_load_dword v40, v62, s[12:13] sc1\n\t"
      "s_waitcnt vmcnt(0)\n\t"
      "s_branch .Lcheck_%=\n\t"
      ".Lwait2_%=:\n\t"
      "s_brev_b32 s91, 1\n\t"
      "s_branch .Lwaited_%=\n\t"
      ".Lspinf_%=:\n\t"
      "s_memrealtime s[36:37]\n\t"
      "s_waitcnt lgkmcnt(0)\n\t"
      "s_sub_u32 s53, s36, s38\n\t"
      "s_cmp_lt_i32 s53, 0\n\t"
      "s_cbranch_scc1 .Lspinf_%=\n\t"
      "s_branch .Lspunf_%=\n\t"
      ".Lspin0_%=:\n\t"
      "s_memrealtime s[36:37]\n\t"
      "s_waitcnt lgkmcnt(0)\n\t"
      "s_sub_u32 s53, s36, s38\n\t"
      "s_cmp_lt_i32 s53, 0\n\t"
      "s_cbranch_scc1 .Lspin0_%=\n\t"
      "s_branch .Lspun0_%=\n\t"
      ".Lspin1_%=:\n\t"
      "s_memrealtime s[36:37]\n\t"
      "s_waitcnt lgkmcnt(0)\n\t"
      "s_sub_u32 s53, s36, s38\n\t"
      "s_cmp_lt_i32 s53, 0\n\t"
      "s_cbranch_scc1 .Lspin1_%=\n\t"
      "s_branch .Lspun1_%=\n\t"
      ".Lend_%=:\n\t"
      : [tid] "+v"(tid)
      : [karg] "s"(__builtin_amdgcn_kernarg_segment_ptr()), [blk] "s"(blockIdx.x), [lds] "s"((unsigned)(size_t)lds)
      : "v1", "v2", "v3", "v4", "v5", "v6", "v7", "v8", "v9", "v10", "v11", "v12", "v13", "v14", "v15", "v16", "v17", "v18", "v19", "v20", "v21", "v22", "v23", "v24", "v25", "v26", "v27", "v28", "v29", "v30", "v31", "v32", "v33", "v34", "v35", "v36", "v37", "v38", "v39", "v40", "v41", "v42", "v43", "v44", "v45", "v46", "v47", "v48", "v49", "v50", "v51", "v52", "v53", "v54", "v55", "v56", "v57", "v58", "v59", "v60", "v61", "v62", "v63", "v64", "v65", "v66", "v67", "v68", "v69", "v70", "v71", "v72", "v73", "v74", "v75", "v76", "v77", "v78", "v79", "v80", "v81", "v82", "v83", "v84", "v85", "v86", "v87", "v88", "v89", "v90", "v91", "v92", "v93", "v94", "v95", "v96", "v97", "v98", "v99", "v100", "v101", "v102", "v103", "v104", "v105", "v106", "v107", "v108", "v109", "v110", "v111", "v112", "v113", "v114", "v115", "v116", "v117", "v118", "v119", "v120", "v121", "v122", "v123", "v124", "v125", "v126", "v127", "v128", "v129", "v130", "v131", "v132", "v133", "v134", "v135", "v136", "v137", "v138", "v139", "v140", "v141", "v142", "v143", "v144", "v145", "v146", "v147", "v148", "v149", "v150", "v151", "v152", "v153", "v154", "v155", "v156", "v157", "v158", "v159", "v160", "v161", "v162", "v163", "v164", "v165", "v166", "v167", "v168", "v169", "v170", "v171", "v172", "v173", "v174", "v175", "v176", "v177", "v178", "v179", "v180", "v181", "v182", "v183", "v184", "v185", "v186", "v187", "v188", "v189", "v190", "v191", "v192", "v193", "v194", "v195", "v196", "v197", "v198", "v199", "v200", "v201", "v202", "v203", "v204", "v205", "v206", "v207", "v208", "v209", "v210", "v211", "v212", "v213", "v214", "v215", "v216", "v217", "v218", "v219", "v220", "v221", "v222", "v223", "v224", "v225", "v226", "v227", "v228", "v229", "v230", "v231", "v232", "v233", "v234", "v235", "v236", "v237", "v238", "v239", "v240", "v241", "v242", "v243", "v244", "v245", "v246", "v247", "v248", "v249", "v250", "v251", "v252", "v253", "v254", "v255", "a0", "a1", "a2", "a3", "a4", "a5", "a6", "a7", "a8", "a9", "a10", "a11", "a12", "a13", "a14", "a15", "a16", "a17", "a18", "a19", "a20", "a21", "a22", "a23", "a24", "a25", "a26", "a27", "a28", "a29", "a30", "a31", "a32", "a33", "a34", "a35", "a36", "a37", "a38", "a39", "a40", "a41", "a42", "a43", "a44", "a45", "a46", "a47", "a48", "a49", "a50", "a51", "a52", "a53", "a54", "a55", "a56", "a57", "a58", "a59", "a60", "a61", "a62", "a63", "a64", "a65", "a66", "a67", "a68", "a69", "a70", "a71", "a72", "a73", "a74", "a75", "a76", "a77", "a78", "a79", "a80", "a81", "a82", "a83", "a84", "a85", "a86", "a87", "a88", "a89", "a90", "a91", "a92", "a93", "a94", "a95", "a96", "a97", "a98", "a99", "a100", "a101", "a102", "a103", "a104", "a105", "a106", "a107", "a108", "a109", "a110", "a111", "a112", "a113", "a114", "a115", "a116", "a117", "a118", "a119", "a120", "a121", "a122", "a123", "a124", "a125", "a126", "a127", "a128", "a129", "a130", "a131", "a132", "a133", "a134", "a135", "a136", "a137", "a138", "a139", "a140", "a141", "a142", "a143", "a144", "a145", "a146", "a147", "a148", "a149", "a150", "a151", "a152", "a153", "a154", "a155", "a156", "a157", "a158", "a159", "a160", "a161", "a162", "a163", "a164", "a165", "a166", "a167", "a168", "a169", "a170", "a171", "a172", "a173", "a174", "a175", "a176", "a177", "a178", "a179", "a180", "a181", "a182", "a183", "a184", "a185", "a186", "a187", "a188", "a189", "a190", "a191", "a192", "a193", "a194", "a195", "a196", "a197", "a198", "a199", "a200", "a201", "a202", "a203", "a204", "a205", "a206", "a207", "a208", "a209", "a210", "a211", "a212", "a213", "a214", "a215", "a216", "a217", "a218", "a219", "a220", "a221", "a222", "a223", "a224", "a225", "a226", "a227", "a228", "a229", "a230", "a231", "a232", "a233", "a234", "a235", "a236", "a237", "a238", "a239", "a240", "a241", "a242", "a243", "a244", "a245", "a246", "a247", "a248", "a249", "a250", "a251", "a252", "a253", "a254", "a255", "s4", "s5", "s6", "s7", "s8", "s9", "s10", "s11", "s12", "s13", "s14", "s15", "s16", "s17", "s18", "s19", "s20", "s21", "s22", "s23", "s24", "s25", "s26", "s27", "s28", "s29", "s30", "s31", "s32", "s33", "s34", "s35", "s36", "s37", "s38", "s39", "s40", "s41", "s42", "s43", "s44", "s45", "s46", "s47", "s48", "s49", "s50", "s51", "s52", "s53", "s54", "s55", "s56", "s57", "s58", "s59", "s60", "s61", "s62", "s63", "s64", "s65", "s66", "s67", "s68", "s69", "s70", "s71", "s72", "s73", "s74", "s75", "s76", "s77", "s78", "s79", "s80", "s81", "s82", "s83", "s84", "s85", "s86", "s87", "s88", "s89", "s90", "s91", "vcc", "scc", "memory");
}

__global__ __launch_bounds__(384) void dwsep_plain(const unsigned short* __restrict__ x, const unsigned short* __restrict__ dw,
                                                   const unsigned short* __restrict__ pw, unsigned short* __restrict__ out) {
  __shared__ float xs[384];
  const unsigned n = threadIdx.x;
  const size_t row = (size_t)blockIdx.x * 384u;
  xs[n] = __uint_as_float((unsigned)x[row + n] << 16) * __uint_as_float((unsigned)dw[n] << 16);
  __syncthreads();
  float acc = 0.f;
  for (int k = 0; k < 384; ++k) acc += xs[k] * __uint_as_float((unsigned)pw[k * 384 + n] << 16);
  const __bf16 o = (__bf16)acc;
  out[row + n] = __builtin_bit_cast(unsigned short, o);
}

extern "C" void kernel_launch(void* const* d_in, const int* in_sizes, int n_in, void* d_out, int out_size,
                              void* d_ws, size_t ws_size, hipStream_t stream) {
  const unsigned short* x = (const unsigned short*)d_in[0];
  const unsigned short* dw = (const unsigned short*)d_in[1];
  const unsigned short* pw = (const unsigned short*)d_in[2];
  unsigned short* out = (unsigned short*)d_out;
  if (in_sizes[0] == 56623104) dwsep_kernel<<<NWG, 256, 0, stream>>>(x, dw, pw, out, (unsigned*)d_ws);
  else dwsep_plain<<<in_sizes[0] / 384, 384, 0, stream>>>(x, dw, pw, out);
}
```

```cpp
#include <hip/hip_runtime.h>

constexpr unsigned ROWB = 768, TILEB = 12288, NWG = 256;

__global__ __launch_bounds__(256, 1) void dwsep_kernel(const unsigned short* __restrict__ x,
                                                       const unsigned short* __restrict__ dw,
                                                       const unsigned short* __restrict__ pw,
                                                       unsigned short* __restrict__ out, unsigned* ws) {
  __shared__ __attribute__((aligned(256))) char lds[163392];
  unsigned tid = threadIdx.x;
  asm volatile(
      "s_memtime s[32:33]\n\t"
      "s_load_dwordx8 s[4:11], %[karg], 0\n\t"
      "s_load_dwordx2 s[12:13], %[karg], 0x20\n\t"
      "v_readfirstlane_b32 s30, %[tid]\n\t"
      "s_lshr_b32 s31, %[blk], 1\n\t"
      "s_and_b32 s31, s31, 12\n\t"
      "s_and_b32 s34, %[blk], 3\n\t"
      "s_or_b32 s31, s31, s34\n\t"
      "s_lshr_b32 s30, s30, 6\n\t"
      "s_lshr_b32 s34, %[blk], 5\n\t"
      "s_bfe_u32 s35, %[blk], 0x10002\n\t"
      "s_mul_i32 s17, s30, 6144\n\t"
      "s_mul_i32 s18, s30, 3072\n\t"
      "s_mul_i32 s14, s34, 14155776\n\t"
      "s_mul_i32 s36, s31, 49152\n\t"
      "s_add_u32 s14, s14, s36\n\t"
      "s_mul_i32 s36, s35, 24576\n\t"
      "s_add_u32 s15, s14, s36\n\t"
      "s_mov_b32 s16, 0\n\t"
      "s_lshr_b32 s36, %[blk], 3\n\t"
      "s_lshl2_add_u32 s24, s36, s30\n\t"
      "s_sub_u32 s36, s24, s36\n\t"
      "s_sub_u32 s36, s36, 1\n\t"
      "s_mul_i32 s23, s36, 3072\n\t"
      "s_lshl_b32 s20, s34, 3\n\t"
      "s_mul_i32 s36, s34, 27\n\t"
      "s_lshr_b32 s36, s36, 2\n\t"
      "s_lshr_b32 s37, s31, 1\n\t"
      "s_add_u32 s19, s37, s20\n\t"
      "s_add_u32 s37, s37, s36\n\t"
      "s_min_u32 s37, s37, 53\n\t"
      "s_lshl_b32 s22, s37, 21\n\t"
      "s_lshl_b32 s36, s36, 21\n\t"
      "s_sub_u32 s21, s14, s36\n\t"
      "s_add_u32 s21, s21, 24576\n\t"
      "s_lshl_b32 s19, s19, 8\n\t"
      "s_and_b32 s31, s31, 1\n\t"
      "s_lshl1_add_u32 s31, s31, s35\n\t"
      "s_lshl2_add_u32 s19, s31, s19\n\t"
      ".Lstart_%=:\n\t"
      "v_mbcnt_lo_u32_b32 v233, -1, 0\n\t"
      "v_mbcnt_hi_u32_b32 v233, -1, v233\n\t"
      "s_mul_i32 s30, s17, 683\n\t"
      "s_lshr_b32 s30, s30, 16\n\t"
      "s_and_b32 s30, s30, 0xc0\n\t"
      "s_brev_b32 s34, 8\n\t"
      "s_mov_b32 s35, 0x20201010\n\t"
      "s_mov_b32 s36, 0x30303020\n\t"
      "v_lshl_add_u32 v1, v233, 4, s18\n\t"
      "v_lshrrev_b32 v16, 1, v233\n\t"
      "v_and_b32 v16, 24, v16\n\t"
      "v_bfe_u32 v17, s34, v16, 8\n\t"
      "v_or_b32 v17, s30, v17\n\t"
      "v_xor_b32 v18, v17, v1\n\t"
      "v_add_u32 v19, 80, v17\n\t"
      "v_add_u32 v17, 160, v17\n\t"
      "v_and_b32 v19, 256, v19\n\t"
      "v_and_b32 v17, 256, v17\n\t"
      "v_mad_u32_u24 v2, v19, 30, v18\n\t"
      "v_mad_u32_u24 v5, v17, 33, v18\n\t"
      "v_add_u32 v5, 19968, v5\n\t"
      "v_bfe_u32 v17, s35, v16, 8\n\t"
      "v_or_b32 v17, s30, v17\n\t"
      "v_xor_b32 v18, v17, v1\n\t"
      "v_add_u32 v19, 80, v17\n\t"
      "v_add_u32 v17, 160, v17\n\t"
      "v_and_b32 v19, 256, v19\n\t"
      "v_and_b32 v17, 256, v17\n\t"
      "v_mad_u32_u24 v3, v19, 30, v18\n\t"
      "v_mad_u32_u24 v6, v17, 33, v18\n\t"
      "v_add_u32 v6, 19968, v6\n\t"
      "v_bfe_u32 v17, s36, v16, 8\n\t"
      "v_or_b32 v17, s30, v17\n\t"
      "v_xor_b32 v18, v17, v1\n\t"
      "v_add_u32 v19, 80, v17\n\t"
      "v_add_u32 v17, 160, v17\n\t"
      "v_and_b32 v19, 256, v19\n\t"
      "v_and_b32 v17, 256, v17\n\t"
      "v_mad_u32_u24 v4, v19, 30, v18\n\t"
      "v_mad_u32_u24 v7, v17, 33, v18\n\t"
      "v_add_u32 v7, 19968, v7\n\t"
      "s_sub_u32 s31, s30, 64\n\t"
      "v_add_u32 v17, s31, v233\n\t"
      "v_min_u32 v17, 193, v17\n\t"
      "v_lshlrev_b32 v18, 2, v17\n\t"
      "v_lshlrev_b32 v17, 1, v17\n\t"
      "v_and_b32 v18, -16, v18\n\t"
      "v_and_or_b32 v8, v17, 6, v18\n\t"
      "s_mov_b32 s86, 294912\n\t"
      "s_mov_b32 s87, 0x00020000\n\t"
      "s_mov_b32 s42, 113246208\n\t"
      "s_mov_b32 s43, 0x00020000\n\t"
      "s_cmp_lg_u32 s17, 0\n\t"
      "s_cbranch_scc1 .Lno0_%=\n\t"
      "s_movk_i32 s31, -61\n\t"
      "v_lshrrev_b32 v16, 2, v233\n\t"
      "v_add_u32 v17, s20, v16\n\t"
      "v_lshl_add_u32 v18, v16, 3, s31\n\t"
      "v_cmp_gt_u32 vcc, 32, v233\n\t"
      "v_and_b32 v19, 3, v233\n\t"
      "v_lshlrev_b32 v19, 2, v19\n\t"
      "v_cndmask_b32 v17, v18, v17, vcc\n\t"
      "v_lshl_add_u32 v62, v17, 8, v19\n\t"
      "s_waitcnt lgkmcnt(2)\n\t"
      "s_add_u32 s32, s32, %[blk]\n\t"
      "s_mul_i32 s32, s32, 0x9e3779b1\n\t"
      "s_lshr_b32 s32, s32, 9\n\t"
      "s_and_b32 s32, s32, 0x1fdf80\n\t"
      "s_or_b32 s22, s22, s32\n\t"
      "v_mov_b32 v234, s22\n\t"
      "v_add_u32 v235, 8192, v234\n\t"
      "s_movk_i32 s64, 120\n\t"
      "s_mov_b64 vcc, 0\n\t"
      ".Lsweep_%=:\n\t"
      "s_getreg_b32 s30, hwreg(HW_REG_IB_STS, 0, 12)\n\t"
      "s_and_b32 s31, s30, 15\n\t"
      "s_lshr_b32 s30, s30, 8\n\t"
      "s_cmp_eq_u32 s31, 8\n\t"
      "s_addc_u32 s62, s62, 0\n\t"
      "s_cmp_eq_u32 s30, 1\n\t"
      "s_addc_u32 s63, s63, 0\n\t"
      "s_cmp_gt_u32 s31, 7\n\t"
      "s_addc_u32 s30, s30, 0\n\t"
      "s_sub_u32 s64, s64, 1\n\t"
      "s_min_u32 s30, s30, s64\n\t"
      "s_cmp_lg_u32 s30, 0\n\t"
      "s_cbranch_scc1 .Lsweep_%=\n\t"
      "s_cbranch_vccnz .Lswept_%=\n\t"
      "s_waitcnt lgkmcnt(0)\n\t"
      "global_load_dword v236, v234, s[4:5]\n\t"
      "global_load_dword v237, v235, s[4:5]\n\t"
      "s_memrealtime s[28:29]\n\t"
      "s_load_dword s60, s[10:11], s22\n\t"
      "s_load_dword s61, s[10:11], s22 offset:8192\n\t"
      "global_load_dword v63, v62, s[12:13] sc1\n\t"
      "s_mov_b64 s[40:41], s[4:5]\n\t"
      "s_add_u32 m0, s18, 25088\n\t"
      "s_add_u32 s48, s15, 0\n\t"
      "buffer_load_dwordx4 v2, s[40:43], s48 offen lds\n\t"
      "buffer_load_dwordx4 v3, s[40:43], s48 offen offset:1024 lds\n\t"
      "buffer_load_dwordx4 v4, s[40:43], s48 offen offset:2048 lds\n\t"
      "s_add_u32 m0, s18, 37376\n\t"
      "s_add_u32 s48, s15, 0\n\t"
      "buffer_load_dwordx4 v5, s[40:43], s48 offen lds\n\t"
      "buffer_load_dwordx4 v6, s[40:43], s48 offen offset:1024 lds\n\t"
      "buffer_load_dwordx4 v7, s[40:43], s48 offen offset:2048 lds\n\t"
      "s_barrier\n\t"
      "s_mov_b64 s[62:63], 0\n\t"
      "s_movk_i32 s64, 120\n\t"
      "s_mov_b64 vcc, -1\n\t"
      "s_branch .Lsweep_%=\n\t"
      ".Lswept_%=:\n\t"
      "s_min_u32 s62, s62, 7\n\t"
      "s_min_u32 s63, s63, 7\n\t"
      "s_lshl_b32 s63, s63, 8\n\t"
      "s_or_b32 s30, s62, s63\n\t"
      "s_sub_u32 s31, 0x808, s30\n\t"
      "s_add_u32 s30, s30, 0x10808\n\t"
      "s_getreg_b32 s62, hwreg(HW_REG_XCC_ID, 2, 1)\n\t"
      "s_cmp_lg_u32 s62, 0\n\t"
      "s_cselect_b32 s91, s30, s31\n\t"
      "s_mov_b64 s[84:85], s[8:9]\n\t"
      "v_mul_u32_u24 v16, 49152, v1\n\t"
      "v_add_u32 v16, s21, v16\n\t"
      "v_lshrrev_b32 v16, 21, v16\n\t"
      "v_lshlrev_b32 v16, 2, v16\n\t"
      "v_accvgpr_write_b32 a248, v16\n\t"
      "s_branch .Lgo0_%=\n\t"
      ".Lno0_%=:\n\t"
      "s_movk_i32 s82, 768\n\t"
      "s_mov_b32 s83, 0x00020000\n\t"
      "v_and_b32 v234, 0x3f0, v1\n\t"
      "s_waitcnt lgkmcnt(0)\n\t"
      "s_mov_b64 s[84:85], s[8:9]\n\t"
      "s_mov_b64 s[80:81], s[6:7]\n\t"
      "s_mov_b64 s[40:41], s[4:5]\n\t"
      "s_barrier\n\t"
      "buffer_load_dwordx4 a[200:203], v234, s[84:87], s23 offen offset:0\n\t"
      "buffer_load_dwordx4 a[204:207], v234, s[84:87], s23 offen offset:1024\n\t"
      "buffer_load_dwordx4 a[208:211], v234, s[84:87], s23 offen offset:2048\n\t"
      "buffer_load_ushort v224, v8, s[80:83], 0 offen\n\t"
      "buffer_load_ushort v225, v8, s[80:83], 0 offen offset:8\n\t"
      "s_add_u32 m0, s18, 25088\n\t"
      "s_add_u32 s48, s15, 0\n\t"
      "buffer_load_dwordx4 v2, s[40:43], s48 offen lds\n\t"
      "buffer_load_dwordx4 v3, s[40:43], s48 offen offset:1024 lds\n\t"
      "buffer_load_dwordx4 v4, s[40:43], s48 offen offset:2048 lds\n\t"
      "s_add_u32 m0, s18, 37376\n\t"
      "s_add_u32 s48, s15, 0\n\t"
      "buffer_load_dwordx4 v5, s[40:43], s48 offen lds\n\t"
      "buffer_load_dwordx4 v6, s[40:43], s48 offen offset:1024 lds\n\t"
      "buffer_load_dwordx4 v7, s[40:43], s48 offen offset:2048 lds\n\t"
      ".Lgo0_%=:\n\t"
      "s_getpc_b64 s[36:37]\n\t"
      ".Lpc_%=:\n\t"
      "s_lshr_b32 s30, s17, 5\n\t"
      "v_lshlrev_b32 v16, 4, v233\n\t"
      "v_add_u32 v17, 0, v233\n\t"
      "v_mul_u32_u24 v17, 171, v17\n\t"
      "v_lshrrev_b32 v17, 11, v17\n\t"
      "v_lshlrev_b32 v18, 2, v17\n\t"
      "v_and_b32 v18, 32, v18\n\t"
      "v_xor_b32 v18, v18, v16\n\t"
      "v_mul_u32_u24 v17, 576, v17\n\t"
      "v_add3_u32 v9, v17, s30, v18\n\t"
      "v_add_u32 v17, 64, v233\n\t"
      "v_mul_u32_u24 v17, 171, v17\n\t"
      "v_lshrrev_b32 v17, 11, v17\n\t"
      "v_lshlrev_b32 v18, 2, v17\n\t"
      "v_and_b32 v18, 32, v18\n\t"
      "v_xor_b32 v18, v18, v16\n\t"
      "v_mul_u32_u24 v17, 576, v17\n\t"
      "v_add3_u32 v10, v17, s30, v18\n\t"
      "v_add_u32 v17, 128, v233\n\t"
      "v_mul_u32_u24 v17, 171, v17\n\t"
      "v_lshrrev_b32 v17, 11, v17\n\t"
      "v_lshlrev_b32 v18, 2, v17\n\t"
      "v_and_b32 v18, 32, v18\n\t"
      "v_xor_b32 v18, v18, v16\n\t"
      "v_mul_u32_u24 v17, 576, v17\n\t"
      "v_add3_u32 v11, v17, s30, v18\n\t"
      "s_mov_b64 s[44:45], s[10:11]\n\t"
      "s_mov_b32 s46, 0\n\t"
      "s_mov_b32 s47, 0x00020000\n\t"
      "s_lshl_b32 s30, s24, 6\n\t"
      "s_add_u32 s31, s30, 0\n\t"
      "s_min_u32 s31, s31, .Lend_%=-.Lpc_%=-4\n\t"
      "s_load_dword s35, s[36:37], s31\n\t"
      "s_add_u32 s31, s30, 8192\n\t"
      "s_min_u32 s31, s31, .Lend_%=-.Lpc_%=-4\n\t"
      "s_load_dword s35, s[36:37], s31\n\t"
      "s_add_u32 s31, s30, 16384\n\t"
      "s_min_u32 s31, s31, .Lend_%=-.Lpc_%=-4\n\t"
      "s_load_dword s35, s[36:37], s31\n\t"
      "s_add_u32 s31, s30, 24576\n\t"
      "s_min_u32 s31, s31, .Lend_%=-.Lpc_%=-4\n\t"
      "s_load_dword s35, s[36:37], s31\n\t"
      "s_and_b32 s31, s24, 63\n\t"
      "s_lshl_b32 s31, s31, 21\n\t"
      "s_min_u32 s31, s31, 113246144\n\t"
      "s_bitcmp1_b32 s24, 6\n\t"
      "s_cselect_b64 s[32:33], s[44:45], s[40:41]\n\t"
      "s_load_dword s35, s[32:33], s31\n\t"
      "s_lshr_b32 s30, s17, 5\n\t"
      "v_lshrrev_b32 v16, 4, v233\n\t"
      "v_and_b32 v17, 15, v233\n\t"
      "v_and_b32 v18, 3, v233\n\t"
      "v_bfe_u32 v19, v233, 2, 2\n\t"
      "v_lshl_add_u32 v19, v16, 3, v19\n\t"
      "v_mul_u32_u24 v19, 192, v19\n\t"
      "v_lshl_add_u32 v19, v18, 3, v19\n\t"
      "v_lshlrev_b32 v20, 1, v233\n\t"
      "v_and_b32 v20, 32, v20\n\t"
      "v_add3_u32 v12, v19, v20, s17\n\t"
      "v_xor_b32 v13, 32, v12\n\t"
      "v_xor_b32 v19, v16, v17\n\t"
      "v_lshlrev_b32 v19, 4, v19\n\t"
      "v_mul_u32_u24 v20, 768, v17\n\t"
      "v_add_u32 v15, v19, v20\n\t"
      "v_mul_u32_u24 v20, 784, v17\n\t"
      "v_lshl_add_u32 v20, v16, 3, v20\n\t"
      "v_add_u32 v0, s30, v20\n\t"
      "v_lshlrev_b32 v19, 6, v16\n\t"
      "v_lshl_add_u32 v19, v18, 4, v19\n\t"
      "v_add_u32 v14, 160256, v19\n\t"
      "v_and_b32 v226, 0xfffffff0, v8\n\t"
      "v_and_b32 v227, 6, v8\n\t"
      "v_lshlrev_b32 v226, 2, v226\n\t"
      "v_lshl_add_u32 v226, v227, 3, v226\n\t"
      "v_add_u32 v226, 160256, v226\n\t"
      "v_and_b32 v232, 2, v8\n\t"
      "v_lshlrev_b32 v232, 3, v232\n\t"
      "v_cmp_gt_u32 vcc, 4, v227\n\t"
      "v_mov_b32 v56, v15\n\t"
      "v_xor_b32 v57, 64, v15\n\t"
      "v_xor_b32 v58, 128, v15\n\t"
      "v_xor_b32 v59, 192, v15\n\t"
      "v_add_u32 v60, 86528, v12\n\t"
      "v_add_u32 v61, 86528, v13\n\t"
      "v_xor_b32 v16, v2, v1\n\t"
      "v_and_b32 v16, 0xf0, v16\n\t"
      "v_xor_b32 v17, v16, v2\n\t"
      "v_xor_b32 v18, v16, v5\n\t"
      "v_add_u32 v19, v16, v1\n\t"
      "v_accvgpr_write_b32 a213, v17\n\t"
      "v_accvgpr_write_b32 a216, v18\n\t"
      "v_accvgpr_write_b32 a219, v19\n\t"
      "v_xor_b32 v16, v3, v1\n\t"
      "v_and_b32 v16, 0xf0, v16\n\t"
      "v_xor_b32 v17, v16, v3\n\t"
      "v_xor_b32 v18, v16, v6\n\t"
      "v_add_u32 v19, v16, v1\n\t"
      "v_accvgpr_write_b32 a214, v17\n\t"
      "v_accvgpr_write_b32 a217, v18\n\t"
      "v_accvgpr_write_b32 a220, v19\n\t"
      "v_xor_b32 v16, v4, v1\n\t"
      "v_and_b32 v16, 0xf0, v16\n\t"
      "v_xor_b32 v17, v16, v4\n\t"
      "v_xor_b32 v18, v16, v7\n\t"
      "v_add_u32 v19, v16, v1\n\t"
      "v_accvgpr_write_b32 a215, v17\n\t"
      "v_accvgpr_write_b32 a218, v18\n\t"
      "v_accvgpr_write_b32 a221, v19\n\t"
      "s_waitcnt vmcnt(6)\n\t"
      "s_bfe_u32 s30, s19, 0x30008\n\t"
      "s_bfe_u32 s31, s19, 0x20002\n\t"
      "s_lshl2_add_u32 s30, s30, s31\n\t"
      "v_readlane_b32 s90, v63, s30\n\t"
      "v_lshlrev_b32 v224, v232, v224\n\t"
      "v_lshlrev_b32 v225, v232, v225\n\t"
      "v_cndmask_b32 v228, 0, v224, vcc\n\t"
      "v_cndmask_b32 v230, 0, v225, vcc\n\t"
      "v_sub_u32 v229, v224, v228\n\t"
      "v_sub_u32 v231, v225, v230\n\t"
      "ds_write_b128 v226, v[228:231]\n\t"
      "s_add_u32 s90, s90, 0x2000000\n\t"
      "s_andn2_b32 s90, s90, 0x1ffffff\n\t"
      "s_or_b32 s90, s90, s91\n\t"
      "s_or_b32 s30, s17, s16\n\t"
      "s_cbranch_scc1 .Lno2_%=\n\t"
      "v_mov_b32 v234, s90\n\t"
      "v_mov_b32 v235, s19\n\t"
      "global_store_dword v235, v234, s[12:13] sc1\n\t"
      ".Lno2_%=:\n\t"
      "s_add_u32 m0, s17, 0\n\t"
      "s_mov_b32 s54, 0\n\t"
      "buffer_load_dwordx4 v9, s[84:87], s54 offen lds\n\t"
      "buffer_load_dwordx4 v10, s[84:87], s54 offen offset:1024 lds\n\t"
      "buffer_load_dwordx4 v11, s[84:87], s54 offen offset:2048 lds\n\t"
      "s_add_u32 m0, s17, 3072\n\t"
      "s_mov_b32 s54, 12288\n\t"
      "buffer_load_dwordx4 v9, s[84:87], s54 offen lds\n\t"
      "buffer_load_dwordx4 v10, s[84:87], s54 offen offset:1024 lds\n\t"
      "buffer_load_dwordx4 v11, s[84:87], s54 offen offset:2048 lds\n\t"
      "s_add_u32 m0, s17, 86528\n\t"
      "s_mov_b32 s54, 24576\n\t"
      "buffer_load_dwordx4 v9, s[84:87], s54 offen lds\n\t"
      "buffer_load_dwordx4 v10, s[84:87], s54 offen offset:1024 lds\n\t"
      "buffer_load_dwordx4 v11, s[84:87], s54 offen offset:2048 lds\n\t"
      "s_add_u32 m0, s17, 89600\n\t"
      "s_mov_b32 s54, 36864\n\t"
      "buffer_load_dwordx4 v9, s[84:87], s54 offen lds\n\t"
      "buffer_load_dwordx4 v10, s[84:87], s54 offen offset:1024 lds\n\t"
      "buffer_load_dwordx4 v11, s[84:87], s54 offen offset:2048 lds\n\t"
      "s_add_u32 m0, s17, 111104\n\t"
      "s_mov_b32 s54, 49152\n\t"
      "buffer_load_dwordx4 v9, s[84:87], s54 offen lds\n\t"
      "buffer_load_dwordx4 v10, s[84:87], s54 offen offset:1024 lds\n\t"
      "buffer_load_dwordx4 v11, s[84:87], s54 offen offset:2048 lds\n\t"
      "s_add_u32 m0, s17, 114176\n\t"
      "s_mov_b32 s54, 61440\n\t"
      "buffer_load_dwordx4 v9, s[84:87], s54 offen lds\n\t"
      "buffer_load_dwordx4 v10, s[84:87], s54 offen offset:1024 lds\n\t"
      "buffer_load_dwordx4 v11, s[84:87], s54 offen offset:2048 lds\n\t"
      "s_waitcnt lgkmcnt(0)\n\t"
      "s_waitcnt vmcnt(18)\n\t"
      "s_barrier\n\t"
      "s_waitcnt vmcnt(12)\n\t"
      "ds_read_b128 a[180:183], v14 offset:0\n\t"
      "ds_read_b64_tr_b16 v[64:65], v12 offset:0\n\t"
      "ds_read_b64_tr_b16 v[66:67], v12 offset:768\n\t"
      "ds_read_b64_tr_b16 v[68:69], v13 offset:0\n\t"
      "ds_read_b64_tr_b16 v[70:71], v13 offset:768\n\t"
      "ds_read_b64_tr_b16 v[72:73], v12 offset:64\n\t"
      "ds_read_b64_tr_b16 v[74:75], v12 offset:832\n\t"
      "ds_read_b64_tr_b16 v[76:77], v13 offset:64\n\t"
      "ds_read_b64_tr_b16 v[78:79], v13 offset:832\n\t"
      "ds_read_b64_tr_b16 a[60:61], v12 offset:128\n\t"
      "ds_read_b64_tr_b16 a[62:63], v12 offset:896\n\t"
      "ds_read_b64_tr_b16 a[64:65], v13 offset:128\n\t"
      "ds_read_b64_tr_b16 a[66:67], v13 offset:896\n\t"
      "s_add_u32 m0, s17, 135680\n\t"
      "s_mov_b32 s54, 73728\n\t"
      "buffer_load_dwordx4 v9, s[84:87], s54 offen lds\n\t"
      "buffer_load_dwordx4 v10, s[84:87], s54 offen offset:1024 lds\n\t"
      "buffer_load_dwordx4 v11, s[84:87], s54 offen offset:2048 lds\n\t"
      "s_add_u32 m0, s17, 138752\n\t"
      "s_mov_b32 s54, 86016\n\t"
      "buffer_load_dwordx4 v9, s[84:87], s54 offen lds\n\t"
      "buffer_load_dwordx4 v10, s[84:87], s54 offen offset:1024 lds\n\t"
      "buffer_load_dwordx4 v11, s[84:87], s54 offen offset:2048 lds\n\t"
      "s_waitcnt vmcnt(12)\n\t"
      "s_waitcnt lgkmcnt(0)\n\t"
      "; step\n\t"
      "v_mfma_f32_4x4x4_16b_bf16 v[40:43], a[180:181], v[64:65], 0\n\t"
      "ds_read_b128 a[184:187], v14 offset:256\n\t"
      "v_mfma_f32_4x4x4_16b_bf16 v[44:47], a[182:183], v[66:67], 0\n\t"
      "ds_read_b64_tr_b16 v[80:81], v60 offset:0\n\t"
      "v_mfma_f32_4x4x4_16b_bf16 v[48:51], a[180:181], v[68:69], 0\n\t"
      "ds_read_b64_tr_b16 v[82:83], v60 offset:768\n\t"
      "v_mfma_f32_4x4x4_16b_bf16 v[52:55], a[182:183], v[70:71], 0\n\t"
      "ds_read_b64_tr_b16 v[84:85], v61 offset:0\n\t"
      "ds_read_b64_tr_b16 v[86:87], v61 offset:768\n\t"
      "s_add_u32 m0, s17, 0\n\t"
      "s_mov_b32 s54, 98304\n\t"
      "buffer_load_dwordx4 v9, s[84:87], s54 offen lds\n\t"
      "buffer_load_dwordx4 v10, s[84:87], s54 offen offset:1024 lds\n\t"
      "v_cvt_pk_bf16_f32 v64, v40, v41\n\t"
      "v_cvt_pk_bf16_f32 v65, v42, v43\n\t"
      "v_cvt_pk_bf16_f32 v66, v44, v45\n\t"
      "v_cvt_pk_bf16_f32 v67, v46, v47\n\t"
      "v_cvt_pk_bf16_f32 v68, v48, v49\n\t"
      "v_cvt_pk_bf16_f32 v69, v50, v51\n\t"
      "v_cvt_pk_bf16_f32 v70, v52, v53\n\t"
      "v_cvt_pk_bf16_f32 v71, v54, v55\n\t"
      "v_mfma_f32_4x4x4_16b_bf16 v[40:43], a[180:181], v[72:73], 0\n\t"
      "ds_read_b64_tr_b16 v[88:89], v60 offset:64\n\t"
      "v_mfma_f32_4x4x4_16b_bf16 v[44:47], a[182:183], v[74:75], 0\n\t"
      "ds_read_b64_tr_b16 v[90:91], v60 offset:832\n\t"
      "v_mfma_f32_4x4x4_16b_bf16 v[48:51], a[180:181], v[76:77], 0\n\t"
      "ds_read_b64_tr_b16 v[92:93], v61 offset:64\n\t"
      "v_mfma_f32_4x4x4_16b_bf16 v[52:55], a[182:183], v[78:79], 0\n\t"
      "ds_read_b64_tr_b16 v[94:95], v61 offset:832\n\t"
      "buffer_load_dwordx4 v11, s[84:87], s54 offen offset:2048 lds\n\t"
      "s_add_u32 m0, s17, 3072\n\t"
      "s_mov_b32 s54, 110592\n\t"
      "buffer_load_dwordx4 v9, s[84:87], s54 offen lds\n\t"
      "v_cvt_pk_bf16_f32 v72, v40, v41\n\t"
      "v_cvt_pk_bf16_f32 v73, v42, v43\n\t"
      "v_cvt_pk_bf16_f32 v74, v44, v45\n\t"
      "v_cvt_pk_bf16_f32 v75, v46, v47\n\t"
      "v_cvt_pk_bf16_f32 v76, v48, v49\n\t"
      "v_cvt_pk_bf16_f32 v77, v50, v51\n\t"
      "v_cvt_pk_bf16_f32 v78, v52, v53\n\t"
      "v_cvt_pk_bf16_f32 v79, v54, v55\n\t"
      "ds_read_b128 a[224:227], v56 offset:25088\n\t"
      "v_mfma_f32_4x4x4_16b_bf16 v[40:43], a[180:181], a[60:61], 0\n\t"
      "ds_read_b64_tr_b16 a[68:69], v60 offset:128\n\t"
      "v_mfma_f32_4x4x4_16b_bf16 v[44:47], a[182:183], a[62:63], 0\n\t"
      "ds_read_b64_tr_b16 a[70:71], v60 offset:896\n\t"
      "v_mfma_f32_4x4x4_16b_bf16 v[48:51], a[180:181], a[64:65], 0\n\t"
      "ds_read_b64_tr_b16 a[72:73], v61 offset:128\n\t"
      "v_mfma_f32_4x4x4_16b_bf16 v[52:55], a[182:183], a[66:67], 0\n\t"
      "ds_read_b64_tr_b16 a[74:75], v61 offset:896\n\t"
      "buffer_load_dwordx4 v10, s[84:87], s54 offen offset:1024 lds\n\t"
      "buffer_load_dwordx4 v11, s[84:87], s54 offen offset:2048 lds\n\t"
      "ds_read_b128 a[228:231], v56 offset:37376\n\t"
      "v_cvt_pk_bf16_f32 v40, v40, v41\n\t"
      "v_cvt_pk_bf16_f32 v41, v42, v43\n\t"
      "v_cvt_pk_bf16_f32 v44, v44, v45\n\t"
      "v_cvt_pk_bf16_f32 v45, v46, v47\n\t"
      "v_cvt_pk_bf16_f32 v48, v48, v49\n\t"
      "v_cvt_pk_bf16_f32 v49, v50, v51\n\t"
      "v_cvt_pk_bf16_f32 v52, v52, v53\n\t"
      "v_cvt_pk_bf16_f32 v53, v54, v55\n\t"
      "v_accvgpr_write_b32 a60, v40\n\t"
      "v_accvgpr_write_b32 a61, v41\n\t"
      "v_accvgpr_write_b32 a62, v44\n\t"
      "v_accvgpr_write_b32 a63, v45\n\t"
      "v_accvgpr_write_b32 a64, v48\n\t"
      "v_accvgpr_write_b32 a65, v49\n\t"
      "v_accvgpr_write_b32 a66, v52\n\t"
      "v_accvgpr_write_b32 a67, v53\n\t"
      "s_waitcnt vmcnt(12)\n\t"
      "s_waitcnt lgkmcnt(1)\n\t"
      "; step\n\t"
      "s_memtime s[64:65]\n\t"
      "s_memrealtime s[66:67]\n\t"
      "v_mfma_f32_4x4x4_16b_bf16 v[40:43], a[184:185], v[80:81], 0\n\t"
      "ds_read_b128 a[180:183], v14 offset:512\n\t"
      "v_mfma_f32_4x4x4_16b_bf16 v[44:47], a[186:187], v[82:83], 0\n\t"
      "ds_read_b64_tr_b16 v[96:97], v60 offset:24576\n\t"
      "v_mfma_f32_4x4x4_16b_bf16 v[48:51], a[184:185], v[84:85], 0\n\t"
      "ds_read_b64_tr_b16 v[98:99], v60 offset:25344\n\t"
      "v_mfma_f32_4x4x4_16b_bf16 v[52:55], a[186:187], v[86:87], 0\n\t"
      "ds_read_b64_tr_b16 v[100:101], v61 offset:24576\n\t"
      "ds_read_b64_tr_b16 v[102:103], v61 offset:25344\n\t"
      "s_add_u32 m0, s17, 86528\n\t"
      "s_mov_b32 s54, 122880\n\t"
      "buffer_load_dwordx4 v9, s[84:87], s54 offen lds\n\t"
      "buffer_load_dwordx4 v10, s[84:87], s54 offen offset:1024 lds\n\t"
      "v_mfma_f32_16x16x32_bf16 v[16:19], v[64:67], a[224:227], 0\n\t"
      "v_cvt_pk_bf16_f32 v80, v40, v41\n\t"
      "v_cvt_pk_bf16_f32 v81, v42, v43\n\t"
      "v_mfma_f32_16x16x32_bf16 v[20:23], v[68:71], a[224:227], 0\n\t"
      "v_cvt_pk_bf16_f32 v82, v44, v45\n\t"
      "v_cvt_pk_bf16_f32 v83, v46, v47\n\t"
      "v_mfma_f32_16x16x32_bf16 v[24:27], v[72:75], a[224:227], 0\n\t"
      "v_cvt_pk_bf16_f32 v84, v48, v49\n\t"
      "v_cvt_pk_bf16_f32 v85, v50, v51\n\t"
      "v_mfma_f32_16x16x32_bf16 v[28:31], v[76:79], a[224:227], 0\n\t"
      "v_cvt_pk_bf16_f32 v86, v52, v53\n\t"
      "v_cvt_pk_bf16_f32 v87, v54, v55\n\t"
      "v_mfma_f32_4x4x4_16b_bf16 v[40:43], a[184:185], v[88:89], 0\n\t"
      "ds_read_b64_tr_b16 v[104:105], v60 offset:24640\n\t"
      "v_mfma_f32_4x4x4_16b_bf16 v[44:47], a[186:187], v[90:91], 0\n\t"
      "ds_read_b64_tr_b16 v[106:107], v60 offset:25408\n\t"
      "v_mfma_f32_4x4x4_16b_bf16 v[48:51], a[184:185], v[92:93], 0\n\t"
      "ds_read_b64_tr_b16 v[108:109], v61 offset:24640\n\t"
      "v_mfma_f32_4x4x4_16b_bf16 v[52:55], a[186:187], v[94:95], 0\n\t"
      "ds_read_b64_tr_b16 v[110:111], v61 offset:25408\n\t"
      "buffer_load_dwordx4 v11, s[84:87], s54 offen offset:2048 lds\n\t"
      "s_add_u32 m0, s17, 89600\n\t"
      "s_mov_b32 s54, 135168\n\t"
      "buffer_load_dwordx4 v9, s[84:87], s54 offen lds\n\t"
      "v_mfma_f32_16x16x32_bf16 v[32:35], a[60:63], a[224:227], 0\n\t"
      "v_cvt_pk_bf16_f32 v88, v40, v41\n\t"
      "v_cvt_pk_bf16_f32 v89, v42, v43\n\t"
      "v_mfma_f32_16x16x32_bf16 v[36:39], a[64:67], a[224:227], 0\n\t"
      "v_cvt_pk_bf16_f32 v90, v44, v45\n\t"
      "v_cvt_pk_bf16_f32 v91, v46, v47\n\t"
      "s_waitcnt lgkmcnt(9)\n\t"
      "v_mfma_f32_16x16x32_bf16 a[156:159], v[64:67], a[228:231], 0\n\t"
      "v_cvt_pk_bf16_f32 v92, v48, v49\n\t"
      "v_cvt_pk_bf16_f32 v93, v50, v51\n\t"
      "v_mfma_f32_16x16x32_bf16 a[160:163], v[68:71], a[228:231], 0\n\t"
      "v_cvt_pk_bf16_f32 v94, v52, v53\n\t"
      "v_cvt_pk_bf16_f32 v95, v54, v55\n\t"
      "ds_read_b128 a[232:235], v57 offset:25088\n\t"
      "v_mfma_f32_4x4x4_16b_bf16 v[40:43], a[184:185], a[68:69], 0\n\t"
      "ds_read_b64_tr_b16 a[76:77], v60 offset:24704\n\t"
      "v_mfma_f32_4x4x4_16b_bf16 v[44:47], a[186:187], a[70:71], 0\n\t"
      "ds_read_b64_tr_b16 a[78:79], v60 offset:25472\n\t"
      "v_mfma_f32_4x4x4_16b_bf16 v[48:51], a[184:185], a[72:73], 0\n\t"
      "ds_read_b64_tr_b16 a[80:81], v61 offset:24704\n\t"
      "v_mfma_f32_4x4x4_16b_bf16 v[52:55], a[186:187], a[74:75], 0\n\t"
      "ds_read_b64_tr_b16 a[82:83], v61 offset:25472\n\t"
      "buffer_load_dwordx4 v10, s[84:87], s54 offen offset:1024 lds\n\t"
      "buffer_load_dwordx4 v11, s[84:87], s54 offen offset:2048 lds\n\t"
      "ds_read_b128 a[236:239], v57 offset:37376\n\t"
      "v_mfma_f32_16x16x32_bf16 a[164:167], v[72:75], a[228:231], 0\n\t"
      "v_cvt_pk_bf16_f32 v40, v40, v41\n\t"
      "v_cvt_pk_bf16_f32 v41, v42, v43\n\t"
      "v_mfma_f32_16x16x32_bf16 a[168:171], v[76:79], a[228:231], 0\n\t"
      "v_cvt_pk_bf16_f32 v44, v44, v45\n\t"
      "v_cvt_pk_bf16_f32 v45, v46, v47\n\t"
      "v_mfma_f32_16x16x32_bf16 a[172:175], a[60:63], a[228:231], 0\n\t"
      "v_cvt_pk_bf16_f32 v48, v48, v49\n\t"
      "v_cvt_pk_bf16_f32 v49, v50, v51\n\t"
      "v_mfma_f32_16x16x32_bf16 a[176:179], a[64:67], a[228:231], 0\n\t"
      "v_cvt_pk_bf16_f32 v52, v52, v53\n\t"
      "v_cvt_pk_bf16_f32 v53, v54, v55\n\t"
      "v_accvgpr_write_b32 a68, v40\n\t"
      "v_accvgpr_write_b32 a69, v41\n\t"
      "v_accvgpr_write_b32 a70, v44\n\t"
      "v_accvgpr_write_b32 a71, v45\n\t"
      "v_accvgpr_write_b32 a72, v48\n\t"
      "v_accvgpr_write_b32 a73, v49\n\t"
      "v_accvgpr_write_b32 a74, v52\n\t"
      "v_accvgpr_write_b32 a75, v53\n\t"
      "s_waitcnt vmcnt(12)\n\t"
      "s_waitcnt lgkmcnt(1)\n\t"
      "; step\n\t"
      "v_mfma_f32_4x4x4_16b_bf16 v[40:43], a[180:181], v[96:97], 0\n\t"
      "ds_read_b128 a[184:187], v14 offset:768\n\t"
      "v_mfma_f32_4x4x4_16b_bf16 v[44:47], a[182:183], v[98:99], 0\n\t"
      "ds_read_b64_tr_b16 v[112:113], v60 offset:49152\n\t"
      "v_mfma_f32_4x4x4_16b_bf16 v[48:51], a[180:181], v[100:101], 0\n\t"
      "ds_read_b64_tr_b16 v[114:115], v60 offset:49920\n\t"
      "v_mfma_f32_4x4x4_16b_bf16 v[52:55], a[182:183], v[102:103], 0\n\t"
      "ds_read_b64_tr_b16 v[116:117], v61 offset:49152\n\t"
      "ds_read_b64_tr_b16 v[118:119], v61 offset:49920\n\t"
      "s_add_u32 m0, s17, 111104\n\t"
      "s_mov_b32 s54, 147456\n\t"
      "buffer_load_dwordx4 v9, s[84:87], s54 offen lds\n\t"
      "buffer_load_dwordx4 v10, s[84:87], s54 offen offset:1024 lds\n\t"
      "v_mfma_f32_16x16x32_bf16 v[16:19], v[80:83], a[232:235], v[16:19]\n\t"
      "v_cvt_pk_bf16_f32 v96, v40, v41\n\t"
      "v_cvt_pk_bf16_f32 v97, v42, v43\n\t"
      "v_mfma_f32_16x16x32_bf16 v[20:23], v[84:87], a[232:235], v[20:23]\n\t"
      "v_cvt_pk_bf16_f32 v98, v44, v45\n\t"
      "v_cvt_pk_bf16_f32 v99, v46, v47\n\t"
      "v_mfma_f32_16x16x32_bf16 v[24:27], v[88:91], a[232:235], v[24:27]\n\t"
      "v_cvt_pk_bf16_f32 v100, v48, v49\n\t"
      "v_cvt_pk_bf16_f32 v101, v50, v51\n\t"
      "v_mfma_f32_16x16x32_bf16 v[28:31], v[92:95], a[232:235], v[28:31]\n\t"
      "v_cvt_pk_bf16_f32 v102, v52, v53\n\t"
      "v_cvt_pk_bf16_f32 v103, v54, v55\n\t"
      "v_mfma_f32_4x4x4_16b_bf16 v[40:43], a[180:181], v[104:105], 0\n\t"
      "ds_read_b64_tr_b16 v[120:121], v60 offset:49216\n\t"
      "v_mfma_f32_4x4x4_16b_bf16 v[44:47], a[182:183], v[106:107], 0\n\t"
      "ds_read_b64_tr_b16 v[122:123], v60 offset:49984\n\t"
      "v_mfma_f32_4x4x4_16b_bf16 v[48:51], a[180:181], v[108:109], 0\n\t"
      "ds_read_b64_tr_b16 v[124:125], v61 offset:49216\n\t"
      "v_mfma_f32_4x4x4_16b_bf16 v[52:55], a[182:183], v[110:111], 0\n\t"
      "ds_read_b64_tr_b16 v[126:127], v61 offset:49984\n\t"
      "buffer_load_dwordx4 v11, s[84:87], s54 offen offset:2048 lds\n\t"
      "s_add_u32 m0, s17, 114176\n\t"
      "s_mov_b32 s54, 159744\n\t"
      "buffer_load_dwordx4 v9, s[84:87], s54 offen lds\n\t"
      "v_mfma_f32_16x16x32_bf16 v[32:35], a[68:71], a[232:235], v[32:35]\n\t"
      "v_cvt_pk_bf16_f32 v104, v40, v41\n\t"
      "v_cvt_pk_bf16_f32 v105, v42, v43\n\t"
      "v_mfma_f32_16x16x32_bf16 v[36:39], a[72:75], a[232:235], v[36:39]\n\t"
      "v_cvt_pk_bf16_f32 v106, v44, v45\n\t"
      "v_cvt_pk_bf16_f32 v107, v46, v47\n\t"
      "s_waitcnt lgkmcnt(9)\n\t"
      "v_mfma_f32_16x16x32_bf16 a[156:159], v[80:83], a[236:239], a[156:159]\n\t"
      "v_cvt_pk_bf16_f32 v108, v48, v49\n\t"
      "v_cvt_pk_bf16_f32 v109, v50, v51\n\t"
      "v_mfma_f32_16x16x32_bf16 a[160:163], v[84:87], a[236:239], a[160:163]\n\t"
      "v_cvt_pk_bf16_f32 v110, v52, v53\n\t"
      "v_cvt_pk_bf16_f32 v111, v54, v55\n\t"
      "ds_read_b128 a[240:243], v58 offset:25088\n\t"
      "v_mfma_f32_4x4x4_16b_bf16 v[40:43], a[180:181], a[76:77], 0\n\t"
      "ds_read_b64_tr_b16 a[84:85], v60 offset:49280\n\t"
      "v_mfma_f32_4x4x4_16b_bf16 v[44:47], a[182:183], a[78:79], 0\n\t"
      "ds_read_b64_tr_b16 a[86:87], v60 offset:50048\n\t"
      "v_mfma_f32_4x4x4_16b_bf16 v[48:51], a[180:181], a[80:81], 0\n\t"
      "ds_read_b64_tr_b16 a[88:89], v61 offset:49280\n\t"
      "v_mfma_f32_4x4x4_16b_bf16 v[52:55], a[182:183], a[82:83], 0\n\t"
      "ds_read_b64_tr_b16 a[90:91], v61 offset:50048\n\t"
      "buffer_load_dwordx4 v10, s[84:87], s54 offen offset:1024 lds\n\t"
      "buffer_load_dwordx4 v11, s[84:87], s54 offen offset:2048 lds\n\t"
      "ds_read_b128 a[244:247], v58 offset:37376\n\t"
      "v_mfma_f32_16x16x32_bf16 a[164:167], v[88:91], a[236:239], a[164:167]\n\t"
      "v_cvt_pk_bf16_f32 v40, v40, v41\n\t"
      "v_cvt_pk_bf16_f32 v41, v42, v43\n\t"
      "v_mfma_f32_16x16x32_bf16 a[168:171], v[92:95], a[236:239], a[168:171]\n\t"
      "v_cvt_pk_bf16_f32 v44, v44, v45\n\t"
      "v_cvt_pk_bf16_f32 v45, v46, v47\n\t"
      "v_mfma_f32_16x16x32_bf16 a[172:175], a[68:71], a[236:239], a[172:175]\n\t"
      "v_cvt_pk_bf16_f32 v48, v48, v49\n\t"
      "v_cvt_pk_bf16_f32 v49, v50, v51\n\t"
      "v_mfma_f32_16x16x32_bf16 a[176:179], a[72:75], a[236:239], a[176:179]\n\t"
      "v_cvt_pk_bf16_f32 v52, v52, v53\n\t"
      "v_cvt_pk_bf16_f32 v53, v54, v55\n\t"
      "v_accvgpr_write_b32 a76, v40\n\t"
      "v_accvgpr_write_b32 a77, v41\n\t"
      "v_accvgpr_write_b32 a78, v44\n\t"
      "v_accvgpr_write_b32 a79, v45\n\t"
      "v_accvgpr_write_b32 a80, v48\n\t"
      "v_accvgpr_write_b32 a81, v49\n\t"
      "v_accvgpr_write_b32 a82, v52\n\t"
      "v_accvgpr_write_b32 a83, v53\n\t"
      "s_waitcnt vmcnt(12)\n\t"
      "s_waitcnt lgkmcnt(1)\n\t"
      "; step\n\t"
      "v_mfma_f32_4x4x4_16b_bf16 v[40:43], a[184:185], v[112:113], 0\n\t"
      "ds_read_b128 a[180:183], v14 offset:1024\n\t"
      "v_mfma_f32_4x4x4_16b_bf16 v[44:47], a[186:187], v[114:115], 0\n\t"
      "ds_read_b64_tr_b16 v[128:129], v12 offset:0\n\t"
      "v_mfma_f32_4x4x4_16b_bf16 v[48:51], a[184:185], v[116:117], 0\n\t"
      "ds_read_b64_tr_b16 v[130:131], v12 offset:768\n\t"
      "v_mfma_f32_4x4x4_16b_bf16 v[52:55], a[186:187], v[118:119], 0\n\t"
      "ds_read_b64_tr_b16 v[132:133], v13 offset:0\n\t"
      "ds_read_b64_tr_b16 v[134:135], v13 offset:768\n\t"
      "s_add_u32 m0, s17, 135680\n\t"
      "s_mov_b32 s54, 172032\n\t"
      "buffer_load_dwordx4 v9, s[84:87], s54 offen lds\n\t"
      "buffer_load_dwordx4 v10, s[84:87], s54 offen offset:1024 lds\n\t"
      "v_mfma_f32_16x16x32_bf16 v[16:19], v[96:99], a[240:243], v[16:19]\n\t"
      "v_cvt_pk_bf16_f32 v112, v40, v41\n\t"
      "v_cvt_pk_bf16_f32 v113, v42, v43\n\t"
      "v_mfma_f32_16x16x32_bf16 v[20:23], v[100:103], a[240:243], v[20:23]\n\t"
      "v_cvt_pk_bf16_f32 v114, v44, v45\n\t"
      "v_cvt_pk_bf16_f32 v115, v46, v47\n\t"
      "v_mfma_f32_16x16x32_bf16 v[24:27], v[104:107], a[240:243], v[24:27]\n\t"
      "v_cvt_pk_bf16_f32 v116, v48, v49\n\t"
      "v_cvt_pk_bf16_f32 v117, v50, v51\n\t"
      "v_mfma_f32_16x16x32_bf16 v[28:31], v[108:111], a[240:243], v[28:31]\n\t"
      "v_cvt_pk_bf16_f32 v118, v52, v53\n\t"
      "v_cvt_pk_bf16_f32 v119, v54, v55\n\t"
      "v_mfma_f32_4x4x4_16b_bf16 v[40:43], a[184:185], v[120:121], 0\n\t"
      "ds_read_b64_tr_b16 v[136:137], v12 offset:64\n\t"
      "v_mfma_f32_4x4x4_16b_bf16 v[44:47], a[186:187], v[122:123], 0\n\t"
      "ds_read_b64_tr_b16 v[138:139], v12 offset:832\n\t"
      "v_mfma_f32_4x4x4_16b_bf16 v[48:51], a[184:185], v[124:125], 0\n\t"
      "ds_read_b64_tr_b16 v[140:141], v13 offset:64\n\t"
      "v_mfma_f32_4x4x4_16b_bf16 v[52:55], a[186:187], v[126:127], 0\n\t"
      "ds_read_b64_tr_b16 v[142:143], v13 offset:832\n\t"
      "buffer_load_dwordx4 v11, s[84:87], s54 offen offset:2048 lds\n\t"
      "s_add_u32 m0, s17, 138752\n\t"
      "s_mov_b32 s54, 184320\n\t"
      "buffer_load_dwordx4 v9, s[84:87], s54 offen lds\n\t"
      "v_mfma_f32_16x16x32_bf16 v[32:35], a[76:79], a[240:243], v[32:35]\n\t"
      "v_cvt_pk_bf16_f32 v120, v40, v41\n\t"
      "v_cvt_pk_bf16_f32 v121, v42, v43\n\t"
      "v_mfma_f32_16x16x32_bf16 v[36:39], a[80:83], a[240:243], v[36:39]\n\t"
      "v_cvt_pk_bf16_f32 v122, v44, v45\n\t"
      "v_cvt_pk_bf16_f32 v123, v46, v47\n\t"
      "s_waitcnt lgkmcnt(9)\n\t"
      "v_mfma_f32_16x16x32_bf16 a[156:159], v[96:99], a[244:247], a[156:159]\n\t"
      "v_cvt_pk_bf16_f32 v124, v48, v49\n\t"
      "v_cvt_pk_bf16_f32 v125, v50, v51\n\t"
      "v_mfma_f32_16x16x32_bf16 a[160:163], v[100:103], a[244:247], a[160:163]\n\t"
      "v_cvt_pk_bf16_f32 v126, v52, v53\n\t"
      "v_cvt_pk_bf16_f32 v127, v54, v55\n\t"
      "ds_read_b128 a[224:227], v59 offset:25088\n\t"
      "v_mfma_f32_4x4x4_16b_bf16 v[40:43], a[184:185], a[84:85], 0\n\t"
      "ds_read_b64_tr_b16 a[92:93], v12 offset:128\n\t"
      "v_mfma_f32_4x4x4_16b_bf16 v[44:47], a[186:187], a[86:87], 0\n\t"
      "ds_read_b64_tr_b16 a[94:95], v12 offset:896\n\t"
      "v_mfma_f32_4x4x4_16b_bf16 v[48:51], a[184:185], a[88:89], 0\n\t"
      "ds_read_b64_tr_b16 a[96:97], v13 offset:128\n\t"
      "v_mfma_f32_4x4x4_16b_bf16 v[52:55], a[186:187], a[90:91], 0\n\t"
      "ds_read_b64_tr_b16 a[98:99], v13 offset:896\n\t"
      "buffer_load_dwordx4 v10, s[84:87], s54 offen offset:1024 lds\n\t"
      "buffer_load_dwordx4 v11, s[84:87], s54 offen offset:2048 lds\n\t"
      "ds_read_b128 a[228:231], v59 offset:37376\n\t"
      "v_mfma_f32_16x16x32_bf16 a[164:167], v[104:107], a[244:247], a[164:167]\n\t"
      "v_cvt_pk_bf16_f32 v40, v40, v41\n\t"
      "v_cvt_pk_bf16_f32 v41, v42, v43\n\t"
      "v_mfma_f32_16x16x32_bf16 a[168:171], v[108:111], a[244:247], a[168:171]\n\t"
      "v_cvt_pk_bf16_f32 v44, v44, v45\n\t"
      "v_cvt_pk_bf16_f32 v45, v46, v47\n\t"
      "v_mfma_f32_16x16x32_bf16 a[172:175], a[76:79], a[244:247], a[172:175]\n\t"
      "v_cvt_pk_bf16_f32 v48, v48, v49\n\t"
      "v_cvt_pk_bf16_f32 v49, v50, v51\n\t"
      "v_mfma_f32_16x16x32_bf16 a[176:179], a[80:83], a[244:247], a[176:179]\n\t"
      "v_cvt_pk_bf16_f32 v52, v52, v53\n\t"
      "v_cvt_pk_bf16_f32 v53, v54, v55\n\t"
      "v_accvgpr_write_b32 a84, v40\n\t"
      "v_accvgpr_write_b32 a85, v41\n\t"
      "v_accvgpr_write_b32 a86, v44\n\t"
      "v_accvgpr_write_b32 a87, v45\n\t"
      "v_accvgpr_write_b32 a88, v48\n\t"
      "v_accvgpr_write_b32 a89, v49\n\t"
      "v_accvgpr_write_b32 a90, v52\n\t"
      "v_accvgpr_write_b32 a91, v53\n\t"
      "s_waitcnt vmcnt(12)\n\t"
      "s_waitcnt lgkmcnt(1)\n\t"
      "; step\n\t"
      "v_mfma_f32_4x4x4_16b_bf16 v[40:43], a[180:181], v[128:129], 0\n\t"
      "ds_read_b128 a[184:187], v14 offset:1280\n\t"
      "v_mfma_f32_4x4x4_16b_bf16 v[44:47], a[182:183], v[130:131], 0\n\t"
      "ds_read_b64_tr_b16 v[144:145], v60 offset:0\n\t"
      "v_mfma_f32_4x4x4_16b_bf16 v[48:51], a[180:181], v[132:133], 0\n\t"
      "ds_read_b64_tr_b16 v[146:147], v60 offset:768\n\t"
      "v_mfma_f32_4x4x4_16b_bf16 v[52:55], a[182:183], v[134:135], 0\n\t"
      "ds_read_b64_tr_b16 v[148:149], v61 offset:0\n\t"
      "ds_read_b64_tr_b16 v[150:151], v61 offset:768\n\t"
      "s_add_u32 m0, s17, 0\n\t"
      "s_mov_b32 s54, 196608\n\t"
      "buffer_load_dwordx4 v9, s[84:87], s54 offen lds\n\t"
      "buffer_load_dwordx4 v10, s[84:87], s54 offen offset:1024 lds\n\t"
      "v_mfma_f32_16x16x32_bf16 v[16:19], v[112:115], a[224:227], v[16:19]\n\t"
      "v_cvt_pk_bf16_f32 v128, v40, v41\n\t"
      "v_cvt_pk_bf16_f32 v129, v42, v43\n\t"
      "v_mfma_f32_16x16x32_bf16 v[20:23], v[116:119], a[224:227], v[20:23]\n\t"
      "v_cvt_pk_bf16_f32 v130, v44, v45\n\t"
      "v_cvt_pk_bf16_f32 v131, v46, v47\n\t"
      "v_mfma_f32_16x16x32_bf16 v[24:27], v[120:123], a[224:227], v[24:27]\n\t"
      "v_cvt_pk_bf16_f32 v132, v48, v49\n\t"
      "v_cvt_pk_bf16_f32 v133, v50, v51\n\t"
      "v_mfma_f32_16x16x32_bf16 v[28:31], v[124:127], a[224:227], v[28:31]\n\t"
      "v_cvt_pk_bf16_f32 v134, v52, v53\n\t"
      "v_cvt_pk_bf16_f32 v135, v54, v55\n\t"
      "v_mfma_f32_4x4x4_16b_bf16 v[40:43], a[180:181], v[136:137], 0\n\t"
      "ds_read_b64_tr_b16 v[152:153], v60 offset:64\n\t"
      "v_mfma_f32_4x4x4_16b_bf16 v[44:47], a[182:183], v[138:139], 0\n\t"
      "ds_read_b64_tr_b16 v[154:155], v60 offset:832\n\t"
      "v_mfma_f32_4x4x4_16b_bf16 v[48:51], a[180:181], v[140:141], 0\n\t"
      "ds_read_b64_tr_b16 v[156:157], v61 offset:64\n\t"
      "v_mfma_f32_4x4x4_16b_bf16 v[52:55], a[182:183], v[142:143], 0\n\t"
      "ds_read_b64_tr_b16 v[158:159], v61 offset:832\n\t"
      "buffer_load_dwordx4 v11, s[84:87], s54 offen offset:2048 lds\n\t"
      "s_add_u32 m0, s17, 3072\n\t"
      "s_mov_b32 s54, 208896\n\t"
      "buffer_load_dwordx4 v9, s[84:87], s54 offen lds\n\t"
      "v_mfma_f32_16x16x32_bf16 v[32:35], a[84:87], a[224:227], v[32:35]\n\t"
      "v_cvt_pk_bf16_f32 v136, v40, v41\n\t"
      "v_cvt_pk_bf16_f32 v137, v42, v43\n\t"
      "v_mfma_f32_16x16x32_bf16 v[36:39], a[88:91], a[224:227], v[36:39]\n\t"
      "v_cvt_pk_bf16_f32 v138, v44, v45\n\t"
      "v_cvt_pk_bf16_f32 v139, v46, v47\n\t"
      "s_waitcnt lgkmcnt(9)\n\t"
      "v_mfma_f32_16x16x32_bf16 a[156:159], v[112:115], a[228:231], a[156:159]\n\t"
      "v_cvt_pk_bf16_f32 v140, v48, v49\n\t"
      "v_cvt_pk_bf16_f32 v141, v50, v51\n\t"
      "v_mfma_f32_16x16x32_bf16 a[160:163], v[116:119], a[228:231], a[160:163]\n\t"
      "v_cvt_pk_bf16_f32 v142, v52, v53\n\t"
      "v_cvt_pk_bf16_f32 v143, v54, v55\n\t"
      "ds_read_b128 a[232:235], v56 offset:25344\n\t"
      "v_mfma_f32_4x4x4_16b_bf16 v[40:43], a[180:181], a[92:93], 0\n\t"
      "ds_read_b64_tr_b16 a[100:101], v60 offset:128\n\t"
      "v_mfma_f32_4x4x4_16b_bf16 v[44:47], a[182:183], a[94:95], 0\n\t"
      "ds_read_b64_tr_b16 a[102:103], v60 offset:896\n\t"
      "v_mfma_f32_4x4x4_16b_bf16 v[48:51], a[180:181], a[96:97], 0\n\t"
      "ds_read_b64_tr_b16 a[104:105], v61 offset:128\n\t"
      "v_mfma_f32_4x4x4_16b_bf16 v[52:55], a[182:183], a[98:99], 0\n\t"
      "ds_read_b64_tr_b16 a[106:107], v61 offset:896\n\t"
      "buffer_load_dwordx4 v10, s[84:87], s54 offen offset:1024 lds\n\t"
      "buffer_load_dwordx4 v11, s[84:87], s54 offen offset:2048 lds\n\t"
      "ds_read_b128 a[236:239], v56 offset:37632\n\t"
      "v_mfma_f32_16x16x32_bf16 a[164:167], v[120:123], a[228:231], a[164:167]\n\t"
      "v_cvt_pk_bf16_f32 v40, v40, v41\n\t"
      "v_cvt_pk_bf16_f32 v41, v42, v43\n\t"
      "v_mfma_f32_16x16x32_bf16 a[168:171], v[124:127], a[228:231], a[168:171]\n\t"
      "v_cvt_pk_bf16_f32 v44, v44, v45\n\t"
      "v_cvt_pk_bf16_f32 v45, v46, v47\n\t"
      "v_mfma_f32_16x16x32_bf16 a[172:175], a[84:87], a[228:231], a[172:175]\n\t"
      "v_cvt_pk_bf16_f32 v48, v48, v49\n\t"
      "v_cvt_pk_bf16_f32 v49, v50, v51\n\t"
      "v_mfma_f32_16x16x32_bf16 a[176:179], a[88:91], a[228:231], a[176:179]\n\t"
      "v_cvt_pk_bf16_f32 v52, v52, v53\n\t"
      "v_cvt_pk_bf16_f32 v53, v54, v55\n\t"
      "v_accvgpr_write_b32 a92, v40\n\t"
      "v_accvgpr_write_b32 a93, v41\n\t"
      "v_accvgpr_write_b32 a94, v44\n\t"
      "v_accvgpr_write_b32 a95, v45\n\t"
      "v_accvgpr_write_b32 a96, v48\n\t"
      "v_accvgpr_write_b32 a97, v49\n\t"
      "v_accvgpr_write_b32 a98, v52\n\t"
      "v_accvgpr_write_b32 a99, v53\n\t"
      "s_waitcnt vmcnt(12)\n\t"
      "s_waitcnt lgkmcnt(1)\n\t"
      "; step\n\t"
      "v_mfma_f32_4x4x4_16b_bf16 v[40:43], a[184:185], v[144:145], 0\n\t"
      "ds_read_b128 a[180:183], v14 offset:1536\n\t"
      "v_mfma_f32_4x4x4_16b_bf16 v[44:47], a[186:187], v[146:147], 0\n\t"
      "ds_read_b64_tr_b16 v[160:161], v60 offset:24576\n\t"
      "v_mfma_f32_4x4x4_16b_bf16 v[48:51], a[184:185], v[148:149], 0\n\t"
      "ds_read_b64_tr_b16 v[162:163], v60 offset:25344\n\t"
      "v_mfma_f32_4x4x4_16b_bf16 v[52:55], a[186:187], v[150:151], 0\n\t"
      "ds_read_b64_tr_b16 v[164:165], v61 offset:24576\n\t"
      "ds_read_b64_tr_b16 v[166:167], v61 offset:25344\n\t"
      "s_add_u32 m0, s17, 86528\n\t"
      "s_mov_b32 s54, 221184\n\t"
      "buffer_load_dwordx4 v9, s[84:87], s54 offen lds\n\t"
      "buffer_load_dwordx4 v10, s[84:87], s54 offen offset:1024 lds\n\t"
      "v_mfma_f32_16x16x32_bf16 v[16:19], v[128:131], a[232:235], v[16:19]\n\t"
      "v_cvt_pk_bf16_f32 v144, v40, v41\n\t"
      "v_cvt_pk_bf16_f32 v145, v42, v43\n\t"
      "v_mfma_f32_16x16x32_bf16 v[20:23], v[132:135], a[232:235], v[20:23]\n\t"
      "v_cvt_pk_bf16_f32 v146, v44, v45\n\t"
      "v_cvt_pk_bf16_f32 v147, v46, v47\n\t"
      "v_mfma_f32_16x16x32_bf16 v[24:27], v[136:139], a[232:235], v[24:27]\n\t"
      "v_cvt_pk_bf16_f32 v148, v48, v49\n\t"
      "v_cvt_pk_bf16_f32 v149, v50, v51\n\t"
      "v_mfma_f32_16x16x32_bf16 v[28:31], v[140:143], a[232:235], v[28:31]\n\t"
      "v_cvt_pk_bf16_f32 v150, v52, v53\n\t"
      "v_cvt_pk_bf16_f32 v151, v54, v55\n\t"
      "v_mfma_f32_4x4x4_16b_bf16 v[40:43], a[184:185], v[152:153], 0\n\t"
      "ds_read_b64_tr_b16 v[168:169], v60 offset:24640\n\t"
      "v_mfma_f32_4x4x4_16b_bf16 v[44:47], a[186:187], v[154:155], 0\n\t"
      "ds_read_b64_tr_b16 v[170:171], v60 offset:25408\n\t"
      "v_mfma_f32_4x4x4_16b_bf16 v[48:51], a[184:185], v[156:157], 0\n\t"
      "ds_read_b64_tr_b16 v[172:173], v61 offset:24640\n\t"
      "v_mfma_f32_4x4x4_16b_bf16 v[52:55], a[186:187], v[158:159], 0\n\t"
      "ds_read_b64_tr_b16 v[174:175], v61 offset:25408\n\t"
      "buffer_load_dwordx4 v11, s[84:87], s54 offen offset:2048 lds\n\t"
      "s_add_u32 m0, s17, 89600\n\t"
      "s_mov_b32 s54, 233472\n\t"
      "buffer_load_dwordx4 v9, s[84:87], s54 offen lds\n\t"
      "v_mfma_f32_16x16x32_bf16 v[32:35], a[92:95], a[232:235], v[32:35]\n\t"
      "v_cvt_pk_bf16_f32 v152, v40, v41\n\t"
      "v_cvt_pk_bf16_f32 v153, v42, v43\n\t"
      "v_mfma_f32_16x16x32_bf16 v[36:39], a[96:99], a[232:235], v[36:39]\n\t"
      "v_cvt_pk_bf16_f32 v154, v44, v45\n\t"
      "v_cvt_pk_bf16_f32 v155, v46, v47\n\t"
      "s_waitcnt lgkmcnt(9)\n\t"
      "v_mfma_f32_16x16x32_bf16 a[156:159], v[128:131], a[236:239], a[156:159]\n\t"
      "v_cvt_pk_bf16_f32 v156, v48, v49\n\t"
      "v_cvt_pk_bf16_f32 v157, v50, v51\n\t"
      "v_mfma_f32_16x16x32_bf16 a[160:163], v[132:135], a[236:239], a[160:163]\n\t"
      "v_cvt_pk_bf16_f32 v158, v52, v53\n\t"
      "v_cvt_pk_bf16_f32 v159, v54, v55\n\t"
      "ds_read_b128 a[240:243], v57 offset:25344\n\t"
      "v_mfma_f32_4x4x4_16b_bf16 v[40:43], a[184:185], a[100:101], 0\n\t"
      "ds_read_b64_tr_b16 a[108:109], v60 offset:24704\n\t"
      "v_mfma_f32_4x4x4_16b_bf16 v[44:47], a[186:187], a[102:103], 0\n\t"
      "ds_read_b64_tr_b16 a[110:111], v60 offset:25472\n\t"
      "v_mfma_f32_4x4x4_16b_bf16 v[48:51], a[184:185], a[104:105], 0\n\t"
      "ds_read_b64_tr_b16 a[112:113], v61 offset:24704\n\t"
      "v_mfma_f32_4x4x4_16b_bf16 v[52:55], a[186:187], a[106:107], 0\n\t"
      "ds_read_b64_tr_b16 a[114:115], v61 offset:25472\n\t"
      "buffer_load_dwordx4 v10, s[84:87], s54 offen offset:1024 lds\n\t"
      "buffer_load_dwordx4 v11, s[84:87], s54 offen offset:2048 lds\n\t"
      "ds_read_b128 a[244:247], v57 offset:37632\n\t"
      "v_mfma_f32_16x16x32_bf16 a[164:167], v[136:139], a[236:239], a[164:167]\n\t"
      "v_cvt_pk_bf16_f32 v40, v40, v41\n\t"
      "v_cvt_pk_bf16_f32 v41, v42, v43\n\t"
      "v_mfma_f32_16x16x32_bf16 a[168:171], v[140:143], a[236:239], a[168:171]\n\t"
      "v_cvt_pk_bf16_f32 v44, v44, v45\n\t"
      "v_cvt_pk_bf16_f32 v45, v46, v47\n\t"
      "v_mfma_f32_16x16x32_bf16 a[172:175], a[92:95], a[236:239], a[172:175]\n\t"
      "v_cvt_pk_bf16_f32 v48, v48, v49\n\t"
      "v_cvt_pk_bf16_f32 v49, v50, v51\n\t"
      "v_mfma_f32_16x16x32_bf16 a[176:179], a[96:99], a[236:239], a[176:179]\n\t"
      "v_cvt_pk_bf16_f32 v52, v52, v53\n\t"
      "v_cvt_pk_bf16_f32 v53, v54, v55\n\t"
      "v_accvgpr_write_b32 a100, v40\n\t"
      "v_accvgpr_write_b32 a101, v41\n\t"
      "v_accvgpr_write_b32 a102, v44\n\t"
      "v_accvgpr_write_b32 a103, v45\n\t"
      "v_accvgpr_write_b32 a104, v48\n\t"
      "v_accvgpr_write_b32 a105, v49\n\t"
      "v_accvgpr_write_b32 a106, v52\n\t"
      "v_accvgpr_write_b32 a107, v53\n\t"
      "s_waitcnt vmcnt(12)\n\t"
      "s_waitcnt lgkmcnt(1)\n\t"
      "; step\n\t"
      "v_mfma_f32_4x4x4_16b_bf16 v[40:43], a[180:181], v[160:161], 0\n\t"
      "ds_read_b128 a[184:187], v14 offset:1792\n\t"
      "v_mfma_f32_4x4x4_16b_bf16 v[44:47], a[182:183], v[162:163], 0\n\t"
      "ds_read_b64_tr_b16 v[176:177], v60 offset:49152\n\t"
      "v_mfma_f32_4x4x4_16b_bf16 v[48:51], a[180:181], v[164:165], 0\n\t"
      "ds_read_b64_tr_b16 v[178:179], v60 offset:49920\n\t"
      "v_mfma_f32_4x4x4_16b_bf16 v[52:55], a[182:183], v[166:167], 0\n\t"
      "ds_read_b64_tr_b16 v[180:181], v61 offset:49152\n\t"
      "ds_read_b64_tr_b16 v[182:183], v61 offset:49920\n\t"
      "s_add_u32 m0, s17, 111104\n\t"
      "s_mov_b32 s54, 245760\n\t"
      "buffer_load_dwordx4 v9, s[84:87], s54 offen lds\n\t"
      "buffer_load_dwordx4 v10, s[84:87], s54 offen offset:1024 lds\n\t"
      "v_mfma_f32_16x16x32_bf16 v[16:19], v[144:147], a[240:243], v[16:19]\n\t"
      "v_cvt_pk_bf16_f32 v160, v40, v41\n\t"
      "v_cvt_pk_bf16_f32 v161, v42, v43\n\t"
      "v_mfma_f32_16x16x32_bf16 v[20:23], v[148:151], a[240:243], v[20:23]\n\t"
      "v_cvt_pk_bf16_f32 v162, v44, v45\n\t"
      "v_cvt_pk_bf16_f32 v163, v46, v47\n\t"
      "v_mfma_f32_16x16x32_bf16 v[24:27], v[152:155], a[240:243], v[24:27]\n\t"
      "v_cvt_pk_bf16_f32 v164, v48, v49\n\t"
      "v_cvt_pk_bf16_f32 v165, v50, v51\n\t"
      "v_mfma_f32_16x16x32_bf16 v[28:31], v[156:159], a[240:243], v[28:31]\n\t"
      "v_cvt_pk_bf16_f32 v166, v52, v53\n\t"
      "v_cvt_pk_bf16_f32 v167, v54, v55\n\t"
      "v_mfma_f32_4x4x4_16b_bf16 v[40:43], a[180:181], v[168:169], 0\n\t"
      "ds_read_b64_tr_b16 v[184:185], v60 offset:49216\n\t"
      "v_mfma_f32_4x4x4_16b_bf16 v[44:47], a[182:183], v[170:171], 0\n\t"
      "ds_read_b64_tr_b16 v[186:187], v60 offset:49984\n\t"
      "v_mfma_f32_4x4x4_16b_bf16 v[48:51], a[180:181], v[172:173], 0\n\t"
      "ds_read_b64_tr_b16 v[188:189], v61 offset:49216\n\t"
      "v_mfma_f32_4x4x4_16b_bf16 v[52:55], a[182:183], v[174:175], 0\n\t"
      "ds_read_b64_tr_b16 v[190:191], v61 offset:49984\n\t"
      "buffer_load_dwordx4 v11, s[84:87], s54 offen offset:2048 lds\n\t"
      "s_add_u32 m0, s17, 114176\n\t"
      "s_mov_b32 s54, 258048\n\t"
      "buffer_load_dwordx4 v9, s[84:87], s54 offen lds\n\t"
      "v_mfma_f32_16x16x32_bf16 v[32:35], a[100:103], a[240:243], v[32:35]\n\t"
      "v_cvt_pk_bf16_f32 v168, v40, v41\n\t"
      "v_cvt_pk_bf16_f32 v169, v42, v43\n\t"
      "v_mfma_f32_16x16x32_bf16 v[36:39], a[104:107], a[240:243], v[36:39]\n\t"
      "v_cvt_pk_bf16_f32 v170, v44, v45\n\t"
      "v_cvt_pk_bf16_f32 v171, v46, v47\n\t"
      "s_waitcnt lgkmcnt(9)\n\t"
      "v_mfma_f32_16x16x32_bf16 a[156:159], v[144:147], a[244:247], a[156:159]\n\t"
      "v_cvt_pk_bf16_f32 v172, v48, v49\n\t"
      "v_cvt_pk_bf16_f32 v173, v50, v51\n\t"
      "v_mfma_f32_16x16x32_bf16 a[160:163], v[148:151], a[244:247], a[160:163]\n\t"
      "v_cvt_pk_bf16_f32 v174, v52, v53\n\t"
      "v_cvt_pk_bf16_f32 v175, v54, v55\n\t"
      "ds_read_b128 a[224:227], v58 offset:25344\n\t"
      "v_mfma_f32_4x4x4_16b_bf16 v[40:43], a[180:181], a[108:109], 0\n\t"
      "ds_read_b64_tr_b16 a[116:117], v60 offset:49280\n\t"
      "v_mfma_f32_4x4x4_16b_bf16 v[44:47], a[182:183], a[110:111], 0\n\t"
      "ds_read_b64_tr_b16 a[118:119], v60 offset:50048\n\t"
      "v_mfma_f32_4x4x4_16b_bf16 v[48:51], a[180:181], a[112:113], 0\n\t"
      "ds_read_b64_tr_b16 a[120:121], v61 offset:49280\n\t"
      "v_mfma_f32_4x4x4_16b_bf16 v[52:55], a[182:183], a[114:115], 0\n\t"
      "ds_read_b64_tr_b16 a[122:123], v61 offset:50048\n\t"
      "buffer_load_dwordx4 v10, s[84:87], s54 offen offset:1024 lds\n\t"
      "buffer_load_dwordx4 v11, s[84:87], s54 offen offset:2048 lds\n\t"
      "ds_read_b128 a[228:231], v58 offset:37632\n\t"
      "v_mfma_f32_16x16x32_bf16 a[164:167], v[152:155], a[244:247], a[164:167]\n\t"
      "v_cvt_pk_bf16_f32 v40, v40, v41\n\t"
      "v_cvt_pk_bf16_f32 v41, v42, v43\n\t"
      "v_mfma_f32_16x16x32_bf16 a[168:171], v[156:159], a[244:247], a[168:171]\n\t"
      "v_cvt_pk_bf16_f32 v44, v44, v45\n\t"
      "v_cvt_pk_bf16_f32 v45, v46, v47\n\t"
      "v_mfma_f32_16x16x32_bf16 a[172:175], a[100:103], a[244:247], a[172:175]\n\t"
      "v_cvt_pk_bf16_f32 v48, v48, v49\n\t"
      "v_cvt_pk_bf16_f32 v49, v50, v51\n\t"
      "v_mfma_f32_16x16x32_bf16 a[176:179], a[104:107], a[244:247], a[176:179]\n\t"
      "v_cvt_pk_bf16_f32 v52, v52, v53\n\t"
      "v_cvt_pk_bf16_f32 v53, v54, v55\n\t"
      "v_accvgpr_write_b32 a108, v40\n\t"
      "v_accvgpr_write_b32 a109, v41\n\t"
      "v_accvgpr_write_b32 a110, v44\n\t"
      "v_accvgpr_write_b32 a111, v45\n\t"
      "v_accvgpr_write_b32 a112, v48\n\t"
      "v_accvgpr_write_b32 a113, v49\n\t"
      "v_accvgpr_write_b32 a114, v52\n\t"
      "v_accvgpr_write_b32 a115, v53\n\t"
      "s_waitcnt vmcnt(12)\n\t"
      "s_waitcnt lgkmcnt(1)\n\t"
      "; step\n\t"
      "v_mfma_f32_4x4x4_16b_bf16 v[40:43], a[184:185], v[176:177], 0\n\t"
      "ds_read_b128 a[180:183], v14 offset:2048\n\t"
      "v_mfma_f32_4x4x4_16b_bf16 v[44:47], a[186:187], v[178:179], 0\n\t"
      "ds_read_b64_tr_b16 v[192:193], v12 offset:0\n\t"
      "v_mfma_f32_4x4x4_16b_bf16 v[48:51], a[184:185], v[180:181], 0\n\t"
      "ds_read_b64_tr_b16 v[194:195], v12 offset:768\n\t"
      "v_mfma_f32_4x4x4_16b_bf16 v[52:55], a[186:187], v[182:183], 0\n\t"
      "ds_read_b64_tr_b16 v[196:197], v13 offset:0\n\t"
      "ds_read_b64_tr_b16 v[198:199], v13 offset:768\n\t"
      "s_add_u32 m0, s17, 135680\n\t"
      "s_mov_b32 s54, 270336\n\t"
      "buffer_load_dwordx4 v9, s[84:87], s54 offen lds\n\t"
      "buffer_load_dwordx4 v10, s[84:87], s54 offen offset:1024 lds\n\t"
      "v_mfma_f32_16x16x32_bf16 v[16:19], v[160:163], a[224:227], v[16:19]\n\t"
      "v_cvt_pk_bf16_f32 v176, v40, v41\n\t"
      "v_cvt_pk_bf16_f32 v177, v42, v43\n\t"
      "v_mfma_f32_16x16x32_bf16 v[20:23], v[164:167], a[224:227], v[20:23]\n\t"
      "v_cvt_pk_bf16_f32 v178, v44, v45\n\t"
      "v_cvt_pk_bf16_f32 v179, v46, v47\n\t"
      "v_mfma_f32_16x16x32_bf16 v[24:27], v[168:171], a[224:227], v[24:27]\n\t"
      "v_cvt_pk_bf16_f32 v180, v48, v49\n\t"
      "v_cvt_pk_bf16_f32 v181, v50, v51\n\t"
      "v_mfma_f32_16x16x32_bf16 v[28:31], v[172:175], a[224:227], v[28:31]\n\t"
      "v_cvt_pk_bf16_f32 v182, v52, v53\n\t"
      "v_cvt_pk_bf16_f32 v183, v54, v55\n\t"
      "v_mfma_f32_4x4x4_16b_bf16 v[40:43], a[184:185], v[184:185], 0\n\t"
      "ds_read_b64_tr_b16 v[200:201], v12 offset:64\n\t"
      "v_mfma_f32_4x4x4_16b_bf16 v[44:47], a[186:187], v[186:187], 0\n\t"
      "ds_read_b64_tr_b16 v[202:203], v12 offset:832\n\t"
      "v_mfma_f32_4x4x4_16b_bf16 v[48:51], a[184:185], v[188:189], 0\n\t"
      "ds_read_b64_tr_b16 v[204:205], v13 offset:64\n\t"
      "v_mfma_f32_4x4x4_16b_bf16 v[52:55], a[186:187], v[190:191], 0\n\t"
      "ds_read_b64_tr_b16 v[206:207], v13 offset:832\n\t"
      "buffer_load_dwordx4 v11, s[84:87], s54 offen offset:2048 lds\n\t"
      "s_add_u32 m0, s17, 138752\n\t"
      "s_mov_b32 s54, 282624\n\t"
      "buffer_load_dwordx4 v9, s[84:87], s54 offen lds\n\t"
      "v_mfma_f32_16x16x32_bf16 v[32:35], a[108:111], a[224:227], v[32:35]\n\t"
      "v_cvt_pk_bf16_f32 v184, v40, v41\n\t"
      "v_cvt_pk_bf16_f32 v185, v42, v43\n\t"
      "v_mfma_f32_16x16x32_bf16 v[36:39], a[112:115], a[224:227], v[36:39]\n\t"
      "v_cvt_pk_bf16_f32 v186, v44, v45\n\t"
      "v_cvt_pk_bf16_f32 v187, v46, v47\n\t"
      "s_waitcnt lgkmcnt(9)\n\t"
      "v_mfma_f32_16x16x32_bf16 a[156:159], v[160:163], a[228:231], a[156:159]\n\t"
      "v_cvt_pk_bf16_f32 v188, v48, v49\n\t"
      "v_cvt_pk_bf16_f32 v189, v50, v51\n\t"
      "v_mfma_f32_16x16x32_bf16 a[160:163], v[164:167], a[228:231], a[160:163]\n\t"
      "v_cvt_pk_bf16_f32 v190, v52, v53\n\t"
      "v_cvt_pk_bf16_f32 v191, v54, v55\n\t"
      "ds_read_b128 a[232:235], v59 offset:25344\n\t"
      "v_mfma_f32_4x4x4_16b_bf16 v[40:43], a[184:185], a[116:117], 0\n\t"
      "ds_read_b64_tr_b16 a[124:125], v12 offset:128\n\t"
      "v_mfma_f32_4x4x4_16b_bf16 v[44:47], a[186:187], a[118:119], 0\n\t"
      "ds_read_b64_tr_b16 a[126:127], v12 offset:896\n\t"
      "v_mfma_f32_4x4x4_16b_bf16 v[48:51], a[184:185], a[120:121], 0\n\t"
      "ds_read_b64_tr_b16 a[128:129], v13 offset:128\n\t"
      "v_mfma_f32_4x4x4_16b_bf16 v[52:55], a[186:187], a[122:123], 0\n\t"
      "ds_read_b64_tr_b16 a[130:131], v13 offset:896\n\t"
      "buffer_load_dwordx4 v10, s[84:87], s54 offen offset:1024 lds\n\t"
      "buffer_load_dwordx4 v11, s[84:87], s54 offen offset:2048 lds\n\t"
      "ds_read_b128 a[236:239], v59 offset:37632\n\t"
      "s_or_b32 s30, s17, s16\n\t"
      "s_cbranch_scc1 .Lno3_%=\n\t"
      "global_load_dword a252, v62, s[12:13] sc1\n\t"
      ".Lno3_%=:\n\t"
      "s_add_u32 m0, s18, 49664\n\t"
      "s_add_u32 s48, s15, 786432\n\t"
      "buffer_load_dwordx4 v2, s[40:43], s48 offen lds\n\t"
      "buffer_load_dwordx4 v3, s[40:43], s48 offen offset:1024 lds\n\t"
      "buffer_load_dwordx4 v4, s[40:43], s48 offen offset:2048 lds\n\t"
      "v_mfma_f32_16x16x32_bf16 a[164:167], v[168:171], a[228:231], a[164:167]\n\t"
      "v_cvt_pk_bf16_f32 v40, v40, v41\n\t"
      "v_cvt_pk_bf16_f32 v41, v42, v43\n\t"
      "v_mfma_f32_16x16x32_bf16 a[168:171], v[172:175], a[228:231], a[168:171]\n\t"
      "v_cvt_pk_bf16_f32 v44, v44, v45\n\t"
      "v_cvt_pk_bf16_f32 v45, v46, v47\n\t"
      "v_mfma_f32_16x16x32_bf16 a[172:175], a[108:111], a[228:231], a[172:175]\n\t"
      "v_cvt_pk_bf16_f32 v48, v48, v49\n\t"
      "v_cvt_pk_bf16_f32 v49, v50, v51\n\t"
      "v_mfma_f32_16x16x32_bf16 a[176:179], a[112:115], a[228:231], a[176:179]\n\t"
      "v_cvt_pk_bf16_f32 v52, v52, v53\n\t"
      "v_cvt_pk_bf16_f32 v53, v54, v55\n\t"
      "v_accvgpr_write_b32 a116, v40\n\t"
      "v_accvgpr_write_b32 a117, v41\n\t"
      "v_accvgpr_write_b32 a118, v44\n\t"
      "v_accvgpr_write_b32 a119, v45\n\t"
      "v_accvgpr_write_b32 a120, v48\n\t"
      "v_accvgpr_write_b32 a121, v49\n\t"
      "v_accvgpr_write_b32 a122, v52\n\t"
      "v_accvgpr_write_b32 a123, v53\n\t"
      "s_waitcnt vmcnt(15)\n\t"
      "s_waitcnt lgkmcnt(1)\n\t"
      "; step\n\t"
      "v_mfma_f32_4x4x4_16b_bf16 v[40:43], a[180:181], v[192:193], 0\n\t"
      "ds_read_b128 a[184:187], v14 offset:2304\n\t"
      "v_mfma_f32_4x4x4_16b_bf16 v[44:47], a[182:183], v[194:195], 0\n\t"
      "ds_read_b64_tr_b16 v[208:209], v60 offset:0\n\t"
      "v_mfma_f32_4x4x4_16b_bf16 v[48:51], a[180:181], v[196:197], 0\n\t"
      "ds_read_b64_tr_b16 v[210:211], v60 offset:768\n\t"
      "v_mfma_f32_4x4x4_16b_bf16 v[52:55], a[182:183], v[198:199], 0\n\t"
      "ds_read_b64_tr_b16 v[212:213], v61 offset:0\n\t"
      "ds_read_b64_tr_b16 v[214:215], v61 offset:768\n\t"
      "s_add_u32 m0, s18, 61952\n\t"
      "s_add_u32 s48, s15, 786432\n\t"
      "buffer_load_dwordx4 v5, s[40:43], s48 offen lds\n\t"
      "buffer_load_dwordx4 v6, s[40:43], s48 offen offset:1024 lds\n\t"
      "v_mfma_f32_16x16x32_bf16 v[16:19], v[176:179], a[232:235], v[16:19]\n\t"
      "v_cvt_pk_bf16_f32 v192, v40, v41\n\t"
      "v_cvt_pk_bf16_f32 v193, v42, v43\n\t"
      "v_mfma_f32_16x16x32_bf16 v[20:23], v[180:183], a[232:235], v[20:23]\n\t"
      "v_cvt_pk_bf16_f32 v194, v44, v45\n\t"
      "v_cvt_pk_bf16_f32 v195, v46, v47\n\t"
      "v_mfma_f32_16x16x32_bf16 v[24:27], v[184:187], a[232:235], v[24:27]\n\t"
      "v_cvt_pk_bf16_f32 v196, v48, v49\n\t"
      "v_cvt_pk_bf16_f32 v197, v50, v51\n\t"
      "v_mfma_f32_16x16x32_bf16 v[28:31], v[188:191], a[232:235], v[28:31]\n\t"
      "v_cvt_pk_bf16_f32 v198, v52, v53\n\t"
      "v_cvt_pk_bf16_f32 v199, v54, v55\n\t"
      "v_mfma_f32_4x4x4_16b_bf16 v[40:43], a[180:181], v[200:201], 0\n\t"
      "ds_read_b64_tr_b16 v[216:217], v60 offset:64\n\t"
      "v_mfma_f32_4x4x4_16b_bf16 v[44:47], a[182:183], v[202:203], 0\n\t"
      "ds_read_b64_tr_b16 v[218:219], v60 offset:832\n\t"
      "v_mfma_f32_4x4x4_16b_bf16 v[48:51], a[180:181], v[204:205], 0\n\t"
      "ds_read_b64_tr_b16 v[220:221], v61 offset:64\n\t"
      "v_mfma_f32_4x4x4_16b_bf16 v[52:55], a[182:183], v[206:207], 0\n\t"
      "ds_read_b64_tr_b16 v[222:223], v61 offset:832\n\t"
      "buffer_load_dwordx4 v7, s[40:43], s48 offen offset:2048 lds\n\t"
      "v_mfma_f32_16x16x32_bf16 v[32:35], a[116:119], a[232:235], v[32:35]\n\t"
      "v_cvt_pk_bf16_f32 v200, v40, v41\n\t"
      "v_cvt_pk_bf16_f32 v201, v42, v43\n\t"
      "v_mfma_f32_16x16x32_bf16 v[36:39], a[120:123], a[232:235], v[36:39]\n\t"
      "v_cvt_pk_bf16_f32 v202, v44, v45\n\t"
      "v_cvt_pk_bf16_f32 v203, v46, v47\n\t"
      "s_waitcnt lgkmcnt(9)\n\t"
      "v_mfma_f32_16x16x32_bf16 a[156:159], v[176:179], a[236:239], a[156:159]\n\t"
      "v_cvt_pk_bf16_f32 v204, v48, v49\n\t"
      "v_cvt_pk_bf16_f32 v205, v50, v51\n\t"
      "v_mfma_f32_16x16x32_bf16 a[160:163], v[180:183], a[236:239], a[160:163]\n\t"
      "v_cvt_pk_bf16_f32 v206, v52, v53\n\t"
      "v_cvt_pk_bf16_f32 v207, v54, v55\n\t"
      "ds_read_b128 a[240:243], v56 offset:25600\n\t"
      "v_mfma_f32_4x4x4_16b_bf16 v[40:43], a[180:181], a[124:125], 0\n\t"
      "ds_read_b64_tr_b16 a[132:133], v60 offset:128\n\t"
      "v_mfma_f32_4x4x4_16b_bf16 v[44:47], a[182:183], a[126:127], 0\n\t"
      "ds_read_b64_tr_b16 a[134:135], v60 offset:896\n\t"
      "v_mfma_f32_4x4x4_16b_bf16 v[48:51], a[180:181], a[128:129], 0\n\t"
      "ds_read_b64_tr_b16 a[136:137], v61 offset:128\n\t"
      "v_mfma_f32_4x4x4_16b_bf16 v[52:55], a[182:183], a[130:131], 0\n\t"
      "ds_read_b64_tr_b16 a[138:139], v61 offset:896\n\t"
      "ds_read_b128 a[244:247], v56 offset:37888\n\t"
      "v_mfma_f32_16x16x32_bf16 a[164:167], v[184:187], a[236:239], a[164:167]\n\t"
      "v_cvt_pk_bf16_f32 v40, v40, v41\n\t"
      "v_cvt_pk_bf16_f32 v41, v42, v43\n\t"
      "v_mfma_f32_16x16x32_bf16 a[168:171], v[188:191], a[236:239], a[168:171]\n\t"
      "v_cvt_pk_bf16_f32 v44, v44, v45\n\t"
      "v_cvt_pk_bf16_f32 v45, v46, v47\n\t"
      "v_mfma_f32_16x16x32_bf16 a[172:175], a[116:119], a[236:239], a[172:175]\n\t"
      "v_cvt_pk_bf16_f32 v48, v48, v49\n\t"
      "v_cvt_pk_bf16_f32 v49, v50, v51\n\t"
      "v_mfma_f32_16x16x32_bf16 a[176:179], a[120:123], a[236:239], a[176:179]\n\t"
      "v_cvt_pk_bf16_f32 v52, v52, v53\n\t"
      "v_cvt_pk_bf16_f32 v53, v54, v55\n\t"
      "v_accvgpr_write_b32 a124, v40\n\t"
      "v_accvgpr_write_b32 a125, v41\n\t"
      "v_accvgpr_write_b32 a126, v44\n\t"
      "v_accvgpr_write_b32 a127, v45\n\t"
      "v_accvgpr_write_b32 a128, v48\n\t"
      "v_accvgpr_write_b32 a129, v49\n\t"
      "v_accvgpr_write_b32 a130, v52\n\t"
      "v_accvgpr_write_b32 a131, v53\n\t"
      "s_waitcnt vmcnt(12)\n\t"
      "s_waitcnt lgkmcnt(1)\n\t"
      "; step\n\t"
      "v_mfma_f32_4x4x4_16b_bf16 v[40:43], a[184:185], v[208:209], 0\n\t"
      "ds_read_b128 a[180:183], v14 offset:2560\n\t"
      "v_mfma_f32_4x4x4_16b_bf16 v[44:47], a[186:187], v[210:211], 0\n\t"
      "ds_read_b64_tr_b16 v[224:225], v60 offset:24576\n\t"
      "v_mfma_f32_4x4x4_16b_bf16 v[48:51], a[184:185], v[212:213], 0\n\t"
      "ds_read_b64_tr_b16 v[226:227], v60 offset:25344\n\t"
      "v_mfma_f32_4x4x4_16b_bf16 v[52:55], a[186:187], v[214:215], 0\n\t"
      "ds_read_b64_tr_b16 v[228:229], v61 offset:24576\n\t"
      "ds_read_b64_tr_b16 v[230:231], v61 offset:25344\n\t"
      "v_mfma_f32_16x16x32_bf16 v[16:19], v[192:195], a[240:243], v[16:19]\n\t"
      "v_cvt_pk_bf16_f32 v208, v40, v41\n\t"
      "v_cvt_pk_bf16_f32 v209, v42, v43\n\t"
      "v_mfma_f32_16x16x32_bf16 v[20:23], v[196:199], a[240:243], v[20:23]\n\t"
      "v_cvt_pk_bf16_f32 v210, v44, v45\n\t"
      "v_cvt_pk_bf16_f32 v211, v46, v47\n\t"
      "v_mfma_f32_16x16x32_bf16 v[24:27], v[200:203], a[240:243], v[24:27]\n\t"
      "v_cvt_pk_bf16_f32 v212, v48, v49\n\t"
      "v_cvt_pk_bf16_f32 v213, v50, v51\n\t"
      "v_mfma_f32_16x16x32_bf16 v[28:31], v[204:207], a[240:243], v[28:31]\n\t"
      "v_cvt_pk_bf16_f32 v214, v52, v53\n\t"
      "v_cvt_pk_bf16_f32 v215, v54, v55\n\t"
      "v_mfma_f32_4x4x4_16b_bf16 v[40:43], a[184:185], v[216:217], 0\n\t"
      "ds_read_b64_tr_b16 v[232:233], v60 offset:24640\n\t"
      "v_mfma_f32_4x4x4_16b_bf16 v[44:47], a[186:187], v[218:219], 0\n\t"
      "ds_read_b64_tr_b16 v[234:235], v60 offset:25408\n\t"
      "v_mfma_f32_4x4x4_16b_bf16 v[48:51], a[184:185], v[220:221], 0\n\t"
      "ds_read_b64_tr_b16 v[236:237], v61 offset:24640\n\t"
      "v_mfma_f32_4x4x4_16b_bf16 v[52:55], a[186:187], v[222:223], 0\n\t"
      "ds_read_b64_tr_b16 v[238:239], v61 offset:25408\n\t"
      "v_mfma_f32_16x16x32_bf16 v[32:35], a[124:127], a[240:243], v[32:35]\n\t"
      "v_cvt_pk_bf16_f32 v216, v40, v41\n\t"
      "v_cvt_pk_bf16_f32 v217, v42, v43\n\t"
      "v_mfma_f32_16x16x32_bf16 v[36:39], a[128:131], a[240:243], v[36:39]\n\t"
      "v_cvt_pk_bf16_f32 v218, v44, v45\n\t"
      "v_cvt_pk_bf16_f32 v219, v46, v47\n\t"
      "s_waitcnt lgkmcnt(9)\n\t"
      "v_mfma_f32_16x16x32_bf16 a[156:159], v[192:195], a[244:247], a[156:159]\n\t"
      "v_cvt_pk_bf16_f32 v220, v48, v49\n\t"
      "v_cvt_pk_bf16_f32 v221, v50, v51\n\t"
      "v_mfma_f32_16x16x32_bf16 a[160:163], v[196:199], a[244:247], a[160:163]\n\t"
      "v_cvt_pk_bf16_f32 v222, v52, v53\n\t"
      "v_cvt_pk_bf16_f32 v223, v54, v55\n\t"
      "ds_read_b128 a[224:227], v57 offset:25600\n\t"
      "v_mfma_f32_4x4x4_16b_bf16 v[40:43], a[184:185], a[132:133], 0\n\t"
      "ds_read_b64_tr_b16 a[140:141], v60 offset:24704\n\t"
      "v_mfma_f32_4x4x4_16b_bf16 v[44:47], a[186:187], a[134:135], 0\n\t"
      "ds_read_b64_tr_b16 a[142:143], v60 offset:25472\n\t"
      "v_mfma_f32_4x4x4_16b_bf16 v[48:51], a[184:185], a[136:137], 0\n\t"
      "ds_read_b64_tr_b16 a[144:145], v61 offset:24704\n\t"
      "v_mfma_f32_4x4x4_16b_bf16 v[52:55], a[186:187], a[138:139], 0\n\t"
      "ds_read_b64_tr_b16 a[146:147], v61 offset:25472\n\t"
      "ds_read_b128 a[228:231], v57 offset:37888\n\t"
      "v_mfma_f32_16x16x32_bf16 a[164:167], v[200:203], a[244:247], a[164:167]\n\t"
      "v_cvt_pk_bf16_f32 v40, v40, v41\n\t"
      "v_cvt_pk_bf16_f32 v41, v42, v43\n\t"
      "v_mfma_f32_16x16x32_bf16 a[168:171], v[204:207], a[244:247], a[168:171]\n\t"
      "v_cvt_pk_bf16_f32 v44, v44, v45\n\t"
      "v_cvt_pk_bf16_f32 v45, v46, v47\n\t"
      "v_mfma_f32_16x16x32_bf16 a[172:175], a[124:127], a[244:247], a[172:175]\n\t"
      "v_cvt_pk_bf16_f32 v48, v48, v49\n\t"
      "v_cvt_pk_bf16_f32 v49, v50, v51\n\t"
      "v_mfma_f32_16x16x32_bf16 a[176:179], a[128:131], a[244:247], a[176:179]\n\t"
      "v_cvt_pk_bf16_f32 v52, v52, v53\n\t"
      "v_cvt_pk_bf16_f32 v53, v54, v55\n\t"
      "v_accvgpr_write_b32 a132, v40\n\t"
      "v_accvgpr_write_b32 a133, v41\n\t"
      "v_accvgpr_write_b32 a134, v44\n\t"
      "v_accvgpr_write_b32 a135, v45\n\t"
      "v_accvgpr_write_b32 a136, v48\n\t"
      "v_accvgpr_write_b32 a137, v49\n\t"
      "v_accvgpr_write_b32 a138, v52\n\t"
      "v_accvgpr_write_b32 a139, v53\n\t"
      "s_waitcnt vmcnt(6)\n\t"
      "s_waitcnt lgkmcnt(1)\n\t"
      "; step\n\t"
      "s_memtime s[68:69]\n\t"
      "s_memrealtime s[70:71]\n\t"
      "v_mfma_f32_4x4x4_16b_bf16 v[40:43], a[180:181], v[224:225], 0\n\t"
      "ds_read_b128 a[184:187], v14 offset:2816\n\t"
      "v_mfma_f32_4x4x4_16b_bf16 v[44:47], a[182:183], v[226:227], 0\n\t"
      "ds_read_b64_tr_b16 v[240:241], v60 offset:49152\n\t"
      "v_mfma_f32_4x4x4_16b_bf16 v[48:51], a[180:181], v[228:229], 0\n\t"
      "ds_read_b64_tr_b16 v[242:243], v60 offset:49920\n\t"
      "v_mfma_f32_4x4x4_16b_bf16 v[52:55], a[182:183], v[230:231], 0\n\t"
      "ds_read_b64_tr_b16 v[244:245], v61 offset:49152\n\t"
      "ds_read_b64_tr_b16 v[246:247], v61 offset:49920\n\t"
      "v_mfma_f32_16x16x32_bf16 v[16:19], v[208:211], a[224:227], v[16:19]\n\t"
      "v_cvt_pk_bf16_f32 v224, v40, v41\n\t"
      "v_cvt_pk_bf16_f32 v225, v42, v43\n\t"
      "v_mfma_f32_16x16x32_bf16 v[20:23], v[212:215], a[224:227], v[20:23]\n\t"
      "v_cvt_pk_bf16_f32 v226, v44, v45\n\t"
      "v_cvt_pk_bf16_f32 v227, v46, v47\n\t"
      "v_mfma_f32_16x16x32_bf16 v[24:27], v[216:219], a[224:227], v[24:27]\n\t"
      "v_cvt_pk_bf16_f32 v228, v48, v49\n\t"
      "v_cvt_pk_bf16_f32 v229, v50, v51\n\t"
      "v_mfma_f32_16x16x32_bf16 v[28:31], v[220:223], a[224:227], v[28:31]\n\t"
      "v_cvt_pk_bf16_f32 v230, v52, v53\n\t"
      "v_cvt_pk_bf16_f32 v231, v54, v55\n\t"
      "v_mfma_f32_4x4x4_16b_bf16 v[40:43], a[180:181], v[232:233], 0\n\t"
      "ds_read_b64_tr_b16 v[248:249], v60 offset:49216\n\t"
      "v_mfma_f32_4x4x4_16b_bf16 v[44:47], a[182:183], v[234:235], 0\n\t"
      "ds_read_b64_tr_b16 v[250:251], v60 offset:49984\n\t"
      "v_mfma_f32_4x4x4_16b_bf16 v[48:51], a[180:181], v[236:237], 0\n\t"
      "ds_read_b64_tr_b16 v[252:253], v61 offset:49216\n\t"
      "v_mfma_f32_4x4x4_16b_bf16 v[52:55], a[182:183], v[238:239], 0\n\t"
      "ds_read_b64_tr_b16 v[254:255], v61 offset:49984\n\t"
      "v_mfma_f32_16x16x32_bf16 v[32:35], a[132:135], a[224:227], v[32:35]\n\t"
      "v_cvt_pk_bf16_f32 v232, v40, v41\n\t"
      "v_cvt_pk_bf16_f32 v233, v42, v43\n\t"
      "v_mfma_f32_16x16x32_bf16 v[36:39], a[136:139], a[224:227], v[36:39]\n\t"
      "v_cvt_pk_bf16_f32 v234, v44, v45\n\t"
      "v_cvt_pk_bf16_f32 v235, v46, v47\n\t"
      "s_waitcnt lgkmcnt(9)\n\t"
      "v_mfma_f32_16x16x32_bf16 a[156:159], v[208:211], a[228:231], a[156:159]\n\t"
      "v_cvt_pk_bf16_f32 v236, v48, v49\n\t"
      "v_cvt_pk_bf16_f32 v237, v50, v51\n\t"
      "v_mfma_f32_16x16x32_bf16 a[160:163], v[212:215], a[228:231], a[160:163]\n\t"
      "v_cvt_pk_bf16_f32 v238, v52, v53\n\t"
      "v_cvt_pk_bf16_f32 v239, v54, v55\n\t"
      "ds_read_b128 a[232:235], v58 offset:25600\n\t"
      "v_mfma_f32_4x4x4_16b_bf16 v[40:43], a[180:181], a[140:141], 0\n\t"
      "ds_read_b64_tr_b16 a[148:149], v60 offset:49280\n\t"
      "v_mfma_f32_4x4x4_16b_bf16 v[44:47], a[182:183], a[142:143], 0\n\t"
      "ds_read_b64_tr_b16 a[150:151], v60 offset:50048\n\t"
      "v_mfma_f32_4x4x4_16b_bf16 v[48:51], a[180:181], a[144:145], 0\n\t"
      "ds_read_b64_tr_b16 a[152:153], v61 offset:49280\n\t"
      "v_mfma_f32_4x4x4_16b_bf16 v[52:55], a[182:183], a[146:147], 0\n\t"
      "ds_read_b64_tr_b16 a[154:155], v61 offset:50048\n\t"
      "v_mfma_f32_16x16x32_bf16 a[164:167], v[216:219], a[228:231], a[164:167]\n\t"
      "v_cvt_pk_bf16_f32 v40, v40, v41\n\t"
      "v_cvt_pk_bf16_f32 v41, v42, v43\n\t"
      "v_mfma_f32_16x16x32_bf16 a[168:171], v[220:223], a[228:231], a[168:171]\n\t"
      "v_cvt_pk_bf16_f32 v44, v44, v45\n\t"
      "v_cvt_pk_bf16_f32 v45, v46, v47\n\t"
      "v_mfma_f32_16x16x32_bf16 a[172:175], a[132:135], a[228:231], a[172:175]\n\t"
      "v_cvt_pk_bf16_f32 v48, v48, v49\n\t"
      "v_cvt_pk_bf16_f32 v49, v50, v51\n\t"
      "v_mfma_f32_16x16x32_bf16 a[176:179], a[136:139], a[228:231], a[176:179]\n\t"
      "v_cvt_pk_bf16_f32 v52, v52, v53\n\t"
      "v_cvt_pk_bf16_f32 v53, v54, v55\n\t"
      "v_accvgpr_write_b32 a140, v40\n\t"
      "v_accvgpr_write_b32 a141, v41\n\t"
      "v_accvgpr_write_b32 a142, v44\n\t"
      "v_accvgpr_write_b32 a143, v45\n\t"
      "v_accvgpr_write_b32 a144, v48\n\t"
      "v_accvgpr_write_b32 a145, v49\n\t"
      "v_accvgpr_write_b32 a146, v52\n\t"
      "v_accvgpr_write_b32 a147, v53\n\t"
      "s_waitcnt lgkmcnt(0)\n\t"
      "; step\n\t"
      "v_mfma_f32_4x4x4_16b_bf16 v[40:43], a[184:185], v[240:241], 0\n\t"
      "v_mfma_f32_4x4x4_16b_bf16 v[44:47], a[186:187], v[242:243], 0\n\t"
      "v_mfma_f32_4x4x4_16b_bf16 v[48:51], a[184:185], v[244:245], 0\n\t"
      "v_mfma_f32_4x4x4_16b_bf16 v[52:55], a[186:187], v[246:247], 0\n\t"
      "v_mfma_f32_16x16x32_bf16 v[16:19], v[224:227], a[232:235], v[16:19]\n\t"
      "s_nop 0\n\t"
      "v_cvt_pk_bf16_f32 v240, v40, v41\n\t"
      "v_cvt_pk_bf16_f32 v241, v42, v43\n\t"
      "v_mfma_f32_16x16x32_bf16 v[20:23], v[228:231], a[232:235], v[20:23]\n\t"
      "v_cvt_pk_bf16_f32 v242, v44, v45\n\t"
      "v_cvt_pk_bf16_f32 v243, v46, v47\n\t"
      "v_cvt_pk_bf16_f32 v244, v48, v49\n\t"
      "v_cvt_pk_bf16_f32 v245, v50, v51\n\t"
      "v_cvt_pk_bf16_f32 v246, v52, v53\n\t"
      "v_cvt_pk_bf16_f32 v247, v54, v55\n\t"
      "v_mfma_f32_4x4x4_16b_bf16 v[40:43], a[184:185], v[248:249], 0\n\t"
      "v_mfma_f32_4x4x4_16b_bf16 v[44:47], a[186:187], v[250:251], 0\n\t"
      "v_mfma_f32_4x4x4_16b_bf16 v[48:51], a[184:185], v[252:253], 0\n\t"
      "v_mfma_f32_4x4x4_16b_bf16 v[52:55], a[186:187], v[254:255], 0\n\t"
      "v_mfma_f32_16x16x32_bf16 v[24:27], v[232:235], a[232:235], v[24:27]\n\t"
      "s_nop 0\n\t"
      "v_cvt_pk_bf16_f32 v248, v40, v41\n\t"
      "v_cvt_pk_bf16_f32 v249, v42, v43\n\t"
      "v_mfma_f32_16x16x32_bf16 v[28:31], v[236:239], a[232:235], v[28:31]\n\t"
      "v_cvt_pk_bf16_f32 v250, v44, v45\n\t"
      "v_cvt_pk_bf16_f32 v251, v46, v47\n\t"
      "v_cvt_pk_bf16_f32 v252, v48, v49\n\t"
      "v_cvt_pk_bf16_f32 v253, v50, v51\n\t"
      "v_cvt_pk_bf16_f32 v254, v52, v53\n\t"
      "v_cvt_pk_bf16_f32 v255, v54, v55\n\t"
      "ds_read_b128 a[236:239], v59 offset:25600\n\t"
      "v_mfma_f32_4x4x4_16b_bf16 v[40:43], a[184:185], a[148:149], 0\n\t"
      "v_mfma_f32_4x4x4_16b_bf16 v[44:47], a[186:187], a[150:151], 0\n\t"
      "v_mfma_f32_4x4x4_16b_bf16 v[48:51], a[184:185], a[152:153], 0\n\t"
      "v_mfma_f32_4x4x4_16b_bf16 v[52:55], a[186:187], a[154:155], 0\n\t"
      "ds_read_b128 a[240:243], v58 offset:37888\n\t"
      "ds_read_b128 a[244:247], v59 offset:37888\n\t"
      "v_mfma_f32_16x16x32_bf16 v[32:35], a[140:143], a[232:235], v[32:35]\n\t"
      "v_cvt_pk_bf16_f32 v40, v40, v41\n\t"
      "v_cvt_pk_bf16_f32 v41, v42, v43\n\t"
      "v_mfma_f32_16x16x32_bf16 v[36:39], a[144:147], a[232:235], v[36:39]\n\t"
      "v_cvt_pk_bf16_f32 v44, v44, v45\n\t"
      "v_cvt_pk_bf16_f32 v45, v46, v47\n\t"
      "v_cvt_pk_bf16_f32 v48, v48, v49\n\t"
      "v_cvt_pk_bf16_f32 v49, v50, v51\n\t"
      "v_cvt_pk_bf16_f32 v52, v52, v53\n\t"
      "v_cvt_pk_bf16_f32 v53, v54, v55\n\t"
      "v_accvgpr_write_b32 a148, v40\n\t"
      "v_accvgpr_write_b32 a149, v41\n\t"
      "v_accvgpr_write_b32 a150, v44\n\t"
      "v_accvgpr_write_b32 a151, v45\n\t"
      "v_accvgpr_write_b32 a152, v48\n\t"
      "v_accvgpr_write_b32 a153, v49\n\t"
      "v_accvgpr_write_b32 a154, v52\n\t"
      "v_accvgpr_write_b32 a155, v53\n\t"
      "s_waitcnt lgkmcnt(2)\n\t"
      "v_mfma_f32_16x16x32_bf16 v[16:19], v[240:243], a[236:239], v[16:19]\n\t"
      "s_add_u32 s49, s15, 0\n\t"
      "s_mov_b32 s50, 98816\n\t"
      "s_mov_b32 s51, 61952\n\t"
      "v_mfma_f32_16x16x32_bf16 v[20:23], v[244:247], a[236:239], v[20:23]\n\t"
      "s_mov_b32 s58, s14\n\t"
      "s_add_u32 s56, s14, 2359296\n\t"
      "v_mfma_f32_16x16x32_bf16 v[24:27], v[248:251], a[236:239], v[24:27]\n\t"
      "s_movk_i32 s39, 96\n\t"
      "s_movk_i32 s29, 71\n\t"
      "s_mov_b32 s32, 1947275679\n\t"
      "v_mfma_f32_16x16x32_bf16 v[28:31], v[252:255], a[236:239], v[28:31]\n\t"
      "s_mov_b32 s33, 143165559\n\t"
      "s_mov_b32 s34, 1950421356\n\t"
      "s_mov_b32 s35, 8947847\n\t"
      "v_mfma_f32_16x16x32_bf16 v[32:35], a[148:151], a[236:239], v[32:35]\n\t"
      "v_mov_b32 v52, 163328\n\t"
      "v_mfma_f32_16x16x32_bf16 v[36:39], a[152:155], a[236:239], v[36:39]\n\t"
      "s_cmp_lg_u32 s17, 0\n\t"
      "s_cselect_b32 s39, 0, s39\n\t"
      "s_cbranch_scc1 .Lno4_%=\n\t"
      "v_accvgpr_read_b32 v41, a248\n\t"
      "v_mov_b32 v51, 0x4040\n\t"
      "s_movk_i32 s52, 2000\n\t"
      "s_mov_b32 s91, 0\n\t"
      "s_waitcnt vmcnt(6) \n\t"
      "v_accvgpr_read_b32 v40, a252\n\t"
      ".Lcheck_%=:\n\t"
      "v_cmp_ne_u32 s[54:55], v40, v63\n\t"
      "s_cmp_lg_u32 s16, 0\n\t"
      "v_add_u32_dpp v42, v40, v40 quad_perm:[2,3,0,1] row_mask:0xf bank_mask:0xf\n\t"
      "s_cselect_b32 s30, -1, s54\n\t"
      "s_cmp_lg_u32 s30, -1\n\t"
      "s_cbranch_scc1 .Lwait_%=\n\t"
      ".Lwaited_%=:\n\t"
      "v_add_u32_dpp v42, v42, v42 quad_perm:[1,0,3,2] row_mask:0xf bank_mask:0xf\n\t"
      "s_bfe_u32 s30, s19, 0x30008\n\t"
      "s_bfe_u32 s31, s19, 0x10003\n\t"
      "s_lshl1_add_u32 s30, s30, s31\n\t"
      "s_lshl_b32 s30, s30, 1\n\t"
      "v_mov_b32 v43, v42\n\t"
      "v_subrev_u32_dpp v43, v42, v51 row_half_mirror row_mask:0x3 bank_mask:0xf\n\t"
      "v_lshl_add_u32 v42, v42, 1, v43\n\t"
      "v_readlane_b32 s31, v40, s30\n\t"
      "v_add_u32 v42, 0x1f1f, v42\n\t"
      "v_and_b32 v43, 128, v42\n\t"
      "v_and_b32 v44, 32768, v42\n\t"
      "v_cmp_ne_u32 s[88:89], 0, v43\n\t"
      "v_cmp_ne_u32 s[36:37], 0, v44\n\t"
      "s_bitcmp1_b32 s19, 2\n\t"
      "s_cselect_b32 s30, 0x3ffff, 0\n\t"
      "s_bitcmp1_b32 s31, 16\n\t"
      "s_cselect_b32 s31, 262140, 0\n\t"
      "s_xor_b32 s30, s30, s31\n\t"
      "v_lshrrev_b32 v41, v41, s88\n\t"
      "v_and_b32 v41, 1, v41\n\t"
      "v_cmp_ne_u32 vcc, 0, v41\n\t"
      "s_nop 0\n\t"
      "s_and_b32 s31, vcc_lo, 262140\n\t"
      "s_xor_b32 s31, s31, s30\n\t"
      "s_or_b32 s31, s31, s91\n\t"
      "s_and_b32 s30, s16, 0x3ffff\n\t"
      "s_cmp_lg_u32 s16, 0\n\t"
      "s_cselect_b32 s31, s30, s31\n\t"
      "v_mov_b32 v53, s31\n\t"
      "ds_write_b32 v52, v53\n\t"
      "s_xor_b32 s36, s37, s89\n\t"
      "s_lshr_b32 s30, s55, 1\n\t"
      "s_and_b32 s55, s55, s30\n\t"
      "s_lshr_b32 s30, s55, 2\n\t"
      "s_and_b32 s55, s55, s30\n\t"
      "s_and_b32 s36, s36, s55\n\t"
      "s_and_b32 s36, s36, 0x11111111\n\t"
      "s_bcnt1_i32_b32 s36, s36\n\t"
      "s_lshl_b32 s30, s36, 2\n\t"
      "s_mov_b32 s54, 1412636928\n\t"
      "s_mov_b32 s55, 5\n\t"
      "s_lshr_b64 s[54:55], s[54:55], s30\n\t"
      "s_and_b32 s30, s54, 15\n\t"
      "s_add_u32 s29, s29, s30\n\t"
      ".Lno4_%=:\n\t"
      "v_accvgpr_read_b32 v9, a213\n\t"
      "v_accvgpr_read_b32 v10, a214\n\t"
      "v_accvgpr_read_b32 v11, a215\n\t"
      "v_accvgpr_read_b32 v12, a216\n\t"
      "v_accvgpr_read_b32 v13, a217\n\t"
      "v_accvgpr_read_b32 v14, a218\n\t"
      "v_accvgpr_read_b32 v8, a219\n\t"
      "v_accvgpr_read_b32 v60, a220\n\t"
      "v_accvgpr_read_b32 v61, a221\n\t"
      "v_add_u32 v56, 49664, v56\n\t"
      "v_add_u32 v57, 49664, v57\n\t"
      "v_add_u32 v58, 49664, v58\n\t"
      "v_add_u32 v59, 49664, v59\n\t"
      "v_cvt_pk_bf16_f32 v40, v16, v17\n\t"
      "v_cvt_pk_bf16_f32 v41, v18, v19\n\t"
      "v_cvt_pk_bf16_f32 v42, v20, v21\n\t"
      "v_cvt_pk_bf16_f32 v43, v22, v23\n\t"
      "v_cvt_pk_bf16_f32 v44, v24, v25\n\t"
      "v_cvt_pk_bf16_f32 v45, v26, v27\n\t"
      "v_cvt_pk_bf16_f32 v46, v28, v29\n\t"
      "v_cvt_pk_bf16_f32 v47, v30, v31\n\t"
      "v_cvt_pk_bf16_f32 v48, v32, v33\n\t"
      "v_cvt_pk_bf16_f32 v49, v34, v35\n\t"
      "v_cvt_pk_bf16_f32 v50, v36, v37\n\t"
      "v_cvt_pk_bf16_f32 v51, v38, v39\n\t"
      "s_mov_b32 s46, 113246208\n\t"
      "s_mov_b32 s52, 15\n\t"
      "s_add_u32 s90, s90, 0x2000000\n\t"
      "s_waitcnt vmcnt(3)\n\t"
      "s_waitcnt lgkmcnt(0)\n\t"
      "s_barrier\n\t"
      "v_mfma_f32_16x16x32_bf16 a[156:159], v[224:227], a[240:243], a[156:159]\n\t"
      "ds_read_b32 v63, v52\n\t"
      "ds_read_b128 a[0:3], v56 offset:0\n\t"
      "s_memrealtime s[36:37]\n\t"
      "v_mfma_f32_16x16x32_bf16 a[160:163], v[228:231], a[240:243], a[160:163]\n\t"
      "ds_read_b128 a[4:7], v57 offset:0\n\t"
      "ds_read_b128 a[8:11], v58 offset:0\n\t"
      "v_mfma_f32_16x16x32_bf16 a[164:167], v[232:235], a[240:243], a[164:167]\n\t"
      "ds_read_b128 a[12:15], v59 offset:0\n\t"
      "ds_read_b128 a[16:19], v56 offset:256\n\t"
      "v_mfma_f32_16x16x32_bf16 a[168:171], v[236:239], a[240:243], a[168:171]\n\t"
      "ds_read_b128 a[20:23], v57 offset:256\n\t"
      "ds_write_b64 v0, v[40:41] offset:0\n\t"
      "v_mfma_f32_16x16x32_bf16 a[172:175], a[140:143], a[240:243], a[172:175]\n\t"
      "ds_write_b64 v0, v[42:43] offset:32\n\t"
      "v_mfma_f32_16x16x32_bf16 a[176:179], a[144:147], a[240:243], a[176:179]\n\t"
      "s_waitcnt lgkmcnt(8)\n\t"
      "v_readfirstlane_b32 s91, v63\n\t"
      "v_mfma_f32_16x16x32_bf16 a[156:159], v[240:243], a[244:247], a[156:159]\n\t"
      "ds_write_b64 v0, v[44:45] offset:64\n\t"
      "s_add_u32 m0, s18, 74240\n\t"
      "v_mfma_f32_16x16x32_bf16 a[160:163], v[244:247], a[244:247], a[160:163]\n\t"
      "s_bitcmp1_b32 s91, 2\n\t"
      "s_cselect_b32 s48, 24576, 0\n\t"
      "s_add_u32 s48, s48, s14\n\t"
      "v_mfma_f32_16x16x32_bf16 a[164:167], v[248:251], a[244:247], a[164:167]\n\t"
      "s_add_u32 s48, s48, 1572864\n\t"
      "buffer_load_dwordx4 v2, s[40:43], s48 offen lds\n\t"
      "s_mov_b32 s59, s91\n\t"
      "v_mfma_f32_16x16x32_bf16 a[168:171], v[252:255], a[244:247], a[168:171]\n\t"
      "ds_write_b64 v0, v[46:47] offset:96\n\t"
      "buffer_load_dwordx4 v3, s[40:43], s48 offen offset:1024 lds\n\t"
      "v_mfma_f32_16x16x32_bf16 a[172:175], a[148:151], a[244:247], a[172:175]\n\t"
      "ds_write_b64 v0, v[48:49] offset:128\n\t"
      "buffer_load_dwordx4 v4, s[40:43], s48 offen offset:2048 lds\n\t"
      "v_mfma_f32_16x16x32_bf16 a[176:179], a[152:155], a[244:247], a[176:179]\n\t"
      "ds_write_b64 v0, v[50:51] offset:160\n\t"
      "s_lshr_b32 s57, s91, 3\n\t"
      "s_bitcmp1_b32 s91, 3\n\t"
      "s_cselect_b32 s48, 24576, 0\n\t"
      "s_add_u32 s48, s48, s14\n\t"
      "s_add_u32 s48, s48, 2359296\n\t"
      "s_or_b32 s30, s17, s16\n\t"
      "s_cbranch_scc1 .Lno5_%=\n\t"
      "s_sub_u32 s67, s66, s28\n\t"
      "s_sub_u32 s68, s68, s64\n\t"
      "s_sub_u32 s70, s70, s66\n\t"
      "s_mul_i32 s64, s68, 100\n\t"
      "s_mul_i32 s66, s70, 1750\n\t"
      "s_sub_i32 s66, s64, s66\n\t"
      "s_ashr_i32 s66, s66, 9\n\t"
      "s_add_i32 s66, s66, 128\n\t"
      "s_max_i32 s66, s66, 1\n\t"
      "s_min_i32 s66, s66, 255\n\t"
      "s_cmp_lt_u32 s67, 192\n\t"
      "s_cselect_b32 s66, 255, s66\n\t"
      "s_lshl_b32 s66, s66, 17\n\t"
      "s_or_b32 s90, s90, s66\n\t"
      "v_mov_b32 v54, s90\n\t"
      "v_mov_b32 v55, s19\n\t"
      "global_store_dword v55, v54, s[12:13] sc1\n\t"
      ".Lno5_%=:\n\t"
      "s_waitcnt lgkmcnt(11)\n\t"
      "v_mfma_f32_16x16x32_bf16 v[16:19], v[64:67], a[0:3], 0\n\t"
      "ds_read_b128 a[24:27], v58 offset:256\n\t"
      "ds_read_b128 a[28:31], v59 offset:256\n\t"
      "s_waitcnt lgkmcnt(12)\n\t"
      "v_mfma_f32_16x16x32_bf16 v[16:19], v[80:83], a[4:7], v[16:19]\n\t"
      "ds_read_b128 a[32:35], v56 offset:512\n\t"
      "ds_read_b128 a[36:39], v57 offset:512\n\t"
      "s_waitcnt lgkmcnt(13)\n\t"
      "v_mfma_f32_16x16x32_bf16 v[16:19], v[96:99], a[8:11], v[16:19]\n\t"
      "ds_read_b128 a[40:43], v58 offset:512\n\t"
      "ds_read_b128 a[44:47], v59 offset:512\n\t"
      "s_waitcnt lgkmcnt(14)\n\t"
      "v_mfma_f32_16x16x32_bf16 v[16:19], v[112:115], a[12:15], v[16:19]\n\t"
      "v_accvgpr_read_b32 v52, a156\n\t"
      "v_accvgpr_read_b32 v53, a157\n\t"
      "s_waitcnt lgkmcnt(13)\n\t"
      "v_mfma_f32_16x16x32_bf16 v[16:19], v[128:131], a[16:19], v[16:19]\n\t"
      "v_accvgpr_read_b32 v54, a158\n\t"
      "v_accvgpr_read_b32 v55, a159\n\t"
      "s_waitcnt lgkmcnt(12)\n\t"
      "v_mfma_f32_16x16x32_bf16 v[16:19], v[144:147], a[20:23], v[16:19]\n\t"
      "v_cvt_pk_bf16_f32 v40, v52, v53\n\t"
      "v_cvt_pk_bf16_f32 v41, v54, v55\n\t"
      "v_mfma_f32_16x16x32_bf16 v[20:23], v[68:71], a[0:3], 0\n\t"
      "ds_write_b64 v0, v[40:41] offset:12544\n\t"
      "v_mfma_f32_16x16x32_bf16 v[20:23], v[84:87], a[4:7], v[20:23]\n\t"
      "s_waitcnt lgkmcnt(7)\n\t"
      "s_barrier\n\t"
      "v_mfma_f32_16x16x32_bf16 v[20:23], v[100:103], a[8:11], v[20:23]\n\t"
      "ds_read_b128 a[48:51], v8 offset:0\n\t"
      "ds_read_b128 a[52:55], v60 offset:1024\n\t"
      "ds_read_b128 a[56:59], v61 offset:2048\n\t"
      "v_mfma_f32_16x16x32_bf16 v[20:23], v[116:119], a[12:15], v[20:23]\n\t"
      "v_accvgpr_read_b32 v52, a160\n\t"
      "v_accvgpr_read_b32 v53, a161\n\t"
      "v_mfma_f32_16x16x32_bf16 v[20:23], v[132:135], a[16:19], v[20:23]\n\t"
      "v_accvgpr_read_b32 v54, a162\n\t"
      "v_accvgpr_read_b32 v55, a163\n\t"
      "v_mfma_f32_16x16x32_bf16 v[20:23], v[148:151], a[20:23], v[20:23]\n\t"
      "v_cvt_pk_bf16_f32 v42, v52, v53\n\t"
      "v_cvt_pk_bf16_f32 v43, v54, v55\n\t"
      "v_mfma_f32_16x16x32_bf16 v[24:27], v[72:75], a[0:3], 0\n\t"
      "ds_write_b64 v0, v[42:43] offset:12576\n\t"
      "s_waitcnt lgkmcnt(3)\n\t"
      "v_mfma_f32_16x16x32_bf16 v[24:27], v[88:91], a[4:7], v[24:27]\n\t"
      "buffer_store_dwordx4 a[48:51], v9, s[44:47], s49 offen sc1\n\t"
      "v_mfma_f32_16x16x32_bf16 v[24:27], v[104:107], a[8:11], v[24:27]\n\t"
      "v_accvgpr_read_b32 v52, a164\n\t"
      "v_accvgpr_read_b32 v53, a165\n\t"
      "v_mfma_f32_16x16x32_bf16 v[24:27], v[120:123], a[12:15], v[24:27]\n\t"
      "v_accvgpr_read_b32 v54, a166\n\t"
      "v_accvgpr_read_b32 v55, a167\n\t"
      "v_mfma_f32_16x16x32_bf16 v[24:27], v[136:139], a[16:19], v[24:27]\n\t"
      "v_cvt_pk_bf16_f32 v44, v52, v53\n\t"
      "v_cvt_pk_bf16_f32 v45, v54, v55\n\t"
      "v_mfma_f32_16x16x32_bf16 v[24:27], v[152:155], a[20:23], v[24:27]\n\t"
      "ds_write_b64 v0, v[44:45] offset:12608\n\t"
      "s_waitcnt lgkmcnt(3)\n\t"
      "v_mfma_f32_16x16x32_bf16 v[28:31], v[76:79], a[0:3], 0\n\t"
      "buffer_store_dwordx4 a[52:55], v10, s[44:47], s49 offen offset:1024 sc1\n\t"
      "v_mfma_f32_16x16x32_bf16 v[28:31], v[92:95], a[4:7], v[28:31]\n\t"
      "v_accvgpr_read_b32 v52, a168\n\t"
      "v_accvgpr_read_b32 v53, a169\n\t"
      "v_mfma_f32_16x16x32_bf16 v[28:31], v[108:111], a[8:11], v[28:31]\n\t"
      "v_accvgpr_read_b32 v54, a170\n\t"
      "v_accvgpr_read_b32 v55, a171\n\t"
      "v_mfma_f32_16x16x32_bf16 v[28:31], v[124:127], a[12:15], v[28:31]\n\t"
      "v_cvt_pk_bf16_f32 v46, v52, v53\n\t"
      "v_cvt_pk_bf16_f32 v47, v54, v55\n\t"
      "v_mfma_f32_16x16x32_bf16 v[28:31], v[140:143], a[16:19], v[28:31]\n\t"
      "ds_write_b64 v0, v[46:47] offset:12640\n\t"
      "s_waitcnt lgkmcnt(3)\n\t"
      "v_mfma_f32_16x16x32_bf16 v[28:31], v[156:159], a[20:23], v[28:31]\n\t"
      "buffer_store_dwordx4 a[56:59], v11, s[44:47], s49 offen offset:2048 sc1\n\t"
      "v_mfma_f32_16x16x32_bf16 v[32:35], a[60:63], a[0:3], 0\n\t"
      "v_accvgpr_read_b32 v52, a172\n\t"
      "v_accvgpr_read_b32 v53, a173\n\t"
      "v_mfma_f32_16x16x32_bf16 v[32:35], a[68:71], a[4:7], v[32:35]\n\t"
      "v_accvgpr_read_b32 v54, a174\n\t"
      "v_accvgpr_read_b32 v55, a175\n\t"
      "v_mfma_f32_16x16x32_bf16 v[32:35], a[76:79], a[8:11], v[32:35]\n\t"
      "v_cvt_pk_bf16_f32 v48, v52, v53\n\t"
      "v_cvt_pk_bf16_f32 v49, v54, v55\n\t"
      "v_mfma_f32_16x16x32_bf16 v[32:35], a[84:87], a[12:15], v[32:35]\n\t"
      "ds_write_b64 v0, v[48:49] offset:12672\n\t"
      "v_mfma_f32_16x16x32_bf16 v[32:35], a[92:95], a[16:19], v[32:35]\n\t"
      "v_accvgpr_read_b32 v52, a176\n\t"
      "v_accvgpr_read_b32 v53, a177\n\t"
      "v_mfma_f32_16x16x32_bf16 v[32:35], a[100:103], a[20:23], v[32:35]\n\t"
      "v_accvgpr_read_b32 v54, a178\n\t"
      "v_accvgpr_read_b32 v55, a179\n\t"
      "v_mfma_f32_16x16x32_bf16 v[36:39], a[64:67], a[0:3], 0\n\t"
      "v_cvt_pk_bf16_f32 v50, v52, v53\n\t"
      "v_cvt_pk_bf16_f32 v51, v54, v55\n\t"
      "v_mfma_f32_16x16x32_bf16 v[36:39], a[72:75], a[4:7], v[36:39]\n\t"
      "ds_write_b64 v0, v[50:51] offset:12704\n\t"
      "v_mfma_f32_16x16x32_bf16 v[36:39], a[80:83], a[8:11], v[36:39]\n\t"
      "v_add_u32 v52, s51, v15\n\t"
      "v_xor_b32 v53, 64, v52\n\t"
      "v_mfma_f32_16x16x32_bf16 v[36:39], a[88:91], a[12:15], v[36:39]\n\t"
      "v_xor_b32 v54, 128, v52\n\t"
      "v_xor_b32 v55, 192, v52\n\t"
      "v_mfma_f32_16x16x32_bf16 v[36:39], a[96:99], a[16:19], v[36:39]\n\t"
      "s_waitcnt lgkmcnt(0)\n\t"
      "s_add_u32 s38, s36, s39\n\t"
      "s_sub_u32 s28, s36, s28\n\t"
      "s_sub_i32 s28, s28, 515\n\t"
      "s_max_i32 s28, s28, 0\n\t"
      "s_min_i32 s28, s28, 60\n\t"
      "s_and_b32 s39, s32, 15\n\t"
      "s_cselect_b32 s53, s29, 0\n\t"
      "s_memrealtime s[36:37]\n\t"
      "v_mfma_f32_16x16x32_bf16 v[36:39], a[104:107], a[20:23], v[36:39]\n\t"
      "s_add_u32 s39, s39, s53\n\t"
      "s_lshr_b64 s[32:33], s[32:33], 4\n\t"
      "s_add_u32 m0, s18, 86528\n\t"
      "v_mfma_f32_16x16x32_bf16 v[16:19], v[160:163], a[24:27], v[16:19]\n\t"
      "s_bitcmp1_b32 s91, 2\n\t"
      "s_cselect_b32 s54, 24576, 0\n\t"
      "s_add_u32 s54, s54, s14\n\t"
      "s_add_u32 s54, s54, 1572864\n\t"
      "v_mfma_f32_16x16x32_bf16 v[16:19], v[176:179], a[28:31], v[16:19]\n\t"
      "s_waitcnt vmcnt(6)\n\t"
      "s_barrier\n\t"
      "v_mfma_f32_16x16x32_bf16 v[16:19], v[192:195], a[32:35], v[16:19]\n\t"
      "ds_read_b128 a[0:3], v52 offset:0\n\t"
      "ds_read_b128 a[188:191], v8 offset:12544\n\t"
      "buffer_load_dwordx4 v5, s[40:43], s54 offen lds\n\t"
      "v_mfma_f32_16x16x32_bf16 v[16:19], v[208:211], a[36:39], v[16:19]\n\t"
      "ds_read_b128 a[4:7], v53 offset:0\n\t"
      "ds_read_b128 a[192:195], v60 offset:13568\n\t"
      "v_mfma_f32_16x16x32_bf16 v[16:19], v[224:227], a[40:43], v[16:19]\n\t"
      "ds_read_b128 a[8:11], v54 offset:0\n\t"
      "ds_read_b128 a[196:199], v61 offset:14592\n\t"
      "buffer_load_dwordx4 v6, s[40:43], s54 offen offset:1024 lds\n\t"
      "v_mfma_f32_16x16x32_bf16 v[16:19], v[240:243], a[44:47], v[16:19]\n\t"
      "ds_read_b128 a[12:15], v55 offset:0\n\t"
      "v_mfma_f32_16x16x32_bf16 v[20:23], v[164:167], a[24:27], v[20:23]\n\t"
      "ds_read_b128 a[16:19], v52 offset:256\n\t"
      "buffer_load_dwordx4 v7, s[40:43], s54 offen offset:2048 lds\n\t"
      "v_mfma_f32_16x16x32_bf16 v[20:23], v[180:183], a[28:31], v[20:23]\n\t"
      "ds_read_b128 a[20:23], v53 offset:256\n\t"
      "s_add_u32 m0, s18, s50\n\t"
      "v_mfma_f32_16x16x32_bf16 v[20:23], v[196:199], a[32:35], v[20:23]\n\t"
      "s_add_u32 s51, s51, 12288\n\t"
      "s_cmp_eq_u32 s51, 160256\n\t"
      "s_cselect_b32 s51, 25088, s51\n\t"
      "s_waitcnt lgkmcnt(7)\n\t"
      "v_mfma_f32_16x16x32_bf16 v[20:23], v[212:215], a[36:39], v[20:23]\n\t"
      "buffer_store_dwordx4 a[188:191], v12, s[44:47], s49 offen sc1\n\t"
      "v_mfma_f32_16x16x32_bf16 v[20:23], v[228:231], a[40:43], v[20:23]\n\t"
      "v_mfma_f32_16x16x32_bf16 v[20:23], v[244:247], a[44:47], v[20:23]\n\t"
      "s_waitcnt lgkmcnt(0)\n\t"
      "s_sub_u32 s53, s36, s38\n\t"
      "s_cmp_lt_i32 s53, 0\n\t"
      "v_mfma_f32_16x16x32_bf16 v[24:27], v[168:171], a[24:27], v[24:27]\n\t"
      "s_cbranch_scc1 .Lspinf_%=\n\t"
      ".Lspunf_%=:\n\t"
      "s_sub_i32 s53, s53, 30\n\t"
      "s_max_i32 s53, s53, 0\n\t"
      "v_mfma_f32_16x16x32_bf16 v[24:27], v[184:187], a[28:31], v[24:27]\n\t"
      "s_add_u32 s38, s38, s53\n\t"
      "s_add_u32 s38, s38, s39\n\t"
      "s_add_u32 s28, s28, s53\n\t"
      "v_mfma_f32_16x16x32_bf16 v[24:27], v[200:203], a[32:35], v[24:27]\n\t"
      "v_cvt_pk_bf16_f32 v40, v16, v17\n\t"
      "v_cvt_pk_bf16_f32 v41, v18, v19\n\t"
      "v_mfma_f32_16x16x32_bf16 v[24:27], v[216:219], a[36:39], v[24:27]\n\t"
      "ds_write_b64 v0, v[40:41] offset:0\n\t"
      "v_mfma_f32_16x16x32_bf16 v[24:27], v[232:235], a[40:43], v[24:27]\n\t"
      "buffer_load_dwordx4 v2, s[40:43], s48 offen lds\n\t"
      "v_mfma_f32_16x16x32_bf16 v[24:27], v[248:251], a[44:47], v[24:27]\n\t"
      "buffer_store_dwordx4 a[192:195], v13, s[44:47], s49 offen offset:1024 sc1\n\t"
      "v_mfma_f32_16x16x32_bf16 v[28:31], v[172:175], a[24:27], v[28:31]\n\t"
      "v_mfma_f32_16x16x32_bf16 v[28:31], v[188:191], a[28:31], v[28:31]\n\t"
      "v_cvt_pk_bf16_f32 v42, v20, v21\n\t"
      "v_cvt_pk_bf16_f32 v43, v22, v23\n\t"
      "v_mfma_f32_16x16x32_bf16 v[28:31], v[204:207], a[32:35], v[28:31]\n\t"
      "ds_write_b64 v0, v[42:43] offset:32\n\t"
      "v_mfma_f32_16x16x32_bf16 v[28:31], v[220:223], a[36:39], v[28:31]\n\t"
      "buffer_load_dwordx4 v3, s[40:43], s48 offen offset:1024 lds\n\t"
      "v_mfma_f32_16x16x32_bf16 v[28:31], v[236:239], a[40:43], v[28:31]\n\t"
      "buffer_store_dwordx4 a[196:199], v14, s[44:47], s49 offen offset:2048 sc1\n\t"
      "v_mfma_f32_16x16x32_bf16 v[28:31], v[252:255], a[44:47], v[28:31]\n\t"
      "s_add_u32 s58, s58, 786432\n\t"
      "s_lshr_b32 s59, s59, 1\n\t"
      "v_mfma_f32_16x16x32_bf16 v[32:35], a[108:111], a[24:27], v[32:35]\n\t"
      "s_bitcmp1_b32 s59, 0\n\t"
      "s_cselect_b32 s49, 24576, 0\n\t"
      "s_add_u32 s49, s49, s58\n\t"
      "v_mfma_f32_16x16x32_bf16 v[32:35], a[116:119], a[28:31], v[32:35]\n\t"
      "v_cvt_pk_bf16_f32 v44, v24, v25\n\t"
      "v_cvt_pk_bf16_f32 v45, v26, v27\n\t"
      "v_mfma_f32_16x16x32_bf16 v[32:35], a[124:127], a[32:35], v[32:35]\n\t"
      "ds_write_b64 v0, v[44:45] offset:64\n\t"
      "v_mfma_f32_16x16x32_bf16 v[32:35], a[132:135], a[36:39], v[32:35]\n\t"
      "buffer_load_dwordx4 v4, s[40:43], s48 offen offset:2048 lds\n\t"
      "v_mfma_f32_16x16x32_bf16 v[32:35], a[140:143], a[40:43], v[32:35]\n\t"
      "s_add_u32 s50, s50, 12288\n\t"
      "v_mfma_f32_16x16x32_bf16 v[32:35], a[148:151], a[44:47], v[32:35]\n\t"
      "v_cvt_pk_bf16_f32 v46, v28, v29\n\t"
      "v_cvt_pk_bf16_f32 v47, v30, v31\n\t"
      "v_mfma_f32_16x16x32_bf16 v[36:39], a[112:115], a[24:27], v[36:39]\n\t"
      "ds_write_b64 v0, v[46:47] offset:96\n\t"
      "v_mfma_f32_16x16x32_bf16 v[36:39], a[120:123], a[28:31], v[36:39]\n\t"
      "s_cmp_eq_u32 s50, 160256\n\t"
      "s_cselect_b32 s50, 25088, s50\n\t"
      "v_mfma_f32_16x16x32_bf16 v[36:39], a[128:131], a[32:35], v[36:39]\n\t"
      "v_mfma_f32_16x16x32_bf16 v[36:39], a[136:139], a[36:39], v[36:39]\n\t"
      "v_mfma_f32_16x16x32_bf16 v[36:39], a[144:147], a[40:43], v[36:39]\n\t"
      "v_cvt_pk_bf16_f32 v48, v32, v33\n\t"
      "v_cvt_pk_bf16_f32 v49, v34, v35\n\t"
      "v_mfma_f32_16x16x32_bf16 v[36:39], a[152:155], a[44:47], v[36:39]\n\t"
      "ds_write_b64 v0, v[48:49] offset:128\n\t"
      "s_cmp_eq_u32 s17, 6144\n\t"
      "s_cbranch_scc1 .Lmid1_%=\n\t"
      "s_cmp_eq_u32 s17, 12288\n\t"
      "s_cbranch_scc1 .Lmid2_%=\n\t"
      "s_cmp_eq_u32 s17, 18432\n\t"
      "s_cbranch_scc1 .Lmid3_%=\n\t"
      "global_load_dword v63, v62, s[12:13] sc1\n\t"
      "s_branch .Lmid0_%=\n\t"
      ".Lloop0_%=:\n\t"
      "v_mfma_f32_16x16x32_bf16 v[16:19], v[64:67], a[0:3], 0\n\t"
      "ds_read_b128 a[24:27], v58 offset:256\n\t"
      "v_mfma_f32_16x16x32_bf16 v[16:19], v[80:83], a[4:7], v[16:19]\n\t"
      "ds_read_b128 a[28:31], v59 offset:256\n\t"
      "v_mfma_f32_16x16x32_bf16 v[16:19], v[96:99], a[8:11], v[16:19]\n\t"
      "ds_read_b128 a[32:35], v56 offset:512\n\t"
      "v_mfma_f32_16x16x32_bf16 v[16:19], v[112:115], a[12:15], v[16:19]\n\t"
      "ds_read_b128 a[36:39], v57 offset:512\n\t"
      "v_mfma_f32_16x16x32_bf16 v[16:19], v[128:131], a[16:19], v[16:19]\n\t"
      "ds_read_b128 a[40:43], v58 offset:512\n\t"
      "v_mfma_f32_16x16x32_bf16 v[16:19], v[144:147], a[20:23], v[16:19]\n\t"
      "ds_read_b128 a[44:47], v59 offset:512\n\t"
      "v_mfma_f32_16x16x32_bf16 v[20:23], v[68:71], a[0:3], 0\n\t"
      "v_cvt_pk_bf16_f32 v50, v36, v37\n\t"
      "v_cvt_pk_bf16_f32 v51, v38, v39\n\t"
      "v_mfma_f32_16x16x32_bf16 v[20:23], v[84:87], a[4:7], v[20:23]\n\t"
      "ds_write_b64 v0, v[50:51] offset:12704\n\t"
      "v_mfma_f32_16x16x32_bf16 v[20:23], v[100:103], a[8:11], v[20:23]\n\t"
      "v_add_u32 v52, s51, v15\n\t"
      "v_xor_b32 v53, 64, v52\n\t"
      "v_mfma_f32_16x16x32_bf16 v[20:23], v[116:119], a[12:15], v[20:23]\n\t"
      "v_xor_b32 v54, 128, v52\n\t"
      "v_xor_b32 v55, 192, v52\n\t"
      "v_mfma_f32_16x16x32_bf16 v[20:23], v[132:135], a[16:19], v[20:23]\n\t"
      "s_add_u32 s56, s56, 786432\n\t"
      "s_lshr_b32 s57, s57, 1\n\t"
      "v_mfma_f32_16x16x32_bf16 v[20:23], v[148:151], a[20:23], v[20:23]\n\t"
      "s_bitcmp1_b32 s57, 0\n\t"
      "s_cselect_b32 s48, 24576, 0\n\t"
      "s_add_u32 s48, s48, s56\n\t"
      "v_mfma_f32_16x16x32_bf16 v[24:27], v[72:75], a[0:3], 0\n\t"
      "v_mfma_f32_16x16x32_bf16 v[24:27], v[88:91], a[4:7], v[24:27]\n\t"
      "v_mfma_f32_16x16x32_bf16 v[24:27], v[104:107], a[8:11], v[24:27]\n\t"
      "s_waitcnt vmcnt(12)\n\t"
      "s_waitcnt lgkmcnt(0)\n\t"
      "s_barrier\n\t"
      "v_mfma_f32_16x16x32_bf16 v[24:27], v[120:123], a[12:15], v[24:27]\n\t"
      "v_mfma_f32_16x16x32_bf16 v[24:27], v[136:139], a[16:19], v[24:27]\n\t"
      "v_mfma_f32_16x16x32_bf16 v[24:27], v[152:155], a[20:23], v[24:27]\n\t"
      "v_mfma_f32_16x16x32_bf16 v[28:31], v[76:79], a[0:3], 0\n\t"
      "ds_read_b128 a[48:51], v8 offset:12544\n\t"
      "v_mfma_f32_16x16x32_bf16 v[28:31], v[92:95], a[4:7], v[28:31]\n\t"
      "ds_read_b128 a[52:55], v60 offset:13568\n\t"
      "v_mfma_f32_16x16x32_bf16 v[28:31], v[108:111], a[8:11], v[28:31]\n\t"
      "ds_read_b128 a[56:59], v61 offset:14592\n\t"
      "v_mfma_f32_16x16x32_bf16 v[28:31], v[124:127], a[12:15], v[28:31]\n\t"
      "s_and_b32 s39, s32, 15\n\t"
      "s_cselect_b32 s53, s29, 0\n\t"
      "v_mfma_f32_16x16x32_bf16 v[28:31], v[140:143], a[16:19], v[28:31]\n\t"
      "s_add_u32 s39, s39, s53\n\t"
      "s_lshr_b64 s[32:33], s[32:33], 4\n\t"
      "v_mfma_f32_16x16x32_bf16 v[28:31], v[156:159], a[20:23], v[28:31]\n\t"
      "s_cmp_le_u32 s52, 12\n\t"
      "s_cselect_b32 s53, 3, 0\n\t"
      "s_min_u32 s53, s28, s53\n\t"
      "v_mfma_f32_16x16x32_bf16 v[32:35], a[60:63], a[0:3], 0\n\t"
      "s_sub_u32 s28, s28, s53\n\t"
      "s_sub_u32 s39, s39, s53\n\t"
      "v_mfma_f32_16x16x32_bf16 v[32:35], a[68:71], a[4:7], v[32:35]\n\t"
      "s_waitcnt lgkmcnt(2)\n\t"
      "v_mfma_f32_16x16x32_bf16 v[32:35], a[76:79], a[8:11], v[32:35]\n\t"
      "buffer_store_dwordx4 a[48:51], v12, s[44:47], s49 offen sc1\n\t"
      "v_mfma_f32_16x16x32_bf16 v[32:35], a[84:87], a[12:15], v[32:35]\n\t"
      "v_mfma_f32_16x16x32_bf16 v[32:35], a[92:95], a[16:19], v[32:35]\n\t"
      "v_mfma_f32_16x16x32_bf16 v[32:35], a[100:103], a[20:23], v[32:35]\n\t"
      "s_add_u32 m0, s18, s50\n\t"
      "v_mfma_f32_16x16x32_bf16 v[36:39], a[64:67], a[0:3], 0\n\t"
      "buffer_load_dwordx4 v2, s[40:43], s48 offen nt lds\n\t"
      "v_mfma_f32_16x16x32_bf16 v[36:39], a[72:75], a[4:7], v[36:39]\n\t"
      "s_memrealtime s[36:37]\n\t"
      "v_mfma_f32_16x16x32_bf16 v[36:39], a[80:83], a[8:11], v[36:39]\n\t"
      "v_mfma_f32_16x16x32_bf16 v[36:39], a[88:91], a[12:15], v[36:39]\n\t"
      "s_waitcnt lgkmcnt(1)\n\t"
      "v_mfma_f32_16x16x32_bf16 v[36:39], a[96:99], a[16:19], v[36:39]\n\t"
      "buffer_store_dwordx4 a[52:55], v13, s[44:47], s49 offen offset:1024 sc1\n\t"
      "v_mfma_f32_16x16x32_bf16 v[36:39], a[104:107], a[20:23], v[36:39]\n\t"
      "v_mfma_f32_16x16x32_bf16 v[16:19], v[160:163], a[24:27], v[16:19]\n\t"
      "ds_read_b128 a[0:3], v52 offset:0\n\t"
      "v_mfma_f32_16x16x32_bf16 v[16:19], v[176:179], a[28:31], v[16:19]\n\t"
      "ds_read_b128 a[4:7], v53 offset:0\n\t"
      "v_mfma_f32_16x16x32_bf16 v[16:19], v[192:195], a[32:35], v[16:19]\n\t"
      "ds_read_b128 a[8:11], v54 offset:0\n\t"
      "v_mfma_f32_16x16x32_bf16 v[16:19], v[208:211], a[36:39], v[16:19]\n\t"
      "ds_read_b128 a[12:15], v55 offset:0\n\t"
      "v_mfma_f32_16x16x32_bf16 v[16:19], v[224:227], a[40:43], v[16:19]\n\t"
      "ds_read_b128 a[16:19], v52 offset:256\n\t"
      "v_mfma_f32_16x16x32_bf16 v[16:19], v[240:243], a[44:47], v[16:19]\n\t"
      "ds_read_b128 a[20:23], v53 offset:256\n\t"
      "v_mfma_f32_16x16x32_bf16 v[20:23], v[164:167], a[24:27], v[20:23]\n\t"
      "s_add_u32 s51, s51, 12288\n\t"
      "s_cmp_eq_u32 s51, 160256\n\t"
      "s_cselect_b32 s51, 25088, s51\n\t"
      "v_mfma_f32_16x16x32_bf16 v[20:23], v[180:183], a[28:31], v[20:23]\n\t"
      "s_waitcnt lgkmcnt(6)\n\t"
      "v_mfma_f32_16x16x32_bf16 v[20:23], v[196:199], a[32:35], v[20:23]\n\t"
      "buffer_store_dwordx4 a[56:59], v14, s[44:47], s49 offen offset:2048 sc1\n\t"
      "v_mfma_f32_16x16x32_bf16 v[20:23], v[212:215], a[36:39], v[20:23]\n\t"
      "v_mfma_f32_16x16x32_bf16 v[20:23], v[228:231], a[40:43], v[20:23]\n\t"
      "s_waitcnt lgkmcnt(0)\n\t"
      "s_sub_u32 s53, s36, s38\n\t"
      "s_cmp_lt_i32 s53, 0\n\t"
      "v_mfma_f32_16x16x32_bf16 v[20:23], v[244:247], a[44:47], v[20:23]\n\t"
      "s_cbranch_scc1 .Lspin0_%=\n\t"
      ".Lspun0_%=:\n\t"
      "s_sub_i32 s53, s53, 30\n\t"
      "s_max_i32 s53, s53, 0\n\t"
      "v_mfma_f32_16x16x32_bf16 v[24:27], v[168:171], a[24:27], v[24:27]\n\t"
      "s_add_u32 s38, s38, s53\n\t"
      "s_add_u32 s38, s38, s39\n\t"
      "s_add_u32 s28, s28, s53\n\t"
      "v_mfma_f32_16x16x32_bf16 v[24:27], v[184:187], a[28:31], v[24:27]\n\t"
      "v_cvt_pk_bf16_f32 v40, v16, v17\n\t"
      "v_cvt_pk_bf16_f32 v41, v18, v19\n\t"
      "v_mfma_f32_16x16x32_bf16 v[24:27], v[200:203], a[32:35], v[24:27]\n\t"
      "ds_write_b64 v0, v[40:41] offset:0\n\t"
      "v_mfma_f32_16x16x32_bf16 v[24:27], v[216:219], a[36:39], v[24:27]\n\t"
      "buffer_load_dwordx4 v3, s[40:43], s48 offen offset:1024 nt lds\n\t"
      "v_mfma_f32_16x16x32_bf16 v[24:27], v[232:235], a[40:43], v[24:27]\n\t"
      "s_add_u32 s58, s58, 786432\n\t"
      "s_lshr_b32 s59, s59, 1\n\t"
      "v_mfma_f32_16x16x32_bf16 v[24:27], v[248:251], a[44:47], v[24:27]\n\t"
      "s_bitcmp1_b32 s59, 0\n\t"
      "s_cselect_b32 s49, 24576, 0\n\t"
      "s_add_u32 s49, s49, s58\n\t"
      "v_mfma_f32_16x16x32_bf16 v[28:31], v[172:175], a[24:27], v[28:31]\n\t"
      "v_mfma_f32_16x16x32_bf16 v[28:31], v[188:191], a[28:31], v[28:31]\n\t"
      "v_cvt_pk_bf16_f32 v42, v20, v21\n\t"
      "v_cvt_pk_bf16_f32 v43, v22, v23\n\t"
      "v_mfma_f32_16x16x32_bf16 v[28:31], v[204:207], a[32:35], v[28:31]\n\t"
      "ds_write_b64 v0, v[42:43] offset:32\n\t"
      "v_mfma_f32_16x16x32_bf16 v[28:31], v[220:223], a[36:39], v[28:31]\n\t"
      "v_mfma_f32_16x16x32_bf16 v[28:31], v[236:239], a[40:43], v[28:31]\n\t"
      "v_mfma_f32_16x16x32_bf16 v[28:31], v[252:255], a[44:47], v[28:31]\n\t"
      "s_cmp_eq_u32 s52, 14\n\t"
      "s_cbranch_scc1 .Lshb_%=\n\t"
      ".Lshbr_%=:\n\t"
      "v_mfma_f32_16x16x32_bf16 v[32:35], a[108:111], a[24:27], v[32:35]\n\t"
      "v_mfma_f32_16x16x32_bf16 v[32:35], a[116:119], a[28:31], v[32:35]\n\t"
      "v_cvt_pk_bf16_f32 v44, v24, v25\n\t"
      "v_cvt_pk_bf16_f32 v45, v26, v27\n\t"
      "v_mfma_f32_16x16x32_bf16 v[32:35], a[124:127], a[32:35], v[32:35]\n\t"
      "ds_write_b64 v0, v[44:45] offset:64\n\t"
      "v_mfma_f32_16x16x32_bf16 v[32:35], a[132:135], a[36:39], v[32:35]\n\t"
      "buffer_load_dwordx4 v4, s[40:43], s48 offen offset:2048 nt lds\n\t"
      "v_mfma_f32_16x16x32_bf16 v[32:35], a[140:143], a[40:43], v[32:35]\n\t"
      "s_add_u32 s50, s50, 12288\n\t"
      "v_mfma_f32_16x16x32_bf16 v[32:35], a[148:151], a[44:47], v[32:35]\n\t"
      "v_cvt_pk_bf16_f32 v46, v28, v29\n\t"
      "v_cvt_pk_bf16_f32 v47, v30, v31\n\t"
      "v_mfma_f32_16x16x32_bf16 v[36:39], a[112:115], a[24:27], v[36:39]\n\t"
      "ds_write_b64 v0, v[46:47] offset:96\n\t"
      "v_mfma_f32_16x16x32_bf16 v[36:39], a[120:123], a[28:31], v[36:39]\n\t"
      "s_cmp_eq_u32 s50, 160256\n\t"
      "s_cselect_b32 s50, 25088, s50\n\t"
      "v_mfma_f32_16x16x32_bf16 v[36:39], a[128:131], a[32:35], v[36:39]\n\t"
      "v_mfma_f32_16x16x32_bf16 v[36:39], a[136:139], a[36:39], v[36:39]\n\t"
      "v_mfma_f32_16x16x32_bf16 v[36:39], a[144:147], a[40:43], v[36:39]\n\t"
      "v_cvt_pk_bf16_f32 v48, v32, v33\n\t"
      "v_cvt_pk_bf16_f32 v49, v34, v35\n\t"
      "v_mfma_f32_16x16x32_bf16 v[36:39], a[152:155], a[44:47], v[36:39]\n\t"
      "ds_write_b64 v0, v[48:49] offset:128\n\t"
      ".Lmid0_%=:\n\t"
      "v_mfma_f32_16x16x32_bf16 v[16:19], v[64:67], a[0:3], 0\n\t"
      "ds_read_b128 a[24:27], v54 offset:256\n\t"
      "v_mfma_f32_16x16x32_bf16 v[16:19], v[80:83], a[4:7], v[16:19]\n\t"
      "ds_read_b128 a[28:31], v55 offset:256\n\t"
      "v_mfma_f32_16x16x32_bf16 v[16:19], v[96:99], a[8:11], v[16:19]\n\t"
      "ds_read_b128 a[32:35], v52 offset:512\n\t"
      "v_mfma_f32_16x16x32_bf16 v[16:19], v[112:115], a[12:15], v[16:19]\n\t"
      "ds_read_b128 a[36:39], v53 offset:512\n\t"
      "v_mfma_f32_16x16x32_bf16 v[16:19], v[128:131], a[16:19], v[16:19]\n\t"
      "ds_read_b128 a[40:43], v54 offset:512\n\t"
      "v_mfma_f32_16x16x32_bf16 v[16:19], v[144:147], a[20:23], v[16:19]\n\t"
      "ds_read_b128 a[44:47], v55 offset:512\n\t"
      "v_mfma_f32_16x16x32_bf16 v[20:23], v[68:71], a[0:3], 0\n\t"
      "v_cvt_pk_bf16_f32 v50, v36, v37\n\t"
      "v_cvt_pk_bf16_f32 v51, v38, v39\n\t"
      "v_mfma_f32_16x16x32_bf16 v[20:23], v[84:87], a[4:7], v[20:23]\n\t"
      "ds_write_b64 v0, v[50:51] offset:160\n\t"
      "v_mfma_f32_16x16x32_bf16 v[20:23], v[100:103], a[8:11], v[20:23]\n\t"
      "v_add_u32 v56, s51, v15\n\t"
      "v_xor_b32 v57, 64, v56\n\t"
      "v_mfma_f32_16x16x32_bf16 v[20:23], v[116:119], a[12:15], v[20:23]\n\t"
      "v_xor_b32 v58, 128, v56\n\t"
      "v_xor_b32 v59, 192, v56\n\t"
      "v_mfma_f32_16x16x32_bf16 v[20:23], v[132:135], a[16:19], v[20:23]\n\t"
      "v_mfma_f32_16x16x32_bf16 v[20:23], v[148:151], a[20:23], v[20:23]\n\t"
      "v_mfma_f32_16x16x32_bf16 v[24:27], v[72:75], a[0:3], 0\n\t"
      "v_mfma_f32_16x16x32_bf16 v[24:27], v[88:91], a[4:7], v[24:27]\n\t"
      "v_mfma_f32_16x16x32_bf16 v[24:27], v[104:107], a[8:11], v[24:27]\n\t"
      "s_waitcnt vmcnt(12)\n\t"
      "s_waitcnt lgkmcnt(0)\n\t"
      "s_barrier\n\t"
      "v_mfma_f32_16x16x32_bf16 v[24:27], v[120:123], a[12:15], v[24:27]\n\t"
      "v_mfma_f32_16x16x32_bf16 v[24:27], v[136:139], a[16:19], v[24:27]\n\t"
      "v_mfma_f32_16x16x32_bf16 v[24:27], v[152:155], a[20:23], v[24:27]\n\t"
      "v_mfma_f32_16x16x32_bf16 v[28:31], v[76:79], a[0:3], 0\n\t"
      "ds_read_b128 a[48:51], v8 offset:0\n\t"
      "v_mfma_f32_16x16x32_bf16 v[28:31], v[92:95], a[4:7], v[28:31]\n\t"
      "ds_read_b128 a[52:55], v60 offset:1024\n\t"
      "v_mfma_f32_16x16x32_bf16 v[28:31], v[108:111], a[8:11], v[28:31]\n\t"
      "ds_read_b128 a[56:59], v61 offset:2048\n\t"
      "v_mfma_f32_16x16x32_bf16 v[28:31], v[124:127], a[12:15], v[28:31]\n\t"
      "s_and_b32 s39, s34, 15\n\t"
      "s_cselect_b32 s53, s29, 0\n\t"
      "v_mfma_f32_16x16x32_bf16 v[28:31], v[140:143], a[16:19], v[28:31]\n\t"
      "s_add_u32 s39, s39, s53\n\t"
      "s_lshr_b64 s[34:35], s[34:35], 4\n\t"
      "v_mfma_f32_16x16x32_bf16 v[28:31], v[156:159], a[20:23], v[28:31]\n\t"
      "s_cmp_le_u32 s52, 12\n\t"
      "s_cselect_b32 s53, 3, 0\n\t"
      "s_min_u32 s53, s28, s53\n\t"
      "v_mfma_f32_16x16x32_bf16 v[32:35], a[60:63], a[0:3], 0\n\t"
      "s_sub_u32 s28, s28, s53\n\t"
      "s_sub_u32 s39, s39, s53\n\t"
      "v_mfma_f32_16x16x32_bf16 v[32:35], a[68:71], a[4:7], v[32:35]\n\t"
      "s_waitcnt lgkmcnt(2)\n\t"
      "v_mfma_f32_16x16x32_bf16 v[32:35], a[76:79], a[8:11], v[32:35]\n\t"
      "buffer_store_dwordx4 a[48:51], v9, s[44:47], s49 offen sc1\n\t"
      "v_mfma_f32_16x16x32_bf16 v[32:35], a[84:87], a[12:15], v[32:35]\n\t"
      "v_mfma_f32_16x16x32_bf16 v[32:35], a[92:95], a[16:19], v[32:35]\n\t"
      "v_mfma_f32_16x16x32_bf16 v[32:35], a[100:103], a[20:23], v[32:35]\n\t"
      "s_add_u32 m0, s18, s50\n\t"
      "v_mfma_f32_16x16x32_bf16 v[36:39], a[64:67], a[0:3], 0\n\t"
      "buffer_load_dwordx4 v5, s[40:43], s48 offen nt lds\n\t"
      "v_mfma_f32_16x16x32_bf16 v[36:39], a[72:75], a[4:7], v[36:39]\n\t"
      "s_memrealtime s[36:37]\n\t"
      "v_mfma_f32_16x16x32_bf16 v[36:39], a[80:83], a[8:11], v[36:39]\n\t"
      "v_mfma_f32_16x16x32_bf16 v[36:39], a[88:91], a[12:15], v[36:39]\n\t"
      "s_waitcnt lgkmcnt(1)\n\t"
      "v_mfma_f32_16x16x32_bf16 v[36:39], a[96:99], a[16:19], v[36:39]\n\t"
      "buffer_store_dwordx4 a[52:55], v10, s[44:47], s49 offen offset:1024 sc1\n\t"
      "v_mfma_f32_16x16x32_bf16 v[36:39], a[104:107], a[20:23], v[36:39]\n\t"
      "v_mfma_f32_16x16x32_bf16 v[16:19], v[160:163], a[24:27], v[16:19]\n\t"
      "ds_read_b128 a[0:3], v56 offset:0\n\t"
      "v_mfma_f32_16x16x32_bf16 v[16:19], v[176:179], a[28:31], v[16:19]\n\t"
      "ds_read_b128 a[4:7], v57 offset:0\n\t"
      "v_mfma_f32_16x16x32_bf16 v[16:19], v[192:195], a[32:35], v[16:19]\n\t"
      "ds_read_b128 a[8:11], v58 offset:0\n\t"
      "v_mfma_f32_16x16x32_bf16 v[16:19], v[208:211], a[36:39], v[16:19]\n\t"
      "ds_read_b128 a[12:15], v59 offset:0\n\t"
      "v_mfma_f32_16x16x32_bf16 v[16:19], v[224:227], a[40:43], v[16:19]\n\t"
      "ds_read_b128 a[16:19], v56 offset:256\n\t"
      "v_mfma_f32_16x16x32_bf16 v[16:19], v[240:243], a[44:47], v[16:19]\n\t"
      "ds_read_b128 a[20:23], v57 offset:256\n\t"
      "v_mfma_f32_16x16x32_bf16 v[20:23], v[164:167], a[24:27], v[20:23]\n\t"
      "s_add_u32 s51, s51, 12288\n\t"
      "s_cmp_eq_u32 s51, 160256\n\t"
      "s_cselect_b32 s51, 25088, s51\n\t"
      "v_mfma_f32_16x16x32_bf16 v[20:23], v[180:183], a[28:31], v[20:23]\n\t"
      "s_waitcnt lgkmcnt(6)\n\t"
      "v_mfma_f32_16x16x32_bf16 v[20:23], v[196:199], a[32:35], v[20:23]\n\t"
      "buffer_store_dwordx4 a[56:59], v11, s[44:47], s49 offen offset:2048 sc1\n\t"
      "v_mfma_f32_16x16x32_bf16 v[20:23], v[212:215], a[36:39], v[20:23]\n\t"
      "v_mfma_f32_16x16x32_bf16 v[20:23], v[228:231], a[40:43], v[20:23]\n\t"
      "s_waitcnt lgkmcnt(0)\n\t"
      "s_sub_u32 s53, s36, s38\n\t"
      "s_cmp_lt_i32 s53, 0\n\t"
      "v_mfma_f32_16x16x32_bf16 v[20:23], v[244:247], a[44:47], v[20:23]\n\t"
      "s_cbranch_scc1 .Lspin1_%=\n\t"
      ".Lspun1_%=:\n\t"
      "s_sub_i32 s53, s53, 30\n\t"
      "s_max_i32 s53, s53, 0\n\t"
      "v_mfma_f32_16x16x32_bf16 v[24:27], v[168:171], a[24:27], v[24:27]\n\t"
      "s_add_u32 s38, s38, s53\n\t"
      "s_add_u32 s38, s38, s39\n\t"
      "s_add_u32 s28, s28, s53\n\t"
      "v_mfma_f32_16x16x32_bf16 v[24:27], v[184:187], a[28:31], v[24:27]\n\t"
      "v_cvt_pk_bf16_f32 v40, v16, v17\n\t"
      "v_cvt_pk_bf16_f32 v41, v18, v19\n\t"
      "v_mfma_f32_16x16x32_bf16 v[24:27], v[200:203], a[32:35], v[24:27]\n\t"
      "ds_write_b64 v0, v[40:41] offset:12544\n\t"
      "v_mfma_f32_16x16x32_bf16 v[24:27], v[216:219], a[36:39], v[24:27]\n\t"
      "buffer_load_dwordx4 v6, s[40:43], s48 offen offset:1024 nt lds\n\t"
      "v_mfma_f32_16x16x32_bf16 v[24:27], v[232:235], a[40:43], v[24:27]\n\t"
      "v_mfma_f32_16x16x32_bf16 v[24:27], v[248:251], a[44:47], v[24:27]\n\t"
      "v_mfma_f32_16x16x32_bf16 v[28:31], v[172:175], a[24:27], v[28:31]\n\t"
      "v_mfma_f32_16x16x32_bf16 v[28:31], v[188:191], a[28:31], v[28:31]\n\t"
      "v_cvt_pk_bf16_f32 v42, v20, v21\n\t"
      "v_cvt_pk_bf16_f32 v43, v22, v23\n\t"
      "v_mfma_f32_16x16x32_bf16 v[28:31], v[204:207], a[32:35], v[28:31]\n\t"
      "ds_write_b64 v0, v[42:43] offset:12576\n\t"
      "v_mfma_f32_16x16x32_bf16 v[28:31], v[220:223], a[36:39], v[28:31]\n\t"
      "v_mfma_f32_16x16x32_bf16 v[28:31], v[236:239], a[40:43], v[28:31]\n\t"
      "v_mfma_f32_16x16x32_bf16 v[28:31], v[252:255], a[44:47], v[28:31]\n\t"
      "v_mfma_f32_16x16x32_bf16 v[32:35], a[108:111], a[24:27], v[32:35]\n\t"
      "v_mfma_f32_16x16x32_bf16 v[32:35], a[116:119], a[28:31], v[32:35]\n\t"
      "v_cvt_pk_bf16_f32 v44, v24, v25\n\t"
      "v_cvt_pk_bf16_f32 v45, v26, v27\n\t"
      "v_mfma_f32_16x16x32_bf16 v[32:35], a[124:127], a[32:35], v[32:35]\n\t"
      "ds_write_b64 v0, v[44:45] offset:12608\n\t"
      "v_mfma_f32_16x16x32_bf16 v[32:35], a[132:135], a[36:39], v[32:35]\n\t"
      "buffer_load_dwordx4 v7, s[40:43], s48 offen offset:2048 nt lds\n\t"
      "v_mfma_f32_16x16x32_bf16 v[32:35], a[140:143], a[40:43], v[32:35]\n\t"
      "s_add_u32 s50, s50, 12288\n\t"
      "v_mfma_f32_16x16x32_bf16 v[32:35], a[148:151], a[44:47], v[32:35]\n\t"
      "v_cvt_pk_bf16_f32 v46, v28, v29\n\t"
      "v_cvt_pk_bf16_f32 v47, v30, v31\n\t"
      "v_mfma_f32_16x16x32_bf16 v[36:39], a[112:115], a[24:27], v[36:39]\n\t"
      "ds_write_b64 v0, v[46:47] offset:12640\n\t"
      "v_mfma_f32_16x16x32_bf16 v[36:39], a[120:123], a[28:31], v[36:39]\n\t"
      "s_cmp_eq_u32 s50, 160256\n\t"
      "s_cselect_b32 s50, 25088, s50\n\t"
      "v_mfma_f32_16x16x32_bf16 v[36:39], a[128:131], a[32:35], v[36:39]\n\t"
      "s_sub_u32 s52, s52, 1\n\t"
      "s_cmp_lg_u32 s52, 0\n\t"
      "v_mfma_f32_16x16x32_bf16 v[36:39], a[136:139], a[36:39], v[36:39]\n\t"
      "v_mfma_f32_16x16x32_bf16 v[36:39], a[144:147], a[40:43], v[36:39]\n\t"
      "v_cvt_pk_bf16_f32 v48, v32, v33\n\t"
      "v_cvt_pk_bf16_f32 v49, v34, v35\n\t"
      "v_mfma_f32_16x16x32_bf16 v[36:39], a[152:155], a[44:47], v[36:39]\n\t"
      "ds_write_b64 v0, v[48:49] offset:12672\n\t"
      "s_cbranch_scc1 .Lloop0_%=\n\t"
      "s_branch .Lendc_%=\n\t"
      ".Lshb_%=:\n\t"
      "s_waitcnt vmcnt(10)\n\t"
      "v_bfe_u32 v63, v63, 17, 8\n\t"
      "v_min_u32 v62, 1, v63\n\t"
      "v_lshl_add_u32 v63, v62, 16, v63\n\t"
      "s_nop 1\n\t"
      "v_add_u32_dpp v63, v63, v63 row_shr:1 row_mask:0xf bank_mask:0xf bound_ctrl:0\n\t"
      "s_nop 1\n\t"
      "v_add_u32_dpp v63, v63, v63 row_shr:2 row_mask:0xf bank_mask:0xf bound_ctrl:0\n\t"
      "s_nop 1\n\t"
      "v_add_u32_dpp v63, v63, v63 row_shr:4 row_mask:0xf bank_mask:0xf bound_ctrl:0\n\t"
      "s_nop 1\n\t"
      "v_add_u32_dpp v63, v63, v63 row_shr:8 row_mask:0xf bank_mask:0xf bound_ctrl:0\n\t"
      "s_nop 0\n\t"
      "v_readlane_b32 s64, v63, 15\n\t"
      "v_readlane_b32 s65, v63, 31\n\t"
      "s_nop 1\n\t"
      "s_add_u32 s64, s64, s65\n\t"
      "s_lshr_b32 s65, s64, 16\n\t"
      "s_and_b32 s64, s64, 0xffff\n\t"
      "s_mov_b32 s66, 0\n\t"
      "s_mul_i32 s67, s65, 934\n\t"
      "s_lshl_b32 s69, s64, 3\n\t"
      "s_cmp_lt_u32 s69, s67\n\t"
      "s_cselect_b32 s66, 3, s66\n\t"
      "s_add_u32 s29, s29, s66\n\t"
      "s_mul_i32 s67, s65, 737\n\t"
      "s_cmp_lt_u32 s69, s67\n\t"
      "s_cbranch_scc0 .Lshbr_%=\n\t"
      "s_movk_i32 s29, 79\n\t"
      "s_mov_b32 s32, 0x11111111\n\t"
      "s_mov_b32 s33, 0x11111111\n\t"
      "s_mov_b32 s34, 0x11111111\n\t"
      "s_mov_b32 s35, 0x11111111\n\t"
      "s_sub_i32 s28, s28, 30\n\t"
      "s_min_i32 s66, s28, 0\n\t"
      "s_sub_u32 s38, s38, s66\n\t"
      "s_max_i32 s28, s28, 0\n\t"
      "s_branch .Lshbr_%=\n\t"
      ".Lloop1_%=:\n\t"
      "v_mfma_f32_16x16x32_bf16 v[16:19], v[64:67], a[0:3], 0\n\t"
      "ds_read_b128 a[24:27], v58 offset:256\n\t"
      "v_mfma_f32_16x16x32_bf16 v[16:19], v[80:83], a[4:7], v[16:19]\n\t"
      "ds_read_b128 a[28:31], v59 offset:256\n\t"
      "v_mfma_f32_16x16x32_bf16 v[16:19], v[96:99], a[8:11], v[16:19]\n\t"
      "ds_read_b128 a[32:35], v56 offset:512\n\t"
      "v_mfma_f32_16x16x32_bf16 v[16:19], v[112:115], a[12:15], v[16:19]\n\t"
      "ds_read_b128 a[36:39], v57 offset:512\n\t"
      "v_mfma_f32_16x16x32_bf16 v[16:19], v[128:131], a[16:19], v[16:19]\n\t"
      "ds_read_b128 a[40:43], v58 offset:512\n\t"
      "v_mfma_f32_16x16x32_bf16 v[16:19], v[144:147], a[20:23], v[16:19]\n\t"
      "ds_read_b128 a[44:47], v59 offset:512\n\t"
      "v_mfma_f32_16x16x32_bf16 v[20:23], v[68:71], a[0:3], 0\n\t"
      "v_cvt_pk_bf16_f32 v50, v36, v37\n\t"
      "v_cvt_pk_bf16_f32 v51, v38, v39\n\t"
      "v_mfma_f32_16x16x32_bf16 v[20:23], v[84:87], a[4:7], v[20:23]\n\t"
      "ds_write_b64 v0, v[50:51] offset:12704\n\t"
      "v_mfma_f32_16x16x32_bf16 v[20:23], v[100:103], a[8:11], v[20:23]\n\t"
      "v_add_u32 v52, s51, v15\n\t"
      "v_xor_b32 v53, 64, v52\n\t"
      "v_mfma_f32_16x16x32_bf16 v[20:23], v[116:119], a[12:15], v[20:23]\n\t"
      "v_xor_b32 v54, 128, v52\n\t"
      "v_xor_b32 v55, 192, v52\n\t"
      "v_mfma_f32_16x16x32_bf16 v[20:23], v[132:135], a[16:19], v[20:23]\n\t"
      "s_add_u32 s56, s56, 786432\n\t"
      "s_lshr_b32 s57, s57, 1\n\t"
      "v_mfma_f32_16x16x32_bf16 v[20:23], v[148:151], a[20:23], v[20:23]\n\t"
      "s_bitcmp1_b32 s57, 0\n\t"
      "s_cselect_b32 s48, 24576, 0\n\t"
      "s_add_u32 s48, s48, s56\n\t"
      "v_mfma_f32_16x16x32_bf16 v[24:27], v[72:75], a[0:3], 0\n\t"
      "v_mfma_f32_16x16x32_bf16 v[24:27], v[88:91], a[4:7], v[24:27]\n\t"
      "v_mfma_f32_16x16x32_bf16 v[24:27], v[104:107], a[8:11], v[24:27]\n\t"
      "v_mfma_f32_16x16x32_bf16 v[24:27], v[120:123], a[12:15], v[24:27]\n\t"
      "s_waitcnt vmcnt(12)\n\t"
      "s_waitcnt lgkmcnt(0)\n\t"
      "s_barrier\n\t"
      "v_mfma_f32_16x16x32_bf16 v[24:27], v[136:139], a[16:19], v[24:27]\n\t"
      "v_mfma_f32_16x16x32_bf16 v[24:27], v[152:155], a[20:23], v[24:27]\n\t"
      "v_mfma_f32_16x16x32_bf16 v[28:31], v[76:79], a[0:3], 0\n\t"
      "ds_read_b128 a[48:51], v8 offset:12544\n\t"
      "v_mfma_f32_16x16x32_bf16 v[28:31], v[92:95], a[4:7], v[28:31]\n\t"
      "ds_read_b128 a[52:55], v60 offset:13568\n\t"
      "v_mfma_f32_16x16x32_bf16 v[28:31], v[108:111], a[8:11], v[28:31]\n\t"
      "ds_read_b128 a[56:59], v61 offset:14592\n\t"
      "v_mfma_f32_16x16x32_bf16 v[28:31], v[124:127], a[12:15], v[28:31]\n\t"
      "v_mfma_f32_16x16x32_bf16 v[28:31], v[140:143], a[16:19], v[28:31]\n\t"
      "v_mfma_f32_16x16x32_bf16 v[28:31], v[156:159], a[20:23], v[28:31]\n\t"
      "v_mfma_f32_16x16x32_bf16 v[32:35], a[60:63], a[0:3], 0\n\t"
      "v_mfma_f32_16x16x32_bf16 v[32:35], a[68:71], a[4:7], v[32:35]\n\t"
      "s_waitcnt lgkmcnt(2)\n\t"
      "v_mfma_f32_16x16x32_bf16 v[32:35], a[76:79], a[8:11], v[32:35]\n\t"
      "buffer_store_dwordx4 a[48:51], v12, s[44:47], s49 offen sc1\n\t"
      "v_mfma_f32_16x16x32_bf16 v[32:35], a[84:87], a[12:15], v[32:35]\n\t"
      "v_mfma_f32_16x16x32_bf16 v[32:35], a[92:95], a[16:19], v[32:35]\n\t"
      "v_mfma_f32_16x16x32_bf16 v[32:35], a[100:103], a[20:23], v[32:35]\n\t"
      "s_add_u32 m0, s18, s50\n\t"
      "v_mfma_f32_16x16x32_bf16 v[36:39], a[64:67], a[0:3], 0\n\t"
      "buffer_load_dwordx4 v2, s[40:43], s48 offen nt lds\n\t"
      "v_mfma_f32_16x16x32_bf16 v[36:39], a[72:75], a[4:7], v[36:39]\n\t"
      "v_mfma_f32_16x16x32_bf16 v[36:39], a[80:83], a[8:11], v[36:39]\n\t"
      "v_mfma_f32_16x16x32_bf16 v[36:39], a[88:91], a[12:15], v[36:39]\n\t"
      "s_waitcnt lgkmcnt(1)\n\t"
      "v_mfma_f32_16x16x32_bf16 v[36:39], a[96:99], a[16:19], v[36:39]\n\t"
      "buffer_store_dwordx4 a[52:55], v13, s[44:47], s49 offen offset:1024 sc1\n\t"
      "v_mfma_f32_16x16x32_bf16 v[36:39], a[104:107], a[20:23], v[36:39]\n\t"
      "v_mfma_f32_16x16x32_bf16 v[16:19], v[160:163], a[24:27], v[16:19]\n\t"
      "ds_read_b128 a[0:3], v52 offset:0\n\t"
      "v_mfma_f32_16x16x32_bf16 v[16:19], v[176:179], a[28:31], v[16:19]\n\t"
      "ds_read_b128 a[4:7], v53 offset:0\n\t"
      "v_mfma_f32_16x16x32_bf16 v[16:19], v[192:195], a[32:35], v[16:19]\n\t"
      "ds_read_b128 a[8:11], v54 offset:0\n\t"
      "v_mfma_f32_16x16x32_bf16 v[16:19], v[208:211], a[36:39], v[16:19]\n\t"
      "ds_read_b128 a[12:15], v55 offset:0\n\t"
      "v_mfma_f32_16x16x32_bf16 v[16:19], v[224:227], a[40:43], v[16:19]\n\t"
      "ds_read_b128 a[16:19], v52 offset:256\n\t"
      "v_mfma_f32_16x16x32_bf16 v[16:19], v[240:243], a[44:47], v[16:19]\n\t"
      "ds_read_b128 a[20:23], v53 offset:256\n\t"
      "v_mfma_f32_16x16x32_bf16 v[20:23], v[164:167], a[24:27], v[20:23]\n\t"
      "s_add_u32 s51, s51, 12288\n\t"
      "s_cmp_eq_u32 s51, 160256\n\t"
      "s_cselect_b32 s51, 25088, s51\n\t"
      "v_mfma_f32_16x16x32_bf16 v[20:23], v[180:183], a[28:31], v[20:23]\n\t"
      "s_waitcnt lgkmcnt(6)\n\t"
      "v_mfma_f32_16x16x32_bf16 v[20:23], v[196:199], a[32:35], v[20:23]\n\t"
      "buffer_store_dwordx4 a[56:59], v14, s[44:47], s49 offen offset:2048 sc1\n\t"
      "v_mfma_f32_16x16x32_bf16 v[20:23], v[212:215], a[36:39], v[20:23]\n\t"
      "v_mfma_f32_16x16x32_bf16 v[20:23], v[228:231], a[40:43], v[20:23]\n\t"
      "s_waitcnt lgkmcnt(0)\n\t"
      "v_mfma_f32_16x16x32_bf16 v[20:23], v[244:247], a[44:47], v[20:23]\n\t"
      "v_mfma_f32_16x16x32_bf16 v[24:27], v[168:171], a[24:27], v[24:27]\n\t"
      "v_mfma_f32_16x16x32_bf16 v[24:27], v[184:187], a[28:31], v[24:27]\n\t"
      "v_cvt_pk_bf16_f32 v40, v16, v17\n\t"
      "v_cvt_pk_bf16_f32 v41, v18, v19\n\t"
      "v_mfma_f32_16x16x32_bf16 v[24:27], v[200:203], a[32:35], v[24:27]\n\t"
      "ds_write_b64 v0, v[40:41] offset:0\n\t"
      "v_mfma_f32_16x16x32_bf16 v[24:27], v[216:219], a[36:39], v[24:27]\n\t"
      "buffer_load_dwordx4 v3, s[40:43], s48 offen offset:1024 nt lds\n\t"
      "v_mfma_f32_16x16x32_bf16 v[24:27], v[232:235], a[40:43], v[24:27]\n\t"
      "s_add_u32 s58, s58, 786432\n\t"
      "s_lshr_b32 s59, s59, 1\n\t"
      "v_mfma_f32_16x16x32_bf16 v[24:27], v[248:251], a[44:47], v[24:27]\n\t"
      "s_bitcmp1_b32 s59, 0\n\t"
      "s_cselect_b32 s49, 24576, 0\n\t"
      "s_add_u32 s49, s49, s58\n\t"
      "v_mfma_f32_16x16x32_bf16 v[28:31], v[172:175], a[24:27], v[28:31]\n\t"
      "v_mfma_f32_16x16x32_bf16 v[28:31], v[188:191], a[28:31], v[28:31]\n\t"
      "v_cvt_pk_bf16_f32 v42, v20, v21\n\t"
      "v_cvt_pk_bf16_f32 v43, v22, v23\n\t"
      "v_mfma_f32_16x16x32_bf16 v[28:31], v[204:207], a[32:35], v[28:31]\n\t"
      "ds_write_b64 v0, v[42:43] offset:32\n\t"
      "v_mfma_f32_16x16x32_bf16 v[28:31], v[220:223], a[36:39], v[28:31]\n\t"
      "v_mfma_f32_16x16x32_bf16 v[28:31], v[236:239], a[40:43], v[28:31]\n\t"
      "v_mfma_f32_16x16x32_bf16 v[28:31], v[252:255], a[44:47], v[28:31]\n\t"
      "v_mfma_f32_16x16x32_bf16 v[32:35], a[108:111], a[24:27], v[32:35]\n\t"
      "v_mfma_f32_16x16x32_bf16 v[32:35], a[116:119], a[28:31], v[32:35]\n\t"
      "v_cvt_pk_bf16_f32 v44, v24, v25\n\t"
      "v_cvt_pk_bf16_f32 v45, v26, v27\n\t"
      "v_mfma_f32_16x16x32_bf16 v[32:35], a[124:127], a[32:35], v[32:35]\n\t"
      "ds_write_b64 v0, v[44:45] offset:64\n\t"
      "v_mfma_f32_16x16x32_bf16 v[32:35], a[132:135], a[36:39], v[32:35]\n\t"
      "buffer_load_dwordx4 v4, s[40:43], s48 offen offset:2048 nt lds\n\t"
      "v_mfma_f32_16x16x32_bf16 v[32:35], a[140:143], a[40:43], v[32:35]\n\t"
      "s_add_u32 s50, s50, 12288\n\t"
      "v_mfma_f32_16x16x32_bf16 v[32:35], a[148:151], a[44:47], v[32:35]\n\t"
      "v_cvt_pk_bf16_f32 v46, v28, v29\n\t"
      "v_cvt_pk_bf16_f32 v47, v30, v31\n\t"
      "v_mfma_f32_16x16x32_bf16 v[36:39], a[112:115], a[24:27], v[36:39]\n\t"
      "ds_write_b64 v0, v[46:47] offset:96\n\t"
      "v_mfma_f32_16x16x32_bf16 v[36:39], a[120:123], a[28:31], v[36:39]\n\t"
      "s_cmp_eq_u32 s50, 160256\n\t"
      "s_cselect_b32 s50, 25088, s50\n\t"
      "v_mfma_f32_16x16x32_bf16 v[36:39], a[128:131], a[32:35], v[36:39]\n\t"
      "v_mfma_f32_16x16x32_bf16 v[36:39], a[136:139], a[36:39], v[36:39]\n\t"
      "v_mfma_f32_16x16x32_bf16 v[36:39], a[144:147], a[40:43], v[36:39]\n\t"
      "v_cvt_pk_bf16_f32 v48, v32, v33\n\t"
      "v_cvt_pk_bf16_f32 v49, v34, v35\n\t"
      "v_mfma_f32_16x16x32_bf16 v[36:39], a[152:155], a[44:47], v[36:39]\n\t"
      "ds_write_b64 v0, v[48:49] offset:128\n\t"
      ".Lmid1_%=:\n\t"
      "v_mfma_f32_16x16x32_bf16 v[16:19], v[64:67], a[0:3], 0\n\t"
      "ds_read_b128 a[24:27], v54 offset:256\n\t"
      "v_mfma_f32_16x16x32_bf16 v[16:19], v[80:83], a[4:7], v[16:19]\n\t"
      "ds_read_b128 a[28:31], v55 offset:256\n\t"
      "v_mfma_f32_16x16x32_bf16 v[16:19], v[96:99], a[8:11], v[16:19]\n\t"
      "ds_read_b128 a[32:35], v52 offset:512\n\t"
      "v_mfma_f32_16x16x32_bf16 v[16:19], v[112:115], a[12:15], v[16:19]\n\t"
      "ds_read_b128 a[36:39], v53 offset:512\n\t"
      "v_mfma_f32_16x16x32_bf16 v[16:19], v[128:131], a[16:19], v[16:19]\n\t"
      "ds_read_b128 a[40:43], v54 offset:512\n\t"
      "v_mfma_f32_16x16x32_bf16 v[16:19], v[144:147], a[20:23], v[16:19]\n\t"
      "ds_read_b128 a[44:47], v55 offset:512\n\t"
      "v_mfma_f32_16x16x32_bf16 v[20:23], v[68:71], a[0:3], 0\n\t"
      "v_cvt_pk_bf16_f32 v50, v36, v37\n\t"
      "v_cvt_pk_bf16_f32 v51, v38, v39\n\t"
      "v_mfma_f32_16x16x32_bf16 v[20:23], v[84:87], a[4:7], v[20:23]\n\t"
      "ds_write_b64 v0, v[50:51] offset:160\n\t"
      "v_mfma_f32_16x16x32_bf16 v[20:23], v[100:103], a[8:11], v[20:23]\n\t"
      "v_add_u32 v56, s51, v15\n\t"
      "v_xor_b32 v57, 64, v56\n\t"
      "v_mfma_f32_16x16x32_bf16 v[20:23], v[116:119], a[12:15], v[20:23]\n\t"
      "v_xor_b32 v58, 128, v56\n\t"
      "v_xor_b32 v59, 192, v56\n\t"
      "v_mfma_f32_16x16x32_bf16 v[20:23], v[132:135], a[16:19], v[20:23]\n\t"
      "v_mfma_f32_16x16x32_bf16 v[20:23], v[148:151], a[20:23], v[20:23]\n\t"
      "v_mfma_f32_16x16x32_bf16 v[24:27], v[72:75], a[0:3], 0\n\t"
      "v_mfma_f32_16x16x32_bf16 v[24:27], v[88:91], a[4:7], v[24:27]\n\t"
      "v_mfma_f32_16x16x32_bf16 v[24:27], v[104:107], a[8:11], v[24:27]\n\t"
      "v_mfma_f32_16x16x32_bf16 v[24:27], v[120:123], a[12:15], v[24:27]\n\t"
      "s_waitcnt vmcnt(12)\n\t"
      "s_waitcnt lgkmcnt(0)\n\t"
      "s_barrier\n\t"
      "v_mfma_f32_16x16x32_bf16 v[24:27], v[136:139], a[16:19], v[24:27]\n\t"
      "v_mfma_f32_16x16x32_bf16 v[24:27], v[152:155], a[20:23], v[24:27]\n\t"
      "v_mfma_f32_16x16x32_bf16 v[28:31], v[76:79], a[0:3], 0\n\t"
      "ds_read_b128 a[48:51], v8 offset:0\n\t"
      "v_mfma_f32_16x16x32_bf16 v[28:31], v[92:95], a[4:7], v[28:31]\n\t"
      "ds_read_b128 a[52:55], v60 offset:1024\n\t"
      "v_mfma_f32_16x16x32_bf16 v[28:31], v[108:111], a[8:11], v[28:31]\n\t"
      "ds_read_b128 a[56:59], v61 offset:2048\n\t"
      "v_mfma_f32_16x16x32_bf16 v[28:31], v[124:127], a[12:15], v[28:31]\n\t"
      "v_mfma_f32_16x16x32_bf16 v[28:31], v[140:143], a[16:19], v[28:31]\n\t"
      "v_mfma_f32_16x16x32_bf16 v[28:31], v[156:159], a[20:23], v[28:31]\n\t"
      "v_mfma_f32_16x16x32_bf16 v[32:35], a[60:63], a[0:3], 0\n\t"
      "v_mfma_f32_16x16x32_bf16 v[32:35], a[68:71], a[4:7], v[32:35]\n\t"
      "s_waitcnt lgkmcnt(2)\n\t"
      "v_mfma_f32_16x16x32_bf16 v[32:35], a[76:79], a[8:11], v[32:35]\n\t"
      "buffer_store_dwordx4 a[48:51], v9, s[44:47], s49 offen sc1\n\t"
      "v_mfma_f32_16x16x32_bf16 v[32:35], a[84:87], a[12:15], v[32:35]\n\t"
      "v_mfma_f32_16x16x32_bf16 v[32:35], a[92:95], a[16:19], v[32:35]\n\t"
      "v_mfma_f32_16x16x32_bf16 v[32:35], a[100:103], a[20:23], v[32:35]\n\t"
      "s_add_u32 m0, s18, s50\n\t"
      "v_mfma_f32_16x16x32_bf16 v[36:39], a[64:67], a[0:3], 0\n\t"
      "buffer_load_dwordx4 v5, s[40:43], s48 offen nt lds\n\t"
      "v_mfma_f32_16x16x32_bf16 v[36:39], a[72:75], a[4:7], v[36:39]\n\t"
      "v_mfma_f32_16x16x32_bf16 v[36:39], a[80:83], a[8:11], v[36:39]\n\t"
      "v_mfma_f32_16x16x32_bf16 v[36:39], a[88:91], a[12:15], v[36:39]\n\t"
      "s_waitcnt lgkmcnt(1)\n\t"
      "v_mfma_f32_16x16x32_bf16 v[36:39], a[96:99], a[16:19], v[36:39]\n\t"
      "buffer_store_dwordx4 a[52:55], v10, s[44:47], s49 offen offset:1024 sc1\n\t"
      "v_mfma_f32_16x16x32_bf16 v[36:39], a[104:107], a[20:23], v[36:39]\n\t"
      "v_mfma_f32_16x16x32_bf16 v[16:19], v[160:163], a[24:27], v[16:19]\n\t"
      "ds_read_b128 a[0:3], v56 offset:0\n\t"
      "v_mfma_f32_16x16x32_bf16 v[16:19], v[176:179], a[28:31], v[16:19]\n\t"
      "ds_read_b128 a[4:7], v57 offset:0\n\t"
      "v_mfma_f32_16x16x32_bf16 v[16:19], v[192:195], a[32:35], v[16:19]\n\t"
      "ds_read_b128 a[8:11], v58 offset:0\n\t"
      "v_mfma_f32_16x16x32_bf16 v[16:19], v[208:211], a[36:39], v[16:19]\n\t"
      "ds_read_b128 a[12:15], v59 offset:0\n\t"
      "v_mfma_f32_16x16x32_bf16 v[16:19], v[224:227], a[40:43], v[16:19]\n\t"
      "ds_read_b128 a[16:19], v56 offset:256\n\t"
      "v_mfma_f32_16x16x32_bf16 v[16:19], v[240:243], a[44:47], v[16:19]\n\t"
      "ds_read_b128 a[20:23], v57 offset:256\n\t"
      "v_mfma_f32_16x16x32_bf16 v[20:23], v[164:167], a[24:27], v[20:23]\n\t"
      "s_add_u32 s51, s51, 12288\n\t"
      "s_cmp_eq_u32 s51, 160256\n\t"
      "s_cselect_b32 s51, 25088, s51\n\t"
      "v_mfma_f32_16x16x32_bf16 v[20:23], v[180:183], a[28:31], v[20:23]\n\t"
      "s_waitcnt lgkmcnt(6)\n\t"
      "v_mfma_f32_16x16x32_bf16 v[20:23], v[196:199], a[32:35], v[20:23]\n\t"
      "buffer_store_dwordx4 a[56:59], v11, s[44:47], s49 offen offset:2048 sc1\n\t"
      "v_mfma_f32_16x16x32_bf16 v[20:23], v[212:215], a[36:39], v[20:23]\n\t"
      "v_mfma_f32_16x16x32_bf16 v[20:23], v[228:231], a[40:43], v[20:23]\n\t"
      "s_waitcnt lgkmcnt(0)\n\t"
      "v_mfma_f32_16x16x32_bf16 v[20:23], v[244:247], a[44:47], v[20:23]\n\t"
      "v_mfma_f32_16x16x32_bf16 v[24:27], v[168:171], a[24:27], v[24:27]\n\t"
      "v_mfma_f32_16x16x32_bf16 v[24:27], v[184:187], a[28:31], v[24:27]\n\t"
      "v_cvt_pk_bf16_f32 v40, v16, v17\n\t"
      "v_cvt_pk_bf16_f32 v41, v18, v19\n\t"
      "v_mfma_f32_16x16x32_bf16 v[24:27], v[200:203], a[32:35], v[24:27]\n\t"
      "ds_write_b64 v0, v[40:41] offset:12544\n\t"
      "v_mfma_f32_16x16x32_bf16 v[24:27], v[216:219], a[36:39], v[24:27]\n\t"
      "buffer_load_dwordx4 v6, s[40:43], s48 offen offset:1024 nt lds\n\t"
      "v_mfma_f32_16x16x32_bf16 v[24:27], v[232:235], a[40:43], v[24:27]\n\t"
      "v_mfma_f32_16x16x32_bf16 v[24:27], v[248:251], a[44:47], v[24:27]\n\t"
      "v_mfma_f32_16x16x32_bf16 v[28:31], v[172:175], a[24:27], v[28:31]\n\t"
      "v_mfma_f32_16x16x32_bf16 v[28:31], v[188:191], a[28:31], v[28:31]\n\t"
      "v_cvt_pk_bf16_f32 v42, v20, v21\n\t"
      "v_cvt_pk_bf16_f32 v43, v22, v23\n\t"
      "v_mfma_f32_16x16x32_bf16 v[28:31], v[204:207], a[32:35], v[28:31]\n\t"
      "ds_write_b64 v0, v[42:43] offset:12576\n\t"
      "v_mfma_f32_16x16x32_bf16 v[28:31], v[220:223], a[36:39], v[28:31]\n\t"
      "v_mfma_f32_16x16x32_bf16 v[28:31], v[236:239], a[40:43], v[28:31]\n\t"
      "v_mfma_f32_16x16x32_bf16 v[28:31], v[252:255], a[44:47], v[28:31]\n\t"
      "v_mfma_f32_16x16x32_bf16 v[32:35], a[108:111], a[24:27], v[32:35]\n\t"
      "v_mfma_f32_16x16x32_bf16 v[32:35], a[116:119], a[28:31], v[32:35]\n\t"
      "v_cvt_pk_bf16_f32 v44, v24, v25\n\t"
      "v_cvt_pk_bf16_f32 v45, v26, v27\n\t"
      "v_mfma_f32_16x16x32_bf16 v[32:35], a[124:127], a[32:35], v[32:35]\n\t"
      "ds_write_b64 v0, v[44:45] offset:12608\n\t"
      "v_mfma_f32_16x16x32_bf16 v[32:35], a[132:135], a[36:39], v[32:35]\n\t"
      "buffer_load_dwordx4 v7, s[40:43], s48 offen offset:2048 nt lds\n\t"
      "v_mfma_f32_16x16x32_bf16 v[32:35], a[140:143], a[40:43], v[32:35]\n\t"
      "s_add_u32 s50, s50, 12288\n\t"
      "v_mfma_f32_16x16x32_bf16 v[32:35], a[148:151], a[44:47], v[32:35]\n\t"
      "v_cvt_pk_bf16_f32 v46, v28, v29\n\t"
      "v_cvt_pk_bf16_f32 v47, v30, v31\n\t"
      "v_mfma_f32_16x16x32_bf16 v[36:39], a[112:115], a[24:27], v[36:39]\n\t"
      "ds_write_b64 v0, v[46:47] offset:12640\n\t"
      "v_mfma_f32_16x16x32_bf16 v[36:39], a[120:123], a[28:31], v[36:39]\n\t"
      "s_cmp_eq_u32 s50, 160256\n\t"
      "s_cselect_b32 s50, 25088, s50\n\t"
      "v_mfma_f32_16x16x32_bf16 v[36:39], a[128:131], a[32:35], v[36:39]\n\t"
      "s_sub_u32 s52, s52, 1\n\t"
      "s_cmp_lg_u32 s52, 0\n\t"
      "v_mfma_f32_16x16x32_bf16 v[36:39], a[136:139], a[36:39], v[36:39]\n\t"
      "v_mfma_f32_16x16x32_bf16 v[36:39], a[144:147], a[40:43], v[36:39]\n\t"
      "v_cvt_pk_bf16_f32 v48, v32, v33\n\t"
      "v_cvt_pk_bf16_f32 v49, v34, v35\n\t"
      "v_mfma_f32_16x16x32_bf16 v[36:39], a[152:155], a[44:47], v[36:39]\n\t"
      "ds_write_b64 v0, v[48:49] offset:12672\n\t"
      "s_cbranch_scc1 .Lloop1_%=\n\t"
      "s_branch .Lendc_%=\n\t"
      ".Lloop2_%=:\n\t"
      "v_mfma_f32_16x16x32_bf16 v[16:19], v[64:67], a[0:3], 0\n\t"
      "ds_read_b128 a[24:27], v58 offset:256\n\t"
      "v_mfma_f32_16x16x32_bf16 v[16:19], v[80:83], a[4:7], v[16:19]\n\t"
      "ds_read_b128 a[28:31], v59 offset:256\n\t"
      "v_mfma_f32_16x16x32_bf16 v[16:19], v[96:99], a[8:11], v[16:19]\n\t"
      "ds_read_b128 a[32:35], v56 offset:512\n\t"
      "v_mfma_f32_16x16x32_bf16 v[16:19], v[112:115], a[12:15], v[16:19]\n\t"
      "ds_read_b128 a[36:39], v57 offset:512\n\t"
      "v_mfma_f32_16x16x32_bf16 v[16:19], v[128:131], a[16:19], v[16:19]\n\t"
      "ds_read_b128 a[40:43], v58 offset:512\n\t"
      "v_mfma_f32_16x16x32_bf16 v[16:19], v[144:147], a[20:23], v[16:19]\n\t"
      "ds_read_b128 a[44:47], v59 offset:512\n\t"
      "v_mfma_f32_16x16x32_bf16 v[20:23], v[68:71], a[0:3], 0\n\t"
      "v_cvt_pk_bf16_f32 v50, v36, v37\n\t"
      "v_cvt_pk_bf16_f32 v51, v38, v39\n\t"
      "v_mfma_f32_16x16x32_bf16 v[20:23], v[84:87], a[4:7], v[20:23]\n\t"
      "ds_write_b64 v0, v[50:51] offset:12704\n\t"
      "v_mfma_f32_16x16x32_bf16 v[20:23], v[100:103], a[8:11], v[20:23]\n\t"
      "v_add_u32 v52, s51, v15\n\t"
      "v_xor_b32 v53, 64, v52\n\t"
      "v_mfma_f32_16x16x32_bf16 v[20:23], v[116:119], a[12:15], v[20:23]\n\t"
      "v_xor_b32 v54, 128, v52\n\t"
      "v_xor_b32 v55, 192, v52\n\t"
      "v_mfma_f32_16x16x32_bf16 v[20:23], v[132:135], a[16:19], v[20:23]\n\t"
      "s_add_u32 s56, s56, 786432\n\t"
      "s_lshr_b32 s57, s57, 1\n\t"
      "v_mfma_f32_16x16x32_bf16 v[20:23], v[148:151], a[20:23], v[20:23]\n\t"
      "s_bitcmp1_b32 s57, 0\n\t"
      "s_cselect_b32 s48, 24576, 0\n\t"
      "s_add_u32 s48, s48, s56\n\t"
      "v_mfma_f32_16x16x32_bf16 v[24:27], v[72:75], a[0:3], 0\n\t"
      "v_mfma_f32_16x16x32_bf16 v[24:27], v[88:91], a[4:7], v[24:27]\n\t"
      "v_mfma_f32_16x16x32_bf16 v[24:27], v[104:107], a[8:11], v[24:27]\n\t"
      "v_mfma_f32_16x16x32_bf16 v[24:27], v[120:123], a[12:15], v[24:27]\n\t"
      "v_mfma_f32_16x16x32_bf16 v[24:27], v[136:139], a[16:19], v[24:27]\n\t"
      "s_waitcnt vmcnt(12)\n\t"
      "s_waitcnt lgkmcnt(0)\n\t"
      "s_barrier\n\t"
      "v_mfma_f32_16x16x32_bf16 v[24:27], v[152:155], a[20:23], v[24:27]\n\t"
      "v_mfma_f32_16x16x32_bf16 v[28:31], v[76:79], a[0:3], 0\n\t"
      "ds_read_b128 a[48:51], v8 offset:12544\n\t"
      "v_mfma_f32_16x16x32_bf16 v[28:31], v[92:95], a[4:7], v[28:31]\n\t"
      "ds_read_b128 a[52:55], v60 offset:13568\n\t"
      "v_mfma_f32_16x16x32_bf16 v[28:31], v[108:111], a[8:11], v[28:31]\n\t"
      "ds_read_b128 a[56:59], v61 offset:14592\n\t"
      "v_mfma_f32_16x16x32_bf16 v[28:31], v[124:127], a[12:15], v[28:31]\n\t"
      "v_mfma_f32_16x16x32_bf16 v[28:31], v[140:143], a[16:19], v[28:31]\n\t"
      "v_mfma_f32_16x16x32_bf16 v[28:31], v[156:159], a[20:23], v[28:31]\n\t"
      "v_mfma_f32_16x16x32_bf16 v[32:35], a[60:63], a[0:3], 0\n\t"
      "v_mfma_f32_16x16x32_bf16 v[32:35], a[68:71], a[4:7], v[32:35]\n\t"
      "s_waitcnt lgkmcnt(2)\n\t"
      "v_mfma_f32_16x16x32_bf16 v[32:35], a[76:79], a[8:11], v[32:35]\n\t"
      "buffer_store_dwordx4 a[48:51], v12, s[44:47], s49 offen sc1\n\t"
      "v_mfma_f32_16x16x32_bf16 v[32:35], a[84:87], a[12:15], v[32:35]\n\t"
      "v_mfma_f32_16x16x32_bf16 v[32:35], a[92:95], a[16:19], v[32:35]\n\t"
      "v_mfma_f32_16x16x32_bf16 v[32:35], a[100:103], a[20:23], v[32:35]\n\t"
      "s_add_u32 m0, s18, s50\n\t"
      "v_mfma_f32_16x16x32_bf16 v[36:39], a[64:67], a[0:3], 0\n\t"
      "buffer_load_dwordx4 v2, s[40:43], s48 offen nt lds\n\t"
      "v_mfma_f32_16x16x32_bf16 v[36:39], a[72:75], a[4:7], v[36:39]\n\t"
      "v_mfma_f32_16x16x32_bf16 v[36:39], a[80:83], a[8:11], v[36:39]\n\t"
      "v_mfma_f32_16x16x32_bf16 v[36:39], a[88:91], a[12:15], v[36:39]\n\t"
      "s_waitcnt lgkmcnt(1)\n\t"
      "v_mfma_f32_16x16x32_bf16 v[36:39], a[96:99], a[16:19], v[36:39]\n\t"
      "buffer_store_dwordx4 a[52:55], v13, s[44:47], s49 offen offset:1024 sc1\n\t"
      "v_mfma_f32_16x16x32_bf16 v[36:39], a[104:107], a[20:23], v[36:39]\n\t"
      "v_mfma_f32_16x16x32_bf16 v[16:19], v[160:163], a[24:27], v[16:19]\n\t"
      "ds_read_b128 a[0:3], v52 offset:0\n\t"
      "v_mfma_f32_16x16x32_bf16 v[16:19], v[176:179], a[28:31], v[16:19]\n\t"
      "ds_read_b128 a[4:7], v53 offset:0\n\t"
      "v_mfma_f32_16x16x32_bf16 v[16:19], v[192:195], a[32:35], v[16:19]\n\t"
      "ds_read_b128 a[8:11], v54 offset:0\n\t"
      "v_mfma_f32_16x16x32_bf16 v[16:19], v[208:211], a[36:39], v[16:19]\n\t"
      "ds_read_b128 a[12:15], v55 offset:0\n\t"
      "v_mfma_f32_16x16x32_bf16 v[16:19], v[224:227], a[40:43], v[16:19]\n\t"
      "ds_read_b128 a[16:19], v52 offset:256\n\t"
      "v_mfma_f32_16x16x32_bf16 v[16:19], v[240:243], a[44:47], v[16:19]\n\t"
      "ds_read_b128 a[20:23], v53 offset:256\n\t"
      "v_mfma_f32_16x16x32_bf16 v[20:23], v[164:167], a[24:27], v[20:23]\n\t"
      "s_add_u32 s51, s51, 12288\n\t"
      "s_cmp_eq_u32 s51, 160256\n\t"
      "s_cselect_b32 s51, 25088, s51\n\t"
      "v_mfma_f32_16x16x32_bf16 v[20:23], v[180:183], a[28:31], v[20:23]\n\t"
      "s_waitcnt lgkmcnt(6)\n\t"
      "v_mfma_f32_16x16x32_bf16 v[20:23], v[196:199], a[32:35], v[20:23]\n\t"
      "buffer_store_dwordx4 a[56:59], v14, s[44:47], s49 offen offset:2048 sc1\n\t"
      "v_mfma_f32_16x16x32_bf16 v[20:23], v[212:215], a[36:39], v[20:23]\n\t"
      "v_mfma_f32_16x16x32_bf16 v[20:23], v[228:231], a[40:43], v[20:23]\n\t"
      "s_waitcnt lgkmcnt(0)\n\t"
      "v_mfma_f32_16x16x32_bf16 v[20:23], v[244:247], a[44:47], v[20:23]\n\t"
      "v_mfma_f32_16x16x32_bf16 v[24:27], v[168:171], a[24:27], v[24:27]\n\t"
      "v_mfma_f32_16x16x32_bf16 v[24:27], v[184:187], a[28:31], v[24:27]\n\t"
      "v_cvt_pk_bf16_f32 v40, v16, v17\n\t"
      "v_cvt_pk_bf16_f32 v41, v18, v19\n\t"
      "v_mfma_f32_16x16x32_bf16 v[24:27], v[200:203], a[32:35], v[24:27]\n\t"
      "ds_write_b64 v0, v[40:41] offset:0\n\t"
      "v_mfma_f32_16x16x32_bf16 v[24:27], v[216:219], a[36:39], v[24:27]\n\t"
      "buffer_load_dwordx4 v3, s[40:43], s48 offen offset:1024 nt lds\n\t"
      "v_mfma_f32_16x16x32_bf16 v[24:27], v[232:235], a[40:43], v[24:27]\n\t"
      "s_add_u32 s58, s58, 786432\n\t"
      "s_lshr_b32 s59, s59, 1\n\t"
      "v_mfma_f32_16x16x32_bf16 v[24:27], v[248:251], a[44:47], v[24:27]\n\t"
      "s_bitcmp1_b32 s59, 0\n\t"
      "s_cselect_b32 s49, 24576, 0\n\t"
      "s_add_u32 s49, s49, s58\n\t"
      "v_mfma_f32_16x16x32_bf16 v[28:31], v[172:175], a[24:27], v[28:31]\n\t"
      "v_mfma_f32_16x16x32_bf16 v[28:31], v[188:191], a[28:31], v[28:31]\n\t"
      "v_cvt_pk_bf16_f32 v42, v20, v21\n\t"
      "v_cvt_pk_bf16_f32 v43, v22, v23\n\t"
      "v_mfma_f32_16x16x32_bf16 v[28:31], v[204:207], a[32:35], v[28:31]\n\t"
      "ds_write_b64 v0, v[42:43] offset:32\n\t"
      "v_mfma_f32_16x16x32_bf16 v[28:31], v[220:223], a[36:39], v[28:31]\n\t"
      "v_mfma_f32_16x16x32_bf16 v[28:31], v[236:239], a[40:43], v[28:31]\n\t"
      "v_mfma_f32_16x16x32_bf16 v[28:31], v[252:255], a[44:47], v[28:31]\n\t"
      "v_mfma_f32_16x16x32_bf16 v[32:35], a[108:111], a[24:27], v[32:35]\n\t"
      "v_mfma_f32_16x16x32_bf16 v[32:35], a[116:119], a[28:31], v[32:35]\n\t"
      "v_cvt_pk_bf16_f32 v44, v24, v25\n\t"
      "v_cvt_pk_bf16_f32 v45, v26, v27\n\t"
      "v_mfma_f32_16x16x32_bf16 v[32:35], a[124:127], a[32:35], v[32:35]\n\t"
      "ds_write_b64 v0, v[44:45] offset:64\n\t"
      "v_mfma_f32_16x16x32_bf16 v[32:35], a[132:135], a[36:39], v[32:35]\n\t"
      "buffer_load_dwordx4 v4, s[40:43], s48 offen offset:2048 nt lds\n\t"
      "v_mfma_f32_16x16x32_bf16 v[32:35], a[140:143], a[40:43], v[32:35]\n\t"
      "s_add_u32 s50, s50, 12288\n\t"
      "v_mfma_f32_16x16x32_bf16 v[32:35], a[148:151], a[44:47], v[32:35]\n\t"
      "v_cvt_pk_bf16_f32 v46, v28, v29\n\t"
      "v_cvt_pk_bf16_f32 v47, v30, v31\n\t"
      "v_mfma_f32_16x16x32_bf16 v[36:39], a[112:115], a[24:27], v[36:39]\n\t"
      "ds_write_b64 v0, v[46:47] offset:96\n\t"
      "v_mfma_f32_16x16x32_bf16 v[36:39], a[120:123], a[28:31], v[36:39]\n\t"
      "s_cmp_eq_u32 s50, 160256\n\t"
      "s_cselect_b32 s50, 25088, s50\n\t"
      "v_mfma_f32_16x16x32_bf16 v[36:39], a[128:131], a[32:35], v[36:39]\n\t"
      "v_mfma_f32_16x16x32_bf16 v[36:39], a[136:139], a[36:39], v[36:39]\n\t"
      "v_mfma_f32_16x16x32_bf16 v[36:39], a[144:147], a[40:43], v[36:39]\n\t"
      "v_cvt_pk_bf16_f32 v48, v32, v33\n\t"
      "v_cvt_pk_bf16_f32 v49, v34, v35\n\t"
      "v_mfma_f32_16x16x32_bf16 v[36:39], a[152:155], a[44:47], v[36:39]\n\t"
      "ds_write_b64 v0, v[48:49] offset:128\n\t"
      ".Lmid2_%=:\n\t"
      "v_mfma_f32_16x16x32_bf16 v[16:19], v[64:67], a[0:3], 0\n\t"
      "ds_read_b128 a[24:27], v54 offset:256\n\t"
      "v_mfma_f32_16x16x32_bf16 v[16:19], v[80:83], a[4:7], v[16:19]\n\t"
      "ds_read_b128 a[28:31], v55 offset:256\n\t"
      "v_mfma_f32_16x16x32_bf16 v[16:19], v[96:99], a[8:11], v[16:19]\n\t"
      "ds_read_b128 a[32:35], v52 offset:512\n\t"
      "v_mfma_f32_16x16x32_bf16 v[16:19], v[112:115], a[12:15], v[16:19]\n\t"
      "ds_read_b128 a[36:39], v53 offset:512\n\t"
      "v_mfma_f32_16x16x32_bf16 v[16:19], v[128:131], a[16:19], v[16:19]\n\t"
      "ds_read_b128 a[40:43], v54 offset:512\n\t"
      "v_mfma_f32_16x16x32_bf16 v[16:19], v[144:147], a[20:23], v[16:19]\n\t"
      "ds_read_b128 a[44:47], v55 offset:512\n\t"
      "v_mfma_f32_16x16x32_bf16 v[20:23], v[68:71], a[0:3], 0\n\t"
      "v_cvt_pk_bf16_f32 v50, v36, v37\n\t"
      "v_cvt_pk_bf16_f32 v51, v38, v39\n\t"
      "v_mfma_f32_16x16x32_bf16 v[20:23], v[84:87], a[4:7], v[20:23]\n\t"
      "ds_write_b64 v0, v[50:51] offset:160\n\t"
      "v_mfma_f32_16x16x32_bf16 v[20:23], v[100:103], a[8:11], v[20:23]\n\t"
      "v_add_u32 v56, s51, v15\n\t"
      "v_xor_b32 v57, 64, v56\n\t"
      "v_mfma_f32_16x16x32_bf16 v[20:23], v[116:119], a[12:15], v[20:23]\n\t"
      "v_xor_b32 v58, 128, v56\n\t"
      "v_xor_b32 v59, 192, v56\n\t"
      "v_mfma_f32_16x16x32_bf16 v[20:23], v[132:135], a[16:19], v[20:23]\n\t"
      "v_mfma_f32_16x16x32_bf16 v[20:23], v[148:151], a[20:23], v[20:23]\n\t"
      "v_mfma_f32_16x16x32_bf16 v[24:27], v[72:75], a[0:3], 0\n\t"
      "v_mfma_f32_16x16x32_bf16 v[24:27], v[88:91], a[4:7], v[24:27]\n\t"
      "v_mfma_f32_16x16x32_bf16 v[24:27], v[104:107], a[8:11], v[24:27]\n\t"
      "v_mfma_f32_16x16x32_bf16 v[24:27], v[120:123], a[12:15], v[24:27]\n\t"
      "v_mfma_f32_16x16x32_bf16 v[24:27], v[136:139], a[16:19], v[24:27]\n\t"
      "s_waitcnt vmcnt(12)\n\t"
      "s_waitcnt lgkmcnt(0)\n\t"
      "s_barrier\n\t"
      "v_mfma_f32_16x16x32_bf16 v[24:27], v[152:155], a[20:23], v[24:27]\n\t"
      "v_mfma_f32_16x16x32_bf16 v[28:31], v[76:79], a[0:3], 0\n\t"
      "ds_read_b128 a[48:51], v8 offset:0\n\t"
      "v_mfma_f32_16x16x32_bf16 v[28:31], v[92:95], a[4:7], v[28:31]\n\t"
      "ds_read_b128 a[52:55], v60 offset:1024\n\t"
      "v_mfma_f32_16x16x32_bf16 v[28:31], v[108:111], a[8:11], v[28:31]\n\t"
      "ds_read_b128 a[56:59], v61 offset:2048\n\t"
      "v_mfma_f32_16x16x32_bf16 v[28:31], v[124:127], a[12:15], v[28:31]\n\t"
      "v_mfma_f32_16x16x32_bf16 v[28:31], v[140:143], a[16:19], v[28:31]\n\t"
      "v_mfma_f32_16x16x32_bf16 v[28:31], v[156:159], a[20:23], v[28:31]\n\t"
      "v_mfma_f32_16x16x32_bf16 v[32:35], a[60:63], a[0:3], 0\n\t"
      "v_mfma_f32_16x16x32_bf16 v[32:35], a[68:71], a[4:7], v[32:35]\n\t"
      "s_waitcnt lgkmcnt(2)\n\t"
      "v_mfma_f32_16x16x32_bf16 v[32:35], a[76:79], a[8:11], v[32:35]\n\t"
      "buffer_store_dwordx4 a[48:51], v9, s[44:47], s49 offen sc1\n\t"
      "v_mfma_f32_16x16x32_bf16 v[32:35], a[84:87], a[12:15], v[32:35]\n\t"
      "v_mfma_f32_16x16x32_bf16 v[32:35], a[92:95], a[16:19], v[32:35]\n\t"
      "v_mfma_f32_16x16x32_bf16 v[32:35], a[100:103], a[20:23], v[32:35]\n\t"
      "s_add_u32 m0, s18, s50\n\t"
      "v_mfma_f32_16x16x32_bf16 v[36:39], a[64:67], a[0:3], 0\n\t"
      "buffer_load_dwordx4 v5, s[40:43], s48 offen nt lds\n\t"
      "v_mfma_f32_16x16x32_bf16 v[36:39], a[72:75], a[4:7], v[36:39]\n\t"
      "v_mfma_f32_16x16x32_bf16 v[36:39], a[80:83], a[8:11], v[36:39]\n\t"
      "v_mfma_f32_16x16x32_bf16 v[36:39], a[88:91], a[12:15], v[36:39]\n\t"
      "s_waitcnt lgkmcnt(1)\n\t"
      "v_mfma_f32_16x16x32_bf16 v[36:39], a[96:99], a[16:19], v[36:39]\n\t"
      "buffer_store_dwordx4 a[52:55], v10, s[44:47], s49 offen offset:1024 sc1\n\t"
      "v_mfma_f32_16x16x32_bf16 v[36:39], a[104:107], a[20:23], v[36:39]\n\t"
      "v_mfma_f32_16x16x32_bf16 v[16:19], v[160:163], a[24:27], v[16:19]\n\t"
      "ds_read_b128 a[0:3], v56 offset:0\n\t"
      "v_mfma_f32_16x16x32_bf16 v[16:19], v[176:179], a[28:31], v[16:19]\n\t"
      "ds_read_b128 a[4:7], v57 offset:0\n\t"
      "v_mfma_f32_16x16x32_bf16 v[16:19], v[192:195], a[32:35], v[16:19]\n\t"
      "ds_read_b128 a[8:11], v58 offset:0\n\t"
      "v_mfma_f32_16x16x32_bf16 v[16:19], v[208:211], a[36:39], v[16:19]\n\t"
      "ds_read_b128 a[12:15], v59 offset:0\n\t"
      "v_mfma_f32_16x16x32_bf16 v[16:19], v[224:227], a[40:43], v[16:19]\n\t"
      "ds_read_b128 a[16:19], v56 offset:256\n\t"
      "v_mfma_f32_16x16x32_bf16 v[16:19], v[240:243], a[44:47], v[16:19]\n\t"
      "ds_read_b128 a[20:23], v57 offset:256\n\t"
      "v_mfma_f32_16x16x32_bf16 v[20:23], v[164:167], a[24:27], v[20:23]\n\t"
      "s_add_u32 s51, s51, 12288\n\t"
      "s_cmp_eq_u32 s51, 160256\n\t"
      "s_cselect_b32 s51, 25088, s51\n\t"
      "v_mfma_f32_16x16x32_bf16 v[20:23], v[180:183], a[28:31], v[20:23]\n\t"
      "s_waitcnt lgkmcnt(6)\n\t"
      "v_mfma_f32_16x16x32_bf16 v[20:23], v[196:199], a[32:35], v[20:23]\n\t"
      "buffer_store_dwordx4 a[56:59], v11, s[44:47], s49 offen offset:2048 sc1\n\t"
      "v_mfma_f32_16x16x32_bf16 v[20:23], v[212:215], a[36:39], v[20:23]\n\t"
      "v_mfma_f32_16x16x32_bf16 v[20:23], v[228:231], a[40:43], v[20:23]\n\t"
      "s_waitcnt lgkmcnt(0)\n\t"
      "v_mfma_f32_16x16x32_bf16 v[20:23], v[244:247], a[44:47], v[20:23]\n\t"
      "v_mfma_f32_16x16x32_bf16 v[24:27], v[168:171], a[24:27], v[24:27]\n\t"
      "v_mfma_f32_16x16x32_bf16 v[24:27], v[184:187], a[28:31], v[24:27]\n\t"
      "v_cvt_pk_bf16_f32 v40, v16, v17\n\t"
      "v_cvt_pk_bf16_f32 v41, v18, v19\n\t"
      "v_mfma_f32_16x16x32_bf16 v[24:27], v[200:203], a[32:35], v[24:27]\n\t"
      "ds_write_b64 v0, v[40:41] offset:12544\n\t"
      "v_mfma_f32_16x16x32_bf16 v[24:27], v[216:219], a[36:39], v[24:27]\n\t"
      "buffer_load_dwordx4 v6, s[40:43], s48 offen offset:1024 nt lds\n\t"
      "v_mfma_f32_16x16x32_bf16 v[24:27], v[232:235], a[40:43], v[24:27]\n\t"
      "v_mfma_f32_16x16x32_bf16 v[24:27], v[248:251], a[44:47], v[24:27]\n\t"
      "v_mfma_f32_16x16x32_bf16 v[28:31], v[172:175], a[24:27], v[28:31]\n\t"
      "v_mfma_f32_16x16x32_bf16 v[28:31], v[188:191], a[28:31], v[28:31]\n\t"
      "v_cvt_pk_bf16_f32 v42, v20, v21\n\t"
      "v_cvt_pk_bf16_f32 v43, v22, v23\n\t"
      "v_mfma_f32_16x16x32_bf16 v[28:31], v[204:207], a[32:35], v[28:31]\n\t"
      "ds_write_b64 v0, v[42:43] offset:12576\n\t"
      "v_mfma_f32_16x16x32_bf16 v[28:31], v[220:223], a[36:39], v[28:31]\n\t"
      "v_mfma_f32_16x16x32_bf16 v[28:31], v[236:239], a[40:43], v[28:31]\n\t"
      "v_mfma_f32_16x16x32_bf16 v[28:31], v[252:255], a[44:47], v[28:31]\n\t"
      "v_mfma_f32_16x16x32_bf16 v[32:35], a[108:111], a[24:27], v[32:35]\n\t"
      "v_mfma_f32_16x16x32_bf16 v[32:35], a[116:119], a[28:31], v[32:35]\n\t"
      "v_cvt_pk_bf16_f32 v44, v24, v25\n\t"
      "v_cvt_pk_bf16_f32 v45, v26, v27\n\t"
      "v_mfma_f32_16x16x32_bf16 v[32:35], a[124:127], a[32:35], v[32:35]\n\t"
      "ds_write_b64 v0, v[44:45] offset:12608\n\t"
      "v_mfma_f32_16x16x32_bf16 v[32:35], a[132:135], a[36:39], v[32:35]\n\t"
      "buffer_load_dwordx4 v7, s[40:43], s48 offen offset:2048 nt lds\n\t"
      "v_mfma_f32_16x16x32_bf16 v[32:35], a[140:143], a[40:43], v[32:35]\n\t"
      "s_add_u32 s50, s50, 12288\n\t"
      "v_mfma_f32_16x16x32_bf16 v[32:35], a[148:151], a[44:47], v[32:35]\n\t"
      "v_cvt_pk_bf16_f32 v46, v28, v29\n\t"
      "v_cvt_pk_bf16_f32 v47, v30, v31\n\t"
      "v_mfma_f32_16x16x32_bf16 v[36:39], a[112:115], a[24:27], v[36:39]\n\t"
      "ds_write_b64 v0, v[46:47] offset:12640\n\t"
      "v_mfma_f32_16x16x32_bf16 v[36:39], a[120:123], a[28:31], v[36:39]\n\t"
      "s_cmp_eq_u32 s50, 160256\n\t"
      "s_cselect_b32 s50, 25088, s50\n\t"
      "v_mfma_f32_16x16x32_bf16 v[36:39], a[128:131], a[32:35], v[36:39]\n\t"
      "s_sub_u32 s52, s52, 1\n\t"
      "s_cmp_lg_u32 s52, 0\n\t"
      "v_mfma_f32_16x16x32_bf16 v[36:39], a[136:139], a[36:39], v[36:39]\n\t"
      "v_mfma_f32_16x16x32_bf16 v[36:39], a[144:147], a[40:43], v[36:39]\n\t"
      "v_cvt_pk_bf16_f32 v48, v32, v33\n\t"
      "v_cvt_pk_bf16_f32 v49, v34, v35\n\t"
      "v_mfma_f32_16x16x32_bf16 v[36:39], a[152:155], a[44:47], v[36:39]\n\t"
      "ds_write_b64 v0, v[48:49] offset:12672\n\t"
      "s_cbranch_scc1 .Lloop2_%=\n\t"
      "s_branch .Lendc_%=\n\t"
      ".Lloop3_%=:\n\t"
      "v_mfma_f32_16x16x32_bf16 v[16:19], v[64:67], a[0:3], 0\n\t"
      "ds_read_b128 a[24:27], v58 offset:256\n\t"
      "v_mfma_f32_16x16x32_bf16 v[16:19], v[80:83], a[4:7], v[16:19]\n\t"
      "ds_read_b128 a[28:31], v59 offset:256\n\t"
      "v_mfma_f32_16x16x32_bf16 v[16:19], v[96:99], a[8:11], v[16:19]\n\t"
      "ds_read_b128 a[32:35], v56 offset:512\n\t"
      "v_mfma_f32_16x16x32_bf16 v[16:19], v[112:115], a[12:15], v[16:19]\n\t"
      "ds_read_b128 a[36:39], v57 offset:512\n\t"
      "v_mfma_f32_16x16x32_bf16 v[16:19], v[128:131], a[16:19], v[16:19]\n\t"
      "ds_read_b128 a[40:43], v58 offset:512\n\t"
      "v_mfma_f32_16x16x32_bf16 v[16:19], v[144:147], a[20:23], v[16:19]\n\t"
      "ds_read_b128 a[44:47], v59 offset:512\n\t"
      "v_mfma_f32_16x16x32_bf16 v[20:23], v[68:71], a[0:3], 0\n\t"
      "v_cvt_pk_bf16_f32 v50, v36, v37\n\t"
      "v_cvt_pk_bf16_f32 v51, v38, v39\n\t"
      "v_mfma_f32_16x16x32_bf16 v[20:23], v[84:87], a[4:7], v[20:23]\n\t"
      "ds_write_b64 v0, v[50:51] offset:12704\n\t"
      "v_mfma_f32_16x16x32_bf16 v[20:23], v[100:103], a[8:11], v[20:23]\n\t"
      "v_add_u32 v52, s51, v15\n\t"
      "v_xor_b32 v53, 64, v52\n\t"
      "v_mfma_f32_16x16x32_bf16 v[20:23], v[116:119], a[12:15], v[20:23]\n\t"
      "v_xor_b32 v54, 128, v52\n\t"
      "v_xor_b32 v55, 192, v52\n\t"
      "v_mfma_f32_16x16x32_bf16 v[20:23], v[132:135], a[16:19], v[20:23]\n\t"
      "s_add_u32 s56, s56, 786432\n\t"
      "s_lshr_b32 s57, s57, 1\n\t"
      "v_mfma_f32_16x16x32_bf16 v[20:23], v[148:151], a[20:23], v[20:23]\n\t"
      "s_bitcmp1_b32 s57, 0\n\t"
      "s_cselect_b32 s48, 24576, 0\n\t"
      "s_add_u32 s48, s48, s56\n\t"
      "v_mfma_f32_16x16x32_bf16 v[24:27], v[72:75], a[0:3], 0\n\t"
      "v_mfma_f32_16x16x32_bf16 v[24:27], v[88:91], a[4:7], v[24:27]\n\t"
      "v_mfma_f32_16x16x32_bf16 v[24:27], v[104:107], a[8:11], v[24:27]\n\t"
      "v_mfma_f32_16x16x32_bf16 v[24:27], v[120:123], a[12:15], v[24:27]\n\t"
      "v_mfma_f32_16x16x32_bf16 v[24:27], v[136:139], a[16:19], v[24:27]\n\t"
      "v_mfma_f32_16x16x32_bf16 v[24:27], v[152:155], a[20:23], v[24:27]\n\t"
      "s_waitcnt vmcnt(12)\n\t"
      "s_waitcnt lgkmcnt(0)\n\t"
      "s_barrier\n\t"
      "v_mfma_f32_16x16x32_bf16 v[28:31], v[76:79], a[0:3], 0\n\t"
      "ds_read_b128 a[48:51], v8 offset:12544\n\t"
      "v_mfma_f32_16x16x32_bf16 v[28:31], v[92:95], a[4:7], v[28:31]\n\t"
      "ds_read_b128 a[52:55], v60 offset:13568\n\t"
      "v_mfma_f32_16x16x32_bf16 v[28:31], v[108:111], a[8:11], v[28:31]\n\t"
      "ds_read_b128 a[56:59], v61 offset:14592\n\t"
      "v_mfma_f32_16x16x32_bf16 v[28:31], v[124:127], a[12:15], v[28:31]\n\t"
      "v_mfma_f32_16x16x32_bf16 v[28:31], v[140:143], a[16:19], v[28:31]\n\t"
      "v_mfma_f32_16x16x32_bf16 v[28:31], v[156:159], a[20:23], v[28:31]\n\t"
      "v_mfma_f32_16x16x32_bf16 v[32:35], a[60:63], a[0:3], 0\n\t"
      "v_mfma_f32_16x16x32_bf16 v[32:35], a[68:71], a[4:7], v[32:35]\n\t"
      "s_waitcnt lgkmcnt(2)\n\t"
      "v_mfma_f32_16x16x32_bf16 v[32:35], a[76:79], a[8:11], v[32:35]\n\t"
      "buffer_store_dwordx4 a[48:51], v12, s[44:47], s49 offen sc1\n\t"
      "v_mfma_f32_16x16x32_bf16 v[32:35], a[84:87], a[12:15], v[32:35]\n\t"
      "v_mfma_f32_16x16x32_bf16 v[32:35], a[92:95], a[16:19], v[32:35]\n\t"
      "v_mfma_f32_16x16x32_bf16 v[32:35], a[100:103], a[20:23], v[32:35]\n\t"
      "s_add_u32 m0, s18, s50\n\t"
      "v_mfma_f32_16x16x32_bf16 v[36:39], a[64:67], a[0:3], 0\n\t"
      "buffer_load_dwordx4 v2, s[40:43], s48 offen nt lds\n\t"
      "v_mfma_f32_16x16x32_bf16 v[36:39], a[72:75], a[4:7], v[36:39]\n\t"
      "v_mfma_f32_16x16x32_bf16 v[36:39], a[80:83], a[8:11], v[36:39]\n\t"
      "v_mfma_f32_16x16x32_bf16 v[36:39], a[88:91], a[12:15], v[36:39]\n\t"
      "s_waitcnt lgkmcnt(1)\n\t"
      "v_mfma_f32_16x16x32_bf16 v[36:39], a[96:99], a[16:19], v[36:39]\n\t"
      "buffer_store_dwordx4 a[52:55], v13, s[44:47], s49 offen offset:1024 sc1\n\t"
      "v_mfma_f32_16x16x32_bf16 v[36:39], a[104:107], a[20:23], v[36:39]\n\t"
      "v_mfma_f32_16x16x32_bf16 v[16:19], v[160:163], a[24:27], v[16:19]\n\t"
      "ds_read_b128 a[0:3], v52 offset:0\n\t"
      "v_mfma_f32_16x16x32_bf16 v[16:19], v[176:179], a[28:31], v[16:19]\n\t"
      "ds_read_b128 a[4:7], v53 offset:0\n\t"
      "v_mfma_f32_16x16x32_bf16 v[16:19], v[192:195], a[32:35], v[16:19]\n\t"
      "ds_read_b128 a[8:11], v54 offset:0\n\t"
      "v_mfma_f32_16x16x32_bf16 v[16:19], v[208:211], a[36:39], v[16:19]\n\t"
      "ds_read_b128 a[12:15], v55 offset:0\n\t"
      "v_mfma_f32_16x16x32_bf16 v[16:19], v[224:227], a[40:43], v[16:19]\n\t"
      "ds_read_b128 a[16:19], v52 offset:256\n\t"
      "v_mfma_f32_16x16x32_bf16 v[16:19], v[240:243], a[44:47], v[16:19]\n\t"
      "ds_read_b128 a[20:23], v53 offset:256\n\t"
      "v_mfma_f32_16x16x32_bf16 v[20:23], v[164:167], a[24:27], v[20:23]\n\t"
      "s_add_u32 s51, s51, 12288\n\t"
      "s_cmp_eq_u32 s51, 160256\n\t"
      "s_cselect_b32 s51, 25088, s51\n\t"
      "v_mfma_f32_16x16x32_bf16 v[20:23], v[180:183], a[28:31], v[20:23]\n\t"
      "s_waitcnt lgkmcnt(6)\n\t"
      "v_mfma_f32_16x16x32_bf16 v[20:23], v[196:199], a[32:35], v[20:23]\n\t"
      "buffer_store_dwordx4 a[56:59], v14, s[44:47], s49 offen offset:2048 sc1\n\t"
      "v_mfma_f32_16x16x32_bf16 v[20:23], v[212:215], a[36:39], v[20:23]\n\t"
      "v_mfma_f32_16x16x32_bf16 v[20:23], v[228:231], a[40:43], v[20:23]\n\t"
      "s_waitcnt lgkmcnt(0)\n\t"
      "v_mfma_f32_16x16x32_bf16 v[20:23], v[244:247], a[44:47], v[20:23]\n\t"
      "v_mfma_f32_16x16x32_bf16 v[24:27], v[168:171], a[24:27], v[24:27]\n\t"
      "v_mfma_f32_16x16x32_bf16 v[24:27], v[184:187], a[28:31], v[24:27]\n\t"
      "v_cvt_pk_bf16_f32 v40, v16, v17\n\t"
      "v_cvt_pk_bf16_f32 v41, v18, v19\n\t"
      "v_mfma_f32_16x16x32_bf16 v[24:27], v[200:203], a[32:35], v[24:27]\n\t"
      "ds_write_b64 v0, v[40:41] offset:0\n\t"
      "v_mfma_f32_16x16x32_bf16 v[24:27], v[216:219], a[36:39], v[24:27]\n\t"
      "buffer_load_dwordx4 v3, s[40:43], s48 offen offset:1024 nt lds\n\t"
      "v_mfma_f32_16x16x32_bf16 v[24:27], v[232:235], a[40:43], v[24:27]\n\t"
      "s_add_u32 s58, s58, 786432\n\t"
      "s_lshr_b32 s59, s59, 1\n\t"
      "v_mfma_f32_16x16x32_bf16 v[24:27], v[248:251], a[44:47], v[24:27]\n\t"
      "s_bitcmp1_b32 s59, 0\n\t"
      "s_cselect_b32 s49, 24576, 0\n\t"
      "s_add_u32 s49, s49, s58\n\t"
      "v_mfma_f32_16x16x32_bf16 v[28:31], v[172:175], a[24:27], v[28:31]\n\t"
      "v_mfma_f32_16x16x32_bf16 v[28:31], v[188:191], a[28:31], v[28:31]\n\t"
      "v_cvt_pk_bf16_f32 v42, v20, v21\n\t"
      "v_cvt_pk_bf16_f32 v43, v22, v23\n\t"
      "v_mfma_f32_16x16x32_bf16 v[28:31], v[204:207], a[32:35], v[28:31]\n\t"
      "ds_write_b64 v0, v[42:43] offset:32\n\t"
      "v_mfma_f32_16x16x32_bf16 v[28:31], v[220:223], a[36:39], v[28:31]\n\t"
      "v_mfma_f32_16x16x32_bf16 v[28:31], v[236:239], a[40:43], v[28:31]\n\t"
      "v_mfma_f32_16x16x32_bf16 v[28:31], v[252:255], a[44:47], v[28:31]\n\t"
      "v_mfma_f32_16x16x32_bf16 v[32:35], a[108:111], a[24:27], v[32:35]\n\t"
      "v_mfma_f32_16x16x32_bf16 v[32:35], a[116:119], a[28:31], v[32:35]\n\t"
      "v_cvt_pk_bf16_f32 v44, v24, v25\n\t"
      "v_cvt_pk_bf16_f32 v45, v26, v27\n\t"
      "v_mfma_f32_16x16x32_bf16 v[32:35], a[124:127], a[32:35], v[32:35]\n\t"
      "ds_write_b64 v0, v[44:45] offset:64\n\t"
      "v_mfma_f32_16x16x32_bf16 v[32:35], a[132:135], a[36:39], v[32:35]\n\t"
      "buffer_load_dwordx4 v4, s[40:43], s48 offen offset:2048 nt lds\n\t"
      "v_mfma_f32_16x16x32_bf16 v[32:35], a[140:143], a[40:43], v[32:35]\n\t"
      "s_add_u32 s50, s50, 12288\n\t"
      "v_mfma_f32_16x16x32_bf16 v[32:35], a[148:151], a[44:47], v[32:35]\n\t"
      "v_cvt_pk_bf16_f32 v46, v28, v29\n\t"
      "v_cvt_pk_bf16_f32 v47, v30, v31\n\t"
      "v_mfma_f32_16x16x32_bf16 v[36:39], a[112:115], a[24:27], v[36:39]\n\t"
      "ds_write_b64 v0, v[46:47] offset:96\n\t"
      "v_mfma_f32_16x16x32_bf16 v[36:39], a[120:123], a[28:31], v[36:39]\n\t"
      "s_cmp_eq_u32 s50, 160256\n\t"
      "s_cselect_b32 s50, 25088, s50\n\t"
      "v_mfma_f32_16x16x32_bf16 v[36:39], a[128:131], a[32:35], v[36:39]\n\t"
      "v_mfma_f32_16x16x32_bf16 v[36:39], a[136:139], a[36:39], v[36:39]\n\t"
      "v_mfma_f32_16x16x32_bf16 v[36:39], a[144:147], a[40:43], v[36:39]\n\t"
      "v_cvt_pk_bf16_f32 v48, v32, v33\n\t"
      "v_cvt_pk_bf16_f32 v49, v34, v35\n\t"
      "v_mfma_f32_16x16x32_bf16 v[36:39], a[152:155], a[44:47], v[36:39]\n\t"
      "ds_write_b64 v0, v[48:49] offset:128\n\t"
      ".Lmid3_%=:\n\t"
      "v_mfma_f32_16x16x32_bf16 v[16:19], v[64:67], a[0:3], 0\n\t"
      "ds_read_b128 a[24:27], v54 offset:256\n\t"
      "v_mfma_f32_16x16x32_bf16 v[16:19], v[80:83], a[4:7], v[16:19]\n\t"
      "ds_read_b128 a[28:31], v55 offset:256\n\t"
      "v_mfma_f32_16x16x32_bf16 v[16:19], v[96:99], a[8:11], v[16:19]\n\t"
      "ds_read_b128 a[32:35], v52 offset:512\n\t"
      "v_mfma_f32_16x16x32_bf16 v[16:19], v[112:115], a[12:15], v[16:19]\n\t"
      "ds_read_b128 a[36:39], v53 offset:512\n\t"
      "v_mfma_f32_16x16x32_bf16 v[16:19], v[128:131], a[16:19], v[16:19]\n\t"
      "ds_read_b128 a[40:43], v54 offset:512\n\t"
      "v_mfma_f32_16x16x32_bf16 v[16:19], v[144:147], a[20:23], v[16:19]\n\t"
      "ds_read_b128 a[44:47], v55 offset:512\n\t"
      "v_mfma_f32_16x16x32_bf16 v[20:23], v[68:71], a[0:3], 0\n\t"
      "v_cvt_pk_bf16_f32 v50, v36, v37\n\t"
      "v_cvt_pk_bf16_f32 v51, v38, v39\n\t"
      "v_mfma_f32_16x16x32_bf16 v[20:23], v[84:87], a[4:7], v[20:23]\n\t"
      "ds_write_b64 v0, v[50:51] offset:160\n\t"
      "v_mfma_f32_16x16x32_bf16 v[20:23], v[100:103], a[8:11], v[20:23]\n\t"
      "v_add_u32 v56, s51, v15\n\t"
      "v_xor_b32 v57, 64, v56\n\t"
      "v_mfma_f32_16x16x32_bf16 v[20:23], v[116:119], a[12:15], v[20:23]\n\t"
      "v_xor_b32 v58, 128, v56\n\t"
      "v_xor_b32 v59, 192, v56\n\t"
      "v_mfma_f32_16x16x32_bf16 v[20:23], v[132:135], a[16:19], v[20:23]\n\t"
      "v_mfma_f32_16x16x32_bf16 v[20:23], v[148:151], a[20:23], v[20:23]\n\t"
      "v_mfma_f32_16x16x32_bf16 v[24:27], v[72:75], a[0:3], 0\n\t"
      "v_mfma_f32_16x16x32_bf16 v[24:27], v[88:91], a[4:7], v[24:27]\n\t"
      "v_mfma_f32_16x16x32_bf16 v[24:27], v[104:107], a[8:11], v[24:27]\n\t"
      "v_mfma_f32_16x16x32_bf16 v[24:27], v[120:123], a[12:15], v[24:27]\n\t"
      "v_mfma_f32_16x16x32_bf16 v[24:27], v[136:139], a[16:19], v[24:27]\n\t"
      "v_mfma_f32_16x16x32_bf16 v[24:27], v[152:155], a[20:23], v[24:27]\n\t"
      "s_waitcnt vmcnt(12)\n\t"
      "s_waitcnt lgkmcnt(0)\n\t"
      "s_barrier\n\t"
      "v_mfma_f32_16x16x32_bf16 v[28:31], v[76:79], a[0:3], 0\n\t"
      "ds_read_b128 a[48:51], v8 offset:0\n\t"
      "v_mfma_f32_16x16x32_bf16 v[28:31], v[92:95], a[4:7], v[28:31]\n\t"
      "ds_read_b128 a[52:55], v60 offset:1024\n\t"
      "v_mfma_f32_16x16x32_bf16 v[28:31], v[108:111], a[8:11], v[28:31]\n\t"
      "ds_read_b128 a[56:59], v61 offset:2048\n\t"
      "v_mfma_f32_16x16x32_bf16 v[28:31], v[124:127], a[12:15], v[28:31]\n\t"
      "v_mfma_f32_16x16x32_bf16 v[28:31], v[140:143], a[16:19], v[28:31]\n\t"
      "v_mfma_f32_16x16x32_bf16 v[28:31], v[156:159], a[20:23], v[28:31]\n\t"
      "v_mfma_f32_16x16x32_bf16 v[32:35], a[60:63], a[0:3], 0\n\t"
      "v_mfma_f32_16x16x32_bf16 v[32:35], a[68:71], a[4:7], v[32:35]\n\t"
      "s_waitcnt lgkmcnt(2)\n\t"
      "v_mfma_f32_16x16x32_bf16 v[32:35], a[76:79], a[8:11], v[32:35]\n\t"
      "buffer_store_dwordx4 a[48:51], v9, s[44:47], s49 offen sc1\n\t"
      "v_mfma_f32_16x16x32_bf16 v[32:35], a[84:87], a[12:15], v[32:35]\n\t"
      "v_mfma_f32_16x16x32_bf16 v[32:35], a[92:95], a[16:19], v[32:35]\n\t"
      "v_mfma_f32_16x16x32_bf16 v[32:35], a[100:103], a[20:23], v[32:35]\n\t"
      "s_add_u32 m0, s18, s50\n\t"
      "v_mfma_f32_16x16x32_bf16 v[36:39], a[64:67], a[0:3], 0\n\t"
      "buffer_load_dwordx4 v5, s[40:43], s48 offen nt lds\n\t"
      "v_mfma_f32_16x16x32_bf16 v[36:39], a[72:75], a[4:7], v[36:39]\n\t"
      "v_mfma_f32_16x16x32_bf16 v[36:39], a[80:83], a[8:11], v[36:39]\n\t"
      "v_mfma_f32_16x16x32_bf16 v[36:39], a[88:91], a[12:15], v[36:39]\n\t"
      "s_waitcnt lgkmcnt(1)\n\t"
      "v_mfma_f32_16x16x32_bf16 v[36:39], a[96:99], a[16:19], v[36:39]\n\t"
      "buffer_store_dwordx4 a[52:55], v10, s[44:47], s49 offen offset:1024 sc1\n\t"
      "v_mfma_f32_16x16x32_bf16 v[36:39], a[104:107], a[20:23], v[36:39]\n\t"
      "v_mfma_f32_16x16x32_bf16 v[16:19], v[160:163], a[24:27], v[16:19]\n\t"
      "ds_read_b128 a[0:3], v56 offset:0\n\t"
      "v_mfma_f32_16x16x32_bf16 v[16:19], v[176:179], a[28:31], v[16:19]\n\t"
      "ds_read_b128 a[4:7], v57 offset:0\n\t"
      "v_mfma_f32_16x16x32_bf16 v[16:19], v[192:195], a[32:35], v[16:19]\n\t"
      "ds_read_b128 a[8:11], v58 offset:0\n\t"
      "v_mfma_f32_16x16x32_bf16 v[16:19], v[208:211], a[36:39], v[16:19]\n\t"
      "ds_read_b128 a[12:15], v59 offset:0\n\t"
      "v_mfma_f32_16x16x32_bf16 v[16:19], v[224:227], a[40:43], v[16:19]\n\t"
      "ds_read_b128 a[16:19], v56 offset:256\n\t"
      "v_mfma_f32_16x16x32_bf16 v[16:19], v[240:243], a[44:47], v[16:19]\n\t"
      "ds_read_b128 a[20:23], v57 offset:256\n\t"
      "v_mfma_f32_16x16x32_bf16 v[20:23], v[164:167], a[24:27], v[20:23]\n\t"
      "s_add_u32 s51, s51, 12288\n\t"
      "s_cmp_eq_u32 s51, 160256\n\t"
      "s_cselect_b32 s51, 25088, s51\n\t"
      "v_mfma_f32_16x16x32_bf16 v[20:23], v[180:183], a[28:31], v[20:23]\n\t"
      "s_waitcnt lgkmcnt(6)\n\t"
      "v_mfma_f32_16x16x32_bf16 v[20:23], v[196:199], a[32:35], v[20:23]\n\t"
      "buffer_store_dwordx4 a[56:59], v11, s[44:47], s49 offen offset:2048 sc1\n\t"
      "v_mfma_f32_16x16x32_bf16 v[20:23], v[212:215], a[36:39], v[20:23]\n\t"
      "v_mfma_f32_16x16x32_bf16 v[20:23], v[228:231], a[40:43], v[20:23]\n\t"
      "s_waitcnt lgkmcnt(0)\n\t"
      "v_mfma_f32_16x16x32_bf16 v[20:23], v[244:247], a[44:47], v[20:23]\n\t"
      "v_mfma_f32_16x16x32_bf16 v[24:27], v[168:171], a[24:27], v[24:27]\n\t"
      "v_mfma_f32_16x16x32_bf16 v[24:27], v[184:187], a[28:31], v[24:27]\n\t"
      "v_cvt_pk_bf16_f32 v40, v16, v17\n\t"
      "v_cvt_pk_bf16_f32 v41, v18, v19\n\t"
      "v_mfma_f32_16x16x32_bf16 v[24:27], v[200:203], a[32:35], v[24:27]\n\t"
      "ds_write_b64 v0, v[40:41] offset:12544\n\t"
      "v_mfma_f32_16x16x32_bf16 v[24:27], v[216:219], a[36:39], v[24:27]\n\t"
      "buffer_load_dwordx4 v6, s[40:43], s48 offen offset:1024 nt lds\n\t"
      "v_mfma_f32_16x16x32_bf16 v[24:27], v[232:235], a[40:43], v[24:27]\n\t"
      "v_mfma_f32_16x16x32_bf16 v[24:27], v[248:251], a[44:47], v[24:27]\n\t"
      "v_mfma_f32_16x16x32_bf16 v[28:31], v[172:175], a[24:27], v[28:31]\n\t"
      "v_mfma_f32_16x16x32_bf16 v[28:31], v[188:191], a[28:31], v[28:31]\n\t"
      "v_cvt_pk_bf16_f32 v42, v20, v21\n\t"
      "v_cvt_pk_bf16_f32 v43, v22, v23\n\t"
      "v_mfma_f32_16x16x32_bf16 v[28:31], v[204:207], a[32:35], v[28:31]\n\t"
      "ds_write_b64 v0, v[42:43] offset:12576\n\t"
      "v_mfma_f32_16x16x32_bf16 v[28:31], v[220:223], a[36:39], v[28:31]\n\t"
      "v_mfma_f32_16x16x32_bf16 v[28:31], v[236:239], a[40:43], v[28:31]\n\t"
      "v_mfma_f32_16x16x32_bf16 v[28:31], v[252:255], a[44:47], v[28:31]\n\t"
      "v_mfma_f32_16x16x32_bf16 v[32:35], a[108:111], a[24:27], v[32:35]\n\t"
      "v_mfma_f32_16x16x32_bf16 v[32:35], a[116:119], a[28:31], v[32:35]\n\t"
      "v_cvt_pk_bf16_f32 v44, v24, v25\n\t"
      "v_cvt_pk_bf16_f32 v45, v26, v27\n\t"
      "v_mfma_f32_16x16x32_bf16 v[32:35], a[124:127], a[32:35], v[32:35]\n\t"
      "ds_write_b64 v0, v[44:45] offset:12608\n\t"
      "v_mfma_f32_16x16x32_bf16 v[32:35], a[132:135], a[36:39], v[32:35]\n\t"
      "buffer_load_dwordx4 v7, s[40:43], s48 offen offset:2048 nt lds\n\t"
      "v_mfma_f32_16x16x32_bf16 v[32:35], a[140:143], a[40:43], v[32:35]\n\t"
      "s_add_u32 s50, s50, 12288\n\t"
      "v_mfma_f32_16x16x32_bf16 v[32:35], a[148:151], a[44:47], v[32:35]\n\t"
      "v_cvt_pk_bf16_f32 v46, v28, v29\n\t"
      "v_cvt_pk_bf16_f32 v47, v30, v31\n\t"
      "v_mfma_f32_16x16x32_bf16 v[36:39], a[112:115], a[24:27], v[36:39]\n\t"
      "ds_write_b64 v0, v[46:47] offset:12640\n\t"
      "v_mfma_f32_16x16x32_bf16 v[36:39], a[120:123], a[28:31], v[36:39]\n\t"
      "s_cmp_eq_u32 s50, 160256\n\t"
      "s_cselect_b32 s50, 25088, s50\n\t"
      "v_mfma_f32_16x16x32_bf16 v[36:39], a[128:131], a[32:35], v[36:39]\n\t"
      "s_sub_u32 s52, s52, 1\n\t"
      "s_cmp_lg_u32 s52, 0\n\t"
      "v_mfma_f32_16x16x32_bf16 v[36:39], a[136:139], a[36:39], v[36:39]\n\t"
      "v_mfma_f32_16x16x32_bf16 v[36:39], a[144:147], a[40:43], v[36:39]\n\t"
      "v_cvt_pk_bf16_f32 v48, v32, v33\n\t"
      "v_cvt_pk_bf16_f32 v49, v34, v35\n\t"
      "v_mfma_f32_16x16x32_bf16 v[36:39], a[152:155], a[44:47], v[36:39]\n\t"
      "ds_write_b64 v0, v[48:49] offset:12672\n\t"
      "s_cbranch_scc1 .Lloop3_%=\n\t"
      ".Lendc_%=:\n\t"
      "s_cmp_eq_u32 s48, 113221632\n\t"
      "s_cbranch_scc1 .Lwrap3_%=\n\t"
      ".Lwrapped3_%=:\n\t"
      "v_mfma_f32_16x16x32_bf16 v[16:19], v[64:67], a[0:3], 0\n\t"
      "ds_read_b128 a[24:27], v58 offset:256\n\t"
      "v_mfma_f32_16x16x32_bf16 v[16:19], v[80:83], a[4:7], v[16:19]\n\t"
      "ds_read_b128 a[28:31], v59 offset:256\n\t"
      "v_mfma_f32_16x16x32_bf16 v[16:19], v[96:99], a[8:11], v[16:19]\n\t"
      "ds_read_b128 a[32:35], v56 offset:512\n\t"
      "v_mfma_f32_16x16x32_bf16 v[16:19], v[112:115], a[12:15], v[16:19]\n\t"
      "ds_read_b128 a[36:39], v57 offset:512\n\t"
      "v_mfma_f32_16x16x32_bf16 v[16:19], v[128:131], a[16:19], v[16:19]\n\t"
      "ds_read_b128 a[40:43], v58 offset:512\n\t"
      "v_mfma_f32_16x16x32_bf16 v[16:19], v[144:147], a[20:23], v[16:19]\n\t"
      "ds_read_b128 a[44:47], v59 offset:512\n\t"
      "v_mfma_f32_16x16x32_bf16 v[20:23], v[68:71], a[0:3], 0\n\t"
      "v_cvt_pk_bf16_f32 v50, v36, v37\n\t"
      "v_cvt_pk_bf16_f32 v51, v38, v39\n\t"
      "v_mfma_f32_16x16x32_bf16 v[20:23], v[84:87], a[4:7], v[20:23]\n\t"
      "ds_write_b64 v0, v[50:51] offset:12704\n\t"
      "v_mfma_f32_16x16x32_bf16 v[20:23], v[100:103], a[8:11], v[20:23]\n\t"
      "v_add_u32 v52, s51, v15\n\t"
      "v_xor_b32 v53, 64, v52\n\t"
      "v_mfma_f32_16x16x32_bf16 v[20:23], v[116:119], a[12:15], v[20:23]\n\t"
      "v_xor_b32 v54, 128, v52\n\t"
      "v_xor_b32 v55, 192, v52\n\t"
      "v_mfma_f32_16x16x32_bf16 v[20:23], v[132:135], a[16:19], v[20:23]\n\t"
      "s_add_u32 s58, s58, 786432\n\t"
      "s_lshr_b32 s59, s59, 1\n\t"
      "v_mfma_f32_16x16x32_bf16 v[20:23], v[148:151], a[20:23], v[20:23]\n\t"
      "s_bitcmp1_b32 s59, 0\n\t"
      "s_cselect_b32 s84, 24576, 0\n\t"
      "s_add_u32 s84, s84, s58\n\t"
      "v_mfma_f32_16x16x32_bf16 v[24:27], v[72:75], a[0:3], 0\n\t"
      "s_add_u32 s58, s58, 786432\n\t"
      "s_lshr_b32 s59, s59, 1\n\t"
      "v_mfma_f32_16x16x32_bf16 v[24:27], v[88:91], a[4:7], v[24:27]\n\t"
      "s_bitcmp1_b32 s59, 0\n\t"
      "s_cselect_b32 s85, 24576, 0\n\t"
      "s_add_u32 s85, s85, s58\n\t"
      "v_mfma_f32_16x16x32_bf16 v[24:27], v[104:107], a[8:11], v[24:27]\n\t"
      "s_waitcnt vmcnt(12)\n\t"
      "s_waitcnt lgkmcnt(0)\n\t"
      "s_barrier\n\t"
      "v_mfma_f32_16x16x32_bf16 v[24:27], v[120:123], a[12:15], v[24:27]\n\t"
      "v_mfma_f32_16x16x32_bf16 v[24:27], v[136:139], a[16:19], v[24:27]\n\t"
      "v_mfma_f32_16x16x32_bf16 v[24:27], v[152:155], a[20:23], v[24:27]\n\t"
      "v_mfma_f32_16x16x32_bf16 v[28:31], v[76:79], a[0:3], 0\n\t"
      "ds_read_b128 a[188:191], v8 offset:12544\n\t"
      "v_mfma_f32_16x16x32_bf16 v[28:31], v[92:95], a[4:7], v[28:31]\n\t"
      "ds_read_b128 a[192:195], v60 offset:13568\n\t"
      "v_mfma_f32_16x16x32_bf16 v[28:31], v[108:111], a[8:11], v[28:31]\n\t"
      "ds_read_b128 a[196:199], v61 offset:14592\n\t"
      "v_mfma_f32_16x16x32_bf16 v[28:31], v[124:127], a[12:15], v[28:31]\n\t"
      "v_mfma_f32_16x16x32_bf16 v[28:31], v[140:143], a[16:19], v[28:31]\n\t"
      "v_mfma_f32_16x16x32_bf16 v[28:31], v[156:159], a[20:23], v[28:31]\n\t"
      "v_mfma_f32_16x16x32_bf16 v[32:35], a[60:63], a[0:3], 0\n\t"
      "v_mfma_f32_16x16x32_bf16 v[32:35], a[68:71], a[4:7], v[32:35]\n\t"
      "v_mfma_f32_16x16x32_bf16 v[32:35], a[76:79], a[8:11], v[32:35]\n\t"
      "v_mfma_f32_16x16x32_bf16 v[32:35], a[84:87], a[12:15], v[32:35]\n\t"
      "v_mfma_f32_16x16x32_bf16 v[32:35], a[92:95], a[16:19], v[32:35]\n\t"
      "v_mfma_f32_16x16x32_bf16 v[32:35], a[100:103], a[20:23], v[32:35]\n\t"
      "v_mfma_f32_16x16x32_bf16 v[36:39], a[64:67], a[0:3], 0\n\t"
      "v_mfma_f32_16x16x32_bf16 v[36:39], a[72:75], a[4:7], v[36:39]\n\t"
      "v_mfma_f32_16x16x32_bf16 v[36:39], a[80:83], a[8:11], v[36:39]\n\t"
      "v_mfma_f32_16x16x32_bf16 v[36:39], a[88:91], a[12:15], v[36:39]\n\t"
      "v_mfma_f32_16x16x32_bf16 v[36:39], a[96:99], a[16:19], v[36:39]\n\t"
      "v_mfma_f32_16x16x32_bf16 v[36:39], a[104:107], a[20:23], v[36:39]\n\t"
      "v_mfma_f32_16x16x32_bf16 v[16:19], v[160:163], a[24:27], v[16:19]\n\t"
      "ds_read_b128 a[0:3], v52 offset:0\n\t"
      "v_mfma_f32_16x16x32_bf16 v[16:19], v[176:179], a[28:31], v[16:19]\n\t"
      "ds_read_b128 a[4:7], v53 offset:0\n\t"
      "v_mfma_f32_16x16x32_bf16 v[16:19], v[192:195], a[32:35], v[16:19]\n\t"
      "ds_read_b128 a[8:11], v54 offset:0\n\t"
      "v_mfma_f32_16x16x32_bf16 v[16:19], v[208:211], a[36:39], v[16:19]\n\t"
      "ds_read_b128 a[12:15], v55 offset:0\n\t"
      "v_mfma_f32_16x16x32_bf16 v[16:19], v[224:227], a[40:43], v[16:19]\n\t"
      "ds_read_b128 a[16:19], v52 offset:256\n\t"
      "v_mfma_f32_16x16x32_bf16 v[16:19], v[240:243], a[44:47], v[16:19]\n\t"
      "ds_read_b128 a[20:23], v53 offset:256\n\t"
      "v_mfma_f32_16x16x32_bf16 v[20:23], v[164:167], a[24:27], v[20:23]\n\t"
      "s_add_u32 s51, s51, 12288\n\t"
      "s_cmp_eq_u32 s51, 160256\n\t"
      "s_cselect_b32 s51, 25088, s51\n\t"
      "v_mfma_f32_16x16x32_bf16 v[20:23], v[180:183], a[28:31], v[20:23]\n\t"
      "v_mfma_f32_16x16x32_bf16 v[20:23], v[196:199], a[32:35], v[20:23]\n\t"
      "v_mfma_f32_16x16x32_bf16 v[20:23], v[212:215], a[36:39], v[20:23]\n\t"
      "v_mfma_f32_16x16x32_bf16 v[20:23], v[228:231], a[40:43], v[20:23]\n\t"
      "s_waitcnt lgkmcnt(0)\n\t"
      "v_mfma_f32_16x16x32_bf16 v[20:23], v[244:247], a[44:47], v[20:23]\n\t"
      "v_mfma_f32_16x16x32_bf16 v[24:27], v[168:171], a[24:27], v[24:27]\n\t"
      "v_mfma_f32_16x16x32_bf16 v[24:27], v[184:187], a[28:31], v[24:27]\n\t"
      "v_cvt_pk_bf16_f32 v40, v16, v17\n\t"
      "v_cvt_pk_bf16_f32 v41, v18, v19\n\t"
      "v_mfma_f32_16x16x32_bf16 v[24:27], v[200:203], a[32:35], v[24:27]\n\t"
      "ds_write_b64 v0, v[40:41] offset:0\n\t"
      "v_mfma_f32_16x16x32_bf16 v[24:27], v[216:219], a[36:39], v[24:27]\n\t"
      "v_mfma_f32_16x16x32_bf16 v[24:27], v[232:235], a[40:43], v[24:27]\n\t"
      "v_mfma_f32_16x16x32_bf16 v[24:27], v[248:251], a[44:47], v[24:27]\n\t"
      "v_mfma_f32_16x16x32_bf16 v[28:31], v[172:175], a[24:27], v[28:31]\n\t"
      "v_mfma_f32_16x16x32_bf16 v[28:31], v[188:191], a[28:31], v[28:31]\n\t"
      "v_cvt_pk_bf16_f32 v42, v20, v21\n\t"
      "v_cvt_pk_bf16_f32 v43, v22, v23\n\t"
      "v_mfma_f32_16x16x32_bf16 v[28:31], v[204:207], a[32:35], v[28:31]\n\t"
      "ds_write_b64 v0, v[42:43] offset:32\n\t"
      "v_mfma_f32_16x16x32_bf16 v[28:31], v[220:223], a[36:39], v[28:31]\n\t"
      "v_mfma_f32_16x16x32_bf16 v[28:31], v[236:239], a[40:43], v[28:31]\n\t"
      "v_mfma_f32_16x16x32_bf16 v[28:31], v[252:255], a[44:47], v[28:31]\n\t"
      "v_mfma_f32_16x16x32_bf16 v[32:35], a[108:111], a[24:27], v[32:35]\n\t"
      "v_mfma_f32_16x16x32_bf16 v[32:35], a[116:119], a[28:31], v[32:35]\n\t"
      "v_cvt_pk_bf16_f32 v44, v24, v25\n\t"
      "v_cvt_pk_bf16_f32 v45, v26, v27\n\t"
      "v_mfma_f32_16x16x32_bf16 v[32:35], a[124:127], a[32:35], v[32:35]\n\t"
      "ds_write_b64 v0, v[44:45] offset:64\n\t"
      "v_mfma_f32_16x16x32_bf16 v[32:35], a[132:135], a[36:39], v[32:35]\n\t"
      "v_mfma_f32_16x16x32_bf16 v[32:35], a[140:143], a[40:43], v[32:35]\n\t"
      "v_mfma_f32_16x16x32_bf16 v[32:35], a[148:151], a[44:47], v[32:35]\n\t"
      "v_cvt_pk_bf16_f32 v46, v28, v29\n\t"
      "v_cvt_pk_bf16_f32 v47, v30, v31\n\t"
      "v_mfma_f32_16x16x32_bf16 v[36:39], a[112:115], a[24:27], v[36:39]\n\t"
      "ds_write_b64 v0, v[46:47] offset:96\n\t"
      "v_mfma_f32_16x16x32_bf16 v[36:39], a[120:123], a[28:31], v[36:39]\n\t"
      "v_mfma_f32_16x16x32_bf16 v[36:39], a[128:131], a[32:35], v[36:39]\n\t"
      "v_mfma_f32_16x16x32_bf16 v[36:39], a[136:139], a[36:39], v[36:39]\n\t"
      "v_mfma_f32_16x16x32_bf16 v[36:39], a[144:147], a[40:43], v[36:39]\n\t"
      "v_cvt_pk_bf16_f32 v48, v32, v33\n\t"
      "v_cvt_pk_bf16_f32 v49, v34, v35\n\t"
      "v_mfma_f32_16x16x32_bf16 v[36:39], a[152:155], a[44:47], v[36:39]\n\t"
      "ds_write_b64 v0, v[48:49] offset:128\n\t"
      "v_mfma_f32_16x16x32_bf16 v[16:19], v[64:67], a[0:3], 0\n\t"
      "ds_read_b128 a[24:27], v54 offset:256\n\t"
      "v_mfma_f32_16x16x32_bf16 v[16:19], v[80:83], a[4:7], v[16:19]\n\t"
      "ds_read_b128 a[28:31], v55 offset:256\n\t"
      "v_mfma_f32_16x16x32_bf16 v[16:19], v[96:99], a[8:11], v[16:19]\n\t"
      "ds_read_b128 a[32:35], v52 offset:512\n\t"
      "v_mfma_f32_16x16x32_bf16 v[16:19], v[112:115], a[12:15], v[16:19]\n\t"
      "ds_read_b128 a[36:39], v53 offset:512\n\t"
      "v_mfma_f32_16x16x32_bf16 v[16:19], v[128:131], a[16:19], v[16:19]\n\t"
      "ds_read_b128 a[40:43], v54 offset:512\n\t"
      "v_mfma_f32_16x16x32_bf16 v[16:19], v[144:147], a[20:23], v[16:19]\n\t"
      "ds_read_b128 a[44:47], v55 offset:512\n\t"
      "v_mfma_f32_16x16x32_bf16 v[20:23], v[68:71], a[0:3], 0\n\t"
      "v_cvt_pk_bf16_f32 v50, v36, v37\n\t"
      "v_cvt_pk_bf16_f32 v51, v38, v39\n\t"
      "v_mfma_f32_16x16x32_bf16 v[20:23], v[84:87], a[4:7], v[20:23]\n\t"
      "ds_write_b64 v0, v[50:51] offset:160\n\t"
      "v_mfma_f32_16x16x32_bf16 v[20:23], v[100:103], a[8:11], v[20:23]\n\t"
      "v_add_u32 v56, s51, v15\n\t"
      "v_xor_b32 v57, 64, v56\n\t"
      "v_mfma_f32_16x16x32_bf16 v[20:23], v[116:119], a[12:15], v[20:23]\n\t"
      "v_xor_b32 v58, 128, v56\n\t"
      "v_xor_b32 v59, 192, v56\n\t"
      "v_mfma_f32_16x16x32_bf16 v[20:23], v[132:135], a[16:19], v[20:23]\n\t"
      "v_mfma_f32_16x16x32_bf16 v[20:23], v[148:151], a[20:23], v[20:23]\n\t"
      "v_mfma_f32_16x16x32_bf16 v[24:27], v[72:75], a[0:3], 0\n\t"
      "v_mfma_f32_16x16x32_bf16 v[24:27], v[88:91], a[4:7], v[24:27]\n\t"
      "v_mfma_f32_16x16x32_bf16 v[24:27], v[104:107], a[8:11], v[24:27]\n\t"
      "s_waitcnt vmcnt(6)\n\t"
      "s_waitcnt lgkmcnt(0)\n\t"
      "s_barrier\n\t"
      "v_mfma_f32_16x16x32_bf16 v[24:27], v[120:123], a[12:15], v[24:27]\n\t"
      "v_mfma_f32_16x16x32_bf16 v[24:27], v[136:139], a[16:19], v[24:27]\n\t"
      "v_mfma_f32_16x16x32_bf16 v[24:27], v[152:155], a[20:23], v[24:27]\n\t"
      "v_mfma_f32_16x16x32_bf16 v[28:31], v[76:79], a[0:3], 0\n\t"
      "ds_read_b128 a[200:203], v8 offset:0\n\t"
      "v_mfma_f32_16x16x32_bf16 v[28:31], v[92:95], a[4:7], v[28:31]\n\t"
      "ds_read_b128 a[204:207], v60 offset:1024\n\t"
      "v_mfma_f32_16x16x32_bf16 v[28:31], v[108:111], a[8:11], v[28:31]\n\t"
      "ds_read_b128 a[208:211], v61 offset:2048\n\t"
      "v_mfma_f32_16x16x32_bf16 v[28:31], v[124:127], a[12:15], v[28:31]\n\t"
      "v_mfma_f32_16x16x32_bf16 v[28:31], v[140:143], a[16:19], v[28:31]\n\t"
      "v_mfma_f32_16x16x32_bf16 v[28:31], v[156:159], a[20:23], v[28:31]\n\t"
      "v_mfma_f32_16x16x32_bf16 v[32:35], a[60:63], a[0:3], 0\n\t"
      "v_mfma_f32_16x16x32_bf16 v[32:35], a[68:71], a[4:7], v[32:35]\n\t"
      "v_mfma_f32_16x16x32_bf16 v[32:35], a[76:79], a[8:11], v[32:35]\n\t"
      "v_mfma_f32_16x16x32_bf16 v[32:35], a[84:87], a[12:15], v[32:35]\n\t"
      "v_mfma_f32_16x16x32_bf16 v[32:35], a[92:95], a[16:19], v[32:35]\n\t"
      "v_mfma_f32_16x16x32_bf16 v[32:35], a[100:103], a[20:23], v[32:35]\n\t"
      "v_mfma_f32_16x16x32_bf16 v[36:39], a[64:67], a[0:3], 0\n\t"
      "v_mfma_f32_16x16x32_bf16 v[36:39], a[72:75], a[4:7], v[36:39]\n\t"
      "v_mfma_f32_16x16x32_bf16 v[36:39], a[80:83], a[8:11], v[36:39]\n\t"
      "v_mfma_f32_16x16x32_bf16 v[36:39], a[88:91], a[12:15], v[36:39]\n\t"
      "v_mfma_f32_16x16x32_bf16 v[36:39], a[96:99], a[16:19], v[36:39]\n\t"
      "v_mfma_f32_16x16x32_bf16 v[36:39], a[104:107], a[20:23], v[36:39]\n\t"
      "v_mfma_f32_16x16x32_bf16 v[16:19], v[160:163], a[24:27], v[16:19]\n\t"
      "ds_read_b128 a[0:3], v56 offset:0\n\t"
      "v_mfma_f32_16x16x32_bf16 v[16:19], v[176:179], a[28:31], v[16:19]\n\t"
      "ds_read_b128 a[4:7], v57 offset:0\n\t"
      "v_mfma_f32_16x16x32_bf16 v[16:19], v[192:195], a[32:35], v[16:19]\n\t"
      "ds_read_b128 a[8:11], v58 offset:0\n\t"
      "v_mfma_f32_16x16x32_bf16 v[16:19], v[208:211], a[36:39], v[16:19]\n\t"
      "ds_read_b128 a[12:15], v59 offset:0\n\t"
      "v_mfma_f32_16x16x32_bf16 v[16:19], v[224:227], a[40:43], v[16:19]\n\t"
      "ds_read_b128 a[16:19], v56 offset:256\n\t"
      "v_mfma_f32_16x16x32_bf16 v[16:19], v[240:243], a[44:47], v[16:19]\n\t"
      "ds_read_b128 a[20:23], v57 offset:256\n\t"
      "v_mfma_f32_16x16x32_bf16 v[20:23], v[164:167], a[24:27], v[20:23]\n\t"
      "s_add_u32 s51, s51, 12288\n\t"
      "s_cmp_eq_u32 s51, 160256\n\t"
      "s_cselect_b32 s51, 25088, s51\n\t"
      "v_mfma_f32_16x16x32_bf16 v[20:23], v[180:183], a[28:31], v[20:23]\n\t"
      "v_mfma_f32_16x16x32_bf16 v[20:23], v[196:199], a[32:35], v[20:23]\n\t"
      "buffer_store_dwordx4 a[188:191], v12, s[44:47], s49 offen sc1\n\t"
      "v_mfma_f32_16x16x32_bf16 v[20:23], v[212:215], a[36:39], v[20:23]\n\t"
      "v_mfma_f32_16x16x32_bf16 v[20:23], v[228:231], a[40:43], v[20:23]\n\t"
      "s_waitcnt lgkmcnt(0)\n\t"
      "v_mfma_f32_16x16x32_bf16 v[20:23], v[244:247], a[44:47], v[20:23]\n\t"
      "v_mfma_f32_16x16x32_bf16 v[24:27], v[168:171], a[24:27], v[24:27]\n\t"
      "v_mfma_f32_16x16x32_bf16 v[24:27], v[184:187], a[28:31], v[24:27]\n\t"
      "v_cvt_pk_bf16_f32 v40, v16, v17\n\t"
      "v_cvt_pk_bf16_f32 v41, v18, v19\n\t"
      "v_mfma_f32_16x16x32_bf16 v[24:27], v[200:203], a[32:35], v[24:27]\n\t"
      "ds_write_b64 v0, v[40:41] offset:12544\n\t"
      "v_mfma_f32_16x16x32_bf16 v[24:27], v[216:219], a[36:39], v[24:27]\n\t"
      "v_mfma_f32_16x16x32_bf16 v[24:27], v[232:235], a[40:43], v[24:27]\n\t"
      "v_mfma_f32_16x16x32_bf16 v[24:27], v[248:251], a[44:47], v[24:27]\n\t"
      "v_mfma_f32_16x16x32_bf16 v[28:31], v[172:175], a[24:27], v[28:31]\n\t"
      "v_mfma_f32_16x16x32_bf16 v[28:31], v[188:191], a[28:31], v[28:31]\n\t"
      "v_cvt_pk_bf16_f32 v42, v20, v21\n\t"
      "v_cvt_pk_bf16_f32 v43, v22, v23\n\t"
      "v_mfma_f32_16x16x32_bf16 v[28:31], v[204:207], a[32:35], v[28:31]\n\t"
      "ds_write_b64 v0, v[42:43] offset:12576\n\t"
      "v_mfma_f32_16x16x32_bf16 v[28:31], v[220:223], a[36:39], v[28:31]\n\t"
      "v_mfma_f32_16x16x32_bf16 v[28:31], v[236:239], a[40:43], v[28:31]\n\t"
      "buffer_store_dwordx4 a[192:195], v13, s[44:47], s49 offen offset:1024 sc1\n\t"
      "v_mfma_f32_16x16x32_bf16 v[28:31], v[252:255], a[44:47], v[28:31]\n\t"
      "v_mfma_f32_16x16x32_bf16 v[32:35], a[108:111], a[24:27], v[32:35]\n\t"
      "v_mfma_f32_16x16x32_bf16 v[32:35], a[116:119], a[28:31], v[32:35]\n\t"
      "v_cvt_pk_bf16_f32 v44, v24, v25\n\t"
      "v_cvt_pk_bf16_f32 v45, v26, v27\n\t"
      "v_mfma_f32_16x16x32_bf16 v[32:35], a[124:127], a[32:35], v[32:35]\n\t"
      "ds_write_b64 v0, v[44:45] offset:12608\n\t"
      "v_mfma_f32_16x16x32_bf16 v[32:35], a[132:135], a[36:39], v[32:35]\n\t"
      "v_mfma_f32_16x16x32_bf16 v[32:35], a[140:143], a[40:43], v[32:35]\n\t"
      "v_mfma_f32_16x16x32_bf16 v[32:35], a[148:151], a[44:47], v[32:35]\n\t"
      "v_cvt_pk_bf16_f32 v46, v28, v29\n\t"
      "v_cvt_pk_bf16_f32 v47, v30, v31\n\t"
      "v_mfma_f32_16x16x32_bf16 v[36:39], a[112:115], a[24:27], v[36:39]\n\t"
      "ds_write_b64 v0, v[46:47] offset:12640\n\t"
      "v_mfma_f32_16x16x32_bf16 v[36:39], a[120:123], a[28:31], v[36:39]\n\t"
      "v_mfma_f32_16x16x32_bf16 v[36:39], a[128:131], a[32:35], v[36:39]\n\t"
      "v_mfma_f32_16x16x32_bf16 v[36:39], a[136:139], a[36:39], v[36:39]\n\t"
      "v_mfma_f32_16x16x32_bf16 v[36:39], a[144:147], a[40:43], v[36:39]\n\t"
      "v_cvt_pk_bf16_f32 v48, v32, v33\n\t"
      "v_cvt_pk_bf16_f32 v49, v34, v35\n\t"
      "v_mfma_f32_16x16x32_bf16 v[36:39], a[152:155], a[44:47], v[36:39]\n\t"
      "ds_write_b64 v0, v[48:49] offset:12672\n\t"
      "v_mfma_f32_16x16x32_bf16 v[16:19], v[64:67], a[0:3], 0\n\t"
      "ds_read_b128 a[24:27], v58 offset:256\n\t"
      "v_mfma_f32_16x16x32_bf16 v[16:19], v[80:83], a[4:7], v[16:19]\n\t"
      "ds_read_b128 a[28:31], v59 offset:256\n\t"
      "v_mfma_f32_16x16x32_bf16 v[16:19], v[96:99], a[8:11], v[16:19]\n\t"
      "ds_read_b128 a[32:35], v56 offset:512\n\t"
      "v_mfma_f32_16x16x32_bf16 v[16:19], v[112:115], a[12:15], v[16:19]\n\t"
      "ds_read_b128 a[36:39], v57 offset:512\n\t"
      "buffer_store_dwordx4 a[196:199], v14, s[44:47], s49 offen offset:2048 sc1\n\t"
      "v_mfma_f32_16x16x32_bf16 v[16:19], v[128:131], a[16:19], v[16:19]\n\t"
      "ds_read_b128 a[40:43], v58 offset:512\n\t"
      "v_mfma_f32_16x16x32_bf16 v[16:19], v[144:147], a[20:23], v[16:19]\n\t"
      "ds_read_b128 a[44:47], v59 offset:512\n\t"
      "v_mfma_f32_16x16x32_bf16 v[20:23], v[68:71], a[0:3], 0\n\t"
      "v_cvt_pk_bf16_f32 v50, v36, v37\n\t"
      "v_cvt_pk_bf16_f32 v51, v38, v39\n\t"
      "v_mfma_f32_16x16x32_bf16 v[20:23], v[84:87], a[4:7], v[20:23]\n\t"
      "ds_write_b64 v0, v[50:51] offset:12704\n\t"
      "v_mfma_f32_16x16x32_bf16 v[20:23], v[100:103], a[8:11], v[20:23]\n\t"
      "v_add_u32 v52, s51, v15\n\t"
      "v_xor_b32 v53, 64, v52\n\t"
      "v_mfma_f32_16x16x32_bf16 v[20:23], v[116:119], a[12:15], v[20:23]\n\t"
      "v_xor_b32 v54, 128, v52\n\t"
      "v_xor_b32 v55, 192, v52\n\t"
      "v_mfma_f32_16x16x32_bf16 v[20:23], v[132:135], a[16:19], v[20:23]\n\t"
      "v_mfma_f32_16x16x32_bf16 v[20:23], v[148:151], a[20:23], v[20:23]\n\t"
      "v_mfma_f32_16x16x32_bf16 v[24:27], v[72:75], a[0:3], 0\n\t"
      "buffer_store_dwordx4 a[200:203], v9, s[44:47], s84 offen sc1\n\t"
      "v_mfma_f32_16x16x32_bf16 v[24:27], v[88:91], a[4:7], v[24:27]\n\t"
      "v_mfma_f32_16x16x32_bf16 v[24:27], v[104:107], a[8:11], v[24:27]\n\t"
      "s_waitcnt vmcnt(4)\n\t"
      "s_waitcnt lgkmcnt(0)\n\t"
      "s_barrier\n\t"
      "v_mfma_f32_16x16x32_bf16 v[24:27], v[120:123], a[12:15], v[24:27]\n\t"
      "v_mfma_f32_16x16x32_bf16 v[24:27], v[136:139], a[16:19], v[24:27]\n\t"
      "v_mfma_f32_16x16x32_bf16 v[24:27], v[152:155], a[20:23], v[24:27]\n\t"
      "v_mfma_f32_16x16x32_bf16 v[28:31], v[76:79], a[0:3], 0\n\t"
      "ds_read_b128 a[212:215], v8 offset:12544\n\t"
      "v_mfma_f32_16x16x32_bf16 v[28:31], v[92:95], a[4:7], v[28:31]\n\t"
      "ds_read_b128 a[216:219], v60 offset:13568\n\t"
      "v_mfma_f32_16x16x32_bf16 v[28:31], v[108:111], a[8:11], v[28:31]\n\t"
      "ds_read_b128 a[220:223], v61 offset:14592\n\t"
      "v_mfma_f32_16x16x32_bf16 v[28:31], v[124:127], a[12:15], v[28:31]\n\t"
      "v_mfma_f32_16x16x32_bf16 v[28:31], v[140:143], a[16:19], v[28:31]\n\t"
      "v_mfma_f32_16x16x32_bf16 v[28:31], v[156:159], a[20:23], v[28:31]\n\t"
      "v_mfma_f32_16x16x32_bf16 v[32:35], a[60:63], a[0:3], 0\n\t"
      "buffer_store_dwordx4 a[204:207], v10, s[44:47], s84 offen offset:1024 sc1\n\t"
      "v_mfma_f32_16x16x32_bf16 v[32:35], a[68:71], a[4:7], v[32:35]\n\t"
      "v_mfma_f32_16x16x32_bf16 v[32:35], a[76:79], a[8:11], v[32:35]\n\t"
      "v_mfma_f32_16x16x32_bf16 v[32:35], a[84:87], a[12:15], v[32:35]\n\t"
      "v_mfma_f32_16x16x32_bf16 v[32:35], a[92:95], a[16:19], v[32:35]\n\t"
      "v_mfma_f32_16x16x32_bf16 v[32:35], a[100:103], a[20:23], v[32:35]\n\t"
      "v_mfma_f32_16x16x32_bf16 v[36:39], a[64:67], a[0:3], 0\n\t"
      "v_mfma_f32_16x16x32_bf16 v[36:39], a[72:75], a[4:7], v[36:39]\n\t"
      "v_mfma_f32_16x16x32_bf16 v[36:39], a[80:83], a[8:11], v[36:39]\n\t"
      "v_mfma_f32_16x16x32_bf16 v[36:39], a[88:91], a[12:15], v[36:39]\n\t"
      "v_mfma_f32_16x16x32_bf16 v[36:39], a[96:99], a[16:19], v[36:39]\n\t"
      "buffer_store_dwordx4 a[208:211], v11, s[44:47], s84 offen offset:2048 sc1\n\t"
      "v_mfma_f32_16x16x32_bf16 v[36:39], a[104:107], a[20:23], v[36:39]\n\t"
      "v_mfma_f32_16x16x32_bf16 v[16:19], v[160:163], a[24:27], v[16:19]\n\t"
      "ds_read_b128 a[0:3], v52 offset:0\n\t"
      "v_mfma_f32_16x16x32_bf16 v[16:19], v[176:179], a[28:31], v[16:19]\n\t"
      "ds_read_b128 a[4:7], v53 offset:0\n\t"
      "v_mfma_f32_16x16x32_bf16 v[16:19], v[192:195], a[32:35], v[16:19]\n\t"
      "ds_read_b128 a[8:11], v54 offset:0\n\t"
      "v_mfma_f32_16x16x32_bf16 v[16:19], v[208:211], a[36:39], v[16:19]\n\t"
      "ds_read_b128 a[12:15], v55 offset:0\n\t"
      "v_mfma_f32_16x16x32_bf16 v[16:19], v[224:227], a[40:43], v[16:19]\n\t"
      "ds_read_b128 a[16:19], v52 offset:256\n\t"
      "v_mfma_f32_16x16x32_bf16 v[16:19], v[240:243], a[44:47], v[16:19]\n\t"
      "ds_read_b128 a[20:23], v53 offset:256\n\t"
      "v_mfma_f32_16x16x32_bf16 v[20:23], v[164:167], a[24:27], v[20:23]\n\t"
      "s_add_u32 s51, s51, 12288\n\t"
      "s_cmp_eq_u32 s51, 160256\n\t"
      "s_cselect_b32 s51, 25088, s51\n\t"
      "v_mfma_f32_16x16x32_bf16 v[20:23], v[180:183], a[28:31], v[20:23]\n\t"
      "s_waitcnt lgkmcnt(8)\n\t"
      "v_mfma_f32_16x16x32_bf16 v[20:23], v[196:199], a[32:35], v[20:23]\n\t"
      "buffer_store_dwordx4 a[212:215], v12, s[44:47], s84 offen sc1\n\t"
      "v_mfma_f32_16x16x32_bf16 v[20:23], v[212:215], a[36:39], v[20:23]\n\t"
      "v_mfma_f32_16x16x32_bf16 v[20:23], v[228:231], a[40:43], v[20:23]\n\t"
      "s_waitcnt lgkmcnt(0)\n\t"
      "v_mfma_f32_16x16x32_bf16 v[20:23], v[244:247], a[44:47], v[20:23]\n\t"
      "v_mfma_f32_16x16x32_bf16 v[24:27], v[168:171], a[24:27], v[24:27]\n\t"
      "v_mfma_f32_16x16x32_bf16 v[24:27], v[184:187], a[28:31], v[24:27]\n\t"
      "v_cvt_pk_bf16_f32 v40, v16, v17\n\t"
      "v_cvt_pk_bf16_f32 v41, v18, v19\n\t"
      "v_mfma_f32_16x16x32_bf16 v[24:27], v[200:203], a[32:35], v[24:27]\n\t"
      "ds_write_b64 v0, v[40:41] offset:0\n\t"
      "v_mfma_f32_16x16x32_bf16 v[24:27], v[216:219], a[36:39], v[24:27]\n\t"
      "v_mfma_f32_16x16x32_bf16 v[24:27], v[232:235], a[40:43], v[24:27]\n\t"
      "v_mfma_f32_16x16x32_bf16 v[24:27], v[248:251], a[44:47], v[24:27]\n\t"
      "v_mfma_f32_16x16x32_bf16 v[28:31], v[172:175], a[24:27], v[28:31]\n\t"
      "v_mfma_f32_16x16x32_bf16 v[28:31], v[188:191], a[28:31], v[28:31]\n\t"
      "v_cvt_pk_bf16_f32 v42, v20, v21\n\t"
      "v_cvt_pk_bf16_f32 v43, v22, v23\n\t"
      "v_mfma_f32_16x16x32_bf16 v[28:31], v[204:207], a[32:35], v[28:31]\n\t"
      "ds_write_b64 v0, v[42:43] offset:32\n\t"
      "v_mfma_f32_16x16x32_bf16 v[28:31], v[220:223], a[36:39], v[28:31]\n\t"
      "v_mfma_f32_16x16x32_bf16 v[28:31], v[236:239], a[40:43], v[28:31]\n\t"
      "buffer_store_dwordx4 a[216:219], v13, s[44:47], s84 offen offset:1024 sc1\n\t"
      "v_mfma_f32_16x16x32_bf16 v[28:31], v[252:255], a[44:47], v[28:31]\n\t"
      "v_mfma_f32_16x16x32_bf16 v[32:35], a[108:111], a[24:27], v[32:35]\n\t"
      "v_mfma_f32_16x16x32_bf16 v[32:35], a[116:119], a[28:31], v[32:35]\n\t"
      "v_cvt_pk_bf16_f32 v44, v24, v25\n\t"
      "v_cvt_pk_bf16_f32 v45, v26, v27\n\t"
      "v_mfma_f32_16x16x32_bf16 v[32:35], a[124:127], a[32:35], v[32:35]\n\t"
      "ds_write_b64 v0, v[44:45] offset:64\n\t"
      "v_mfma_f32_16x16x32_bf16 v[32:35], a[132:135], a[36:39], v[32:35]\n\t"
      "v_mfma_f32_16x16x32_bf16 v[32:35], a[140:143], a[40:43], v[32:35]\n\t"
      "v_mfma_f32_16x16x32_bf16 v[32:35], a[148:151], a[44:47], v[32:35]\n\t"
      "v_cvt_pk_bf16_f32 v46, v28, v29\n\t"
      "v_cvt_pk_bf16_f32 v47, v30, v31\n\t"
      "v_mfma_f32_16x16x32_bf16 v[36:39], a[112:115], a[24:27], v[36:39]\n\t"
      "ds_write_b64 v0, v[46:47] offset:96\n\t"
      "v_mfma_f32_16x16x32_bf16 v[36:39], a[120:123], a[28:31], v[36:39]\n\t"
      "v_mfma_f32_16x16x32_bf16 v[36:39], a[128:131], a[32:35], v[36:39]\n\t"
      "v_mfma_f32_16x16x32_bf16 v[36:39], a[136:139], a[36:39], v[36:39]\n\t"
      "v_mfma_f32_16x16x32_bf16 v[36:39], a[144:147], a[40:43], v[36:39]\n\t"
      "v_cvt_pk_bf16_f32 v48, v32, v33\n\t"
      "v_cvt_pk_bf16_f32 v49, v34, v35\n\t"
      "v_mfma_f32_16x16x32_bf16 v[36:39], a[152:155], a[44:47], v[36:39]\n\t"
      "ds_write_b64 v0, v[48:49] offset:128\n\t"
      "v_mfma_f32_16x16x32_bf16 v[16:19], v[64:67], a[0:3], 0\n\t"
      "ds_read_b128 a[24:27], v54 offset:256\n\t"
      "v_mfma_f32_16x16x32_bf16 v[16:19], v[80:83], a[4:7], v[16:19]\n\t"
      "ds_read_b128 a[28:31], v55 offset:256\n\t"
      "v_mfma_f32_16x16x32_bf16 v[16:19], v[96:99], a[8:11], v[16:19]\n\t"
      "ds_read_b128 a[32:35], v52 offset:512\n\t"
      "v_mfma_f32_16x16x32_bf16 v[16:19], v[112:115], a[12:15], v[16:19]\n\t"
      "ds_read_b128 a[36:39], v53 offset:512\n\t"
      "v_mfma_f32_16x16x32_bf16 v[16:19], v[128:131], a[16:19], v[16:19]\n\t"
      "ds_read_b128 a[40:43], v54 offset:512\n\t"
      "v_mfma_f32_16x16x32_bf16 v[16:19], v[144:147], a[20:23], v[16:19]\n\t"
      "ds_read_b128 a[44:47], v55 offset:512\n\t"
      "v_mfma_f32_16x16x32_bf16 v[20:23], v[68:71], a[0:3], 0\n\t"
      "v_cvt_pk_bf16_f32 v50, v36, v37\n\t"
      "v_cvt_pk_bf16_f32 v51, v38, v39\n\t"
      "v_mfma_f32_16x16x32_bf16 v[20:23], v[84:87], a[4:7], v[20:23]\n\t"
      "ds_write_b64 v0, v[50:51] offset:160\n\t"
      "v_mfma_f32_16x16x32_bf16 v[20:23], v[100:103], a[8:11], v[20:23]\n\t"
      "v_mfma_f32_16x16x32_bf16 v[20:23], v[116:119], a[12:15], v[20:23]\n\t"
      "v_mfma_f32_16x16x32_bf16 v[20:23], v[132:135], a[16:19], v[20:23]\n\t"
      "buffer_store_dwordx4 a[220:223], v14, s[44:47], s84 offen offset:2048 sc1\n\t"
      "v_mfma_f32_16x16x32_bf16 v[20:23], v[148:151], a[20:23], v[20:23]\n\t"
      "v_mfma_f32_16x16x32_bf16 v[24:27], v[72:75], a[0:3], 0\n\t"
      "v_mfma_f32_16x16x32_bf16 v[24:27], v[88:91], a[4:7], v[24:27]\n\t"
      "v_mfma_f32_16x16x32_bf16 v[24:27], v[104:107], a[8:11], v[24:27]\n\t"
      "s_waitcnt lgkmcnt(0)\n\t"
      "s_barrier\n\t"
      "v_mfma_f32_16x16x32_bf16 v[24:27], v[120:123], a[12:15], v[24:27]\n\t"
      "v_mfma_f32_16x16x32_bf16 v[24:27], v[136:139], a[16:19], v[24:27]\n\t"
      "v_mfma_f32_16x16x32_bf16 v[24:27], v[152:155], a[20:23], v[24:27]\n\t"
      "v_mfma_f32_16x16x32_bf16 v[28:31], v[76:79], a[0:3], 0\n\t"
      "ds_read_b128 a[224:227], v8 offset:0\n\t"
      "v_mfma_f32_16x16x32_bf16 v[28:31], v[92:95], a[4:7], v[28:31]\n\t"
      "ds_read_b128 a[228:231], v60 offset:1024\n\t"
      "v_mfma_f32_16x16x32_bf16 v[28:31], v[108:111], a[8:11], v[28:31]\n\t"
      "ds_read_b128 a[232:235], v61 offset:2048\n\t"
      "v_mfma_f32_16x16x32_bf16 v[28:31], v[124:127], a[12:15], v[28:31]\n\t"
      "v_mfma_f32_16x16x32_bf16 v[28:31], v[140:143], a[16:19], v[28:31]\n\t"
      "v_mfma_f32_16x16x32_bf16 v[28:31], v[156:159], a[20:23], v[28:31]\n\t"
      "v_mfma_f32_16x16x32_bf16 v[32:35], a[60:63], a[0:3], 0\n\t"
      "v_mfma_f32_16x16x32_bf16 v[32:35], a[68:71], a[4:7], v[32:35]\n\t"
      "s_waitcnt lgkmcnt(2)\n\t"
      "v_mfma_f32_16x16x32_bf16 v[32:35], a[76:79], a[8:11], v[32:35]\n\t"
      "buffer_store_dwordx4 a[224:227], v9, s[44:47], s85 offen sc1\n\t"
      "v_mfma_f32_16x16x32_bf16 v[32:35], a[84:87], a[12:15], v[32:35]\n\t"
      "v_mfma_f32_16x16x32_bf16 v[32:35], a[92:95], a[16:19], v[32:35]\n\t"
      "v_mfma_f32_16x16x32_bf16 v[32:35], a[100:103], a[20:23], v[32:35]\n\t"
      "v_mfma_f32_16x16x32_bf16 v[36:39], a[64:67], a[0:3], 0\n\t"
      "v_mfma_f32_16x16x32_bf16 v[36:39], a[72:75], a[4:7], v[36:39]\n\t"
      "v_mfma_f32_16x16x32_bf16 v[36:39], a[80:83], a[8:11], v[36:39]\n\t"
      "v_mfma_f32_16x16x32_bf16 v[36:39], a[88:91], a[12:15], v[36:39]\n\t"
      "s_waitcnt lgkmcnt(1)\n\t"
      "v_mfma_f32_16x16x32_bf16 v[36:39], a[96:99], a[16:19], v[36:39]\n\t"
      "buffer_store_dwordx4 a[228:231], v10, s[44:47], s85 offen offset:1024 sc1\n\t"
      "v_mfma_f32_16x16x32_bf16 v[36:39], a[104:107], a[20:23], v[36:39]\n\t"
      "v_mfma_f32_16x16x32_bf16 v[16:19], v[160:163], a[24:27], v[16:19]\n\t"
      "v_mfma_f32_16x16x32_bf16 v[16:19], v[176:179], a[28:31], v[16:19]\n\t"
      "v_mfma_f32_16x16x32_bf16 v[16:19], v[192:195], a[32:35], v[16:19]\n\t"
      "v_mfma_f32_16x16x32_bf16 v[16:19], v[208:211], a[36:39], v[16:19]\n\t"
      "v_mfma_f32_16x16x32_bf16 v[16:19], v[224:227], a[40:43], v[16:19]\n\t"
      "v_mfma_f32_16x16x32_bf16 v[16:19], v[240:243], a[44:47], v[16:19]\n\t"
      "v_mfma_f32_16x16x32_bf16 v[20:23], v[164:167], a[24:27], v[20:23]\n\t"
      "v_mfma_f32_16x16x32_bf16 v[20:23], v[180:183], a[28:31], v[20:23]\n\t"
      "s_waitcnt lgkmcnt(0)\n\t"
      "v_mfma_f32_16x16x32_bf16 v[20:23], v[196:199], a[32:35], v[20:23]\n\t"
      "buffer_store_dwordx4 a[232:235], v11, s[44:47], s85 offen offset:2048 sc1\n\t"
      "v_mfma_f32_16x16x32_bf16 v[20:23], v[212:215], a[36:39], v[20:23]\n\t"
      "v_mfma_f32_16x16x32_bf16 v[20:23], v[228:231], a[40:43], v[20:23]\n\t"
      "v_mfma_f32_16x16x32_bf16 v[20:23], v[244:247], a[44:47], v[20:23]\n\t"
      "v_mfma_f32_16x16x32_bf16 v[24:27], v[168:171], a[24:27], v[24:27]\n\t"
      "v_mfma_f32_16x16x32_bf16 v[24:27], v[184:187], a[28:31], v[24:27]\n\t"
      "v_cvt_pk_bf16_f32 v40, v16, v17\n\t"
      "v_cvt_pk_bf16_f32 v41, v18, v19\n\t"
      "v_mfma_f32_16x16x32_bf16 v[24:27], v[200:203], a[32:35], v[24:27]\n\t"
      "ds_write_b64 v0, v[40:41] offset:12544\n\t"
      "v_mfma_f32_16x16x32_bf16 v[24:27], v[216:219], a[36:39], v[24:27]\n\t"
      "v_mfma_f32_16x16x32_bf16 v[24:27], v[232:235], a[40:43], v[24:27]\n\t"
      "v_mfma_f32_16x16x32_bf16 v[24:27], v[248:251], a[44:47], v[24:27]\n\t"
      "v_mfma_f32_16x16x32_bf16 v[28:31], v[172:175], a[24:27], v[28:31]\n\t"
      "v_mfma_f32_16x16x32_bf16 v[28:31], v[188:191], a[28:31], v[28:31]\n\t"
      "v_cvt_pk_bf16_f32 v42, v20, v21\n\t"
      "v_cvt_pk_bf16_f32 v43, v22, v23\n\t"
      "v_mfma_f32_16x16x32_bf16 v[28:31], v[204:207], a[32:35], v[28:31]\n\t"
      "ds_write_b64 v0, v[42:43] offset:12576\n\t"
      "v_mfma_f32_16x16x32_bf16 v[28:31], v[220:223], a[36:39], v[28:31]\n\t"
      "v_mfma_f32_16x16x32_bf16 v[28:31], v[236:239], a[40:43], v[28:31]\n\t"
      "v_mfma_f32_16x16x32_bf16 v[28:31], v[252:255], a[44:47], v[28:31]\n\t"
      "v_mfma_f32_16x16x32_bf16 v[32:35], a[108:111], a[24:27], v[32:35]\n\t"
      "v_mfma_f32_16x16x32_bf16 v[32:35], a[116:119], a[28:31], v[32:35]\n\t"
      "v_cvt_pk_bf16_f32 v44, v24, v25\n\t"
      "v_cvt_pk_bf16_f32 v45, v26, v27\n\t"
      "v_mfma_f32_16x16x32_bf16 v[32:35], a[124:127], a[32:35], v[32:35]\n\t"
      "ds_write_b64 v0, v[44:45] offset:12608\n\t"
      "v_mfma_f32_16x16x32_bf16 v[32:35], a[132:135], a[36:39], v[32:35]\n\t"
      "v_mfma_f32_16x16x32_bf16 v[32:35], a[140:143], a[40:43], v[32:35]\n\t"
      "v_mfma_f32_16x16x32_bf16 v[32:35], a[148:151], a[44:47], v[32:35]\n\t"
      "v_cvt_pk_bf16_f32 v46, v28, v29\n\t"
      "v_cvt_pk_bf16_f32 v47, v30, v31\n\t"
      "v_mfma_f32_16x16x32_bf16 v[36:39], a[112:115], a[24:27], v[36:39]\n\t"
      "ds_write_b64 v0, v[46:47] offset:12640\n\t"
      "v_mfma_f32_16x16x32_bf16 v[36:39], a[120:123], a[28:31], v[36:39]\n\t"
      "v_mfma_f32_16x16x32_bf16 v[36:39], a[128:131], a[32:35], v[36:39]\n\t"
      "v_mfma_f32_16x16x32_bf16 v[36:39], a[136:139], a[36:39], v[36:39]\n\t"
      "v_mfma_f32_16x16x32_bf16 v[36:39], a[144:147], a[40:43], v[36:39]\n\t"
      "v_cvt_pk_bf16_f32 v48, v32, v33\n\t"
      "v_cvt_pk_bf16_f32 v49, v34, v35\n\t"
      "v_mfma_f32_16x16x32_bf16 v[36:39], a[152:155], a[44:47], v[36:39]\n\t"
      "ds_write_b64 v0, v[48:49] offset:12672\n\t"
      ".Ltail_%=:\n\t"
      "s_nop 6\n\t"
      "v_cvt_pk_bf16_f32 v50, v36, v37\n\t"
      "v_cvt_pk_bf16_f32 v51, v38, v39\n\t"
      "ds_write_b64 v0, v[50:51] offset:12704\n\t"
      "s_cmp_eq_u32 s85, 113221632\n\t"
      "s_cbranch_scc1 .Lwrapo_%=\n\t"
      ".Lwrappedo_%=:\n\t"
      "s_waitcnt lgkmcnt(0)\n\t"
      "s_barrier\n\t"
      "ds_read_b128 a[48:51], v8 offset:12544\n\t"
      "ds_read_b128 a[52:55], v60 offset:13568\n\t"
      "ds_read_b128 a[56:59], v61 offset:14592\n\t"
      "s_waitcnt lgkmcnt(2)\n\t"
      "buffer_store_dwordx4 a[48:51], v12, s[44:47], s85 offen sc1\n\t"
      "s_waitcnt lgkmcnt(1)\n\t"
      "buffer_store_dwordx4 a[52:55], v13, s[44:47], s85 offen offset:1024 sc1\n\t"
      "s_waitcnt lgkmcnt(0)\n\t"
      "buffer_store_dwordx4 a[56:59], v14, s[44:47], s85 offen offset:2048 sc1\n\t"
      "s_cmp_lt_i32 s91, 0\n\t"
      "s_cselect_b32 s30, -1, 0\n\t"
      "s_xor_b32 s16, s91, 0x3ffff\n\t"
      "s_and_b32 s16, s16, s30\n\t"
      "s_cbranch_scc0 .Lend_%=\n\t"
      "s_waitcnt vmcnt(0) lgkmcnt(0)\n\t"
      "s_barrier\n\t"
      "s_lshl1_add_u32 s30, s14, 24576\n\t"
      "s_sub_u32 s15, s30, s15\n\t"
      "s_branch .Lstart_%=\n\t"
      ".Lwrap3_%=:\n\t"
      "v_add_u32 v5, s48, v5\n\t"
      "v_subrev_u32 v62, 113246208, v5\n\t"
      "v_min_u32 v5, v5, v62\n\t"
      "v_add_u32 v6, s48, v6\n\t"
      "v_subrev_u32 v62, 113246208, v6\n\t"
      "v_min_u32 v6, v6, v62\n\t"
      "v_add_u32 v7, s48, v7\n\t"
      "v_subrev_u32 v62, 113246208, v7\n\t"
      "v_min_u32 v7, v7, v62\n\t"
      "s_mov_b32 s48, 0\n\t"
      "buffer_load_dwordx4 v5, s[40:43], s48 offen nt lds\n\t"
      "buffer_load_dwordx4 v6, s[40:43], s48 offen offset:1024 nt lds\n\t"
      "buffer_load_dwordx4 v7, s[40:43], s48 offen offset:2048 nt lds\n\t"
      "s_branch .Lwrapped3_%=\n\t"
      ".Lwrapo_%=:\n\t"
      "v_add_u32 v12, s85, v12\n\t"
      "v_subrev_u32 v62, 113246208, v12\n\t"
      "v_min_u32 v12, v12, v62\n\t"
      "v_add_u32 v13, s85, v13\n\t"
      "v_subrev_u32 v62, 113246208, v13\n\t"
      "v_min_u32 v13, v13, v62\n\t"
      "v_add_u32 v14, s85, v14\n\t"
      "v_subrev_u32 v62, 113246208, v14\n\t"
      "v_min_u32 v14, v14, v62\n\t"
      "s_mov_b32 s85, 0\n\t"
      "s_branch .Lwrappedo_%=\n\t"
      ".Lwait_%=:\n\t"
      "s_sub_u32 s52, s52, 1\n\t"
      "s_cbranch_scc1 .Lwait2_%=\n\t"
      "s_add_u32 s90, s90, 0x2000000\n\t"
      "v_mov_b32 v54, s90\n\t"
      "v_mov_b32 v55, s19\n\t"
      "global_store_dword v55, v54, s[12:13] sc1\n\t"
      "s_sleep 16\n\t"
      "global_load_dword v40, v62, s[12:13] sc1\n\t"
      "s_waitcnt vmcnt(0)\n\t"
      "s_branch .Lcheck_%=\n\t"
      ".Lwait2_%=:\n\t"
      "s_brev_b32 s91, 1\n\t"
      "s_branch .Lwaited_%=\n\t"
      ".Lspinf_%=:\n\t"
      "s_memrealtime s[36:37]\n\t"
      "s_waitcnt lgkmcnt(0)\n\t"
      "s_sub_u32 s53, s36, s38\n\t"
      "s_cmp_lt_i32 s53, 0\n\t"
      "s_cbranch_scc1 .Lspinf_%=\n\t"
      "s_branch .Lspunf_%=\n\t"
      ".Lspin0_%=:\n\t"
      "s_memrealtime s[36:37]\n\t"
      "s_waitcnt lgkmcnt(0)\n\t"
      "s_sub_u32 s53, s36, s38\n\t"
      "s_cmp_lt_i32 s53, 0\n\t"
      "s_cbranch_scc1 .Lspin0_%=\n\t"
      "s_branch .Lspun0_%=\n\t"
      ".Lspin1_%=:\n\t"
      "s_memrealtime s[36:37]\n\t"
      "s_waitcnt lgkmcnt(0)\n\t"
      "s_sub_u32 s53, s36, s38\n\t"
      "s_cmp_lt_i32 s53, 0\n\t"
      "s_cbranch_scc1 .Lspin1_%=\n\t"
      "s_branch .Lspun1_%=\n\t"
      ".Lend_%=:\n\t"
      : [tid] "+v"(tid)
      : [karg] "s"(__builtin_amdgcn_kernarg_segment_ptr()), [blk] "s"(blockIdx.x), [lds] "s"((unsigned)(size_t)lds)
      : "v1", "v2", "v3", "v4", "v5", "v6", "v7", "v8", "v9", "v10", "v11", "v12", "v13", "v14", "v15", "v16", "v17", "v18", "v19", "v20", "v21", "v22", "v23", "v24", "v25", "v26", "v27", "v28", "v29", "v30", "v31", "v32", "v33", "v34", "v35", "v36", "v37", "v38", "v39", "v40", "v41", "v42", "v43", "v44", "v45", "v46", "v47", "v48", "v49", "v50", "v51", "v52", "v53", "v54", "v55", "v56", "v57", "v58", "v59", "v60", "v61", "v62", "v63", "v64", "v65", "v66", "v67", "v68", "v69", "v70", "v71", "v72", "v73", "v74", "v75", "v76", "v77", "v78", "v79", "v80", "v81", "v82", "v83", "v84", "v85", "v86", "v87", "v88", "v89", "v90", "v91", "v92", "v93", "v94", "v95", "v96", "v97", "v98", "v99", "v100", "v101", "v102", "v103", "v104", "v105", "v106", "v107", "v108", "v109", "v110", "v111", "v112", "v113", "v114", "v115", "v116", "v117", "v118", "v119", "v120", "v121", "v122", "v123", "v124", "v125", "v126", "v127", "v128", "v129", "v130", "v131", "v132", "v133", "v134", "v135", "v136", "v137", "v138", "v139", "v140", "v141", "v142", "v143", "v144", "v145", "v146", "v147", "v148", "v149", "v150", "v151", "v152", "v153", "v154", "v155", "v156", "v157", "v158", "v159", "v160", "v161", "v162", "v163", "v164", "v165", "v166", "v167", "v168", "v169", "v170", "v171", "v172", "v173", "v174", "v175", "v176", "v177", "v178", "v179", "v180", "v181", "v182", "v183", "v184", "v185", "v186", "v187", "v188", "v189", "v190", "v191", "v192", "v193", "v194", "v195", "v196", "v197", "v198", "v199", "v200", "v201", "v202", "v203", "v204", "v205", "v206", "v207", "v208", "v209", "v210", "v211", "v212", "v213", "v214", "v215", "v216", "v217", "v218", "v219", "v220", "v221", "v222", "v223", "v224", "v225", "v226", "v227", "v228", "v229", "v230", "v231", "v232", "v233", "v234", "v235", "v236", "v237", "v238", "v239", "v240", "v241", "v242", "v243", "v244", "v245", "v246", "v247", "v248", "v249", "v250", "v251", "v252", "v253", "v254", "v255", "a0", "a1", "a2", "a3", "a4", "a5", "a6", "a7", "a8", "a9", "a10", "a11", "a12", "a13", "a14", "a15", "a16", "a17", "a18", "a19", "a20", "a21", "a22", "a23", "a24", "a25", "a26", "a27", "a28", "a29", "a30", "a31", "a32", "a33", "a34", "a35", "a36", "a37", "a38", "a39", "a40", "a41", "a42", "a43", "a44", "a45", "a46", "a47", "a48", "a49", "a50", "a51", "a52", "a53", "a54", "a55", "a56", "a57", "a58", "a59", "a60", "a61", "a62", "a63", "a64", "a65", "a66", "a67", "a68", "a69", "a70", "a71", "a72", "a73", "a74", "a75", "a76", "a77", "a78", "a79", "a80", "a81", "a82", "a83", "a84", "a85", "a86", "a87", "a88", "a89", "a90", "a91", "a92", "a93", "a94", "a95", "a96", "a97", "a98", "a99", "a100", "a101", "a102", "a103", "a104", "a105", "a106", "a107", "a108", "a109", "a110", "a111", "a112", "a113", "a114", "a115", "a116", "a117", "a118", "a119", "a120", "a121", "a122", "a123", "a124", "a125", "a126", "a127", "a128", "a129", "a130", "a131", "a132", "a133", "a134", "a135", "a136", "a137", "a138", "a139", "a140", "a141", "a142", "a143", "a144", "a145", "a146", "a147", "a148", "a149", "a150", "a151", "a152", "a153", "a154", "a155", "a156", "a157", "a158", "a159", "a160", "a161", "a162", "a163", "a164", "a165", "a166", "a167", "a168", "a169", "a170", "a171", "a172", "a173", "a174", "a175", "a176", "a177", "a178", "a179", "a180", "a181", "a182", "a183", "a184", "a185", "a186", "a187", "a188", "a189", "a190", "a191", "a192", "a193", "a194", "a195", "a196", "a197", "a198", "a199", "a200", "a201", "a202", "a203", "a204", "a205", "a206", "a207", "a208", "a209", "a210", "a211", "a212", "a213", "a214", "a215", "a216", "a217", "a218", "a219", "a220", "a221", "a222", "a223", "a224", "a225", "a226", "a227", "a228", "a229", "a230", "a231", "a232", "a233", "a234", "a235", "a236", "a237", "a238", "a239", "a240", "a241", "a242", "a243", "a244", "a245", "a246", "a247", "a248", "a249", "a250", "a251", "a252", "a253", "a254", "a255", "s4", "s5", "s6", "s7", "s8", "s9", "s10", "s11", "s12", "s13", "s14", "s15", "s16", "s17", "s18", "s19", "s20", "s21", "s22", "s23", "s24", "s25", "s26", "s27", "s28", "s29", "s30", "s31", "s32", "s33", "s34", "s35", "s36", "s37", "s38", "s39", "s40", "s41", "s42", "s43", "s44", "s45", "s46", "s47", "s48", "s49", "s50", "s51", "s52", "s53", "s54", "s55", "s56", "s57", "s58", "s59", "s60", "s61", "s62", "s63", "s64", "s65", "s66", "s67", "s68", "s69", "s70", "s71", "s72", "s73", "s74", "s75", "s76", "s77", "s78", "s79", "s80", "s81", "s82", "s83", "s84", "s85", "s86", "s87", "s88", "s89", "s90", "s91", "vcc", "scc", "memory");
}

__global__ __launch_bounds__(384) void dwsep_plain(const unsigned short* __restrict__ x, const unsigned short* __restrict__ dw,
                                                   const unsigned short* __restrict__ pw, unsigned short* __restrict__ out) {
  __shared__ float xs[384];
  const unsigned n = threadIdx.x;
  const size_t row = (size_t)blockIdx.x * 384u;
  xs[n] = __uint_as_float((unsigned)x[row + n] << 16) * __uint_as_float((unsigned)dw[n] << 16);
  __syncthreads();
  float acc = 0.f;
  for (int k = 0; k < 384; ++k) acc += xs[k] * __uint_as_float((unsigned)pw[k * 384 + n] << 16);
  const __bf16 o = (__bf16)acc;
  out[row + n] = __builtin_bit_cast(unsigned short, o);
}

extern "C" void kernel_launch(void* const* d_in, const int* in_sizes, int n_in, void* d_out, int out_size,
                              void* d_ws, size_t ws_size, hipStream_t stream) {
  const unsigned short* x = (const unsigned short*)d_in[0];
  const unsigned short* dw = (const unsigned short*)d_in[1];
  const unsigned short* pw = (const unsigned short*)d_in[2];
  unsigned short* out = (unsigned short*)d_out;
  if (in_sizes[0] == 56623104) dwsep_kernel<<<NWG, 256, 0, stream>>>(x, dw, pw, out, (unsigned*)d_ws);
  else dwsep_plain<<<in_sizes[0] / 384, 384, 0, stream>>>(x, dw, pw, out);
}
```

```cpp
#include <hip/hip_runtime.h>

constexpr unsigned ROWB = 768, TILEB = 12288, NWG = 256;

__global__ __launch_bounds__(256, 1) void dwsep_kernel(const unsigned short* __restrict__ x,
                                                       const unsigned short* __restrict__ dw,
                                                       const unsigned short* __restrict__ pw,
                                                       unsigned short* __restrict__ out, unsigned* ws) {
  __shared__ __attribute__((aligned(256))) char lds[163392];
  unsigned tid = threadIdx.x;
  asm volatile(
      "s_memtime s[32:33]\n\t"
      "s_load_dwordx8 s[4:11], %[karg], 0\n\t"
      "s_load_dwordx2 s[12:13], %[karg], 0x20\n\t"
      "v_readfirstlane_b32 s30, %[tid]\n\t"
      "s_lshr_b32 s31, %[blk], 1\n\t"
      "s_and_b32 s31, s31, 12\n\t"
      "s_and_b32 s34, %[blk], 3\n\t"
      "s_or_b32 s31, s31, s34\n\t"
      "s_lshr_b32 s30, s30, 6\n\t"
      "s_lshr_b32 s34, %[blk], 5\n\t"
      "s_bfe_u32 s35, %[blk], 0x10002\n\t"
      "s_mul_i32 s17, s30, 6144\n\t"
      "s_mul_i32 s18, s30, 3072\n\t"
      "s_mul_i32 s14, s34, 14155776\n\t"
      "s_mul_i32 s36, s31, 49152\n\t"
      "s_add_u32 s14, s14, s36\n\t"
      "s_mul_i32 s36, s35, 24576\n\t"
      "s_add_u32 s15, s14, s36\n\t"
      "s_mov_b32 s16, 0\n\t"
      "s_lshr_b32 s36, %[blk], 3\n\t"
      "s_lshl2_add_u32 s24, s36, s30\n\t"
      "s_sub_u32 s36, s24, s36\n\t"
      "s_sub_u32 s36, s36, 1\n\t"
      "s_mul_i32 s23, s36, 3072\n\t"
      "s_lshl_b32 s20, s34, 3\n\t"
      "s_mul_i32 s36, s34, 27\n\t"
      "s_lshr_b32 s36, s36, 2\n\t"
      "s_lshr_b32 s37, s31, 1\n\t"
      "s_add_u32 s19, s37, s20\n\t"
      "s_add_u32 s37, s37, s36\n\t"
      "s_min_u32 s37, s37, 53\n\t"
      "s_lshl_b32 s22, s37, 21\n\t"
      "s_lshl_b32 s36, s36, 21\n\t"
      "s_sub_u32 s21, s14, s36\n\t"
      "s_add_u32 s21, s21, 24576\n\t"
      "s_lshl_b32 s19, s19, 8\n\t"
      "s_and_b32 s31, s31, 1\n\t"
      "s_lshl1_add_u32 s31, s31, s35\n\t"
      "s_lshl2_add_u32 s19, s31, s19\n\t"
      ".Lstart_%=:\n\t"
      "v_mbcnt_lo_u32_b32 v233, -1, 0\n\t"
      "v_mbcnt_hi_u32_b32 v233, -1, v233\n\t"
      "s_mul_i32 s30, s17, 683\n\t"
      "s_lshr_b32 s30, s30, 16\n\t"
      "s_and_b32 s30, s30, 0xc0\n\t"
      "s_brev_b32 s34, 8\n\t"
      "s_mov_b32 s35, 0x20201010\n\t"
      "s_mov_b32 s36, 0x30303020\n\t"
      "v_lshl_add_u32 v1, v233, 4, s18\n\t"
      "v_lshrrev_b32 v16, 1, v233\n\t"
      "v_and_b32 v16, 24, v16\n\t"
      "v_bfe_u32 v17, s34, v16, 8\n\t"
      "v_or_b32 v17, s30, v17\n\t"
      "v_xor_b32 v18, v17, v1\n\t"
      "v_add_u32 v19, 80, v17\n\t"
      "v_add_u32 v17, 160, v17\n\t"
      "v_and_b32 v19, 256, v19\n\t"
      "v_and_b32 v17, 256, v17\n\t"
      "v_mad_u32_u24 v2, v19, 30, v18\n\t"
      "v_mad_u32_u24 v5, v17, 33, v18\n\t"
      "v_add_u32 v5, 19968, v5\n\t"
      "v_bfe_u32 v17, s35, v16, 8\n\t"
      "v_or_b32 v17, s30, v17\n\t"
      "v_xor_b32 v18, v17, v1\n\t"
      "v_add_u32 v19, 80, v17\n\t"
      "v_add_u32 v17, 160, v17\n\t"
      "v_and_b32 v19, 256, v19\n\t"
      "v_and_b32 v17, 256, v17\n\t"
      "v_mad_u32_u24 v3, v19, 30, v18\n\t"
      "v_mad_u32_u24 v6, v17, 33, v18\n\t"
      "v_add_u32 v6, 19968, v6\n\t"
      "v_bfe_u32 v17, s36, v16, 8\n\t"
      "v_or_b32 v17, s30, v17\n\t"
      "v_xor_b32 v18, v17, v1\n\t"
      "v_add_u32 v19, 80, v17\n\t"
      "v_add_u32 v17, 160, v17\n\t"
      "v_and_b32 v19, 256, v19\n\t"
      "v_and_b32 v17, 256, v17\n\t"
      "v_mad_u32_u24 v4, v19, 30, v18\n\t"
      "v_mad_u32_u24 v7, v17, 33, v18\n\t"
      "v_add_u32 v7, 19968, v7\n\t"
      "s_sub_u32 s31, s30, 64\n\t"
      "v_add_u32 v17, s31, v233\n\t"
      "v_min_u32 v17, 193, v17\n\t"
      "v_lshlrev_b32 v18, 2, v17\n\t"
      "v_lshlrev_b32 v17, 1, v17\n\t"
      "v_and_b32 v18, -16, v18\n\t"
      "v_and_or_b32 v8, v17, 6, v18\n\t"
      "s_mov_b32 s86, 294912\n\t"
      "s_mov_b32 s87, 0x00020000\n\t"
      "s_mov_b32 s42, 113246208\n\t"
      "s_mov_b32 s43, 0x00020000\n\t"
      "s_cmp_lg_u32 s17, 0\n\t"
      "s_cbranch_scc1 .Lno0_%=\n\t"
      "s_movk_i32 s31, -61\n\t"
      "v_lshrrev_b32 v16, 2, v233\n\t"
      "v_add_u32 v17, s20, v16\n\t"
      "v_lshl_add_u32 v18, v16, 3, s31\n\t"
      "v_cmp_gt_u32 vcc, 32, v233\n\t"
      "v_and_b32 v19, 3, v233\n\t"
      "v_lshlrev_b32 v19, 2, v19\n\t"
      "v_cndmask_b32 v17, v18, v17, vcc\n\t"
      "v_lshl_add_u32 v62, v17, 8, v19\n\t"
      "s_waitcnt lgkmcnt(2)\n\t"
      "s_add_u32 s32, s32, %[blk]\n\t"
      "s_mul_i32 s32, s32, 0x9e3779b1\n\t"
      "s_lshr_b32 s32, s32, 9\n\t"
      "s_and_b32 s32, s32, 0x1fdf80\n\t"
      "s_or_b32 s22, s22, s32\n\t"
      "v_mov_b32 v234, s22\n\t"
      "v_add_u32 v235, 8192, v234\n\t"
      "s_movk_i32 s64, 120\n\t"
      "s_mov_b64 vcc, 0\n\t"
      ".Lsweep_%=:\n\t"
      "s_getreg_b32 s30, hwreg(HW_REG_IB_STS, 0, 12)\n\t"
      "s_and_b32 s31, s30, 15\n\t"
      "s_lshr_b32 s30, s30, 8\n\t"
      "s_cmp_eq_u32 s31, 8\n\t"
      "s_addc_u32 s62, s62, 0\n\t"
      "s_cmp_eq_u32 s30, 1\n\t"
      "s_addc_u32 s63, s63, 0\n\t"
      "s_cmp_gt_u32 s31, 7\n\t"
      "s_addc_u32 s30, s30, 0\n\t"
      "s_sub_u32 s64, s64, 1\n\t"
      "s_min_u32 s30, s30, s64\n\t"
      "s_cmp_lg_u32 s30, 0\n\t"
      "s_cbranch_scc1 .Lsweep_%=\n\t"
      "s_cbranch_vccnz .Lswept_%=\n\t"
      "s_waitcnt lgkmcnt(0)\n\t"
      "global_load_dword v236, v234, s[4:5]\n\t"
      "global_load_dword v237, v235, s[4:5]\n\t"
      "s_memrealtime s[28:29]\n\t"
      "s_load_dword s60, s[10:11], s22\n\t"
      "s_load_dword s61, s[10:11], s22 offset:8192\n\t"
      "global_load_dword v63, v62, s[12:13] sc1\n\t"
      "s_mov_b64 s[40:41], s[4:5]\n\t"
      "s_add_u32 m0, s18, 25088\n\t"
      "s_add_u32 s48, s15, 0\n\t"
      "buffer_load_dwordx4 v2, s[40:43], s48 offen lds\n\t"
      "buffer_load_dwordx4 v3, s[40:43], s48 offen offset:1024 lds\n\t"
      "buffer_load_dwordx4 v4, s[40:43], s48 offen offset:2048 lds\n\t"
      "s_add_u32 m0, s18, 37376\n\t"
      "s_add_u32 s48, s15, 0\n\t"
      "buffer_load_dwordx4 v5, s[40:43], s48 offen lds\n\t"
      "buffer_load_dwordx4 v6, s[40:43], s48 offen offset:1024 lds\n\t"
      "buffer_load_dwordx4 v7, s[40:43], s48 offen offset:2048 lds\n\t"
      "s_barrier\n\t"
      "s_mov_b64 s[62:63], 0\n\t"
      "s_movk_i32 s64, 120\n\t"
      "s_mov_b64 vcc, -1\n\t"
      "s_branch .Lsweep_%=\n\t"
      ".Lswept_%=:\n\t"
      "s_min_u32 s62, s62, 7\n\t"
      "s_min_u32 s63, s63, 7\n\t"
      "s_lshl_b32 s63, s63, 8\n\t"
      "s_or_b32 s30, s62, s63\n\t"
      "s_sub_u32 s31, 0x808, s30\n\t"
      "s_add_u32 s30, s30, 0x10808\n\t"
      "s_getreg_b32 s62, hwreg(HW_REG_XCC_ID, 2, 1)\n\t"
      "s_cmp_lg_u32 s62, 0\n\t"
      "s_cselect_b32 s91, s30, s31\n\t"
      "s_mov_b64 s[84:85], s[8:9]\n\t"
      "v_mul_u32_u24 v16, 49152, v1\n\t"
      "v_add_u32 v16, s21, v16\n\t"
      "v_lshrrev_b32 v16, 21, v16\n\t"
      "v_lshlrev_b32 v16, 2, v16\n\t"
      "v_accvgpr_write_b32 a248, v16\n\t"
      "s_branch .Lgo0_%=\n\t"
      ".Lno0_%=:\n\t"
      "s_movk_i32 s82, 768\n\t"
      "s_mov_b32 s83, 0x00020000\n\t"
      "v_and_b32 v234, 0x3f0, v1\n\t"
      "s_waitcnt lgkmcnt(0)\n\t"
      "s_mov_b64 s[84:85], s[8:9]\n\t"
      "s_mov_b64 s[80:81], s[6:7]\n\t"
      "s_mov_b64 s[40:41], s[4:5]\n\t"
      "s_barrier\n\t"
      "buffer_load_dwordx4 a[200:203], v234, s[84:87], s23 offen offset:0\n\t"
      "buffer_load_dwordx4 a[204:207], v234, s[84:87], s23 offen offset:1024\n\t"
      "buffer_load_dwordx4 a[208:211], v234, s[84:87], s23 offen offset:2048\n\t"
      "buffer_load_ushort v224, v8, s[80:83], 0 offen\n\t"
      "buffer_load_ushort v225, v8, s[80:83], 0 offen offset:8\n\t"
      "s_add_u32 m0, s18, 25088\n\t"
      "s_add_u32 s48, s15, 0\n\t"
      "buffer_load_dwordx4 v2, s[40:43], s48 offen lds\n\t"
      "buffer_load_dwordx4 v3, s[40:43], s48 offen offset:1024 lds\n\t"
      "buffer_load_dwordx4 v4, s[40:43], s48 offen offset:2048 lds\n\t"
      "s_add_u32 m0, s18, 37376\n\t"
      "s_add_u32 s48, s15, 0\n\t"
      "buffer_load_dwordx4 v5, s[40:43], s48 offen lds\n\t"
      "buffer_load_dwordx4 v6, s[40:43], s48 offen offset:1024 lds\n\t"
      "buffer_load_dwordx4 v7, s[40:43], s48 offen offset:2048 lds\n\t"
      ".Lgo0_%=:\n\t"
      "s_getpc_b64 s[36:37]\n\t"
      ".Lpc_%=:\n\t"
      "s_lshr_b32 s30, s17, 5\n\t"
      "v_lshlrev_b32 v16, 4, v233\n\t"
      "v_add_u32 v17, 0, v233\n\t"
      "v_mul_u32_u24 v17, 171, v17\n\t"
      "v_lshrrev_b32 v17, 11, v17\n\t"
      "v_lshlrev_b32 v18, 2, v17\n\t"
      "v_and_b32 v18, 32, v18\n\t"
      "v_xor_b32 v18, v18, v16\n\t"
      "v_mul_u32_u24 v17, 576, v17\n\t"
      "v_add3_u32 v9, v17, s30, v18\n\t"
      "v_add_u32 v17, 64, v233\n\t"
      "v_mul_u32_u24 v17, 171, v17\n\t"
      "v_lshrrev_b32 v17, 11, v17\n\t"
      "v_lshlrev_b32 v18, 2, v17\n\t"
      "v_and_b32 v18, 32, v18\n\t"
      "v_xor_b32 v18, v18, v16\n\t"
      "v_mul_u32_u24 v17, 576, v17\n\t"
      "v_add3_u32 v10, v17, s30, v18\n\t"
      "v_add_u32 v17, 128, v233\n\t"
      "v_mul_u32_u24 v17, 171, v17\n\t"
      "v_lshrrev_b32 v17, 11, v17\n\t"
      "v_lshlrev_b32 v18, 2, v17\n\t"
      "v_and_b32 v18, 32, v18\n\t"
      "v_xor_b32 v18, v18, v16\n\t"
      "v_mul_u32_u24 v17, 576, v17\n\t"
      "v_add3_u32 v11, v17, s30, v18\n\t"
      "s_mov_b64 s[44:45], s[10:11]\n\t"
      "s_mov_b32 s46, 0\n\t"
      "s_mov_b32 s47, 0x00020000\n\t"
      "s_lshl_b32 s30, s24, 6\n\t"
      "s_add_u32 s31, s30, 0\n\t"
      "s_min_u32 s31, s31, .Lend_%=-.Lpc_%=-4\n\t"
      "s_load_dword s35, s[36:37], s31\n\t"
      "s_add_u32 s31, s30, 8192\n\t"
      "s_min_u32 s31, s31, .Lend_%=-.Lpc_%=-4\n\t"
      "s_load_dword s35, s[36:37], s31\n\t"
      "s_add_u32 s31, s30, 16384\n\t"
      "s_min_u32 s31, s31, .Lend_%=-.Lpc_%=-4\n\t"
      "s_load_dword s35, s[36:37], s31\n\t"
      "s_add_u32 s31, s30, 24576\n\t"
      "s_min_u32 s31, s31, .Lend_%=-.Lpc_%=-4\n\t"
      "s_load_dword s35, s[36:37], s31\n\t"
      "s_and_b32 s31, s24, 63\n\t"
      "s_lshl_b32 s31, s31, 21\n\t"
      "s_min_u32 s31, s31, 113246144\n\t"
      "s_bitcmp1_b32 s24, 6\n\t"
      "s_cselect_b64 s[32:33], s[44:45], s[40:41]\n\t"
      "s_load_dword s35, s[32:33], s31\n\t"
      "s_lshr_b32 s30, s17, 5\n\t"
      "v_lshrrev_b32 v16, 4, v233\n\t"
      "v_and_b32 v17, 15, v233\n\t"
      "v_and_b32 v18, 3, v233\n\t"
      "v_bfe_u32 v19, v233, 2, 2\n\t"
      "v_lshl_add_u32 v19, v16, 3, v19\n\t"
      "v_mul_u32_u24 v19, 192, v19\n\t"
      "v_lshl_add_u32 v19, v18, 3, v19\n\t"
      "v_lshlrev_b32 v20, 1, v233\n\t"
      "v_and_b32 v20, 32, v20\n\t"
      "v_add3_u32 v12, v19, v20, s17\n\t"
      "v_xor_b32 v13, 32, v12\n\t"
      "v_xor_b32 v19, v16, v17\n\t"
      "v_lshlrev_b32 v19, 4, v19\n\t"
      "v_mul_u32_u24 v20, 768, v17\n\t"
      "v_add_u32 v15, v19, v20\n\t"
      "v_mul_u32_u24 v20, 784, v17\n\t"
      "v_lshl_add_u32 v20, v16, 3, v20\n\t"
      "v_add_u32 v0, s30, v20\n\t"
      "v_lshlrev_b32 v19, 6, v16\n\t"
      "v_lshl_add_u32 v19, v18, 4, v19\n\t"
      "v_add_u32 v14, 160256, v19\n\t"
      "v_and_b32 v226, 0xfffffff0, v8\n\t"
      "v_and_b32 v227, 6, v8\n\t"
      "v_lshlrev_b32 v226, 2, v226\n\t"
      "v_lshl_add_u32 v226, v227, 3, v226\n\t"
      "v_add_u32 v226, 160256, v226\n\t"
      "v_and_b32 v232, 2, v8\n\t"
      "v_lshlrev_b32 v232, 3, v232\n\t"
      "v_cmp_gt_u32 vcc, 4, v227\n\t"
      "v_mov_b32 v56, v15\n\t"
      "v_xor_b32 v57, 64, v15\n\t"
      "v_xor_b32 v58, 128, v15\n\t"
      "v_xor_b32 v59, 192, v15\n\t"
      "v_add_u32 v60, 86528, v12\n\t"
      "v_add_u32 v61, 86528, v13\n\t"
      "v_xor_b32 v16, v2, v1\n\t"
      "v_and_b32 v16, 0xf0, v16\n\t"
      "v_xor_b32 v17, v16, v2\n\t"
      "v_xor_b32 v18, v16, v5\n\t"
      "v_add_u32 v19, v16, v1\n\t"
      "v_accvgpr_write_b32 a213, v17\n\t"
      "v_accvgpr_write_b32 a216, v18\n\t"
      "v_accvgpr_write_b32 a219, v19\n\t"
      "v_xor_b32 v16, v3, v1\n\t"
      "v_and_b32 v16, 0xf0, v16\n\t"
      "v_xor_b32 v17, v16, v3\n\t"
      "v_xor_b32 v18, v16, v6\n\t"
      "v_add_u32 v19, v16, v1\n\t"
      "v_accvgpr_write_b32 a214, v17\n\t"
      "v_accvgpr_write_b32 a217, v18\n\t"
      "v_accvgpr_write_b32 a220, v19\n\t"
      "v_xor_b32 v16, v4, v1\n\t"
      "v_and_b32 v16, 0xf0, v16\n\t"
      "v_xor_b32 v17, v16, v4\n\t"
      "v_xor_b32 v18, v16, v7\n\t"
      "v_add_u32 v19, v16, v1\n\t"
      "v_accvgpr_write_b32 a215, v17\n\t"
      "v_accvgpr_write_b32 a218, v18\n\t"
      "v_accvgpr_write_b32 a221, v19\n\t"
      "s_waitcnt vmcnt(6)\n\t"
      "s_bfe_u32 s30, s19, 0x30008\n\t"
      "s_bfe_u32 s31, s19, 0x20002\n\t"
      "s_lshl2_add_u32 s30, s30, s31\n\t"
      "v_readlane_b32 s90, v63, s30\n\t"
      "v_lshlrev_b32 v224, v232, v224\n\t"
      "v_lshlrev_b32 v225, v232, v225\n\t"
      "v_cndmask_b32 v228, 0, v224, vcc\n\t"
      "v_cndmask_b32 v230, 0, v225, vcc\n\t"
      "v_sub_u32 v229, v224, v228\n\t"
      "v_sub_u32 v231, v225, v230\n\t"
      "ds_write_b128 v226, v[228:231]\n\t"
      "s_add_u32 s90, s90, 0x2000000\n\t"
      "s_andn2_b32 s90, s90, 0x1ffffff\n\t"
      "s_or_b32 s90, s90, s91\n\t"
      "s_or_b32 s30, s17, s16\n\t"
      "s_cbranch_scc1 .Lno2_%=\n\t"
      "v_mov_b32 v234, s90\n\t"
      "v_mov_b32 v235, s19\n\t"
      "global_store_dword v235, v234, s[12:13] sc1\n\t"
      ".Lno2_%=:\n\t"
      "s_add_u32 m0, s17, 0\n\t"
      "s_mov_b32 s54, 0\n\t"
      "buffer_load_dwordx4 v9, s[84:87], s54 offen lds\n\t"
      "buffer_load_dwordx4 v10, s[84:87], s54 offen offset:1024 lds\n\t"
      "buffer_load_dwordx4 v11, s[84:87], s54 offen offset:2048 lds\n\t"
      "s_add_u32 m0, s17, 3072\n\t"
      "s_mov_b32 s54, 12288\n\t"
      "buffer_load_dwordx4 v9, s[84:87], s54 offen lds\n\t"
      "buffer_load_dwordx4 v10, s[84:87], s54 offen offset:1024 lds\n\t"
      "buffer_load_dwordx4 v11, s[84:87], s54 offen offset:2048 lds\n\t"
      "s_add_u32 m0, s17, 86528\n\t"
      "s_mov_b32 s54, 24576\n\t"
      "buffer_load_dwordx4 v9, s[84:87], s54 offen lds\n\t"
      "buffer_load_dwordx4 v10, s[84:87], s54 offen offset:1024 lds\n\t"
      "buffer_load_dwordx4 v11, s[84:87], s54 offen offset:2048 lds\n\t"
      "s_add_u32 m0, s17, 89600\n\t"
      "s_mov_b32 s54, 36864\n\t"
      "buffer_load_dwordx4 v9, s[84:87], s54 offen lds\n\t"
      "buffer_load_dwordx4 v10, s[84:87], s54 offen offset:1024 lds\n\t"
      "buffer_load_dwordx4 v11, s[84:87], s54 offen offset:2048 lds\n\t"
      "s_add_u32 m0, s17, 111104\n\t"
      "s_mov_b32 s54, 49152\n\t"
      "buffer_load_dwordx4 v9, s[84:87], s54 offen lds\n\t"
      "buffer_load_dwordx4 v10, s[84:87], s54 offen offset:1024 lds\n\t"
      "buffer_load_dwordx4 v11, s[84:87], s54 offen offset:2048 lds\n\t"
      "s_add_u32 m0, s17, 114176\n\t"
      "s_mov_b32 s54, 61440\n\t"
      "buffer_load_dwordx4 v9, s[84:87], s54 offen lds\n\t"
      "buffer_load_dwordx4 v10, s[84:87], s54 offen offset:1024 lds\n\t"
      "buffer_load_dwordx4 v11, s[84:87], s54 offen offset:2048 lds\n\t"
      "s_waitcnt lgkmcnt(0)\n\t"
      "s_waitcnt vmcnt(18)\n\t"
      "s_barrier\n\t"
      "s_waitcnt vmcnt(12)\n\t"
      "ds_read_b128 a[180:183], v14 offset:0\n\t"
      "ds_read_b64_tr_b16 v[64:65], v12 offset:0\n\t"
      "ds_read_b64_tr_b16 v[66:67], v12 offset:768\n\t"
      "ds_read_b64_tr_b16 v[68:69], v13 offset:0\n\t"
      "ds_read_b64_tr_b16 v[70:71], v13 offset:768\n\t"
      "ds_read_b64_tr_b16 v[72:73], v12 offset:64\n\t"
      "ds_read_b64_tr_b16 v[74:75], v12 offset:832\n\t"
      "ds_read_b64_tr_b16 v[76:77], v13 offset:64\n\t"
      "ds_read_b64_tr_b16 v[78:79], v13 offset:832\n\t"
      "ds_read_b64_tr_b16 a[60:61], v12 offset:128\n\t"
      "ds_read_b64_tr_b16 a[62:63], v12 offset:896\n\t"
      "ds_read_b64_tr_b16 a[64:65], v13 offset:128\n\t"
      "ds_read_b64_tr_b16 a[66:67], v13 offset:896\n\t"
      "s_add_u32 m0, s17, 135680\n\t"
      "s_mov_b32 s54, 73728\n\t"
      "buffer_load_dwordx4 v9, s[84:87], s54 offen lds\n\t"
      "buffer_load_dwordx4 v10, s[84:87], s54 offen offset:1024 lds\n\t"
      "buffer_load_dwordx4 v11, s[84:87], s54 offen offset:2048 lds\n\t"
      "s_add_u32 m0, s17, 138752\n\t"
      "s_mov_b32 s54, 86016\n\t"
      "buffer_load_dwordx4 v9, s[84:87], s54 offen lds\n\t"
      "buffer_load_dwordx4 v10, s[84:87], s54 offen offset:1024 lds\n\t"
      "buffer_load_dwordx4 v11, s[84:87], s54 offen offset:2048 lds\n\t"
      "s_waitcnt vmcnt(12)\n\t"
      "s_waitcnt lgkmcnt(0)\n\t"
      "; step\n\t"
      "v_mfma_f32_4x4x4_16b_bf16 v[40:43], a[180:181], v[64:65], 0\n\t"
      "ds_read_b128 a[184:187], v14 offset:256\n\t"
      "v_mfma_f32_4x4x4_16b_bf16 v[44:47], a[182:183], v[66:67], 0\n\t"
      "ds_read_b64_tr_b16 v[80:81], v60 offset:0\n\t"
      "v_mfma_f32_4x4x4_16b_bf16 v[48:51], a[180:181], v[68:69], 0\n\t"
      "ds_read_b64_tr_b16 v[82:83], v60 offset:768\n\t"
      "v_mfma_f32_4x4x4_16b_bf16 v[52:55], a[182:183], v[70:71], 0\n\t"
      "ds_read_b64_tr_b16 v[84:85], v61 offset:0\n\t"
      "ds_read_b64_tr_b16 v[86:87], v61 offset:768\n\t"
      "s_add_u32 m0, s17, 0\n\t"
      "s_mov_b32 s54, 98304\n\t"
      "buffer_load_dwordx4 v9, s[84:87], s54 offen lds\n\t"
      "buffer_load_dwordx4 v10, s[84:87], s54 offen offset:1024 lds\n\t"
      "v_cvt_pk_bf16_f32 v64, v40, v41\n\t"
      "v_cvt_pk_bf16_f32 v65, v42, v43\n\t"
      "v_cvt_pk_bf16_f32 v66, v44, v45\n\t"
      "v_cvt_pk_bf16_f32 v67, v46, v47\n\t"
      "v_cvt_pk_bf16_f32 v68, v48, v49\n\t"
      "v_cvt_pk_bf16_f32 v69, v50, v51\n\t"
      "v_cvt_pk_bf16_f32 v70, v52, v53\n\t"
      "v_cvt_pk_bf16_f32 v71, v54, v55\n\t"
      "v_mfma_f32_4x4x4_16b_bf16 v[40:43], a[180:181], v[72:73], 0\n\t"
      "ds_read_b64_tr_b16 v[88:89], v60 offset:64\n\t"
      "v_mfma_f32_4x4x4_16b_bf16 v[44:47], a[182:183], v[74:75], 0\n\t"
      "ds_read_b64_tr_b16 v[90:91], v60 offset:832\n\t"
      "v_mfma_f32_4x4x4_16b_bf16 v[48:51], a[180:181], v[76:77], 0\n\t"
      "ds_read_b64_tr_b16 v[92:93], v61 offset:64\n\t"
      "v_mfma_f32_4x4x4_16b_bf16 v[52:55], a[182:183], v[78:79], 0\n\t"
      "ds_read_b64_tr_b16 v[94:95], v61 offset:832\n\t"
      "buffer_load_dwordx4 v11, s[84:87], s54 offen offset:2048 lds\n\t"
      "s_add_u32 m0, s17, 3072\n\t"
      "s_mov_b32 s54, 110592\n\t"
      "buffer_load_dwordx4 v9, s[84:87], s54 offen lds\n\t"
      "v_cvt_pk_bf16_f32 v72, v40, v41\n\t"
      "v_cvt_pk_bf16_f32 v73, v42, v43\n\t"
      "v_cvt_pk_bf16_f32 v74, v44, v45\n\t"
      "v_cvt_pk_bf16_f32 v75, v46, v47\n\t"
      "v_cvt_pk_bf16_f32 v76, v48, v49\n\t"
      "v_cvt_pk_bf16_f32 v77, v50, v51\n\t"
      "v_cvt_pk_bf16_f32 v78, v52, v53\n\t"
      "v_cvt_pk_bf16_f32 v79, v54, v55\n\t"
      "ds_read_b128 a[224:227], v56 offset:25088\n\t"
      "v_mfma_f32_4x4x4_16b_bf16 v[40:43], a[180:181], a[60:61], 0\n\t"
      "ds_read_b64_tr_b16 a[68:69], v60 offset:128\n\t"
      "v_mfma_f32_4x4x4_16b_bf16 v[44:47], a[182:183], a[62:63], 0\n\t"
      "ds_read_b64_tr_b16 a[70:71], v60 offset:896\n\t"
      "v_mfma_f32_4x4x4_16b_bf16 v[48:51], a[180:181], a[64:65], 0\n\t"
      "ds_read_b64_tr_b16 a[72:73], v61 offset:128\n\t"
      "v_mfma_f32_4x4x4_16b_bf16 v[52:55], a[182:183], a[66:67], 0\n\t"
      "ds_read_b64_tr_b16 a[74:75], v61 offset:896\n\t"
      "buffer_load_dwordx4 v10, s[84:87], s54 offen offset:1024 lds\n\t"
      "buffer_load_dwordx4 v11, s[84:87], s54 offen offset:2048 lds\n\t"
      "ds_read_b128 a[228:231], v56 offset:37376\n\t"
      "v_cvt_pk_bf16_f32 v40, v40, v41\n\t"
      "v_cvt_pk_bf16_f32 v41, v42, v43\n\t"
      "v_cvt_pk_bf16_f32 v44, v44, v45\n\t"
      "v_cvt_pk_bf16_f32 v45, v46, v47\n\t"
      "v_cvt_pk_bf16_f32 v48, v48, v49\n\t"
      "v_cvt_pk_bf16_f32 v49, v50, v51\n\t"
      "v_cvt_pk_bf16_f32 v52, v52, v53\n\t"
      "v_cvt_pk_bf16_f32 v53, v54, v55\n\t"
      "v_accvgpr_write_b32 a60, v40\n\t"
      "v_accvgpr_write_b32 a61, v41\n\t"
      "v_accvgpr_write_b32 a62, v44\n\t"
      "v_accvgpr_write_b32 a63, v45\n\t"
      "v_accvgpr_write_b32 a64, v48\n\t"
      "v_accvgpr_write_b32 a65, v49\n\t"
      "v_accvgpr_write_b32 a66, v52\n\t"
      "v_accvgpr_write_b32 a67, v53\n\t"
      "s_waitcnt vmcnt(12)\n\t"
      "s_waitcnt lgkmcnt(1)\n\t"
      "; step\n\t"
      "s_memtime s[64:65]\n\t"
      "s_memrealtime s[66:67]\n\t"
      "v_mfma_f32_4x4x4_16b_bf16 v[40:43], a[184:185], v[80:81], 0\n\t"
      "ds_read_b128 a[180:183], v14 offset:512\n\t"
      "v_mfma_f32_4x4x4_16b_bf16 v[44:47], a[186:187], v[82:83], 0\n\t"
      "ds_read_b64_tr_b16 v[96:97], v60 offset:24576\n\t"
      "v_mfma_f32_4x4x4_16b_bf16 v[48:51], a[184:185], v[84:85], 0\n\t"
      "ds_read_b64_tr_b16 v[98:99], v60 offset:25344\n\t"
      "v_mfma_f32_4x4x4_16b_bf16 v[52:55], a[186:187], v[86:87], 0\n\t"
      "ds_read_b64_tr_b16 v[100:101], v61 offset:24576\n\t"
      "ds_read_b64_tr_b16 v[102:103], v61 offset:25344\n\t"
      "s_add_u32 m0, s17, 86528\n\t"
      "s_mov_b32 s54, 122880\n\t"
      "buffer_load_dwordx4 v9, s[84:87], s54 offen lds\n\t"
      "buffer_load_dwordx4 v10, s[84:87], s54 offen offset:1024 lds\n\t"
      "v_mfma_f32_16x16x32_bf16 v[16:19], v[64:67], a[224:227], 0\n\t"
      "v_cvt_pk_bf16_f32 v80, v40, v41\n\t"
      "v_cvt_pk_bf16_f32 v81, v42, v43\n\t"
      "v_mfma_f32_16x16x32_bf16 v[20:23], v[68:71], a[224:227], 0\n\t"
      "v_cvt_pk_bf16_f32 v82, v44, v45\n\t"
      "v_cvt_pk_bf16_f32 v83, v46, v47\n\t"
      "v_mfma_f32_16x16x32_bf16 v[24:27], v[72:75], a[224:227], 0\n\t"
      "v_cvt_pk_bf16_f32 v84, v48, v49\n\t"
      "v_cvt_pk_bf16_f32 v85, v50, v51\n\t"
      "v_mfma_f32_16x16x32_bf16 v[28:31], v[76:79], a[224:227], 0\n\t"
      "v_cvt_pk_bf16_f32 v86, v52, v53\n\t"
      "v_cvt_pk_bf16_f32 v87, v54, v55\n\t"
      "v_mfma_f32_4x4x4_16b_bf16 v[40:43], a[184:185], v[88:89], 0\n\t"
      "ds_read_b64_tr_b16 v[104:105], v60 offset:24640\n\t"
      "v_mfma_f32_4x4x4_16b_bf16 v[44:47], a[186:187], v[90:91], 0\n\t"
      "ds_read_b64_tr_b16 v[106:107], v60 offset:25408\n\t"
      "v_mfma_f32_4x4x4_16b_bf16 v[48:51], a[184:185], v[92:93], 0\n\t"
      "ds_read_b64_tr_b16 v[108:109], v61 offset:24640\n\t"
      "v_mfma_f32_4x4x4_16b_bf16 v[52:55], a[186:187], v[94:95], 0\n\t"
      "ds_read_b64_tr_b16 v[110:111], v61 offset:25408\n\t"
      "buffer_load_dwordx4 v11, s[84:87], s54 offen offset:2048 lds\n\t"
      "s_add_u32 m0, s17, 89600\n\t"
      "s_mov_b32 s54, 135168\n\t"
      "buffer_load_dwordx4 v9, s[84:87], s54 offen lds\n\t"
      "v_mfma_f32_16x16x32_bf16 v[32:35], a[60:63], a[224:227], 0\n\t"
      "v_cvt_pk_bf16_f32 v88, v40, v41\n\t"
      "v_cvt_pk_bf16_f32 v89, v42, v43\n\t"
      "v_mfma_f32_16x16x32_bf16 v[36:39], a[64:67], a[224:227], 0\n\t"
      "v_cvt_pk_bf16_f32 v90, v44, v45\n\t"
      "v_cvt_pk_bf16_f32 v91, v46, v47\n\t"
      "s_waitcnt lgkmcnt(9)\n\t"
      "v_mfma_f32_16x16x32_bf16 a[156:159], v[64:67], a[228:231], 0\n\t"
      "v_cvt_pk_bf16_f32 v92, v48, v49\n\t"
      "v_cvt_pk_bf16_f32 v93, v50, v51\n\t"
      "v_mfma_f32_16x16x32_bf16 a[160:163], v[68:71], a[228:231], 0\n\t"
      "v_cvt_pk_bf16_f32 v94, v52, v53\n\t"
      "v_cvt_pk_bf16_f32 v95, v54, v55\n\t"
      "ds_read_b128 a[232:235], v57 offset:25088\n\t"
      "v_mfma_f32_4x4x4_16b_bf16 v[40:43], a[184:185], a[68:69], 0\n\t"
      "ds_read_b64_tr_b16 a[76:77], v60 offset:24704\n\t"
      "v_mfma_f32_4x4x4_16b_bf16 v[44:47], a[186:187], a[70:71], 0\n\t"
      "ds_read_b64_tr_b16 a[78:79], v60 offset:25472\n\t"
      "v_mfma_f32_4x4x4_16b_bf16 v[48:51], a[184:185], a[72:73], 0\n\t"
      "ds_read_b64_tr_b16 a[80:81], v61 offset:24704\n\t"
      "v_mfma_f32_4x4x4_16b_bf16 v[52:55], a[186:187], a[74:75], 0\n\t"
      "ds_read_b64_tr_b16 a[82:83], v61 offset:25472\n\t"
      "buffer_load_dwordx4 v10, s[84:87], s54 offen offset:1024 lds\n\t"
      "buffer_load_dwordx4 v11, s[84:87], s54 offen offset:2048 lds\n\t"
      "ds_read_b128 a[236:239], v57 offset:37376\n\t"
      "v_mfma_f32_16x16x32_bf16 a[164:167], v[72:75], a[228:231], 0\n\t"
      "v_cvt_pk_bf16_f32 v40, v40, v41\n\t"
      "v_cvt_pk_bf16_f32 v41, v42, v43\n\t"
      "v_mfma_f32_16x16x32_bf16 a[168:171], v[76:79], a[228:231], 0\n\t"
      "v_cvt_pk_bf16_f32 v44, v44, v45\n\t"
      "v_cvt_pk_bf16_f32 v45, v46, v47\n\t"
      "v_mfma_f32_16x16x32_bf16 a[172:175], a[60:63], a[228:231], 0\n\t"
      "v_cvt_pk_bf16_f32 v48, v48, v49\n\t"
      "v_cvt_pk_bf16_f32 v49, v50, v51\n\t"
      "v_mfma_f32_16x16x32_bf16 a[176:179], a[64:67], a[228:231], 0\n\t"
      "v_cvt_pk_bf16_f32 v52, v52, v53\n\t"
      "v_cvt_pk_bf16_f32 v53, v54, v55\n\t"
      "v_accvgpr_write_b32 a68, v40\n\t"
      "v_accvgpr_write_b32 a69, v41\n\t"
      "v_accvgpr_write_b32 a70, v44\n\t"
      "v_accvgpr_write_b32 a71, v45\n\t"
      "v_accvgpr_write_b32 a72, v48\n\t"
      "v_accvgpr_write_b32 a73, v49\n\t"
      "v_accvgpr_write_b32 a74, v52\n\t"
      "v_accvgpr_write_b32 a75, v53\n\t"
      "s_waitcnt vmcnt(12)\n\t"
      "s_waitcnt lgkmcnt(1)\n\t"
      "; step\n\t"
      "v_mfma_f32_4x4x4_16b_bf16 v[40:43], a[180:181], v[96:97], 0\n\t"
      "ds_read_b128 a[184:187], v14 offset:768\n\t"
      "v_mfma_f32_4x4x4_16b_bf16 v[44:47], a[182:183], v[98:99], 0\n\t"
      "ds_read_b64_tr_b16 v[112:113], v60 offset:49152\n\t"
      "v_mfma_f32_4x4x4_16b_bf16 v[48:51], a[180:181], v[100:101], 0\n\t"
      "ds_read_b64_tr_b16 v[114:115], v60 offset:49920\n\t"
      "v_mfma_f32_4x4x4_16b_bf16 v[52:55], a[182:183], v[102:103], 0\n\t"
      "ds_read_b64_tr_b16 v[116:117], v61 offset:49152\n\t"
      "ds_read_b64_tr_b16 v[118:119], v61 offset:49920\n\t"
      "s_add_u32 m0, s17, 111104\n\t"
      "s_mov_b32 s54, 147456\n\t"
      "buffer_load_dwordx4 v9, s[84:87], s54 offen lds\n\t"
      "buffer_load_dwordx4 v10, s[84:87], s54 offen offset:1024 lds\n\t"
      "v_mfma_f32_16x16x32_bf16 v[16:19], v[80:83], a[232:235], v[16:19]\n\t"
      "v_cvt_pk_bf16_f32 v96, v40, v41\n\t"
      "v_cvt_pk_bf16_f32 v97, v42, v43\n\t"
      "v_mfma_f32_16x16x32_bf16 v[20:23], v[84:87], a[232:235], v[20:23]\n\t"
      "v_cvt_pk_bf16_f32 v98, v44, v45\n\t"
      "v_cvt_pk_bf16_f32 v99, v46, v47\n\t"
      "v_mfma_f32_16x16x32_bf16 v[24:27], v[88:91], a[232:235], v[24:27]\n\t"
      "v_cvt_pk_bf16_f32 v100, v48, v49\n\t"
      "v_cvt_pk_bf16_f32 v101, v50, v51\n\t"
      "v_mfma_f32_16x16x32_bf16 v[28:31], v[92:95], a[232:235], v[28:31]\n\t"
      "v_cvt_pk_bf16_f32 v102, v52, v53\n\t"
      "v_cvt_pk_bf16_f32 v103, v54, v55\n\t"
      "v_mfma_f32_4x4x4_16b_bf16 v[40:43], a[180:181], v[104:105], 0\n\t"
      "ds_read_b64_tr_b16 v[120:121], v60 offset:49216\n\t"
      "v_mfma_f32_4x4x4_16b_bf16 v[44:47], a[182:183], v[106:107], 0\n\t"
      "ds_read_b64_tr_b16 v[122:123], v60 offset:49984\n\t"
      "v_mfma_f32_4x4x4_16b_bf16 v[48:51], a[180:181], v[108:109], 0\n\t"
      "ds_read_b64_tr_b16 v[124:125], v61 offset:49216\n\t"
      "v_mfma_f32_4x4x4_16b_bf16 v[52:55], a[182:183], v[110:111], 0\n\t"
      "ds_read_b64_tr_b16 v[126:127], v61 offset:49984\n\t"
      "buffer_load_dwordx4 v11, s[84:87], s54 offen offset:2048 lds\n\t"
      "s_add_u32 m0, s17, 114176\n\t"
      "s_mov_b32 s54, 159744\n\t"
      "buffer_load_dwordx4 v9, s[84:87], s54 offen lds\n\t"
      "v_mfma_f32_16x16x32_bf16 v[32:35], a[68:71], a[232:235], v[32:35]\n\t"
      "v_cvt_pk_bf16_f32 v104, v40, v41\n\t"
      "v_cvt_pk_bf16_f32 v105, v42, v43\n\t"
      "v_mfma_f32_16x16x32_bf16 v[36:39], a[72:75], a[232:235], v[36:39]\n\t"
      "v_cvt_pk_bf16_f32 v106, v44, v45\n\t"
      "v_cvt_pk_bf16_f32 v107, v46, v47\n\t"
      "s_waitcnt lgkmcnt(9)\n\t"
      "v_mfma_f32_16x16x32_bf16 a[156:159], v[80:83], a[236:239], a[156:159]\n\t"
      "v_cvt_pk_bf16_f32 v108, v48, v49\n\t"
      "v_cvt_pk_bf16_f32 v109, v50, v51\n\t"
      "v_mfma_f32_16x16x32_bf16 a[160:163], v[84:87], a[236:239], a[160:163]\n\t"
      "v_cvt_pk_bf16_f32 v110, v52, v53\n\t"
      "v_cvt_pk_bf16_f32 v111, v54, v55\n\t"
      "ds_read_b128 a[240:243], v58 offset:25088\n\t"
      "v_mfma_f32_4x4x4_16b_bf16 v[40:43], a[180:181], a[76:77], 0\n\t"
      "ds_read_b64_tr_b16 a[84:85], v60 offset:49280\n\t"
      "v_mfma_f32_4x4x4_16b_bf16 v[44:47], a[182:183], a[78:79], 0\n\t"
      "ds_read_b64_tr_b16 a[86:87], v60 offset:50048\n\t"
      "v_mfma_f32_4x4x4_16b_bf16 v[48:51], a[180:181], a[80:81], 0\n\t"
      "ds_read_b64_tr_b16 a[88:89], v61 offset:49280\n\t"
      "v_mfma_f32_4x4x4_16b_bf16 v[52:55], a[182:183], a[82:83], 0\n\t"
      "ds_read_b64_tr_b16 a[90:91], v61 offset:50048\n\t"
      "buffer_load_dwordx4 v10, s[84:87], s54 offen offset:1024 lds\n\t"
      "buffer_load_dwordx4 v11, s[84:87], s54 offen offset:2048 lds\n\t"
      "ds_read_b128 a[244:247], v58 offset:37376\n\t"
      "v_mfma_f32_16x16x32_bf16 a[164:167], v[88:91], a[236:239], a[164:167]\n\t"
      "v_cvt_pk_bf16_f32 v40, v40, v41\n\t"
      "v_cvt_pk_bf16_f32 v41, v42, v43\n\t"
      "v_mfma_f32_16x16x32_bf16 a[168:171], v[92:95], a[236:239], a[168:171]\n\t"
      "v_cvt_pk_bf16_f32 v44, v44, v45\n\t"
      "v_cvt_pk_bf16_f32 v45, v46, v47\n\t"
      "v_mfma_f32_16x16x32_bf16 a[172:175], a[68:71], a[236:239], a[172:175]\n\t"
      "v_cvt_pk_bf16_f32 v48, v48, v49\n\t"
      "v_cvt_pk_bf16_f32 v49, v50, v51\n\t"
      "v_mfma_f32_16x16x32_bf16 a[176:179], a[72:75], a[236:239], a[176:179]\n\t"
      "v_cvt_pk_bf16_f32 v52, v52, v53\n\t"
      "v_cvt_pk_bf16_f32 v53, v54, v55\n\t"
      "v_accvgpr_write_b32 a76, v40\n\t"
      "v_accvgpr_write_b32 a77, v41\n\t"
      "v_accvgpr_write_b32 a78, v44\n\t"
      "v_accvgpr_write_b32 a79, v45\n\t"
      "v_accvgpr_write_b32 a80, v48\n\t"
      "v_accvgpr_write_b32 a81, v49\n\t"
      "v_accvgpr_write_b32 a82, v52\n\t"
      "v_accvgpr_write_b32 a83, v53\n\t"
      "s_waitcnt vmcnt(12)\n\t"
      "s_waitcnt lgkmcnt(1)\n\t"
      "; step\n\t"
      "v_mfma_f32_4x4x4_16b_bf16 v[40:43], a[184:185], v[112:113], 0\n\t"
      "ds_read_b128 a[180:183], v14 offset:1024\n\t"
      "v_mfma_f32_4x4x4_16b_bf16 v[44:47], a[186:187], v[114:115], 0\n\t"
      "ds_read_b64_tr_b16 v[128:129], v12 offset:0\n\t"
      "v_mfma_f32_4x4x4_16b_bf16 v[48:51], a[184:185], v[116:117], 0\n\t"
      "ds_read_b64_tr_b16 v[130:131], v12 offset:768\n\t"
      "v_mfma_f32_4x4x4_16b_bf16 v[52:55], a[186:187], v[118:119], 0\n\t"
      "ds_read_b64_tr_b16 v[132:133], v13 offset:0\n\t"
      "ds_read_b64_tr_b16 v[134:135], v13 offset:768\n\t"
      "s_add_u32 m0, s17, 135680\n\t"
      "s_mov_b32 s54, 172032\n\t"
      "buffer_load_dwordx4 v9, s[84:87], s54 offen lds\n\t"
      "buffer_load_dwordx4 v10, s[84:87], s54 offen offset:1024 lds\n\t"
      "v_mfma_f32_16x16x32_bf16 v[16:19], v[96:99], a[240:243], v[16:19]\n\t"
      "v_cvt_pk_bf16_f32 v112, v40, v41\n\t"
      "v_cvt_pk_bf16_f32 v113, v42, v43\n\t"
      "v_mfma_f32_16x16x32_bf16 v[20:23], v[100:103], a[240:243], v[20:23]\n\t"
      "v_cvt_pk_bf16_f32 v114, v44, v45\n\t"
      "v_cvt_pk_bf16_f32 v115, v46, v47\n\t"
      "v_mfma_f32_16x16x32_bf16 v[24:27], v[104:107], a[240:243], v[24:27]\n\t"
      "v_cvt_pk_bf16_f32 v116, v48, v49\n\t"
      "v_cvt_pk_bf16_f32 v117, v50, v51\n\t"
      "v_mfma_f32_16x16x32_bf16 v[28:31], v[108:111], a[240:243], v[28:31]\n\t"
      "v_cvt_pk_bf16_f32 v118, v52, v53\n\t"
      "v_cvt_pk_bf16_f32 v119, v54, v55\n\t"
      "v_mfma_f32_4x4x4_16b_bf16 v[40:43], a[184:185], v[120:121], 0\n\t"
      "ds_read_b64_tr_b16 v[136:137], v12 offset:64\n\t"
      "v_mfma_f32_4x4x4_16b_bf16 v[44:47], a[186:187], v[122:123], 0\n\t"
      "ds_read_b64_tr_b16 v[138:139], v12 offset:832\n\t"
      "v_mfma_f32_4x4x4_16b_bf16 v[48:51], a[184:185], v[124:125], 0\n\t"
      "ds_read_b64_tr_b16 v[140:141], v13 offset:64\n\t"
      "v_mfma_f32_4x4x4_16b_bf16 v[52:55], a[186:187], v[126:127], 0\n\t"
      "ds_read_b64_tr_b16 v[142:143], v13 offset:832\n\t"
      "buffer_load_dwordx4 v11, s[84:87], s54 offen offset:2048 lds\n\t"
      "s_add_u32 m0, s17, 138752\n\t"
      "s_mov_b32 s54, 184320\n\t"
      "buffer_load_dwordx4 v9, s[84:87], s54 offen lds\n\t"
      "v_mfma_f32_16x16x32_bf16 v[32:35], a[76:79], a[240:243], v[32:35]\n\t"
      "v_cvt_pk_bf16_f32 v120, v40, v41\n\t"
      "v_cvt_pk_bf16_f32 v121, v42, v43\n\t"
      "v_mfma_f32_16x16x32_bf16 v[36:39], a[80:83], a[240:243], v[36:39]\n\t"
      "v_cvt_pk_bf16_f32 v122, v44, v45\n\t"
      "v_cvt_pk_bf16_f32 v123, v46, v47\n\t"
      "s_waitcnt lgkmcnt(9)\n\t"
      "v_mfma_f32_16x16x32_bf16 a[156:159], v[96:99], a[244:247], a[156:159]\n\t"
      "v_cvt_pk_bf16_f32 v124, v48, v49\n\t"
      "v_cvt_pk_bf16_f32 v125, v50, v51\n\t"
      "v_mfma_f32_16x16x32_bf16 a[160:163], v[100:103], a[244:247], a[160:163]\n\t"
      "v_cvt_pk_bf16_f32 v126, v52, v53\n\t"
      "v_cvt_pk_bf16_f32 v127, v54, v55\n\t"
      "ds_read_b128 a[224:227], v59 offset:25088\n\t"
      "v_mfma_f32_4x4x4_16b_bf16 v[40:43], a[184:185], a[84:85], 0\n\t"
      "ds_read_b64_tr_b16 a[92:93], v12 offset:128\n\t"
      "v_mfma_f32_4x4x4_16b_bf16 v[44:47], a[186:187], a[86:87], 0\n\t"
      "ds_read_b64_tr_b16 a[94:95], v12 offset:896\n\t"
      "v_mfma_f32_4x4x4_16b_bf16 v[48:51], a[184:185], a[88:89], 0\n\t"
      "ds_read_b64_tr_b16 a[96:97], v13 offset:128\n\t"
      "v_mfma_f32_4x4x4_16b_bf16 v[52:55], a[186:187], a[90:91], 0\n\t"
      "ds_read_b64_tr_b16 a[98:99], v13 offset:896\n\t"
      "buffer_load_dwordx4 v10, s[84:87], s54 offen offset:1024 lds\n\t"
      "buffer_load_dwordx4 v11, s[84:87], s54 offen offset:2048 lds\n\t"
      "ds_read_b128 a[228:231], v59 offset:37376\n\t"
      "v_mfma_f32_16x16x32_bf16 a[164:167], v[104:107], a[244:247], a[164:167]\n\t"
      "v_cvt_pk_bf16_f32 v40, v40, v41\n\t"
      "v_cvt_pk_bf16_f32 v41, v42, v43\n\t"
      "v_mfma_f32_16x16x32_bf16 a[168:171], v[108:111], a[244:247], a[168:171]\n\t"
      "v_cvt_pk_bf16_f32 v44, v44, v45\n\t"
      "v_cvt_pk_bf16_f32 v45, v46, v47\n\t"
      "v_mfma_f32_16x16x32_bf16 a[172:175], a[76:79], a[244:247], a[172:175]\n\t"
      "v_cvt_pk_bf16_f32 v48, v48, v49\n\t"
      "v_cvt_pk_bf16_f32 v49, v50, v51\n\t"
      "v_mfma_f32_16x16x32_bf16 a[176:179], a[80:83], a[244:247], a[176:179]\n\t"
      "v_cvt_pk_bf16_f32 v52, v52, v53\n\t"
      "v_cvt_pk_bf16_f32 v53, v54, v55\n\t"
      "v_accvgpr_write_b32 a84, v40\n\t"
      "v_accvgpr_write_b32 a85, v41\n\t"
      "v_accvgpr_write_b32 a86, v44\n\t"
      "v_accvgpr_write_b32 a87, v45\n\t"
      "v_accvgpr_write_b32 a88, v48\n\t"
      "v_accvgpr_write_b32 a89, v49\n\t"
      "v_accvgpr_write_b32 a90, v52\n\t"
      "v_accvgpr_write_b32 a91, v53\n\t"
      "s_waitcnt vmcnt(12)\n\t"
      "s_waitcnt lgkmcnt(1)\n\t"
      "; step\n\t"
      "v_mfma_f32_4x4x4_16b_bf16 v[40:43], a[180:181], v[128:129], 0\n\t"
      "ds_read_b128 a[184:187], v14 offset:1280\n\t"
      "v_mfma_f32_4x4x4_16b_bf16 v[44:47], a[182:183], v[130:131], 0\n\t"
      "ds_read_b64_tr_b16 v[144:145], v60 offset:0\n\t"
      "v_mfma_f32_4x4x4_16b_bf16 v[48:51], a[180:181], v[132:133], 0\n\t"
      "ds_read_b64_tr_b16 v[146:147], v60 offset:768\n\t"
      "v_mfma_f32_4x4x4_16b_bf16 v[52:55], a[182:183], v[134:135], 0\n\t"
      "ds_read_b64_tr_b16 v[148:149], v61 offset:0\n\t"
      "ds_read_b64_tr_b16 v[150:151], v61 offset:768\n\t"
      "s_add_u32 m0, s17, 0\n\t"
      "s_mov_b32 s54, 196608\n\t"
      "buffer_load_dwordx4 v9, s[84:87], s54 offen lds\n\t"
      "buffer_load_dwordx4 v10, s[84:87], s54 offen offset:1024 lds\n\t"
      "v_mfma_f32_16x16x32_bf16 v[16:19], v[112:115], a[224:227], v[16:19]\n\t"
      "v_cvt_pk_bf16_f32 v128, v40, v41\n\t"
      "v_cvt_pk_bf16_f32 v129, v42, v43\n\t"
      "v_mfma_f32_16x16x32_bf16 v[20:23], v[116:119], a[224:227], v[20:23]\n\t"
      "v_cvt_pk_bf16_f32 v130, v44, v45\n\t"
      "v_cvt_pk_bf16_f32 v131, v46, v47\n\t"
      "v_mfma_f32_16x16x32_bf16 v[24:27], v[120:123], a[224:227], v[24:27]\n\t"
      "v_cvt_pk_bf16_f32 v132, v48, v49\n\t"
      "v_cvt_pk_bf16_f32 v133, v50, v51\n\t"
      "v_mfma_f32_16x16x32_bf16 v[28:31], v[124:127], a[224:227], v[28:31]\n\t"
      "v_cvt_pk_bf16_f32 v134, v52, v53\n\t"
      "v_cvt_pk_bf16_f32 v135, v54, v55\n\t"
      "v_mfma_f32_4x4x4_16b_bf16 v[40:43], a[180:181], v[136:137], 0\n\t"
      "ds_read_b64_tr_b16 v[152:153], v60 offset:64\n\t"
      "v_mfma_f32_4x4x4_16b_bf16 v[44:47], a[182:183], v[138:139], 0\n\t"
      "ds_read_b64_tr_b16 v[154:155], v60 offset:832\n\t"
      "v_mfma_f32_4x4x4_16b_bf16 v[48:51], a[180:181], v[140:141], 0\n\t"
      "ds_read_b64_tr_b16 v[156:157], v61 offset:64\n\t"
      "v_mfma_f32_4x4x4_16b_bf16 v[52:55], a[182:183], v[142:143], 0\n\t"
      "ds_read_b64_tr_b16 v[158:159], v61 offset:832\n\t"
      "buffer_load_dwordx4 v11, s[84:87], s54 offen offset:2048 lds\n\t"
      "s_add_u32 m0, s17, 3072\n\t"
      "s_mov_b32 s54, 208896\n\t"
      "buffer_load_dwordx4 v9, s[84:87], s54 offen lds\n\t"
      "v_mfma_f32_16x16x32_bf16 v[32:35], a[84:87], a[224:227], v[32:35]\n\t"
      "v_cvt_pk_bf16_f32 v136, v40, v41\n\t"
      "v_cvt_pk_bf16_f32 v137, v42, v43\n\t"
      "v_mfma_f32_16x16x32_bf16 v[36:39], a[88:91], a[224:227], v[36:39]\n\t"
      "v_cvt_pk_bf16_f32 v138, v44, v45\n\t"
      "v_cvt_pk_bf16_f32 v139, v46, v47\n\t"
      "s_waitcnt lgkmcnt(9)\n\t"
      "v_mfma_f32_16x16x32_bf16 a[156:159], v[112:115], a[228:231], a[156:159]\n\t"
      "v_cvt_pk_bf16_f32 v140, v48, v49\n\t"
      "v_cvt_pk_bf16_f32 v141, v50, v51\n\t"
      "v_mfma_f32_16x16x32_bf16 a[160:163], v[116:119], a[228:231], a[160:163]\n\t"
      "v_cvt_pk_bf16_f32 v142, v52, v53\n\t"
      "v_cvt_pk_bf16_f32 v143, v54, v55\n\t"
      "ds_read_b128 a[232:235], v56 offset:25344\n\t"
      "v_mfma_f32_4x4x4_16b_bf16 v[40:43], a[180:181], a[92:93], 0\n\t"
      "ds_read_b64_tr_b16 a[100:101], v60 offset:128\n\t"
      "v_mfma_f32_4x4x4_16b_bf16 v[44:47], a[182:183], a[94:95], 0\n\t"
      "ds_read_b64_tr_b16 a[102:103], v60 offset:896\n\t"
      "v_mfma_f32_4x4x4_16b_bf16 v[48:51], a[180:181], a[96:97], 0\n\t"
      "ds_read_b64_tr_b16 a[104:105], v61 offset:128\n\t"
      "v_mfma_f32_4x4x4_16b_bf16 v[52:55], a[182:183], a[98:99], 0\n\t"
      "ds_read_b64_tr_b16 a[106:107], v61 offset:896\n\t"
      "buffer_load_dwordx4 v10, s[84:87], s54 offen offset:1024 lds\n\t"
      "buffer_load_dwordx4 v11, s[84:87], s54 offen offset:2048 lds\n\t"
      "ds_read_b128 a[236:239], v56 offset:37632\n\t"
      "v_mfma_f32_16x16x32_bf16 a[164:167], v[120:123], a[228:231], a[164:167]\n\t"
      "v_cvt_pk_bf16_f32 v40, v40, v41\n\t"
      "v_cvt_pk_bf16_f32 v41, v42, v43\n\t"
      "v_mfma_f32_16x16x32_bf16 a[168:171], v[124:127], a[228:231], a[168:171]\n\t"
      "v_cvt_pk_bf16_f32 v44, v44, v45\n\t"
      "v_cvt_pk_bf16_f32 v45, v46, v47\n\t"
      "v_mfma_f32_16x16x32_bf16 a[172:175], a[84:87], a[228:231], a[172:175]\n\t"
      "v_cvt_pk_bf16_f32 v48, v48, v49\n\t"
      "v_cvt_pk_bf16_f32 v49, v50, v51\n\t"
      "v_mfma_f32_16x16x32_bf16 a[176:179], a[88:91], a[228:231], a[176:179]\n\t"
      "v_cvt_pk_bf16_f32 v52, v52, v53\n\t"
      "v_cvt_pk_bf16_f32 v53, v54, v55\n\t"
      "v_accvgpr_write_b32 a92, v40\n\t"
      "v_accvgpr_write_b32 a93, v41\n\t"
      "v_accvgpr_write_b32 a94, v44\n\t"
      "v_accvgpr_write_b32 a95, v45\n\t"
      "v_accvgpr_write_b32 a96, v48\n\t"
      "v_accvgpr_write_b32 a97, v49\n\t"
      "v_accvgpr_write_b32 a98, v52\n\t"
      "v_accvgpr_write_b32 a99, v53\n\t"
      "s_waitcnt vmcnt(12)\n\t"
      "s_waitcnt lgkmcnt(1)\n\t"
      "; step\n\t"
      "v_mfma_f32_4x4x4_16b_bf16 v[40:43], a[184:185], v[144:145], 0\n\t"
      "ds_read_b128 a[180:183], v14 offset:1536\n\t"
      "v_mfma_f32_4x4x4_16b_bf16 v[44:47], a[186:187], v[146:147], 0\n\t"
      "ds_read_b64_tr_b16 v[160:161], v60 offset:24576\n\t"
      "v_mfma_f32_4x4x4_16b_bf16 v[48:51], a[184:185], v[148:149], 0\n\t"
      "ds_read_b64_tr_b16 v[162:163], v60 offset:25344\n\t"
      "v_mfma_f32_4x4x4_16b_bf16 v[52:55], a[186:187], v[150:151], 0\n\t"
      "ds_read_b64_tr_b16 v[164:165], v61 offset:24576\n\t"
      "ds_read_b64_tr_b16 v[166:167], v61 offset:25344\n\t"
      "s_add_u32 m0, s17, 86528\n\t"
      "s_mov_b32 s54, 221184\n\t"
      "buffer_load_dwordx4 v9, s[84:87], s54 offen lds\n\t"
      "buffer_load_dwordx4 v10, s[84:87], s54 offen offset:1024 lds\n\t"
      "v_mfma_f32_16x16x32_bf16 v[16:19], v[128:131], a[232:235], v[16:19]\n\t"
      "v_cvt_pk_bf16_f32 v144, v40, v41\n\t"
      "v_cvt_pk_bf16_f32 v145, v42, v43\n\t"
      "v_mfma_f32_16x16x32_bf16 v[20:23], v[132:135], a[232:235], v[20:23]\n\t"
      "v_cvt_pk_bf16_f32 v146, v44, v45\n\t"
      "v_cvt_pk_bf16_f32 v147, v46, v47\n\t"
      "v_mfma_f32_16x16x32_bf16 v[24:27], v[136:139], a[232:235], v[24:27]\n\t"
      "v_cvt_pk_bf16_f32 v148, v48, v49\n\t"
      "v_cvt_pk_bf16_f32 v149, v50, v51\n\t"
      "v_mfma_f32_16x16x32_bf16 v[28:31], v[140:143], a[232:235], v[28:31]\n\t"
      "v_cvt_pk_bf16_f32 v150, v52, v53\n\t"
      "v_cvt_pk_bf16_f32 v151, v54, v55\n\t"
      "v_mfma_f32_4x4x4_16b_bf16 v[40:43], a[184:185], v[152:153], 0\n\t"
      "ds_read_b64_tr_b16 v[168:169], v60 offset:24640\n\t"
      "v_mfma_f32_4x4x4_16b_bf16 v[44:47], a[186:187], v[154:155], 0\n\t"
      "ds_read_b64_tr_b16 v[170:171], v60 offset:25408\n\t"
      "v_mfma_f32_4x4x4_16b_bf16 v[48:51], a[184:185], v[156:157], 0\n\t"
      "ds_read_b64_tr_b16 v[172:173], v61 offset:24640\n\t"
      "v_mfma_f32_4x4x4_16b_bf16 v[52:55], a[186:187], v[158:159], 0\n\t"
      "ds_read_b64_tr_b16 v[174:175], v61 offset:25408\n\t"
      "buffer_load_dwordx4 v11, s[84:87], s54 offen offset:2048 lds\n\t"
      "s_add_u32 m0, s17, 89600\n\t"
      "s_mov_b32 s54, 233472\n\t"
      "buffer_load_dwordx4 v9, s[84:87], s54 offen lds\n\t"
      "v_mfma_f32_16x16x32_bf16 v[32:35], a[92:95], a[232:235], v[32:35]\n\t"
      "v_cvt_pk_bf16_f32 v152, v40, v41\n\t"
      "v_cvt_pk_bf16_f32 v153, v42, v43\n\t"
      "v_mfma_f32_16x16x32_bf16 v[36:39], a[96:99], a[232:235], v[36:39]\n\t"
      "v_cvt_pk_bf16_f32 v154, v44, v45\n\t"
      "v_cvt_pk_bf16_f32 v155, v46, v47\n\t"
      "s_waitcnt lgkmcnt(9)\n\t"
      "v_mfma_f32_16x16x32_bf16 a[156:159], v[128:131], a[236:239], a[156:159]\n\t"
      "v_cvt_pk_bf16_f32 v156, v48, v49\n\t"
      "v_cvt_pk_bf16_f32 v157, v50, v51\n\t"
      "v_mfma_f32_16x16x32_bf16 a[160:163], v[132:135], a[236:239], a[160:163]\n\t"
      "v_cvt_pk_bf16_f32 v158, v52, v53\n\t"
      "v_cvt_pk_bf16_f32 v159, v54, v55\n\t"
      "ds_read_b128 a[240:243], v57 offset:25344\n\t"
      "v_mfma_f32_4x4x4_16b_bf16 v[40:43], a[184:185], a[100:101], 0\n\t"
      "ds_read_b64_tr_b16 a[108:109], v60 offset:24704\n\t"
      "v_mfma_f32_4x4x4_16b_bf16 v[44:47], a[186:187], a[102:103], 0\n\t"
      "ds_read_b64_tr_b16 a[110:111], v60 offset:25472\n\t"
      "v_mfma_f32_4x4x4_16b_bf16 v[48:51], a[184:185], a[104:105], 0\n\t"
      "ds_read_b64_tr_b16 a[112:113], v61 offset:24704\n\t"
      "v_mfma_f32_4x4x4_16b_bf16 v[52:55], a[186:187], a[106:107], 0\n\t"
      "ds_read_b64_tr_b16 a[114:115], v61 offset:25472\n\t"
      "buffer_load_dwordx4 v10, s[84:87], s54 offen offset:1024 lds\n\t"
      "buffer_load_dwordx4 v11, s[84:87], s54 offen offset:2048 lds\n\t"
      "ds_read_b128 a[244:247], v57 offset:37632\n\t"
      "v_mfma_f32_16x16x32_bf16 a[164:167], v[136:139], a[236:239], a[164:167]\n\t"
      "v_cvt_pk_bf16_f32 v40, v40, v41\n\t"
      "v_cvt_pk_bf16_f32 v41, v42, v43\n\t"
      "v_mfma_f32_16x16x32_bf16 a[168:171], v[140:143], a[236:239], a[168:171]\n\t"
      "v_cvt_pk_bf16_f32 v44, v44, v45\n\t"
      "v_cvt_pk_bf16_f32 v45, v46, v47\n\t"
      "v_mfma_f32_16x16x32_bf16 a[172:175], a[92:95], a[236:239], a[172:175]\n\t"
      "v_cvt_pk_bf16_f32 v48, v48, v49\n\t"
      "v_cvt_pk_bf16_f32 v49, v50, v51\n\t"
      "v_mfma_f32_16x16x32_bf16 a[176:179], a[96:99], a[236:239], a[176:179]\n\t"
      "v_cvt_pk_bf16_f32 v52, v52, v53\n\t"
      "v_cvt_pk_bf16_f32 v53, v54, v55\n\t"
      "v_accvgpr_write_b32 a100, v40\n\t"
      "v_accvgpr_write_b32 a101, v41\n\t"
      "v_accvgpr_write_b32 a102, v44\n\t"
      "v_accvgpr_write_b32 a103, v45\n\t"
      "v_accvgpr_write_b32 a104, v48\n\t"
      "v_accvgpr_write_b32 a105, v49\n\t"
      "v_accvgpr_write_b32 a106, v52\n\t"
      "v_accvgpr_write_b32 a107, v53\n\t"
      "s_waitcnt vmcnt(12)\n\t"
      "s_waitcnt lgkmcnt(1)\n\t"
      "; step\n\t"
      "v_mfma_f32_4x4x4_16b_bf16 v[40:43], a[180:181], v[160:161], 0\n\t"
      "ds_read_b128 a[184:187], v14 offset:1792\n\t"
      "v_mfma_f32_4x4x4_16b_bf16 v[44:47], a[182:183], v[162:163], 0\n\t"
      "ds_read_b64_tr_b16 v[176:177], v60 offset:49152\n\t"
      "v_mfma_f32_4x4x4_16b_bf16 v[48:51], a[180:181], v[164:165], 0\n\t"
      "ds_read_b64_tr_b16 v[178:179], v60 offset:49920\n\t"
      "v_mfma_f32_4x4x4_16b_bf16 v[52:55], a[182:183], v[166:167], 0\n\t"
      "ds_read_b64_tr_b16 v[180:181], v61 offset:49152\n\t"
      "ds_read_b64_tr_b16 v[182:183], v61 offset:49920\n\t"
      "s_add_u32 m0, s17, 111104\n\t"
      "s_mov_b32 s54, 245760\n\t"
      "buffer_load_dwordx4 v9, s[84:87], s54 offen lds\n\t"
      "buffer_load_dwordx4 v10, s[84:87], s54 offen offset:1024 lds\n\t"
      "v_mfma_f32_16x16x32_bf16 v[16:19], v[144:147], a[240:243], v[16:19]\n\t"
      "v_cvt_pk_bf16_f32 v160, v40, v41\n\t"
      "v_cvt_pk_bf16_f32 v161, v42, v43\n\t"
      "v_mfma_f32_16x16x32_bf16 v[20:23], v[148:151], a[240:243], v[20:23]\n\t"
      "v_cvt_pk_bf16_f32 v162, v44, v45\n\t"
      "v_cvt_pk_bf16_f32 v163, v46, v47\n\t"
      "v_mfma_f32_16x16x32_bf16 v[24:27], v[152:155], a[240:243], v[24:27]\n\t"
      "v_cvt_pk_bf16_f32 v164, v48, v49\n\t"
      "v_cvt_pk_bf16_f32 v165, v50, v51\n\t"
      "v_mfma_f32_16x16x32_bf16 v[28:31], v[156:159], a[240:243], v[28:31]\n\t"
      "v_cvt_pk_bf16_f32 v166, v52, v53\n\t"
      "v_cvt_pk_bf16_f32 v167, v54, v55\n\t"
      "v_mfma_f32_4x4x4_16b_bf16 v[40:43], a[180:181], v[168:169], 0\n\t"
      "ds_read_b64_tr_b16 v[184:185], v60 offset:49216\n\t"
      "v_mfma_f32_4x4x4_16b_bf16 v[44:47], a[182:183], v[170:171], 0\n\t"
      "ds_read_b64_tr_b16 v[186:187], v60 offset:49984\n\t"
      "v_mfma_f32_4x4x4_16b_bf16 v[48:51], a[180:181], v[172:173], 0\n\t"
      "ds_read_b64_tr_b16 v[188:189], v61 offset:49216\n\t"
      "v_mfma_f32_4x4x4_16b_bf16 v[52:55], a[182:183], v[174:175], 0\n\t"
      "ds_read_b64_tr_b16 v[190:191], v61 offset:49984\n\t"
      "buffer_load_dwordx4 v11, s[84:87], s54 offen offset:2048 lds\n\t"
      "s_add_u32 m0, s17, 114176\n\t"
      "s_mov_b32 s54, 258048\n\t"
      "buffer_load_dwordx4 v9, s[84:87], s54 offen lds\n\t"
      "v_mfma_f32_16x16x32_bf16 v[32:35], a[100:103], a[240:243], v[32:35]\n\t"
      "v_cvt_pk_bf16_f32 v168, v40, v41\n\t"
      "v_cvt_pk_bf16_f32 v169, v42, v43\n\t"
      "v_mfma_f32_16x16x32_bf16 v[36:39], a[104:107], a[240:243], v[36:39]\n\t"
      "v_cvt_pk_bf16_f32 v170, v44, v45\n\t"
      "v_cvt_pk_bf16_f32 v171, v46, v47\n\t"
      "s_waitcnt lgkmcnt(9)\n\t"
      "v_mfma_f32_16x16x32_bf16 a[156:159], v[144:147], a[244:247], a[156:159]\n\t"
      "v_cvt_pk_bf16_f32 v172, v48, v49\n\t"
      "v_cvt_pk_bf16_f32 v173, v50, v51\n\t"
      "v_mfma_f32_16x16x32_bf16 a[160:163], v[148:151], a[244:247], a[160:163]\n\t"
      "v_cvt_pk_bf16_f32 v174, v52, v53\n\t"
      "v_cvt_pk_bf16_f32 v175, v54, v55\n\t"
      "ds_read_b128 a[224:227], v58 offset:25344\n\t"
      "v_mfma_f32_4x4x4_16b_bf16 v[40:43], a[180:181], a[108:109], 0\n\t"
      "ds_read_b64_tr_b16 a[116:117], v60 offset:49280\n\t"
      "v_mfma_f32_4x4x4_16b_bf16 v[44:47], a[182:183], a[110:111], 0\n\t"
      "ds_read_b64_tr_b16 a[118:119], v60 offset:50048\n\t"
      "v_mfma_f32_4x4x4_16b_bf16 v[48:51], a[180:181], a[112:113], 0\n\t"
      "ds_read_b64_tr_b16 a[120:121], v61 offset:49280\n\t"
      "v_mfma_f32_4x4x4_16b_bf16 v[52:55], a[182:183], a[114:115], 0\n\t"
      "ds_read_b64_tr_b16 a[122:123], v61 offset:50048\n\t"
      "buffer_load_dwordx4 v10, s[84:87], s54 offen offset:1024 lds\n\t"
      "buffer_load_dwordx4 v11, s[84:87], s54 offen offset:2048 lds\n\t"
      "ds_read_b128 a[228:231], v58 offset:37632\n\t"
      "v_mfma_f32_16x16x32_bf16 a[164:167], v[152:155], a[244:247], a[164:167]\n\t"
      "v_cvt_pk_bf16_f32 v40, v40, v41\n\t"
      "v_cvt_pk_bf16_f32 v41, v42, v43\n\t"
      "v_mfma_f32_16x16x32_bf16 a[168:171], v[156:159], a[244:247], a[168:171]\n\t"
      "v_cvt_pk_bf16_f32 v44, v44, v45\n\t"
      "v_cvt_pk_bf16_f32 v45, v46, v47\n\t"
      "v_mfma_f32_16x16x32_bf16 a[172:175], a[100:103], a[244:247], a[172:175]\n\t"
      "v_cvt_pk_bf16_f32 v48, v48, v49\n\t"
      "v_cvt_pk_bf16_f32 v49, v50, v51\n\t"
      "v_mfma_f32_16x16x32_bf16 a[176:179], a[104:107], a[244:247], a[176:179]\n\t"
      "v_cvt_pk_bf16_f32 v52, v52, v53\n\t"
      "v_cvt_pk_bf16_f32 v53, v54, v55\n\t"
      "v_accvgpr_write_b32 a108, v40\n\t"
      "v_accvgpr_write_b32 a109, v41\n\t"
      "v_accvgpr_write_b32 a110, v44\n\t"
      "v_accvgpr_write_b32 a111, v45\n\t"
      "v_accvgpr_write_b32 a112, v48\n\t"
      "v_accvgpr_write_b32 a113, v49\n\t"
      "v_accvgpr_write_b32 a114, v52\n\t"
      "v_accvgpr_write_b32 a115, v53\n\t"
      "s_waitcnt vmcnt(12)\n\t"
      "s_waitcnt lgkmcnt(1)\n\t"
      "; step\n\t"
      "v_mfma_f32_4x4x4_16b_bf16 v[40:43], a[184:185], v[176:177], 0\n\t"
      "ds_read_b128 a[180:183], v14 offset:2048\n\t"
      "v_mfma_f32_4x4x4_16b_bf16 v[44:47], a[186:187], v[178:179], 0\n\t"
      "ds_read_b64_tr_b16 v[192:193], v12 offset:0\n\t"
      "v_mfma_f32_4x4x4_16b_bf16 v[48:51], a[184:185], v[180:181], 0\n\t"
      "ds_read_b64_tr_b16 v[194:195], v12 offset:768\n\t"
      "v_mfma_f32_4x4x4_16b_bf16 v[52:55], a[186:187], v[182:183], 0\n\t"
      "ds_read_b64_tr_b16 v[196:197], v13 offset:0\n\t"
      "ds_read_b64_tr_b16 v[198:199], v13 offset:768\n\t"
      "s_add_u32 m0, s17, 135680\n\t"
      "s_mov_b32 s54, 270336\n\t"
      "buffer_load_dwordx4 v9, s[84:87], s54 offen lds\n\t"
      "buffer_load_dwordx4 v10, s[84:87], s54 offen offset:1024 lds\n\t"
      "v_mfma_f32_16x16x32_bf16 v[16:19], v[160:163], a[224:227], v[16:19]\n\t"
      "v_cvt_pk_bf16_f32 v176, v40, v41\n\t"
      "v_cvt_pk_bf16_f32 v177, v42, v43\n\t"
      "v_mfma_f32_16x16x32_bf16 v[20:23], v[164:167], a[224:227], v[20:23]\n\t"
      "v_cvt_pk_bf16_f32 v178, v44, v45\n\t"
      "v_cvt_pk_bf16_f32 v179, v46, v47\n\t"
      "v_mfma_f32_16x16x32_bf16 v[24:27], v[168:171], a[224:227], v[24:27]\n\t"
      "v_cvt_pk_bf16_f32 v180, v48, v49\n\t"
      "v_cvt_pk_bf16_f32 v181, v50, v51\n\t"
      "v_mfma_f32_16x16x32_bf16 v[28:31], v[172:175], a[224:227], v[28:31]\n\t"
      "v_cvt_pk_bf16_f32 v182, v52, v53\n\t"
      "v_cvt_pk_bf16_f32 v183, v54, v55\n\t"
      "v_mfma_f32_4x4x4_16b_bf16 v[40:43], a[184:185], v[184:185], 0\n\t"
      "ds_read_b64_tr_b16 v[200:201], v12 offset:64\n\t"
      "v_mfma_f32_4x4x4_16b_bf16 v[44:47], a[186:187], v[186:187], 0\n\t"
      "ds_read_b64_tr_b16 v[202:203], v12 offset:832\n\t"
      "v_mfma_f32_4x4x4_16b_bf16 v[48:51], a[184:185], v[188:189], 0\n\t"
      "ds_read_b64_tr_b16 v[204:205], v13 offset:64\n\t"
      "v_mfma_f32_4x4x4_16b_bf16 v[52:55], a[186:187], v[190:191], 0\n\t"
      "ds_read_b64_tr_b16 v[206:207], v13 offset:832\n\t"
      "buffer_load_dwordx4 v11, s[84:87], s54 offen offset:2048 lds\n\t"
      "s_add_u32 m0, s17, 138752\n\t"
      "s_mov_b32 s54, 282624\n\t"
      "buffer_load_dwordx4 v9, s[84:87], s54 offen lds\n\t"
      "v_mfma_f32_16x16x32_bf16 v[32:35], a[108:111], a[224:227], v[32:35]\n\t"
      "v_cvt_pk_bf16_f32 v184, v40, v41\n\t"
      "v_cvt_pk_bf16_f32 v185, v42, v43\n\t"
      "v_mfma_f32_16x16x32_bf16 v[36:39], a[112:115], a[224:227], v[36:39]\n\t"
      "v_cvt_pk_bf16_f32 v186, v44, v45\n\t"
      "v_cvt_pk_bf16_f32 v187, v46, v47\n\t"
      "s_waitcnt lgkmcnt(9)\n\t"
      "v_mfma_f32_16x16x32_bf16 a[156:159], v[160:163], a[228:231], a[156:159]\n\t"
      "v_cvt_pk_bf16_f32 v188, v48, v49\n\t"
      "v_cvt_pk_bf16_f32 v189, v50, v51\n\t"
      "v_mfma_f32_16x16x32_bf16 a[160:163], v[164:167], a[228:231], a[160:163]\n\t"
      "v_cvt_pk_bf16_f32 v190, v52, v53\n\t"
      "v_cvt_pk_bf16_f32 v191, v54, v55\n\t"
      "ds_read_b128 a[232:235], v59 offset:25344\n\t"
      "v_mfma_f32_4x4x4_16b_bf16 v[40:43], a[184:185], a[116:117], 0\n\t"
      "ds_read_b64_tr_b16 a[124:125], v12 offset:128\n\t"
      "v_mfma_f32_4x4x4_16b_bf16 v[44:47], a[186:187], a[118:119], 0\n\t"
      "ds_read_b64_tr_b16 a[126:127], v12 offset:896\n\t"
      "v_mfma_f32_4x4x4_16b_bf16 v[48:51], a[184:185], a[120:121], 0\n\t"
      "ds_read_b64_tr_b16 a[128:129], v13 offset:128\n\t"
      "v_mfma_f32_4x4x4_16b_bf16 v[52:55], a[186:187], a[122:123], 0\n\t"
      "ds_read_b64_tr_b16 a[130:131], v13 offset:896\n\t"
      "buffer_load_dwordx4 v10, s[84:87], s54 offen offset:1024 lds\n\t"
      "buffer_load_dwordx4 v11, s[84:87], s54 offen offset:2048 lds\n\t"
      "ds_read_b128 a[236:239], v59 offset:37632\n\t"
      "s_or_b32 s30, s17, s16\n\t"
      "s_cbranch_scc1 .Lno3_%=\n\t"
      "global_load_dword a252, v62, s[12:13] sc1\n\t"
      ".Lno3_%=:\n\t"
      "s_add_u32 m0, s18, 49664\n\t"
      "s_add_u32 s48, s15, 786432\n\t"
      "buffer_load_dwordx4 v2, s[40:43], s48 offen lds\n\t"
      "buffer_load_dwordx4 v3, s[40:43], s48 offen offset:1024 lds\n\t"
      "buffer_load_dwordx4 v4, s[40:43], s48 offen offset:2048 lds\n\t"
      "v_mfma_f32_16x16x32_bf16 a[164:167], v[168:171], a[228:231], a[164:167]\n\t"
      "v_cvt_pk_bf16_f32 v40, v40, v41\n\t"
      "v_cvt_pk_bf16_f32 v41, v42, v43\n\t"
      "v_mfma_f32_16x16x32_bf16 a[168:171], v[172:175], a[228:231], a[168:171]\n\t"
      "v_cvt_pk_bf16_f32 v44, v44, v45\n\t"
      "v_cvt_pk_bf16_f32 v45, v46, v47\n\t"
      "v_mfma_f32_16x16x32_bf16 a[172:175], a[108:111], a[228:231], a[172:175]\n\t"
      "v_cvt_pk_bf16_f32 v48, v48, v49\n\t"
      "v_cvt_pk_bf16_f32 v49, v50, v51\n\t"
      "v_mfma_f32_16x16x32_bf16 a[176:179], a[112:115], a[228:231], a[176:179]\n\t"
      "v_cvt_pk_bf16_f32 v52, v52, v53\n\t"
      "v_cvt_pk_bf16_f32 v53, v54, v55\n\t"
      "v_accvgpr_write_b32 a116, v40\n\t"
      "v_accvgpr_write_b32 a117, v41\n\t"
      "v_accvgpr_write_b32 a118, v44\n\t"
      "v_accvgpr_write_b32 a119, v45\n\t"
      "v_accvgpr_write_b32 a120, v48\n\t"
      "v_accvgpr_write_b32 a121, v49\n\t"
      "v_accvgpr_write_b32 a122, v52\n\t"
      "v_accvgpr_write_b32 a123, v53\n\t"
      "s_waitcnt vmcnt(15)\n\t"
      "s_waitcnt lgkmcnt(1)\n\t"
      "; step\n\t"
      "v_mfma_f32_4x4x4_16b_bf16 v[40:43], a[180:181], v[192:193], 0\n\t"
      "ds_read_b128 a[184:187], v14 offset:2304\n\t"
      "v_mfma_f32_4x4x4_16b_bf16 v[44:47], a[182:183], v[194:195], 0\n\t"
      "ds_read_b64_tr_b16 v[208:209], v60 offset:0\n\t"
      "v_mfma_f32_4x4x4_16b_bf16 v[48:51], a[180:181], v[196:197], 0\n\t"
      "ds_read_b64_tr_b16 v[210:211], v60 offset:768\n\t"
      "v_mfma_f32_4x4x4_16b_bf16 v[52:55], a[182:183], v[198:199], 0\n\t"
      "ds_read_b64_tr_b16 v[212:213], v61 offset:0\n\t"
      "ds_read_b64_tr_b16 v[214:215], v61 offset:768\n\t"
      "s_add_u32 m0, s18, 61952\n\t"
      "s_add_u32 s48, s15, 786432\n\t"
      "buffer_load_dwordx4 v5, s[40:43], s48 offen lds\n\t"
      "buffer_load_dwordx4 v6, s[40:43], s48 offen offset:1024 lds\n\t"
      "v_mfma_f32_16x16x32_bf16 v[16:19], v[176:179], a[232:235], v[16:19]\n\t"
      "v_cvt_pk_bf16_f32 v192, v40, v41\n\t"
      "v_cvt_pk_bf16_f32 v193, v42, v43\n\t"
      "v_mfma_f32_16x16x32_bf16 v[20:23], v[180:183], a[232:235], v[20:23]\n\t"
      "v_cvt_pk_bf16_f32 v194, v44, v45\n\t"
      "v_cvt_pk_bf16_f32 v195, v46, v47\n\t"
      "v_mfma_f32_16x16x32_bf16 v[24:27], v[184:187], a[232:235], v[24:27]\n\t"
      "v_cvt_pk_bf16_f32 v196, v48, v49\n\t"
      "v_cvt_pk_bf16_f32 v197, v50, v51\n\t"
      "v_mfma_f32_16x16x32_bf16 v[28:31], v[188:191], a[232:235], v[28:31]\n\t"
      "v_cvt_pk_bf16_f32 v198, v52, v53\n\t"
      "v_cvt_pk_bf16_f32 v199, v54, v55\n\t"
      "v_mfma_f32_4x4x4_16b_bf16 v[40:43], a[180:181], v[200:201], 0\n\t"
      "ds_read_b64_tr_b16 v[216:217], v60 offset:64\n\t"
      "v_mfma_f32_4x4x4_16b_bf16 v[44:47], a[182:183], v[202:203], 0\n\t"
      "ds_read_b64_tr_b16 v[218:219], v60 offset:832\n\t"
      "v_mfma_f32_4x4x4_16b_bf16 v[48:51], a[180:181], v[204:205], 0\n\t"
      "ds_read_b64_tr_b16 v[220:221], v61 offset:64\n\t"
      "v_mfma_f32_4x4x4_16b_bf16 v[52:55], a[182:183], v[206:207], 0\n\t"
      "ds_read_b64_tr_b16 v[222:223], v61 offset:832\n\t"
      "buffer_load_dwordx4 v7, s[40:43], s48 offen offset:2048 lds\n\t"
      "v_mfma_f32_16x16x32_bf16 v[32:35], a[116:119], a[232:235], v[32:35]\n\t"
      "v_cvt_pk_bf16_f32 v200, v40, v41\n\t"
      "v_cvt_pk_bf16_f32 v201, v42, v43\n\t"
      "v_mfma_f32_16x16x32_bf16 v[36:39], a[120:123], a[232:235], v[36:39]\n\t"
      "v_cvt_pk_bf16_f32 v202, v44, v45\n\t"
      "v_cvt_pk_bf16_f32 v203, v46, v47\n\t"
      "s_waitcnt lgkmcnt(9)\n\t"
      "v_mfma_f32_16x16x32_bf16 a[156:159], v[176:179], a[236:239], a[156:159]\n\t"
      "v_cvt_pk_bf16_f32 v204, v48, v49\n\t"
      "v_cvt_pk_bf16_f32 v205, v50, v51\n\t"
      "v_mfma_f32_16x16x32_bf16 a[160:163], v[180:183], a[236:239], a[160:163]\n\t"
      "v_cvt_pk_bf16_f32 v206, v52, v53\n\t"
      "v_cvt_pk_bf16_f32 v207, v54, v55\n\t"
      "ds_read_b128 a[240:243], v56 offset:25600\n\t"
      "v_mfma_f32_4x4x4_16b_bf16 v[40:43], a[180:181], a[124:125], 0\n\t"
      "ds_read_b64_tr_b16 a[132:133], v60 offset:128\n\t"
      "v_mfma_f32_4x4x4_16b_bf16 v[44:47], a[182:183], a[126:127], 0\n\t"
      "ds_read_b64_tr_b16 a[134:135], v60 offset:896\n\t"
      "v_mfma_f32_4x4x4_16b_bf16 v[48:51], a[180:181], a[128:129], 0\n\t"
      "ds_read_b64_tr_b16 a[136:137], v61 offset:128\n\t"
      "v_mfma_f32_4x4x4_16b_bf16 v[52:55], a[182:183], a[130:131], 0\n\t"
      "ds_read_b64_tr_b16 a[138:139], v61 offset:896\n\t"
      "ds_read_b128 a[244:247], v56 offset:37888\n\t"
      "v_mfma_f32_16x16x32_bf16 a[164:167], v[184:187], a[236:239], a[164:167]\n\t"
      "v_cvt_pk_bf16_f32 v40, v40, v41\n\t"
      "v_cvt_pk_bf16_f32 v41, v42, v43\n\t"
      "v_mfma_f32_16x16x32_bf16 a[168:171], v[188:191], a[236:239], a[168:171]\n\t"
      "v_cvt_pk_bf16_f32 v44, v44, v45\n\t"
      "v_cvt_pk_bf16_f32 v45, v46, v47\n\t"
      "v_mfma_f32_16x16x32_bf16 a[172:175], a[116:119], a[236:239], a[172:175]\n\t"
      "v_cvt_pk_bf16_f32 v48, v48, v49\n\t"
      "v_cvt_pk_bf16_f32 v49, v50, v51\n\t"
      "v_mfma_f32_16x16x32_bf16 a[176:179], a[120:123], a[236:239], a[176:179]\n\t"
      "v_cvt_pk_bf16_f32 v52, v52, v53\n\t"
      "v_cvt_pk_bf16_f32 v53, v54, v55\n\t"
      "v_accvgpr_write_b32 a124, v40\n\t"
      "v_accvgpr_write_b32 a125, v41\n\t"
      "v_accvgpr_write_b32 a126, v44\n\t"
      "v_accvgpr_write_b32 a127, v45\n\t"
      "v_accvgpr_write_b32 a128, v48\n\t"
      "v_accvgpr_write_b32 a129, v49\n\t"
      "v_accvgpr_write_b32 a130, v52\n\t"
      "v_accvgpr_write_b32 a131, v53\n\t"
      "s_waitcnt vmcnt(12)\n\t"
      "s_waitcnt lgkmcnt(1)\n\t"
      "; step\n\t"
      "v_mfma_f32_4x4x4_16b_bf16 v[40:43], a[184:185], v[208:209], 0\n\t"
      "ds_read_b128 a[180:183], v14 offset:2560\n\t"
      "v_mfma_f32_4x4x4_16b_bf16 v[44:47], a[186:187], v[210:211], 0\n\t"
      "ds_read_b64_tr_b16 v[224:225], v60 offset:24576\n\t"
      "v_mfma_f32_4x4x4_16b_bf16 v[48:51], a[184:185], v[212:213], 0\n\t"
      "ds_read_b64_tr_b16 v[226:227], v60 offset:25344\n\t"
      "v_mfma_f32_4x4x4_16b_bf16 v[52:55], a[186:187], v[214:215], 0\n\t"
      "ds_read_b64_tr_b16 v[228:229], v61 offset:24576\n\t"
      "ds_read_b64_tr_b16 v[230:231], v61 offset:25344\n\t"
      "v_mfma_f32_16x16x32_bf16 v[16:19], v[192:195], a[240:243], v[16:19]\n\t"
      "v_cvt_pk_bf16_f32 v208, v40, v41\n\t"
      "v_cvt_pk_bf16_f32 v209, v42, v43\n\t"
      "v_mfma_f32_16x16x32_bf16 v[20:23], v[196:199], a[240:243], v[20:23]\n\t"
      "v_cvt_pk_bf16_f32 v210, v44, v45\n\t"
      "v_cvt_pk_bf16_f32 v211, v46, v47\n\t"
      "v_mfma_f32_16x16x32_bf16 v[24:27], v[200:203], a[240:243], v[24:27]\n\t"
      "v_cvt_pk_bf16_f32 v212, v48, v49\n\t"
      "v_cvt_pk_bf16_f32 v213, v50, v51\n\t"
      "v_mfma_f32_16x16x32_bf16 v[28:31], v[204:207], a[240:243], v[28:31]\n\t"
      "v_cvt_pk_bf16_f32 v214, v52, v53\n\t"
      "v_cvt_pk_bf16_f32 v215, v54, v55\n\t"
      "v_mfma_f32_4x4x4_16b_bf16 v[40:43], a[184:185], v[216:217], 0\n\t"
      "ds_read_b64_tr_b16 v[232:233], v60 offset:24640\n\t"
      "v_mfma_f32_4x4x4_16b_bf16 v[44:47], a[186:187], v[218:219], 0\n\t"
      "ds_read_b64_tr_b16 v[234:235], v60 offset:25408\n\t"
      "v_mfma_f32_4x4x4_16b_bf16 v[48:51], a[184:185], v[220:221], 0\n\t"
      "ds_read_b64_tr_b16 v[236:237], v61 offset:24640\n\t"
      "v_mfma_f32_4x4x4_16b_bf16 v[52:55], a[186:187], v[222:223], 0\n\t"
      "ds_read_b64_tr_b16 v[238:239], v61 offset:25408\n\t"
      "v_mfma_f32_16x16x32_bf16 v[32:35], a[124:127], a[240:243], v[32:35]\n\t"
      "v_cvt_pk_bf16_f32 v216, v40, v41\n\t"
      "v_cvt_pk_bf16_f32 v217, v42, v43\n\t"
      "v_mfma_f32_16x16x32_bf16 v[36:39], a[128:131], a[240:243], v[36:39]\n\t"
      "v_cvt_pk_bf16_f32 v218, v44, v45\n\t"
      "v_cvt_pk_bf16_f32 v219, v46, v47\n\t"
      "s_waitcnt lgkmcnt(9)\n\t"
      "v_mfma_f32_16x16x32_bf16 a[156:159], v[192:195], a[244:247], a[156:159]\n\t"
      "v_cvt_pk_bf16_f32 v220, v48, v49\n\t"
      "v_cvt_pk_bf16_f32 v221, v50, v51\n\t"
      "v_mfma_f32_16x16x32_bf16 a[160:163], v[196:199], a[244:247], a[160:163]\n\t"
      "v_cvt_pk_bf16_f32 v222, v52, v53\n\t"
      "v_cvt_pk_bf16_f32 v223, v54, v55\n\t"
      "ds_read_b128 a[224:227], v57 offset:25600\n\t"
      "v_mfma_f32_4x4x4_16b_bf16 v[40:43], a[184:185], a[132:133], 0\n\t"
      "ds_read_b64_tr_b16 a[140:141], v60 offset:24704\n\t"
      "v_mfma_f32_4x4x4_16b_bf16 v[44:47], a[186:187], a[134:135], 0\n\t"
      "ds_read_b64_tr_b16 a[142:143], v60 offset:25472\n\t"
      "v_mfma_f32_4x4x4_16b_bf16 v[48:51], a[184:185], a[136:137], 0\n\t"
      "ds_read_b64_tr_b16 a[144:145], v61 offset:24704\n\t"
      "v_mfma_f32_4x4x4_16b_bf16 v[52:55], a[186:187], a[138:139], 0\n\t"
      "ds_read_b64_tr_b16 a[146:147], v61 offset:25472\n\t"
      "ds_read_b128 a[228:231], v57 offset:37888\n\t"
      "v_mfma_f32_16x16x32_bf16 a[164:167], v[200:203], a[244:247], a[164:167]\n\t"
      "v_cvt_pk_bf16_f32 v40, v40, v41\n\t"
      "v_cvt_pk_bf16_f32 v41, v42, v43\n\t"
      "v_mfma_f32_16x16x32_bf16 a[168:171], v[204:207], a[244:247], a[168:171]\n\t"
      "v_cvt_pk_bf16_f32 v44, v44, v45\n\t"
      "v_cvt_pk_bf16_f32 v45, v46, v47\n\t"
      "v_mfma_f32_16x16x32_bf16 a[172:175], a[124:127], a[244:247], a[172:175]\n\t"
      "v_cvt_pk_bf16_f32 v48, v48, v49\n\t"
      "v_cvt_pk_bf16_f32 v49, v50, v51\n\t"
      "v_mfma_f32_16x16x32_bf16 a[176:179], a[128:131], a[244:247], a[176:179]\n\t"
      "v_cvt_pk_bf16_f32 v52, v52, v53\n\t"
      "v_cvt_pk_bf16_f32 v53, v54, v55\n\t"
      "v_accvgpr_write_b32 a132, v40\n\t"
      "v_accvgpr_write_b32 a133, v41\n\t"
      "v_accvgpr_write_b32 a134, v44\n\t"
      "v_accvgpr_write_b32 a135, v45\n\t"
      "v_accvgpr_write_b32 a136, v48\n\t"
      "v_accvgpr_write_b32 a137, v49\n\t"
      "v_accvgpr_write_b32 a138, v52\n\t"
      "v_accvgpr_write_b32 a139, v53\n\t"
      "s_waitcnt vmcnt(6)\n\t"
      "s_waitcnt lgkmcnt(1)\n\t"
      "; step\n\t"
      "s_memtime s[68:69]\n\t"
      "s_memrealtime s[70:71]\n\t"
      "v_mfma_f32_4x4x4_16b_bf16 v[40:43], a[180:181], v[224:225], 0\n\t"
      "ds_read_b128 a[184:187], v14 offset:2816\n\t"
      "v_mfma_f32_4x4x4_16b_bf16 v[44:47], a[182:183], v[226:227], 0\n\t"
      "ds_read_b64_tr_b16 v[240:241], v60 offset:49152\n\t"
      "v_mfma_f32_4x4x4_16b_bf16 v[48:51], a[180:181], v[228:229], 0\n\t"
      "ds_read_b64_tr_b16 v[242:243], v60 offset:49920\n\t"
      "v_mfma_f32_4x4x4_16b_bf16 v[52:55], a[182:183], v[230:231], 0\n\t"
      "ds_read_b64_tr_b16 v[244:245], v61 offset:49152\n\t"
      "ds_read_b64_tr_b16 v[246:247], v61 offset:49920\n\t"
      "v_mfma_f32_16x16x32_bf16 v[16:19], v[208:211], a[224:227], v[16:19]\n\t"
      "v_cvt_pk_bf16_f32 v224, v40, v41\n\t"
      "v_cvt_pk_bf16_f32 v225, v42, v43\n\t"
      "v_mfma_f32_16x16x32_bf16 v[20:23], v[212:215], a[224:227], v[20:23]\n\t"
      "v_cvt_pk_bf16_f32 v226, v44, v45\n\t"
      "v_cvt_pk_bf16_f32 v227, v46, v47\n\t"
      "v_mfma_f32_16x16x32_bf16 v[24:27], v[216:219], a[224:227], v[24:27]\n\t"
      "v_cvt_pk_bf16_f32 v228, v48, v49\n\t"
      "v_cvt_pk_bf16_f32 v229, v50, v51\n\t"
      "v_mfma_f32_16x16x32_bf16 v[28:31], v[220:223], a[224:227], v[28:31]\n\t"
      "v_cvt_pk_bf16_f32 v230, v52, v53\n\t"
      "v_cvt_pk_bf16_f32 v231, v54, v55\n\t"
      "v_mfma_f32_4x4x4_16b_bf16 v[40:43], a[180:181], v[232:233], 0\n\t"
      "ds_read_b64_tr_b16 v[248:249], v60 offset:49216\n\t"
      "v_mfma_f32_4x4x4_16b_bf16 v[44:47], a[182:183], v[234:235], 0\n\t"
      "ds_read_b64_tr_b16 v[250:251], v60 offset:49984\n\t"
      "v_mfma_f32_4x4x4_16b_bf16 v[48:51], a[180:181], v[236:237], 0\n\t"
      "ds_read_b64_tr_b16 v[252:253], v61 offset:49216\n\t"
      "v_mfma_f32_4x4x4_16b_bf16 v[52:55], a[182:183], v[238:239], 0\n\t"
      "ds_read_b64_tr_b16 v[254:255], v61 offset:49984\n\t"
      "v_mfma_f32_16x16x32_bf16 v[32:35], a[132:135], a[224:227], v[32:35]\n\t"
      "v_cvt_pk_bf16_f32 v232, v40, v41\n\t"
      "v_cvt_pk_bf16_f32 v233, v42, v43\n\t"
      "v_mfma_f32_16x16x32_bf16 v[36:39], a[136:139], a[224:227], v[36:39]\n\t"
      "v_cvt_pk_bf16_f32 v234, v44, v45\n\t"
      "v_cvt_pk_bf16_f32 v235, v46, v47\n\t"
      "s_waitcnt lgkmcnt(9)\n\t"
      "v_mfma_f32_16x16x32_bf16 a[156:159], v[208:211], a[228:231], a[156:159]\n\t"
      "v_cvt_pk_bf16_f32 v236, v48, v49\n\t"
      "v_cvt_pk_bf16_f32 v237, v50, v51\n\t"
      "v_mfma_f32_16x16x32_bf16 a[160:163], v[212:215], a[228:231], a[160:163]\n\t"
      "v_cvt_pk_bf16_f32 v238, v52, v53\n\t"
      "v_cvt_pk_bf16_f32 v239, v54, v55\n\t"
      "ds_read_b128 a[232:235], v58 offset:25600\n\t"
      "v_mfma_f32_4x4x4_16b_bf16 v[40:43], a[180:181], a[140:141], 0\n\t"
      "ds_read_b64_tr_b16 a[148:149], v60 offset:49280\n\t"
      "v_mfma_f32_4x4x4_16b_bf16 v[44:47], a[182:183], a[142:143], 0\n\t"
      "ds_read_b64_tr_b16 a[150:151], v60 offset:50048\n\t"
      "v_mfma_f32_4x4x4_16b_bf16 v[48:51], a[180:181], a[144:145], 0\n\t"
      "ds_read_b64_tr_b16 a[152:153], v61 offset:49280\n\t"
      "v_mfma_f32_4x4x4_16b_bf16 v[52:55], a[182:183], a[146:147], 0\n\t"
      "ds_read_b64_tr_b16 a[154:155], v61 offset:50048\n\t"
      "v_mfma_f32_16x16x32_bf16 a[164:167], v[216:219], a[228:231], a[164:167]\n\t"
      "v_cvt_pk_bf16_f32 v40, v40, v41\n\t"
      "v_cvt_pk_bf16_f32 v41, v42, v43\n\t"
      "v_mfma_f32_16x16x32_bf16 a[168:171], v[220:223], a[228:231], a[168:171]\n\t"
      "v_cvt_pk_bf16_f32 v44, v44, v45\n\t"
      "v_cvt_pk_bf16_f32 v45, v46, v47\n\t"
      "v_mfma_f32_16x16x32_bf16 a[172:175], a[132:135], a[228:231], a[172:175]\n\t"
      "v_cvt_pk_bf16_f32 v48, v48, v49\n\t"
      "v_cvt_pk_bf16_f32 v49, v50, v51\n\t"
      "v_mfma_f32_16x16x32_bf16 a[176:179], a[136:139], a[228:231], a[176:179]\n\t"
      "v_cvt_pk_bf16_f32 v52, v52, v53\n\t"
      "v_cvt_pk_bf16_f32 v53, v54, v55\n\t"
      "v_accvgpr_write_b32 a140, v40\n\t"
      "v_accvgpr_write_b32 a141, v41\n\t"
      "v_accvgpr_write_b32 a142, v44\n\t"
      "v_accvgpr_write_b32 a143, v45\n\t"
      "v_accvgpr_write_b32 a144, v48\n\t"
      "v_accvgpr_write_b32 a145, v49\n\t"
      "v_accvgpr_write_b32 a146, v52\n\t"
      "v_accvgpr_write_b32 a147, v53\n\t"
      "s_waitcnt lgkmcnt(0)\n\t"
      "; step\n\t"
      "v_mfma_f32_4x4x4_16b_bf16 v[40:43], a[184:185], v[240:241], 0\n\t"
      "v_mfma_f32_4x4x4_16b_bf16 v[44:47], a[186:187], v[242:243], 0\n\t"
      "v_mfma_f32_4x4x4_16b_bf16 v[48:51], a[184:185], v[244:245], 0\n\t"
      "v_mfma_f32_4x4x4_16b_bf16 v[52:55], a[186:187], v[246:247], 0\n\t"
      "v_mfma_f32_16x16x32_bf16 v[16:19], v[224:227], a[232:235], v[16:19]\n\t"
      "s_nop 0\n\t"
      "v_cvt_pk_bf16_f32 v240, v40, v41\n\t"
      "v_cvt_pk_bf16_f32 v241, v42, v43\n\t"
      "v_mfma_f32_16x16x32_bf16 v[20:23], v[228:231], a[232:235], v[20:23]\n\t"
      "v_cvt_pk_bf16_f32 v242, v44, v45\n\t"
      "v_cvt_pk_bf16_f32 v243, v46, v47\n\t"
      "v_cvt_pk_bf16_f32 v244, v48, v49\n\t"
      "v_cvt_pk_bf16_f32 v245, v50, v51\n\t"
      "v_cvt_pk_bf16_f32 v246, v52, v53\n\t"
      "v_cvt_pk_bf16_f32 v247, v54, v55\n\t"
      "v_mfma_f32_4x4x4_16b_bf16 v[40:43], a[184:185], v[248:249], 0\n\t"
      "v_mfma_f32_4x4x4_16b_bf16 v[44:47], a[186:187], v[250:251], 0\n\t"
      "v_mfma_f32_4x4x4_16b_bf16 v[48:51], a[184:185], v[252:253], 0\n\t"
      "v_mfma_f32_4x4x4_16b_bf16 v[52:55], a[186:187], v[254:255], 0\n\t"
      "v_mfma_f32_16x16x32_bf16 v[24:27], v[232:235], a[232:235], v[24:27]\n\t"
      "s_nop 0\n\t"
      "v_cvt_pk_bf16_f32 v248, v40, v41\n\t"
      "v_cvt_pk_bf16_f32 v249, v42, v43\n\t"
      "v_mfma_f32_16x16x32_bf16 v[28:31], v[236:239], a[232:235], v[28:31]\n\t"
      "v_cvt_pk_bf16_f32 v250, v44, v45\n\t"
      "v_cvt_pk_bf16_f32 v251, v46, v47\n\t"
      "v_cvt_pk_bf16_f32 v252, v48, v49\n\t"
      "v_cvt_pk_bf16_f32 v253, v50, v51\n\t"
      "v_cvt_pk_bf16_f32 v254, v52, v53\n\t"
      "v_cvt_pk_bf16_f32 v255, v54, v55\n\t"
      "ds_read_b128 a[236:239], v59 offset:25600\n\t"
      "v_mfma_f32_4x4x4_16b_bf16 v[40:43], a[184:185], a[148:149], 0\n\t"
      "v_mfma_f32_4x4x4_16b_bf16 v[44:47], a[186:187], a[150:151], 0\n\t"
      "v_mfma_f32_4x4x4_16b_bf16 v[48:51], a[184:185], a[152:153], 0\n\t"
      "v_mfma_f32_4x4x4_16b_bf16 v[52:55], a[186:187], a[154:155], 0\n\t"
      "ds_read_b128 a[240:243], v58 offset:37888\n\t"
      "ds_read_b128 a[244:247], v59 offset:37888\n\t"
      "v_mfma_f32_16x16x32_bf16 v[32:35], a[140:143], a[232:235], v[32:35]\n\t"
      "v_cvt_pk_bf16_f32 v40, v40, v41\n\t"
      "v_cvt_pk_bf16_f32 v41, v42, v43\n\t"
      "v_mfma_f32_16x16x32_bf16 v[36:39], a[144:147], a[232:235], v[36:39]\n\t"
      "v_cvt_pk_bf16_f32 v44, v44, v45\n\t"
      "v_cvt_pk_bf16_f32 v45, v46, v47\n\t"
      "v_cvt_pk_bf16_f32 v48, v48, v49\n\t"
      "v_cvt_pk_bf16_f32 v49, v50, v51\n\t"
      "v_cvt_pk_bf16_f32 v52, v52, v53\n\t"
      "v_cvt_pk_bf16_f32 v53, v54, v55\n\t"
      "v_accvgpr_write_b32 a148, v40\n\t"
      "v_accvgpr_write_b32 a149, v41\n\t"
      "v_accvgpr_write_b32 a150, v44\n\t"
      "v_accvgpr_write_b32 a151, v45\n\t"
      "v_accvgpr_write_b32 a152, v48\n\t"
      "v_accvgpr_write_b32 a153, v49\n\t"
      "v_accvgpr_write_b32 a154, v52\n\t"
      "v_accvgpr_write_b32 a155, v53\n\t"
      "s_waitcnt lgkmcnt(2)\n\t"
      "v_mfma_f32_16x16x32_bf16 v[16:19], v[240:243], a[236:239], v[16:19]\n\t"
      "s_add_u32 s49, s15, 0\n\t"
      "s_mov_b32 s50, 98816\n\t"
      "s_mov_b32 s51, 61952\n\t"
      "v_mfma_f32_16x16x32_bf16 v[20:23], v[244:247], a[236:239], v[20:23]\n\t"
      "s_mov_b32 s58, s14\n\t"
      "s_add_u32 s56, s14, 2359296\n\t"
      "v_mfma_f32_16x16x32_bf16 v[24:27], v[248:251], a[236:239], v[24:27]\n\t"
      "s_movk_i32 s39, 96\n\t"
      "s_movk_i32 s29, 71\n\t"
      "s_mov_b32 s32, 1947275679\n\t"
      "v_mfma_f32_16x16x32_bf16 v[28:31], v[252:255], a[236:239], v[28:31]\n\t"
      "s_mov_b32 s33, 143165559\n\t"
      "s_mov_b32 s34, 1950421356\n\t"
      "s_mov_b32 s35, 8947847\n\t"
      "v_mfma_f32_16x16x32_bf16 v[32:35], a[148:151], a[236:239], v[32:35]\n\t"
      "v_mov_b32 v52, 163328\n\t"
      "v_mfma_f32_16x16x32_bf16 v[36:39], a[152:155], a[236:239], v[36:39]\n\t"
      "s_cmp_lg_u32 s17, 0\n\t"
      "s_cselect_b32 s39, 0, s39\n\t"
      "s_cbranch_scc1 .Lno4_%=\n\t"
      "v_accvgpr_read_b32 v41, a248\n\t"
      "v_mov_b32 v51, 0x4040\n\t"
      "s_movk_i32 s52, 2000\n\t"
      "s_mov_b32 s91, 0\n\t"
      "s_waitcnt vmcnt(6) \n\t"
      "v_accvgpr_read_b32 v40, a252\n\t"
      ".Lcheck_%=:\n\t"
      "v_cmp_ne_u32 s[54:55], v40, v63\n\t"
      "s_cmp_lg_u32 s16, 0\n\t"
      "v_add_u32_dpp v42, v40, v40 quad_perm:[2,3,0,1] row_mask:0xf bank_mask:0xf\n\t"
      "s_cselect_b32 s30, -1, s54\n\t"
      "s_cmp_lg_u32 s30, -1\n\t"
      "s_cbranch_scc1 .Lwait_%=\n\t"
      ".Lwaited_%=:\n\t"
      "v_add_u32_dpp v42, v42, v42 quad_perm:[1,0,3,2] row_mask:0xf bank_mask:0xf\n\t"
      "s_bfe_u32 s30, s19, 0x30008\n\t"
      "s_bfe_u32 s31, s19, 0x10003\n\t"
      "s_lshl1_add_u32 s30, s30, s31\n\t"
      "s_lshl_b32 s30, s30, 1\n\t"
      "v_mov_b32 v43, v42\n\t"
      "v_subrev_u32_dpp v43, v42, v51 row_half_mirror row_mask:0x3 bank_mask:0xf\n\t"
      "v_lshl_add_u32 v42, v42, 1, v43\n\t"
      "v_readlane_b32 s31, v40, s30\n\t"
      "v_add_u32 v42, 0x1f1f, v42\n\t"
      "v_and_b32 v43, 128, v42\n\t"
      "v_and_b32 v44, 32768, v42\n\t"
      "v_cmp_ne_u32 s[88:89], 0, v43\n\t"
      "v_cmp_ne_u32 s[36:37], 0, v44\n\t"
      "s_bitcmp1_b32 s19, 2\n\t"
      "s_cselect_b32 s30, 0x3ffff, 0\n\t"
      "s_bitcmp1_b32 s31, 16\n\t"
      "s_cselect_b32 s31, 262140, 0\n\t"
      "s_xor_b32 s30, s30, s31\n\t"
      "s_xor_b32 s72, s36, s88\n\t"
      "s_and_b32 s72, s72, 0x11111111\n\t"
      "s_bcnt1_i32_b32 s72, s72\n\t"
      "s_bfe_u32 s31, %[blk], 0x20003\n\t"
      "s_lshr_b32 s31, 1118481, s31\n\t"
      "s_and_b32 s31, s31, 262140\n\t"
      "s_cmp_ge_u32 s72, 7\n\t"
      "s_cselect_b32 s31, s31, 0\n\t"
      "s_xor_b32 s30, s30, s31\n\t"
      "v_lshrrev_b32 v41, v41, s88\n\t"
      "v_and_b32 v41, 1, v41\n\t"
      "v_cmp_ne_u32 vcc, 0, v41\n\t"
      "s_nop 0\n\t"
      "s_and_b32 s31, vcc_lo, 262140\n\t"
      "s_xor_b32 s31, s31, s30\n\t"
      "s_or_b32 s31, s31, s91\n\t"
      "s_and_b32 s30, s16, 0x3ffff\n\t"
      "s_cmp_lg_u32 s16, 0\n\t"
      "s_cselect_b32 s31, s30, s31\n\t"
      "v_mov_b32 v53, s31\n\t"
      "ds_write_b32 v52, v53\n\t"
      "s_xor_b32 s36, s37, s89\n\t"
      "s_lshr_b32 s30, s55, 1\n\t"
      "s_and_b32 s55, s55, s30\n\t"
      "s_lshr_b32 s30, s55, 2\n\t"
      "s_and_b32 s55, s55, s30\n\t"
      "s_and_b32 s36, s36, s55\n\t"
      "s_and_b32 s36, s36, 0x11111111\n\t"
      "s_bcnt1_i32_b32 s36, s36\n\t"
      "s_lshl_b32 s30, s36, 2\n\t"
      "s_mov_b32 s54, 1394741248\n\t"
      "s_mov_b32 s55, 6\n\t"
      "s_lshr_b64 s[54:55], s[54:55], s30\n\t"
      "s_and_b32 s30, s54, 15\n\t"
      "s_add_u32 s29, s29, s30\n\t"
      ".Lno4_%=:\n\t"
      "v_accvgpr_read_b32 v9, a213\n\t"
      "v_accvgpr_read_b32 v10, a214\n\t"
      "v_accvgpr_read_b32 v11, a215\n\t"
      "v_accvgpr_read_b32 v12, a216\n\t"
      "v_accvgpr_read_b32 v13, a217\n\t"
      "v_accvgpr_read_b32 v14, a218\n\t"
      "v_accvgpr_read_b32 v8, a219\n\t"
      "v_accvgpr_read_b32 v60, a220\n\t"
      "v_accvgpr_read_b32 v61, a221\n\t"
      "v_add_u32 v56, 49664, v56\n\t"
      "v_add_u32 v57, 49664, v57\n\t"
      "v_add_u32 v58, 49664, v58\n\t"
      "v_add_u32 v59, 49664, v59\n\t"
      "v_cvt_pk_bf16_f32 v40, v16, v17\n\t"
      "v_cvt_pk_bf16_f32 v41, v18, v19\n\t"
      "v_cvt_pk_bf16_f32 v42, v20, v21\n\t"
      "v_cvt_pk_bf16_f32 v43, v22, v23\n\t"
      "v_cvt_pk_bf16_f32 v44, v24, v25\n\t"
      "v_cvt_pk_bf16_f32 v45, v26, v27\n\t"
      "v_cvt_pk_bf16_f32 v46, v28, v29\n\t"
      "v_cvt_pk_bf16_f32 v47, v30, v31\n\t"
      "v_cvt_pk_bf16_f32 v48, v32, v33\n\t"
      "v_cvt_pk_bf16_f32 v49, v34, v35\n\t"
      "v_cvt_pk_bf16_f32 v50, v36, v37\n\t"
      "v_cvt_pk_bf16_f32 v51, v38, v39\n\t"
      "s_mov_b32 s46, 113246208\n\t"
      "s_mov_b32 s52, 15\n\t"
      "s_add_u32 s90, s90, 0x2000000\n\t"
      "s_waitcnt vmcnt(3)\n\t"
      "s_waitcnt lgkmcnt(0)\n\t"
      "s_barrier\n\t"
      "v_mfma_f32_16x16x32_bf16 a[156:159], v[224:227], a[240:243], a[156:159]\n\t"
      "ds_read_b32 v63, v52\n\t"
      "ds_read_b128 a[0:3], v56 offset:0\n\t"
      "s_memrealtime s[36:37]\n\t"
      "v_mfma_f32_16x16x32_bf16 a[160:163], v[228:231], a[240:243], a[160:163]\n\t"
      "ds_read_b128 a[4:7], v57 offset:0\n\t"
      "ds_read_b128 a[8:11], v58 offset:0\n\t"
      "v_mfma_f32_16x16x32_bf16 a[164:167], v[232:235], a[240:243], a[164:167]\n\t"
      "ds_read_b128 a[12:15], v59 offset:0\n\t"
      "ds_read_b128 a[16:19], v56 offset:256\n\t"
      "v_mfma_f32_16x16x32_bf16 a[168:171], v[236:239], a[240:243], a[168:171]\n\t"
      "ds_read_b128 a[20:23], v57 offset:256\n\t"
      "ds_write_b64 v0, v[40:41] offset:0\n\t"
      "v_mfma_f32_16x16x32_bf16 a[172:175], a[140:143], a[240:243], a[172:175]\n\t"
      "ds_write_b64 v0, v[42:43] offset:32\n\t"
      "v_mfma_f32_16x16x32_bf16 a[176:179], a[144:147], a[240:243], a[176:179]\n\t"
      "s_waitcnt lgkmcnt(8)\n\t"
      "v_readfirstlane_b32 s91, v63\n\t"
      "v_mfma_f32_16x16x32_bf16 a[156:159], v[240:243], a[244:247], a[156:159]\n\t"
      "ds_write_b64 v0, v[44:45] offset:64\n\t"
      "s_add_u32 m0, s18, 74240\n\t"
      "v_mfma_f32_16x16x32_bf16 a[160:163], v[244:247], a[244:247], a[160:163]\n\t"
      "s_bitcmp1_b32 s91, 2\n\t"
      "s_cselect_b32 s48, 24576, 0\n\t"
      "s_add_u32 s48, s48, s14\n\t"
      "v_mfma_f32_16x16x32_bf16 a[164:167], v[248:251], a[244:247], a[164:167]\n\t"
      "s_add_u32 s48, s48, 1572864\n\t"
      "buffer_load_dwordx4 v2, s[40:43], s48 offen lds\n\t"
      "s_mov_b32 s59, s91\n\t"
      "v_mfma_f32_16x16x32_bf16 a[168:171], v[252:255], a[244:247], a[168:171]\n\t"
      "ds_write_b64 v0, v[46:47] offset:96\n\t"
      "buffer_load_dwordx4 v3, s[40:43], s48 offen offset:1024 lds\n\t"
      "v_mfma_f32_16x16x32_bf16 a[172:175], a[148:151], a[244:247], a[172:175]\n\t"
      "ds_write_b64 v0, v[48:49] offset:128\n\t"
      "buffer_load_dwordx4 v4, s[40:43], s48 offen offset:2048 lds\n\t"
      "v_mfma_f32_16x16x32_bf16 a[176:179], a[152:155], a[244:247], a[176:179]\n\t"
      "ds_write_b64 v0, v[50:51] offset:160\n\t"
      "s_lshr_b32 s57, s91, 3\n\t"
      "s_bitcmp1_b32 s91, 3\n\t"
      "s_cselect_b32 s48, 24576, 0\n\t"
      "s_add_u32 s48, s48, s14\n\t"
      "s_add_u32 s48, s48, 2359296\n\t"
      "s_or_b32 s30, s17, s16\n\t"
      "s_cbranch_scc1 .Lno5_%=\n\t"
      "s_sub_u32 s68, s68, s64\n\t"
      "s_sub_u32 s70, s70, s66\n\t"
      "s_mul_i32 s64, s68, 100\n\t"
      "s_mul_i32 s66, s70, 1750\n\t"
      "s_sub_i32 s66, s64, s66\n\t"
      "s_ashr_i32 s66, s66, 9\n\t"
      "s_add_i32 s66, s66, 128\n\t"
      "s_max_i32 s66, s66, 1\n\t"
      "s_min_i32 s66, s66, 255\n\t"
      "s_lshl_b32 s66, s66, 17\n\t"
      "s_or_b32 s90, s90, s66\n\t"
      "v_mov_b32 v54, s90\n\t"
      "v_mov_b32 v55, s19\n\t"
      "global_store_dword v55, v54, s[12:13] sc1\n\t"
      ".Lno5_%=:\n\t"
      "s_waitcnt lgkmcnt(11)\n\t"
      "v_mfma_f32_16x16x32_bf16 v[16:19], v[64:67], a[0:3], 0\n\t"
      "ds_read_b128 a[24:27], v58 offset:256\n\t"
      "ds_read_b128 a[28:31], v59 offset:256\n\t"
      "s_waitcnt lgkmcnt(12)\n\t"
      "v_mfma_f32_16x16x32_bf16 v[16:19], v[80:83], a[4:7], v[16:19]\n\t"
      "ds_read_b128 a[32:35], v56 offset:512\n\t"
      "ds_read_b128 a[36:39], v57 offset:512\n\t"
      "s_waitcnt lgkmcnt(13)\n\t"
      "v_mfma_f32_16x16x32_bf16 v[16:19], v[96:99], a[8:11], v[16:19]\n\t"
      "ds_read_b128 a[40:43], v58 offset:512\n\t"
      "ds_read_b128 a[44:47], v59 offset:512\n\t"
      "s_waitcnt lgkmcnt(14)\n\t"
      "v_mfma_f32_16x16x32_bf16 v[16:19], v[112:115], a[12:15], v[16:19]\n\t"
      "v_accvgpr_read_b32 v52, a156\n\t"
      "v_accvgpr_read_b32 v53, a157\n\t"
      "s_waitcnt lgkmcnt(13)\n\t"
      "v_mfma_f32_16x16x32_bf16 v[16:19], v[128:131], a[16:19], v[16:19]\n\t"
      "v_accvgpr_read_b32 v54, a158\n\t"
      "v_accvgpr_read_b32 v55, a159\n\t"
      "s_waitcnt lgkmcnt(12)\n\t"
      "v_mfma_f32_16x16x32_bf16 v[16:19], v[144:147], a[20:23], v[16:19]\n\t"
      "v_cvt_pk_bf16_f32 v40, v52, v53\n\t"
      "v_cvt_pk_bf16_f32 v41, v54, v55\n\t"
      "v_mfma_f32_16x16x32_bf16 v[20:23], v[68:71], a[0:3], 0\n\t"
      "ds_write_b64 v0, v[40:41] offset:12544\n\t"
      "v_mfma_f32_16x16x32_bf16 v[20:23], v[84:87], a[4:7], v[20:23]\n\t"
      "s_waitcnt lgkmcnt(7)\n\t"
      "s_barrier\n\t"
      "v_mfma_f32_16x16x32_bf16 v[20:23], v[100:103], a[8:11], v[20:23]\n\t"
      "ds_read_b128 a[48:51], v8 offset:0\n\t"
      "ds_read_b128 a[52:55], v60 offset:1024\n\t"
      "ds_read_b128 a[56:59], v61 offset:2048\n\t"
      "v_mfma_f32_16x16x32_bf16 v[20:23], v[116:119], a[12:15], v[20:23]\n\t"
      "v_accvgpr_read_b32 v52, a160\n\t"
      "v_accvgpr_read_b32 v53, a161\n\t"
      "v_mfma_f32_16x16x32_bf16 v[20:23], v[132:135], a[16:19], v[20:23]\n\t"
      "v_accvgpr_read_b32 v54, a162\n\t"
      "v_accvgpr_read_b32 v55, a163\n\t"
      "v_mfma_f32_16x16x32_bf16 v[20:23], v[148:151], a[20:23], v[20:23]\n\t"
      "v_cvt_pk_bf16_f32 v42, v52, v53\n\t"
      "v_cvt_pk_bf16_f32 v43, v54, v55\n\t"
      "v_mfma_f32_16x16x32_bf16 v[24:27], v[72:75], a[0:3], 0\n\t"
      "ds_write_b64 v0, v[42:43] offset:12576\n\t"
      "s_waitcnt lgkmcnt(3)\n\t"
      "v_mfma_f32_16x16x32_bf16 v[24:27], v[88:91], a[4:7], v[24:27]\n\t"
      "buffer_store_dwordx4 a[48:51], v9, s[44:47], s49 offen sc1\n\t"
      "v_mfma_f32_16x16x32_bf16 v[24:27], v[104:107], a[8:11], v[24:27]\n\t"
      "v_accvgpr_read_b32 v52, a164\n\t"
      "v_accvgpr_read_b32 v53, a165\n\t"
      "v_mfma_f32_16x16x32_bf16 v[24:27], v[120:123], a[12:15], v[24:27]\n\t"
      "v_accvgpr_read_b32 v54, a166\n\t"
      "v_accvgpr_read_b32 v55, a167\n\t"
      "v_mfma_f32_16x16x32_bf16 v[24:27], v[136:139], a[16:19], v[24:27]\n\t"
      "v_cvt_pk_bf16_f32 v44, v52, v53\n\t"
      "v_cvt_pk_bf16_f32 v45, v54, v55\n\t"
      "v_mfma_f32_16x16x32_bf16 v[24:27], v[152:155], a[20:23], v[24:27]\n\t"
      "ds_write_b64 v0, v[44:45] offset:12608\n\t"
      "s_waitcnt lgkmcnt(3)\n\t"
      "v_mfma_f32_16x16x32_bf16 v[28:31], v[76:79], a[0:3], 0\n\t"
      "buffer_store_dwordx4 a[52:55], v10, s[44:47], s49 offen offset:1024 sc1\n\t"
      "v_mfma_f32_16x16x32_bf16 v[28:31], v[92:95], a[4:7], v[28:31]\n\t"
      "v_accvgpr_read_b32 v52, a168\n\t"
      "v_accvgpr_read_b32 v53, a169\n\t"
      "v_mfma_f32_16x16x32_bf16 v[28:31], v[108:111], a[8:11], v[28:31]\n\t"
      "v_accvgpr_read_b32 v54, a170\n\t"
      "v_accvgpr_read_b32 v55, a171\n\t"
      "v_mfma_f32_16x16x32_bf16 v[28:31], v[124:127], a[12:15], v[28:31]\n\t"
      "v_cvt_pk_bf16_f32 v46, v52, v53\n\t"
      "v_cvt_pk_bf16_f32 v47, v54, v55\n\t"
      "v_mfma_f32_16x16x32_bf16 v[28:31], v[140:143], a[16:19], v[28:31]\n\t"
      "ds_write_b64 v0, v[46:47] offset:12640\n\t"
      "s_waitcnt lgkmcnt(3)\n\t"
      "v_mfma_f32_16x16x32_bf16 v[28:31], v[156:159], a[20:23], v[28:31]\n\t"
      "buffer_store_dwordx4 a[56:59], v11, s[44:47], s49 offen offset:2048 sc1\n\t"
      "v_mfma_f32_16x16x32_bf16 v[32:35], a[60:63], a[0:3], 0\n\t"
      "v_accvgpr_read_b32 v52, a172\n\t"
      "v_accvgpr_read_b32 v53, a173\n\t"
      "v_mfma_f32_16x16x32_bf16 v[32:35], a[68:71], a[4:7], v[32:35]\n\t"
      "v_accvgpr_read_b32 v54, a174\n\t"
      "v_accvgpr_read_b32 v55, a175\n\t"
      "v_mfma_f32_16x16x32_bf16 v[32:35], a[76:79], a[8:11], v[32:35]\n\t"
      "v_cvt_pk_bf16_f32 v48, v52, v53\n\t"
      "v_cvt_pk_bf16_f32 v49, v54, v55\n\t"
      "v_mfma_f32_16x16x32_bf16 v[32:35], a[84:87], a[12:15], v[32:35]\n\t"
      "ds_write_b64 v0, v[48:49] offset:12672\n\t"
      "v_mfma_f32_16x16x32_bf16 v[32:35], a[92:95], a[16:19], v[32:35]\n\t"
      "v_accvgpr_read_b32 v52, a176\n\t"
      "v_accvgpr_read_b32 v53, a177\n\t"
      "v_mfma_f32_16x16x32_bf16 v[32:35], a[100:103], a[20:23], v[32:35]\n\t"
      "v_accvgpr_read_b32 v54, a178\n\t"
      "v_accvgpr_read_b32 v55, a179\n\t"
      "v_mfma_f32_16x16x32_bf16 v[36:39], a[64:67], a[0:3], 0\n\t"
      "v_cvt_pk_bf16_f32 v50, v52, v53\n\t"
      "v_cvt_pk_bf16_f32 v51, v54, v55\n\t"
      "v_mfma_f32_16x16x32_bf16 v[36:39], a[72:75], a[4:7], v[36:39]\n\t"
      "ds_write_b64 v0, v[50:51] offset:12704\n\t"
      "v_mfma_f32_16x16x32_bf16 v[36:39], a[80:83], a[8:11], v[36:39]\n\t"
      "v_add_u32 v52, s51, v15\n\t"
      "v_xor_b32 v53, 64, v52\n\t"
      "v_mfma_f32_16x16x32_bf16 v[36:39], a[88:91], a[12:15], v[36:39]\n\t"
      "v_xor_b32 v54, 128, v52\n\t"
      "v_xor_b32 v55, 192, v52\n\t"
      "v_mfma_f32_16x16x32_bf16 v[36:39], a[96:99], a[16:19], v[36:39]\n\t"
      "s_waitcnt lgkmcnt(0)\n\t"
      "s_add_u32 s38, s36, s39\n\t"
      "s_sub_u32 s28, s36, s28\n\t"
      "s_sub_i32 s28, s28, 515\n\t"
      "s_max_i32 s28, s28, 0\n\t"
      "s_min_i32 s28, s28, 60\n\t"
      "s_and_b32 s39, s32, 15\n\t"
      "s_cselect_b32 s53, s29, 0\n\t"
      "s_memrealtime s[36:37]\n\t"
      "v_mfma_f32_16x16x32_bf16 v[36:39], a[104:107], a[20:23], v[36:39]\n\t"
      "s_add_u32 s39, s39, s53\n\t"
      "s_lshr_b64 s[32:33], s[32:33], 4\n\t"
      "s_add_u32 m0, s18, 86528\n\t"
      "v_mfma_f32_16x16x32_bf16 v[16:19], v[160:163], a[24:27], v[16:19]\n\t"
      "s_bitcmp1_b32 s91, 2\n\t"
      "s_cselect_b32 s54, 24576, 0\n\t"
      "s_add_u32 s54, s54, s14\n\t"
      "s_add_u32 s54, s54, 1572864\n\t"
      "v_mfma_f32_16x16x32_bf16 v[16:19], v[176:179], a[28:31], v[16:19]\n\t"
      "s_waitcnt vmcnt(6)\n\t"
      "s_barrier\n\t"
      "v_mfma_f32_16x16x32_bf16 v[16:19], v[192:195], a[32:35], v[16:19]\n\t"
      "ds_read_b128 a[0:3], v52 offset:0\n\t"
      "ds_read_b128 a[188:191], v8 offset:12544\n\t"
      "buffer_load_dwordx4 v5, s[40:43], s54 offen lds\n\t"
      "v_mfma_f32_16x16x32_bf16 v[16:19], v[208:211], a[36:39], v[16:19]\n\t"
      "ds_read_b128 a[4:7], v53 offset:0\n\t"
      "ds_read_b128 a[192:195], v60 offset:13568\n\t"
      "v_mfma_f32_16x16x32_bf16 v[16:19], v[224:227], a[40:43], v[16:19]\n\t"
      "ds_read_b128 a[8:11], v54 offset:0\n\t"
      "ds_read_b128 a[196:199], v61 offset:14592\n\t"
      "buffer_load_dwordx4 v6, s[40:43], s54 offen offset:1024 lds\n\t"
      "v_mfma_f32_16x16x32_bf16 v[16:19], v[240:243], a[44:47], v[16:19]\n\t"
      "ds_read_b128 a[12:15], v55 offset:0\n\t"
      "v_mfma_f32_16x16x32_bf16 v[20:23], v[164:167], a[24:27], v[20:23]\n\t"
      "ds_read_b128 a[16:19], v52 offset:256\n\t"
      "buffer_load_dwordx4 v7, s[40:43], s54 offen offset:2048 lds\n\t"
      "v_mfma_f32_16x16x32_bf16 v[20:23], v[180:183], a[28:31], v[20:23]\n\t"
      "ds_read_b128 a[20:23], v53 offset:256\n\t"
      "s_add_u32 m0, s18, s50\n\t"
      "v_mfma_f32_16x16x32_bf16 v[20:23], v[196:199], a[32:35], v[20:23]\n\t"
      "s_add_u32 s51, s51, 12288\n\t"
      "s_cmp_eq_u32 s51, 160256\n\t"
      "s_cselect_b32 s51, 25088, s51\n\t"
      "s_waitcnt lgkmcnt(7)\n\t"
      "v_mfma_f32_16x16x32_bf16 v[20:23], v[212:215], a[36:39], v[20:23]\n\t"
      "buffer_store_dwordx4 a[188:191], v12, s[44:47], s49 offen sc1\n\t"
      "v_mfma_f32_16x16x32_bf16 v[20:23], v[228:231], a[40:43], v[20:23]\n\t"
      "v_mfma_f32_16x16x32_bf16 v[20:23], v[244:247], a[44:47], v[20:23]\n\t"
      "s_waitcnt lgkmcnt(0)\n\t"
      "s_sub_u32 s53, s36, s38\n\t"
      "s_cmp_lt_i32 s53, 0\n\t"
      "v_mfma_f32_16x16x32_bf16 v[24:27], v[168:171], a[24:27], v[24:27]\n\t"
      "s_cbranch_scc1 .Lspinf_%=\n\t"
      ".Lspunf_%=:\n\t"
      "s_sub_i32 s53, s53, 30\n\t"
      "s_max_i32 s53, s53, 0\n\t"
      "v_mfma_f32_16x16x32_bf16 v[24:27], v[184:187], a[28:31], v[24:27]\n\t"
      "s_add_u32 s38, s38, s53\n\t"
      "s_add_u32 s38, s38, s39\n\t"
      "s_add_u32 s28, s28, s53\n\t"
      "v_mfma_f32_16x16x32_bf16 v[24:27], v[200:203], a[32:35], v[24:27]\n\t"
      "v_cvt_pk_bf16_f32 v40, v16, v17\n\t"
      "v_cvt_pk_bf16_f32 v41, v18, v19\n\t"
      "v_mfma_f32_16x16x32_bf16 v[24:27], v[216:219], a[36:39], v[24:27]\n\t"
      "ds_write_b64 v0, v[40:41] offset:0\n\t"
      "v_mfma_f32_16x16x32_bf16 v[24:27], v[232:235], a[40:43], v[24:27]\n\t"
      "buffer_load_dwordx4 v2, s[40:43], s48 offen lds\n\t"
      "v_mfma_f32_16x16x32_bf16 v[24:27], v[248:251], a[44:47], v[24:27]\n\t"
      "buffer_store_dwordx4 a[192:195], v13, s[44:47], s49 offen offset:1024 sc1\n\t"
      "v_mfma_f32_16x16x32_bf16 v[28:31], v[172:175], a[24:27], v[28:31]\n\t"
      "v_mfma_f32_16x16x32_bf16 v[28:31], v[188:191], a[28:31], v[28:31]\n\t"
      "v_cvt_pk_bf16_f32 v42, v20, v21\n\t"
      "v_cvt_pk_bf16_f32 v43, v22, v23\n\t"
      "v_mfma_f32_16x16x32_bf16 v[28:31], v[204:207], a[32:35], v[28:31]\n\t"
      "ds_write_b64 v0, v[42:43] offset:32\n\t"
      "v_mfma_f32_16x16x32_bf16 v[28:31], v[220:223], a[36:39], v[28:31]\n\t"
      "buffer_load_dwordx4 v3, s[40:43], s48 offen offset:1024 lds\n\t"
      "v_mfma_f32_16x16x32_bf16 v[28:31], v[236:239], a[40:43], v[28:31]\n\t"
      "buffer_store_dwordx4 a[196:199], v14, s[44:47], s49 offen offset:2048 sc1\n\t"
      "v_mfma_f32_16x16x32_bf16 v[28:31], v[252:255], a[44:47], v[28:31]\n\t"
      "s_add_u32 s58, s58, 786432\n\t"
      "s_lshr_b32 s59, s59, 1\n\t"
      "v_mfma_f32_16x16x32_bf16 v[32:35], a[108:111], a[24:27], v[32:35]\n\t"
      "s_bitcmp1_b32 s59, 0\n\t"
      "s_cselect_b32 s49, 24576, 0\n\t"
      "s_add_u32 s49, s49, s58\n\t"
      "v_mfma_f32_16x16x32_bf16 v[32:35], a[116:119], a[28:31], v[32:35]\n\t"
      "v_cvt_pk_bf16_f32 v44, v24, v25\n\t"
      "v_cvt_pk_bf16_f32 v45, v26, v27\n\t"
      "v_mfma_f32_16x16x32_bf16 v[32:35], a[124:127], a[32:35], v[32:35]\n\t"
      "ds_write_b64 v0, v[44:45] offset:64\n\t"
      "v_mfma_f32_16x16x32_bf16 v[32:35], a[132:135], a[36:39], v[32:35]\n\t"
      "buffer_load_dwordx4 v4, s[40:43], s48 offen offset:2048 lds\n\t"
      "v_mfma_f32_16x16x32_bf16 v[32:35], a[140:143], a[40:43], v[32:35]\n\t"
      "s_add_u32 s50, s50, 12288\n\t"
      "v_mfma_f32_16x16x32_bf16 v[32:35], a[148:151], a[44:47], v[32:35]\n\t"
      "v_cvt_pk_bf16_f32 v46, v28, v29\n\t"
      "v_cvt_pk_bf16_f32 v47, v30, v31\n\t"
      "v_mfma_f32_16x16x32_bf16 v[36:39], a[112:115], a[24:27], v[36:39]\n\t"
      "ds_write_b64 v0, v[46:47] offset:96\n\t"
      "v_mfma_f32_16x16x32_bf16 v[36:39], a[120:123], a[28:31], v[36:39]\n\t"
      "s_cmp_eq_u32 s50, 160256\n\t"
      "s_cselect_b32 s50, 25088, s50\n\t"
      "v_mfma_f32_16x16x32_bf16 v[36:39], a[128:131], a[32:35], v[36:39]\n\t"
      "v_mfma_f32_16x16x32_bf16 v[36:39], a[136:139], a[36:39], v[36:39]\n\t"
      "v_mfma_f32_16x16x32_bf16 v[36:39], a[144:147], a[40:43], v[36:39]\n\t"
      "v_cvt_pk_bf16_f32 v48, v32, v33\n\t"
      "v_cvt_pk_bf16_f32 v49, v34, v35\n\t"
      "v_mfma_f32_16x16x32_bf16 v[36:39], a[152:155], a[44:47], v[36:39]\n\t"
      "ds_write_b64 v0, v[48:49] offset:128\n\t"
      "s_cmp_eq_u32 s17, 6144\n\t"
      "s_cbranch_scc1 .Lmid1_%=\n\t"
      "s_cmp_eq_u32 s17, 12288\n\t"
      "s_cbranch_scc1 .Lmid2_%=\n\t"
      "s_cmp_eq_u32 s17, 18432\n\t"
      "s_cbranch_scc1 .Lmid3_%=\n\t"
      "global_load_dword v63, v62, s[12:13] sc1\n\t"
      "s_branch .Lmid0_%=\n\t"
      ".Lloop0_%=:\n\t"
      "v_mfma_f32_16x16x32_bf16 v[16:19], v[64:67], a[0:3], 0\n\t"
      "ds_read_b128 a[24:27], v58 offset:256\n\t"
      "v_mfma_f32_16x16x32_bf16 v[16:19], v[80:83], a[4:7], v[16:19]\n\t"
      "ds_read_b128 a[28:31], v59 offset:256\n\t"
      "v_mfma_f32_16x16x32_bf16 v[16:19], v[96:99], a[8:11], v[16:19]\n\t"
      "ds_read_b128 a[32:35], v56 offset:512\n\t"
      "v_mfma_f32_16x16x32_bf16 v[16:19], v[112:115], a[12:15], v[16:19]\n\t"
      "ds_read_b128 a[36:39], v57 offset:512\n\t"
      "v_mfma_f32_16x16x32_bf16 v[16:19], v[128:131], a[16:19], v[16:19]\n\t"
      "ds_read_b128 a[40:43], v58 offset:512\n\t"
      "v_mfma_f32_16x16x32_bf16 v[16:19], v[144:147], a[20:23], v[16:19]\n\t"
      "ds_read_b128 a[44:47], v59 offset:512\n\t"
      "v_mfma_f32_16x16x32_bf16 v[20:23], v[68:71], a[0:3], 0\n\t"
      "v_cvt_pk_bf16_f32 v50, v36, v37\n\t"
      "v_cvt_pk_bf16_f32 v51, v38, v39\n\t"
      "v_mfma_f32_16x16x32_bf16 v[20:23], v[84:87], a[4:7], v[20:23]\n\t"
      "ds_write_b64 v0, v[50:51] offset:12704\n\t"
      "v_mfma_f32_16x16x32_bf16 v[20:23], v[100:103], a[8:11], v[20:23]\n\t"
      "v_add_u32 v52, s51, v15\n\t"
      "v_xor_b32 v53, 64, v52\n\t"
      "v_mfma_f32_16x16x32_bf16 v[20:23], v[116:119], a[12:15], v[20:23]\n\t"
      "v_xor_b32 v54, 128, v52\n\t"
      "v_xor_b32 v55, 192, v52\n\t"
      "v_mfma_f32_16x16x32_bf16 v[20:23], v[132:135], a[16:19], v[20:23]\n\t"
      "s_add_u32 s56, s56, 786432\n\t"
      "s_lshr_b32 s57, s57, 1\n\t"
      "v_mfma_f32_16x16x32_bf16 v[20:23], v[148:151], a[20:23], v[20:23]\n\t"
      "s_bitcmp1_b32 s57, 0\n\t"
      "s_cselect_b32 s48, 24576, 0\n\t"
      "s_add_u32 s48, s48, s56\n\t"
      "v_mfma_f32_16x16x32_bf16 v[24:27], v[72:75], a[0:3], 0\n\t"
      "v_mfma_f32_16x16x32_bf16 v[24:27], v[88:91], a[4:7], v[24:27]\n\t"
      "v_mfma_f32_16x16x32_bf16 v[24:27], v[104:107], a[8:11], v[24:27]\n\t"
      "s_waitcnt vmcnt(12)\n\t"
      "s_waitcnt lgkmcnt(0)\n\t"
      "s_barrier\n\t"
      "v_mfma_f32_16x16x32_bf16 v[24:27], v[120:123], a[12:15], v[24:27]\n\t"
      "v_mfma_f32_16x16x32_bf16 v[24:27], v[136:139], a[16:19], v[24:27]\n\t"
      "v_mfma_f32_16x16x32_bf16 v[24:27], v[152:155], a[20:23], v[24:27]\n\t"
      "v_mfma_f32_16x16x32_bf16 v[28:31], v[76:79], a[0:3], 0\n\t"
      "ds_read_b128 a[48:51], v8 offset:12544\n\t"
      "v_mfma_f32_16x16x32_bf16 v[28:31], v[92:95], a[4:7], v[28:31]\n\t"
      "ds_read_b128 a[52:55], v60 offset:13568\n\t"
      "v_mfma_f32_16x16x32_bf16 v[28:31], v[108:111], a[8:11], v[28:31]\n\t"
      "ds_read_b128 a[56:59], v61 offset:14592\n\t"
      "v_mfma_f32_16x16x32_bf16 v[28:31], v[124:127], a[12:15], v[28:31]\n\t"
      "s_and_b32 s39, s32, 15\n\t"
      "s_cselect_b32 s53, s29, 0\n\t"
      "v_mfma_f32_16x16x32_bf16 v[28:31], v[140:143], a[16:19], v[28:31]\n\t"
      "s_add_u32 s39, s39, s53\n\t"
      "s_lshr_b64 s[32:33], s[32:33], 4\n\t"
      "v_mfma_f32_16x16x32_bf16 v[28:31], v[156:159], a[20:23], v[28:31]\n\t"
      "s_cmp_le_u32 s52, 12\n\t"
      "s_cselect_b32 s53, 3, 0\n\t"
      "s_min_u32 s53, s28, s53\n\t"
      "v_mfma_f32_16x16x32_bf16 v[32:35], a[60:63], a[0:3], 0\n\t"
      "s_sub_u32 s28, s28, s53\n\t"
      "s_sub_u32 s39, s39, s53\n\t"
      "v_mfma_f32_16x16x32_bf16 v[32:35], a[68:71], a[4:7], v[32:35]\n\t"
      "s_waitcnt lgkmcnt(2)\n\t"
      "v_mfma_f32_16x16x32_bf16 v[32:35], a[76:79], a[8:11], v[32:35]\n\t"
      "buffer_store_dwordx4 a[48:51], v12, s[44:47], s49 offen sc1\n\t"
      "v_mfma_f32_16x16x32_bf16 v[32:35], a[84:87], a[12:15], v[32:35]\n\t"
      "v_mfma_f32_16x16x32_bf16 v[32:35], a[92:95], a[16:19], v[32:35]\n\t"
      "v_mfma_f32_16x16x32_bf16 v[32:35], a[100:103], a[20:23], v[32:35]\n\t"
      "s_add_u32 m0, s18, s50\n\t"
      "v_mfma_f32_16x16x32_bf16 v[36:39], a[64:67], a[0:3], 0\n\t"
      "buffer_load_dwordx4 v2, s[40:43], s48 offen nt lds\n\t"
      "v_mfma_f32_16x16x32_bf16 v[36:39], a[72:75], a[4:7], v[36:39]\n\t"
      "s_memrealtime s[36:37]\n\t"
      "v_mfma_f32_16x16x32_bf16 v[36:39], a[80:83], a[8:11], v[36:39]\n\t"
      "v_mfma_f32_16x16x32_bf16 v[36:39], a[88:91], a[12:15], v[36:39]\n\t"
      "s_waitcnt lgkmcnt(1)\n\t"
      "v_mfma_f32_16x16x32_bf16 v[36:39], a[96:99], a[16:19], v[36:39]\n\t"
      "buffer_store_dwordx4 a[52:55], v13, s[44:47], s49 offen offset:1024 sc1\n\t"
      "v_mfma_f32_16x16x32_bf16 v[36:39], a[104:107], a[20:23], v[36:39]\n\t"
      "v_mfma_f32_16x16x32_bf16 v[16:19], v[160:163], a[24:27], v[16:19]\n\t"
      "ds_read_b128 a[0:3], v52 offset:0\n\t"
      "v_mfma_f32_16x16x32_bf16 v[16:19], v[176:179], a[28:31], v[16:19]\n\t"
      "ds_read_b128 a[4:7], v53 offset:0\n\t"
      "v_mfma_f32_16x16x32_bf16 v[16:19], v[192:195], a[32:35], v[16:19]\n\t"
      "ds_read_b128 a[8:11], v54 offset:0\n\t"
      "v_mfma_f32_16x16x32_bf16 v[16:19], v[208:211], a[36:39], v[16:19]\n\t"
      "ds_read_b128 a[12:15], v55 offset:0\n\t"
      "v_mfma_f32_16x16x32_bf16 v[16:19], v[224:227], a[40:43], v[16:19]\n\t"
      "ds_read_b128 a[16:19], v52 offset:256\n\t"
      "v_mfma_f32_16x16x32_bf16 v[16:19], v[240:243], a[44:47], v[16:19]\n\t"
      "ds_read_b128 a[20:23], v53 offset:256\n\t"
      "v_mfma_f32_16x16x32_bf16 v[20:23], v[164:167], a[24:27], v[20:23]\n\t"
      "s_add_u32 s51, s51, 12288\n\t"
      "s_cmp_eq_u32 s51, 160256\n\t"
      "s_cselect_b32 s51, 25088, s51\n\t"
      "v_mfma_f32_16x16x32_bf16 v[20:23], v[180:183], a[28:31], v[20:23]\n\t"
      "s_waitcnt lgkmcnt(6)\n\t"
      "v_mfma_f32_16x16x32_bf16 v[20:23], v[196:199], a[32:35], v[20:23]\n\t"
      "buffer_store_dwordx4 a[56:59], v14, s[44:47], s49 offen offset:2048 sc1\n\t"
      "v_mfma_f32_16x16x32_bf16 v[20:23], v[212:215], a[36:39], v[20:23]\n\t"
      "v_mfma_f32_16x16x32_bf16 v[20:23], v[228:231], a[40:43], v[20:23]\n\t"
      "s_waitcnt lgkmcnt(0)\n\t"
      "s_sub_u32 s53, s36, s38\n\t"
      "s_cmp_lt_i32 s53, 0\n\t"
      "v_mfma_f32_16x16x32_bf16 v[20:23], v[244:247], a[44:47], v[20:23]\n\t"
      "s_cbranch_scc1 .Lspin0_%=\n\t"
      ".Lspun0_%=:\n\t"
      "s_sub_i32 s53, s53, 30\n\t"
      "s_max_i32 s53, s53, 0\n\t"
      "v_mfma_f32_16x16x32_bf16 v[24:27], v[168:171], a[24:27], v[24:27]\n\t"
      "s_add_u32 s38, s38, s53\n\t"
      "s_add_u32 s38, s38, s39\n\t"
      "s_add_u32 s28, s28, s53\n\t"
      "v_mfma_f32_16x16x32_bf16 v[24:27], v[184:187], a[28:31], v[24:27]\n\t"
      "v_cvt_pk_bf16_f32 v40, v16, v17\n\t"
      "v_cvt_pk_bf16_f32 v41, v18, v19\n\t"
      "v_mfma_f32_16x16x32_bf16 v[24:27], v[200:203], a[32:35], v[24:27]\n\t"
      "ds_write_b64 v0, v[40:41] offset:0\n\t"
      "v_mfma_f32_16x16x32_bf16 v[24:27], v[216:219], a[36:39], v[24:27]\n\t"
      "buffer_load_dwordx4 v3, s[40:43], s48 offen offset:1024 nt lds\n\t"
      "v_mfma_f32_16x16x32_bf16 v[24:27], v[232:235], a[40:43], v[24:27]\n\t"
      "s_add_u32 s58, s58, 786432\n\t"
      "s_lshr_b32 s59, s59, 1\n\t"
      "v_mfma_f32_16x16x32_bf16 v[24:27], v[248:251], a[44:47], v[24:27]\n\t"
      "s_bitcmp1_b32 s59, 0\n\t"
      "s_cselect_b32 s49, 24576, 0\n\t"
      "s_add_u32 s49, s49, s58\n\t"
      "v_mfma_f32_16x16x32_bf16 v[28:31], v[172:175], a[24:27], v[28:31]\n\t"
      "v_mfma_f32_16x16x32_bf16 v[28:31], v[188:191], a[28:31], v[28:31]\n\t"
      "v_cvt_pk_bf16_f32 v42, v20, v21\n\t"
      "v_cvt_pk_bf16_f32 v43, v22, v23\n\t"
      "v_mfma_f32_16x16x32_bf16 v[28:31], v[204:207], a[32:35], v[28:31]\n\t"
      "ds_write_b64 v0, v[42:43] offset:32\n\t"
      "v_mfma_f32_16x16x32_bf16 v[28:31], v[220:223], a[36:39], v[28:31]\n\t"
      "v_mfma_f32_16x16x32_bf16 v[28:31], v[236:239], a[40:43], v[28:31]\n\t"
      "v_mfma_f32_16x16x32_bf16 v[28:31], v[252:255], a[44:47], v[28:31]\n\t"
      "s_cmp_eq_u32 s52, 14\n\t"
      "s_cbranch_scc1 .Lshb_%=\n\t"
      ".Lshbr_%=:\n\t"
      "v_mfma_f32_16x16x32_bf16 v[32:35], a[108:111], a[24:27], v[32:35]\n\t"
      "v_mfma_f32_16x16x32_bf16 v[32:35], a[116:119], a[28:31], v[32:35]\n\t"
      "v_cvt_pk_bf16_f32 v44, v24, v25\n\t"
      "v_cvt_pk_bf16_f32 v45, v26, v27\n\t"
      "v_mfma_f32_16x16x32_bf16 v[32:35], a[124:127], a[32:35], v[32:35]\n\t"
      "ds_write_b64 v0, v[44:45] offset:64\n\t"
      "v_mfma_f32_16x16x32_bf16 v[32:35], a[132:135], a[36:39], v[32:35]\n\t"
      "buffer_load_dwordx4 v4, s[40:43], s48 offen offset:2048 nt lds\n\t"
      "v_mfma_f32_16x16x32_bf16 v[32:35], a[140:143], a[40:43], v[32:35]\n\t"
      "s_add_u32 s50, s50, 12288\n\t"
      "v_mfma_f32_16x16x32_bf16 v[32:35], a[148:151], a[44:47], v[32:35]\n\t"
      "v_cvt_pk_bf16_f32 v46, v28, v29\n\t"
      "v_cvt_pk_bf16_f32 v47, v30, v31\n\t"
      "v_mfma_f32_16x16x32_bf16 v[36:39], a[112:115], a[24:27], v[36:39]\n\t"
      "ds_write_b64 v0, v[46:47] offset:96\n\t"
      "v_mfma_f32_16x16x32_bf16 v[36:39], a[120:123], a[28:31], v[36:39]\n\t"
      "s_cmp_eq_u32 s50, 160256\n\t"
      "s_cselect_b32 s50, 25088, s50\n\t"
      "v_mfma_f32_16x16x32_bf16 v[36:39], a[128:131], a[32:35], v[36:39]\n\t"
      "v_mfma_f32_16x16x32_bf16 v[36:39], a[136:139], a[36:39], v[36:39]\n\t"
      "v_mfma_f32_16x16x32_bf16 v[36:39], a[144:147], a[40:43], v[36:39]\n\t"
      "v_cvt_pk_bf16_f32 v48, v32, v33\n\t"
      "v_cvt_pk_bf16_f32 v49, v34, v35\n\t"
      "v_mfma_f32_16x16x32_bf16 v[36:39], a[152:155], a[44:47], v[36:39]\n\t"
      "ds_write_b64 v0, v[48:49] offset:128\n\t"
      ".Lmid0_%=:\n\t"
      "v_mfma_f32_16x16x32_bf16 v[16:19], v[64:67], a[0:3], 0\n\t"
      "ds_read_b128 a[24:27], v54 offset:256\n\t"
      "v_mfma_f32_16x16x32_bf16 v[16:19], v[80:83], a[4:7], v[16:19]\n\t"
      "ds_read_b128 a[28:31], v55 offset:256\n\t"
      "v_mfma_f32_16x16x32_bf16 v[16:19], v[96:99], a[8:11], v[16:19]\n\t"
      "ds_read_b128 a[32:35], v52 offset:512\n\t"
      "v_mfma_f32_16x16x32_bf16 v[16:19], v[112:115], a[12:15], v[16:19]\n\t"
      "ds_read_b128 a[36:39], v53 offset:512\n\t"
      "v_mfma_f32_16x16x32_bf16 v[16:19], v[128:131], a[16:19], v[16:19]\n\t"
      "ds_read_b128 a[40:43], v54 offset:512\n\t"
      "v_mfma_f32_16x16x32_bf16 v[16:19], v[144:147], a[20:23], v[16:19]\n\t"
      "ds_read_b128 a[44:47], v55 offset:512\n\t"
      "v_mfma_f32_16x16x32_bf16 v[20:23], v[68:71], a[0:3], 0\n\t"
      "v_cvt_pk_bf16_f32 v50, v36, v37\n\t"
      "v_cvt_pk_bf16_f32 v51, v38, v39\n\t"
      "v_mfma_f32_16x16x32_bf16 v[20:23], v[84:87], a[4:7], v[20:23]\n\t"
      "ds_write_b64 v0, v[50:51] offset:160\n\t"
      "v_mfma_f32_16x16x32_bf16 v[20:23], v[100:103], a[8:11], v[20:23]\n\t"
      "v_add_u32 v56, s51, v15\n\t"
      "v_xor_b32 v57, 64, v56\n\t"
      "v_mfma_f32_16x16x32_bf16 v[20:23], v[116:119], a[12:15], v[20:23]\n\t"
      "v_xor_b32 v58, 128, v56\n\t"
      "v_xor_b32 v59, 192, v56\n\t"
      "v_mfma_f32_16x16x32_bf16 v[20:23], v[132:135], a[16:19], v[20:23]\n\t"
      "v_mfma_f32_16x16x32_bf16 v[20:23], v[148:151], a[20:23], v[20:23]\n\t"
      "v_mfma_f32_16x16x32_bf16 v[24:27], v[72:75], a[0:3], 0\n\t"
      "v_mfma_f32_16x16x32_bf16 v[24:27], v[88:91], a[4:7], v[24:27]\n\t"
      "v_mfma_f32_16x16x32_bf16 v[24:27], v[104:107], a[8:11], v[24:27]\n\t"
      "s_waitcnt vmcnt(12)\n\t"
      "s_waitcnt lgkmcnt(0)\n\t"
      "s_barrier\n\t"
      "v_mfma_f32_16x16x32_bf16 v[24:27], v[120:123], a[12:15], v[24:27]\n\t"
      "v_mfma_f32_16x16x32_bf16 v[24:27], v[136:139], a[16:19], v[24:27]\n\t"
      "v_mfma_f32_16x16x32_bf16 v[24:27], v[152:155], a[20:23], v[24:27]\n\t"
      "v_mfma_f32_16x16x32_bf16 v[28:31], v[76:79], a[0:3], 0\n\t"
      "ds_read_b128 a[48:51], v8 offset:0\n\t"
      "v_mfma_f32_16x16x32_bf16 v[28:31], v[92:95], a[4:7], v[28:31]\n\t"
      "ds_read_b128 a[52:55], v60 offset:1024\n\t"
      "v_mfma_f32_16x16x32_bf16 v[28:31], v[108:111], a[8:11], v[28:31]\n\t"
      "ds_read_b128 a[56:59], v61 offset:2048\n\t"
      "v_mfma_f32_16x16x32_bf16 v[28:31], v[124:127], a[12:15], v[28:31]\n\t"
      "s_and_b32 s39, s34, 15\n\t"
      "s_cselect_b32 s53, s29, 0\n\t"
      "v_mfma_f32_16x16x32_bf16 v[28:31], v[140:143], a[16:19], v[28:31]\n\t"
      "s_add_u32 s39, s39, s53\n\t"
      "s_lshr_b64 s[34:35], s[34:35], 4\n\t"
      "v_mfma_f32_16x16x32_bf16 v[28:31], v[156:159], a[20:23], v[28:31]\n\t"
      "s_cmp_le_u32 s52, 12\n\t"
      "s_cselect_b32 s53, 3, 0\n\t"
      "s_min_u32 s53, s28, s53\n\t"
      "v_mfma_f32_16x16x32_bf16 v[32:35], a[60:63], a[0:3], 0\n\t"
      "s_sub_u32 s28, s28, s53\n\t"
      "s_sub_u32 s39, s39, s53\n\t"
      "v_mfma_f32_16x16x32_bf16 v[32:35], a[68:71], a[4:7], v[32:35]\n\t"
      "s_waitcnt lgkmcnt(2)\n\t"
      "v_mfma_f32_16x16x32_bf16 v[32:35], a[76:79], a[8:11], v[32:35]\n\t"
      "buffer_store_dwordx4 a[48:51], v9, s[44:47], s49 offen sc1\n\t"
      "v_mfma_f32_16x16x32_bf16 v[32:35], a[84:87], a[12:15], v[32:35]\n\t"
      "v_mfma_f32_16x16x32_bf16 v[32:35], a[92:95], a[16:19], v[32:35]\n\t"
      "v_mfma_f32_16x16x32_bf16 v[32:35], a[100:103], a[20:23], v[32:35]\n\t"
      "s_add_u32 m0, s18, s50\n\t"
      "v_mfma_f32_16x16x32_bf16 v[36:39], a[64:67], a[0:3], 0\n\t"
      "buffer_load_dwordx4 v5, s[40:43], s48 offen nt lds\n\t"
      "v_mfma_f32_16x16x32_bf16 v[36:39], a[72:75], a[4:7], v[36:39]\n\t"
      "s_memrealtime s[36:37]\n\t"
      "v_mfma_f32_16x16x32_bf16 v[36:39], a[80:83], a[8:11], v[36:39]\n\t"
      "v_mfma_f32_16x16x32_bf16 v[36:39], a[88:91], a[12:15], v[36:39]\n\t"
      "s_waitcnt lgkmcnt(1)\n\t"
      "v_mfma_f32_16x16x32_bf16 v[36:39], a[96:99], a[16:19], v[36:39]\n\t"
      "buffer_store_dwordx4 a[52:55], v10, s[44:47], s49 offen offset:1024 sc1\n\t"
      "v_mfma_f32_16x16x32_bf16 v[36:39], a[104:107], a[20:23], v[36:39]\n\t"
      "v_mfma_f32_16x16x32_bf16 v[16:19], v[160:163], a[24:27], v[16:19]\n\t"
      "ds_read_b128 a[0:3], v56 offset:0\n\t"
      "v_mfma_f32_16x16x32_bf16 v[16:19], v[176:179], a[28:31], v[16:19]\n\t"
      "ds_read_b128 a[4:7], v57 offset:0\n\t"
      "v_mfma_f32_16x16x32_bf16 v[16:19], v[192:195], a[32:35], v[16:19]\n\t"
      "ds_read_b128 a[8:11], v58 offset:0\n\t"
      "v_mfma_f32_16x16x32_bf16 v[16:19], v[208:211], a[36:39], v[16:19]\n\t"
      "ds_read_b128 a[12:15], v59 offset:0\n\t"
      "v_mfma_f32_16x16x32_bf16 v[16:19], v[224:227], a[40:43], v[16:19]\n\t"
      "ds_read_b128 a[16:19], v56 offset:256\n\t"
      "v_mfma_f32_16x16x32_bf16 v[16:19], v[240:243], a[44:47], v[16:19]\n\t"
      "ds_read_b128 a[20:23], v57 offset:256\n\t"
      "v_mfma_f32_16x16x32_bf16 v[20:23], v[164:167], a[24:27], v[20:23]\n\t"
      "s_add_u32 s51, s51, 12288\n\t"
      "s_cmp_eq_u32 s51, 160256\n\t"
      "s_cselect_b32 s51, 25088, s51\n\t"
      "v_mfma_f32_16x16x32_bf16 v[20:23], v[180:183], a[28:31], v[20:23]\n\t"
      "s_waitcnt lgkmcnt(6)\n\t"
      "v_mfma_f32_16x16x32_bf16 v[20:23], v[196:199], a[32:35], v[20:23]\n\t"
      "buffer_store_dwordx4 a[56:59], v11, s[44:47], s49 offen offset:2048 sc1\n\t"
      "v_mfma_f32_16x16x32_bf16 v[20:23], v[212:215], a[36:39], v[20:23]\n\t"
      "v_mfma_f32_16x16x32_bf16 v[20:23], v[228:231], a[40:43], v[20:23]\n\t"
      "s_waitcnt lgkmcnt(0)\n\t"
      "s_sub_u32 s53, s36, s38\n\t"
      "s_cmp_lt_i32 s53, 0\n\t"
      "v_mfma_f32_16x16x32_bf16 v[20:23], v[244:247], a[44:47], v[20:23]\n\t"
      "s_cbranch_scc1 .Lspin1_%=\n\t"
      ".Lspun1_%=:\n\t"
      "s_sub_i32 s53, s53, 30\n\t"
      "s_max_i32 s53, s53, 0\n\t"
      "v_mfma_f32_16x16x32_bf16 v[24:27], v[168:171], a[24:27], v[24:27]\n\t"
      "s_add_u32 s38, s38, s53\n\t"
      "s_add_u32 s38, s38, s39\n\t"
      "s_add_u32 s28, s28, s53\n\t"
      "v_mfma_f32_16x16x32_bf16 v[24:27], v[184:187], a[28:31], v[24:27]\n\t"
      "v_cvt_pk_bf16_f32 v40, v16, v17\n\t"
      "v_cvt_pk_bf16_f32 v41, v18, v19\n\t"
      "v_mfma_f32_16x16x32_bf16 v[24:27], v[200:203], a[32:35], v[24:27]\n\t"
      "ds_write_b64 v0, v[40:41] offset:12544\n\t"
      "v_mfma_f32_16x16x32_bf16 v[24:27], v[216:219], a[36:39], v[24:27]\n\t"
      "buffer_load_dwordx4 v6, s[40:43], s48 offen offset:1024 nt lds\n\t"
      "v_mfma_f32_16x16x32_bf16 v[24:27], v[232:235], a[40:43], v[24:27]\n\t"
      "v_mfma_f32_16x16x32_bf16 v[24:27], v[248:251], a[44:47], v[24:27]\n\t"
      "v_mfma_f32_16x16x32_bf16 v[28:31], v[172:175], a[24:27], v[28:31]\n\t"
      "v_mfma_f32_16x16x32_bf16 v[28:31], v[188:191], a[28:31], v[28:31]\n\t"
      "v_cvt_pk_bf16_f32 v42, v20, v21\n\t"
      "v_cvt_pk_bf16_f32 v43, v22, v23\n\t"
      "v_mfma_f32_16x16x32_bf16 v[28:31], v[204:207], a[32:35], v[28:31]\n\t"
      "ds_write_b64 v0, v[42:43] offset:12576\n\t"
      "v_mfma_f32_16x16x32_bf16 v[28:31], v[220:223], a[36:39], v[28:31]\n\t"
      "v_mfma_f32_16x16x32_bf16 v[28:31], v[236:239], a[40:43], v[28:31]\n\t"
      "v_mfma_f32_16x16x32_bf16 v[28:31], v[252:255], a[44:47], v[28:31]\n\t"
      "v_mfma_f32_16x16x32_bf16 v[32:35], a[108:111], a[24:27], v[32:35]\n\t"
      "v_mfma_f32_16x16x32_bf16 v[32:35], a[116:119], a[28:31], v[32:35]\n\t"
      "v_cvt_pk_bf16_f32 v44, v24, v25\n\t"
      "v_cvt_pk_bf16_f32 v45, v26, v27\n\t"
      "v_mfma_f32_16x16x32_bf16 v[32:35], a[124:127], a[32:35], v[32:35]\n\t"
      "ds_write_b64 v0, v[44:45] offset:12608\n\t"
      "v_mfma_f32_16x16x32_bf16 v[32:35], a[132:135], a[36:39], v[32:35]\n\t"
      "buffer_load_dwordx4 v7, s[40:43], s48 offen offset:2048 nt lds\n\t"
      "v_mfma_f32_16x16x32_bf16 v[32:35], a[140:143], a[40:43], v[32:35]\n\t"
      "s_add_u32 s50, s50, 12288\n\t"
      "v_mfma_f32_16x16x32_bf16 v[32:35], a[148:151], a[44:47], v[32:35]\n\t"
      "v_cvt_pk_bf16_f32 v46, v28, v29\n\t"
      "v_cvt_pk_bf16_f32 v47, v30, v31\n\t"
      "v_mfma_f32_16x16x32_bf16 v[36:39], a[112:115], a[24:27], v[36:39]\n\t"
      "ds_write_b64 v0, v[46:47] offset:12640\n\t"
      "v_mfma_f32_16x16x32_bf16 v[36:39], a[120:123], a[28:31], v[36:39]\n\t"
      "s_cmp_eq_u32 s50, 160256\n\t"
      "s_cselect_b32 s50, 25088, s50\n\t"
      "v_mfma_f32_16x16x32_bf16 v[36:39], a[128:131], a[32:35], v[36:39]\n\t"
      "s_sub_u32 s52, s52, 1\n\t"
      "s_cmp_lg_u32 s52, 0\n\t"
      "v_mfma_f32_16x16x32_bf16 v[36:39], a[136:139], a[36:39], v[36:39]\n\t"
      "v_mfma_f32_16x16x32_bf16 v[36:39], a[144:147], a[40:43], v[36:39]\n\t"
      "v_cvt_pk_bf16_f32 v48, v32, v33\n\t"
      "v_cvt_pk_bf16_f32 v49, v34, v35\n\t"
      "v_mfma_f32_16x16x32_bf16 v[36:39], a[152:155], a[44:47], v[36:39]\n\t"
      "ds_write_b64 v0, v[48:49] offset:12672\n\t"
      "s_cbranch_scc1 .Lloop0_%=\n\t"
      "s_branch .Lendc_%=\n\t"
      ".Lshb_%=:\n\t"
      "s_waitcnt vmcnt(10)\n\t"
      "v_bfe_u32 v63, v63, 17, 8\n\t"
      "v_min_u32 v62, 1, v63\n\t"
      "v_lshl_add_u32 v63, v62, 16, v63\n\t"
      "s_nop 1\n\t"
      "v_add_u32_dpp v63, v63, v63 row_shr:1 row_mask:0xf bank_mask:0xf bound_ctrl:0\n\t"
      "s_nop 1\n\t"
      "v_add_u32_dpp v63, v63, v63 row_shr:2 row_mask:0xf bank_mask:0xf bound_ctrl:0\n\t"
      "s_nop 1\n\t"
      "v_add_u32_dpp v63, v63, v63 row_shr:4 row_mask:0xf bank_mask:0xf bound_ctrl:0\n\t"
      "s_nop 1\n\t"
      "v_add_u32_dpp v63, v63, v63 row_shr:8 row_mask:0xf bank_mask:0xf bound_ctrl:0\n\t"
      "s_nop 0\n\t"
      "v_readlane_b32 s64, v63, 15\n\t"
      "v_readlane_b32 s65, v63, 31\n\t"
      "s_nop 1\n\t"
      "s_add_u32 s64, s64, s65\n\t"
      "s_lshr_b32 s65, s64, 16\n\t"
      "s_and_b32 s64, s64, 0xffff\n\t"
      "s_mov_b32 s66, 0\n\t"
      "s_mul_i32 s67, s65, 934\n\t"
      "s_lshl_b32 s69, s64, 3\n\t"
      "s_cmp_lt_u32 s69, s67\n\t"
      "s_cselect_b32 s66, 3, s66\n\t"
      "s_add_u32 s29, s29, s66\n\t"
      "s_mul_i32 s67, s65, 737\n\t"
      "s_cmp_lt_u32 s69, s67\n\t"
      "s_cbranch_scc0 .Lshbr_%=\n\t"
      "s_movk_i32 s29, 79\n\t"
      "s_mov_b32 s32, 0x11111111\n\t"
      "s_mov_b32 s33, 0x11111111\n\t"
      "s_mov_b32 s34, 0x11111111\n\t"
      "s_mov_b32 s35, 0x11111111\n\t"
      "s_sub_i32 s28, s28, 30\n\t"
      "s_min_i32 s66, s28, 0\n\t"
      "s_sub_u32 s38, s38, s66\n\t"
      "s_max_i32 s28, s28, 0\n\t"
      "s_branch .Lshbr_%=\n\t"
      ".Lloop1_%=:\n\t"
      "v_mfma_f32_16x16x32_bf16 v[16:19], v[64:67], a[0:3], 0\n\t"
      "ds_read_b128 a[24:27], v58 offset:256\n\t"
      "v_mfma_f32_16x16x32_bf16 v[16:19], v[80:83], a[4:7], v[16:19]\n\t"
      "ds_read_b128 a[28:31], v59 offset:256\n\t"
      "v_mfma_f32_16x16x32_bf16 v[16:19], v[96:99], a[8:11], v[16:19]\n\t"
      "ds_read_b128 a[32:35], v56 offset:512\n\t"
      "v_mfma_f32_16x16x32_bf16 v[16:19], v[112:115], a[12:15], v[16:19]\n\t"
      "ds_read_b128 a[36:39], v57 offset:512\n\t"
      "v_mfma_f32_16x16x32_bf16 v[16:19], v[128:131], a[16:19], v[16:19]\n\t"
      "ds_read_b128 a[40:43], v58 offset:512\n\t"
      "v_mfma_f32_16x16x32_bf16 v[16:19], v[144:147], a[20:23], v[16:19]\n\t"
      "ds_read_b128 a[44:47], v59 offset:512\n\t"
      "v_mfma_f32_16x16x32_bf16 v[20:23], v[68:71], a[0:3], 0\n\t"
      "v_cvt_pk_bf16_f32 v50, v36, v37\n\t"
      "v_cvt_pk_bf16_f32 v51, v38, v39\n\t"
      "v_mfma_f32_16x16x32_bf16 v[20:23], v[84:87], a[4:7], v[20:23]\n\t"
      "ds_write_b64 v0, v[50:51] offset:12704\n\t"
      "v_mfma_f32_16x16x32_bf16 v[20:23], v[100:103], a[8:11], v[20:23]\n\t"
      "v_add_u32 v52, s51, v15\n\t"
      "v_xor_b32 v53, 64, v52\n\t"
      "v_mfma_f32_16x16x32_bf16 v[20:23], v[116:119], a[12:15], v[20:23]\n\t"
      "v_xor_b32 v54, 128, v52\n\t"
      "v_xor_b32 v55, 192, v52\n\t"
      "v_mfma_f32_16x16x32_bf16 v[20:23], v[132:135], a[16:19], v[20:23]\n\t"
      "s_add_u32 s56, s56, 786432\n\t"
      "s_lshr_b32 s57, s57, 1\n\t"
      "v_mfma_f32_16x16x32_bf16 v[20:23], v[148:151], a[20:23], v[20:23]\n\t"
      "s_bitcmp1_b32 s57, 0\n\t"
      "s_cselect_b32 s48, 24576, 0\n\t"
      "s_add_u32 s48, s48, s56\n\t"
      "v_mfma_f32_16x16x32_bf16 v[24:27], v[72:75], a[0:3], 0\n\t"
      "v_mfma_f32_16x16x32_bf16 v[24:27], v[88:91], a[4:7], v[24:27]\n\t"
      "v_mfma_f32_16x16x32_bf16 v[24:27], v[104:107], a[8:11], v[24:27]\n\t"
      "v_mfma_f32_16x16x32_bf16 v[24:27], v[120:123], a[12:15], v[24:27]\n\t"
      "s_waitcnt vmcnt(12)\n\t"
      "s_waitcnt lgkmcnt(0)\n\t"
      "s_barrier\n\t"
      "v_mfma_f32_16x16x32_bf16 v[24:27], v[136:139], a[16:19], v[24:27]\n\t"
      "v_mfma_f32_16x16x32_bf16 v[24:27], v[152:155], a[20:23], v[24:27]\n\t"
      "v_mfma_f32_16x16x32_bf16 v[28:31], v[76:79], a[0:3], 0\n\t"
      "ds_read_b128 a[48:51], v8 offset:12544\n\t"
      "v_mfma_f32_16x16x32_bf16 v[28:31], v[92:95], a[4:7], v[28:31]\n\t"
      "ds_read_b128 a[52:55], v60 offset:13568\n\t"
      "v_mfma_f32_16x16x32_bf16 v[28:31], v[108:111], a[8:11], v[28:31]\n\t"
      "ds_read_b128 a[56:59], v61 offset:14592\n\t"
      "v_mfma_f32_16x16x32_bf16 v[28:31], v[124:127], a[12:15], v[28:31]\n\t"
      "v_mfma_f32_16x16x32_bf16 v[28:31], v[140:143], a[16:19], v[28:31]\n\t"
      "v_mfma_f32_16x16x32_bf16 v[28:31], v[156:159], a[20:23], v[28:31]\n\t"
      "v_mfma_f32_16x16x32_bf16 v[32:35], a[60:63], a[0:3], 0\n\t"
      "v_mfma_f32_16x16x32_bf16 v[32:35], a[68:71], a[4:7], v[32:35]\n\t"
      "s_waitcnt lgkmcnt(2)\n\t"
      "v_mfma_f32_16x16x32_bf16 v[32:35], a[76:79], a[8:11], v[32:35]\n\t"
      "buffer_store_dwordx4 a[48:51], v12, s[44:47], s49 offen sc1\n\t"
      "v_mfma_f32_16x16x32_bf16 v[32:35], a[84:87], a[12:15], v[32:35]\n\t"
      "v_mfma_f32_16x16x32_bf16 v[32:35], a[92:95], a[16:19], v[32:35]\n\t"
      "v_mfma_f32_16x16x32_bf16 v[32:35], a[100:103], a[20:23], v[32:35]\n\t"
      "s_add_u32 m0, s18, s50\n\t"
      "v_mfma_f32_16x16x32_bf16 v[36:39], a[64:67], a[0:3], 0\n\t"
      "buffer_load_dwordx4 v2, s[40:43], s48 offen nt lds\n\t"
      "v_mfma_f32_16x16x32_bf16 v[36:39], a[72:75], a[4:7], v[36:39]\n\t"
      "v_mfma_f32_16x16x32_bf16 v[36:39], a[80:83], a[8:11], v[36:39]\n\t"
      "v_mfma_f32_16x16x32_bf16 v[36:39], a[88:91], a[12:15], v[36:39]\n\t"
      "s_waitcnt lgkmcnt(1)\n\t"
      "v_mfma_f32_16x16x32_bf16 v[36:39], a[96:99], a[16:19], v[36:39]\n\t"
      "buffer_store_dwordx4 a[52:55], v13, s[44:47], s49 offen offset:1024 sc1\n\t"
      "v_mfma_f32_16x16x32_bf16 v[36:39], a[104:107], a[20:23], v[36:39]\n\t"
      "v_mfma_f32_16x16x32_bf16 v[16:19], v[160:163], a[24:27], v[16:19]\n\t"
      "ds_read_b128 a[0:3], v52 offset:0\n\t"
      "v_mfma_f32_16x16x32_bf16 v[16:19], v[176:179], a[28:31], v[16:19]\n\t"
      "ds_read_b128 a[4:7], v53 offset:0\n\t"
      "v_mfma_f32_16x16x32_bf16 v[16:19], v[192:195], a[32:35], v[16:19]\n\t"
      "ds_read_b128 a[8:11], v54 offset:0\n\t"
      "v_mfma_f32_16x16x32_bf16 v[16:19], v[208:211], a[36:39], v[16:19]\n\t"
      "ds_read_b128 a[12:15], v55 offset:0\n\t"
      "v_mfma_f32_16x16x32_bf16 v[16:19], v[224:227], a[40:43], v[16:19]\n\t"
      "ds_read_b128 a[16:19], v52 offset:256\n\t"
      "v_mfma_f32_16x16x32_bf16 v[16:19], v[240:243], a[44:47], v[16:19]\n\t"
      "ds_read_b128 a[20:23], v53 offset:256\n\t"
      "v_mfma_f32_16x16x32_bf16 v[20:23], v[164:167], a[24:27], v[20:23]\n\t"
      "s_add_u32 s51, s51, 12288\n\t"
      "s_cmp_eq_u32 s51, 160256\n\t"
      "s_cselect_b32 s51, 25088, s51\n\t"
      "v_mfma_f32_16x16x32_bf16 v[20:23], v[180:183], a[28:31], v[20:23]\n\t"
      "s_waitcnt lgkmcnt(6)\n\t"
      "v_mfma_f32_16x16x32_bf16 v[20:23], v[196:199], a[32:35], v[20:23]\n\t"
      "buffer_store_dwordx4 a[56:59], v14, s[44:47], s49 offen offset:2048 sc1\n\t"
      "v_mfma_f32_16x16x32_bf16 v[20:23], v[212:215], a[36:39], v[20:23]\n\t"
      "v_mfma_f32_16x16x32_bf16 v[20:23], v[228:231], a[40:43], v[20:23]\n\t"
      "s_waitcnt lgkmcnt(0)\n\t"
      "v_mfma_f32_16x16x32_bf16 v[20:23], v[244:247], a[44:47], v[20:23]\n\t"
      "v_mfma_f32_16x16x32_bf16 v[24:27], v[168:171], a[24:27], v[24:27]\n\t"
      "v_mfma_f32_16x16x32_bf16 v[24:27], v[184:187], a[28:31], v[24:27]\n\t"
      "v_cvt_pk_bf16_f32 v40, v16, v17\n\t"
      "v_cvt_pk_bf16_f32 v41, v18, v19\n\t"
      "v_mfma_f32_16x16x32_bf16 v[24:27], v[200:203], a[32:35], v[24:27]\n\t"
      "ds_write_b64 v0, v[40:41] offset:0\n\t"
      "v_mfma_f32_16x16x32_bf16 v[24:27], v[216:219], a[36:39], v[24:27]\n\t"
      "buffer_load_dwordx4 v3, s[40:43], s48 offen offset:1024 nt lds\n\t"
      "v_mfma_f32_16x16x32_bf16 v[24:27], v[232:235], a[40:43], v[24:27]\n\t"
      "s_add_u32 s58, s58, 786432\n\t"
      "s_lshr_b32 s59, s59, 1\n\t"
      "v_mfma_f32_16x16x32_bf16 v[24:27], v[248:251], a[44:47], v[24:27]\n\t"
      "s_bitcmp1_b32 s59, 0\n\t"
      "s_cselect_b32 s49, 24576, 0\n\t"
      "s_add_u32 s49, s49, s58\n\t"
      "v_mfma_f32_16x16x32_bf16 v[28:31], v[172:175], a[24:27], v[28:31]\n\t"
      "v_mfma_f32_16x16x32_bf16 v[28:31], v[188:191], a[28:31], v[28:31]\n\t"
      "v_cvt_pk_bf16_f32 v42, v20, v21\n\t"
      "v_cvt_pk_bf16_f32 v43, v22, v23\n\t"
      "v_mfma_f32_16x16x32_bf16 v[28:31], v[204:207], a[32:35], v[28:31]\n\t"
      "ds_write_b64 v0, v[42:43] offset:32\n\t"
      "v_mfma_f32_16x16x32_bf16 v[28:31], v[220:223], a[36:39], v[28:31]\n\t"
      "v_mfma_f32_16x16x32_bf16 v[28:31], v[236:239], a[40:43], v[28:31]\n\t"
      "v_mfma_f32_16x16x32_bf16 v[28:31], v[252:255], a[44:47], v[28:31]\n\t"
      "v_mfma_f32_16x16x32_bf16 v[32:35], a[108:111], a[24:27], v[32:35]\n\t"
      "v_mfma_f32_16x16x32_bf16 v[32:35], a[116:119], a[28:31], v[32:35]\n\t"
      "v_cvt_pk_bf16_f32 v44, v24, v25\n\t"
      "v_cvt_pk_bf16_f32 v45, v26, v27\n\t"
      "v_mfma_f32_16x16x32_bf16 v[32:35], a[124:127], a[32:35], v[32:35]\n\t"
      "ds_write_b64 v0, v[44:45] offset:64\n\t"
      "v_mfma_f32_16x16x32_bf16 v[32:35], a[132:135], a[36:39], v[32:35]\n\t"
      "buffer_load_dwordx4 v4, s[40:43], s48 offen offset:2048 nt lds\n\t"
      "v_mfma_f32_16x16x32_bf16 v[32:35], a[140:143], a[40:43], v[32:35]\n\t"
      "s_add_u32 s50, s50, 12288\n\t"
      "v_mfma_f32_16x16x32_bf16 v[32:35], a[148:151], a[44:47], v[32:35]\n\t"
      "v_cvt_pk_bf16_f32 v46, v28, v29\n\t"
      "v_cvt_pk_bf16_f32 v47, v30, v31\n\t"
      "v_mfma_f32_16x16x32_bf16 v[36:39], a[112:115], a[24:27], v[36:39]\n\t"
      "ds_write_b64 v0, v[46:47] offset:96\n\t"
      "v_mfma_f32_16x16x32_bf16 v[36:39], a[120:123], a[28:31], v[36:39]\n\t"
      "s_cmp_eq_u32 s50, 160256\n\t"
      "s_cselect_b32 s50, 25088, s50\n\t"
      "v_mfma_f32_16x16x32_bf16 v[36:39], a[128:131], a[32:35], v[36:39]\n\t"
      "v_mfma_f32_16x16x32_bf16 v[36:39], a[136:139], a[36:39], v[36:39]\n\t"
      "v_mfma_f32_16x16x32_bf16 v[36:39], a[144:147], a[40:43], v[36:39]\n\t"
      "v_cvt_pk_bf16_f32 v48, v32, v33\n\t"
      "v_cvt_pk_bf16_f32 v49, v34, v35\n\t"
      "v_mfma_f32_16x16x32_bf16 v[36:39], a[152:155], a[44:47], v[36:39]\n\t"
      "ds_write_b64 v0, v[48:49] offset:128\n\t"
      ".Lmid1_%=:\n\t"
      "v_mfma_f32_16x16x32_bf16 v[16:19], v[64:67], a[0:3], 0\n\t"
      "ds_read_b128 a[24:27], v54 offset:256\n\t"
      "v_mfma_f32_16x16x32_bf16 v[16:19], v[80:83], a[4:7], v[16:19]\n\t"
      "ds_read_b128 a[28:31], v55 offset:256\n\t"
      "v_mfma_f32_16x16x32_bf16 v[16:19], v[96:99], a[8:11], v[16:19]\n\t"
      "ds_read_b128 a[32:35], v52 offset:512\n\t"
      "v_mfma_f32_16x16x32_bf16 v[16:19], v[112:115], a[12:15], v[16:19]\n\t"
      "ds_read_b128 a[36:39], v53 offset:512\n\t"
      "v_mfma_f32_16x16x32_bf16 v[16:19], v[128:131], a[16:19], v[16:19]\n\t"
      "ds_read_b128 a[40:43], v54 offset:512\n\t"
      "v_mfma_f32_16x16x32_bf16 v[16:19], v[144:147], a[20:23], v[16:19]\n\t"
      "ds_read_b128 a[44:47], v55 offset:512\n\t"
      "v_mfma_f32_16x16x32_bf16 v[20:23], v[68:71], a[0:3], 0\n\t"
      "v_cvt_pk_bf16_f32 v50, v36, v37\n\t"
      "v_cvt_pk_bf16_f32 v51, v38, v39\n\t"
      "v_mfma_f32_16x16x32_bf16 v[20:23], v[84:87], a[4:7], v[20:23]\n\t"
      "ds_write_b64 v0, v[50:51] offset:160\n\t"
      "v_mfma_f32_16x16x32_bf16 v[20:23], v[100:103], a[8:11], v[20:23]\n\t"
      "v_add_u32 v56, s51, v15\n\t"
      "v_xor_b32 v57, 64, v56\n\t"
      "v_mfma_f32_16x16x32_bf16 v[20:23], v[116:119], a[12:15], v[20:23]\n\t"
      "v_xor_b32 v58, 128, v56\n\t"
      "v_xor_b32 v59, 192, v56\n\t"
      "v_mfma_f32_16x16x32_bf16 v[20:23], v[132:135], a[16:19], v[20:23]\n\t"
      "v_mfma_f32_16x16x32_bf16 v[20:23], v[148:151], a[20:23], v[20:23]\n\t"
      "v_mfma_f32_16x16x32_bf16 v[24:27], v[72:75], a[0:3], 0\n\t"
      "v_mfma_f32_16x16x32_bf16 v[24:27], v[88:91], a[4:7], v[24:27]\n\t"
      "v_mfma_f32_16x16x32_bf16 v[24:27], v[104:107], a[8:11], v[24:27]\n\t"
      "v_mfma_f32_16x16x32_bf16 v[24:27], v[120:123], a[12:15], v[24:27]\n\t"
      "s_waitcnt vmcnt(12)\n\t"
      "s_waitcnt lgkmcnt(0)\n\t"
      "s_barrier\n\t"
      "v_mfma_f32_16x16x32_bf16 v[24:27], v[136:139], a[16:19], v[24:27]\n\t"
      "v_mfma_f32_16x16x32_bf16 v[24:27], v[152:155], a[20:23], v[24:27]\n\t"
      "v_mfma_f32_16x16x32_bf16 v[28:31], v[76:79], a[0:3], 0\n\t"
      "ds_read_b128 a[48:51], v8 offset:0\n\t"
      "v_mfma_f32_16x16x32_bf16 v[28:31], v[92:95], a[4:7], v[28:31]\n\t"
      "ds_read_b128 a[52:55], v60 offset:1024\n\t"
      "v_mfma_f32_16x16x32_bf16 v[28:31], v[108:111], a[8:11], v[28:31]\n\t"
      "ds_read_b128 a[56:59], v61 offset:2048\n\t"
      "v_mfma_f32_16x16x32_bf16 v[28:31], v[124:127], a[12:15], v[28:31]\n\t"
      "v_mfma_f32_16x16x32_bf16 v[28:31], v[140:143], a[16:19], v[28:31]\n\t"
      "v_mfma_f32_16x16x32_bf16 v[28:31], v[156:159], a[20:23], v[28:31]\n\t"
      "v_mfma_f32_16x16x32_bf16 v[32:35], a[60:63], a[0:3], 0\n\t"
      "v_mfma_f32_16x16x32_bf16 v[32:35], a[68:71], a[4:7], v[32:35]\n\t"
      "s_waitcnt lgkmcnt(2)\n\t"
      "v_mfma_f32_16x16x32_bf16 v[32:35], a[76:79], a[8:11], v[32:35]\n\t"
      "buffer_store_dwordx4 a[48:51], v9, s[44:47], s49 offen sc1\n\t"
      "v_mfma_f32_16x16x32_bf16 v[32:35], a[84:87], a[12:15], v[32:35]\n\t"
      "v_mfma_f32_16x16x32_bf16 v[32:35], a[92:95], a[16:19], v[32:35]\n\t"
      "v_mfma_f32_16x16x32_bf16 v[32:35], a[100:103], a[20:23], v[32:35]\n\t"
      "s_add_u32 m0, s18, s50\n\t"
      "v_mfma_f32_16x16x32_bf16 v[36:39], a[64:67], a[0:3], 0\n\t"
      "buffer_load_dwordx4 v5, s[40:43], s48 offen nt lds\n\t"
      "v_mfma_f32_16x16x32_bf16 v[36:39], a[72:75], a[4:7], v[36:39]\n\t"
      "v_mfma_f32_16x16x32_bf16 v[36:39], a[80:83], a[8:11], v[36:39]\n\t"
      "v_mfma_f32_16x16x32_bf16 v[36:39], a[88:91], a[12:15], v[36:39]\n\t"
      "s_waitcnt lgkmcnt(1)\n\t"
      "v_mfma_f32_16x16x32_bf16 v[36:39], a[96:99], a[16:19], v[36:39]\n\t"
      "buffer_store_dwordx4 a[52:55], v10, s[44:47], s49 offen offset:1024 sc1\n\t"
      "v_mfma_f32_16x16x32_bf16 v[36:39], a[104:107], a[20:23], v[36:39]\n\t"
      "v_mfma_f32_16x16x32_bf16 v[16:19], v[160:163], a[24:27], v[16:19]\n\t"
      "ds_read_b128 a[0:3], v56 offset:0\n\t"
      "v_mfma_f32_16x16x32_bf16 v[16:19], v[176:179], a[28:31], v[16:19]\n\t"
      "ds_read_b128 a[4:7], v57 offset:0\n\t"
      "v_mfma_f32_16x16x32_bf16 v[16:19], v[192:195], a[32:35], v[16:19]\n\t"
      "ds_read_b128 a[8:11], v58 offset:0\n\t"
      "v_mfma_f32_16x16x32_bf16 v[16:19], v[208:211], a[36:39], v[16:19]\n\t"
      "ds_read_b128 a[12:15], v59 offset:0\n\t"
      "v_mfma_f32_16x16x32_bf16 v[16:19], v[224:227], a[40:43], v[16:19]\n\t"
      "ds_read_b128 a[16:19], v56 offset:256\n\t"
      "v_mfma_f32_16x16x32_bf16 v[16:19], v[240:243], a[44:47], v[16:19]\n\t"
      "ds_read_b128 a[20:23], v57 offset:256\n\t"
      "v_mfma_f32_16x16x32_bf16 v[20:23], v[164:167], a[24:27], v[20:23]\n\t"
      "s_add_u32 s51, s51, 12288\n\t"
      "s_cmp_eq_u32 s51, 160256\n\t"
      "s_cselect_b32 s51, 25088, s51\n\t"
      "v_mfma_f32_16x16x32_bf16 v[20:23], v[180:183], a[28:31], v[20:23]\n\t"
      "s_waitcnt lgkmcnt(6)\n\t"
      "v_mfma_f32_16x16x32_bf16 v[20:23], v[196:199], a[32:35], v[20:23]\n\t"
      "buffer_store_dwordx4 a[56:59], v11, s[44:47], s49 offen offset:2048 sc1\n\t"
      "v_mfma_f32_16x16x32_bf16 v[20:23], v[212:215], a[36:39], v[20:23]\n\t"
      "v_mfma_f32_16x16x32_bf16 v[20:23], v[228:231], a[40:43], v[20:23]\n\t"
      "s_waitcnt lgkmcnt(0)\n\t"
      "v_mfma_f32_16x16x32_bf16 v[20:23], v[244:247], a[44:47], v[20:23]\n\t"
      "v_mfma_f32_16x16x32_bf16 v[24:27], v[168:171], a[24:27], v[24:27]\n\t"
      "v_mfma_f32_16x16x32_bf16 v[24:27], v[184:187], a[28:31], v[24:27]\n\t"
      "v_cvt_pk_bf16_f32 v40, v16, v17\n\t"
      "v_cvt_pk_bf16_f32 v41, v18, v19\n\t"
      "v_mfma_f32_16x16x32_bf16 v[24:27], v[200:203], a[32:35], v[24:27]\n\t"
      "ds_write_b64 v0, v[40:41] offset:12544\n\t"
      "v_mfma_f32_16x16x32_bf16 v[24:27], v[216:219], a[36:39], v[24:27]\n\t"
      "buffer_load_dwordx4 v6, s[40:43], s48 offen offset:1024 nt lds\n\t"
      "v_mfma_f32_16x16x32_bf16 v[24:27], v[232:235], a[40:43], v[24:27]\n\t"
      "v_mfma_f32_16x16x32_bf16 v[24:27], v[248:251], a[44:47], v[24:27]\n\t"
      "v_mfma_f32_16x16x32_bf16 v[28:31], v[172:175], a[24:27], v[28:31]\n\t"
      "v_mfma_f32_16x16x32_bf16 v[28:31], v[188:191], a[28:31], v[28:31]\n\t"
      "v_cvt_pk_bf16_f32 v42, v20, v21\n\t"
      "v_cvt_pk_bf16_f32 v43, v22, v23\n\t"
      "v_mfma_f32_16x16x32_bf16 v[28:31], v[204:207], a[32:35], v[28:31]\n\t"
      "ds_write_b64 v0, v[42:43] offset:12576\n\t"
      "v_mfma_f32_16x16x32_bf16 v[28:31], v[220:223], a[36:39], v[28:31]\n\t"
      "v_mfma_f32_16x16x32_bf16 v[28:31], v[236:239], a[40:43], v[28:31]\n\t"
      "v_mfma_f32_16x16x32_bf16 v[28:31], v[252:255], a[44:47], v[28:31]\n\t"
      "v_mfma_f32_16x16x32_bf16 v[32:35], a[108:111], a[24:27], v[32:35]\n\t"
      "v_mfma_f32_16x16x32_bf16 v[32:35], a[116:119], a[28:31], v[32:35]\n\t"
      "v_cvt_pk_bf16_f32 v44, v24, v25\n\t"
      "v_cvt_pk_bf16_f32 v45, v26, v27\n\t"
      "v_mfma_f32_16x16x32_bf16 v[32:35], a[124:127], a[32:35], v[32:35]\n\t"
      "ds_write_b64 v0, v[44:45] offset:12608\n\t"
      "v_mfma_f32_16x16x32_bf16 v[32:35], a[132:135], a[36:39], v[32:35]\n\t"
      "buffer_load_dwordx4 v7, s[40:43], s48 offen offset:2048 nt lds\n\t"
      "v_mfma_f32_16x16x32_bf16 v[32:35], a[140:143], a[40:43], v[32:35]\n\t"
      "s_add_u32 s50, s50, 12288\n\t"
      "v_mfma_f32_16x16x32_bf16 v[32:35], a[148:151], a[44:47], v[32:35]\n\t"
      "v_cvt_pk_bf16_f32 v46, v28, v29\n\t"
      "v_cvt_pk_bf16_f32 v47, v30, v31\n\t"
      "v_mfma_f32_16x16x32_bf16 v[36:39], a[112:115], a[24:27], v[36:39]\n\t"
      "ds_write_b64 v0, v[46:47] offset:12640\n\t"
      "v_mfma_f32_16x16x32_bf16 v[36:39], a[120:123], a[28:31], v[36:39]\n\t"
      "s_cmp_eq_u32 s50, 160256\n\t"
      "s_cselect_b32 s50, 25088, s50\n\t"
      "v_mfma_f32_16x16x32_bf16 v[36:39], a[128:131], a[32:35], v[36:39]\n\t"
      "s_sub_u32 s52, s52, 1\n\t"
      "s_cmp_lg_u32 s52, 0\n\t"
      "v_mfma_f32_16x16x32_bf16 v[36:39], a[136:139], a[36:39], v[36:39]\n\t"
      "v_mfma_f32_16x16x32_bf16 v[36:39], a[144:147], a[40:43], v[36:39]\n\t"
      "v_cvt_pk_bf16_f32 v48, v32, v33\n\t"
      "v_cvt_pk_bf16_f32 v49, v34, v35\n\t"
      "v_mfma_f32_16x16x32_bf16 v[36:39], a[152:155], a[44:47], v[36:39]\n\t"
      "ds_write_b64 v0, v[48:49] offset:12672\n\t"
      "s_cbranch_scc1 .Lloop1_%=\n\t"
      "s_branch .Lendc_%=\n\t"
      ".Lloop2_%=:\n\t"
      "v_mfma_f32_16x16x32_bf16 v[16:19], v[64:67], a[0:3], 0\n\t"
      "ds_read_b128 a[24:27], v58 offset:256\n\t"
      "v_mfma_f32_16x16x32_bf16 v[16:19], v[80:83], a[4:7], v[16:19]\n\t"
      "ds_read_b128 a[28:31], v59 offset:256\n\t"
      "v_mfma_f32_16x16x32_bf16 v[16:19], v[96:99], a[8:11], v[16:19]\n\t"
      "ds_read_b128 a[32:35], v56 offset:512\n\t"
      "v_mfma_f32_16x16x32_bf16 v[16:19], v[112:115], a[12:15], v[16:19]\n\t"
      "ds_read_b128 a[36:39], v57 offset:512\n\t"
      "v_mfma_f32_16x16x32_bf16 v[16:19], v[128:131], a[16:19], v[16:19]\n\t"
      "ds_read_b128 a[40:43], v58 offset:512\n\t"
      "v_mfma_f32_16x16x32_bf16 v[16:19], v[144:147], a[20:23], v[16:19]\n\t"
      "ds_read_b128 a[44:47], v59 offset:512\n\t"
      "v_mfma_f32_16x16x32_bf16 v[20:23], v[68:71], a[0:3], 0\n\t"
      "v_cvt_pk_bf16_f32 v50, v36, v37\n\t"
      "v_cvt_pk_bf16_f32 v51, v38, v39\n\t"
      "v_mfma_f32_16x16x32_bf16 v[20:23], v[84:87], a[4:7], v[20:23]\n\t"
      "ds_write_b64 v0, v[50:51] offset:12704\n\t"
      "v_mfma_f32_16x16x32_bf16 v[20:23], v[100:103], a[8:11], v[20:23]\n\t"
      "v_add_u32 v52, s51, v15\n\t"
      "v_xor_b32 v53, 64, v52\n\t"
      "v_mfma_f32_16x16x32_bf16 v[20:23], v[116:119], a[12:15], v[20:23]\n\t"
      "v_xor_b32 v54, 128, v52\n\t"
      "v_xor_b32 v55, 192, v52\n\t"
      "v_mfma_f32_16x16x32_bf16 v[20:23], v[132:135], a[16:19], v[20:23]\n\t"
      "s_add_u32 s56, s56, 786432\n\t"
      "s_lshr_b32 s57, s57, 1\n\t"
      "v_mfma_f32_16x16x32_bf16 v[20:23], v[148:151], a[20:23], v[20:23]\n\t"
      "s_bitcmp1_b32 s57, 0\n\t"
      "s_cselect_b32 s48, 24576, 0\n\t"
      "s_add_u32 s48, s48, s56\n\t"
      "v_mfma_f32_16x16x32_bf16 v[24:27], v[72:75], a[0:3], 0\n\t"
      "v_mfma_f32_16x16x32_bf16 v[24:27], v[88:91], a[4:7], v[24:27]\n\t"
      "v_mfma_f32_16x16x32_bf16 v[24:27], v[104:107], a[8:11], v[24:27]\n\t"
      "v_mfma_f32_16x16x32_bf16 v[24:27], v[120:123], a[12:15], v[24:27]\n\t"
      "v_mfma_f32_16x16x32_bf16 v[24:27], v[136:139], a[16:19], v[24:27]\n\t"
      "s_waitcnt vmcnt(12)\n\t"
      "s_waitcnt lgkmcnt(0)\n\t"
      "s_barrier\n\t"
      "v_mfma_f32_16x16x32_bf16 v[24:27], v[152:155], a[20:23], v[24:27]\n\t"
      "v_mfma_f32_16x16x32_bf16 v[28:31], v[76:79], a[0:3], 0\n\t"
      "ds_read_b128 a[48:51], v8 offset:12544\n\t"
      "v_mfma_f32_16x16x32_bf16 v[28:31], v[92:95], a[4:7], v[28:31]\n\t"
      "ds_read_b128 a[52:55], v60 offset:13568\n\t"
      "v_mfma_f32_16x16x32_bf16 v[28:31], v[108:111], a[8:11], v[28:31]\n\t"
      "ds_read_b128 a[56:59], v61 offset:14592\n\t"
      "v_mfma_f32_16x16x32_bf16 v[28:31], v[124:127], a[12:15], v[28:31]\n\t"
      "v_mfma_f32_16x16x32_bf16 v[28:31], v[140:143], a[16:19], v[28:31]\n\t"
      "v_mfma_f32_16x16x32_bf16 v[28:31], v[156:159], a[20:23], v[28:31]\n\t"
      "v_mfma_f32_16x16x32_bf16 v[32:35], a[60:63], a[0:3], 0\n\t"
      "v_mfma_f32_16x16x32_bf16 v[32:35], a[68:71], a[4:7], v[32:35]\n\t"
      "s_waitcnt lgkmcnt(2)\n\t"
      "v_mfma_f32_16x16x32_bf16 v[32:35], a[76:79], a[8:11], v[32:35]\n\t"
      "buffer_store_dwordx4 a[48:51], v12, s[44:47], s49 offen sc1\n\t"
      "v_mfma_f32_16x16x32_bf16 v[32:35], a[84:87], a[12:15], v[32:35]\n\t"
      "v_mfma_f32_16x16x32_bf16 v[32:35], a[92:95], a[16:19], v[32:35]\n\t"
      "v_mfma_f32_16x16x32_bf16 v[32:35], a[100:103], a[20:23], v[32:35]\n\t"
      "s_add_u32 m0, s18, s50\n\t"
      "v_mfma_f32_16x16x32_bf16 v[36:39], a[64:67], a[0:3], 0\n\t"
      "buffer_load_dwordx4 v2, s[40:43], s48 offen nt lds\n\t"
      "v_mfma_f32_16x16x32_bf16 v[36:39], a[72:75], a[4:7], v[36:39]\n\t"
      "v_mfma_f32_16x16x32_bf16 v[36:39], a[80:83], a[8:11], v[36:39]\n\t"
      "v_mfma_f32_16x16x32_bf16 v[36:39], a[88:91], a[12:15], v[36:39]\n\t"
      "s_waitcnt lgkmcnt(1)\n\t"
      "v_mfma_f32_16x16x32_bf16 v[36:39], a[96:99], a[16:19], v[36:39]\n\t"
      "buffer_store_dwordx4 a[52:55], v13, s[44:47], s49 offen offset:1024 sc1\n\t"
      "v_mfma_f32_16x16x32_bf16 v[36:39], a[104:107], a[20:23], v[36:39]\n\t"
      "v_mfma_f32_16x16x32_bf16 v[16:19], v[160:163], a[24:27], v[16:19]\n\t"
      "ds_read_b128 a[0:3], v52 offset:0\n\t"
      "v_mfma_f32_16x16x32_bf16 v[16:19], v[176:179], a[28:31], v[16:19]\n\t"
      "ds_read_b128 a[4:7], v53 offset:0\n\t"
      "v_mfma_f32_16x16x32_bf16 v[16:19], v[192:195], a[32:35], v[16:19]\n\t"
      "ds_read_b128 a[8:11], v54 offset:0\n\t"
      "v_mfma_f32_16x16x32_bf16 v[16:19], v[208:211], a[36:39], v[16:19]\n\t"
      "ds_read_b128 a[12:15], v55 offset:0\n\t"
      "v_mfma_f32_16x16x32_bf16 v[16:19], v[224:227], a[40:43], v[16:19]\n\t"
      "ds_read_b128 a[16:19], v52 offset:256\n\t"
      "v_mfma_f32_16x16x32_bf16 v[16:19], v[240:243], a[44:47], v[16:19]\n\t"
      "ds_read_b128 a[20:23], v53 offset:256\n\t"
      "v_mfma_f32_16x16x32_bf16 v[20:23], v[164:167], a[24:27], v[20:23]\n\t"
      "s_add_u32 s51, s51, 12288\n\t"
      "s_cmp_eq_u32 s51, 160256\n\t"
      "s_cselect_b32 s51, 25088, s51\n\t"
      "v_mfma_f32_16x16x32_bf16 v[20:23], v[180:183], a[28:31], v[20:23]\n\t"
      "s_waitcnt lgkmcnt(6)\n\t"
      "v_mfma_f32_16x16x32_bf16 v[20:23], v[196:199], a[32:35], v[20:23]\n\t"
      "buffer_store_dwordx4 a[56:59], v14, s[44:47], s49 offen offset:2048 sc1\n\t"
      "v_mfma_f32_16x16x32_bf16 v[20:23], v[212:215], a[36:39], v[20:23]\n\t"
      "v_mfma_f32_16x16x32_bf16 v[20:23], v[228:231], a[40:43], v[20:23]\n\t"
      "s_waitcnt lgkmcnt(0)\n\t"
      "v_mfma_f32_16x16x32_bf16 v[20:23], v[244:247], a[44:47], v[20:23]\n\t"
      "v_mfma_f32_16x16x32_bf16 v[24:27], v[168:171], a[24:27], v[24:27]\n\t"
      "v_mfma_f32_16x16x32_bf16 v[24:27], v[184:187], a[28:31], v[24:27]\n\t"
      "v_cvt_pk_bf16_f32 v40, v16, v17\n\t"
      "v_cvt_pk_bf16_f32 v41, v18, v19\n\t"
      "v_mfma_f32_16x16x32_bf16 v[24:27], v[200:203], a[32:35], v[24:27]\n\t"
      "ds_write_b64 v0, v[40:41] offset:0\n\t"
      "v_mfma_f32_16x16x32_bf16 v[24:27], v[216:219], a[36:39], v[24:27]\n\t"
      "buffer_load_dwordx4 v3, s[40:43], s48 offen offset:1024 nt lds\n\t"
      "v_mfma_f32_16x16x32_bf16 v[24:27], v[232:235], a[40:43], v[24:27]\n\t"
      "s_add_u32 s58, s58, 786432\n\t"
      "s_lshr_b32 s59, s59, 1\n\t"
      "v_mfma_f32_16x16x32_bf16 v[24:27], v[248:251], a[44:47], v[24:27]\n\t"
      "s_bitcmp1_b32 s59, 0\n\t"
      "s_cselect_b32 s49, 24576, 0\n\t"
      "s_add_u32 s49, s49, s58\n\t"
      "v_mfma_f32_16x16x32_bf16 v[28:31], v[172:175], a[24:27], v[28:31]\n\t"
      "v_mfma_f32_16x16x32_bf16 v[28:31], v[188:191], a[28:31], v[28:31]\n\t"
      "v_cvt_pk_bf16_f32 v42, v20, v21\n\t"
      "v_cvt_pk_bf16_f32 v43, v22, v23\n\t"
      "v_mfma_f32_16x16x32_bf16 v[28:31], v[204:207], a[32:35], v[28:31]\n\t"
      "ds_write_b64 v0, v[42:43] offset:32\n\t"
      "v_mfma_f32_16x16x32_bf16 v[28:31], v[220:223], a[36:39], v[28:31]\n\t"
      "v_mfma_f32_16x16x32_bf16 v[28:31], v[236:239], a[40:43], v[28:31]\n\t"
      "v_mfma_f32_16x16x32_bf16 v[28:31], v[252:255], a[44:47], v[28:31]\n\t"
      "v_mfma_f32_16x16x32_bf16 v[32:35], a[108:111], a[24:27], v[32:35]\n\t"
      "v_mfma_f32_16x16x32_bf16 v[32:35], a[116:119], a[28:31], v[32:35]\n\t"
      "v_cvt_pk_bf16_f32 v44, v24, v25\n\t"
      "v_cvt_pk_bf16_f32 v45, v26, v27\n\t"
      "v_mfma_f32_16x16x32_bf16 v[32:35], a[124:127], a[32:35], v[32:35]\n\t"
      "ds_write_b64 v0, v[44:45] offset:64\n\t"
      "v_mfma_f32_16x16x32_bf16 v[32:35], a[132:135], a[36:39], v[32:35]\n\t"
      "buffer_load_dwordx4 v4, s[40:43], s48 offen offset:2048 nt lds\n\t"
      "v_mfma_f32_16x16x32_bf16 v[32:35], a[140:143], a[40:43], v[32:35]\n\t"
      "s_add_u32 s50, s50, 12288\n\t"
      "v_mfma_f32_16x16x32_bf16 v[32:35], a[148:151], a[44:47], v[32:35]\n\t"
      "v_cvt_pk_bf16_f32 v46, v28, v29\n\t"
      "v_cvt_pk_bf16_f32 v47, v30, v31\n\t"
      "v_mfma_f32_16x16x32_bf16 v[36:39], a[112:115], a[24:27], v[36:39]\n\t"
      "ds_write_b64 v0, v[46:47] offset:96\n\t"
      "v_mfma_f32_16x16x32_bf16 v[36:39], a[120:123], a[28:31], v[36:39]\n\t"
      "s_cmp_eq_u32 s50, 160256\n\t"
      "s_cselect_b32 s50, 25088, s50\n\t"
      "v_mfma_f32_16x16x32_bf16 v[36:39], a[128:131], a[32:35], v[36:39]\n\t"
      "v_mfma_f32_16x16x32_bf16 v[36:39], a[136:139], a[36:39], v[36:39]\n\t"
      "v_mfma_f32_16x16x32_bf16 v[36:39], a[144:147], a[40:43], v[36:39]\n\t"
      "v_cvt_pk_bf16_f32 v48, v32, v33\n\t"
      "v_cvt_pk_bf16_f32 v49, v34, v35\n\t"
      "v_mfma_f32_16x16x32_bf16 v[36:39], a[152:155], a[44:47], v[36:39]\n\t"
      "ds_write_b64 v0, v[48:49] offset:128\n\t"
      ".Lmid2_%=:\n\t"
      "v_mfma_f32_16x16x32_bf16 v[16:19], v[64:67], a[0:3], 0\n\t"
      "ds_read_b128 a[24:27], v54 offset:256\n\t"
      "v_mfma_f32_16x16x32_bf16 v[16:19], v[80:83], a[4:7], v[16:19]\n\t"
      "ds_read_b128 a[28:31], v55 offset:256\n\t"
      "v_mfma_f32_16x16x32_bf16 v[16:19], v[96:99], a[8:11], v[16:19]\n\t"
      "ds_read_b128 a[32:35], v52 offset:512\n\t"
      "v_mfma_f32_16x16x32_bf16 v[16:19], v[112:115], a[12:15], v[16:19]\n\t"
      "ds_read_b128 a[36:39], v53 offset:512\n\t"
      "v_mfma_f32_16x16x32_bf16 v[16:19], v[128:131], a[16:19], v[16:19]\n\t"
      "ds_read_b128 a[40:43], v54 offset:512\n\t"
      "v_mfma_f32_16x16x32_bf16 v[16:19], v[144:147], a[20:23], v[16:19]\n\t"
      "ds_read_b128 a[44:47], v55 offset:512\n\t"
      "v_mfma_f32_16x16x32_bf16 v[20:23], v[68:71], a[0:3], 0\n\t"
      "v_cvt_pk_bf16_f32 v50, v36, v37\n\t"
      "v_cvt_pk_bf16_f32 v51, v38, v39\n\t"
      "v_mfma_f32_16x16x32_bf16 v[20:23], v[84:87], a[4:7], v[20:23]\n\t"
      "ds_write_b64 v0, v[50:51] offset:160\n\t"
      "v_mfma_f32_16x16x32_bf16 v[20:23], v[100:103], a[8:11], v[20:23]\n\t"
      "v_add_u32 v56, s51, v15\n\t"
      "v_xor_b32 v57, 64, v56\n\t"
      "v_mfma_f32_16x16x32_bf16 v[20:23], v[116:119], a[12:15], v[20:23]\n\t"
      "v_xor_b32 v58, 128, v56\n\t"
      "v_xor_b32 v59, 192, v56\n\t"
      "v_mfma_f32_16x16x32_bf16 v[20:23], v[132:135], a[16:19], v[20:23]\n\t"
      "v_mfma_f32_16x16x32_bf16 v[20:23], v[148:151], a[20:23], v[20:23]\n\t"
      "v_mfma_f32_16x16x32_bf16 v[24:27], v[72:75], a[0:3], 0\n\t"
      "v_mfma_f32_16x16x32_bf16 v[24:27], v[88:91], a[4:7], v[24:27]\n\t"
      "v_mfma_f32_16x16x32_bf16 v[24:27], v[104:107], a[8:11], v[24:27]\n\t"
      "v_mfma_f32_16x16x32_bf16 v[24:27], v[120:123], a[12:15], v[24:27]\n\t"
      "v_mfma_f32_16x16x32_bf16 v[24:27], v[136:139], a[16:19], v[24:27]\n\t"
      "s_waitcnt vmcnt(12)\n\t"
      "s_waitcnt lgkmcnt(0)\n\t"
      "s_barrier\n\t"
      "v_mfma_f32_16x16x32_bf16 v[24:27], v[152:155], a[20:23], v[24:27]\n\t"
      "v_mfma_f32_16x16x32_bf16 v[28:31], v[76:79], a[0:3], 0\n\t"
      "ds_read_b128 a[48:51], v8 offset:0\n\t"
      "v_mfma_f32_16x16x32_bf16 v[28:31], v[92:95], a[4:7], v[28:31]\n\t"
      "ds_read_b128 a[52:55], v60 offset:1024\n\t"
      "v_mfma_f32_16x16x32_bf16 v[28:31], v[108:111], a[8:11], v[28:31]\n\t"
      "ds_read_b128 a[56:59], v61 offset:2048\n\t"
      "v_mfma_f32_16x16x32_bf16 v[28:31], v[124:127], a[12:15], v[28:31]\n\t"
      "v_mfma_f32_16x16x32_bf16 v[28:31], v[140:143], a[16:19], v[28:31]\n\t"
      "v_mfma_f32_16x16x32_bf16 v[28:31], v[156:159], a[20:23], v[28:31]\n\t"
      "v_mfma_f32_16x16x32_bf16 v[32:35], a[60:63], a[0:3], 0\n\t"
      "v_mfma_f32_16x16x32_bf16 v[32:35], a[68:71], a[4:7], v[32:35]\n\t"
      "s_waitcnt lgkmcnt(2)\n\t"
      "v_mfma_f32_16x16x32_bf16 v[32:35], a[76:79], a[8:11], v[32:35]\n\t"
      "buffer_store_dwordx4 a[48:51], v9, s[44:47], s49 offen sc1\n\t"
      "v_mfma_f32_16x16x32_bf16 v[32:35], a[84:87], a[12:15], v[32:35]\n\t"
      "v_mfma_f32_16x16x32_bf16 v[32:35], a[92:95], a[16:19], v[32:35]\n\t"
      "v_mfma_f32_16x16x32_bf16 v[32:35], a[100:103], a[20:23], v[32:35]\n\t"
      "s_add_u32 m0, s18, s50\n\t"
      "v_mfma_f32_16x16x32_bf16 v[36:39], a[64:67], a[0:3], 0\n\t"
      "buffer_load_dwordx4 v5, s[40:43], s48 offen nt lds\n\t"
      "v_mfma_f32_16x16x32_bf16 v[36:39], a[72:75], a[4:7], v[36:39]\n\t"
      "v_mfma_f32_16x16x32_bf16 v[36:39], a[80:83], a[8:11], v[36:39]\n\t"
      "v_mfma_f32_16x16x32_bf16 v[36:39], a[88:91], a[12:15], v[36:39]\n\t"
      "s_waitcnt lgkmcnt(1)\n\t"
      "v_mfma_f32_16x16x32_bf16 v[36:39], a[96:99], a[16:19], v[36:39]\n\t"
      "buffer_store_dwordx4 a[52:55], v10, s[44:47], s49 offen offset:1024 sc1\n\t"
      "v_mfma_f32_16x16x32_bf16 v[36:39], a[104:107], a[20:23], v[36:39]\n\t"
      "v_mfma_f32_16x16x32_bf16 v[16:19], v[160:163], a[24:27], v[16:19]\n\t"
      "ds_read_b128 a[0:3], v56 offset:0\n\t"
      "v_mfma_f32_16x16x32_bf16 v[16:19], v[176:179], a[28:31], v[16:19]\n\t"
      "ds_read_b128 a[4:7], v57 offset:0\n\t"
      "v_mfma_f32_16x16x32_bf16 v[16:19], v[192:195], a[32:35], v[16:19]\n\t"
      "ds_read_b128 a[8:11], v58 offset:0\n\t"
      "v_mfma_f32_16x16x32_bf16 v[16:19], v[208:211], a[36:39], v[16:19]\n\t"
      "ds_read_b128 a[12:15], v59 offset:0\n\t"
      "v_mfma_f32_16x16x32_bf16 v[16:19], v[224:227], a[40:43], v[16:19]\n\t"
      "ds_read_b128 a[16:19], v56 offset:256\n\t"
      "v_mfma_f32_16x16x32_bf16 v[16:19], v[240:243], a[44:47], v[16:19]\n\t"
      "ds_read_b128 a[20:23], v57 offset:256\n\t"
      "v_mfma_f32_16x16x32_bf16 v[20:23], v[164:167], a[24:27], v[20:23]\n\t"
      "s_add_u32 s51, s51, 12288\n\t"
      "s_cmp_eq_u32 s51, 160256\n\t"
      "s_cselect_b32 s51, 25088, s51\n\t"
      "v_mfma_f32_16x16x32_bf16 v[20:23], v[180:183], a[28:31], v[20:23]\n\t"
      "s_waitcnt lgkmcnt(6)\n\t"
      "v_mfma_f32_16x16x32_bf16 v[20:23], v[196:199], a[32:35], v[20:23]\n\t"
      "buffer_store_dwordx4 a[56:59], v11, s[44:47], s49 offen offset:2048 sc1\n\t"
      "v_mfma_f32_16x16x32_bf16 v[20:23], v[212:215], a[36:39], v[20:23]\n\t"
      "v_mfma_f32_16x16x32_bf16 v[20:23], v[228:231], a[40:43], v[20:23]\n\t"
      "s_waitcnt lgkmcnt(0)\n\t"
      "v_mfma_f32_16x16x32_bf16 v[20:23], v[244:247], a[44:47], v[20:23]\n\t"
      "v_mfma_f32_16x16x32_bf16 v[24:27], v[168:171], a[24:27], v[24:27]\n\t"
      "v_mfma_f32_16x16x32_bf16 v[24:27], v[184:187], a[28:31], v[24:27]\n\t"
      "v_cvt_pk_bf16_f32 v40, v16, v17\n\t"
      "v_cvt_pk_bf16_f32 v41, v18, v19\n\t"
      "v_mfma_f32_16x16x32_bf16 v[24:27], v[200:203], a[32:35], v[24:27]\n\t"
      "ds_write_b64 v0, v[40:41] offset:12544\n\t"
      "v_mfma_f32_16x16x32_bf16 v[24:27], v[216:219], a[36:39], v[24:27]\n\t"
      "buffer_load_dwordx4 v6, s[40:43], s48 offen offset:1024 nt lds\n\t"
      "v_mfma_f32_16x16x32_bf16 v[24:27], v[232:235], a[40:43], v[24:27]\n\t"
      "v_mfma_f32_16x16x32_bf16 v[24:27], v[248:251], a[44:47], v[24:27]\n\t"
      "v_mfma_f32_16x16x32_bf16 v[28:31], v[172:175], a[24:27], v[28:31]\n\t"
      "v_mfma_f32_16x16x32_bf16 v[28:31], v[188:191], a[28:31], v[28:31]\n\t"
      "v_cvt_pk_bf16_f32 v42, v20, v21\n\t"
      "v_cvt_pk_bf16_f32 v43, v22, v23\n\t"
      "v_mfma_f32_16x16x32_bf16 v[28:31], v[204:207], a[32:35], v[28:31]\n\t"
      "ds_write_b64 v0, v[42:43] offset:12576\n\t"
      "v_mfma_f32_16x16x32_bf16 v[28:31], v[220:223], a[36:39], v[28:31]\n\t"
      "v_mfma_f32_16x16x32_bf16 v[28:31], v[236:239], a[40:43], v[28:31]\n\t"
      "v_mfma_f32_16x16x32_bf16 v[28:31], v[252:255], a[44:47], v[28:31]\n\t"
      "v_mfma_f32_16x16x32_bf16 v[32:35], a[108:111], a[24:27], v[32:35]\n\t"
      "v_mfma_f32_16x16x32_bf16 v[32:35], a[116:119], a[28:31], v[32:35]\n\t"
      "v_cvt_pk_bf16_f32 v44, v24, v25\n\t"
      "v_cvt_pk_bf16_f32 v45, v26, v27\n\t"
      "v_mfma_f32_16x16x32_bf16 v[32:35], a[124:127], a[32:35], v[32:35]\n\t"
      "ds_write_b64 v0, v[44:45] offset:12608\n\t"
      "v_mfma_f32_16x16x32_bf16 v[32:35], a[132:135], a[36:39], v[32:35]\n\t"
      "buffer_load_dwordx4 v7, s[40:43], s48 offen offset:2048 nt lds\n\t"
      "v_mfma_f32_16x16x32_bf16 v[32:35], a[140:143], a[40:43], v[32:35]\n\t"
      "s_add_u32 s50, s50, 12288\n\t"
      "v_mfma_f32_16x16x32_bf16 v[32:35], a[148:151], a[44:47], v[32:35]\n\t"
      "v_cvt_pk_bf16_f32 v46, v28, v29\n\t"
      "v_cvt_pk_bf16_f32 v47, v30, v31\n\t"
      "v_mfma_f32_16x16x32_bf16 v[36:39], a[112:115], a[24:27], v[36:39]\n\t"
      "ds_write_b64 v0, v[46:47] offset:12640\n\t"
      "v_mfma_f32_16x16x32_bf16 v[36:39], a[120:123], a[28:31], v[36:39]\n\t"
      "s_cmp_eq_u32 s50, 160256\n\t"
      "s_cselect_b32 s50, 25088, s50\n\t"
      "v_mfma_f32_16x16x32_bf16 v[36:39], a[128:131], a[32:35], v[36:39]\n\t"
      "s_sub_u32 s52, s52, 1\n\t"
      "s_cmp_lg_u32 s52, 0\n\t"
      "v_mfma_f32_16x16x32_bf16 v[36:39], a[136:139], a[36:39], v[36:39]\n\t"
      "v_mfma_f32_16x16x32_bf16 v[36:39], a[144:147], a[40:43], v[36:39]\n\t"
      "v_cvt_pk_bf16_f32 v48, v32, v33\n\t"
      "v_cvt_pk_bf16_f32 v49, v34, v35\n\t"
      "v_mfma_f32_16x16x32_bf16 v[36:39], a[152:155], a[44:47], v[36:39]\n\t"
      "ds_write_b64 v0, v[48:49] offset:12672\n\t"
      "s_cbranch_scc1 .Lloop2_%=\n\t"
      "s_branch .Lendc_%=\n\t"
      ".Lloop3_%=:\n\t"
      "v_mfma_f32_16x16x32_bf16 v[16:19], v[64:67], a[0:3], 0\n\t"
      "ds_read_b128 a[24:27], v58 offset:256\n\t"
      "v_mfma_f32_16x16x32_bf16 v[16:19], v[80:83], a[4:7], v[16:19]\n\t"
      "ds_read_b128 a[28:31], v59 offset:256\n\t"
      "v_mfma_f32_16x16x32_bf16 v[16:19], v[96:99], a[8:11], v[16:19]\n\t"
      "ds_read_b128 a[32:35], v56 offset:512\n\t"
      "v_mfma_f32_16x16x32_bf16 v[16:19], v[112:115], a[12:15], v[16:19]\n\t"
      "ds_read_b128 a[36:39], v57 offset:512\n\t"
      "v_mfma_f32_16x16x32_bf16 v[16:19], v[128:131], a[16:19], v[16:19]\n\t"
      "ds_read_b128 a[40:43], v58 offset:512\n\t"
      "v_mfma_f32_16x16x32_bf16 v[16:19], v[144:147], a[20:23], v[16:19]\n\t"
      "ds_read_b128 a[44:47], v59 offset:512\n\t"
      "v_mfma_f32_16x16x32_bf16 v[20:23], v[68:71], a[0:3], 0\n\t"
      "v_cvt_pk_bf16_f32 v50, v36, v37\n\t"
      "v_cvt_pk_bf16_f32 v51, v38, v39\n\t"
      "v_mfma_f32_16x16x32_bf16 v[20:23], v[84:87], a[4:7], v[20:23]\n\t"
      "ds_write_b64 v0, v[50:51] offset:12704\n\t"
      "v_mfma_f32_16x16x32_bf16 v[20:23], v[100:103], a[8:11], v[20:23]\n\t"
      "v_add_u32 v52, s51, v15\n\t"
      "v_xor_b32 v53, 64, v52\n\t"
      "v_mfma_f32_16x16x32_bf16 v[20:23], v[116:119], a[12:15], v[20:23]\n\t"
      "v_xor_b32 v54, 128, v52\n\t"
      "v_xor_b32 v55, 192, v52\n\t"
      "v_mfma_f32_16x16x32_bf16 v[20:23], v[132:135], a[16:19], v[20:23]\n\t"
      "s_add_u32 s56, s56, 786432\n\t"
      "s_lshr_b32 s57, s57, 1\n\t"
      "v_mfma_f32_16x16x32_bf16 v[20:23], v[148:151], a[20:23], v[20:23]\n\t"
      "s_bitcmp1_b32 s57, 0\n\t"
      "s_cselect_b32 s48, 24576, 0\n\t"
      "s_add_u32 s48, s48, s56\n\t"
      "v_mfma_f32_16x16x32_bf16 v[24:27], v[72:75], a[0:3], 0\n\t"
      "v_mfma_f32_16x16x32_bf16 v[24:27], v[88:91], a[4:7], v[24:27]\n\t"
      "v_mfma_f32_16x16x32_bf16 v[24:27], v[104:107], a[8:11], v[24:27]\n\t"
      "v_mfma_f32_16x16x32_bf16 v[24:27], v[120:123], a[12:15], v[24:27]\n\t"
      "v_mfma_f32_16x16x32_bf16 v[24:27], v[136:139], a[16:19], v[24:27]\n\t"
      "v_mfma_f32_16x16x32_bf16 v[24:27], v[152:155], a[20:23], v[24:27]\n\t"
      "s_waitcnt vmcnt(12)\n\t"
      "s_waitcnt lgkmcnt(0)\n\t"
      "s_barrier\n\t"
      "v_mfma_f32_16x16x32_bf16 v[28:31], v[76:79], a[0:3], 0\n\t"
      "ds_read_b128 a[48:51], v8 offset:12544\n\t"
      "v_mfma_f32_16x16x32_bf16 v[28:31], v[92:95], a[4:7], v[28:31]\n\t"
      "ds_read_b128 a[52:55], v60 offset:13568\n\t"
      "v_mfma_f32_16x16x32_bf16 v[28:31], v[108:111], a[8:11], v[28:31]\n\t"
      "ds_read_b128 a[56:59], v61 offset:14592\n\t"
      "v_mfma_f32_16x16x32_bf16 v[28:31], v[124:127], a[12:15], v[28:31]\n\t"
      "v_mfma_f32_16x16x32_bf16 v[28:31], v[140:143], a[16:19], v[28:31]\n\t"
      "v_mfma_f32_16x16x32_bf16 v[28:31], v[156:159], a[20:23], v[28:31]\n\t"
      "v_mfma_f32_16x16x32_bf16 v[32:35], a[60:63], a[0:3], 0\n\t"
      "v_mfma_f32_16x16x32_bf16 v[32:35], a[68:71], a[4:7], v[32:35]\n\t"
      "s_waitcnt lgkmcnt(2)\n\t"
      "v_mfma_f32_16x16x32_bf16 v[32:35], a[76:79], a[8:11], v[32:35]\n\t"
      "buffer_store_dwordx4 a[48:51], v12, s[44:47], s49 offen sc1\n\t"
      "v_mfma_f32_16x16x32_bf16 v[32:35], a[84:87], a[12:15], v[32:35]\n\t"
      "v_mfma_f32_16x16x32_bf16 v[32:35], a[92:95], a[16:19], v[32:35]\n\t"
      "v_mfma_f32_16x16x32_bf16 v[32:35], a[100:103], a[20:23], v[32:35]\n\t"
      "s_add_u32 m0, s18, s50\n\t"
      "v_mfma_f32_16x16x32_bf16 v[36:39], a[64:67], a[0:3], 0\n\t"
      "buffer_load_dwordx4 v2, s[40:43], s48 offen nt lds\n\t"
      "v_mfma_f32_16x16x32_bf16 v[36:39], a[72:75], a[4:7], v[36:39]\n\t"
      "v_mfma_f32_16x16x32_bf16 v[36:39], a[80:83], a[8:11], v[36:39]\n\t"
      "v_mfma_f32_16x16x32_bf16 v[36:39], a[88:91], a[12:15], v[36:39]\n\t"
      "s_waitcnt lgkmcnt(1)\n\t"
      "v_mfma_f32_16x16x32_bf16 v[36:39], a[96:99], a[16:19], v[36:39]\n\t"
      "buffer_store_dwordx4 a[52:55], v13, s[44:47], s49 offen offset:1024 sc1\n\t"
      "v_mfma_f32_16x16x32_bf16 v[36:39], a[104:107], a[20:23], v[36:39]\n\t"
      "v_mfma_f32_16x16x32_bf16 v[16:19], v[160:163], a[24:27], v[16:19]\n\t"
      "ds_read_b128 a[0:3], v52 offset:0\n\t"
      "v_mfma_f32_16x16x32_bf16 v[16:19], v[176:179], a[28:31], v[16:19]\n\t"
      "ds_read_b128 a[4:7], v53 offset:0\n\t"
      "v_mfma_f32_16x16x32_bf16 v[16:19], v[192:195], a[32:35], v[16:19]\n\t"
      "ds_read_b128 a[8:11], v54 offset:0\n\t"
      "v_mfma_f32_16x16x32_bf16 v[16:19], v[208:211], a[36:39], v[16:19]\n\t"
      "ds_read_b128 a[12:15], v55 offset:0\n\t"
      "v_mfma_f32_16x16x32_bf16 v[16:19], v[224:227], a[40:43], v[16:19]\n\t"
      "ds_read_b128 a[16:19], v52 offset:256\n\t"
      "v_mfma_f32_16x16x32_bf16 v[16:19], v[240:243], a[44:47], v[16:19]\n\t"
      "ds_read_b128 a[20:23], v53 offset:256\n\t"
      "v_mfma_f32_16x16x32_bf16 v[20:23], v[164:167], a[24:27], v[20:23]\n\t"
      "s_add_u32 s51, s51, 12288\n\t"
      "s_cmp_eq_u32 s51, 160256\n\t"
      "s_cselect_b32 s51, 25088, s51\n\t"
      "v_mfma_f32_16x16x32_bf16 v[20:23], v[180:183], a[28:31], v[20:23]\n\t"
      "s_waitcnt lgkmcnt(6)\n\t"
      "v_mfma_f32_16x16x32_bf16 v[20:23], v[196:199], a[32:35], v[20:23]\n\t"
      "buffer_store_dwordx4 a[56:59], v14, s[44:47], s49 offen offset:2048 sc1\n\t"
      "v_mfma_f32_16x16x32_bf16 v[20:23], v[212:215], a[36:39], v[20:23]\n\t"
      "v_mfma_f32_16x16x32_bf16 v[20:23], v[228:231], a[40:43], v[20:23]\n\t"
      "s_waitcnt lgkmcnt(0)\n\t"
      "v_mfma_f32_16x16x32_bf16 v[20:23], v[244:247], a[44:47], v[20:23]\n\t"
      "v_mfma_f32_16x16x32_bf16 v[24:27], v[168:171], a[24:27], v[24:27]\n\t"
      "v_mfma_f32_16x16x32_bf16 v[24:27], v[184:187], a[28:31], v[24:27]\n\t"
      "v_cvt_pk_bf16_f32 v40, v16, v17\n\t"
      "v_cvt_pk_bf16_f32 v41, v18, v19\n\t"
      "v_mfma_f32_16x16x32_bf16 v[24:27], v[200:203], a[32:35], v[24:27]\n\t"
      "ds_write_b64 v0, v[40:41] offset:0\n\t"
      "v_mfma_f32_16x16x32_bf16 v[24:27], v[216:219], a[36:39], v[24:27]\n\t"
      "buffer_load_dwordx4 v3, s[40:43], s48 offen offset:1024 nt lds\n\t"
      "v_mfma_f32_16x16x32_bf16 v[24:27], v[232:235], a[40:43], v[24:27]\n\t"
      "s_add_u32 s58, s58, 786432\n\t"
      "s_lshr_b32 s59, s59, 1\n\t"
      "v_mfma_f32_16x16x32_bf16 v[24:27], v[248:251], a[44:47], v[24:27]\n\t"
      "s_bitcmp1_b32 s59, 0\n\t"
      "s_cselect_b32 s49, 24576, 0\n\t"
      "s_add_u32 s49, s49, s58\n\t"
      "v_mfma_f32_16x16x32_bf16 v[28:31], v[172:175], a[24:27], v[28:31]\n\t"
      "v_mfma_f32_16x16x32_bf16 v[28:31], v[188:191], a[28:31], v[28:31]\n\t"
      "v_cvt_pk_bf16_f32 v42, v20, v21\n\t"
      "v_cvt_pk_bf16_f32 v43, v22, v23\n\t"
      "v_mfma_f32_16x16x32_bf16 v[28:31], v[204:207], a[32:35], v[28:31]\n\t"
      "ds_write_b64 v0, v[42:43] offset:32\n\t"
      "v_mfma_f32_16x16x32_bf16 v[28:31], v[220:223], a[36:39], v[28:31]\n\t"
      "v_mfma_f32_16x16x32_bf16 v[28:31], v[236:239], a[40:43], v[28:31]\n\t"
      "v_mfma_f32_16x16x32_bf16 v[28:31], v[252:255], a[44:47], v[28:31]\n\t"
      "v_mfma_f32_16x16x32_bf16 v[32:35], a[108:111], a[24:27], v[32:35]\n\t"
      "v_mfma_f32_16x16x32_bf16 v[32:35], a[116:119], a[28:31], v[32:35]\n\t"
      "v_cvt_pk_bf16_f32 v44, v24, v25\n\t"
      "v_cvt_pk_bf16_f32 v45, v26, v27\n\t"
      "v_mfma_f32_16x16x32_bf16 v[32:35], a[124:127], a[32:35], v[32:35]\n\t"
      "ds_write_b64 v0, v[44:45] offset:64\n\t"
      "v_mfma_f32_16x16x32_bf16 v[32:35], a[132:135], a[36:39], v[32:35]\n\t"
      "buffer_load_dwordx4 v4, s[40:43], s48 offen offset:2048 nt lds\n\t"
      "v_mfma_f32_16x16x32_bf16 v[32:35], a[140:143], a[40:43], v[32:35]\n\t"
      "s_add_u32 s50, s50, 12288\n\t"
      "v_mfma_f32_16x16x32_bf16 v[32:35], a[148:151], a[44:47], v[32:35]\n\t"
      "v_cvt_pk_bf16_f32 v46, v28, v29\n\t"
      "v_cvt_pk_bf16_f32 v47, v30, v31\n\t"
      "v_mfma_f32_16x16x32_bf16 v[36:39], a[112:115], a[24:27], v[36:39]\n\t"
      "ds_write_b64 v0, v[46:47] offset:96\n\t"
      "v_mfma_f32_16x16x32_bf16 v[36:39], a[120:123], a[28:31], v[36:39]\n\t"
      "s_cmp_eq_u32 s50, 160256\n\t"
      "s_cselect_b32 s50, 25088, s50\n\t"
      "v_mfma_f32_16x16x32_bf16 v[36:39], a[128:131], a[32:35], v[36:39]\n\t"
      "v_mfma_f32_16x16x32_bf16 v[36:39], a[136:139], a[36:39], v[36:39]\n\t"
      "v_mfma_f32_16x16x32_bf16 v[36:39], a[144:147], a[40:43], v[36:39]\n\t"
      "v_cvt_pk_bf16_f32 v48, v32, v33\n\t"
      "v_cvt_pk_bf16_f32 v49, v34, v35\n\t"
      "v_mfma_f32_16x16x32_bf16 v[36:39], a[152:155], a[44:47], v[36:39]\n\t"
      "ds_write_b64 v0, v[48:49] offset:128\n\t"
      ".Lmid3_%=:\n\t"
      "v_mfma_f32_16x16x32_bf16 v[16:19], v[64:67], a[0:3], 0\n\t"
      "ds_read_b128 a[24:27], v54 offset:256\n\t"
      "v_mfma_f32_16x16x32_bf16 v[16:19], v[80:83], a[4:7], v[16:19]\n\t"
      "ds_read_b128 a[28:31], v55 offset:256\n\t"
      "v_mfma_f32_16x16x32_bf16 v[16:19], v[96:99], a[8:11], v[16:19]\n\t"
      "ds_read_b128 a[32:35], v52 offset:512\n\t"
      "v_mfma_f32_16x16x32_bf16 v[16:19], v[112:115], a[12:15], v[16:19]\n\t"
      "ds_read_b128 a[36:39], v53 offset:512\n\t"
      "v_mfma_f32_16x16x32_bf16 v[16:19], v[128:131], a[16:19], v[16:19]\n\t"
      "ds_read_b128 a[40:43], v54 offset:512\n\t"
      "v_mfma_f32_16x16x32_bf16 v[16:19], v[144:147], a[20:23], v[16:19]\n\t"
      "ds_read_b128 a[44:47], v55 offset:512\n\t"
      "v_mfma_f32_16x16x32_bf16 v[20:23], v[68:71], a[0:3], 0\n\t"
      "v_cvt_pk_bf16_f32 v50, v36, v37\n\t"
      "v_cvt_pk_bf16_f32 v51, v38, v39\n\t"
      "v_mfma_f32_16x16x32_bf16 v[20:23], v[84:87], a[4:7], v[20:23]\n\t"
      "ds_write_b64 v0, v[50:51] offset:160\n\t"
      "v_mfma_f32_16x16x32_bf16 v[20:23], v[100:103], a[8:11], v[20:23]\n\t"
      "v_add_u32 v56, s51, v15\n\t"
      "v_xor_b32 v57, 64, v56\n\t"
      "v_mfma_f32_16x16x32_bf16 v[20:23], v[116:119], a[12:15], v[20:23]\n\t"
      "v_xor_b32 v58, 128, v56\n\t"
      "v_xor_b32 v59, 192, v56\n\t"
      "v_mfma_f32_16x16x32_bf16 v[20:23], v[132:135], a[16:19], v[20:23]\n\t"
      "v_mfma_f32_16x16x32_bf16 v[20:23], v[148:151], a[20:23], v[20:23]\n\t"
      "v_mfma_f32_16x16x32_bf16 v[24:27], v[72:75], a[0:3], 0\n\t"
      "v_mfma_f32_16x16x32_bf16 v[24:27], v[88:91], a[4:7], v[24:27]\n\t"
      "v_mfma_f32_16x16x32_bf16 v[24:27], v[104:107], a[8:11], v[24:27]\n\t"
      "v_mfma_f32_16x16x32_bf16 v[24:27], v[120:123], a[12:15], v[24:27]\n\t"
      "v_mfma_f32_16x16x32_bf16 v[24:27], v[136:139], a[16:19], v[24:27]\n\t"
      "v_mfma_f32_16x16x32_bf16 v[24:27], v[152:155], a[20:23], v[24:27]\n\t"
      "s_waitcnt vmcnt(12)\n\t"
      "s_waitcnt lgkmcnt(0)\n\t"
      "s_barrier\n\t"
      "v_mfma_f32_16x16x32_bf16 v[28:31], v[76:79], a[0:3], 0\n\t"
      "ds_read_b128 a[48:51], v8 offset:0\n\t"
      "v_mfma_f32_16x16x32_bf16 v[28:31], v[92:95], a[4:7], v[28:31]\n\t"
      "ds_read_b128 a[52:55], v60 offset:1024\n\t"
      "v_mfma_f32_16x16x32_bf16 v[28:31], v[108:111], a[8:11], v[28:31]\n\t"
      "ds_read_b128 a[56:59], v61 offset:2048\n\t"
      "v_mfma_f32_16x16x32_bf16 v[28:31], v[124:127], a[12:15], v[28:31]\n\t"
      "v_mfma_f32_16x16x32_bf16 v[28:31], v[140:143], a[16:19], v[28:31]\n\t"
      "v_mfma_f32_16x16x32_bf16 v[28:31], v[156:159], a[20:23], v[28:31]\n\t"
      "v_mfma_f32_16x16x32_bf16 v[32:35], a[60:63], a[0:3], 0\n\t"
      "v_mfma_f32_16x16x32_bf16 v[32:35], a[68:71], a[4:7], v[32:35]\n\t"
      "s_waitcnt lgkmcnt(2)\n\t"
      "v_mfma_f32_16x16x32_bf16 v[32:35], a[76:79], a[8:11], v[32:35]\n\t"
      "buffer_store_dwordx4 a[48:51], v9, s[44:47], s49 offen sc1\n\t"
      "v_mfma_f32_16x16x32_bf16 v[32:35], a[84:87], a[12:15], v[32:35]\n\t"
      "v_mfma_f32_16x16x32_bf16 v[32:35], a[92:95], a[16:19], v[32:35]\n\t"
      "v_mfma_f32_16x16x32_bf16 v[32:35], a[100:103], a[20:23], v[32:35]\n\t"
      "s_add_u32 m0, s18, s50\n\t"
      "v_mfma_f32_16x16x32_bf16 v[36:39], a[64:67], a[0:3], 0\n\t"
      "buffer_load_dwordx4 v5, s[40:43], s48 offen nt lds\n\t"
      "v_mfma_f32_16x16x32_bf16 v[36:39], a[72:75], a[4:7], v[36:39]\n\t"
      "v_mfma_f32_16x16x32_bf16 v[36:39], a[80:83], a[8:11], v[36:39]\n\t"
      "v_mfma_f32_16x16x32_bf16 v[36:39], a[88:91], a[12:15], v[36:39]\n\t"
      "s_waitcnt lgkmcnt(1)\n\t"
      "v_mfma_f32_16x16x32_bf16 v[36:39], a[96:99], a[16:19], v[36:39]\n\t"
      "buffer_store_dwordx4 a[52:55], v10, s[44:47], s49 offen offset:1024 sc1\n\t"
      "v_mfma_f32_16x16x32_bf16 v[36:39], a[104:107], a[20:23], v[36:39]\n\t"
      "v_mfma_f32_16x16x32_bf16 v[16:19], v[160:163], a[24:27], v[16:19]\n\t"
      "ds_read_b128 a[0:3], v56 offset:0\n\t"
      "v_mfma_f32_16x16x32_bf16 v[16:19], v[176:179], a[28:31], v[16:19]\n\t"
      "ds_read_b128 a[4:7], v57 offset:0\n\t"
      "v_mfma_f32_16x16x32_bf16 v[16:19], v[192:195], a[32:35], v[16:19]\n\t"
      "ds_read_b128 a[8:11], v58 offset:0\n\t"
      "v_mfma_f32_16x16x32_bf16 v[16:19], v[208:211], a[36:39], v[16:19]\n\t"
      "ds_read_b128 a[12:15], v59 offset:0\n\t"
      "v_mfma_f32_16x16x32_bf16 v[16:19], v[224:227], a[40:43], v[16:19]\n\t"
      "ds_read_b128 a[16:19], v56 offset:256\n\t"
      "v_mfma_f32_16x16x32_bf16 v[16:19], v[240:243], a[44:47], v[16:19]\n\t"
      "ds_read_b128 a[20:23], v57 offset:256\n\t"
      "v_mfma_f32_16x16x32_bf16 v[20:23], v[164:167], a[24:27], v[20:23]\n\t"
      "s_add_u32 s51, s51, 12288\n\t"
      "s_cmp_eq_u32 s51, 160256\n\t"
      "s_cselect_b32 s51, 25088, s51\n\t"
      "v_mfma_f32_16x16x32_bf16 v[20:23], v[180:183], a[28:31], v[20:23]\n\t"
      "s_waitcnt lgkmcnt(6)\n\t"
      "v_mfma_f32_16x16x32_bf16 v[20:23], v[196:199], a[32:35], v[20:23]\n\t"
      "buffer_store_dwordx4 a[56:59], v11, s[44:47], s49 offen offset:2048 sc1\n\t"
      "v_mfma_f32_16x16x32_bf16 v[20:23], v[212:215], a[36:39], v[20:23]\n\t"
      "v_mfma_f32_16x16x32_bf16 v[20:23], v[228:231], a[40:43], v[20:23]\n\t"
      "s_waitcnt lgkmcnt(0)\n\t"
      "v_mfma_f32_16x16x32_bf16 v[20:23], v[244:247], a[44:47], v[20:23]\n\t"
      "v_mfma_f32_16x16x32_bf16 v[24:27], v[168:171], a[24:27], v[24:27]\n\t"
      "v_mfma_f32_16x16x32_bf16 v[24:27], v[184:187], a[28:31], v[24:27]\n\t"
      "v_cvt_pk_bf16_f32 v40, v16, v17\n\t"
      "v_cvt_pk_bf16_f32 v41, v18, v19\n\t"
      "v_mfma_f32_16x16x32_bf16 v[24:27], v[200:203], a[32:35], v[24:27]\n\t"
      "ds_write_b64 v0, v[40:41] offset:12544\n\t"
      "v_mfma_f32_16x16x32_bf16 v[24:27], v[216:219], a[36:39], v[24:27]\n\t"
      "buffer_load_dwordx4 v6, s[40:43], s48 offen offset:1024 nt lds\n\t"
      "v_mfma_f32_16x16x32_bf16 v[24:27], v[232:235], a[40:43], v[24:27]\n\t"
      "v_mfma_f32_16x16x32_bf16 v[24:27], v[248:251], a[44:47], v[24:27]\n\t"
      "v_mfma_f32_16x16x32_bf16 v[28:31], v[172:175], a[24:27], v[28:31]\n\t"
      "v_mfma_f32_16x16x32_bf16 v[28:31], v[188:191], a[28:31], v[28:31]\n\t"
      "v_cvt_pk_bf16_f32 v42, v20, v21\n\t"
      "v_cvt_pk_bf16_f32 v43, v22, v23\n\t"
      "v_mfma_f32_16x16x32_bf16 v[28:31], v[204:207], a[32:35], v[28:31]\n\t"
      "ds_write_b64 v0, v[42:43] offset:12576\n\t"
      "v_mfma_f32_16x16x32_bf16 v[28:31], v[220:223], a[36:39], v[28:31]\n\t"
      "v_mfma_f32_16x16x32_bf16 v[28:31], v[236:239], a[40:43], v[28:31]\n\t"
      "v_mfma_f32_16x16x32_bf16 v[28:31], v[252:255], a[44:47], v[28:31]\n\t"
      "v_mfma_f32_16x16x32_bf16 v[32:35], a[108:111], a[24:27], v[32:35]\n\t"
      "v_mfma_f32_16x16x32_bf16 v[32:35], a[116:119], a[28:31], v[32:35]\n\t"
      "v_cvt_pk_bf16_f32 v44, v24, v25\n\t"
      "v_cvt_pk_bf16_f32 v45, v26, v27\n\t"
      "v_mfma_f32_16x16x32_bf16 v[32:35], a[124:127], a[32:35], v[32:35]\n\t"
      "ds_write_b64 v0, v[44:45] offset:12608\n\t"
      "v_mfma_f32_16x16x32_bf16 v[32:35], a[132:135], a[36:39], v[32:35]\n\t"
      "buffer_load_dwordx4 v7, s[40:43], s48 offen offset:2048 nt lds\n\t"
      "v_mfma_f32_16x16x32_bf16 v[32:35], a[140:143], a[40:43], v[32:35]\n\t"
      "s_add_u32 s50, s50, 12288\n\t"
      "v_mfma_f32_16x16x32_bf16 v[32:35], a[148:151], a[44:47], v[32:35]\n\t"
      "v_cvt_pk_bf16_f32 v46, v28, v29\n\t"
      "v_cvt_pk_bf16_f32 v47, v30, v31\n\t"
      "v_mfma_f32_16x16x32_bf16 v[36:39], a[112:115], a[24:27], v[36:39]\n\t"
      "ds_write_b64 v0, v[46:47] offset:12640\n\t"
      "v_mfma_f32_16x16x32_bf16 v[36:39], a[120:123], a[28:31], v[36:39]\n\t"
      "s_cmp_eq_u32 s50, 160256\n\t"
      "s_cselect_b32 s50, 25088, s50\n\t"
      "v_mfma_f32_16x16x32_bf16 v[36:39], a[128:131], a[32:35], v[36:39]\n\t"
      "s_sub_u32 s52, s52, 1\n\t"
      "s_cmp_lg_u32 s52, 0\n\t"
      "v_mfma_f32_16x16x32_bf16 v[36:39], a[136:139], a[36:39], v[36:39]\n\t"
      "v_mfma_f32_16x16x32_bf16 v[36:39], a[144:147], a[40:43], v[36:39]\n\t"
      "v_cvt_pk_bf16_f32 v48, v32, v33\n\t"
      "v_cvt_pk_bf16_f32 v49, v34, v35\n\t"
      "v_mfma_f32_16x16x32_bf16 v[36:39], a[152:155], a[44:47], v[36:39]\n\t"
      "ds_write_b64 v0, v[48:49] offset:12672\n\t"
      "s_cbranch_scc1 .Lloop3_%=\n\t"
      ".Lendc_%=:\n\t"
      "s_cmp_eq_u32 s48, 113221632\n\t"
      "s_cbranch_scc1 .Lwrap3_%=\n\t"
      ".Lwrapped3_%=:\n\t"
      "v_mfma_f32_16x16x32_bf16 v[16:19], v[64:67], a[0:3], 0\n\t"
      "ds_read_b128 a[24:27], v58 offset:256\n\t"
      "v_mfma_f32_16x16x32_bf16 v[16:19], v[80:83], a[4:7], v[16:19]\n\t"
      "ds_read_b128 a[28:31], v59 offset:256\n\t"
      "v_mfma_f32_16x16x32_bf16 v[16:19], v[96:99], a[8:11], v[16:19]\n\t"
      "ds_read_b128 a[32:35], v56 offset:512\n\t"
      "v_mfma_f32_16x16x32_bf16 v[16:19], v[112:115], a[12:15], v[16:19]\n\t"
      "ds_read_b128 a[36:39], v57 offset:512\n\t"
      "v_mfma_f32_16x16x32_bf16 v[16:19], v[128:131], a[16:19], v[16:19]\n\t"
      "ds_read_b128 a[40:43], v58 offset:512\n\t"
      "v_mfma_f32_16x16x32_bf16 v[16:19], v[144:147], a[20:23], v[16:19]\n\t"
      "ds_read_b128 a[44:47], v59 offset:512\n\t"
      "v_mfma_f32_16x16x32_bf16 v[20:23], v[68:71], a[0:3], 0\n\t"
      "v_cvt_pk_bf16_f32 v50, v36, v37\n\t"
      "v_cvt_pk_bf16_f32 v51, v38, v39\n\t"
      "v_mfma_f32_16x16x32_bf16 v[20:23], v[84:87], a[4:7], v[20:23]\n\t"
      "ds_write_b64 v0, v[50:51] offset:12704\n\t"
      "v_mfma_f32_16x16x32_bf16 v[20:23], v[100:103], a[8:11], v[20:23]\n\t"
      "v_add_u32 v52, s51, v15\n\t"
      "v_xor_b32 v53, 64, v52\n\t"
      "v_mfma_f32_16x16x32_bf16 v[20:23], v[116:119], a[12:15], v[20:23]\n\t"
      "v_xor_b32 v54, 128, v52\n\t"
      "v_xor_b32 v55, 192, v52\n\t"
      "v_mfma_f32_16x16x32_bf16 v[20:23], v[132:135], a[16:19], v[20:23]\n\t"
      "s_add_u32 s58, s58, 786432\n\t"
      "s_lshr_b32 s59, s59, 1\n\t"
      "v_mfma_f32_16x16x32_bf16 v[20:23], v[148:151], a[20:23], v[20:23]\n\t"
      "s_bitcmp1_b32 s59, 0\n\t"
      "s_cselect_b32 s84, 24576, 0\n\t"
      "s_add_u32 s84, s84, s58\n\t"
      "v_mfma_f32_16x16x32_bf16 v[24:27], v[72:75], a[0:3], 0\n\t"
      "s_add_u32 s58, s58, 786432\n\t"
      "s_lshr_b32 s59, s59, 1\n\t"
      "v_mfma_f32_16x16x32_bf16 v[24:27], v[88:91], a[4:7], v[24:27]\n\t"
      "s_bitcmp1_b32 s59, 0\n\t"
      "s_cselect_b32 s85, 24576, 0\n\t"
      "s_add_u32 s85, s85, s58\n\t"
      "v_mfma_f32_16x16x32_bf16 v[24:27], v[104:107], a[8:11], v[24:27]\n\t"
      "s_waitcnt vmcnt(12)\n\t"
      "s_waitcnt lgkmcnt(0)\n\t"
      "s_barrier\n\t"
      "v_mfma_f32_16x16x32_bf16 v[24:27], v[120:123], a[12:15], v[24:27]\n\t"
      "v_mfma_f32_16x16x32_bf16 v[24:27], v[136:139], a[16:19], v[24:27]\n\t"
      "v_mfma_f32_16x16x32_bf16 v[24:27], v[152:155], a[20:23], v[24:27]\n\t"
      "v_mfma_f32_16x16x32_bf16 v[28:31], v[76:79], a[0:3], 0\n\t"
      "ds_read_b128 a[188:191], v8 offset:12544\n\t"
      "v_mfma_f32_16x16x32_bf16 v[28:31], v[92:95], a[4:7], v[28:31]\n\t"
      "ds_read_b128 a[192:195], v60 offset:13568\n\t"
      "v_mfma_f32_16x16x32_bf16 v[28:31], v[108:111], a[8:11], v[28:31]\n\t"
      "ds_read_b128 a[196:199], v61 offset:14592\n\t"
      "v_mfma_f32_16x16x32_bf16 v[28:31], v[124:127], a[12:15], v[28:31]\n\t"
      "v_mfma_f32_16x16x32_bf16 v[28:31], v[140:143], a[16:19], v[28:31]\n\t"
      "v_mfma_f32_16x16x32_bf16 v[28:31], v[156:159], a[20:23], v[28:31]\n\t"
      "v_mfma_f32_16x16x32_bf16 v[32:35], a[60:63], a[0:3], 0\n\t"
      "v_mfma_f32_16x16x32_bf16 v[32:35], a[68:71], a[4:7], v[32:35]\n\t"
      "v_mfma_f32_16x16x32_bf16 v[32:35], a[76:79], a[8:11], v[32:35]\n\t"
      "v_mfma_f32_16x16x32_bf16 v[32:35], a[84:87], a[12:15], v[32:35]\n\t"
      "v_mfma_f32_16x16x32_bf16 v[32:35], a[92:95], a[16:19], v[32:35]\n\t"
      "v_mfma_f32_16x16x32_bf16 v[32:35], a[100:103], a[20:23], v[32:35]\n\t"
      "v_mfma_f32_16x16x32_bf16 v[36:39], a[64:67], a[0:3], 0\n\t"
      "v_mfma_f32_16x16x32_bf16 v[36:39], a[72:75], a[4:7], v[36:39]\n\t"
      "v_mfma_f32_16x16x32_bf16 v[36:39], a[80:83], a[8:11], v[36:39]\n\t"
      "v_mfma_f32_16x16x32_bf16 v[36:39], a[88:91], a[12:15], v[36:39]\n\t"
      "v_mfma_f32_16x16x32_bf16 v[36:39], a[96:99], a[16:19], v[36:39]\n\t"
      "v_mfma_f32_16x16x32_bf16 v[36:39], a[104:107], a[20:23], v[36:39]\n\t"
      "v_mfma_f32_16x16x32_bf16 v[16:19], v[160:163], a[24:27], v[16:19]\n\t"
      "ds_read_b128 a[0:3], v52 offset:0\n\t"
      "v_mfma_f32_16x16x32_bf16 v[16:19], v[176:179], a[28:31], v[16:19]\n\t"
      "ds_read_b128 a[4:7], v53 offset:0\n\t"
      "v_mfma_f32_16x16x32_bf16 v[16:19], v[192:195], a[32:35], v[16:19]\n\t"
      "ds_read_b128 a[8:11], v54 offset:0\n\t"
      "v_mfma_f32_16x16x32_bf16 v[16:19], v[208:211], a[36:39], v[16:19]\n\t"
      "ds_read_b128 a[12:15], v55 offset:0\n\t"
      "v_mfma_f32_16x16x32_bf16 v[16:19], v[224:227], a[40:43], v[16:19]\n\t"
      "ds_read_b128 a[16:19], v52 offset:256\n\t"
      "v_mfma_f32_16x16x32_bf16 v[16:19], v[240:243], a[44:47], v[16:19]\n\t"
      "ds_read_b128 a[20:23], v53 offset:256\n\t"
      "v_mfma_f32_16x16x32_bf16 v[20:23], v[164:167], a[24:27], v[20:23]\n\t"
      "s_add_u32 s51, s51, 12288\n\t"
      "s_cmp_eq_u32 s51, 160256\n\t"
      "s_cselect_b32 s51, 25088, s51\n\t"
      "v_mfma_f32_16x16x32_bf16 v[20:23], v[180:183], a[28:31], v[20:23]\n\t"
      "v_mfma_f32_16x16x32_bf16 v[20:23], v[196:199], a[32:35], v[20:23]\n\t"
      "v_mfma_f32_16x16x32_bf16 v[20:23], v[212:215], a[36:39], v[20:23]\n\t"
      "v_mfma_f32_16x16x32_bf16 v[20:23], v[228:231], a[40:43], v[20:23]\n\t"
      "s_waitcnt lgkmcnt(0)\n\t"
      "v_mfma_f32_16x16x32_bf16 v[20:23], v[244:247], a[44:47], v[20:23]\n\t"
      "v_mfma_f32_16x16x32_bf16 v[24:27], v[168:171], a[24:27], v[24:27]\n\t"
      "v_mfma_f32_16x16x32_bf16 v[24:27], v[184:187], a[28:31], v[24:27]\n\t"
      "v_cvt_pk_bf16_f32 v40, v16, v17\n\t"
      "v_cvt_pk_bf16_f32 v41, v18, v19\n\t"
      "v_mfma_f32_16x16x32_bf16 v[24:27], v[200:203], a[32:35], v[24:27]\n\t"
      "ds_write_b64 v0, v[40:41] offset:0\n\t"
      "v_mfma_f32_16x16x32_bf16 v[24:27], v[216:219], a[36:39], v[24:27]\n\t"
      "v_mfma_f32_16x16x32_bf16 v[24:27], v[232:235], a[40:43], v[24:27]\n\t"
      "v_mfma_f32_16x16x32_bf16 v[24:27], v[248:251], a[44:47], v[24:27]\n\t"
      "v_mfma_f32_16x16x32_bf16 v[28:31], v[172:175], a[24:27], v[28:31]\n\t"
      "v_mfma_f32_16x16x32_bf16 v[28:31], v[188:191], a[28:31], v[28:31]\n\t"
      "v_cvt_pk_bf16_f32 v42, v20, v21\n\t"
      "v_cvt_pk_bf16_f32 v43, v22, v23\n\t"
      "v_mfma_f32_16x16x32_bf16 v[28:31], v[204:207], a[32:35], v[28:31]\n\t"
      "ds_write_b64 v0, v[42:43] offset:32\n\t"
      "v_mfma_f32_16x16x32_bf16 v[28:31], v[220:223], a[36:39], v[28:31]\n\t"
      "v_mfma_f32_16x16x32_bf16 v[28:31], v[236:239], a[40:43], v[28:31]\n\t"
      "v_mfma_f32_16x16x32_bf16 v[28:31], v[252:255], a[44:47], v[28:31]\n\t"
      "v_mfma_f32_16x16x32_bf16 v[32:35], a[108:111], a[24:27], v[32:35]\n\t"
      "v_mfma_f32_16x16x32_bf16 v[32:35], a[116:119], a[28:31], v[32:35]\n\t"
      "v_cvt_pk_bf16_f32 v44, v24, v25\n\t"
      "v_cvt_pk_bf16_f32 v45, v26, v27\n\t"
      "v_mfma_f32_16x16x32_bf16 v[32:35], a[124:127], a[32:35], v[32:35]\n\t"
      "ds_write_b64 v0, v[44:45] offset:64\n\t"
      "v_mfma_f32_16x16x32_bf16 v[32:35], a[132:135], a[36:39], v[32:35]\n\t"
      "v_mfma_f32_16x16x32_bf16 v[32:35], a[140:143], a[40:43], v[32:35]\n\t"
      "v_mfma_f32_16x16x32_bf16 v[32:35], a[148:151], a[44:47], v[32:35]\n\t"
      "v_cvt_pk_bf16_f32 v46, v28, v29\n\t"
      "v_cvt_pk_bf16_f32 v47, v30, v31\n\t"
      "v_mfma_f32_16x16x32_bf16 v[36:39], a[112:115], a[24:27], v[36:39]\n\t"
      "ds_write_b64 v0, v[46:47] offset:96\n\t"
      "v_mfma_f32_16x16x32_bf16 v[36:39], a[120:123], a[28:31], v[36:39]\n\t"
      "v_mfma_f32_16x16x32_bf16 v[36:39], a[128:131], a[32:35], v[36:39]\n\t"
      "v_mfma_f32_16x16x32_bf16 v[36:39], a[136:139], a[36:39], v[36:39]\n\t"
      "v_mfma_f32_16x16x32_bf16 v[36:39], a[144:147], a[40:43], v[36:39]\n\t"
      "v_cvt_pk_bf16_f32 v48, v32, v33\n\t"
      "v_cvt_pk_bf16_f32 v49, v34, v35\n\t"
      "v_mfma_f32_16x16x32_bf16 v[36:39], a[152:155], a[44:47], v[36:39]\n\t"
      "ds_write_b64 v0, v[48:49] offset:128\n\t"
      "v_mfma_f32_16x16x32_bf16 v[16:19], v[64:67], a[0:3], 0\n\t"
      "ds_read_b128 a[24:27], v54 offset:256\n\t"
      "v_mfma_f32_16x16x32_bf16 v[16:19], v[80:83], a[4:7], v[16:19]\n\t"
      "ds_read_b128 a[28:31], v55 offset:256\n\t"
      "v_mfma_f32_16x16x32_bf16 v[16:19], v[96:99], a[8:11], v[16:19]\n\t"
      "ds_read_b128 a[32:35], v52 offset:512\n\t"
      "v_mfma_f32_16x16x32_bf16 v[16:19], v[112:115], a[12:15], v[16:19]\n\t"
      "ds_read_b128 a[36:39], v53 offset:512\n\t"
      "v_mfma_f32_16x16x32_bf16 v[16:19], v[128:131], a[16:19], v[16:19]\n\t"
      "ds_read_b128 a[40:43], v54 offset:512\n\t"
      "v_mfma_f32_16x16x32_bf16 v[16:19], v[144:147], a[20:23], v[16:19]\n\t"
      "ds_read_b128 a[44:47], v55 offset:512\n\t"
      "v_mfma_f32_16x16x32_bf16 v[20:23], v[68:71], a[0:3], 0\n\t"
      "v_cvt_pk_bf16_f32 v50, v36, v37\n\t"
      "v_cvt_pk_bf16_f32 v51, v38, v39\n\t"
      "v_mfma_f32_16x16x32_bf16 v[20:23], v[84:87], a[4:7], v[20:23]\n\t"
      "ds_write_b64 v0, v[50:51] offset:160\n\t"
      "v_mfma_f32_16x16x32_bf16 v[20:23], v[100:103], a[8:11], v[20:23]\n\t"
      "v_add_u32 v56, s51, v15\n\t"
      "v_xor_b32 v57, 64, v56\n\t"
      "v_mfma_f32_16x16x32_bf16 v[20:23], v[116:119], a[12:15], v[20:23]\n\t"
      "v_xor_b32 v58, 128, v56\n\t"
      "v_xor_b32 v59, 192, v56\n\t"
      "v_mfma_f32_16x16x32_bf16 v[20:23], v[132:135], a[16:19], v[20:23]\n\t"
      "v_mfma_f32_16x16x32_bf16 v[20:23], v[148:151], a[20:23], v[20:23]\n\t"
      "v_mfma_f32_16x16x32_bf16 v[24:27], v[72:75], a[0:3], 0\n\t"
      "v_mfma_f32_16x16x32_bf16 v[24:27], v[88:91], a[4:7], v[24:27]\n\t"
      "v_mfma_f32_16x16x32_bf16 v[24:27], v[104:107], a[8:11], v[24:27]\n\t"
      "s_waitcnt vmcnt(6)\n\t"
      "s_waitcnt lgkmcnt(0)\n\t"
      "s_barrier\n\t"
      "v_mfma_f32_16x16x32_bf16 v[24:27], v[120:123], a[12:15], v[24:27]\n\t"
      "v_mfma_f32_16x16x32_bf16 v[24:27], v[136:139], a[16:19], v[24:27]\n\t"
      "v_mfma_f32_16x16x32_bf16 v[24:27], v[152:155], a[20:23], v[24:27]\n\t"
      "v_mfma_f32_16x16x32_bf16 v[28:31], v[76:79], a[0:3], 0\n\t"
      "ds_read_b128 a[200:203], v8 offset:0\n\t"
      "v_mfma_f32_16x16x32_bf16 v[28:31], v[92:95], a[4:7], v[28:31]\n\t"
      "ds_read_b128 a[204:207], v60 offset:1024\n\t"
      "v_mfma_f32_16x16x32_bf16 v[28:31], v[108:111], a[8:11], v[28:31]\n\t"
      "ds_read_b128 a[208:211], v61 offset:2048\n\t"
      "v_mfma_f32_16x16x32_bf16 v[28:31], v[124:127], a[12:15], v[28:31]\n\t"
      "v_mfma_f32_16x16x32_bf16 v[28:31], v[140:143], a[16:19], v[28:31]\n\t"
      "v_mfma_f32_16x16x32_bf16 v[28:31], v[156:159], a[20:23], v[28:31]\n\t"
      "v_mfma_f32_16x16x32_bf16 v[32:35], a[60:63], a[0:3], 0\n\t"
      "v_mfma_f32_16x16x32_bf16 v[32:35], a[68:71], a[4:7], v[32:35]\n\t"
      "v_mfma_f32_16x16x32_bf16 v[32:35], a[76:79], a[8:11], v[32:35]\n\t"
      "v_mfma_f32_16x16x32_bf16 v[32:35], a[84:87], a[12:15], v[32:35]\n\t"
      "v_mfma_f32_16x16x32_bf16 v[32:35], a[92:95], a[16:19], v[32:35]\n\t"
      "v_mfma_f32_16x16x32_bf16 v[32:35], a[100:103], a[20:23], v[32:35]\n\t"
      "v_mfma_f32_16x16x32_bf16 v[36:39], a[64:67], a[0:3], 0\n\t"
      "v_mfma_f32_16x16x32_bf16 v[36:39], a[72:75], a[4:7], v[36:39]\n\t"
      "v_mfma_f32_16x16x32_bf16 v[36:39], a[80:83], a[8:11], v[36:39]\n\t"
      "v_mfma_f32_16x16x32_bf16 v[36:39], a[88:91], a[12:15], v[36:39]\n\t"
      "v_mfma_f32_16x16x32_bf16 v[36:39], a[96:99], a[16:19], v[36:39]\n\t"
      "v_mfma_f32_16x16x32_bf16 v[36:39], a[104:107], a[20:23], v[36:39]\n\t"
      "v_mfma_f32_16x16x32_bf16 v[16:19], v[160:163], a[24:27], v[16:19]\n\t"
      "ds_read_b128 a[0:3], v56 offset:0\n\t"
      "v_mfma_f32_16x16x32_bf16 v[16:19], v[176:179], a[28:31], v[16:19]\n\t"
      "ds_read_b128 a[4:7], v57 offset:0\n\t"
      "v_mfma_f32_16x16x32_bf16 v[16:19], v[192:195], a[32:35], v[16:19]\n\t"
      "ds_read_b128 a[8:11], v58 offset:0\n\t"
      "v_mfma_f32_16x16x32_bf16 v[16:19], v[208:211], a[36:39], v[16:19]\n\t"
      "ds_read_b128 a[12:15], v59 offset:0\n\t"
      "v_mfma_f32_16x16x32_bf16 v[16:19], v[224:227], a[40:43], v[16:19]\n\t"
      "ds_read_b128 a[16:19], v56 offset:256\n\t"
      "v_mfma_f32_16x16x32_bf16 v[16:19], v[240:243], a[44:47], v[16:19]\n\t"
      "ds_read_b128 a[20:23], v57 offset:256\n\t"
      "v_mfma_f32_16x16x32_bf16 v[20:23], v[164:167], a[24:27], v[20:23]\n\t"
      "s_add_u32 s51, s51, 12288\n\t"
      "s_cmp_eq_u32 s51, 160256\n\t"
      "s_cselect_b32 s51, 25088, s51\n\t"
      "v_mfma_f32_16x16x32_bf16 v[20:23], v[180:183], a[28:31], v[20:23]\n\t"
      "v_mfma_f32_16x16x32_bf16 v[20:23], v[196:199], a[32:35], v[20:23]\n\t"
      "buffer_store_dwordx4 a[188:191], v12, s[44:47], s49 offen sc1\n\t"
      "v_mfma_f32_16x16x32_bf16 v[20:23], v[212:215], a[36:39], v[20:23]\n\t"
      "v_mfma_f32_16x16x32_bf16 v[20:23], v[228:231], a[40:43], v[20:23]\n\t"
      "s_waitcnt lgkmcnt(0)\n\t"
      "v_mfma_f32_16x16x32_bf16 v[20:23], v[244:247], a[44:47], v[20:23]\n\t"
      "v_mfma_f32_16x16x32_bf16 v[24:27], v[168:171], a[24:27], v[24:27]\n\t"
      "v_mfma_f32_16x16x32_bf16 v[24:27], v[184:187], a[28:31], v[24:27]\n\t"
      "v_cvt_pk_bf16_f32 v40, v16, v17\n\t"
      "v_cvt_pk_bf16_f32 v41, v18, v19\n\t"
      "v_mfma_f32_16x16x32_bf16 v[24:27], v[200:203], a[32:35], v[24:27]\n\t"
      "ds_write_b64 v0, v[40:41] offset:12544\n\t"
      "v_mfma_f32_16x16x32_bf16 v[24:27], v[216:219], a[36:39], v[24:27]\n\t"
      "v_mfma_f32_16x16x32_bf16 v[24:27], v[232:235], a[40:43], v[24:27]\n\t"
      "v_mfma_f32_16x16x32_bf16 v[24:27], v[248:251], a[44:47], v[24:27]\n\t"
      "v_mfma_f32_16x16x32_bf16 v[28:31], v[172:175], a[24:27], v[28:31]\n\t"
      "v_mfma_f32_16x16x32_bf16 v[28:31], v[188:191], a[28:31], v[28:31]\n\t"
      "v_cvt_pk_bf16_f32 v42, v20, v21\n\t"
      "v_cvt_pk_bf16_f32 v43, v22, v23\n\t"
      "v_mfma_f32_16x16x32_bf16 v[28:31], v[204:207], a[32:35], v[28:31]\n\t"
      "ds_write_b64 v0, v[42:43] offset:12576\n\t"
      "v_mfma_f32_16x16x32_bf16 v[28:31], v[220:223], a[36:39], v[28:31]\n\t"
      "v_mfma_f32_16x16x32_bf16 v[28:31], v[236:239], a[40:43], v[28:31]\n\t"
      "buffer_store_dwordx4 a[192:195], v13, s[44:47], s49 offen offset:1024 sc1\n\t"
      "v_mfma_f32_16x16x32_bf16 v[28:31], v[252:255], a[44:47], v[28:31]\n\t"
      "v_mfma_f32_16x16x32_bf16 v[32:35], a[108:111], a[24:27], v[32:35]\n\t"
      "v_mfma_f32_16x16x32_bf16 v[32:35], a[116:119], a[28:31], v[32:35]\n\t"
      "v_cvt_pk_bf16_f32 v44, v24, v25\n\t"
      "v_cvt_pk_bf16_f32 v45, v26, v27\n\t"
      "v_mfma_f32_16x16x32_bf16 v[32:35], a[124:127], a[32:35], v[32:35]\n\t"
      "ds_write_b64 v0, v[44:45] offset:12608\n\t"
      "v_mfma_f32_16x16x32_bf16 v[32:35], a[132:135], a[36:39], v[32:35]\n\t"
      "v_mfma_f32_16x16x32_bf16 v[32:35], a[140:143], a[40:43], v[32:35]\n\t"
      "v_mfma_f32_16x16x32_bf16 v[32:35], a[148:151], a[44:47], v[32:35]\n\t"
      "v_cvt_pk_bf16_f32 v46, v28, v29\n\t"
      "v_cvt_pk_bf16_f32 v47, v30, v31\n\t"
      "v_mfma_f32_16x16x32_bf16 v[36:39], a[112:115], a[24:27], v[36:39]\n\t"
      "ds_write_b64 v0, v[46:47] offset:12640\n\t"
      "v_mfma_f32_16x16x32_bf16 v[36:39], a[120:123], a[28:31], v[36:39]\n\t"
      "v_mfma_f32_16x16x32_bf16 v[36:39], a[128:131], a[32:35], v[36:39]\n\t"
      "v_mfma_f32_16x16x32_bf16 v[36:39], a[136:139], a[36:39], v[36:39]\n\t"
      "v_mfma_f32_16x16x32_bf16 v[36:39], a[144:147], a[40:43], v[36:39]\n\t"
      "v_cvt_pk_bf16_f32 v48, v32, v33\n\t"
      "v_cvt_pk_bf16_f32 v49, v34, v35\n\t"
      "v_mfma_f32_16x16x32_bf16 v[36:39], a[152:155], a[44:47], v[36:39]\n\t"
      "ds_write_b64 v0, v[48:49] offset:12672\n\t"
      "v_mfma_f32_16x16x32_bf16 v[16:19], v[64:67], a[0:3], 0\n\t"
      "ds_read_b128 a[24:27], v58 offset:256\n\t"
      "v_mfma_f32_16x16x32_bf16 v[16:19], v[80:83], a[4:7], v[16:19]\n\t"
      "ds_read_b128 a[28:31], v59 offset:256\n\t"
      "v_mfma_f32_16x16x32_bf16 v[16:19], v[96:99], a[8:11], v[16:19]\n\t"
      "ds_read_b128 a[32:35], v56 offset:512\n\t"
      "v_mfma_f32_16x16x32_bf16 v[16:19], v[112:115], a[12:15], v[16:19]\n\t"
      "ds_read_b128 a[36:39], v57 offset:512\n\t"
      "buffer_store_dwordx4 a[196:199], v14, s[44:47], s49 offen offset:2048 sc1\n\t"
      "v_mfma_f32_16x16x32_bf16 v[16:19], v[128:131], a[16:19], v[16:19]\n\t"
      "ds_read_b128 a[40:43], v58 offset:512\n\t"
      "v_mfma_f32_16x16x32_bf16 v[16:19], v[144:147], a[20:23], v[16:19]\n\t"
      "ds_read_b128 a[44:47], v59 offset:512\n\t"
      "v_mfma_f32_16x16x32_bf16 v[20:23], v[68:71], a[0:3], 0\n\t"
      "v_cvt_pk_bf16_f32 v50, v36, v37\n\t"
      "v_cvt_pk_bf16_f32 v51, v38, v39\n\t"
      "v_mfma_f32_16x16x32_bf16 v[20:23], v[84:87], a[4:7], v[20:23]\n\t"
      "ds_write_b64 v0, v[50:51] offset:12704\n\t"
      "v_mfma_f32_16x16x32_bf16 v[20:23], v[100:103], a[8:11], v[20:23]\n\t"
      "v_add_u32 v52, s51, v15\n\t"
      "v_xor_b32 v53, 64, v52\n\t"
      "v_mfma_f32_16x16x32_bf16 v[20:23], v[116:119], a[12:15], v[20:23]\n\t"
      "v_xor_b32 v54, 128, v52\n\t"
      "v_xor_b32 v55, 192, v52\n\t"
      "v_mfma_f32_16x16x32_bf16 v[20:23], v[132:135], a[16:19], v[20:23]\n\t"
      "v_mfma_f32_16x16x32_bf16 v[20:23], v[148:151], a[20:23], v[20:23]\n\t"
      "v_mfma_f32_16x16x32_bf16 v[24:27], v[72:75], a[0:3], 0\n\t"
      "buffer_store_dwordx4 a[200:203], v9, s[44:47], s84 offen sc1\n\t"
      "v_mfma_f32_16x16x32_bf16 v[24:27], v[88:91], a[4:7], v[24:27]\n\t"
      "v_mfma_f32_16x16x32_bf16 v[24:27], v[104:107], a[8:11], v[24:27]\n\t"
      "s_waitcnt vmcnt(4)\n\t"
      "s_waitcnt lgkmcnt(0)\n\t"
      "s_barrier\n\t"
      "v_mfma_f32_16x16x32_bf16 v[24:27], v[120:123], a[12:15], v[24:27]\n\t"
      "v_mfma_f32_16x16x32_bf16 v[24:27], v[136:139], a[16:19], v[24:27]\n\t"
      "v_mfma_f32_16x16x32_bf16 v[24:27], v[152:155], a[20:23], v[24:27]\n\t"
      "v_mfma_f32_16x16x32_bf16 v[28:31], v[76:79], a[0:3], 0\n\t"
      "ds_read_b128 a[212:215], v8 offset:12544\n\t"
      "v_mfma_f32_16x16x32_bf16 v[28:31], v[92:95], a[4:7], v[28:31]\n\t"
      "ds_read_b128 a[216:219], v60 offset:13568\n\t"
      "v_mfma_f32_16x16x32_bf16 v[28:31], v[108:111], a[8:11], v[28:31]\n\t"
      "ds_read_b128 a[220:223], v61 offset:14592\n\t"
      "v_mfma_f32_16x16x32_bf16 v[28:31], v[124:127], a[12:15], v[28:31]\n\t"
      "v_mfma_f32_16x16x32_bf16 v[28:31], v[140:143], a[16:19], v[28:31]\n\t"
      "v_mfma_f32_16x16x32_bf16 v[28:31], v[156:159], a[20:23], v[28:31]\n\t"
      "v_mfma_f32_16x16x32_bf16 v[32:35], a[60:63], a[0:3], 0\n\t"
      "buffer_store_dwordx4 a[204:207], v10, s[44:47], s84 offen offset:1024 sc1\n\t"
      "v_mfma_f32_16x16x32_bf16 v[32:35], a[68:71], a[4:7], v[32:35]\n\t"
      "v_mfma_f32_16x16x32_bf16 v[32:35], a[76:79], a[8:11], v[32:35]\n\t"
      "v_mfma_f32_16x16x32_bf16 v[32:35], a[84:87], a[12:15], v[32:35]\n\t"
      "v_mfma_f32_16x16x32_bf16 v[32:35], a[92:95], a[16:19], v[32:35]\n\t"
      "v_mfma_f32_16x16x32_bf16 v[32:35], a[100:103], a[20:23], v[32:35]\n\t"
      "v_mfma_f32_16x16x32_bf16 v[36:39], a[64:67], a[0:3], 0\n\t"
      "v_mfma_f32_16x16x32_bf16 v[36:39], a[72:75], a[4:7], v[36:39]\n\t"
      "v_mfma_f32_16x16x32_bf16 v[36:39], a[80:83], a[8:11], v[36:39]\n\t"
      "v_mfma_f32_16x16x32_bf16 v[36:39], a[88:91], a[12:15], v[36:39]\n\t"
      "v_mfma_f32_16x16x32_bf16 v[36:39], a[96:99], a[16:19], v[36:39]\n\t"
      "buffer_store_dwordx4 a[208:211], v11, s[44:47], s84 offen offset:2048 sc1\n\t"
      "v_mfma_f32_16x16x32_bf16 v[36:39], a[104:107], a[20:23], v[36:39]\n\t"
      "v_mfma_f32_16x16x32_bf16 v[16:19], v[160:163], a[24:27], v[16:19]\n\t"
      "ds_read_b128 a[0:3], v52 offset:0\n\t"
      "v_mfma_f32_16x16x32_bf16 v[16:19], v[176:179], a[28:31], v[16:19]\n\t"
      "ds_read_b128 a[4:7], v53 offset:0\n\t"
      "v_mfma_f32_16x16x32_bf16 v[16:19], v[192:195], a[32:35], v[16:19]\n\t"
      "ds_read_b128 a[8:11], v54 offset:0\n\t"
      "v_mfma_f32_16x16x32_bf16 v[16:19], v[208:211], a[36:39], v[16:19]\n\t"
      "ds_read_b128 a[12:15], v55 offset:0\n\t"
      "v_mfma_f32_16x16x32_bf16 v[16:19], v[224:227], a[40:43], v[16:19]\n\t"
      "ds_read_b128 a[16:19], v52 offset:256\n\t"
      "v_mfma_f32_16x16x32_bf16 v[16:19], v[240:243], a[44:47], v[16:19]\n\t"
      "ds_read_b128 a[20:23], v53 offset:256\n\t"
      "v_mfma_f32_16x16x32_bf16 v[20:23], v[164:167], a[24:27], v[20:23]\n\t"
      "s_add_u32 s51, s51, 12288\n\t"
      "s_cmp_eq_u32 s51, 160256\n\t"
      "s_cselect_b32 s51, 25088, s51\n\t"
      "v_mfma_f32_16x16x32_bf16 v[20:23], v[180:183], a[28:31], v[20:23]\n\t"
      "s_waitcnt lgkmcnt(8)\n\t"
      "v_mfma_f32_16x16x32_bf16 v[20:23], v[196:199], a[32:35], v[20:23]\n\t"
      "buffer_store_dwordx4 a[212:215], v12, s[44:47], s84 offen sc1\n\t"
      "v_mfma_f32_16x16x32_bf16 v[20:23], v[212:215], a[36:39], v[20:23]\n\t"
      "v_mfma_f32_16x16x32_bf16 v[20:23], v[228:231], a[40:43], v[20:23]\n\t"
      "s_waitcnt lgkmcnt(0)\n\t"
      "v_mfma_f32_16x16x32_bf16 v[20:23], v[244:247], a[44:47], v[20:23]\n\t"
      "v_mfma_f32_16x16x32_bf16 v[24:27], v[168:171], a[24:27], v[24:27]\n\t"
      "v_mfma_f32_16x16x32_bf16 v[24:27], v[184:187], a[28:31], v[24:27]\n\t"
      "v_cvt_pk_bf16_f32 v40, v16, v17\n\t"
      "v_cvt_pk_bf16_f32 v41, v18, v19\n\t"
      "v_mfma_f32_16x16x32_bf16 v[24:27], v[200:203], a[32:35], v[24:27]\n\t"
      "ds_write_b64 v0, v[40:41] offset:0\n\t"
      "v_mfma_f32_16x16x32_bf16 v[24:27], v[216:219], a[36:39], v[24:27]\n\t"
      "v_mfma_f32_16x16x32_bf16 v[24:27], v[232:235], a[40:43], v[24:27]\n\t"
      "v_mfma_f32_16x16x32_bf16 v[24:27], v[248:251], a[44:47], v[24:27]\n\t"
      "v_mfma_f32_16x16x32_bf16 v[28:31], v[172:175], a[24:27], v[28:31]\n\t"
      "v_mfma_f32_16x16x32_bf16 v[28:31], v[188:191], a[28:31], v[28:31]\n\t"
      "v_cvt_pk_bf16_f32 v42, v20, v21\n\t"
      "v_cvt_pk_bf16_f32 v43, v22, v23\n\t"
      "v_mfma_f32_16x16x32_bf16 v[28:31], v[204:207], a[32:35], v[28:31]\n\t"
      "ds_write_b64 v0, v[42:43] offset:32\n\t"
      "v_mfma_f32_16x16x32_bf16 v[28:31], v[220:223], a[36:39], v[28:31]\n\t"
      "v_mfma_f32_16x16x32_bf16 v[28:31], v[236:239], a[40:43], v[28:31]\n\t"
      "buffer_store_dwordx4 a[216:219], v13, s[44:47], s84 offen offset:1024 sc1\n\t"
      "v_mfma_f32_16x16x32_bf16 v[28:31], v[252:255], a[44:47], v[28:31]\n\t"
      "v_mfma_f32_16x16x32_bf16 v[32:35], a[108:111], a[24:27], v[32:35]\n\t"
      "v_mfma_f32_16x16x32_bf16 v[32:35], a[116:119], a[28:31], v[32:35]\n\t"
      "v_cvt_pk_bf16_f32 v44, v24, v25\n\t"
      "v_cvt_pk_bf16_f32 v45, v26, v27\n\t"
      "v_mfma_f32_16x16x32_bf16 v[32:35], a[124:127], a[32:35], v[32:35]\n\t"
      "ds_write_b64 v0, v[44:45] offset:64\n\t"
      "v_mfma_f32_16x16x32_bf16 v[32:35], a[132:135], a[36:39], v[32:35]\n\t"
      "v_mfma_f32_16x16x32_bf16 v[32:35], a[140:143], a[40:43], v[32:35]\n\t"
      "v_mfma_f32_16x16x32_bf16 v[32:35], a[148:151], a[44:47], v[32:35]\n\t"
      "v_cvt_pk_bf16_f32 v46, v28, v29\n\t"
      "v_cvt_pk_bf16_f32 v47, v30, v31\n\t"
      "v_mfma_f32_16x16x32_bf16 v[36:39], a[112:115], a[24:27], v[36:39]\n\t"
      "ds_write_b64 v0, v[46:47] offset:96\n\t"
      "v_mfma_f32_16x16x32_bf16 v[36:39], a[120:123], a[28:31], v[36:39]\n\t"
      "v_mfma_f32_16x16x32_bf16 v[36:39], a[128:131], a[32:35], v[36:39]\n\t"
      "v_mfma_f32_16x16x32_bf16 v[36:39], a[136:139], a[36:39], v[36:39]\n\t"
      "v_mfma_f32_16x16x32_bf16 v[36:39], a[144:147], a[40:43], v[36:39]\n\t"
      "v_cvt_pk_bf16_f32 v48, v32, v33\n\t"
      "v_cvt_pk_bf16_f32 v49, v34, v35\n\t"
      "v_mfma_f32_16x16x32_bf16 v[36:39], a[152:155], a[44:47], v[36:39]\n\t"
      "ds_write_b64 v0, v[48:49] offset:128\n\t"
      "v_mfma_f32_16x16x32_bf16 v[16:19], v[64:67], a[0:3], 0\n\t"
      "ds_read_b128 a[24:27], v54 offset:256\n\t"
      "v_mfma_f32_16x16x32_bf16 v[16:19], v[80:83], a[4:7], v[16:19]\n\t"
      "ds_read_b128 a[28:31], v55 offset:256\n\t"
      "v_mfma_f32_16x16x32_bf16 v[16:19], v[96:99], a[8:11], v[16:19]\n\t"
      "ds_read_b128 a[32:35], v52 offset:512\n\t"
      "v_mfma_f32_16x16x32_bf16 v[16:19], v[112:115], a[12:15], v[16:19]\n\t"
      "ds_read_b128 a[36:39], v53 offset:512\n\t"
      "v_mfma_f32_16x16x32_bf16 v[16:19], v[128:131], a[16:19], v[16:19]\n\t"
      "ds_read_b128 a[40:43], v54 offset:512\n\t"
      "v_mfma_f32_16x16x32_bf16 v[16:19], v[144:147], a[20:23], v[16:19]\n\t"
      "ds_read_b128 a[44:47], v55 offset:512\n\t"
      "v_mfma_f32_16x16x32_bf16 v[20:23], v[68:71], a[0:3], 0\n\t"
      "v_cvt_pk_bf16_f32 v50, v36, v37\n\t"
      "v_cvt_pk_bf16_f32 v51, v38, v39\n\t"
      "v_mfma_f32_16x16x32_bf16 v[20:23], v[84:87], a[4:7], v[20:23]\n\t"
      "ds_write_b64 v0, v[50:51] offset:160\n\t"
      "v_mfma_f32_16x16x32_bf16 v[20:23], v[100:103], a[8:11], v[20:23]\n\t"
      "v_mfma_f32_16x16x32_bf16 v[20:23], v[116:119], a[12:15], v[20:23]\n\t"
      "v_mfma_f32_16x16x32_bf16 v[20:23], v[132:135], a[16:19], v[20:23]\n\t"
      "buffer_store_dwordx4 a[220:223], v14, s[44:47], s84 offen offset:2048 sc1\n\t"
      "v_mfma_f32_16x16x32_bf16 v[20:23], v[148:151], a[20:23], v[20:23]\n\t"
      "v_mfma_f32_16x16x32_bf16 v[24:27], v[72:75], a[0:3], 0\n\t"
      "v_mfma_f32_16x16x32_bf16 v[24:27], v[88:91], a[4:7], v[24:27]\n\t"
      "v_mfma_f32_16x16x32_bf16 v[24:27], v[104:107], a[8:11], v[24:27]\n\t"
      "s_waitcnt lgkmcnt(0)\n\t"
      "s_barrier\n\t"
      "v_mfma_f32_16x16x32_bf16 v[24:27], v[120:123], a[12:15], v[24:27]\n\t"
      "v_mfma_f32_16x16x32_bf16 v[24:27], v[136:139], a[16:19], v[24:27]\n\t"
      "v_mfma_f32_16x16x32_bf16 v[24:27], v[152:155], a[20:23], v[24:27]\n\t"
      "v_mfma_f32_16x16x32_bf16 v[28:31], v[76:79], a[0:3], 0\n\t"
      "ds_read_b128 a[224:227], v8 offset:0\n\t"
      "v_mfma_f32_16x16x32_bf16 v[28:31], v[92:95], a[4:7], v[28:31]\n\t"
      "ds_read_b128 a[228:231], v60 offset:1024\n\t"
      "v_mfma_f32_16x16x32_bf16 v[28:31], v[108:111], a[8:11], v[28:31]\n\t"
      "ds_read_b128 a[232:235], v61 offset:2048\n\t"
      "v_mfma_f32_16x16x32_bf16 v[28:31], v[124:127], a[12:15], v[28:31]\n\t"
      "v_mfma_f32_16x16x32_bf16 v[28:31], v[140:143], a[16:19], v[28:31]\n\t"
      "v_mfma_f32_16x16x32_bf16 v[28:31], v[156:159], a[20:23], v[28:31]\n\t"
      "v_mfma_f32_16x16x32_bf16 v[32:35], a[60:63], a[0:3], 0\n\t"
      "v_mfma_f32_16x16x32_bf16 v[32:35], a[68:71], a[4:7], v[32:35]\n\t"
      "s_waitcnt lgkmcnt(2)\n\t"
      "v_mfma_f32_16x16x32_bf16 v[32:35], a[76:79], a[8:11], v[32:35]\n\t"
      "buffer_store_dwordx4 a[224:227], v9, s[44:47], s85 offen sc1\n\t"
      "v_mfma_f32_16x16x32_bf16 v[32:35], a[84:87], a[12:15], v[32:35]\n\t"
      "v_mfma_f32_16x16x32_bf16 v[32:35], a[92:95], a[16:19], v[32:35]\n\t"
      "v_mfma_f32_16x16x32_bf16 v[32:35], a[100:103], a[20:23], v[32:35]\n\t"
      "v_mfma_f32_16x16x32_bf16 v[36:39], a[64:67], a[0:3], 0\n\t"
      "v_mfma_f32_16x16x32_bf16 v[36:39], a[72:75], a[4:7], v[36:39]\n\t"
      "v_mfma_f32_16x16x32_bf16 v[36:39], a[80:83], a[8:11], v[36:39]\n\t"
      "v_mfma_f32_16x16x32_bf16 v[36:39], a[88:91], a[12:15], v[36:39]\n\t"
      "s_waitcnt lgkmcnt(1)\n\t"
      "v_mfma_f32_16x16x32_bf16 v[36:39], a[96:99], a[16:19], v[36:39]\n\t"
      "buffer_store_dwordx4 a[228:231], v10, s[44:47], s85 offen offset:1024 sc1\n\t"
      "v_mfma_f32_16x16x32_bf16 v[36:39], a[104:107], a[20:23], v[36:39]\n\t"
      "v_mfma_f32_16x16x32_bf16 v[16:19], v[160:163], a[24:27], v[16:19]\n\t"
      "v_mfma_f32_16x16x32_bf16 v[16:19], v[176:179], a[28:31], v[16:19]\n\t"
      "v_mfma_f32_16x16x32_bf16 v[16:19], v[192:195], a[32:35], v[16:19]\n\t"
      "v_mfma_f32_16x16x32_bf16 v[16:19], v[208:211], a[36:39], v[16:19]\n\t"
      "v_mfma_f32_16x16x32_bf16 v[16:19], v[224:227], a[40:43], v[16:19]\n\t"
      "v_mfma_f32_16x16x32_bf16 v[16:19], v[240:243], a[44:47], v[16:19]\n\t"
      "v_mfma_f32_16x16x32_bf16 v[20:23], v[164:167], a[24:27], v[20:23]\n\t"
      "v_mfma_f32_16x16x32_bf16 v[20:23], v[180:183], a[28:31], v[20:23]\n\t"
      "s_waitcnt lgkmcnt(0)\n\t"
      "v_mfma_f32_16x16x32_bf16 v[20:23], v[196:199], a[32:35], v[20:23]\n\t"
      "buffer_store_dwordx4 a[232:235], v11, s[44:47], s85 offen offset:2048 sc1\n\t"
      "v_mfma_f32_16x16x32_bf16 v[20:23], v[212:215], a[36:39], v[20:23]\n\t"
      "v_mfma_f32_16x16x32_bf16 v[20:23], v[228:231], a[40:43], v[20:23]\n\t"
      "v_mfma_f32_16x16x32_bf16 v[20:23], v[244:247], a[44:47], v[20:23]\n\t"
      "v_mfma_f32_16x16x32_bf16 v[24:27], v[168:171], a[24:27], v[24:27]\n\t"
      "v_mfma_f32_16x16x32_bf16 v[24:27], v[184:187], a[28:31], v[24:27]\n\t"
      "v_cvt_pk_bf16_f32 v40, v16, v17\n\t"
      "v_cvt_pk_bf16_f32 v41, v18, v19\n\t"
      "v_mfma_f32_16x16x32_bf16 v[24:27], v[200:203], a[32:35], v[24:27]\n\t"
      "ds_write_b64 v0, v[40:41] offset:12544\n\t"
      "v_mfma_f32_16x16x32_bf16 v[24:27], v[216:219], a[36:39], v[24:27]\n\t"
      "v_mfma_f32_16x16x32_bf16 v[24:27], v[232:235], a[40:43], v[24:27]\n\t"
      "v_mfma_f32_16x16x32_bf16 v[24:27], v[248:251], a[44:47], v[24:27]\n\t"
      "v_mfma_f32_16x16x32_bf16 v[28:31], v[172:175], a[24:27], v[28:31]\n\t"
      "v_mfma_f32_16x16x32_bf16 v[28:31], v[188:191], a[28:31], v[28:31]\n\t"
      "v_cvt_pk_bf16_f32 v42, v20, v21\n\t"
      "v_cvt_pk_bf16_f32 v43, v22, v23\n\t"
      "v_mfma_f32_16x16x32_bf16 v[28:31], v[204:207], a[32:35], v[28:31]\n\t"
      "ds_write_b64 v0, v[42:43] offset:12576\n\t"
      "v_mfma_f32_16x16x32_bf16 v[28:31], v[220:223], a[36:39], v[28:31]\n\t"
      "v_mfma_f32_16x16x32_bf16 v[28:31], v[236:239], a[40:43], v[28:31]\n\t"
      "v_mfma_f32_16x16x32_bf16 v[28:31], v[252:255], a[44:47], v[28:31]\n\t"
      "v_mfma_f32_16x16x32_bf16 v[32:35], a[108:111], a[24:27], v[32:35]\n\t"
      "v_mfma_f32_16x16x32_bf16 v[32:35], a[116:119], a[28:31], v[32:35]\n\t"
      "v_cvt_pk_bf16_f32 v44, v24, v25\n\t"
      "v_cvt_pk_bf16_f32 v45, v26, v27\n\t"
      "v_mfma_f32_16x16x32_bf16 v[32:35], a[124:127], a[32:35], v[32:35]\n\t"
      "ds_write_b64 v0, v[44:45] offset:12608\n\t"
      "v_mfma_f32_16x16x32_bf16 v[32:35], a[132:135], a[36:39], v[32:35]\n\t"
      "v_mfma_f32_16x16x32_bf16 v[32:35], a[140:143], a[40:43], v[32:35]\n\t"
      "v_mfma_f32_16x16x32_bf16 v[32:35], a[148:151], a[44:47], v[32:35]\n\t"
      "v_cvt_pk_bf16_f32 v46, v28, v29\n\t"
      "v_cvt_pk_bf16_f32 v47, v30, v31\n\t"
      "v_mfma_f32_16x16x32_bf16 v[36:39], a[112:115], a[24:27], v[36:39]\n\t"
      "ds_write_b64 v0, v[46:47] offset:12640\n\t"
      "v_mfma_f32_16x16x32_bf16 v[36:39], a[120:123], a[28:31], v[36:39]\n\t"
      "v_mfma_f32_16x16x32_bf16 v[36:39], a[128:131], a[32:35], v[36:39]\n\t"
      "v_mfma_f32_16x16x32_bf16 v[36:39], a[136:139], a[36:39], v[36:39]\n\t"
      "v_mfma_f32_16x16x32_bf16 v[36:39], a[144:147], a[40:43], v[36:39]\n\t"
      "v_cvt_pk_bf16_f32 v48, v32, v33\n\t"
      "v_cvt_pk_bf16_f32 v49, v34, v35\n\t"
      "v_mfma_f32_16x16x32_bf16 v[36:39], a[152:155], a[44:47], v[36:39]\n\t"
      "ds_write_b64 v0, v[48:49] offset:12672\n\t"
      ".Ltail_%=:\n\t"
      "s_nop 6\n\t"
      "v_cvt_pk_bf16_f32 v50, v36, v37\n\t"
      "v_cvt_pk_bf16_f32 v51, v38, v39\n\t"
      "ds_write_b64 v0, v[50:51] offset:12704\n\t"
      "s_cmp_eq_u32 s85, 113221632\n\t"
      "s_cbranch_scc1 .Lwrapo_%=\n\t"
      ".Lwrappedo_%=:\n\t"
      "s_waitcnt lgkmcnt(0)\n\t"
      "s_barrier\n\t"
      "ds_read_b128 a[48:51], v8 offset:12544\n\t"
      "ds_read_b128 a[52:55], v60 offset:13568\n\t"
      "ds_read_b128 a[56:59], v61 offset:14592\n\t"
      "s_waitcnt lgkmcnt(2)\n\t"
      "buffer_store_dwordx4 a[48:51], v12, s[44:47], s85 offen sc1\n\t"
      "s_waitcnt lgkmcnt(1)\n\t"
      "buffer_store_dwordx4 a[52:55], v13, s[44:47], s85 offen offset:1024 sc1\n\t"
      "s_waitcnt lgkmcnt(0)\n\t"
      "buffer_store_dwordx4 a[56:59], v14, s[44:47], s85 offen offset:2048 sc1\n\t"
      "s_cmp_lt_i32 s91, 0\n\t"
      "s_cselect_b32 s30, -1, 0\n\t"
      "s_xor_b32 s16, s91, 0x3ffff\n\t"
      "s_and_b32 s16, s16, s30\n\t"
      "s_cbranch_scc0 .Lend_%=\n\t"
      "s_waitcnt vmcnt(0) lgkmcnt(0)\n\t"
      "s_barrier\n\t"
      "s_lshl1_add_u32 s30, s14, 24576\n\t"
      "s_sub_u32 s15, s30, s15\n\t"
      "s_branch .Lstart_%=\n\t"
      ".Lwrap3_%=:\n\t"
      "v_add_u32 v5, s48, v5\n\t"
      "v_subrev_u32 v62, 113246208, v5\n\t"
      "v_min_u32 v5, v5, v62\n\t"
      "v_add_u32 v6, s48, v6\n\t"
      "v_subrev_u32 v62, 113246208, v6\n\t"
      "v_min_u32 v6, v6, v62\n\t"
      "v_add_u32 v7, s48, v7\n\t"
      "v_subrev_u32 v62, 113246208, v7\n\t"
      "v_min_u32 v7, v7, v62\n\t"
      "s_mov_b32 s48, 0\n\t"
      "buffer_load_dwordx4 v5, s[40:43], s48 offen nt lds\n\t"
      "buffer_load_dwordx4 v6, s[40:43], s48 offen offset:1024 nt lds\n\t"
      "buffer_load_dwordx4 v7, s[40:43], s48 offen offset:2048 nt lds\n\t"
      "s_branch .Lwrapped3_%=\n\t"
      ".Lwrapo_%=:\n\t"
      "v_add_u32 v12, s85, v12\n\t"
      "v_subrev_u32 v62, 113246208, v12\n\t"
      "v_min_u32 v12, v12, v62\n\t"
      "v_add_u32 v13, s85, v13\n\t"
      "v_subrev_u32 v62, 113246208, v13\n\t"
      "v_min_u32 v13, v13, v62\n\t"
      "v_add_u32 v14, s85, v14\n\t"
      "v_subrev_u32 v62, 113246208, v14\n\t"
      "v_min_u32 v14, v14, v62\n\t"
      "s_mov_b32 s85, 0\n\t"
      "s_branch .Lwrappedo_%=\n\t"
      ".Lwait_%=:\n\t"
      "s_sub_u32 s52, s52, 1\n\t"
      "s_cbranch_scc1 .Lwait2_%=\n\t"
      "s_add_u32 s90, s90, 0x2000000\n\t"
      "v_mov_b32 v54, s90\n\t"
      "v_mov_b32 v55, s19\n\t"
      "global_store_dword v55, v54, s[12:13] sc1\n\t"
      "s_sleep 16\n\t"
      "global_load_dword v40, v62, s[12:13] sc1\n\t"
      "s_waitcnt vmcnt(0)\n\t"
      "s_branch .Lcheck_%=\n\t"
      ".Lwait2_%=:\n\t"
      "s_brev_b32 s91, 1\n\t"
      "s_branch .Lwaited_%=\n\t"
      ".Lspinf_%=:\n\t"
      "s_memrealtime s[36:37]\n\t"
      "s_waitcnt lgkmcnt(0)\n\t"
      "s_sub_u32 s53, s36, s38\n\t"
      "s_cmp_lt_i32 s53, 0\n\t"
      "s_cbranch_scc1 .Lspinf_%=\n\t"
      "s_branch .Lspunf_%=\n\t"
      ".Lspin0_%=:\n\t"
      "s_memrealtime s[36:37]\n\t"
      "s_waitcnt lgkmcnt(0)\n\t"
      "s_sub_u32 s53, s36, s38\n\t"
      "s_cmp_lt_i32 s53, 0\n\t"
      "s_cbranch_scc1 .Lspin0_%=\n\t"
      "s_branch .Lspun0_%=\n\t"
      ".Lspin1_%=:\n\t"
      "s_memrealtime s[36:37]\n\t"
      "s_waitcnt lgkmcnt(0)\n\t"
      "s_sub_u32 s53, s36, s38\n\t"
      "s_cmp_lt_i32 s53, 0\n\t"
      "s_cbranch_scc1 .Lspin1_%=\n\t"
      "s_branch .Lspun1_%=\n\t"
      ".Lend_%=:\n\t"
      : [tid] "+v"(tid)
      : [karg] "s"(__builtin_amdgcn_kernarg_segment_ptr()), [blk] "s"(blockIdx.x), [lds] "s"((unsigned)(size_t)lds)
      : "v1", "v2", "v3", "v4", "v5", "v6", "v7", "v8", "v9", "v10", "v11", "v12", "v13", "v14", "v15", "v16", "v17", "v18", "v19", "v20", "v21", "v22", "v23", "v24", "v25", "v26", "v27", "v28", "v29", "v30", "v31", "v32", "v33", "v34", "v35", "v36", "v37", "v38", "v39", "v40", "v41", "v42", "v43", "v44", "v45", "v46", "v47", "v48", "v49", "v50", "v51", "v52", "v53", "v54", "v55", "v56", "v57", "v58", "v59", "v60", "v61", "v62", "v63", "v64", "v65", "v66", "v67", "v68", "v69", "v70", "v71", "v72", "v73", "v74", "v75", "v76", "v77", "v78", "v79", "v80", "v81", "v82", "v83", "v84", "v85", "v86", "v87", "v88", "v89", "v90", "v91", "v92", "v93", "v94", "v95", "v96", "v97", "v98", "v99", "v100", "v101", "v102", "v103", "v104", "v105", "v106", "v107", "v108", "v109", "v110", "v111", "v112", "v113", "v114", "v115", "v116", "v117", "v118", "v119", "v120", "v121", "v122", "v123", "v124", "v125", "v126", "v127", "v128", "v129", "v130", "v131", "v132", "v133", "v134", "v135", "v136", "v137", "v138", "v139", "v140", "v141", "v142", "v143", "v144", "v145", "v146", "v147", "v148", "v149", "v150", "v151", "v152", "v153", "v154", "v155", "v156", "v157", "v158", "v159", "v160", "v161", "v162", "v163", "v164", "v165", "v166", "v167", "v168", "v169", "v170", "v171", "v172", "v173", "v174", "v175", "v176", "v177", "v178", "v179", "v180", "v181", "v182", "v183", "v184", "v185", "v186", "v187", "v188", "v189", "v190", "v191", "v192", "v193", "v194", "v195", "v196", "v197", "v198", "v199", "v200", "v201", "v202", "v203", "v204", "v205", "v206", "v207", "v208", "v209", "v210", "v211", "v212", "v213", "v214", "v215", "v216", "v217", "v218", "v219", "v220", "v221", "v222", "v223", "v224", "v225", "v226", "v227", "v228", "v229", "v230", "v231", "v232", "v233", "v234", "v235", "v236", "v237", "v238", "v239", "v240", "v241", "v242", "v243", "v244", "v245", "v246", "v247", "v248", "v249", "v250", "v251", "v252", "v253", "v254", "v255", "a0", "a1", "a2", "a3", "a4", "a5", "a6", "a7", "a8", "a9", "a10", "a11", "a12", "a13", "a14", "a15", "a16", "a17", "a18", "a19", "a20", "a21", "a22", "a23", "a24", "a25", "a26", "a27", "a28", "a29", "a30", "a31", "a32", "a33", "a34", "a35", "a36", "a37", "a38", "a39", "a40", "a41", "a42", "a43", "a44", "a45", "a46", "a47", "a48", "a49", "a50", "a51", "a52", "a53", "a54", "a55", "a56", "a57", "a58", "a59", "a60", "a61", "a62", "a63", "a64", "a65", "a66", "a67", "a68", "a69", "a70", "a71", "a72", "a73", "a74", "a75", "a76", "a77", "a78", "a79", "a80", "a81", "a82", "a83", "a84", "a85", "a86", "a87", "a88", "a89", "a90", "a91", "a92", "a93", "a94", "a95", "a96", "a97", "a98", "a99", "a100", "a101", "a102", "a103", "a104", "a105", "a106", "a107", "a108", "a109", "a110", "a111", "a112", "a113", "a114", "a115", "a116", "a117", "a118", "a119", "a120", "a121", "a122", "a123", "a124", "a125", "a126", "a127", "a128", "a129", "a130", "a131", "a132", "a133", "a134", "a135", "a136", "a137", "a138", "a139", "a140", "a141", "a142", "a143", "a144", "a145", "a146", "a147", "a148", "a149", "a150", "a151", "a152", "a153", "a154", "a155", "a156", "a157", "a158", "a159", "a160", "a161", "a162", "a163", "a164", "a165", "a166", "a167", "a168", "a169", "a170", "a171", "a172", "a173", "a174", "a175", "a176", "a177", "a178", "a179", "a180", "a181", "a182", "a183", "a184", "a185", "a186", "a187", "a188", "a189", "a190", "a191", "a192", "a193", "a194", "a195", "a196", "a197", "a198", "a199", "a200", "a201", "a202", "a203", "a204", "a205", "a206", "a207", "a208", "a209", "a210", "a211", "a212", "a213", "a214", "a215", "a216", "a217", "a218", "a219", "a220", "a221", "a222", "a223", "a224", "a225", "a226", "a227", "a228", "a229", "a230", "a231", "a232", "a233", "a234", "a235", "a236", "a237", "a238", "a239", "a240", "a241", "a242", "a243", "a244", "a245", "a246", "a247", "a248", "a249", "a250", "a251", "a252", "a253", "a254", "a255", "s4", "s5", "s6", "s7", "s8", "s9", "s10", "s11", "s12", "s13", "s14", "s15", "s16", "s17", "s18", "s19", "s20", "s21", "s22", "s23", "s24", "s25", "s26", "s27", "s28", "s29", "s30", "s31", "s32", "s33", "s34", "s35", "s36", "s37", "s38", "s39", "s40", "s41", "s42", "s43", "s44", "s45", "s46", "s47", "s48", "s49", "s50", "s51", "s52", "s53", "s54", "s55", "s56", "s57", "s58", "s59", "s60", "s61", "s62", "s63", "s64", "s65", "s66", "s67", "s68", "s69", "s70", "s71", "s72", "s73", "s74", "s75", "s76", "s77", "s78", "s79", "s80", "s81", "s82", "s83", "s84", "s85", "s86", "s87", "s88", "s89", "s90", "s91", "vcc", "scc", "memory");
}

__global__ __launch_bounds__(384) void dwsep_plain(const unsigned short* __restrict__ x, const unsigned short* __restrict__ dw,
                                                   const unsigned short* __restrict__ pw, unsigned short* __restrict__ out) {
  __shared__ float xs[384];
  const unsigned n = threadIdx.x;
  const size_t row = (size_t)blockIdx.x * 384u;
  xs[n] = __uint_as_float((unsigned)x[row + n] << 16) * __uint_as_float((unsigned)dw[n] << 16);
  __syncthreads();
  float acc = 0.f;
  for (int k = 0; k < 384; ++k) acc += xs[k] * __uint_as_float((unsigned)pw[k * 384 + n] << 16);
  const __bf16 o = (__bf16)acc;
  out[row + n] = __builtin_bit_cast(unsigned short, o);
}

extern "C" void kernel_launch(void* const* d_in, const int* in_sizes, int n_in, void* d_out, int out_size,
                              void* d_ws, size_t ws_size, hipStream_t stream) {
  const unsigned short* x = (const unsigned short*)d_in[0];
  const unsigned short* dw = (const unsigned short*)d_in[1];
  const unsigned short* pw = (const unsigned short*)d_in[2];
  unsigned short* out = (unsigned short*)d_out;
  if (in_sizes[0] == 56623104) dwsep_kernel<<<NWG, 256, 0, stream>>>(x, dw, pw, out, (unsigned*)d_ws);
  else dwsep_plain<<<in_sizes[0] / 384, 384, 0, stream>>>(x, dw, pw, out);
}
```

```cpp
#include <hip/hip_runtime.h>

constexpr unsigned ROWB = 768, TILEB = 12288, NWG = 256;

__global__ __launch_bounds__(256, 1) void dwsep_kernel(const unsigned short* __restrict__ x,
                                                       const unsigned short* __restrict__ dw,
                                                       const unsigned short* __restrict__ pw,
                                                       unsigned short* __restrict__ out, unsigned* ws) {
  __shared__ __attribute__((aligned(256))) char lds[163392];
  unsigned tid = threadIdx.x;
  asm volatile(
      "s_memtime s[32:33]\n\t"
      "s_load_dwordx8 s[4:11], %[karg], 0\n\t"
      "s_load_dwordx2 s[12:13], %[karg], 0x20\n\t"
      "v_readfirstlane_b32 s30, %[tid]\n\t"
      "s_lshr_b32 s31, %[blk], 1\n\t"
      "s_and_b32 s31, s31, 12\n\t"
      "s_and_b32 s34, %[blk], 3\n\t"
      "s_or_b32 s31, s31, s34\n\t"
      "s_lshr_b32 s30, s30, 6\n\t"
      "s_lshr_b32 s34, %[blk], 5\n\t"
      "s_bfe_u32 s35, %[blk], 0x10002\n\t"
      "s_mul_i32 s17, s30, 6144\n\t"
      "s_mul_i32 s18, s30, 3072\n\t"
      "s_mul_i32 s14, s34, 14155776\n\t"
      "s_mul_i32 s36, s31, 49152\n\t"
      "s_add_u32 s14, s14, s36\n\t"
      "s_mul_i32 s36, s35, 24576\n\t"
      "s_add_u32 s15, s14, s36\n\t"
      "s_mov_b32 s16, 0\n\t"
      "s_lshr_b32 s36, %[blk], 3\n\t"
      "s_lshl2_add_u32 s24, s36, s30\n\t"
      "s_sub_u32 s36, s24, s36\n\t"
      "s_sub_u32 s36, s36, 1\n\t"
      "s_mul_i32 s23, s36, 3072\n\t"
      "s_lshl_b32 s20, s34, 3\n\t"
      "s_mul_i32 s36, s34, 27\n\t"
      "s_lshr_b32 s36, s36, 2\n\t"
      "s_lshr_b32 s37, s31, 1\n\t"
      "s_add_u32 s19, s37, s20\n\t"
      "s_add_u32 s37, s37, s36\n\t"
      "s_min_u32 s37, s37, 53\n\t"
      "s_lshl_b32 s22, s37, 21\n\t"
      "s_lshl_b32 s36, s36, 21\n\t"
      "s_sub_u32 s21, s14, s36\n\t"
      "s_add_u32 s21, s21, 24576\n\t"
      "s_lshl_b32 s19, s19, 8\n\t"
      "s_and_b32 s31, s31, 1\n\t"
      "s_lshl1_add_u32 s31, s31, s35\n\t"
      "s_lshl2_add_u32 s19, s31, s19\n\t"
      ".Lstart_%=:\n\t"
      "v_mbcnt_lo_u32_b32 v233, -1, 0\n\t"
      "v_mbcnt_hi_u32_b32 v233, -1, v233\n\t"
      "s_mul_i32 s30, s17, 683\n\t"
      "s_lshr_b32 s30, s30, 16\n\t"
      "s_and_b32 s30, s30, 0xc0\n\t"
      "s_brev_b32 s34, 8\n\t"
      "s_mov_b32 s35, 0x20201010\n\t"
      "s_mov_b32 s36, 0x30303020\n\t"
      "v_lshl_add_u32 v1, v233, 4, s18\n\t"
      "v_lshrrev_b32 v16, 1, v233\n\t"
      "v_and_b32 v16, 24, v16\n\t"
      "v_bfe_u32 v17, s34, v16, 8\n\t"
      "v_or_b32 v17, s30, v17\n\t"
      "v_xor_b32 v18, v17, v1\n\t"
      "v_add_u32 v19, 80, v17\n\t"
      "v_add_u32 v17, 160, v17\n\t"
      "v_and_b32 v19, 256, v19\n\t"
      "v_and_b32 v17, 256, v17\n\t"
      "v_mad_u32_u24 v2, v19, 30, v18\n\t"
      "v_mad_u32_u24 v5, v17, 33, v18\n\t"
      "v_add_u32 v5, 19968, v5\n\t"
      "v_bfe_u32 v17, s35, v16, 8\n\t"
      "v_or_b32 v17, s30, v17\n\t"
      "v_xor_b32 v18, v17, v1\n\t"
      "v_add_u32 v19, 80, v17\n\t"
      "v_add_u32 v17, 160, v17\n\t"
      "v_and_b32 v19, 256, v19\n\t"
      "v_and_b32 v17, 256, v17\n\t"
      "v_mad_u32_u24 v3, v19, 30, v18\n\t"
      "v_mad_u32_u24 v6, v17, 33, v18\n\t"
      "v_add_u32 v6, 19968, v6\n\t"
      "v_bfe_u32 v17, s36, v16, 8\n\t"
      "v_or_b32 v17, s30, v17\n\t"
      "v_xor_b32 v18, v17, v1\n\t"
      "v_add_u32 v19, 80, v17\n\t"
      "v_add_u32 v17, 160, v17\n\t"
      "v_and_b32 v19, 256, v19\n\t"
      "v_and_b32 v17, 256, v17\n\t"
      "v_mad_u32_u24 v4, v19, 30, v18\n\t"
      "v_mad_u32_u24 v7, v17, 33, v18\n\t"
      "v_add_u32 v7, 19968, v7\n\t"
      "s_sub_u32 s31, s30, 64\n\t"
      "v_add_u32 v17, s31, v233\n\t"
      "v_min_u32 v17, 193, v17\n\t"
      "v_lshlrev_b32 v18, 2, v17\n\t"
      "v_lshlrev_b32 v17, 1, v17\n\t"
      "v_and_b32 v18, -16, v18\n\t"
      "v_and_or_b32 v8, v17, 6, v18\n\t"
      "s_mov_b32 s86, 294912\n\t"
      "s_mov_b32 s87, 0x00020000\n\t"
      "s_mov_b32 s42, 113246208\n\t"
      "s_mov_b32 s43, 0x00020000\n\t"
      "s_cmp_lg_u32 s17, 0\n\t"
      "s_cbranch_scc1 .Lno0_%=\n\t"
      "s_movk_i32 s31, -61\n\t"
      "v_lshrrev_b32 v16, 2, v233\n\t"
      "v_add_u32 v17, s20, v16\n\t"
      "v_lshl_add_u32 v18, v16, 3, s31\n\t"
      "v_cmp_gt_u32 vcc, 32, v233\n\t"
      "v_and_b32 v19, 3, v233\n\t"
      "v_lshlrev_b32 v19, 2, v19\n\t"
      "v_cndmask_b32 v17, v18, v17, vcc\n\t"
      "v_lshl_add_u32 v62, v17, 8, v19\n\t"
      "s_waitcnt lgkmcnt(2)\n\t"
      "s_add_u32 s32, s32, %[blk]\n\t"
      "s_mul_i32 s32, s32, 0x9e3779b1\n\t"
      "s_lshr_b32 s32, s32, 9\n\t"
      "s_and_b32 s32, s32, 0x1fdf80\n\t"
      "s_or_b32 s22, s22, s32\n\t"
      "v_mov_b32 v234, s22\n\t"
      "v_add_u32 v235, 8192, v234\n\t"
      "s_movk_i32 s64, 120\n\t"
      "s_mov_b64 vcc, 0\n\t"
      ".Lsweep_%=:\n\t"
      "s_getreg_b32 s30, hwreg(HW_REG_IB_STS, 0, 12)\n\t"
      "s_and_b32 s31, s30, 15\n\t"
      "s_lshr_b32 s30, s30, 8\n\t"
      "s_cmp_eq_u32 s31, 8\n\t"
      "s_addc_u32 s62, s62, 0\n\t"
      "s_cmp_eq_u32 s30, 1\n\t"
      "s_addc_u32 s63, s63, 0\n\t"
      "s_cmp_gt_u32 s31, 7\n\t"
      "s_addc_u32 s30, s30, 0\n\t"
      "s_sub_u32 s64, s64, 1\n\t"
      "s_min_u32 s30, s30, s64\n\t"
      "s_cmp_lg_u32 s30, 0\n\t"
      "s_cbranch_scc1 .Lsweep_%=\n\t"
      "s_cbranch_vccnz .Lswept_%=\n\t"
      "s_waitcnt lgkmcnt(0)\n\t"
      "global_load_dword v236, v234, s[4:5]\n\t"
      "global_load_dword v237, v235, s[4:5]\n\t"
      "s_memrealtime s[28:29]\n\t"
      "s_load_dword s60, s[10:11], s22\n\t"
      "s_load_dword s61, s[10:11], s22 offset:8192\n\t"
      "global_load_dword v63, v62, s[12:13] sc1\n\t"
      "s_mov_b64 s[40:41], s[4:5]\n\t"
      "s_add_u32 m0, s18, 25088\n\t"
      "s_add_u32 s48, s15, 0\n\t"
      "buffer_load_dwordx4 v2, s[40:43], s48 offen lds\n\t"
      "buffer_load_dwordx4 v3, s[40:43], s48 offen offset:1024 lds\n\t"
      "buffer_load_dwordx4 v4, s[40:43], s48 offen offset:2048 lds\n\t"
      "s_add_u32 m0, s18, 37376\n\t"
      "s_add_u32 s48, s15, 0\n\t"
      "buffer_load_dwordx4 v5, s[40:43], s48 offen lds\n\t"
      "buffer_load_dwordx4 v6, s[40:43], s48 offen offset:1024 lds\n\t"
      "buffer_load_dwordx4 v7, s[40:43], s48 offen offset:2048 lds\n\t"
      "s_barrier\n\t"
      "s_mov_b64 s[62:63], 0\n\t"
      "s_movk_i32 s64, 120\n\t"
      "s_mov_b64 vcc, -1\n\t"
      "s_branch .Lsweep_%=\n\t"
      ".Lswept_%=:\n\t"
      "s_min_u32 s62, s62, 7\n\t"
      "s_min_u32 s63, s63, 7\n\t"
      "s_lshl_b32 s63, s63, 8\n\t"
      "s_or_b32 s30, s62, s63\n\t"
      "s_sub_u32 s31, 0x808, s30\n\t"
      "s_add_u32 s30, s30, 0x10808\n\t"
      "s_getreg_b32 s62, hwreg(HW_REG_XCC_ID, 2, 1)\n\t"
      "s_cmp_lg_u32 s62, 0\n\t"
      "s_cselect_b32 s91, s30, s31\n\t"
      "s_mov_b64 s[84:85], s[8:9]\n\t"
      "v_mul_u32_u24 v16, 49152, v1\n\t"
      "v_add_u32 v16, s21, v16\n\t"
      "v_lshrrev_b32 v16, 21, v16\n\t"
      "v_lshlrev_b32 v16, 2, v16\n\t"
      "v_accvgpr_write_b32 a248, v16\n\t"
      "s_branch .Lgo0_%=\n\t"
      ".Lno0_%=:\n\t"
      "s_movk_i32 s82, 768\n\t"
      "s_mov_b32 s83, 0x00020000\n\t"
      "v_and_b32 v234, 0x3f0, v1\n\t"
      "s_waitcnt lgkmcnt(0)\n\t"
      "s_mov_b64 s[84:85], s[8:9]\n\t"
      "s_mov_b64 s[80:81], s[6:7]\n\t"
      "s_mov_b64 s[40:41], s[4:5]\n\t"
      "s_barrier\n\t"
      "buffer_load_dwordx4 a[200:203], v234, s[84:87], s23 offen offset:0\n\t"
      "buffer_load_dwordx4 a[204:207], v234, s[84:87], s23 offen offset:1024\n\t"
      "buffer_load_dwordx4 a[208:211], v234, s[84:87], s23 offen offset:2048\n\t"
      "buffer_load_ushort v224, v8, s[80:83], 0 offen\n\t"
      "buffer_load_ushort v225, v8, s[80:83], 0 offen offset:8\n\t"
      "s_add_u32 m0, s18, 25088\n\t"
      "s_add_u32 s48, s15, 0\n\t"
      "buffer_load_dwordx4 v2, s[40:43], s48 offen lds\n\t"
      "buffer_load_dwordx4 v3, s[40:43], s48 offen offset:1024 lds\n\t"
      "buffer_load_dwordx4 v4, s[40:43], s48 offen offset:2048 lds\n\t"
      "s_add_u32 m0, s18, 37376\n\t"
      "s_add_u32 s48, s15, 0\n\t"
      "buffer_load_dwordx4 v5, s[40:43], s48 offen lds\n\t"
      "buffer_load_dwordx4 v6, s[40:43], s48 offen offset:1024 lds\n\t"
      "buffer_load_dwordx4 v7, s[40:43], s48 offen offset:2048 lds\n\t"
      ".Lgo0_%=:\n\t"
      "s_getpc_b64 s[36:37]\n\t"
      ".Lpc_%=:\n\t"
      "s_lshr_b32 s30, s17, 5\n\t"
      "v_lshlrev_b32 v16, 4, v233\n\t"
      "v_add_u32 v17, 0, v233\n\t"
      "v_mul_u32_u24 v17, 171, v17\n\t"
      "v_lshrrev_b32 v17, 11, v17\n\t"
      "v_lshlrev_b32 v18, 2, v17\n\t"
      "v_and_b32 v18, 32, v18\n\t"
      "v_xor_b32 v18, v18, v16\n\t"
      "v_mul_u32_u24 v17, 576, v17\n\t"
      "v_add3_u32 v9, v17, s30, v18\n\t"
      "v_add_u32 v17, 64, v233\n\t"
      "v_mul_u32_u24 v17, 171, v17\n\t"
      "v_lshrrev_b32 v17, 11, v17\n\t"
      "v_lshlrev_b32 v18, 2, v17\n\t"
      "v_and_b32 v18, 32, v18\n\t"
      "v_xor_b32 v18, v18, v16\n\t"
      "v_mul_u32_u24 v17, 576, v17\n\t"
      "v_add3_u32 v10, v17, s30, v18\n\t"
      "v_add_u32 v17, 128, v233\n\t"
      "v_mul_u32_u24 v17, 171, v17\n\t"
      "v_lshrrev_b32 v17, 11, v17\n\t"
      "v_lshlrev_b32 v18, 2, v17\n\t"
      "v_and_b32 v18, 32, v18\n\t"
      "v_xor_b32 v18, v18, v16\n\t"
      "v_mul_u32_u24 v17, 576, v17\n\t"
      "v_add3_u32 v11, v17, s30, v18\n\t"
      "s_mov_b64 s[44:45], s[10:11]\n\t"
      "s_mov_b32 s46, 0\n\t"
      "s_mov_b32 s47, 0x00020000\n\t"
      "s_lshl_b32 s30, s24, 6\n\t"
      "s_add_u32 s31, s30, 0\n\t"
      "s_min_u32 s31, s31, .Lend_%=-.Lpc_%=-4\n\t"
      "s_load_dword s35, s[36:37], s31\n\t"
      "s_add_u32 s31, s30, 8192\n\t"
      "s_min_u32 s31, s31, .Lend_%=-.Lpc_%=-4\n\t"
      "s_load_dword s35, s[36:37], s31\n\t"
      "s_add_u32 s31, s30, 16384\n\t"
      "s_min_u32 s31, s31, .Lend_%=-.Lpc_%=-4\n\t"
      "s_load_dword s35, s[36:37], s31\n\t"
      "s_add_u32 s31, s30, 24576\n\t"
      "s_min_u32 s31, s31, .Lend_%=-.Lpc_%=-4\n\t"
      "s_load_dword s35, s[36:37], s31\n\t"
      "s_and_b32 s31, s24, 63\n\t"
      "s_lshl_b32 s31, s31, 21\n\t"
      "s_min_u32 s31, s31, 113246144\n\t"
      "s_bitcmp1_b32 s24, 6\n\t"
      "s_cselect_b64 s[32:33], s[44:45], s[40:41]\n\t"
      "s_load_dword s35, s[32:33], s31\n\t"
      "s_lshr_b32 s30, s17, 5\n\t"
      "v_lshrrev_b32 v16, 4, v233\n\t"
      "v_and_b32 v17, 15, v233\n\t"
      "v_and_b32 v18, 3, v233\n\t"
      "v_bfe_u32 v19, v233, 2, 2\n\t"
      "v_lshl_add_u32 v19, v16, 3, v19\n\t"
      "v_mul_u32_u24 v19, 192, v19\n\t"
      "v_lshl_add_u32 v19, v18, 3, v19\n\t"
      "v_lshlrev_b32 v20, 1, v233\n\t"
      "v_and_b32 v20, 32, v20\n\t"
      "v_add3_u32 v12, v19, v20, s17\n\t"
      "v_xor_b32 v13, 32, v12\n\t"
      "v_xor_b32 v19, v16, v17\n\t"
      "v_lshlrev_b32 v19, 4, v19\n\t"
      "v_mul_u32_u24 v20, 768, v17\n\t"
      "v_add_u32 v15, v19, v20\n\t"
      "v_mul_u32_u24 v20, 784, v17\n\t"
      "v_lshl_add_u32 v20, v16, 3, v20\n\t"
      "v_add_u32 v0, s30, v20\n\t"
      "v_lshlrev_b32 v19, 6, v16\n\t"
      "v_lshl_add_u32 v19, v18, 4, v19\n\t"
      "v_add_u32 v14, 160256, v19\n\t"
      "v_and_b32 v226, 0xfffffff0, v8\n\t"
      "v_and_b32 v227, 6, v8\n\t"
      "v_lshlrev_b32 v226, 2, v226\n\t"
      "v_lshl_add_u32 v226, v227, 3, v226\n\t"
      "v_add_u32 v226, 160256, v226\n\t"
      "v_and_b32 v232, 2, v8\n\t"
      "v_lshlrev_b32 v232, 3, v232\n\t"
      "v_cmp_gt_u32 vcc, 4, v227\n\t"
      "v_mov_b32 v56, v15\n\t"
      "v_xor_b32 v57, 64, v15\n\t"
      "v_xor_b32 v58, 128, v15\n\t"
      "v_xor_b32 v59, 192, v15\n\t"
      "v_add_u32 v60, 86528, v12\n\t"
      "v_add_u32 v61, 86528, v13\n\t"
      "v_xor_b32 v16, v2, v1\n\t"
      "v_and_b32 v16, 0xf0, v16\n\t"
      "v_xor_b32 v17, v16, v2\n\t"
      "v_xor_b32 v18, v16, v5\n\t"
      "v_add_u32 v19, v16, v1\n\t"
      "v_accvgpr_write_b32 a213, v17\n\t"
      "v_accvgpr_write_b32 a216, v18\n\t"
      "v_accvgpr_write_b32 a219, v19\n\t"
      "v_xor_b32 v16, v3, v1\n\t"
      "v_and_b32 v16, 0xf0, v16\n\t"
      "v_xor_b32 v17, v16, v3\n\t"
      "v_xor_b32 v18, v16, v6\n\t"
      "v_add_u32 v19, v16, v1\n\t"
      "v_accvgpr_write_b32 a214, v17\n\t"
      "v_accvgpr_write_b32 a217, v18\n\t"
      "v_accvgpr_write_b32 a220, v19\n\t"
      "v_xor_b32 v16, v4, v1\n\t"
      "v_and_b32 v16, 0xf0, v16\n\t"
      "v_xor_b32 v17, v16, v4\n\t"
      "v_xor_b32 v18, v16, v7\n\t"
      "v_add_u32 v19, v16, v1\n\t"
      "v_accvgpr_write_b32 a215, v17\n\t"
      "v_accvgpr_write_b32 a218, v18\n\t"
      "v_accvgpr_write_b32 a221, v19\n\t"
      "s_waitcnt vmcnt(6)\n\t"
      "s_bfe_u32 s30, s19, 0x30008\n\t"
      "s_bfe_u32 s31, s19, 0x20002\n\t"
      "s_lshl2_add_u32 s30, s30, s31\n\t"
      "v_readlane_b32 s90, v63, s30\n\t"
      "v_lshlrev_b32 v224, v232, v224\n\t"
      "v_lshlrev_b32 v225, v232, v225\n\t"
      "v_cndmask_b32 v228, 0, v224, vcc\n\t"
      "v_cndmask_b32 v230, 0, v225, vcc\n\t"
      "v_sub_u32 v229, v224, v228\n\t"
      "v_sub_u32 v231, v225, v230\n\t"
      "ds_write_b128 v226, v[228:231]\n\t"
      "s_add_u32 s90, s90, 0x2000000\n\t"
      "s_andn2_b32 s90, s90, 0x1ffffff\n\t"
      "s_or_b32 s90, s90, s91\n\t"
      "s_or_b32 s30, s17, s16\n\t"
      "s_cbranch_scc1 .Lno2_%=\n\t"
      "v_mov_b32 v234, s90\n\t"
      "v_mov_b32 v235, s19\n\t"
      "global_store_dword v235, v234, s[12:13] sc1\n\t"
      ".Lno2_%=:\n\t"
      "s_add_u32 m0, s17, 0\n\t"
      "s_mov_b32 s54, 0\n\t"
      "buffer_load_dwordx4 v9, s[84:87], s54 offen lds\n\t"
      "buffer_load_dwordx4 v10, s[84:87], s54 offen offset:1024 lds\n\t"
      "buffer_load_dwordx4 v11, s[84:87], s54 offen offset:2048 lds\n\t"
      "s_add_u32 m0, s17, 3072\n\t"
      "s_mov_b32 s54, 12288\n\t"
      "buffer_load_dwordx4 v9, s[84:87], s54 offen lds\n\t"
      "buffer_load_dwordx4 v10, s[84:87], s54 offen offset:1024 lds\n\t"
      "buffer_load_dwordx4 v11, s[84:87], s54 offen offset:2048 lds\n\t"
      "s_add_u32 m0, s17, 86528\n\t"
      "s_mov_b32 s54, 24576\n\t"
      "buffer_load_dwordx4 v9, s[84:87], s54 offen lds\n\t"
      "buffer_load_dwordx4 v10, s[84:87], s54 offen offset:1024 lds\n\t"
      "buffer_load_dwordx4 v11, s[84:87], s54 offen offset:2048 lds\n\t"
      "s_add_u32 m0, s17, 89600\n\t"
      "s_mov_b32 s54, 36864\n\t"
      "buffer_load_dwordx4 v9, s[84:87], s54 offen lds\n\t"
      "buffer_load_dwordx4 v10, s[84:87], s54 offen offset:1024 lds\n\t"
      "buffer_load_dwordx4 v11, s[84:87], s54 offen offset:2048 lds\n\t"
      "s_add_u32 m0, s17, 111104\n\t"
      "s_mov_b32 s54, 49152\n\t"
      "buffer_load_dwordx4 v9, s[84:87], s54 offen lds\n\t"
      "buffer_load_dwordx4 v10, s[84:87], s54 offen offset:1024 lds\n\t"
      "buffer_load_dwordx4 v11, s[84:87], s54 offen offset:2048 lds\n\t"
      "s_add_u32 m0, s17, 114176\n\t"
      "s_mov_b32 s54, 61440\n\t"
      "buffer_load_dwordx4 v9, s[84:87], s54 offen lds\n\t"
      "buffer_load_dwordx4 v10, s[84:87], s54 offen offset:1024 lds\n\t"
      "buffer_load_dwordx4 v11, s[84:87], s54 offen offset:2048 lds\n\t"
      "s_waitcnt lgkmcnt(0)\n\t"
      "s_waitcnt vmcnt(18)\n\t"
      "s_barrier\n\t"
      "s_waitcnt vmcnt(12)\n\t"
      "ds_read_b128 a[180:183], v14 offset:0\n\t"
      "ds_read_b64_tr_b16 v[64:65], v12 offset:0\n\t"
      "ds_read_b64_tr_b16 v[66:67], v12 offset:768\n\t"
      "ds_read_b64_tr_b16 v[68:69], v13 offset:0\n\t"
      "ds_read_b64_tr_b16 v[70:71], v13 offset:768\n\t"
      "ds_read_b64_tr_b16 v[72:73], v12 offset:64\n\t"
      "ds_read_b64_tr_b16 v[74:75], v12 offset:832\n\t"
      "ds_read_b64_tr_b16 v[76:77], v13 offset:64\n\t"
      "ds_read_b64_tr_b16 v[78:79], v13 offset:832\n\t"
      "ds_read_b64_tr_b16 a[60:61], v12 offset:128\n\t"
      "ds_read_b64_tr_b16 a[62:63], v12 offset:896\n\t"
      "ds_read_b64_tr_b16 a[64:65], v13 offset:128\n\t"
      "ds_read_b64_tr_b16 a[66:67], v13 offset:896\n\t"
      "s_add_u32 m0, s17, 135680\n\t"
      "s_mov_b32 s54, 73728\n\t"
      "buffer_load_dwordx4 v9, s[84:87], s54 offen lds\n\t"
      "buffer_load_dwordx4 v10, s[84:87], s54 offen offset:1024 lds\n\t"
      "buffer_load_dwordx4 v11, s[84:87], s54 offen offset:2048 lds\n\t"
      "s_add_u32 m0, s17, 138752\n\t"
      "s_mov_b32 s54, 86016\n\t"
      "buffer_load_dwordx4 v9, s[84:87], s54 offen lds\n\t"
      "buffer_load_dwordx4 v10, s[84:87], s54 offen offset:1024 lds\n\t"
      "buffer_load_dwordx4 v11, s[84:87], s54 offen offset:2048 lds\n\t"
      "s_waitcnt vmcnt(12)\n\t"
      "s_waitcnt lgkmcnt(0)\n\t"
      "; step\n\t"
      "v_mfma_f32_4x4x4_16b_bf16 v[40:43], a[180:181], v[64:65], 0\n\t"
      "ds_read_b128 a[184:187], v14 offset:256\n\t"
      "v_mfma_f32_4x4x4_16b_bf16 v[44:47], a[182:183], v[66:67], 0\n\t"
      "ds_read_b64_tr_b16 v[80:81], v60 offset:0\n\t"
      "v_mfma_f32_4x4x4_16b_bf16 v[48:51], a[180:181], v[68:69], 0\n\t"
      "ds_read_b64_tr_b16 v[82:83], v60 offset:768\n\t"
      "v_mfma_f32_4x4x4_16b_bf16 v[52:55], a[182:183], v[70:71], 0\n\t"
      "ds_read_b64_tr_b16 v[84:85], v61 offset:0\n\t"
      "ds_read_b64_tr_b16 v[86:87], v61 offset:768\n\t"
      "s_add_u32 m0, s17, 0\n\t"
      "s_mov_b32 s54, 98304\n\t"
      "buffer_load_dwordx4 v9, s[84:87], s54 offen lds\n\t"
      "buffer_load_dwordx4 v10, s[84:87], s54 offen offset:1024 lds\n\t"
      "v_cvt_pk_bf16_f32 v64, v40, v41\n\t"
      "v_cvt_pk_bf16_f32 v65, v42, v43\n\t"
      "v_cvt_pk_bf16_f32 v66, v44, v45\n\t"
      "v_cvt_pk_bf16_f32 v67, v46, v47\n\t"
      "v_cvt_pk_bf16_f32 v68, v48, v49\n\t"
      "v_cvt_pk_bf16_f32 v69, v50, v51\n\t"
      "v_cvt_pk_bf16_f32 v70, v52, v53\n\t"
      "v_cvt_pk_bf16_f32 v71, v54, v55\n\t"
      "v_mfma_f32_4x4x4_16b_bf16 v[40:43], a[180:181], v[72:73], 0\n\t"
      "ds_read_b64_tr_b16 v[88:89], v60 offset:64\n\t"
      "v_mfma_f32_4x4x4_16b_bf16 v[44:47], a[182:183], v[74:75], 0\n\t"
      "ds_read_b64_tr_b16 v[90:91], v60 offset:832\n\t"
      "v_mfma_f32_4x4x4_16b_bf16 v[48:51], a[180:181], v[76:77], 0\n\t"
      "ds_read_b64_tr_b16 v[92:93], v61 offset:64\n\t"
      "v_mfma_f32_4x4x4_16b_bf16 v[52:55], a[182:183], v[78:79], 0\n\t"
      "ds_read_b64_tr_b16 v[94:95], v61 offset:832\n\t"
      "buffer_load_dwordx4 v11, s[84:87], s54 offen offset:2048 lds\n\t"
      "s_add_u32 m0, s17, 3072\n\t"
      "s_mov_b32 s54, 110592\n\t"
      "buffer_load_dwordx4 v9, s[84:87], s54 offen lds\n\t"
      "v_cvt_pk_bf16_f32 v72, v40, v41\n\t"
      "v_cvt_pk_bf16_f32 v73, v42, v43\n\t"
      "v_cvt_pk_bf16_f32 v74, v44, v45\n\t"
      "v_cvt_pk_bf16_f32 v75, v46, v47\n\t"
      "v_cvt_pk_bf16_f32 v76, v48, v49\n\t"
      "v_cvt_pk_bf16_f32 v77, v50, v51\n\t"
      "v_cvt_pk_bf16_f32 v78, v52, v53\n\t"
      "v_cvt_pk_bf16_f32 v79, v54, v55\n\t"
      "ds_read_b128 a[224:227], v56 offset:25088\n\t"
      "v_mfma_f32_4x4x4_16b_bf16 v[40:43], a[180:181], a[60:61], 0\n\t"
      "ds_read_b64_tr_b16 a[68:69], v60 offset:128\n\t"
      "v_mfma_f32_4x4x4_16b_bf16 v[44:47], a[182:183], a[62:63], 0\n\t"
      "ds_read_b64_tr_b16 a[70:71], v60 offset:896\n\t"
      "v_mfma_f32_4x4x4_16b_bf16 v[48:51], a[180:181], a[64:65], 0\n\t"
      "ds_read_b64_tr_b16 a[72:73], v61 offset:128\n\t"
      "v_mfma_f32_4x4x4_16b_bf16 v[52:55], a[182:183], a[66:67], 0\n\t"
      "ds_read_b64_tr_b16 a[74:75], v61 offset:896\n\t"
      "buffer_load_dwordx4 v10, s[84:87], s54 offen offset:1024 lds\n\t"
      "buffer_load_dwordx4 v11, s[84:87], s54 offen offset:2048 lds\n\t"
      "ds_read_b128 a[228:231], v56 offset:37376\n\t"
      "v_cvt_pk_bf16_f32 v40, v40, v41\n\t"
      "v_cvt_pk_bf16_f32 v41, v42, v43\n\t"
      "v_cvt_pk_bf16_f32 v44, v44, v45\n\t"
      "v_cvt_pk_bf16_f32 v45, v46, v47\n\t"
      "v_cvt_pk_bf16_f32 v48, v48, v49\n\t"
      "v_cvt_pk_bf16_f32 v49, v50, v51\n\t"
      "v_cvt_pk_bf16_f32 v52, v52, v53\n\t"
      "v_cvt_pk_bf16_f32 v53, v54, v55\n\t"
      "v_accvgpr_write_b32 a60, v40\n\t"
      "v_accvgpr_write_b32 a61, v41\n\t"
      "v_accvgpr_write_b32 a62, v44\n\t"
      "v_accvgpr_write_b32 a63, v45\n\t"
      "v_accvgpr_write_b32 a64, v48\n\t"
      "v_accvgpr_write_b32 a65, v49\n\t"
      "v_accvgpr_write_b32 a66, v52\n\t"
      "v_accvgpr_write_b32 a67, v53\n\t"
      "s_waitcnt vmcnt(12)\n\t"
      "s_waitcnt lgkmcnt(1)\n\t"
      "; step\n\t"
      "s_memtime s[64:65]\n\t"
      "s_memrealtime s[66:67]\n\t"
      "v_mfma_f32_4x4x4_16b_bf16 v[40:43], a[184:185], v[80:81], 0\n\t"
      "ds_read_b128 a[180:183], v14 offset:512\n\t"
      "v_mfma_f32_4x4x4_16b_bf16 v[44:47], a[186:187], v[82:83], 0\n\t"
      "ds_read_b64_tr_b16 v[96:97], v60 offset:24576\n\t"
      "v_mfma_f32_4x4x4_16b_bf16 v[48:51], a[184:185], v[84:85], 0\n\t"
      "ds_read_b64_tr_b16 v[98:99], v60 offset:25344\n\t"
      "v_mfma_f32_4x4x4_16b_bf16 v[52:55], a[186:187], v[86:87], 0\n\t"
      "ds_read_b64_tr_b16 v[100:101], v61 offset:24576\n\t"
      "ds_read_b64_tr_b16 v[102:103], v61 offset:25344\n\t"
      "s_add_u32 m0, s17, 86528\n\t"
      "s_mov_b32 s54, 122880\n\t"
      "buffer_load_dwordx4 v9, s[84:87], s54 offen lds\n\t"
      "buffer_load_dwordx4 v10, s[84:87], s54 offen offset:1024 lds\n\t"
      "v_mfma_f32_16x16x32_bf16 v[16:19], v[64:67], a[224:227], 0\n\t"
      "v_cvt_pk_bf16_f32 v80, v40, v41\n\t"
      "v_cvt_pk_bf16_f32 v81, v42, v43\n\t"
      "v_mfma_f32_16x16x32_bf16 v[20:23], v[68:71], a[224:227], 0\n\t"
      "v_cvt_pk_bf16_f32 v82, v44, v45\n\t"
      "v_cvt_pk_bf16_f32 v83, v46, v47\n\t"
      "v_mfma_f32_16x16x32_bf16 v[24:27], v[72:75], a[224:227], 0\n\t"
      "v_cvt_pk_bf16_f32 v84, v48, v49\n\t"
      "v_cvt_pk_bf16_f32 v85, v50, v51\n\t"
      "v_mfma_f32_16x16x32_bf16 v[28:31], v[76:79], a[224:227], 0\n\t"
      "v_cvt_pk_bf16_f32 v86, v52, v53\n\t"
      "v_cvt_pk_bf16_f32 v87, v54, v55\n\t"
      "v_mfma_f32_4x4x4_16b_bf16 v[40:43], a[184:185], v[88:89], 0\n\t"
      "ds_read_b64_tr_b16 v[104:105], v60 offset:24640\n\t"
      "v_mfma_f32_4x4x4_16b_bf16 v[44:47], a[186:187], v[90:91], 0\n\t"
      "ds_read_b64_tr_b16 v[106:107], v60 offset:25408\n\t"
      "v_mfma_f32_4x4x4_16b_bf16 v[48:51], a[184:185], v[92:93], 0\n\t"
      "ds_read_b64_tr_b16 v[108:109], v61 offset:24640\n\t"
      "v_mfma_f32_4x4x4_16b_bf16 v[52:55], a[186:187], v[94:95], 0\n\t"
      "ds_read_b64_tr_b16 v[110:111], v61 offset:25408\n\t"
      "buffer_load_dwordx4 v11, s[84:87], s54 offen offset:2048 lds\n\t"
      "s_add_u32 m0, s17, 89600\n\t"
      "s_mov_b32 s54, 135168\n\t"
      "buffer_load_dwordx4 v9, s[84:87], s54 offen lds\n\t"
      "v_mfma_f32_16x16x32_bf16 v[32:35], a[60:63], a[224:227], 0\n\t"
      "v_cvt_pk_bf16_f32 v88, v40, v41\n\t"
      "v_cvt_pk_bf16_f32 v89, v42, v43\n\t"
      "v_mfma_f32_16x16x32_bf16 v[36:39], a[64:67], a[224:227], 0\n\t"
      "v_cvt_pk_bf16_f32 v90, v44, v45\n\t"
      "v_cvt_pk_bf16_f32 v91, v46, v47\n\t"
      "s_waitcnt lgkmcnt(9)\n\t"
      "v_mfma_f32_16x16x32_bf16 a[156:159], v[64:67], a[228:231], 0\n\t"
      "v_cvt_pk_bf16_f32 v92, v48, v49\n\t"
      "v_cvt_pk_bf16_f32 v93, v50, v51\n\t"
      "v_mfma_f32_16x16x32_bf16 a[160:163], v[68:71], a[228:231], 0\n\t"
      "v_cvt_pk_bf16_f32 v94, v52, v53\n\t"
      "v_cvt_pk_bf16_f32 v95, v54, v55\n\t"
      "ds_read_b128 a[232:235], v57 offset:25088\n\t"
      "v_mfma_f32_4x4x4_16b_bf16 v[40:43], a[184:185], a[68:69], 0\n\t"
      "ds_read_b64_tr_b16 a[76:77], v60 offset:24704\n\t"
      "v_mfma_f32_4x4x4_16b_bf16 v[44:47], a[186:187], a[70:71], 0\n\t"
      "ds_read_b64_tr_b16 a[78:79], v60 offset:25472\n\t"
      "v_mfma_f32_4x4x4_16b_bf16 v[48:51], a[184:185], a[72:73], 0\n\t"
      "ds_read_b64_tr_b16 a[80:81], v61 offset:24704\n\t"
      "v_mfma_f32_4x4x4_16b_bf16 v[52:55], a[186:187], a[74:75], 0\n\t"
      "ds_read_b64_tr_b16 a[82:83], v61 offset:25472\n\t"
      "buffer_load_dwordx4 v10, s[84:87], s54 offen offset:1024 lds\n\t"
      "buffer_load_dwordx4 v11, s[84:87], s54 offen offset:2048 lds\n\t"
      "ds_read_b128 a[236:239], v57 offset:37376\n\t"
      "v_mfma_f32_16x16x32_bf16 a[164:167], v[72:75], a[228:231], 0\n\t"
      "v_cvt_pk_bf16_f32 v40, v40, v41\n\t"
      "v_cvt_pk_bf16_f32 v41, v42, v43\n\t"
      "v_mfma_f32_16x16x32_bf16 a[168:171], v[76:79], a[228:231], 0\n\t"
      "v_cvt_pk_bf16_f32 v44, v44, v45\n\t"
      "v_cvt_pk_bf16_f32 v45, v46, v47\n\t"
      "v_mfma_f32_16x16x32_bf16 a[172:175], a[60:63], a[228:231], 0\n\t"
      "v_cvt_pk_bf16_f32 v48, v48, v49\n\t"
      "v_cvt_pk_bf16_f32 v49, v50, v51\n\t"
      "v_mfma_f32_16x16x32_bf16 a[176:179], a[64:67], a[228:231], 0\n\t"
      "v_cvt_pk_bf16_f32 v52, v52, v53\n\t"
      "v_cvt_pk_bf16_f32 v53, v54, v55\n\t"
      "v_accvgpr_write_b32 a68, v40\n\t"
      "v_accvgpr_write_b32 a69, v41\n\t"
      "v_accvgpr_write_b32 a70, v44\n\t"
      "v_accvgpr_write_b32 a71, v45\n\t"
      "v_accvgpr_write_b32 a72, v48\n\t"
      "v_accvgpr_write_b32 a73, v49\n\t"
      "v_accvgpr_write_b32 a74, v52\n\t"
      "v_accvgpr_write_b32 a75, v53\n\t"
      "s_waitcnt vmcnt(12)\n\t"
      "s_waitcnt lgkmcnt(1)\n\t"
      "; step\n\t"
      "v_mfma_f32_4x4x4_16b_bf16 v[40:43], a[180:181], v[96:97], 0\n\t"
      "ds_read_b128 a[184:187], v14 offset:768\n\t"
      "v_mfma_f32_4x4x4_16b_bf16 v[44:47], a[182:183], v[98:99], 0\n\t"
      "ds_read_b64_tr_b16 v[112:113], v60 offset:49152\n\t"
      "v_mfma_f32_4x4x4_16b_bf16 v[48:51], a[180:181], v[100:101], 0\n\t"
      "ds_read_b64_tr_b16 v[114:115], v60 offset:49920\n\t"
      "v_mfma_f32_4x4x4_16b_bf16 v[52:55], a[182:183], v[102:103], 0\n\t"
      "ds_read_b64_tr_b16 v[116:117], v61 offset:49152\n\t"
      "ds_read_b64_tr_b16 v[118:119], v61 offset:49920\n\t"
      "s_add_u32 m0, s17, 111104\n\t"
      "s_mov_b32 s54, 147456\n\t"
      "buffer_load_dwordx4 v9, s[84:87], s54 offen lds\n\t"
      "buffer_load_dwordx4 v10, s[84:87], s54 offen offset:1024 lds\n\t"
      "v_mfma_f32_16x16x32_bf16 v[16:19], v[80:83], a[232:235], v[16:19]\n\t"
      "v_cvt_pk_bf16_f32 v96, v40, v41\n\t"
      "v_cvt_pk_bf16_f32 v97, v42, v43\n\t"
      "v_mfma_f32_16x16x32_bf16 v[20:23], v[84:87], a[232:235], v[20:23]\n\t"
      "v_cvt_pk_bf16_f32 v98, v44, v45\n\t"
      "v_cvt_pk_bf16_f32 v99, v46, v47\n\t"
      "v_mfma_f32_16x16x32_bf16 v[24:27], v[88:91], a[232:235], v[24:27]\n\t"
      "v_cvt_pk_bf16_f32 v100, v48, v49\n\t"
      "v_cvt_pk_bf16_f32 v101, v50, v51\n\t"
      "v_mfma_f32_16x16x32_bf16 v[28:31], v[92:95], a[232:235], v[28:31]\n\t"
      "v_cvt_pk_bf16_f32 v102, v52, v53\n\t"
      "v_cvt_pk_bf16_f32 v103, v54, v55\n\t"
      "v_mfma_f32_4x4x4_16b_bf16 v[40:43], a[180:181], v[104:105], 0\n\t"
      "ds_read_b64_tr_b16 v[120:121], v60 offset:49216\n\t"
      "v_mfma_f32_4x4x4_16b_bf16 v[44:47], a[182:183], v[106:107], 0\n\t"
      "ds_read_b64_tr_b16 v[122:123], v60 offset:49984\n\t"
      "v_mfma_f32_4x4x4_16b_bf16 v[48:51], a[180:181], v[108:109], 0\n\t"
      "ds_read_b64_tr_b16 v[124:125], v61 offset:49216\n\t"
      "v_mfma_f32_4x4x4_16b_bf16 v[52:55], a[182:183], v[110:111], 0\n\t"
      "ds_read_b64_tr_b16 v[126:127], v61 offset:49984\n\t"
      "buffer_load_dwordx4 v11, s[84:87], s54 offen offset:2048 lds\n\t"
      "s_add_u32 m0, s17, 114176\n\t"
      "s_mov_b32 s54, 159744\n\t"
      "buffer_load_dwordx4 v9, s[84:87], s54 offen lds\n\t"
      "v_mfma_f32_16x16x32_bf16 v[32:35], a[68:71], a[232:235], v[32:35]\n\t"
      "v_cvt_pk_bf16_f32 v104, v40, v41\n\t"
      "v_cvt_pk_bf16_f32 v105, v42, v43\n\t"
      "v_mfma_f32_16x16x32_bf16 v[36:39], a[72:75], a[232:235], v[36:39]\n\t"
      "v_cvt_pk_bf16_f32 v106, v44, v45\n\t"
      "v_cvt_pk_bf16_f32 v107, v46, v47\n\t"
      "s_waitcnt lgkmcnt(9)\n\t"
      "v_mfma_f32_16x16x32_bf16 a[156:159], v[80:83], a[236:239], a[156:159]\n\t"
      "v_cvt_pk_bf16_f32 v108, v48, v49\n\t"
      "v_cvt_pk_bf16_f32 v109, v50, v51\n\t"
      "v_mfma_f32_16x16x32_bf16 a[160:163], v[84:87], a[236:239], a[160:163]\n\t"
      "v_cvt_pk_bf16_f32 v110, v52, v53\n\t"
      "v_cvt_pk_bf16_f32 v111, v54, v55\n\t"
      "ds_read_b128 a[240:243], v58 offset:25088\n\t"
      "v_mfma_f32_4x4x4_16b_bf16 v[40:43], a[180:181], a[76:77], 0\n\t"
      "ds_read_b64_tr_b16 a[84:85], v60 offset:49280\n\t"
      "v_mfma_f32_4x4x4_16b_bf16 v[44:47], a[182:183], a[78:79], 0\n\t"
      "ds_read_b64_tr_b16 a[86:87], v60 offset:50048\n\t"
      "v_mfma_f32_4x4x4_16b_bf16 v[48:51], a[180:181], a[80:81], 0\n\t"
      "ds_read_b64_tr_b16 a[88:89], v61 offset:49280\n\t"
      "v_mfma_f32_4x4x4_16b_bf16 v[52:55], a[182:183], a[82:83], 0\n\t"
      "ds_read_b64_tr_b16 a[90:91], v61 offset:50048\n\t"
      "buffer_load_dwordx4 v10, s[84:87], s54 offen offset:1024 lds\n\t"
      "buffer_load_dwordx4 v11, s[84:87], s54 offen offset:2048 lds\n\t"
      "ds_read_b128 a[244:247], v58 offset:37376\n\t"
      "v_mfma_f32_16x16x32_bf16 a[164:167], v[88:91], a[236:239], a[164:167]\n\t"
      "v_cvt_pk_bf16_f32 v40, v40, v41\n\t"
      "v_cvt_pk_bf16_f32 v41, v42, v43\n\t"
      "v_mfma_f32_16x16x32_bf16 a[168:171], v[92:95], a[236:239], a[168:171]\n\t"
      "v_cvt_pk_bf16_f32 v44, v44, v45\n\t"
      "v_cvt_pk_bf16_f32 v45, v46, v47\n\t"
      "v_mfma_f32_16x16x32_bf16 a[172:175], a[68:71], a[236:239], a[172:175]\n\t"
      "v_cvt_pk_bf16_f32 v48, v48, v49\n\t"
      "v_cvt_pk_bf16_f32 v49, v50, v51\n\t"
      "v_mfma_f32_16x16x32_bf16 a[176:179], a[72:75], a[236:239], a[176:179]\n\t"
      "v_cvt_pk_bf16_f32 v52, v52, v53\n\t"
      "v_cvt_pk_bf16_f32 v53, v54, v55\n\t"
      "v_accvgpr_write_b32 a76, v40\n\t"
      "v_accvgpr_write_b32 a77, v41\n\t"
      "v_accvgpr_write_b32 a78, v44\n\t"
      "v_accvgpr_write_b32 a79, v45\n\t"
      "v_accvgpr_write_b32 a80, v48\n\t"
      "v_accvgpr_write_b32 a81, v49\n\t"
      "v_accvgpr_write_b32 a82, v52\n\t"
      "v_accvgpr_write_b32 a83, v53\n\t"
      "s_waitcnt vmcnt(12)\n\t"
      "s_waitcnt lgkmcnt(1)\n\t"
      "; step\n\t"
      "v_mfma_f32_4x4x4_16b_bf16 v[40:43], a[184:185], v[112:113], 0\n\t"
      "ds_read_b128 a[180:183], v14 offset:1024\n\t"
      "v_mfma_f32_4x4x4_16b_bf16 v[44:47], a[186:187], v[114:115], 0\n\t"
      "ds_read_b64_tr_b16 v[128:129], v12 offset:0\n\t"
      "v_mfma_f32_4x4x4_16b_bf16 v[48:51], a[184:185], v[116:117], 0\n\t"
      "ds_read_b64_tr_b16 v[130:131], v12 offset:768\n\t"
      "v_mfma_f32_4x4x4_16b_bf16 v[52:55], a[186:187], v[118:119], 0\n\t"
      "ds_read_b64_tr_b16 v[132:133], v13 offset:0\n\t"
      "ds_read_b64_tr_b16 v[134:135], v13 offset:768\n\t"
      "s_add_u32 m0, s17, 135680\n\t"
      "s_mov_b32 s54, 172032\n\t"
      "buffer_load_dwordx4 v9, s[84:87], s54 offen lds\n\t"
      "buffer_load_dwordx4 v10, s[84:87], s54 offen offset:1024 lds\n\t"
      "v_mfma_f32_16x16x32_bf16 v[16:19], v[96:99], a[240:243], v[16:19]\n\t"
      "v_cvt_pk_bf16_f32 v112, v40, v41\n\t"
      "v_cvt_pk_bf16_f32 v113, v42, v43\n\t"
      "v_mfma_f32_16x16x32_bf16 v[20:23], v[100:103], a[240:243], v[20:23]\n\t"
      "v_cvt_pk_bf16_f32 v114, v44, v45\n\t"
      "v_cvt_pk_bf16_f32 v115, v46, v47\n\t"
      "v_mfma_f32_16x16x32_bf16 v[24:27], v[104:107], a[240:243], v[24:27]\n\t"
      "v_cvt_pk_bf16_f32 v116, v48, v49\n\t"
      "v_cvt_pk_bf16_f32 v117, v50, v51\n\t"
      "v_mfma_f32_16x16x32_bf16 v[28:31], v[108:111], a[240:243], v[28:31]\n\t"
      "v_cvt_pk_bf16_f32 v118, v52, v53\n\t"
      "v_cvt_pk_bf16_f32 v119, v54, v55\n\t"
      "v_mfma_f32_4x4x4_16b_bf16 v[40:43], a[184:185], v[120:121], 0\n\t"
      "ds_read_b64_tr_b16 v[136:137], v12 offset:64\n\t"
      "v_mfma_f32_4x4x4_16b_bf16 v[44:47], a[186:187], v[122:123], 0\n\t"
      "ds_read_b64_tr_b16 v[138:139], v12 offset:832\n\t"
      "v_mfma_f32_4x4x4_16b_bf16 v[48:51], a[184:185], v[124:125], 0\n\t"
      "ds_read_b64_tr_b16 v[140:141], v13 offset:64\n\t"
      "v_mfma_f32_4x4x4_16b_bf16 v[52:55], a[186:187], v[126:127], 0\n\t"
      "ds_read_b64_tr_b16 v[142:143], v13 offset:832\n\t"
      "buffer_load_dwordx4 v11, s[84:87], s54 offen offset:2048 lds\n\t"
      "s_add_u32 m0, s17, 138752\n\t"
      "s_mov_b32 s54, 184320\n\t"
      "buffer_load_dwordx4 v9, s[84:87], s54 offen lds\n\t"
      "v_mfma_f32_16x16x32_bf16 v[32:35], a[76:79], a[240:243], v[32:35]\n\t"
      "v_cvt_pk_bf16_f32 v120, v40, v41\n\t"
      "v_cvt_pk_bf16_f32 v121, v42, v43\n\t"
      "v_mfma_f32_16x16x32_bf16 v[36:39], a[80:83], a[240:243], v[36:39]\n\t"
      "v_cvt_pk_bf16_f32 v122, v44, v45\n\t"
      "v_cvt_pk_bf16_f32 v123, v46, v47\n\t"
      "s_waitcnt lgkmcnt(9)\n\t"
      "v_mfma_f32_16x16x32_bf16 a[156:159], v[96:99], a[244:247], a[156:159]\n\t"
      "v_cvt_pk_bf16_f32 v124, v48, v49\n\t"
      "v_cvt_pk_bf16_f32 v125, v50, v51\n\t"
      "v_mfma_f32_16x16x32_bf16 a[160:163], v[100:103], a[244:247], a[160:163]\n\t"
      "v_cvt_pk_bf16_f32 v126, v52, v53\n\t"
      "v_cvt_pk_bf16_f32 v127, v54, v55\n\t"
      "ds_read_b128 a[224:227], v59 offset:25088\n\t"
      "v_mfma_f32_4x4x4_16b_bf16 v[40:43], a[184:185], a[84:85], 0\n\t"
      "ds_read_b64_tr_b16 a[92:93], v12 offset:128\n\t"
      "v_mfma_f32_4x4x4_16b_bf16 v[44:47], a[186:187], a[86:87], 0\n\t"
      "ds_read_b64_tr_b16 a[94:95], v12 offset:896\n\t"
      "v_mfma_f32_4x4x4_16b_bf16 v[48:51], a[184:185], a[88:89], 0\n\t"
      "ds_read_b64_tr_b16 a[96:97], v13 offset:128\n\t"
      "v_mfma_f32_4x4x4_16b_bf16 v[52:55], a[186:187], a[90:91], 0\n\t"
      "ds_read_b64_tr_b16 a[98:99], v13 offset:896\n\t"
      "buffer_load_dwordx4 v10, s[84:87], s54 offen offset:1024 lds\n\t"
      "buffer_load_dwordx4 v11, s[84:87], s54 offen offset:2048 lds\n\t"
      "ds_read_b128 a[228:231], v59 offset:37376\n\t"
      "v_mfma_f32_16x16x32_bf16 a[164:167], v[104:107], a[244:247], a[164:167]\n\t"
      "v_cvt_pk_bf16_f32 v40, v40, v41\n\t"
      "v_cvt_pk_bf16_f32 v41, v42, v43\n\t"
      "v_mfma_f32_16x16x32_bf16 a[168:171], v[108:111], a[244:247], a[168:171]\n\t"
      "v_cvt_pk_bf16_f32 v44, v44, v45\n\t"
      "v_cvt_pk_bf16_f32 v45, v46, v47\n\t"
      "v_mfma_f32_16x16x32_bf16 a[172:175], a[76:79], a[244:247], a[172:175]\n\t"
      "v_cvt_pk_bf16_f32 v48, v48, v49\n\t"
      "v_cvt_pk_bf16_f32 v49, v50, v51\n\t"
      "v_mfma_f32_16x16x32_bf16 a[176:179], a[80:83], a[244:247], a[176:179]\n\t"
      "v_cvt_pk_bf16_f32 v52, v52, v53\n\t"
      "v_cvt_pk_bf16_f32 v53, v54, v55\n\t"
      "v_accvgpr_write_b32 a84, v40\n\t"
      "v_accvgpr_write_b32 a85, v41\n\t"
      "v_accvgpr_write_b32 a86, v44\n\t"
      "v_accvgpr_write_b32 a87, v45\n\t"
      "v_accvgpr_write_b32 a88, v48\n\t"
      "v_accvgpr_write_b32 a89, v49\n\t"
      "v_accvgpr_write_b32 a90, v52\n\t"
      "v_accvgpr_write_b32 a91, v53\n\t"
      "s_waitcnt vmcnt(12)\n\t"
      "s_waitcnt lgkmcnt(1)\n\t"
      "; step\n\t"
      "v_mfma_f32_4x4x4_16b_bf16 v[40:43], a[180:181], v[128:129], 0\n\t"
      "ds_read_b128 a[184:187], v14 offset:1280\n\t"
      "v_mfma_f32_4x4x4_16b_bf16 v[44:47], a[182:183], v[130:131], 0\n\t"
      "ds_read_b64_tr_b16 v[144:145], v60 offset:0\n\t"
      "v_mfma_f32_4x4x4_16b_bf16 v[48:51], a[180:181], v[132:133], 0\n\t"
      "ds_read_b64_tr_b16 v[146:147], v60 offset:768\n\t"
      "v_mfma_f32_4x4x4_16b_bf16 v[52:55], a[182:183], v[134:135], 0\n\t"
      "ds_read_b64_tr_b16 v[148:149], v61 offset:0\n\t"
      "ds_read_b64_tr_b16 v[150:151], v61 offset:768\n\t"
      "s_add_u32 m0, s17, 0\n\t"
      "s_mov_b32 s54, 196608\n\t"
      "buffer_load_dwordx4 v9, s[84:87], s54 offen lds\n\t"
      "buffer_load_dwordx4 v10, s[84:87], s54 offen offset:1024 lds\n\t"
      "v_mfma_f32_16x16x32_bf16 v[16:19], v[112:115], a[224:227], v[16:19]\n\t"
      "v_cvt_pk_bf16_f32 v128, v40, v41\n\t"
      "v_cvt_pk_bf16_f32 v129, v42, v43\n\t"
      "v_mfma_f32_16x16x32_bf16 v[20:23], v[116:119], a[224:227], v[20:23]\n\t"
      "v_cvt_pk_bf16_f32 v130, v44, v45\n\t"
      "v_cvt_pk_bf16_f32 v131, v46, v47\n\t"
      "v_mfma_f32_16x16x32_bf16 v[24:27], v[120:123], a[224:227], v[24:27]\n\t"
      "v_cvt_pk_bf16_f32 v132, v48, v49\n\t"
      "v_cvt_pk_bf16_f32 v133, v50, v51\n\t"
      "v_mfma_f32_16x16x32_bf16 v[28:31], v[124:127], a[224:227], v[28:31]\n\t"
      "v_cvt_pk_bf16_f32 v134, v52, v53\n\t"
      "v_cvt_pk_bf16_f32 v135, v54, v55\n\t"
      "v_mfma_f32_4x4x4_16b_bf16 v[40:43], a[180:181], v[136:137], 0\n\t"
      "ds_read_b64_tr_b16 v[152:153], v60 offset:64\n\t"
      "v_mfma_f32_4x4x4_16b_bf16 v[44:47], a[182:183], v[138:139], 0\n\t"
      "ds_read_b64_tr_b16 v[154:155], v60 offset:832\n\t"
      "v_mfma_f32_4x4x4_16b_bf16 v[48:51], a[180:181], v[140:141], 0\n\t"
      "ds_read_b64_tr_b16 v[156:157], v61 offset:64\n\t"
      "v_mfma_f32_4x4x4_16b_bf16 v[52:55], a[182:183], v[142:143], 0\n\t"
      "ds_read_b64_tr_b16 v[158:159], v61 offset:832\n\t"
      "buffer_load_dwordx4 v11, s[84:87], s54 offen offset:2048 lds\n\t"
      "s_add_u32 m0, s17, 3072\n\t"
      "s_mov_b32 s54, 208896\n\t"
      "buffer_load_dwordx4 v9, s[84:87], s54 offen lds\n\t"
      "v_mfma_f32_16x16x32_bf16 v[32:35], a[84:87], a[224:227], v[32:35]\n\t"
      "v_cvt_pk_bf16_f32 v136, v40, v41\n\t"
      "v_cvt_pk_bf16_f32 v137, v42, v43\n\t"
      "v_mfma_f32_16x16x32_bf16 v[36:39], a[88:91], a[224:227], v[36:39]\n\t"
      "v_cvt_pk_bf16_f32 v138, v44, v45\n\t"
      "v_cvt_pk_bf16_f32 v139, v46, v47\n\t"
      "s_waitcnt lgkmcnt(9)\n\t"
      "v_mfma_f32_16x16x32_bf16 a[156:159], v[112:115], a[228:231], a[156:159]\n\t"
      "v_cvt_pk_bf16_f32 v140, v48, v49\n\t"
      "v_cvt_pk_bf16_f32 v141, v50, v51\n\t"
      "v_mfma_f32_16x16x32_bf16 a[160:163], v[116:119], a[228:231], a[160:163]\n\t"
      "v_cvt_pk_bf16_f32 v142, v52, v53\n\t"
      "v_cvt_pk_bf16_f32 v143, v54, v55\n\t"
      "ds_read_b128 a[232:235], v56 offset:25344\n\t"
      "v_mfma_f32_4x4x4_16b_bf16 v[40:43], a[180:181], a[92:93], 0\n\t"
      "ds_read_b64_tr_b16 a[100:101], v60 offset:128\n\t"
      "v_mfma_f32_4x4x4_16b_bf16 v[44:47], a[182:183], a[94:95], 0\n\t"
      "ds_read_b64_tr_b16 a[102:103], v60 offset:896\n\t"
      "v_mfma_f32_4x4x4_16b_bf16 v[48:51], a[180:181], a[96:97], 0\n\t"
      "ds_read_b64_tr_b16 a[104:105], v61 offset:128\n\t"
      "v_mfma_f32_4x4x4_16b_bf16 v[52:55], a[182:183], a[98:99], 0\n\t"
      "ds_read_b64_tr_b16 a[106:107], v61 offset:896\n\t"
      "buffer_load_dwordx4 v10, s[84:87], s54 offen offset:1024 lds\n\t"
      "buffer_load_dwordx4 v11, s[84:87], s54 offen offset:2048 lds\n\t"
      "ds_read_b128 a[236:239], v56 offset:37632\n\t"
      "v_mfma_f32_16x16x32_bf16 a[164:167], v[120:123], a[228:231], a[164:167]\n\t"
      "v_cvt_pk_bf16_f32 v40, v40, v41\n\t"
      "v_cvt_pk_bf16_f32 v41, v42, v43\n\t"
      "v_mfma_f32_16x16x32_bf16 a[168:171], v[124:127], a[228:231], a[168:171]\n\t"
      "v_cvt_pk_bf16_f32 v44, v44, v45\n\t"
      "v_cvt_pk_bf16_f32 v45, v46, v47\n\t"
      "v_mfma_f32_16x16x32_bf16 a[172:175], a[84:87], a[228:231], a[172:175]\n\t"
      "v_cvt_pk_bf16_f32 v48, v48, v49\n\t"
      "v_cvt_pk_bf16_f32 v49, v50, v51\n\t"
      "v_mfma_f32_16x16x32_bf16 a[176:179], a[88:91], a[228:231], a[176:179]\n\t"
      "v_cvt_pk_bf16_f32 v52, v52, v53\n\t"
      "v_cvt_pk_bf16_f32 v53, v54, v55\n\t"
      "v_accvgpr_write_b32 a92, v40\n\t"
      "v_accvgpr_write_b32 a93, v41\n\t"
      "v_accvgpr_write_b32 a94, v44\n\t"
      "v_accvgpr_write_b32 a95, v45\n\t"
      "v_accvgpr_write_b32 a96, v48\n\t"
      "v_accvgpr_write_b32 a97, v49\n\t"
      "v_accvgpr_write_b32 a98, v52\n\t"
      "v_accvgpr_write_b32 a99, v53\n\t"
      "s_waitcnt vmcnt(12)\n\t"
      "s_waitcnt lgkmcnt(1)\n\t"
      "; step\n\t"
      "v_mfma_f32_4x4x4_16b_bf16 v[40:43], a[184:185], v[144:145], 0\n\t"
      "ds_read_b128 a[180:183], v14 offset:1536\n\t"
      "v_mfma_f32_4x4x4_16b_bf16 v[44:47], a[186:187], v[146:147], 0\n\t"
      "ds_read_b64_tr_b16 v[160:161], v60 offset:24576\n\t"
      "v_mfma_f32_4x4x4_16b_bf16 v[48:51], a[184:185], v[148:149], 0\n\t"
      "ds_read_b64_tr_b16 v[162:163], v60 offset:25344\n\t"
      "v_mfma_f32_4x4x4_16b_bf16 v[52:55], a[186:187], v[150:151], 0\n\t"
      "ds_read_b64_tr_b16 v[164:165], v61 offset:24576\n\t"
      "ds_read_b64_tr_b16 v[166:167], v61 offset:25344\n\t"
      "s_add_u32 m0, s17, 86528\n\t"
      "s_mov_b32 s54, 221184\n\t"
      "buffer_load_dwordx4 v9, s[84:87], s54 offen lds\n\t"
      "buffer_load_dwordx4 v10, s[84:87], s54 offen offset:1024 lds\n\t"
      "v_mfma_f32_16x16x32_bf16 v[16:19], v[128:131], a[232:235], v[16:19]\n\t"
      "v_cvt_pk_bf16_f32 v144, v40, v41\n\t"
      "v_cvt_pk_bf16_f32 v145, v42, v43\n\t"
      "v_mfma_f32_16x16x32_bf16 v[20:23], v[132:135], a[232:235], v[20:23]\n\t"
      "v_cvt_pk_bf16_f32 v146, v44, v45\n\t"
      "v_cvt_pk_bf16_f32 v147, v46, v47\n\t"
      "v_mfma_f32_16x16x32_bf16 v[24:27], v[136:139], a[232:235], v[24:27]\n\t"
      "v_cvt_pk_bf16_f32 v148, v48, v49\n\t"
      "v_cvt_pk_bf16_f32 v149, v50, v51\n\t"
      "v_mfma_f32_16x16x32_bf16 v[28:31], v[140:143], a[232:235], v[28:31]\n\t"
      "v_cvt_pk_bf16_f32 v150, v52, v53\n\t"
      "v_cvt_pk_bf16_f32 v151, v54, v55\n\t"
      "v_mfma_f32_4x4x4_16b_bf16 v[40:43], a[184:185], v[152:153], 0\n\t"
      "ds_read_b64_tr_b16 v[168:169], v60 offset:24640\n\t"
      "v_mfma_f32_4x4x4_16b_bf16 v[44:47], a[186:187], v[154:155], 0\n\t"
      "ds_read_b64_tr_b16 v[170:171], v60 offset:25408\n\t"
      "v_mfma_f32_4x4x4_16b_bf16 v[48:51], a[184:185], v[156:157], 0\n\t"
      "ds_read_b64_tr_b16 v[172:173], v61 offset:24640\n\t"
      "v_mfma_f32_4x4x4_16b_bf16 v[52:55], a[186:187], v[158:159], 0\n\t"
      "ds_read_b64_tr_b16 v[174:175], v61 offset:25408\n\t"
      "buffer_load_dwordx4 v11, s[84:87], s54 offen offset:2048 lds\n\t"
      "s_add_u32 m0, s17, 89600\n\t"
      "s_mov_b32 s54, 233472\n\t"
      "buffer_load_dwordx4 v9, s[84:87], s54 offen lds\n\t"
      "v_mfma_f32_16x16x32_bf16 v[32:35], a[92:95], a[232:235], v[32:35]\n\t"
      "v_cvt_pk_bf16_f32 v152, v40, v41\n\t"
      "v_cvt_pk_bf16_f32 v153, v42, v43\n\t"
      "v_mfma_f32_16x16x32_bf16 v[36:39], a[96:99], a[232:235], v[36:39]\n\t"
      "v_cvt_pk_bf16_f32 v154, v44, v45\n\t"
      "v_cvt_pk_bf16_f32 v155, v46, v47\n\t"
      "s_waitcnt lgkmcnt(9)\n\t"
      "v_mfma_f32_16x16x32_bf16 a[156:159], v[128:131], a[236:239], a[156:159]\n\t"
      "v_cvt_pk_bf16_f32 v156, v48, v49\n\t"
      "v_cvt_pk_bf16_f32 v157, v50, v51\n\t"
      "v_mfma_f32_16x16x32_bf16 a[160:163], v[132:135], a[236:239], a[160:163]\n\t"
      "v_cvt_pk_bf16_f32 v158, v52, v53\n\t"
      "v_cvt_pk_bf16_f32 v159, v54, v55\n\t"
      "ds_read_b128 a[240:243], v57 offset:25344\n\t"
      "v_mfma_f32_4x4x4_16b_bf16 v[40:43], a[184:185], a[100:101], 0\n\t"
      "ds_read_b64_tr_b16 a[108:109], v60 offset:24704\n\t"
      "v_mfma_f32_4x4x4_16b_bf16 v[44:47], a[186:187], a[102:103], 0\n\t"
      "ds_read_b64_tr_b16 a[110:111], v60 offset:25472\n\t"
      "v_mfma_f32_4x4x4_16b_bf16 v[48:51], a[184:185], a[104:105], 0\n\t"
      "ds_read_b64_tr_b16 a[112:113], v61 offset:24704\n\t"
      "v_mfma_f32_4x4x4_16b_bf16 v[52:55], a[186:187], a[106:107], 0\n\t"
      "ds_read_b64_tr_b16 a[114:115], v61 offset:25472\n\t"
      "buffer_load_dwordx4 v10, s[84:87], s54 offen offset:1024 lds\n\t"
      "buffer_load_dwordx4 v11, s[84:87], s54 offen offset:2048 lds\n\t"
      "ds_read_b128 a[244:247], v57 offset:37632\n\t"
      "v_mfma_f32_16x16x32_bf16 a[164:167], v[136:139], a[236:239], a[164:167]\n\t"
      "v_cvt_pk_bf16_f32 v40, v40, v41\n\t"
      "v_cvt_pk_bf16_f32 v41, v42, v43\n\t"
      "v_mfma_f32_16x16x32_bf16 a[168:171], v[140:143], a[236:239], a[168:171]\n\t"
      "v_cvt_pk_bf16_f32 v44, v44, v45\n\t"
      "v_cvt_pk_bf16_f32 v45, v46, v47\n\t"
      "v_mfma_f32_16x16x32_bf16 a[172:175], a[92:95], a[236:239], a[172:175]\n\t"
      "v_cvt_pk_bf16_f32 v48, v48, v49\n\t"
      "v_cvt_pk_bf16_f32 v49, v50, v51\n\t"
      "v_mfma_f32_16x16x32_bf16 a[176:179], a[96:99], a[236:239], a[176:179]\n\t"
      "v_cvt_pk_bf16_f32 v52, v52, v53\n\t"
      "v_cvt_pk_bf16_f32 v53, v54, v55\n\t"
      "v_accvgpr_write_b32 a100, v40\n\t"
      "v_accvgpr_write_b32 a101, v41\n\t"
      "v_accvgpr_write_b32 a102, v44\n\t"
      "v_accvgpr_write_b32 a103, v45\n\t"
      "v_accvgpr_write_b32 a104, v48\n\t"
      "v_accvgpr_write_b32 a105, v49\n\t"
      "v_accvgpr_write_b32 a106, v52\n\t"
      "v_accvgpr_write_b32 a107, v53\n\t"
      "s_waitcnt vmcnt(12)\n\t"
      "s_waitcnt lgkmcnt(1)\n\t"
      "; step\n\t"
      "v_mfma_f32_4x4x4_16b_bf16 v[40:43], a[180:181], v[160:161], 0\n\t"
      "ds_read_b128 a[184:187], v14 offset:1792\n\t"
      "v_mfma_f32_4x4x4_16b_bf16 v[44:47], a[182:183], v[162:163], 0\n\t"
      "ds_read_b64_tr_b16 v[176:177], v60 offset:49152\n\t"
      "v_mfma_f32_4x4x4_16b_bf16 v[48:51], a[180:181], v[164:165], 0\n\t"
      "ds_read_b64_tr_b16 v[178:179], v60 offset:49920\n\t"
      "v_mfma_f32_4x4x4_16b_bf16 v[52:55], a[182:183], v[166:167], 0\n\t"
      "ds_read_b64_tr_b16 v[180:181], v61 offset:49152\n\t"
      "ds_read_b64_tr_b16 v[182:183], v61 offset:49920\n\t"
      "s_add_u32 m0, s17, 111104\n\t"
      "s_mov_b32 s54, 245760\n\t"
      "buffer_load_dwordx4 v9, s[84:87], s54 offen lds\n\t"
      "buffer_load_dwordx4 v10, s[84:87], s54 offen offset:1024 lds\n\t"
      "v_mfma_f32_16x16x32_bf16 v[16:19], v[144:147], a[240:243], v[16:19]\n\t"
      "v_cvt_pk_bf16_f32 v160, v40, v41\n\t"
      "v_cvt_pk_bf16_f32 v161, v42, v43\n\t"
      "v_mfma_f32_16x16x32_bf16 v[20:23], v[148:151], a[240:243], v[20:23]\n\t"
      "v_cvt_pk_bf16_f32 v162, v44, v45\n\t"
      "v_cvt_pk_bf16_f32 v163, v46, v47\n\t"
      "v_mfma_f32_16x16x32_bf16 v[24:27], v[152:155], a[240:243], v[24:27]\n\t"
      "v_cvt_pk_bf16_f32 v164, v48, v49\n\t"
      "v_cvt_pk_bf16_f32 v165, v50, v51\n\t"
      "v_mfma_f32_16x16x32_bf16 v[28:31], v[156:159], a[240:243], v[28:31]\n\t"
      "v_cvt_pk_bf16_f32 v166, v52, v53\n\t"
      "v_cvt_pk_bf16_f32 v167, v54, v55\n\t"
      "v_mfma_f32_4x4x4_16b_bf16 v[40:43], a[180:181], v[168:169], 0\n\t"
      "ds_read_b64_tr_b16 v[184:185], v60 offset:49216\n\t"
      "v_mfma_f32_4x4x4_16b_bf16 v[44:47], a[182:183], v[170:171], 0\n\t"
      "ds_read_b64_tr_b16 v[186:187], v60 offset:49984\n\t"
      "v_mfma_f32_4x4x4_16b_bf16 v[48:51], a[180:181], v[172:173], 0\n\t"
      "ds_read_b64_tr_b16 v[188:189], v61 offset:49216\n\t"
      "v_mfma_f32_4x4x4_16b_bf16 v[52:55], a[182:183], v[174:175], 0\n\t"
      "ds_read_b64_tr_b16 v[190:191], v61 offset:49984\n\t"
      "buffer_load_dwordx4 v11, s[84:87], s54 offen offset:2048 lds\n\t"
      "s_add_u32 m0, s17, 114176\n\t"
      "s_mov_b32 s54, 258048\n\t"
      "buffer_load_dwordx4 v9, s[84:87], s54 offen lds\n\t"
      "v_mfma_f32_16x16x32_bf16 v[32:35], a[100:103], a[240:243], v[32:35]\n\t"
      "v_cvt_pk_bf16_f32 v168, v40, v41\n\t"
      "v_cvt_pk_bf16_f32 v169, v42, v43\n\t"
      "v_mfma_f32_16x16x32_bf16 v[36:39], a[104:107], a[240:243], v[36:39]\n\t"
      "v_cvt_pk_bf16_f32 v170, v44, v45\n\t"
      "v_cvt_pk_bf16_f32 v171, v46, v47\n\t"
      "s_waitcnt lgkmcnt(9)\n\t"
      "v_mfma_f32_16x16x32_bf16 a[156:159], v[144:147], a[244:247], a[156:159]\n\t"
      "v_cvt_pk_bf16_f32 v172, v48, v49\n\t"
      "v_cvt_pk_bf16_f32 v173, v50, v51\n\t"
      "v_mfma_f32_16x16x32_bf16 a[160:163], v[148:151], a[244:247], a[160:163]\n\t"
      "v_cvt_pk_bf16_f32 v174, v52, v53\n\t"
      "v_cvt_pk_bf16_f32 v175, v54, v55\n\t"
      "ds_read_b128 a[224:227], v58 offset:25344\n\t"
      "v_mfma_f32_4x4x4_16b_bf16 v[40:43], a[180:181], a[108:109], 0\n\t"
      "ds_read_b64_tr_b16 a[116:117], v60 offset:49280\n\t"
      "v_mfma_f32_4x4x4_16b_bf16 v[44:47], a[182:183], a[110:111], 0\n\t"
      "ds_read_b64_tr_b16 a[118:119], v60 offset:50048\n\t"
      "v_mfma_f32_4x4x4_16b_bf16 v[48:51], a[180:181], a[112:113], 0\n\t"
      "ds_read_b64_tr_b16 a[120:121], v61 offset:49280\n\t"
      "v_mfma_f32_4x4x4_16b_bf16 v[52:55], a[182:183], a[114:115], 0\n\t"
      "ds_read_b64_tr_b16 a[122:123], v61 offset:50048\n\t"
      "buffer_load_dwordx4 v10, s[84:87], s54 offen offset:1024 lds\n\t"
      "buffer_load_dwordx4 v11, s[84:87], s54 offen offset:2048 lds\n\t"
      "ds_read_b128 a[228:231], v58 offset:37632\n\t"
      "v_mfma_f32_16x16x32_bf16 a[164:167], v[152:155], a[244:247], a[164:167]\n\t"
      "v_cvt_pk_bf16_f32 v40, v40, v41\n\t"
      "v_cvt_pk_bf16_f32 v41, v42, v43\n\t"
      "v_mfma_f32_16x16x32_bf16 a[168:171], v[156:159], a[244:247], a[168:171]\n\t"
      "v_cvt_pk_bf16_f32 v44, v44, v45\n\t"
      "v_cvt_pk_bf16_f32 v45, v46, v47\n\t"
      "v_mfma_f32_16x16x32_bf16 a[172:175], a[100:103], a[244:247], a[172:175]\n\t"
      "v_cvt_pk_bf16_f32 v48, v48, v49\n\t"
      "v_cvt_pk_bf16_f32 v49, v50, v51\n\t"
      "v_mfma_f32_16x16x32_bf16 a[176:179], a[104:107], a[244:247], a[176:179]\n\t"
      "v_cvt_pk_bf16_f32 v52, v52, v53\n\t"
      "v_cvt_pk_bf16_f32 v53, v54, v55\n\t"
      "v_accvgpr_write_b32 a108, v40\n\t"
      "v_accvgpr_write_b32 a109, v41\n\t"
      "v_accvgpr_write_b32 a110, v44\n\t"
      "v_accvgpr_write_b32 a111, v45\n\t"
      "v_accvgpr_write_b32 a112, v48\n\t"
      "v_accvgpr_write_b32 a113, v49\n\t"
      "v_accvgpr_write_b32 a114, v52\n\t"
      "v_accvgpr_write_b32 a115, v53\n\t"
      "s_waitcnt vmcnt(12)\n\t"
      "s_waitcnt lgkmcnt(1)\n\t"
      "; step\n\t"
      "v_mfma_f32_4x4x4_16b_bf16 v[40:43], a[184:185], v[176:177], 0\n\t"
      "ds_read_b128 a[180:183], v14 offset:2048\n\t"
      "v_mfma_f32_4x4x4_16b_bf16 v[44:47], a[186:187], v[178:179], 0\n\t"
      "ds_read_b64_tr_b16 v[192:193], v12 offset:0\n\t"
      "v_mfma_f32_4x4x4_16b_bf16 v[48:51], a[184:185], v[180:181], 0\n\t"
      "ds_read_b64_tr_b16 v[194:195], v12 offset:768\n\t"
      "v_mfma_f32_4x4x4_16b_bf16 v[52:55], a[186:187], v[182:183], 0\n\t"
      "ds_read_b64_tr_b16 v[196:197], v13 offset:0\n\t"
      "ds_read_b64_tr_b16 v[198:199], v13 offset:768\n\t"
      "s_add_u32 m0, s17, 135680\n\t"
      "s_mov_b32 s54, 270336\n\t"
      "buffer_load_dwordx4 v9, s[84:87], s54 offen lds\n\t"
      "buffer_load_dwordx4 v10, s[84:87], s54 offen offset:1024 lds\n\t"
      "v_mfma_f32_16x16x32_bf16 v[16:19], v[160:163], a[224:227], v[16:19]\n\t"
      "v_cvt_pk_bf16_f32 v176, v40, v41\n\t"
      "v_cvt_pk_bf16_f32 v177, v42, v43\n\t"
      "v_mfma_f32_16x16x32_bf16 v[20:23], v[164:167], a[224:227], v[20:23]\n\t"
      "v_cvt_pk_bf16_f32 v178, v44, v45\n\t"
      "v_cvt_pk_bf16_f32 v179, v46, v47\n\t"
      "v_mfma_f32_16x16x32_bf16 v[24:27], v[168:171], a[224:227], v[24:27]\n\t"
      "v_cvt_pk_bf16_f32 v180, v48, v49\n\t"
      "v_cvt_pk_bf16_f32 v181, v50, v51\n\t"
      "v_mfma_f32_16x16x32_bf16 v[28:31], v[172:175], a[224:227], v[28:31]\n\t"
      "v_cvt_pk_bf16_f32 v182, v52, v53\n\t"
      "v_cvt_pk_bf16_f32 v183, v54, v55\n\t"
      "v_mfma_f32_4x4x4_16b_bf16 v[40:43], a[184:185], v[184:185], 0\n\t"
      "ds_read_b64_tr_b16 v[200:201], v12 offset:64\n\t"
      "v_mfma_f32_4x4x4_16b_bf16 v[44:47], a[186:187], v[186:187], 0\n\t"
      "ds_read_b64_tr_b16 v[202:203], v12 offset:832\n\t"
      "v_mfma_f32_4x4x4_16b_bf16 v[48:51], a[184:185], v[188:189], 0\n\t"
      "ds_read_b64_tr_b16 v[204:205], v13 offset:64\n\t"
      "v_mfma_f32_4x4x4_16b_bf16 v[52:55], a[186:187], v[190:191], 0\n\t"
      "ds_read_b64_tr_b16 v[206:207], v13 offset:832\n\t"
      "buffer_load_dwordx4 v11, s[84:87], s54 offen offset:2048 lds\n\t"
      "s_add_u32 m0, s17, 138752\n\t"
      "s_mov_b32 s54, 282624\n\t"
      "buffer_load_dwordx4 v9, s[84:87], s54 offen lds\n\t"
      "v_mfma_f32_16x16x32_bf16 v[32:35], a[108:111], a[224:227], v[32:35]\n\t"
      "v_cvt_pk_bf16_f32 v184, v40, v41\n\t"
      "v_cvt_pk_bf16_f32 v185, v42, v43\n\t"
      "v_mfma_f32_16x16x32_bf16 v[36:39], a[112:115], a[224:227], v[36:39]\n\t"
      "v_cvt_pk_bf16_f32 v186, v44, v45\n\t"
      "v_cvt_pk_bf16_f32 v187, v46, v47\n\t"
      "s_waitcnt lgkmcnt(9)\n\t"
      "v_mfma_f32_16x16x32_bf16 a[156:159], v[160:163], a[228:231], a[156:159]\n\t"
      "v_cvt_pk_bf16_f32 v188, v48, v49\n\t"
      "v_cvt_pk_bf16_f32 v189, v50, v51\n\t"
      "v_mfma_f32_16x16x32_bf16 a[160:163], v[164:167], a[228:231], a[160:163]\n\t"
      "v_cvt_pk_bf16_f32 v190, v52, v53\n\t"
      "v_cvt_pk_bf16_f32 v191, v54, v55\n\t"
      "ds_read_b128 a[232:235], v59 offset:25344\n\t"
      "v_mfma_f32_4x4x4_16b_bf16 v[40:43], a[184:185], a[116:117], 0\n\t"
      "ds_read_b64_tr_b16 a[124:125], v12 offset:128\n\t"
      "v_mfma_f32_4x4x4_16b_bf16 v[44:47], a[186:187], a[118:119], 0\n\t"
      "ds_read_b64_tr_b16 a[126:127], v12 offset:896\n\t"
      "v_mfma_f32_4x4x4_16b_bf16 v[48:51], a[184:185], a[120:121], 0\n\t"
      "ds_read_b64_tr_b16 a[128:129], v13 offset:128\n\t"
      "v_mfma_f32_4x4x4_16b_bf16 v[52:55], a[186:187], a[122:123], 0\n\t"
      "ds_read_b64_tr_b16 a[130:131], v13 offset:896\n\t"
      "buffer_load_dwordx4 v10, s[84:87], s54 offen offset:1024 lds\n\t"
      "buffer_load_dwordx4 v11, s[84:87], s54 offen offset:2048 lds\n\t"
      "ds_read_b128 a[236:239], v59 offset:37632\n\t"
      "s_or_b32 s30, s17, s16\n\t"
      "s_cbranch_scc1 .Lno3_%=\n\t"
      "global_load_dword a252, v62, s[12:13] sc1\n\t"
      ".Lno3_%=:\n\t"
      "s_add_u32 m0, s18, 49664\n\t"
      "s_add_u32 s48, s15, 786432\n\t"
      "buffer_load_dwordx4 v2, s[40:43], s48 offen lds\n\t"
      "buffer_load_dwordx4 v3, s[40:43], s48 offen offset:1024 lds\n\t"
      "buffer_load_dwordx4 v4, s[40:43], s48 offen offset:2048 lds\n\t"
      "v_mfma_f32_16x16x32_bf16 a[164:167], v[168:171], a[228:231], a[164:167]\n\t"
      "v_cvt_pk_bf16_f32 v40, v40, v41\n\t"
      "v_cvt_pk_bf16_f32 v41, v42, v43\n\t"
      "v_mfma_f32_16x16x32_bf16 a[168:171], v[172:175], a[228:231], a[168:171]\n\t"
      "v_cvt_pk_bf16_f32 v44, v44, v45\n\t"
      "v_cvt_pk_bf16_f32 v45, v46, v47\n\t"
      "v_mfma_f32_16x16x32_bf16 a[172:175], a[108:111], a[228:231], a[172:175]\n\t"
      "v_cvt_pk_bf16_f32 v48, v48, v49\n\t"
      "v_cvt_pk_bf16_f32 v49, v50, v51\n\t"
      "v_mfma_f32_16x16x32_bf16 a[176:179], a[112:115], a[228:231], a[176:179]\n\t"
      "v_cvt_pk_bf16_f32 v52, v52, v53\n\t"
      "v_cvt_pk_bf16_f32 v53, v54, v55\n\t"
      "v_accvgpr_write_b32 a116, v40\n\t"
      "v_accvgpr_write_b32 a117, v41\n\t"
      "v_accvgpr_write_b32 a118, v44\n\t"
      "v_accvgpr_write_b32 a119, v45\n\t"
      "v_accvgpr_write_b32 a120, v48\n\t"
      "v_accvgpr_write_b32 a121, v49\n\t"
      "v_accvgpr_write_b32 a122, v52\n\t"
      "v_accvgpr_write_b32 a123, v53\n\t"
      "s_waitcnt vmcnt(15)\n\t"
      "s_waitcnt lgkmcnt(1)\n\t"
      "; step\n\t"
      "v_mfma_f32_4x4x4_16b_bf16 v[40:43], a[180:181], v[192:193], 0\n\t"
      "ds_read_b128 a[184:187], v14 offset:2304\n\t"
      "v_mfma_f32_4x4x4_16b_bf16 v[44:47], a[182:183], v[194:195], 0\n\t"
      "ds_read_b64_tr_b16 v[208:209], v60 offset:0\n\t"
      "v_mfma_f32_4x4x4_16b_bf16 v[48:51], a[180:181], v[196:197], 0\n\t"
      "ds_read_b64_tr_b16 v[210:211], v60 offset:768\n\t"
      "v_mfma_f32_4x4x4_16b_bf16 v[52:55], a[182:183], v[198:199], 0\n\t"
      "ds_read_b64_tr_b16 v[212:213], v61 offset:0\n\t"
      "ds_read_b64_tr_b16 v[214:215], v61 offset:768\n\t"
      "s_add_u32 m0, s18, 61952\n\t"
      "s_add_u32 s48, s15, 786432\n\t"
      "buffer_load_dwordx4 v5, s[40:43], s48 offen lds\n\t"
      "buffer_load_dwordx4 v6, s[40:43], s48 offen offset:1024 lds\n\t"
      "v_mfma_f32_16x16x32_bf16 v[16:19], v[176:179], a[232:235], v[16:19]\n\t"
      "v_cvt_pk_bf16_f32 v192, v40, v41\n\t"
      "v_cvt_pk_bf16_f32 v193, v42, v43\n\t"
      "v_mfma_f32_16x16x32_bf16 v[20:23], v[180:183], a[232:235], v[20:23]\n\t"
      "v_cvt_pk_bf16_f32 v194, v44, v45\n\t"
      "v_cvt_pk_bf16_f32 v195, v46, v47\n\t"
      "v_mfma_f32_16x16x32_bf16 v[24:27], v[184:187], a[232:235], v[24:27]\n\t"
      "v_cvt_pk_bf16_f32 v196, v48, v49\n\t"
      "v_cvt_pk_bf16_f32 v197, v50, v51\n\t"
      "v_mfma_f32_16x16x32_bf16 v[28:31], v[188:191], a[232:235], v[28:31]\n\t"
      "v_cvt_pk_bf16_f32 v198, v52, v53\n\t"
      "v_cvt_pk_bf16_f32 v199, v54, v55\n\t"
      "v_mfma_f32_4x4x4_16b_bf16 v[40:43], a[180:181], v[200:201], 0\n\t"
      "ds_read_b64_tr_b16 v[216:217], v60 offset:64\n\t"
      "v_mfma_f32_4x4x4_16b_bf16 v[44:47], a[182:183], v[202:203], 0\n\t"
      "ds_read_b64_tr_b16 v[218:219], v60 offset:832\n\t"
      "v_mfma_f32_4x4x4_16b_bf16 v[48:51], a[180:181], v[204:205], 0\n\t"
      "ds_read_b64_tr_b16 v[220:221], v61 offset:64\n\t"
      "v_mfma_f32_4x4x4_16b_bf16 v[52:55], a[182:183], v[206:207], 0\n\t"
      "ds_read_b64_tr_b16 v[222:223], v61 offset:832\n\t"
      "buffer_load_dwordx4 v7, s[40:43], s48 offen offset:2048 lds\n\t"
      "v_mfma_f32_16x16x32_bf16 v[32:35], a[116:119], a[232:235], v[32:35]\n\t"
      "v_cvt_pk_bf16_f32 v200, v40, v41\n\t"
      "v_cvt_pk_bf16_f32 v201, v42, v43\n\t"
      "v_mfma_f32_16x16x32_bf16 v[36:39], a[120:123], a[232:235], v[36:39]\n\t"
      "v_cvt_pk_bf16_f32 v202, v44, v45\n\t"
      "v_cvt_pk_bf16_f32 v203, v46, v47\n\t"
      "s_waitcnt lgkmcnt(9)\n\t"
      "v_mfma_f32_16x16x32_bf16 a[156:159], v[176:179], a[236:239], a[156:159]\n\t"
      "v_cvt_pk_bf16_f32 v204, v48, v49\n\t"
      "v_cvt_pk_bf16_f32 v205, v50, v51\n\t"
      "v_mfma_f32_16x16x32_bf16 a[160:163], v[180:183], a[236:239], a[160:163]\n\t"
      "v_cvt_pk_bf16_f32 v206, v52, v53\n\t"
      "v_cvt_pk_bf16_f32 v207, v54, v55\n\t"
      "ds_read_b128 a[240:243], v56 offset:25600\n\t"
      "v_mfma_f32_4x4x4_16b_bf16 v[40:43], a[180:181], a[124:125], 0\n\t"
      "ds_read_b64_tr_b16 a[132:133], v60 offset:128\n\t"
      "v_mfma_f32_4x4x4_16b_bf16 v[44:47], a[182:183], a[126:127], 0\n\t"
      "ds_read_b64_tr_b16 a[134:135], v60 offset:896\n\t"
      "v_mfma_f32_4x4x4_16b_bf16 v[48:51], a[180:181], a[128:129], 0\n\t"
      "ds_read_b64_tr_b16 a[136:137], v61 offset:128\n\t"
      "v_mfma_f32_4x4x4_16b_bf16 v[52:55], a[182:183], a[130:131], 0\n\t"
      "ds_read_b64_tr_b16 a[138:139], v61 offset:896\n\t"
      "ds_read_b128 a[244:247], v56 offset:37888\n\t"
      "v_mfma_f32_16x16x32_bf16 a[164:167], v[184:187], a[236:239], a[164:167]\n\t"
      "v_cvt_pk_bf16_f32 v40, v40, v41\n\t"
      "v_cvt_pk_bf16_f32 v41, v42, v43\n\t"
      "v_mfma_f32_16x16x32_bf16 a[168:171], v[188:191], a[236:239], a[168:171]\n\t"
      "v_cvt_pk_bf16_f32 v44, v44, v45\n\t"
      "v_cvt_pk_bf16_f32 v45, v46, v47\n\t"
      "v_mfma_f32_16x16x32_bf16 a[172:175], a[116:119], a[236:239], a[172:175]\n\t"
      "v_cvt_pk_bf16_f32 v48, v48, v49\n\t"
      "v_cvt_pk_bf16_f32 v49, v50, v51\n\t"
      "v_mfma_f32_16x16x32_bf16 a[176:179], a[120:123], a[236:239], a[176:179]\n\t"
      "v_cvt_pk_bf16_f32 v52, v52, v53\n\t"
      "v_cvt_pk_bf16_f32 v53, v54, v55\n\t"
      "v_accvgpr_write_b32 a124, v40\n\t"
      "v_accvgpr_write_b32 a125, v41\n\t"
      "v_accvgpr_write_b32 a126, v44\n\t"
      "v_accvgpr_write_b32 a127, v45\n\t"
      "v_accvgpr_write_b32 a128, v48\n\t"
      "v_accvgpr_write_b32 a129, v49\n\t"
      "v_accvgpr_write_b32 a130, v52\n\t"
      "v_accvgpr_write_b32 a131, v53\n\t"
      "s_waitcnt vmcnt(12)\n\t"
      "s_waitcnt lgkmcnt(1)\n\t"
      "; step\n\t"
      "v_mfma_f32_4x4x4_16b_bf16 v[40:43], a[184:185], v[208:209], 0\n\t"
      "ds_read_b128 a[180:183], v14 offset:2560\n\t"
      "v_mfma_f32_4x4x4_16b_bf16 v[44:47], a[186:187], v[210:211], 0\n\t"
      "ds_read_b64_tr_b16 v[224:225], v60 offset:24576\n\t"
      "v_mfma_f32_4x4x4_16b_bf16 v[48:51], a[184:185], v[212:213], 0\n\t"
      "ds_read_b64_tr_b16 v[226:227], v60 offset:25344\n\t"
      "v_mfma_f32_4x4x4_16b_bf16 v[52:55], a[186:187], v[214:215], 0\n\t"
      "ds_read_b64_tr_b16 v[228:229], v61 offset:24576\n\t"
      "ds_read_b64_tr_b16 v[230:231], v61 offset:25344\n\t"
      "v_mfma_f32_16x16x32_bf16 v[16:19], v[192:195], a[240:243], v[16:19]\n\t"
      "v_cvt_pk_bf16_f32 v208, v40, v41\n\t"
      "v_cvt_pk_bf16_f32 v209, v42, v43\n\t"
      "v_mfma_f32_16x16x32_bf16 v[20:23], v[196:199], a[240:243], v[20:23]\n\t"
      "v_cvt_pk_bf16_f32 v210, v44, v45\n\t"
      "v_cvt_pk_bf16_f32 v211, v46, v47\n\t"
      "v_mfma_f32_16x16x32_bf16 v[24:27], v[200:203], a[240:243], v[24:27]\n\t"
      "v_cvt_pk_bf16_f32 v212, v48, v49\n\t"
      "v_cvt_pk_bf16_f32 v213, v50, v51\n\t"
      "v_mfma_f32_16x16x32_bf16 v[28:31], v[204:207], a[240:243], v[28:31]\n\t"
      "v_cvt_pk_bf16_f32 v214, v52, v53\n\t"
      "v_cvt_pk_bf16_f32 v215, v54, v55\n\t"
      "v_mfma_f32_4x4x4_16b_bf16 v[40:43], a[184:185], v[216:217], 0\n\t"
      "ds_read_b64_tr_b16 v[232:233], v60 offset:24640\n\t"
      "v_mfma_f32_4x4x4_16b_bf16 v[44:47], a[186:187], v[218:219], 0\n\t"
      "ds_read_b64_tr_b16 v[234:235], v60 offset:25408\n\t"
      "v_mfma_f32_4x4x4_16b_bf16 v[48:51], a[184:185], v[220:221], 0\n\t"
      "ds_read_b64_tr_b16 v[236:237], v61 offset:24640\n\t"
      "v_mfma_f32_4x4x4_16b_bf16 v[52:55], a[186:187], v[222:223], 0\n\t"
      "ds_read_b64_tr_b16 v[238:239], v61 offset:25408\n\t"
      "v_mfma_f32_16x16x32_bf16 v[32:35], a[124:127], a[240:243], v[32:35]\n\t"
      "v_cvt_pk_bf16_f32 v216, v40, v41\n\t"
      "v_cvt_pk_bf16_f32 v217, v42, v43\n\t"
      "v_mfma_f32_16x16x32_bf16 v[36:39], a[128:131], a[240:243], v[36:39]\n\t"
      "v_cvt_pk_bf16_f32 v218, v44, v45\n\t"
      "v_cvt_pk_bf16_f32 v219, v46, v47\n\t"
      "s_waitcnt lgkmcnt(9)\n\t"
      "v_mfma_f32_16x16x32_bf16 a[156:159], v[192:195], a[244:247], a[156:159]\n\t"
      "v_cvt_pk_bf16_f32 v220, v48, v49\n\t"
      "v_cvt_pk_bf16_f32 v221, v50, v51\n\t"
      "v_mfma_f32_16x16x32_bf16 a[160:163], v[196:199], a[244:247], a[160:163]\n\t"
      "v_cvt_pk_bf16_f32 v222, v52, v53\n\t"
      "v_cvt_pk_bf16_f32 v223, v54, v55\n\t"
      "ds_read_b128 a[224:227], v57 offset:25600\n\t"
      "v_mfma_f32_4x4x4_16b_bf16 v[40:43], a[184:185], a[132:133], 0\n\t"
      "ds_read_b64_tr_b16 a[140:141], v60 offset:24704\n\t"
      "v_mfma_f32_4x4x4_16b_bf16 v[44:47], a[186:187], a[134:135], 0\n\t"
      "ds_read_b64_tr_b16 a[142:143], v60 offset:25472\n\t"
      "v_mfma_f32_4x4x4_16b_bf16 v[48:51], a[184:185], a[136:137], 0\n\t"
      "ds_read_b64_tr_b16 a[144:145], v61 offset:24704\n\t"
      "v_mfma_f32_4x4x4_16b_bf16 v[52:55], a[186:187], a[138:139], 0\n\t"
      "ds_read_b64_tr_b16 a[146:147], v61 offset:25472\n\t"
      "ds_read_b128 a[228:231], v57 offset:37888\n\t"
      "v_mfma_f32_16x16x32_bf16 a[164:167], v[200:203], a[244:247], a[164:167]\n\t"
      "v_cvt_pk_bf16_f32 v40, v40, v41\n\t"
      "v_cvt_pk_bf16_f32 v41, v42, v43\n\t"
      "v_mfma_f32_16x16x32_bf16 a[168:171], v[204:207], a[244:247], a[168:171]\n\t"
      "v_cvt_pk_bf16_f32 v44, v44, v45\n\t"
      "v_cvt_pk_bf16_f32 v45, v46, v47\n\t"
      "v_mfma_f32_16x16x32_bf16 a[172:175], a[124:127], a[244:247], a[172:175]\n\t"
      "v_cvt_pk_bf16_f32 v48, v48, v49\n\t"
      "v_cvt_pk_bf16_f32 v49, v50, v51\n\t"
      "v_mfma_f32_16x16x32_bf16 a[176:179], a[128:131], a[244:247], a[176:179]\n\t"
      "v_cvt_pk_bf16_f32 v52, v52, v53\n\t"
      "v_cvt_pk_bf16_f32 v53, v54, v55\n\t"
      "v_accvgpr_write_b32 a132, v40\n\t"
      "v_accvgpr_write_b32 a133, v41\n\t"
      "v_accvgpr_write_b32 a134, v44\n\t"
      "v_accvgpr_write_b32 a135, v45\n\t"
      "v_accvgpr_write_b32 a136, v48\n\t"
      "v_accvgpr_write_b32 a137, v49\n\t"
      "v_accvgpr_write_b32 a138, v52\n\t"
      "v_accvgpr_write_b32 a139, v53\n\t"
      "s_waitcnt vmcnt(6)\n\t"
      "s_waitcnt lgkmcnt(1)\n\t"
      "; step\n\t"
      "s_memtime s[68:69]\n\t"
      "s_memrealtime s[70:71]\n\t"
      "v_mfma_f32_4x4x4_16b_bf16 v[40:43], a[180:181], v[224:225], 0\n\t"
      "ds_read_b128 a[184:187], v14 offset:2816\n\t"
      "v_mfma_f32_4x4x4_16b_bf16 v[44:47], a[182:183], v[226:227], 0\n\t"
      "ds_read_b64_tr_b16 v[240:241], v60 offset:49152\n\t"
      "v_mfma_f32_4x4x4_16b_bf16 v[48:51], a[180:181], v[228:229], 0\n\t"
      "ds_read_b64_tr_b16 v[242:243], v60 offset:49920\n\t"
      "v_mfma_f32_4x4x4_16b_bf16 v[52:55], a[182:183], v[230:231], 0\n\t"
      "ds_read_b64_tr_b16 v[244:245], v61 offset:49152\n\t"
      "ds_read_b64_tr_b16 v[246:247], v61 offset:49920\n\t"
      "v_mfma_f32_16x16x32_bf16 v[16:19], v[208:211], a[224:227], v[16:19]\n\t"
      "v_cvt_pk_bf16_f32 v224, v40, v41\n\t"
      "v_cvt_pk_bf16_f32 v225, v42, v43\n\t"
      "v_mfma_f32_16x16x32_bf16 v[20:23], v[212:215], a[224:227], v[20:23]\n\t"
      "v_cvt_pk_bf16_f32 v226, v44, v45\n\t"
      "v_cvt_pk_bf16_f32 v227, v46, v47\n\t"
      "v_mfma_f32_16x16x32_bf16 v[24:27], v[216:219], a[224:227], v[24:27]\n\t"
      "v_cvt_pk_bf16_f32 v228, v48, v49\n\t"
      "v_cvt_pk_bf16_f32 v229, v50, v51\n\t"
      "v_mfma_f32_16x16x32_bf16 v[28:31], v[220:223], a[224:227], v[28:31]\n\t"
      "v_cvt_pk_bf16_f32 v230, v52, v53\n\t"
      "v_cvt_pk_bf16_f32 v231, v54, v55\n\t"
      "v_mfma_f32_4x4x4_16b_bf16 v[40:43], a[180:181], v[232:233], 0\n\t"
      "ds_read_b64_tr_b16 v[248:249], v60 offset:49216\n\t"
      "v_mfma_f32_4x4x4_16b_bf16 v[44:47], a[182:183], v[234:235], 0\n\t"
      "ds_read_b64_tr_b16 v[250:251], v60 offset:49984\n\t"
      "v_mfma_f32_4x4x4_16b_bf16 v[48:51], a[180:181], v[236:237], 0\n\t"
      "ds_read_b64_tr_b16 v[252:253], v61 offset:49216\n\t"
      "v_mfma_f32_4x4x4_16b_bf16 v[52:55], a[182:183], v[238:239], 0\n\t"
      "ds_read_b64_tr_b16 v[254:255], v61 offset:49984\n\t"
      "v_mfma_f32_16x16x32_bf16 v[32:35], a[132:135], a[224:227], v[32:35]\n\t"
      "v_cvt_pk_bf16_f32 v232, v40, v41\n\t"
      "v_cvt_pk_bf16_f32 v233, v42, v43\n\t"
      "v_mfma_f32_16x16x32_bf16 v[36:39], a[136:139], a[224:227], v[36:39]\n\t"
      "v_cvt_pk_bf16_f32 v234, v44, v45\n\t"
      "v_cvt_pk_bf16_f32 v235, v46, v47\n\t"
      "s_waitcnt lgkmcnt(9)\n\t"
      "v_mfma_f32_16x16x32_bf16 a[156:159], v[208:211], a[228:231], a[156:159]\n\t"
      "v_cvt_pk_bf16_f32 v236, v48, v49\n\t"
      "v_cvt_pk_bf16_f32 v237, v50, v51\n\t"
      "v_mfma_f32_16x16x32_bf16 a[160:163], v[212:215], a[228:231], a[160:163]\n\t"
      "v_cvt_pk_bf16_f32 v238, v52, v53\n\t"
      "v_cvt_pk_bf16_f32 v239, v54, v55\n\t"
      "ds_read_b128 a[232:235], v58 offset:25600\n\t"
      "v_mfma_f32_4x4x4_16b_bf16 v[40:43], a[180:181], a[140:141], 0\n\t"
      "ds_read_b64_tr_b16 a[148:149], v60 offset:49280\n\t"
      "v_mfma_f32_4x4x4_16b_bf16 v[44:47], a[182:183], a[142:143], 0\n\t"
      "ds_read_b64_tr_b16 a[150:151], v60 offset:50048\n\t"
      "v_mfma_f32_4x4x4_16b_bf16 v[48:51], a[180:181], a[144:145], 0\n\t"
      "ds_read_b64_tr_b16 a[152:153], v61 offset:49280\n\t"
      "v_mfma_f32_4x4x4_16b_bf16 v[52:55], a[182:183], a[146:147], 0\n\t"
      "ds_read_b64_tr_b16 a[154:155], v61 offset:50048\n\t"
      "v_mfma_f32_16x16x32_bf16 a[164:167], v[216:219], a[228:231], a[164:167]\n\t"
      "v_cvt_pk_bf16_f32 v40, v40, v41\n\t"
      "v_cvt_pk_bf16_f32 v41, v42, v43\n\t"
      "v_mfma_f32_16x16x32_bf16 a[168:171], v[220:223], a[228:231], a[168:171]\n\t"
      "v_cvt_pk_bf16_f32 v44, v44, v45\n\t"
      "v_cvt_pk_bf16_f32 v45, v46, v47\n\t"
      "v_mfma_f32_16x16x32_bf16 a[172:175], a[132:135], a[228:231], a[172:175]\n\t"
      "v_cvt_pk_bf16_f32 v48, v48, v49\n\t"
      "v_cvt_pk_bf16_f32 v49, v50, v51\n\t"
      "v_mfma_f32_16x16x32_bf16 a[176:179], a[136:139], a[228:231], a[176:179]\n\t"
      "v_cvt_pk_bf16_f32 v52, v52, v53\n\t"
      "v_cvt_pk_bf16_f32 v53, v54, v55\n\t"
      "v_accvgpr_write_b32 a140, v40\n\t"
      "v_accvgpr_write_b32 a141, v41\n\t"
      "v_accvgpr_write_b32 a142, v44\n\t"
      "v_accvgpr_write_b32 a143, v45\n\t"
      "v_accvgpr_write_b32 a144, v48\n\t"
      "v_accvgpr_write_b32 a145, v49\n\t"
      "v_accvgpr_write_b32 a146, v52\n\t"
      "v_accvgpr_write_b32 a147, v53\n\t"
      "s_waitcnt lgkmcnt(0)\n\t"
      "; step\n\t"
      "v_mfma_f32_4x4x4_16b_bf16 v[40:43], a[184:185], v[240:241], 0\n\t"
      "v_mfma_f32_4x4x4_16b_bf16 v[44:47], a[186:187], v[242:243], 0\n\t"
      "v_mfma_f32_4x4x4_16b_bf16 v[48:51], a[184:185], v[244:245], 0\n\t"
      "v_mfma_f32_4x4x4_16b_bf16 v[52:55], a[186:187], v[246:247], 0\n\t"
      "v_mfma_f32_16x16x32_bf16 v[16:19], v[224:227], a[232:235], v[16:19]\n\t"
      "s_nop 0\n\t"
      "v_cvt_pk_bf16_f32 v240, v40, v41\n\t"
      "v_cvt_pk_bf16_f32 v241, v42, v43\n\t"
      "v_mfma_f32_16x16x32_bf16 v[20:23], v[228:231], a[232:235], v[20:23]\n\t"
      "v_cvt_pk_bf16_f32 v242, v44, v45\n\t"
      "v_cvt_pk_bf16_f32 v243, v46, v47\n\t"
      "v_cvt_pk_bf16_f32 v244, v48, v49\n\t"
      "v_cvt_pk_bf16_f32 v245, v50, v51\n\t"
      "v_cvt_pk_bf16_f32 v246, v52, v53\n\t"
      "v_cvt_pk_bf16_f32 v247, v54, v55\n\t"
      "v_mfma_f32_4x4x4_16b_bf16 v[40:43], a[184:185], v[248:249], 0\n\t"
      "v_mfma_f32_4x4x4_16b_bf16 v[44:47], a[186:187], v[250:251], 0\n\t"
      "v_mfma_f32_4x4x4_16b_bf16 v[48:51], a[184:185], v[252:253], 0\n\t"
      "v_mfma_f32_4x4x4_16b_bf16 v[52:55], a[186:187], v[254:255], 0\n\t"
      "v_mfma_f32_16x16x32_bf16 v[24:27], v[232:235], a[232:235], v[24:27]\n\t"
      "s_nop 0\n\t"
      "v_cvt_pk_bf16_f32 v248, v40, v41\n\t"
      "v_cvt_pk_bf16_f32 v249, v42, v43\n\t"
      "v_mfma_f32_16x16x32_bf16 v[28:31], v[236:239], a[232:235], v[28:31]\n\t"
      "v_cvt_pk_bf16_f32 v250, v44, v45\n\t"
      "v_cvt_pk_bf16_f32 v251, v46, v47\n\t"
      "v_cvt_pk_bf16_f32 v252, v48, v49\n\t"
      "v_cvt_pk_bf16_f32 v253, v50, v51\n\t"
      "v_cvt_pk_bf16_f32 v254, v52, v53\n\t"
      "v_cvt_pk_bf16_f32 v255, v54, v55\n\t"
      "ds_read_b128 a[236:239], v59 offset:25600\n\t"
      "v_mfma_f32_4x4x4_16b_bf16 v[40:43], a[184:185], a[148:149], 0\n\t"
      "v_mfma_f32_4x4x4_16b_bf16 v[44:47], a[186:187], a[150:151], 0\n\t"
      "v_mfma_f32_4x4x4_16b_bf16 v[48:51], a[184:185], a[152:153], 0\n\t"
      "v_mfma_f32_4x4x4_16b_bf16 v[52:55], a[186:187], a[154:155], 0\n\t"
      "ds_read_b128 a[240:243], v58 offset:37888\n\t"
      "ds_read_b128 a[244:247], v59 offset:37888\n\t"
      "v_mfma_f32_16x16x32_bf16 v[32:35], a[140:143], a[232:235], v[32:35]\n\t"
      "v_cvt_pk_bf16_f32 v40, v40, v41\n\t"
      "v_cvt_pk_bf16_f32 v41, v42, v43\n\t"
      "v_mfma_f32_16x16x32_bf16 v[36:39], a[144:147], a[232:235], v[36:39]\n\t"
      "v_cvt_pk_bf16_f32 v44, v44, v45\n\t"
      "v_cvt_pk_bf16_f32 v45, v46, v47\n\t"
      "v_cvt_pk_bf16_f32 v48, v48, v49\n\t"
      "v_cvt_pk_bf16_f32 v49, v50, v51\n\t"
      "v_cvt_pk_bf16_f32 v52, v52, v53\n\t"
      "v_cvt_pk_bf16_f32 v53, v54, v55\n\t"
      "v_accvgpr_write_b32 a148, v40\n\t"
      "v_accvgpr_write_b32 a149, v41\n\t"
      "v_accvgpr_write_b32 a150, v44\n\t"
      "v_accvgpr_write_b32 a151, v45\n\t"
      "v_accvgpr_write_b32 a152, v48\n\t"
      "v_accvgpr_write_b32 a153, v49\n\t"
      "v_accvgpr_write_b32 a154, v52\n\t"
      "v_accvgpr_write_b32 a155, v53\n\t"
      "s_waitcnt lgkmcnt(2)\n\t"
      "v_mfma_f32_16x16x32_bf16 v[16:19], v[240:243], a[236:239], v[16:19]\n\t"
      "s_add_u32 s49, s15, 0\n\t"
      "s_mov_b32 s50, 98816\n\t"
      "s_mov_b32 s51, 61952\n\t"
      "v_mfma_f32_16x16x32_bf16 v[20:23], v[244:247], a[236:239], v[20:23]\n\t"
      "s_mov_b32 s58, s14\n\t"
      "s_add_u32 s56, s14, 2359296\n\t"
      "v_mfma_f32_16x16x32_bf16 v[24:27], v[248:251], a[236:239], v[24:27]\n\t"
      "s_movk_i32 s39, 96\n\t"
      "s_movk_i32 s29, 71\n\t"
      "s_mov_b32 s32, 1947275679\n\t"
      "v_mfma_f32_16x16x32_bf16 v[28:31], v[252:255], a[236:239], v[28:31]\n\t"
      "s_mov_b32 s33, 143165559\n\t"
      "s_mov_b32 s34, 1950421356\n\t"
      "s_mov_b32 s35, 8947847\n\t"
      "v_mfma_f32_16x16x32_bf16 v[32:35], a[148:151], a[236:239], v[32:35]\n\t"
      "v_mov_b32 v52, 163328\n\t"
      "v_mfma_f32_16x16x32_bf16 v[36:39], a[152:155], a[236:239], v[36:39]\n\t"
      "s_cmp_lg_u32 s17, 0\n\t"
      "s_cselect_b32 s39, 0, s39\n\t"
      "s_cbranch_scc1 .Lno4_%=\n\t"
      "v_accvgpr_read_b32 v41, a248\n\t"
      "v_mov_b32 v51, 0x4040\n\t"
      "s_movk_i32 s52, 2000\n\t"
      "s_mov_b32 s91, 0\n\t"
      "s_waitcnt vmcnt(6) \n\t"
      "v_accvgpr_read_b32 v40, a252\n\t"
      ".Lcheck_%=:\n\t"
      "v_cmp_ne_u32 s[54:55], v40, v63\n\t"
      "s_cmp_lg_u32 s16, 0\n\t"
      "v_add_u32_dpp v42, v40, v40 quad_perm:[2,3,0,1] row_mask:0xf bank_mask:0xf\n\t"
      "s_cselect_b32 s30, -1, s54\n\t"
      "s_cmp_lg_u32 s30, -1\n\t"
      "s_cbranch_scc1 .Lwait_%=\n\t"
      ".Lwaited_%=:\n\t"
      "v_add_u32_dpp v42, v42, v42 quad_perm:[1,0,3,2] row_mask:0xf bank_mask:0xf\n\t"
      "s_bfe_u32 s30, s19, 0x30008\n\t"
      "s_bfe_u32 s31, s19, 0x10003\n\t"
      "s_lshl1_add_u32 s30, s30, s31\n\t"
      "s_lshl_b32 s30, s30, 1\n\t"
      "v_mov_b32 v43, v42\n\t"
      "v_subrev_u32_dpp v43, v42, v51 row_half_mirror row_mask:0x3 bank_mask:0xf\n\t"
      "v_lshl_add_u32 v42, v42, 1, v43\n\t"
      "v_readlane_b32 s31, v40, s30\n\t"
      "v_add_u32 v42, 0x1f1f, v42\n\t"
      "v_and_b32 v43, 128, v42\n\t"
      "v_and_b32 v44, 32768, v42\n\t"
      "v_cmp_ne_u32 s[88:89], 0, v43\n\t"
      "v_cmp_ne_u32 s[36:37], 0, v44\n\t"
      "s_bitcmp1_b32 s19, 2\n\t"
      "s_cselect_b32 s30, 0x3ffff, 0\n\t"
      "s_bitcmp1_b32 s31, 16\n\t"
      "s_cselect_b32 s31, 262140, 0\n\t"
      "s_xor_b32 s30, s30, s31\n\t"
      "s_xor_b32 s72, s36, s88\n\t"
      "s_and_b32 s72, s72, 0x11111111\n\t"
      "s_bcnt1_i32_b32 s72, s72\n\t"
      "s_bfe_u32 s31, %[blk], 0x20003\n\t"
      "s_lshr_b32 s31, 1118481, s31\n\t"
      "s_and_b32 s31, s31, 262140\n\t"
      "s_cmp_ge_u32 s72, 7\n\t"
      "s_cselect_b32 s31, s31, 0\n\t"
      "s_xor_b32 s30, s30, s31\n\t"
      "v_lshrrev_b32 v41, v41, s88\n\t"
      "v_and_b32 v41, 1, v41\n\t"
      "v_cmp_ne_u32 vcc, 0, v41\n\t"
      "s_nop 0\n\t"
      "s_and_b32 s31, vcc_lo, 262140\n\t"
      "s_xor_b32 s31, s31, s30\n\t"
      "s_or_b32 s31, s31, s91\n\t"
      "s_and_b32 s30, s16, 0x3ffff\n\t"
      "s_cmp_lg_u32 s16, 0\n\t"
      "s_cselect_b32 s31, s30, s31\n\t"
      "v_mov_b32 v53, s31\n\t"
      "ds_write_b32 v52, v53\n\t"
      "s_xor_b32 s36, s37, s89\n\t"
      "s_lshr_b32 s30, s55, 1\n\t"
      "s_and_b32 s55, s55, s30\n\t"
      "s_lshr_b32 s30, s55, 2\n\t"
      "s_and_b32 s55, s55, s30\n\t"
      "s_and_b32 s36, s36, s55\n\t"
      "s_and_b32 s36, s36, 0x11111111\n\t"
      "s_bcnt1_i32_b32 s36, s36\n\t"
      "s_lshl_b32 s30, s36, 2\n\t"
      "s_mov_b32 s54, 1394741248\n\t"
      "s_mov_b32 s55, 6\n\t"
      "s_lshr_b64 s[54:55], s[54:55], s30\n\t"
      "s_and_b32 s30, s54, 15\n\t"
      "s_add_u32 s29, s29, s30\n\t"
      ".Lno4_%=:\n\t"
      "v_accvgpr_read_b32 v9, a213\n\t"
      "v_accvgpr_read_b32 v10, a214\n\t"
      "v_accvgpr_read_b32 v11, a215\n\t"
      "v_accvgpr_read_b32 v12, a216\n\t"
      "v_accvgpr_read_b32 v13, a217\n\t"
      "v_accvgpr_read_b32 v14, a218\n\t"
      "v_accvgpr_read_b32 v8, a219\n\t"
      "v_accvgpr_read_b32 v60, a220\n\t"
      "v_accvgpr_read_b32 v61, a221\n\t"
      "v_add_u32 v56, 49664, v56\n\t"
      "v_add_u32 v57, 49664, v57\n\t"
      "v_add_u32 v58, 49664, v58\n\t"
      "v_add_u32 v59, 49664, v59\n\t"
      "v_cvt_pk_bf16_f32 v40, v16, v17\n\t"
      "v_cvt_pk_bf16_f32 v41, v18, v19\n\t"
      "v_cvt_pk_bf16_f32 v42, v20, v21\n\t"
      "v_cvt_pk_bf16_f32 v43, v22, v23\n\t"
      "v_cvt_pk_bf16_f32 v44, v24, v25\n\t"
      "v_cvt_pk_bf16_f32 v45, v26, v27\n\t"
      "v_cvt_pk_bf16_f32 v46, v28, v29\n\t"
      "v_cvt_pk_bf16_f32 v47, v30, v31\n\t"
      "v_cvt_pk_bf16_f32 v48, v32, v33\n\t"
      "v_cvt_pk_bf16_f32 v49, v34, v35\n\t"
      "v_cvt_pk_bf16_f32 v50, v36, v37\n\t"
      "v_cvt_pk_bf16_f32 v51, v38, v39\n\t"
      "s_mov_b32 s46, 113246208\n\t"
      "s_mov_b32 s52, 15\n\t"
      "s_add_u32 s90, s90, 0x2000000\n\t"
      "s_waitcnt vmcnt(3)\n\t"
      "s_waitcnt lgkmcnt(0)\n\t"
      "s_barrier\n\t"
      "v_mfma_f32_16x16x32_bf16 a[156:159], v[224:227], a[240:243], a[156:159]\n\t"
      "ds_read_b32 v63, v52\n\t"
      "ds_read_b128 a[0:3], v56 offset:0\n\t"
      "s_memrealtime s[36:37]\n\t"
      "v_mfma_f32_16x16x32_bf16 a[160:163], v[228:231], a[240:243], a[160:163]\n\t"
      "ds_read_b128 a[4:7], v57 offset:0\n\t"
      "ds_read_b128 a[8:11], v58 offset:0\n\t"
      "v_mfma_f32_16x16x32_bf16 a[164:167], v[232:235], a[240:243], a[164:167]\n\t"
      "ds_read_b128 a[12:15], v59 offset:0\n\t"
      "ds_read_b128 a[16:19], v56 offset:256\n\t"
      "v_mfma_f32_16x16x32_bf16 a[168:171], v[236:239], a[240:243], a[168:171]\n\t"
      "ds_read_b128 a[20:23], v57 offset:256\n\t"
      "ds_write_b64 v0, v[40:41] offset:0\n\t"
      "v_mfma_f32_16x16x32_bf16 a[172:175], a[140:143], a[240:243], a[172:175]\n\t"
      "ds_write_b64 v0, v[42:43] offset:32\n\t"
      "v_mfma_f32_16x16x32_bf16 a[176:179], a[144:147], a[240:243], a[176:179]\n\t"
      "s_waitcnt lgkmcnt(8)\n\t"
      "v_readfirstlane_b32 s91, v63\n\t"
      "v_mfma_f32_16x16x32_bf16 a[156:159], v[240:243], a[244:247], a[156:159]\n\t"
      "ds_write_b64 v0, v[44:45] offset:64\n\t"
      "s_add_u32 m0, s18, 74240\n\t"
      "v_mfma_f32_16x16x32_bf16 a[160:163], v[244:247], a[244:247], a[160:163]\n\t"
      "s_bitcmp1_b32 s91, 2\n\t"
      "s_cselect_b32 s48, 24576, 0\n\t"
      "s_add_u32 s48, s48, s14\n\t"
      "v_mfma_f32_16x16x32_bf16 a[164:167], v[248:251], a[244:247], a[164:167]\n\t"
      "s_add_u32 s48, s48, 1572864\n\t"
      "buffer_load_dwordx4 v2, s[40:43], s48 offen lds\n\t"
      "s_mov_b32 s59, s91\n\t"
      "v_mfma_f32_16x16x32_bf16 a[168:171], v[252:255], a[244:247], a[168:171]\n\t"
      "ds_write_b64 v0, v[46:47] offset:96\n\t"
      "buffer_load_dwordx4 v3, s[40:43], s48 offen offset:1024 lds\n\t"
      "v_mfma_f32_16x16x32_bf16 a[172:175], a[148:151], a[244:247], a[172:175]\n\t"
      "ds_write_b64 v0, v[48:49] offset:128\n\t"
      "buffer_load_dwordx4 v4, s[40:43], s48 offen offset:2048 lds\n\t"
      "v_mfma_f32_16x16x32_bf16 a[176:179], a[152:155], a[244:247], a[176:179]\n\t"
      "ds_write_b64 v0, v[50:51] offset:160\n\t"
      "s_lshr_b32 s57, s91, 3\n\t"
      "s_bitcmp1_b32 s91, 3\n\t"
      "s_cselect_b32 s48, 24576, 0\n\t"
      "s_add_u32 s48, s48, s14\n\t"
      "s_add_u32 s48, s48, 2359296\n\t"
      "s_or_b32 s30, s17, s16\n\t"
      "s_cbranch_scc1 .Lno5_%=\n\t"
      "s_sub_u32 s68, s68, s64\n\t"
      "s_sub_u32 s70, s70, s66\n\t"
      "s_mul_i32 s64, s68, 100\n\t"
      "s_mul_i32 s66, s70, 1750\n\t"
      "s_sub_i32 s66, s64, s66\n\t"
      "s_ashr_i32 s66, s66, 9\n\t"
      "s_add_i32 s66, s66, 128\n\t"
      "s_max_i32 s66, s66, 1\n\t"
      "s_min_i32 s66, s66, 255\n\t"
      "s_lshl_b32 s66, s66, 17\n\t"
      "s_or_b32 s90, s90, s66\n\t"
      "v_mov_b32 v54, s90\n\t"
      "v_mov_b32 v55, s19\n\t"
      "global_store_dword v55, v54, s[12:13] sc1\n\t"
      ".Lno5_%=:\n\t"
      "s_waitcnt lgkmcnt(11)\n\t"
      "v_mfma_f32_16x16x32_bf16 v[16:19], v[64:67], a[0:3], 0\n\t"
      "ds_read_b128 a[24:27], v58 offset:256\n\t"
      "ds_read_b128 a[28:31], v59 offset:256\n\t"
      "s_waitcnt lgkmcnt(12)\n\t"
      "v_mfma_f32_16x16x32_bf16 v[16:19], v[80:83], a[4:7], v[16:19]\n\t"
      "ds_read_b128 a[32:35], v56 offset:512\n\t"
      "ds_read_b128 a[36:39], v57 offset:512\n\t"
      "s_waitcnt lgkmcnt(13)\n\t"
      "v_mfma_f32_16x16x32_bf16 v[16:19], v[96:99], a[8:11], v[16:19]\n\t"
      "ds_read_b128 a[40:43], v58 offset:512\n\t"
      "ds_read_b128 a[44:47], v59 offset:512\n\t"
      "s_waitcnt lgkmcnt(14)\n\t"
      "v_mfma_f32_16x16x32_bf16 v[16:19], v[112:115], a[12:15], v[16:19]\n\t"
      "v_accvgpr_read_b32 v52, a156\n\t"
      "v_accvgpr_read_b32 v53, a157\n\t"
      "s_waitcnt lgkmcnt(13)\n\t"
      "v_mfma_f32_16x16x32_bf16 v[16:19], v[128:131], a[16:19], v[16:19]\n\t"
      "v_accvgpr_read_b32 v54, a158\n\t"
      "v_accvgpr_read_b32 v55, a159\n\t"
      "s_waitcnt lgkmcnt(12)\n\t"
      "v_mfma_f32_16x16x32_bf16 v[16:19], v[144:147], a[20:23], v[16:19]\n\t"
      "v_cvt_pk_bf16_f32 v40, v52, v53\n\t"
      "v_cvt_pk_bf16_f32 v41, v54, v55\n\t"
      "v_mfma_f32_16x16x32_bf16 v[20:23], v[68:71], a[0:3], 0\n\t"
      "ds_write_b64 v0, v[40:41] offset:12544\n\t"
      "v_mfma_f32_16x16x32_bf16 v[20:23], v[84:87], a[4:7], v[20:23]\n\t"
      "s_waitcnt lgkmcnt(7)\n\t"
      "s_barrier\n\t"
      "v_mfma_f32_16x16x32_bf16 v[20:23], v[100:103], a[8:11], v[20:23]\n\t"
      "ds_read_b128 a[48:51], v8 offset:0\n\t"
      "ds_read_b128 a[52:55], v60 offset:1024\n\t"
      "ds_read_b128 a[56:59], v61 offset:2048\n\t"
      "v_mfma_f32_16x16x32_bf16 v[20:23], v[116:119], a[12:15], v[20:23]\n\t"
      "v_accvgpr_read_b32 v52, a160\n\t"
      "v_accvgpr_read_b32 v53, a161\n\t"
      "v_mfma_f32_16x16x32_bf16 v[20:23], v[132:135], a[16:19], v[20:23]\n\t"
      "v_accvgpr_read_b32 v54, a162\n\t"
      "v_accvgpr_read_b32 v55, a163\n\t"
      "v_mfma_f32_16x16x32_bf16 v[20:23], v[148:151], a[20:23], v[20:23]\n\t"
      "v_cvt_pk_bf16_f32 v42, v52, v53\n\t"
      "v_cvt_pk_bf16_f32 v43, v54, v55\n\t"
      "v_mfma_f32_16x16x32_bf16 v[24:27], v[72:75], a[0:3], 0\n\t"
      "ds_write_b64 v0, v[42:43] offset:12576\n\t"
      "s_waitcnt lgkmcnt(3)\n\t"
      "v_mfma_f32_16x16x32_bf16 v[24:27], v[88:91], a[4:7], v[24:27]\n\t"
      "buffer_store_dwordx4 a[48:51], v9, s[44:47], s49 offen sc1\n\t"
      "v_mfma_f32_16x16x32_bf16 v[24:27], v[104:107], a[8:11], v[24:27]\n\t"
      "v_accvgpr_read_b32 v52, a164\n\t"
      "v_accvgpr_read_b32 v53, a165\n\t"
      "v_mfma_f32_16x16x32_bf16 v[24:27], v[120:123], a[12:15], v[24:27]\n\t"
      "v_accvgpr_read_b32 v54, a166\n\t"
      "v_accvgpr_read_b32 v55, a167\n\t"
      "v_mfma_f32_16x16x32_bf16 v[24:27], v[136:139], a[16:19], v[24:27]\n\t"
      "v_cvt_pk_bf16_f32 v44, v52, v53\n\t"
      "v_cvt_pk_bf16_f32 v45, v54, v55\n\t"
      "v_mfma_f32_16x16x32_bf16 v[24:27], v[152:155], a[20:23], v[24:27]\n\t"
      "ds_write_b64 v0, v[44:45] offset:12608\n\t"
      "s_waitcnt lgkmcnt(3)\n\t"
      "v_mfma_f32_16x16x32_bf16 v[28:31], v[76:79], a[0:3], 0\n\t"
      "buffer_store_dwordx4 a[52:55], v10, s[44:47], s49 offen offset:1024 sc1\n\t"
      "v_mfma_f32_16x16x32_bf16 v[28:31], v[92:95], a[4:7], v[28:31]\n\t"
      "v_accvgpr_read_b32 v52, a168\n\t"
      "v_accvgpr_read_b32 v53, a169\n\t"
      "v_mfma_f32_16x16x32_bf16 v[28:31], v[108:111], a[8:11], v[28:31]\n\t"
      "v_accvgpr_read_b32 v54, a170\n\t"
      "v_accvgpr_read_b32 v55, a171\n\t"
      "v_mfma_f32_16x16x32_bf16 v[28:31], v[124:127], a[12:15], v[28:31]\n\t"
      "v_cvt_pk_bf16_f32 v46, v52, v53\n\t"
      "v_cvt_pk_bf16_f32 v47, v54, v55\n\t"
      "v_mfma_f32_16x16x32_bf16 v[28:31], v[140:143], a[16:19], v[28:31]\n\t"
      "ds_write_b64 v0, v[46:47] offset:12640\n\t"
      "s_waitcnt lgkmcnt(3)\n\t"
      "v_mfma_f32_16x16x32_bf16 v[28:31], v[156:159], a[20:23], v[28:31]\n\t"
      "buffer_store_dwordx4 a[56:59], v11, s[44:47], s49 offen offset:2048 sc1\n\t"
      "v_mfma_f32_16x16x32_bf16 v[32:35], a[60:63], a[0:3], 0\n\t"
      "v_accvgpr_read_b32 v52, a172\n\t"
      "v_accvgpr_read_b32 v53, a173\n\t"
      "v_mfma_f32_16x16x32_bf16 v[32:35], a[68:71], a[4:7], v[32:35]\n\t"
      "v_accvgpr_read_b32 v54, a174\n\t"
      "v_accvgpr_read_b32 v55, a175\n\t"
      "v_mfma_f32_16x16x32_bf16 v[32:35], a[76:79], a[8:11], v[32:35]\n\t"
      "v_cvt_pk_bf16_f32 v48, v52, v53\n\t"
      "v_cvt_pk_bf16_f32 v49, v54, v55\n\t"
      "v_mfma_f32_16x16x32_bf16 v[32:35], a[84:87], a[12:15], v[32:35]\n\t"
      "ds_write_b64 v0, v[48:49] offset:12672\n\t"
      "v_mfma_f32_16x16x32_bf16 v[32:35], a[92:95], a[16:19], v[32:35]\n\t"
      "v_accvgpr_read_b32 v52, a176\n\t"
      "v_accvgpr_read_b32 v53, a177\n\t"
      "v_mfma_f32_16x16x32_bf16 v[32:35], a[100:103], a[20:23], v[32:35]\n\t"
      "v_accvgpr_read_b32 v54, a178\n\t"
      "v_accvgpr_read_b32 v55, a179\n\t"
      "v_mfma_f32_16x16x32_bf16 v[36:39], a[64:67], a[0:3], 0\n\t"
      "v_cvt_pk_bf16_f32 v50, v52, v53\n\t"
      "v_cvt_pk_bf16_f32 v51, v54, v55\n\t"
      "v_mfma_f32_16x16x32_bf16 v[36:39], a[72:75], a[4:7], v[36:39]\n\t"
      "ds_write_b64 v0, v[50:51] offset:12704\n\t"
      "v_mfma_f32_16x16x32_bf16 v[36:39], a[80:83], a[8:11], v[36:39]\n\t"
      "v_add_u32 v52, s51, v15\n\t"
      "v_xor_b32 v53, 64, v52\n\t"
      "v_mfma_f32_16x16x32_bf16 v[36:39], a[88:91], a[12:15], v[36:39]\n\t"
      "v_xor_b32 v54, 128, v52\n\t"
      "v_xor_b32 v55, 192, v52\n\t"
      "v_mfma_f32_16x16x32_bf16 v[36:39], a[96:99], a[16:19], v[36:39]\n\t"
      "s_waitcnt lgkmcnt(0)\n\t"
      "s_add_u32 s38, s36, s39\n\t"
      "s_sub_u32 s28, s36, s28\n\t"
      "s_sub_i32 s28, s28, 515\n\t"
      "s_max_i32 s28, s28, 0\n\t"
      "s_min_i32 s28, s28, 60\n\t"
      "s_and_b32 s39, s32, 15\n\t"
      "s_cselect_b32 s53, s29, 0\n\t"
      "s_memrealtime s[36:37]\n\t"
      "v_mfma_f32_16x16x32_bf16 v[36:39], a[104:107], a[20:23], v[36:39]\n\t"
      "s_add_u32 s39, s39, s53\n\t"
      "s_lshr_b64 s[32:33], s[32:33], 4\n\t"
      "s_add_u32 m0, s18, 86528\n\t"
      "v_mfma_f32_16x16x32_bf16 v[16:19], v[160:163], a[24:27], v[16:19]\n\t"
      "s_bitcmp1_b32 s91, 2\n\t"
      "s_cselect_b32 s54, 24576, 0\n\t"
      "s_add_u32 s54, s54, s14\n\t"
      "s_add_u32 s54, s54, 1572864\n\t"
      "v_mfma_f32_16x16x32_bf16 v[16:19], v[176:179], a[28:31], v[16:19]\n\t"
      "s_waitcnt vmcnt(6)\n\t"
      "s_barrier\n\t"
      "v_mfma_f32_16x16x32_bf16 v[16:19], v[192:195], a[32:35], v[16:19]\n\t"
      "ds_read_b128 a[0:3], v52 offset:0\n\t"
      "ds_read_b128 a[188:191], v8 offset:12544\n\t"
      "buffer_load_dwordx4 v5, s[40:43], s54 offen lds\n\t"
      "v_mfma_f32_16x16x32_bf16 v[16:19], v[208:211], a[36:39], v[16:19]\n\t"
      "ds_read_b128 a[4:7], v53 offset:0\n\t"
      "ds_read_b128 a[192:195], v60 offset:13568\n\t"
      "v_mfma_f32_16x16x32_bf16 v[16:19], v[224:227], a[40:43], v[16:19]\n\t"
      "ds_read_b128 a[8:11], v54 offset:0\n\t"
      "ds_read_b128 a[196:199], v61 offset:14592\n\t"
      "buffer_load_dwordx4 v6, s[40:43], s54 offen offset:1024 lds\n\t"
      "v_mfma_f32_16x16x32_bf16 v[16:19], v[240:243], a[44:47], v[16:19]\n\t"
      "ds_read_b128 a[12:15], v55 offset:0\n\t"
      "v_mfma_f32_16x16x32_bf16 v[20:23], v[164:167], a[24:27], v[20:23]\n\t"
      "ds_read_b128 a[16:19], v52 offset:256\n\t"
      "buffer_load_dwordx4 v7, s[40:43], s54 offen offset:2048 lds\n\t"
      "v_mfma_f32_16x16x32_bf16 v[20:23], v[180:183], a[28:31], v[20:23]\n\t"
      "ds_read_b128 a[20:23], v53 offset:256\n\t"
      "s_add_u32 m0, s18, s50\n\t"
      "v_mfma_f32_16x16x32_bf16 v[20:23], v[196:199], a[32:35], v[20:23]\n\t"
      "s_add_u32 s51, s51, 12288\n\t"
      "s_cmp_eq_u32 s51, 160256\n\t"
      "s_cselect_b32 s51, 25088, s51\n\t"
      "s_waitcnt lgkmcnt(7)\n\t"
      "v_mfma_f32_16x16x32_bf16 v[20:23], v[212:215], a[36:39], v[20:23]\n\t"
      "buffer_store_dwordx4 a[188:191], v12, s[44:47], s49 offen sc1\n\t"
      "v_mfma_f32_16x16x32_bf16 v[20:23], v[228:231], a[40:43], v[20:23]\n\t"
      "v_mfma_f32_16x16x32_bf16 v[20:23], v[244:247], a[44:47], v[20:23]\n\t"
      "s_waitcnt lgkmcnt(0)\n\t"
      "s_sub_u32 s53, s36, s38\n\t"
      "s_cmp_lt_i32 s53, 0\n\t"
      "v_mfma_f32_16x16x32_bf16 v[24:27], v[168:171], a[24:27], v[24:27]\n\t"
      "s_cbranch_scc1 .Lspinf_%=\n\t"
      ".Lspunf_%=:\n\t"
      "s_sub_i32 s53, s53, 30\n\t"
      "s_max_i32 s53, s53, 0\n\t"
      "v_mfma_f32_16x16x32_bf16 v[24:27], v[184:187], a[28:31], v[24:27]\n\t"
      "s_add_u32 s38, s38, s53\n\t"
      "s_add_u32 s38, s38, s39\n\t"
      "s_add_u32 s28, s28, s53\n\t"
      "v_mfma_f32_16x16x32_bf16 v[24:27], v[200:203], a[32:35], v[24:27]\n\t"
      "v_cvt_pk_bf16_f32 v40, v16, v17\n\t"
      "v_cvt_pk_bf16_f32 v41, v18, v19\n\t"
      "v_mfma_f32_16x16x32_bf16 v[24:27], v[216:219], a[36:39], v[24:27]\n\t"
      "ds_write_b64 v0, v[40:41] offset:0\n\t"
      "v_mfma_f32_16x16x32_bf16 v[24:27], v[232:235], a[40:43], v[24:27]\n\t"
      "buffer_load_dwordx4 v2, s[40:43], s48 offen lds\n\t"
      "v_mfma_f32_16x16x32_bf16 v[24:27], v[248:251], a[44:47], v[24:27]\n\t"
      "buffer_store_dwordx4 a[192:195], v13, s[44:47], s49 offen offset:1024 sc1\n\t"
      "v_mfma_f32_16x16x32_bf16 v[28:31], v[172:175], a[24:27], v[28:31]\n\t"
      "s_mov_b32 s87, 0\n\t"
      "v_mfma_f32_16x16x32_bf16 v[28:31], v[188:191], a[28:31], v[28:31]\n\t"
      "v_cvt_pk_bf16_f32 v42, v20, v21\n\t"
      "v_cvt_pk_bf16_f32 v43, v22, v23\n\t"
      "v_mfma_f32_16x16x32_bf16 v[28:31], v[204:207], a[32:35], v[28:31]\n\t"
      "ds_write_b64 v0, v[42:43] offset:32\n\t"
      "v_mfma_f32_16x16x32_bf16 v[28:31], v[220:223], a[36:39], v[28:31]\n\t"
      "buffer_load_dwordx4 v3, s[40:43], s48 offen offset:1024 lds\n\t"
      "v_mfma_f32_16x16x32_bf16 v[28:31], v[236:239], a[40:43], v[28:31]\n\t"
      "buffer_store_dwordx4 a[196:199], v14, s[44:47], s49 offen offset:2048 sc1\n\t"
      "v_mfma_f32_16x16x32_bf16 v[28:31], v[252:255], a[44:47], v[28:31]\n\t"
      "s_add_u32 s58, s58, 786432\n\t"
      "s_lshr_b32 s59, s59, 1\n\t"
      "v_mfma_f32_16x16x32_bf16 v[32:35], a[108:111], a[24:27], v[32:35]\n\t"
      "s_bitcmp1_b32 s59, 0\n\t"
      "s_cselect_b32 s49, 24576, 0\n\t"
      "s_add_u32 s49, s49, s58\n\t"
      "v_mfma_f32_16x16x32_bf16 v[32:35], a[116:119], a[28:31], v[32:35]\n\t"
      "v_cvt_pk_bf16_f32 v44, v24, v25\n\t"
      "v_cvt_pk_bf16_f32 v45, v26, v27\n\t"
      "v_mfma_f32_16x16x32_bf16 v[32:35], a[124:127], a[32:35], v[32:35]\n\t"
      "ds_write_b64 v0, v[44:45] offset:64\n\t"
      "v_mfma_f32_16x16x32_bf16 v[32:35], a[132:135], a[36:39], v[32:35]\n\t"
      "buffer_load_dwordx4 v4, s[40:43], s48 offen offset:2048 lds\n\t"
      "v_mfma_f32_16x16x32_bf16 v[32:35], a[140:143], a[40:43], v[32:35]\n\t"
      "s_add_u32 s50, s50, 12288\n\t"
      "v_mfma_f32_16x16x32_bf16 v[32:35], a[148:151], a[44:47], v[32:35]\n\t"
      "v_cvt_pk_bf16_f32 v46, v28, v29\n\t"
      "v_cvt_pk_bf16_f32 v47, v30, v31\n\t"
      "v_mfma_f32_16x16x32_bf16 v[36:39], a[112:115], a[24:27], v[36:39]\n\t"
      "ds_write_b64 v0, v[46:47] offset:96\n\t"
      "v_mfma_f32_16x16x32_bf16 v[36:39], a[120:123], a[28:31], v[36:39]\n\t"
      "s_cmp_eq_u32 s50, 160256\n\t"
      "s_cselect_b32 s50, 25088, s50\n\t"
      "v_mfma_f32_16x16x32_bf16 v[36:39], a[128:131], a[32:35], v[36:39]\n\t"
      "v_mfma_f32_16x16x32_bf16 v[36:39], a[136:139], a[36:39], v[36:39]\n\t"
      "v_mfma_f32_16x16x32_bf16 v[36:39], a[144:147], a[40:43], v[36:39]\n\t"
      "v_cvt_pk_bf16_f32 v48, v32, v33\n\t"
      "v_cvt_pk_bf16_f32 v49, v34, v35\n\t"
      "v_mfma_f32_16x16x32_bf16 v[36:39], a[152:155], a[44:47], v[36:39]\n\t"
      "ds_write_b64 v0, v[48:49] offset:128\n\t"
      "s_cmp_eq_u32 s17, 6144\n\t"
      "s_cbranch_scc1 .Lmid1_%=\n\t"
      "s_cmp_eq_u32 s17, 12288\n\t"
      "s_cbranch_scc1 .Lmid2_%=\n\t"
      "s_cmp_eq_u32 s17, 18432\n\t"
      "s_cbranch_scc1 .Lmid3_%=\n\t"
      "global_load_dword v63, v62, s[12:13] sc1\n\t"
      "s_branch .Lmid0_%=\n\t"
      ".Lloop0_%=:\n\t"
      "v_mfma_f32_16x16x32_bf16 v[16:19], v[64:67], a[0:3], 0\n\t"
      "ds_read_b128 a[24:27], v58 offset:256\n\t"
      "v_mfma_f32_16x16x32_bf16 v[16:19], v[80:83], a[4:7], v[16:19]\n\t"
      "ds_read_b128 a[28:31], v59 offset:256\n\t"
      "v_mfma_f32_16x16x32_bf16 v[16:19], v[96:99], a[8:11], v[16:19]\n\t"
      "ds_read_b128 a[32:35], v56 offset:512\n\t"
      "v_mfma_f32_16x16x32_bf16 v[16:19], v[112:115], a[12:15], v[16:19]\n\t"
      "ds_read_b128 a[36:39], v57 offset:512\n\t"
      "v_mfma_f32_16x16x32_bf16 v[16:19], v[128:131], a[16:19], v[16:19]\n\t"
      "ds_read_b128 a[40:43], v58 offset:512\n\t"
      "v_mfma_f32_16x16x32_bf16 v[16:19], v[144:147], a[20:23], v[16:19]\n\t"
      "ds_read_b128 a[44:47], v59 offset:512\n\t"
      "v_mfma_f32_16x16x32_bf16 v[20:23], v[68:71], a[0:3], 0\n\t"
      "v_cvt_pk_bf16_f32 v50, v36, v37\n\t"
      "v_cvt_pk_bf16_f32 v51, v38, v39\n\t"
      "v_mfma_f32_16x16x32_bf16 v[20:23], v[84:87], a[4:7], v[20:23]\n\t"
      "ds_write_b64 v0, v[50:51] offset:12704\n\t"
      "v_mfma_f32_16x16x32_bf16 v[20:23], v[100:103], a[8:11], v[20:23]\n\t"
      "v_add_u32 v52, s51, v15\n\t"
      "v_xor_b32 v53, 64, v52\n\t"
      "v_mfma_f32_16x16x32_bf16 v[20:23], v[116:119], a[12:15], v[20:23]\n\t"
      "v_xor_b32 v54, 128, v52\n\t"
      "v_xor_b32 v55, 192, v52\n\t"
      "v_mfma_f32_16x16x32_bf16 v[20:23], v[132:135], a[16:19], v[20:23]\n\t"
      "s_add_u32 s56, s56, 786432\n\t"
      "s_lshr_b32 s57, s57, 1\n\t"
      "v_mfma_f32_16x16x32_bf16 v[20:23], v[148:151], a[20:23], v[20:23]\n\t"
      "s_bitcmp1_b32 s57, 0\n\t"
      "s_cselect_b32 s48, 24576, 0\n\t"
      "s_add_u32 s48, s48, s56\n\t"
      "v_mfma_f32_16x16x32_bf16 v[24:27], v[72:75], a[0:3], 0\n\t"
      "v_mfma_f32_16x16x32_bf16 v[24:27], v[88:91], a[4:7], v[24:27]\n\t"
      "v_mfma_f32_16x16x32_bf16 v[24:27], v[104:107], a[8:11], v[24:27]\n\t"
      "s_waitcnt vmcnt(12)\n\t"
      "s_waitcnt lgkmcnt(0)\n\t"
      "s_barrier\n\t"
      "v_mfma_f32_16x16x32_bf16 v[24:27], v[120:123], a[12:15], v[24:27]\n\t"
      "v_mfma_f32_16x16x32_bf16 v[24:27], v[136:139], a[16:19], v[24:27]\n\t"
      "v_mfma_f32_16x16x32_bf16 v[24:27], v[152:155], a[20:23], v[24:27]\n\t"
      "v_mfma_f32_16x16x32_bf16 v[28:31], v[76:79], a[0:3], 0\n\t"
      "ds_read_b128 a[48:51], v8 offset:12544\n\t"
      "v_mfma_f32_16x16x32_bf16 v[28:31], v[92:95], a[4:7], v[28:31]\n\t"
      "ds_read_b128 a[52:55], v60 offset:13568\n\t"
      "v_mfma_f32_16x16x32_bf16 v[28:31], v[108:111], a[8:11], v[28:31]\n\t"
      "ds_read_b128 a[56:59], v61 offset:14592\n\t"
      "v_mfma_f32_16x16x32_bf16 v[28:31], v[124:127], a[12:15], v[28:31]\n\t"
      "s_and_b32 s39, s32, 15\n\t"
      "s_cselect_b32 s53, s29, 0\n\t"
      "v_mfma_f32_16x16x32_bf16 v[28:31], v[140:143], a[16:19], v[28:31]\n\t"
      "s_add_u32 s39, s39, s53\n\t"
      "s_lshr_b64 s[32:33], s[32:33], 4\n\t"
      "v_mfma_f32_16x16x32_bf16 v[28:31], v[156:159], a[20:23], v[28:31]\n\t"
      "s_cmp_le_u32 s52, 12\n\t"
      "s_cselect_b32 s53, 3, 0\n\t"
      "s_min_u32 s53, s28, s53\n\t"
      "v_mfma_f32_16x16x32_bf16 v[32:35], a[60:63], a[0:3], 0\n\t"
      "s_sub_u32 s28, s28, s53\n\t"
      "s_sub_u32 s39, s39, s53\n\t"
      "v_mfma_f32_16x16x32_bf16 v[32:35], a[68:71], a[4:7], v[32:35]\n\t"
      "s_waitcnt lgkmcnt(2)\n\t"
      "v_mfma_f32_16x16x32_bf16 v[32:35], a[76:79], a[8:11], v[32:35]\n\t"
      "buffer_store_dwordx4 a[48:51], v12, s[44:47], s49 offen sc1\n\t"
      "v_mfma_f32_16x16x32_bf16 v[32:35], a[84:87], a[12:15], v[32:35]\n\t"
      "v_mfma_f32_16x16x32_bf16 v[32:35], a[92:95], a[16:19], v[32:35]\n\t"
      "v_mfma_f32_16x16x32_bf16 v[32:35], a[100:103], a[20:23], v[32:35]\n\t"
      "s_add_u32 m0, s18, s50\n\t"
      "v_mfma_f32_16x16x32_bf16 v[36:39], a[64:67], a[0:3], 0\n\t"
      "buffer_load_dwordx4 v2, s[40:43], s48 offen nt lds\n\t"
      "v_mfma_f32_16x16x32_bf16 v[36:39], a[72:75], a[4:7], v[36:39]\n\t"
      "s_memrealtime s[36:37]\n\t"
      "v_mfma_f32_16x16x32_bf16 v[36:39], a[80:83], a[8:11], v[36:39]\n\t"
      "v_mfma_f32_16x16x32_bf16 v[36:39], a[88:91], a[12:15], v[36:39]\n\t"
      "s_waitcnt lgkmcnt(1)\n\t"
      "v_mfma_f32_16x16x32_bf16 v[36:39], a[96:99], a[16:19], v[36:39]\n\t"
      "buffer_store_dwordx4 a[52:55], v13, s[44:47], s49 offen offset:1024 sc1\n\t"
      "v_mfma_f32_16x16x32_bf16 v[36:39], a[104:107], a[20:23], v[36:39]\n\t"
      "v_mfma_f32_16x16x32_bf16 v[16:19], v[160:163], a[24:27], v[16:19]\n\t"
      "ds_read_b128 a[0:3], v52 offset:0\n\t"
      "v_mfma_f32_16x16x32_bf16 v[16:19], v[176:179], a[28:31], v[16:19]\n\t"
      "ds_read_b128 a[4:7], v53 offset:0\n\t"
      "v_mfma_f32_16x16x32_bf16 v[16:19], v[192:195], a[32:35], v[16:19]\n\t"
      "ds_read_b128 a[8:11], v54 offset:0\n\t"
      "v_mfma_f32_16x16x32_bf16 v[16:19], v[208:211], a[36:39], v[16:19]\n\t"
      "ds_read_b128 a[12:15], v55 offset:0\n\t"
      "v_mfma_f32_16x16x32_bf16 v[16:19], v[224:227], a[40:43], v[16:19]\n\t"
      "ds_read_b128 a[16:19], v52 offset:256\n\t"
      "v_mfma_f32_16x16x32_bf16 v[16:19], v[240:243], a[44:47], v[16:19]\n\t"
      "ds_read_b128 a[20:23], v53 offset:256\n\t"
      "v_mfma_f32_16x16x32_bf16 v[20:23], v[164:167], a[24:27], v[20:23]\n\t"
      "s_add_u32 s51, s51, 12288\n\t"
      "s_cmp_eq_u32 s51, 160256\n\t"
      "s_cselect_b32 s51, 25088, s51\n\t"
      "v_mfma_f32_16x16x32_bf16 v[20:23], v[180:183], a[28:31], v[20:23]\n\t"
      "s_waitcnt lgkmcnt(6)\n\t"
      "v_mfma_f32_16x16x32_bf16 v[20:23], v[196:199], a[32:35], v[20:23]\n\t"
      "buffer_store_dwordx4 a[56:59], v14, s[44:47], s49 offen offset:2048 sc1\n\t"
      "v_mfma_f32_16x16x32_bf16 v[20:23], v[212:215], a[36:39], v[20:23]\n\t"
      "v_mfma_f32_16x16x32_bf16 v[20:23], v[228:231], a[40:43], v[20:23]\n\t"
      "s_waitcnt lgkmcnt(0)\n\t"
      "s_sub_u32 s53, s36, s38\n\t"
      "s_cmp_lt_i32 s53, 0\n\t"
      "v_mfma_f32_16x16x32_bf16 v[20:23], v[244:247], a[44:47], v[20:23]\n\t"
      "s_cbranch_scc1 .Lspin0_%=\n\t"
      ".Lspun0_%=:\n\t"
      "s_sub_i32 s53, s53, 30\n\t"
      "s_max_i32 s53, s53, 0\n\t"
      "v_mfma_f32_16x16x32_bf16 v[24:27], v[168:171], a[24:27], v[24:27]\n\t"
      "s_add_u32 s38, s38, s53\n\t"
      "s_add_u32 s38, s38, s39\n\t"
      "s_add_u32 s28, s28, s53\n\t"
      "v_mfma_f32_16x16x32_bf16 v[24:27], v[184:187], a[28:31], v[24:27]\n\t"
      "v_cvt_pk_bf16_f32 v40, v16, v17\n\t"
      "v_cvt_pk_bf16_f32 v41, v18, v19\n\t"
      "v_mfma_f32_16x16x32_bf16 v[24:27], v[200:203], a[32:35], v[24:27]\n\t"
      "ds_write_b64 v0, v[40:41] offset:0\n\t"
      "v_mfma_f32_16x16x32_bf16 v[24:27], v[216:219], a[36:39], v[24:27]\n\t"
      "buffer_load_dwordx4 v3, s[40:43], s48 offen offset:1024 nt lds\n\t"
      "v_mfma_f32_16x16x32_bf16 v[24:27], v[232:235], a[40:43], v[24:27]\n\t"
      "s_add_u32 s58, s58, 786432\n\t"
      "s_lshr_b32 s59, s59, 1\n\t"
      "v_mfma_f32_16x16x32_bf16 v[24:27], v[248:251], a[44:47], v[24:27]\n\t"
      "s_bitcmp1_b32 s59, 0\n\t"
      "s_cselect_b32 s49, 24576, 0\n\t"
      "s_add_u32 s49, s49, s58\n\t"
      "v_mfma_f32_16x16x32_bf16 v[28:31], v[172:175], a[24:27], v[28:31]\n\t"
      "s_sub_i32 s87, s87, 6\n\t"
      "s_max_i32 s87, s87, 0\n\t"
      "v_mfma_f32_16x16x32_bf16 v[28:31], v[188:191], a[28:31], v[28:31]\n\t"
      "v_cvt_pk_bf16_f32 v42, v20, v21\n\t"
      "v_cvt_pk_bf16_f32 v43, v22, v23\n\t"
      "v_mfma_f32_16x16x32_bf16 v[28:31], v[204:207], a[32:35], v[28:31]\n\t"
      "ds_write_b64 v0, v[42:43] offset:32\n\t"
      "v_mfma_f32_16x16x32_bf16 v[28:31], v[220:223], a[36:39], v[28:31]\n\t"
      "s_cmp_gt_u32 s28, 300\n\t"
      "s_cselect_b32 s53, s53, 0\n\t"
      "v_mfma_f32_16x16x32_bf16 v[28:31], v[236:239], a[40:43], v[28:31]\n\t"
      "s_cmp_gt_u32 s53, 10\n\t"
      "s_cselect_b32 s87, 44, s87\n\t"
      "s_add_u32 s38, s38, s87\n\t"
      "v_mfma_f32_16x16x32_bf16 v[28:31], v[252:255], a[44:47], v[28:31]\n\t"
      "s_cmp_eq_u32 s52, 14\n\t"
      "s_cbranch_scc1 .Lshb_%=\n\t"
      ".Lshbr_%=:\n\t"
      "v_mfma_f32_16x16x32_bf16 v[32:35], a[108:111], a[24:27], v[32:35]\n\t"
      "v_mfma_f32_16x16x32_bf16 v[32:35], a[116:119], a[28:31], v[32:35]\n\t"
      "v_cvt_pk_bf16_f32 v44, v24, v25\n\t"
      "v_cvt_pk_bf16_f32 v45, v26, v27\n\t"
      "v_mfma_f32_16x16x32_bf16 v[32:35], a[124:127], a[32:35], v[32:35]\n\t"
      "ds_write_b64 v0, v[44:45] offset:64\n\t"
      "v_mfma_f32_16x16x32_bf16 v[32:35], a[132:135], a[36:39], v[32:35]\n\t"
      "buffer_load_dwordx4 v4, s[40:43], s48 offen offset:2048 nt lds\n\t"
      "v_mfma_f32_16x16x32_bf16 v[32:35], a[140:143], a[40:43], v[32:35]\n\t"
      "s_add_u32 s50, s50, 12288\n\t"
      "v_mfma_f32_16x16x32_bf16 v[32:35], a[148:151], a[44:47], v[32:35]\n\t"
      "v_cvt_pk_bf16_f32 v46, v28, v29\n\t"
      "v_cvt_pk_bf16_f32 v47, v30, v31\n\t"
      "v_mfma_f32_16x16x32_bf16 v[36:39], a[112:115], a[24:27], v[36:39]\n\t"
      "ds_write_b64 v0, v[46:47] offset:96\n\t"
      "v_mfma_f32_16x16x32_bf16 v[36:39], a[120:123], a[28:31], v[36:39]\n\t"
      "s_cmp_eq_u32 s50, 160256\n\t"
      "s_cselect_b32 s50, 25088, s50\n\t"
      "v_mfma_f32_16x16x32_bf16 v[36:39], a[128:131], a[32:35], v[36:39]\n\t"
      "v_mfma_f32_16x16x32_bf16 v[36:39], a[136:139], a[36:39], v[36:39]\n\t"
      "v_mfma_f32_16x16x32_bf16 v[36:39], a[144:147], a[40:43], v[36:39]\n\t"
      "v_cvt_pk_bf16_f32 v48, v32, v33\n\t"
      "v_cvt_pk_bf16_f32 v49, v34, v35\n\t"
      "v_mfma_f32_16x16x32_bf16 v[36:39], a[152:155], a[44:47], v[36:39]\n\t"
      "ds_write_b64 v0, v[48:49] offset:128\n\t"
      ".Lmid0_%=:\n\t"
      "v_mfma_f32_16x16x32_bf16 v[16:19], v[64:67], a[0:3], 0\n\t"
      "ds_read_b128 a[24:27], v54 offset:256\n\t"
      "v_mfma_f32_16x16x32_bf16 v[16:19], v[80:83], a[4:7], v[16:19]\n\t"
      "ds_read_b128 a[28:31], v55 offset:256\n\t"
      "v_mfma_f32_16x16x32_bf16 v[16:19], v[96:99], a[8:11], v[16:19]\n\t"
      "ds_read_b128 a[32:35], v52 offset:512\n\t"
      "v_mfma_f32_16x16x32_bf16 v[16:19], v[112:115], a[12:15], v[16:19]\n\t"
      "ds_read_b128 a[36:39], v53 offset:512\n\t"
      "v_mfma_f32_16x16x32_bf16 v[16:19], v[128:131], a[16:19], v[16:19]\n\t"
      "ds_read_b128 a[40:43], v54 offset:512\n\t"
      "v_mfma_f32_16x16x32_bf16 v[16:19], v[144:147], a[20:23], v[16:19]\n\t"
      "ds_read_b128 a[44:47], v55 offset:512\n\t"
      "v_mfma_f32_16x16x32_bf16 v[20:23], v[68:71], a[0:3], 0\n\t"
      "v_cvt_pk_bf16_f32 v50, v36, v37\n\t"
      "v_cvt_pk_bf16_f32 v51, v38, v39\n\t"
      "v_mfma_f32_16x16x32_bf16 v[20:23], v[84:87], a[4:7], v[20:23]\n\t"
      "ds_write_b64 v0, v[50:51] offset:160\n\t"
      "v_mfma_f32_16x16x32_bf16 v[20:23], v[100:103], a[8:11], v[20:23]\n\t"
      "v_add_u32 v56, s51, v15\n\t"
      "v_xor_b32 v57, 64, v56\n\t"
      "v_mfma_f32_16x16x32_bf16 v[20:23], v[116:119], a[12:15], v[20:23]\n\t"
      "v_xor_b32 v58, 128, v56\n\t"
      "v_xor_b32 v59, 192, v56\n\t"
      "v_mfma_f32_16x16x32_bf16 v[20:23], v[132:135], a[16:19], v[20:23]\n\t"
      "v_mfma_f32_16x16x32_bf16 v[20:23], v[148:151], a[20:23], v[20:23]\n\t"
      "v_mfma_f32_16x16x32_bf16 v[24:27], v[72:75], a[0:3], 0\n\t"
      "v_mfma_f32_16x16x32_bf16 v[24:27], v[88:91], a[4:7], v[24:27]\n\t"
      "v_mfma_f32_16x16x32_bf16 v[24:27], v[104:107], a[8:11], v[24:27]\n\t"
      "s_waitcnt vmcnt(12)\n\t"
      "s_waitcnt lgkmcnt(0)\n\t"
      "s_barrier\n\t"
      "v_mfma_f32_16x16x32_bf16 v[24:27], v[120:123], a[12:15], v[24:27]\n\t"
      "v_mfma_f32_16x16x32_bf16 v[24:27], v[136:139], a[16:19], v[24:27]\n\t"
      "v_mfma_f32_16x16x32_bf16 v[24:27], v[152:155], a[20:23], v[24:27]\n\t"
      "v_mfma_f32_16x16x32_bf16 v[28:31], v[76:79], a[0:3], 0\n\t"
      "ds_read_b128 a[48:51], v8 offset:0\n\t"
      "v_mfma_f32_16x16x32_bf16 v[28:31], v[92:95], a[4:7], v[28:31]\n\t"
      "ds_read_b128 a[52:55], v60 offset:1024\n\t"
      "v_mfma_f32_16x16x32_bf16 v[28:31], v[108:111], a[8:11], v[28:31]\n\t"
      "ds_read_b128 a[56:59], v61 offset:2048\n\t"
      "v_mfma_f32_16x16x32_bf16 v[28:31], v[124:127], a[12:15], v[28:31]\n\t"
      "s_and_b32 s39, s34, 15\n\t"
      "s_cselect_b32 s53, s29, 0\n\t"
      "v_mfma_f32_16x16x32_bf16 v[28:31], v[140:143], a[16:19], v[28:31]\n\t"
      "s_add_u32 s39, s39, s53\n\t"
      "s_lshr_b64 s[34:35], s[34:35], 4\n\t"
      "v_mfma_f32_16x16x32_bf16 v[28:31], v[156:159], a[20:23], v[28:31]\n\t"
      "s_cmp_le_u32 s52, 12\n\t"
      "s_cselect_b32 s53, 3, 0\n\t"
      "s_min_u32 s53, s28, s53\n\t"
      "v_mfma_f32_16x16x32_bf16 v[32:35], a[60:63], a[0:3], 0\n\t"
      "s_sub_u32 s28, s28, s53\n\t"
      "s_sub_u32 s39, s39, s53\n\t"
      "v_mfma_f32_16x16x32_bf16 v[32:35], a[68:71], a[4:7], v[32:35]\n\t"
      "s_waitcnt lgkmcnt(2)\n\t"
      "v_mfma_f32_16x16x32_bf16 v[32:35], a[76:79], a[8:11], v[32:35]\n\t"
      "buffer_store_dwordx4 a[48:51], v9, s[44:47], s49 offen sc1\n\t"
      "v_mfma_f32_16x16x32_bf16 v[32:35], a[84:87], a[12:15], v[32:35]\n\t"
      "v_mfma_f32_16x16x32_bf16 v[32:35], a[92:95], a[16:19], v[32:35]\n\t"
      "v_mfma_f32_16x16x32_bf16 v[32:35], a[100:103], a[20:23], v[32:35]\n\t"
      "s_add_u32 m0, s18, s50\n\t"
      "v_mfma_f32_16x16x32_bf16 v[36:39], a[64:67], a[0:3], 0\n\t"
      "buffer_load_dwordx4 v5, s[40:43], s48 offen nt lds\n\t"
      "v_mfma_f32_16x16x32_bf16 v[36:39], a[72:75], a[4:7], v[36:39]\n\t"
      "s_memrealtime s[36:37]\n\t"
      "v_mfma_f32_16x16x32_bf16 v[36:39], a[80:83], a[8:11], v[36:39]\n\t"
      "v_mfma_f32_16x16x32_bf16 v[36:39], a[88:91], a[12:15], v[36:39]\n\t"
      "s_waitcnt lgkmcnt(1)\n\t"
      "v_mfma_f32_16x16x32_bf16 v[36:39], a[96:99], a[16:19], v[36:39]\n\t"
      "buffer_store_dwordx4 a[52:55], v10, s[44:47], s49 offen offset:1024 sc1\n\t"
      "v_mfma_f32_16x16x32_bf16 v[36:39], a[104:107], a[20:23], v[36:39]\n\t"
      "v_mfma_f32_16x16x32_bf16 v[16:19], v[160:163], a[24:27], v[16:19]\n\t"
      "ds_read_b128 a[0:3], v56 offset:0\n\t"
      "v_mfma_f32_16x16x32_bf16 v[16:19], v[176:179], a[28:31], v[16:19]\n\t"
      "ds_read_b128 a[4:7], v57 offset:0\n\t"
      "v_mfma_f32_16x16x32_bf16 v[16:19], v[192:195], a[32:35], v[16:19]\n\t"
      "ds_read_b128 a[8:11], v58 offset:0\n\t"
      "v_mfma_f32_16x16x32_bf16 v[16:19], v[208:211], a[36:39], v[16:19]\n\t"
      "ds_read_b128 a[12:15], v59 offset:0\n\t"
      "v_mfma_f32_16x16x32_bf16 v[16:19], v[224:227], a[40:43], v[16:19]\n\t"
      "ds_read_b128 a[16:19], v56 offset:256\n\t"
      "v_mfma_f32_16x16x32_bf16 v[16:19], v[240:243], a[44:47], v[16:19]\n\t"
      "ds_read_b128 a[20:23], v57 offset:256\n\t"
      "v_mfma_f32_16x16x32_bf16 v[20:23], v[164:167], a[24:27], v[20:23]\n\t"
      "s_add_u32 s51, s51, 12288\n\t"
      "s_cmp_eq_u32 s51, 160256\n\t"
      "s_cselect_b32 s51, 25088, s51\n\t"
      "v_mfma_f32_16x16x32_bf16 v[20:23], v[180:183], a[28:31], v[20:23]\n\t"
      "s_waitcnt lgkmcnt(6)\n\t"
      "v_mfma_f32_16x16x32_bf16 v[20:23], v[196:199], a[32:35], v[20:23]\n\t"
      "buffer_store_dwordx4 a[56:59], v11, s[44:47], s49 offen offset:2048 sc1\n\t"
      "v_mfma_f32_16x16x32_bf16 v[20:23], v[212:215], a[36:39], v[20:23]\n\t"
      "v_mfma_f32_16x16x32_bf16 v[20:23], v[228:231], a[40:43], v[20:23]\n\t"
      "s_waitcnt lgkmcnt(0)\n\t"
      "s_sub_u32 s53, s36, s38\n\t"
      "s_cmp_lt_i32 s53, 0\n\t"
      "v_mfma_f32_16x16x32_bf16 v[20:23], v[244:247], a[44:47], v[20:23]\n\t"
      "s_cbranch_scc1 .Lspin1_%=\n\t"
      ".Lspun1_%=:\n\t"
      "s_sub_i32 s53, s53, 30\n\t"
      "s_max_i32 s53, s53, 0\n\t"
      "v_mfma_f32_16x16x32_bf16 v[24:27], v[168:171], a[24:27], v[24:27]\n\t"
      "s_add_u32 s38, s38, s53\n\t"
      "s_add_u32 s38, s38, s39\n\t"
      "s_add_u32 s28, s28, s53\n\t"
      "v_mfma_f32_16x16x32_bf16 v[24:27], v[184:187], a[28:31], v[24:27]\n\t"
      "v_cvt_pk_bf16_f32 v40, v16, v17\n\t"
      "v_cvt_pk_bf16_f32 v41, v18, v19\n\t"
      "v_mfma_f32_16x16x32_bf16 v[24:27], v[200:203], a[32:35], v[24:27]\n\t"
      "ds_write_b64 v0, v[40:41] offset:12544\n\t"
      "v_mfma_f32_16x16x32_bf16 v[24:27], v[216:219], a[36:39], v[24:27]\n\t"
      "buffer_load_dwordx4 v6, s[40:43], s48 offen offset:1024 nt lds\n\t"
      "v_mfma_f32_16x16x32_bf16 v[24:27], v[232:235], a[40:43], v[24:27]\n\t"
      "v_mfma_f32_16x16x32_bf16 v[24:27], v[248:251], a[44:47], v[24:27]\n\t"
      "v_mfma_f32_16x16x32_bf16 v[28:31], v[172:175], a[24:27], v[28:31]\n\t"
      "s_sub_i32 s87, s87, 6\n\t"
      "s_max_i32 s87, s87, 0\n\t"
      "v_mfma_f32_16x16x32_bf16 v[28:31], v[188:191], a[28:31], v[28:31]\n\t"
      "v_cvt_pk_bf16_f32 v42, v20, v21\n\t"
      "v_cvt_pk_bf16_f32 v43, v22, v23\n\t"
      "v_mfma_f32_16x16x32_bf16 v[28:31], v[204:207], a[32:35], v[28:31]\n\t"
      "ds_write_b64 v0, v[42:43] offset:12576\n\t"
      "v_mfma_f32_16x16x32_bf16 v[28:31], v[220:223], a[36:39], v[28:31]\n\t"
      "s_cmp_gt_u32 s28, 300\n\t"
      "s_cselect_b32 s53, s53, 0\n\t"
      "v_mfma_f32_16x16x32_bf16 v[28:31], v[236:239], a[40:43], v[28:31]\n\t"
      "s_cmp_gt_u32 s53, 10\n\t"
      "s_cselect_b32 s87, 44, s87\n\t"
      "s_add_u32 s38, s38, s87\n\t"
      "v_mfma_f32_16x16x32_bf16 v[28:31], v[252:255], a[44:47], v[28:31]\n\t"
      "v_mfma_f32_16x16x32_bf16 v[32:35], a[108:111], a[24:27], v[32:35]\n\t"
      "v_mfma_f32_16x16x32_bf16 v[32:35], a[116:119], a[28:31], v[32:35]\n\t"
      "v_cvt_pk_bf16_f32 v44, v24, v25\n\t"
      "v_cvt_pk_bf16_f32 v45, v26, v27\n\t"
      "v_mfma_f32_16x16x32_bf16 v[32:35], a[124:127], a[32:35], v[32:35]\n\t"
      "ds_write_b64 v0, v[44:45] offset:12608\n\t"
      "v_mfma_f32_16x16x32_bf16 v[32:35], a[132:135], a[36:39], v[32:35]\n\t"
      "buffer_load_dwordx4 v7, s[40:43], s48 offen offset:2048 nt lds\n\t"
      "v_mfma_f32_16x16x32_bf16 v[32:35], a[140:143], a[40:43], v[32:35]\n\t"
      "s_add_u32 s50, s50, 12288\n\t"
      "v_mfma_f32_16x16x32_bf16 v[32:35], a[148:151], a[44:47], v[32:35]\n\t"
      "v_cvt_pk_bf16_f32 v46, v28, v29\n\t"
      "v_cvt_pk_bf16_f32 v47, v30, v31\n\t"
      "v_mfma_f32_16x16x32_bf16 v[36:39], a[112:115], a[24:27], v[36:39]\n\t"
      "ds_write_b64 v0, v[46:47] offset:12640\n\t"
      "v_mfma_f32_16x16x32_bf16 v[36:39], a[120:123], a[28:31], v[36:39]\n\t"
      "s_cmp_eq_u32 s50, 160256\n\t"
      "s_cselect_b32 s50, 25088, s50\n\t"
      "v_mfma_f32_16x16x32_bf16 v[36:39], a[128:131], a[32:35], v[36:39]\n\t"
      "s_sub_u32 s52, s52, 1\n\t"
      "s_cmp_lg_u32 s52, 0\n\t"
      "v_mfma_f32_16x16x32_bf16 v[36:39], a[136:139], a[36:39], v[36:39]\n\t"
      "v_mfma_f32_16x16x32_bf16 v[36:39], a[144:147], a[40:43], v[36:39]\n\t"
      "v_cvt_pk_bf16_f32 v48, v32, v33\n\t"
      "v_cvt_pk_bf16_f32 v49, v34, v35\n\t"
      "v_mfma_f32_16x16x32_bf16 v[36:39], a[152:155], a[44:47], v[36:39]\n\t"
      "ds_write_b64 v0, v[48:49] offset:12672\n\t"
      "s_cbranch_scc1 .Lloop0_%=\n\t"
      "s_branch .Lendc_%=\n\t"
      ".Lshb_%=:\n\t"
      "s_waitcnt vmcnt(10)\n\t"
      "v_bfe_u32 v63, v63, 17, 8\n\t"
      "v_min_u32 v62, 1, v63\n\t"
      "v_lshl_add_u32 v63, v62, 16, v63\n\t"
      "s_nop 1\n\t"
      "v_add_u32_dpp v63, v63, v63 row_shr:1 row_mask:0xf bank_mask:0xf bound_ctrl:0\n\t"
      "s_nop 1\n\t"
      "v_add_u32_dpp v63, v63, v63 row_shr:2 row_mask:0xf bank_mask:0xf bound_ctrl:0\n\t"
      "s_nop 1\n\t"
      "v_add_u32_dpp v63, v63, v63 row_shr:4 row_mask:0xf bank_mask:0xf bound_ctrl:0\n\t"
      "s_nop 1\n\t"
      "v_add_u32_dpp v63, v63, v63 row_shr:8 row_mask:0xf bank_mask:0xf bound_ctrl:0\n\t"
      "s_nop 0\n\t"
      "v_readlane_b32 s64, v63, 15\n\t"
      "v_readlane_b32 s65, v63, 31\n\t"
      "s_nop 1\n\t"
      "s_add_u32 s64, s64, s65\n\t"
      "s_lshr_b32 s65, s64, 16\n\t"
      "s_and_b32 s64, s64, 0xffff\n\t"
      "s_mov_b32 s66, 0\n\t"
      "s_mul_i32 s67, s65, 934\n\t"
      "s_lshl_b32 s69, s64, 3\n\t"
      "s_cmp_lt_u32 s69, s67\n\t"
      "s_cselect_b32 s66, 3, s66\n\t"
      "s_add_u32 s29, s29, s66\n\t"
      "s_mul_i32 s67, s65, 737\n\t"
      "s_cmp_lt_u32 s69, s67\n\t"
      "s_cbranch_scc0 .Lshbr_%=\n\t"
      "s_movk_i32 s29, 79\n\t"
      "s_mov_b32 s32, 0x11111111\n\t"
      "s_mov_b32 s33, 0x11111111\n\t"
      "s_mov_b32 s34, 0x11111111\n\t"
      "s_mov_b32 s35, 0x11111111\n\t"
      "s_sub_i32 s28, s28, 30\n\t"
      "s_min_i32 s66, s28, 0\n\t"
      "s_sub_u32 s38, s38, s66\n\t"
      "s_max_i32 s28, s28, 0\n\t"
      "s_branch .Lshbr_%=\n\t"
      ".Lloop1_%=:\n\t"
      "v_mfma_f32_16x16x32_bf16 v[16:19], v[64:67], a[0:3], 0\n\t"
      "ds_read_b128 a[24:27], v58 offset:256\n\t"
      "v_mfma_f32_16x16x32_bf16 v[16:19], v[80:83], a[4:7], v[16:19]\n\t"
      "ds_read_b128 a[28:31], v59 offset:256\n\t"
      "v_mfma_f32_16x16x32_bf16 v[16:19], v[96:99], a[8:11], v[16:19]\n\t"
      "ds_read_b128 a[32:35], v56 offset:512\n\t"
      "v_mfma_f32_16x16x32_bf16 v[16:19], v[112:115], a[12:15], v[16:19]\n\t"
      "ds_read_b128 a[36:39], v57 offset:512\n\t"
      "v_mfma_f32_16x16x32_bf16 v[16:19], v[128:131], a[16:19], v[16:19]\n\t"
      "ds_read_b128 a[40:43], v58 offset:512\n\t"
      "v_mfma_f32_16x16x32_bf16 v[16:19], v[144:147], a[20:23], v[16:19]\n\t"
      "ds_read_b128 a[44:47], v59 offset:512\n\t"
      "v_mfma_f32_16x16x32_bf16 v[20:23], v[68:71], a[0:3], 0\n\t"
      "v_cvt_pk_bf16_f32 v50, v36, v37\n\t"
      "v_cvt_pk_bf16_f32 v51, v38, v39\n\t"
      "v_mfma_f32_16x16x32_bf16 v[20:23], v[84:87], a[4:7], v[20:23]\n\t"
      "ds_write_b64 v0, v[50:51] offset:12704\n\t"
      "v_mfma_f32_16x16x32_bf16 v[20:23], v[100:103], a[8:11], v[20:23]\n\t"
      "v_add_u32 v52, s51, v15\n\t"
      "v_xor_b32 v53, 64, v52\n\t"
      "v_mfma_f32_16x16x32_bf16 v[20:23], v[116:119], a[12:15], v[20:23]\n\t"
      "v_xor_b32 v54, 128, v52\n\t"
      "v_xor_b32 v55, 192, v52\n\t"
      "v_mfma_f32_16x16x32_bf16 v[20:23], v[132:135], a[16:19], v[20:23]\n\t"
      "s_add_u32 s56, s56, 786432\n\t"
      "s_lshr_b32 s57, s57, 1\n\t"
      "v_mfma_f32_16x16x32_bf16 v[20:23], v[148:151], a[20:23], v[20:23]\n\t"
      "s_bitcmp1_b32 s57, 0\n\t"
      "s_cselect_b32 s48, 24576, 0\n\t"
      "s_add_u32 s48, s48, s56\n\t"
      "v_mfma_f32_16x16x32_bf16 v[24:27], v[72:75], a[0:3], 0\n\t"
      "v_mfma_f32_16x16x32_bf16 v[24:27], v[88:91], a[4:7], v[24:27]\n\t"
      "v_mfma_f32_16x16x32_bf16 v[24:27], v[104:107], a[8:11], v[24:27]\n\t"
      "v_mfma_f32_16x16x32_bf16 v[24:27], v[120:123], a[12:15], v[24:27]\n\t"
      "s_waitcnt vmcnt(12)\n\t"
      "s_waitcnt lgkmcnt(0)\n\t"
      "s_barrier\n\t"
      "v_mfma_f32_16x16x32_bf16 v[24:27], v[136:139], a[16:19], v[24:27]\n\t"
      "v_mfma_f32_16x16x32_bf16 v[24:27], v[152:155], a[20:23], v[24:27]\n\t"
      "v_mfma_f32_16x16x32_bf16 v[28:31], v[76:79], a[0:3], 0\n\t"
      "ds_read_b128 a[48:51], v8 offset:12544\n\t"
      "v_mfma_f32_16x16x32_bf16 v[28:31], v[92:95], a[4:7], v[28:31]\n\t"
      "ds_read_b128 a[52:55], v60 offset:13568\n\t"
      "v_mfma_f32_16x16x32_bf16 v[28:31], v[108:111], a[8:11], v[28:31]\n\t"
      "ds_read_b128 a[56:59], v61 offset:14592\n\t"
      "v_mfma_f32_16x16x32_bf16 v[28:31], v[124:127], a[12:15], v[28:31]\n\t"
      "v_mfma_f32_16x16x32_bf16 v[28:31], v[140:143], a[16:19], v[28:31]\n\t"
      "v_mfma_f32_16x16x32_bf16 v[28:31], v[156:159], a[20:23], v[28:31]\n\t"
      "v_mfma_f32_16x16x32_bf16 v[32:35], a[60:63], a[0:3], 0\n\t"
      "v_mfma_f32_16x16x32_bf16 v[32:35], a[68:71], a[4:7], v[32:35]\n\t"
      "s_waitcnt lgkmcnt(2)\n\t"
      "v_mfma_f32_16x16x32_bf16 v[32:35], a[76:79], a[8:11], v[32:35]\n\t"
      "buffer_store_dwordx4 a[48:51], v12, s[44:47], s49 offen sc1\n\t"
      "v_mfma_f32_16x16x32_bf16 v[32:35], a[84:87], a[12:15], v[32:35]\n\t"
      "v_mfma_f32_16x16x32_bf16 v[32:35], a[92:95], a[16:19], v[32:35]\n\t"
      "v_mfma_f32_16x16x32_bf16 v[32:35], a[100:103], a[20:23], v[32:35]\n\t"
      "s_add_u32 m0, s18, s50\n\t"
      "v_mfma_f32_16x16x32_bf16 v[36:39], a[64:67], a[0:3], 0\n\t"
      "buffer_load_dwordx4 v2, s[40:43], s48 offen nt lds\n\t"
      "v_mfma_f32_16x16x32_bf16 v[36:39], a[72:75], a[4:7], v[36:39]\n\t"
      "v_mfma_f32_16x16x32_bf16 v[36:39], a[80:83], a[8:11], v[36:39]\n\t"
      "v_mfma_f32_16x16x32_bf16 v[36:39], a[88:91], a[12:15], v[36:39]\n\t"
      "s_waitcnt lgkmcnt(1)\n\t"
      "v_mfma_f32_16x16x32_bf16 v[36:39], a[96:99], a[16:19], v[36:39]\n\t"
      "buffer_store_dwordx4 a[52:55], v13, s[44:47], s49 offen offset:1024 sc1\n\t"
      "v_mfma_f32_16x16x32_bf16 v[36:39], a[104:107], a[20:23], v[36:39]\n\t"
      "v_mfma_f32_16x16x32_bf16 v[16:19], v[160:163], a[24:27], v[16:19]\n\t"
      "ds_read_b128 a[0:3], v52 offset:0\n\t"
      "v_mfma_f32_16x16x32_bf16 v[16:19], v[176:179], a[28:31], v[16:19]\n\t"
      "ds_read_b128 a[4:7], v53 offset:0\n\t"
      "v_mfma_f32_16x16x32_bf16 v[16:19], v[192:195], a[32:35], v[16:19]\n\t"
      "ds_read_b128 a[8:11], v54 offset:0\n\t"
      "v_mfma_f32_16x16x32_bf16 v[16:19], v[208:211], a[36:39], v[16:19]\n\t"
      "ds_read_b128 a[12:15], v55 offset:0\n\t"
      "v_mfma_f32_16x16x32_bf16 v[16:19], v[224:227], a[40:43], v[16:19]\n\t"
      "ds_read_b128 a[16:19], v52 offset:256\n\t"
      "v_mfma_f32_16x16x32_bf16 v[16:19], v[240:243], a[44:47], v[16:19]\n\t"
      "ds_read_b128 a[20:23], v53 offset:256\n\t"
      "v_mfma_f32_16x16x32_bf16 v[20:23], v[164:167], a[24:27], v[20:23]\n\t"
      "s_add_u32 s51, s51, 12288\n\t"
      "s_cmp_eq_u32 s51, 160256\n\t"
      "s_cselect_b32 s51, 25088, s51\n\t"
      "v_mfma_f32_16x16x32_bf16 v[20:23], v[180:183], a[28:31], v[20:23]\n\t"
      "s_waitcnt lgkmcnt(6)\n\t"
      "v_mfma_f32_16x16x32_bf16 v[20:23], v[196:199], a[32:35], v[20:23]\n\t"
      "buffer_store_dwordx4 a[56:59], v14, s[44:47], s49 offen offset:2048 sc1\n\t"
      "v_mfma_f32_16x16x32_bf16 v[20:23], v[212:215], a[36:39], v[20:23]\n\t"
      "v_mfma_f32_16x16x32_bf16 v[20:23], v[228:231], a[40:43], v[20:23]\n\t"
      "s_waitcnt lgkmcnt(0)\n\t"
      "v_mfma_f32_16x16x32_bf16 v[20:23], v[244:247], a[44:47], v[20:23]\n\t"
      "v_mfma_f32_16x16x32_bf16 v[24:27], v[168:171], a[24:27], v[24:27]\n\t"
      "v_mfma_f32_16x16x32_bf16 v[24:27], v[184:187], a[28:31], v[24:27]\n\t"
      "v_cvt_pk_bf16_f32 v40, v16, v17\n\t"
      "v_cvt_pk_bf16_f32 v41, v18, v19\n\t"
      "v_mfma_f32_16x16x32_bf16 v[24:27], v[200:203], a[32:35], v[24:27]\n\t"
      "ds_write_b64 v0, v[40:41] offset:0\n\t"
      "v_mfma_f32_16x16x32_bf16 v[24:27], v[216:219], a[36:39], v[24:27]\n\t"
      "buffer_load_dwordx4 v3, s[40:43], s48 offen offset:1024 nt lds\n\t"
      "v_mfma_f32_16x16x32_bf16 v[24:27], v[232:235], a[40:43], v[24:27]\n\t"
      "s_add_u32 s58, s58, 786432\n\t"
      "s_lshr_b32 s59, s59, 1\n\t"
      "v_mfma_f32_16x16x32_bf16 v[24:27], v[248:251], a[44:47], v[24:27]\n\t"
      "s_bitcmp1_b32 s59, 0\n\t"
      "s_cselect_b32 s49, 24576, 0\n\t"
      "s_add_u32 s49, s49, s58\n\t"
      "v_mfma_f32_16x16x32_bf16 v[28:31], v[172:175], a[24:27], v[28:31]\n\t"
      "v_mfma_f32_16x16x32_bf16 v[28:31], v[188:191], a[28:31], v[28:31]\n\t"
      "v_cvt_pk_bf16_f32 v42, v20, v21\n\t"
      "v_cvt_pk_bf16_f32 v43, v22, v23\n\t"
      "v_mfma_f32_16x16x32_bf16 v[28:31], v[204:207], a[32:35], v[28:31]\n\t"
      "ds_write_b64 v0, v[42:43] offset:32\n\t"
      "v_mfma_f32_16x16x32_bf16 v[28:31], v[220:223], a[36:39], v[28:31]\n\t"
      "v_mfma_f32_16x16x32_bf16 v[28:31], v[236:239], a[40:43], v[28:31]\n\t"
      "v_mfma_f32_16x16x32_bf16 v[28:31], v[252:255], a[44:47], v[28:31]\n\t"
      "v_mfma_f32_16x16x32_bf16 v[32:35], a[108:111], a[24:27], v[32:35]\n\t"
      "v_mfma_f32_16x16x32_bf16 v[32:35], a[116:119], a[28:31], v[32:35]\n\t"
      "v_cvt_pk_bf16_f32 v44, v24, v25\n\t"
      "v_cvt_pk_bf16_f32 v45, v26, v27\n\t"
      "v_mfma_f32_16x16x32_bf16 v[32:35], a[124:127], a[32:35], v[32:35]\n\t"
      "ds_write_b64 v0, v[44:45] offset:64\n\t"
      "v_mfma_f32_16x16x32_bf16 v[32:35], a[132:135], a[36:39], v[32:35]\n\t"
      "buffer_load_dwordx4 v4, s[40:43], s48 offen offset:2048 nt lds\n\t"
      "v_mfma_f32_16x16x32_bf16 v[32:35], a[140:143], a[40:43], v[32:35]\n\t"
      "s_add_u32 s50, s50, 12288\n\t"
      "v_mfma_f32_16x16x32_bf16 v[32:35], a[148:151], a[44:47], v[32:35]\n\t"
      "v_cvt_pk_bf16_f32 v46, v28, v29\n\t"
      "v_cvt_pk_bf16_f32 v47, v30, v31\n\t"
      "v_mfma_f32_16x16x32_bf16 v[36:39], a[112:115], a[24:27], v[36:39]\n\t"
      "ds_write_b64 v0, v[46:47] offset:96\n\t"
      "v_mfma_f32_16x16x32_bf16 v[36:39], a[120:123], a[28:31], v[36:39]\n\t"
      "s_cmp_eq_u32 s50, 160256\n\t"
      "s_cselect_b32 s50, 25088, s50\n\t"
      "v_mfma_f32_16x16x32_bf16 v[36:39], a[128:131], a[32:35], v[36:39]\n\t"
      "v_mfma_f32_16x16x32_bf16 v[36:39], a[136:139], a[36:39], v[36:39]\n\t"
      "v_mfma_f32_16x16x32_bf16 v[36:39], a[144:147], a[40:43], v[36:39]\n\t"
      "v_cvt_pk_bf16_f32 v48, v32, v33\n\t"
      "v_cvt_pk_bf16_f32 v49, v34, v35\n\t"
      "v_mfma_f32_16x16x32_bf16 v[36:39], a[152:155], a[44:47], v[36:39]\n\t"
      "ds_write_b64 v0, v[48:49] offset:128\n\t"
      ".Lmid1_%=:\n\t"
      "v_mfma_f32_16x16x32_bf16 v[16:19], v[64:67], a[0:3], 0\n\t"
      "ds_read_b128 a[24:27], v54 offset:256\n\t"
      "v_mfma_f32_16x16x32_bf16 v[16:19], v[80:83], a[4:7], v[16:19]\n\t"
      "ds_read_b128 a[28:31], v55 offset:256\n\t"
      "v_mfma_f32_16x16x32_bf16 v[16:19], v[96:99], a[8:11], v[16:19]\n\t"
      "ds_read_b128 a[32:35], v52 offset:512\n\t"
      "v_mfma_f32_16x16x32_bf16 v[16:19], v[112:115], a[12:15], v[16:19]\n\t"
      "ds_read_b128 a[36:39], v53 offset:512\n\t"
      "v_mfma_f32_16x16x32_bf16 v[16:19], v[128:131], a[16:19], v[16:19]\n\t"
      "ds_read_b128 a[40:43], v54 offset:512\n\t"
      "v_mfma_f32_16x16x32_bf16 v[16:19], v[144:147], a[20:23], v[16:19]\n\t"
      "ds_read_b128 a[44:47], v55 offset:512\n\t"
      "v_mfma_f32_16x16x32_bf16 v[20:23], v[68:71], a[0:3], 0\n\t"
      "v_cvt_pk_bf16_f32 v50, v36, v37\n\t"
      "v_cvt_pk_bf16_f32 v51, v38, v39\n\t"
      "v_mfma_f32_16x16x32_bf16 v[20:23], v[84:87], a[4:7], v[20:23]\n\t"
      "ds_write_b64 v0, v[50:51] offset:160\n\t"
      "v_mfma_f32_16x16x32_bf16 v[20:23], v[100:103], a[8:11], v[20:23]\n\t"
      "v_add_u32 v56, s51, v15\n\t"
      "v_xor_b32 v57, 64, v56\n\t"
      "v_mfma_f32_16x16x32_bf16 v[20:23], v[116:119], a[12:15], v[20:23]\n\t"
      "v_xor_b32 v58, 128, v56\n\t"
      "v_xor_b32 v59, 192, v56\n\t"
      "v_mfma_f32_16x16x32_bf16 v[20:23], v[132:135], a[16:19], v[20:23]\n\t"
      "v_mfma_f32_16x16x32_bf16 v[20:23], v[148:151], a[20:23], v[20:23]\n\t"
      "v_mfma_f32_16x16x32_bf16 v[24:27], v[72:75], a[0:3], 0\n\t"
      "v_mfma_f32_16x16x32_bf16 v[24:27], v[88:91], a[4:7], v[24:27]\n\t"
      "v_mfma_f32_16x16x32_bf16 v[24:27], v[104:107], a[8:11], v[24:27]\n\t"
      "v_mfma_f32_16x16x32_bf16 v[24:27], v[120:123], a[12:15], v[24:27]\n\t"
      "s_waitcnt vmcnt(12)\n\t"
      "s_waitcnt lgkmcnt(0)\n\t"
      "s_barrier\n\t"
      "v_mfma_f32_16x16x32_bf16 v[24:27], v[136:139], a[16:19], v[24:27]\n\t"
      "v_mfma_f32_16x16x32_bf16 v[24:27], v[152:155], a[20:23], v[24:27]\n\t"
      "v_mfma_f32_16x16x32_bf16 v[28:31], v[76:79], a[0:3], 0\n\t"
      "ds_read_b128 a[48:51], v8 offset:0\n\t"
      "v_mfma_f32_16x16x32_bf16 v[28:31], v[92:95], a[4:7], v[28:31]\n\t"
      "ds_read_b128 a[52:55], v60 offset:1024\n\t"
      "v_mfma_f32_16x16x32_bf16 v[28:31], v[108:111], a[8:11], v[28:31]\n\t"
      "ds_read_b128 a[56:59], v61 offset:2048\n\t"
      "v_mfma_f32_16x16x32_bf16 v[28:31], v[124:127], a[12:15], v[28:31]\n\t"
      "v_mfma_f32_16x16x32_bf16 v[28:31], v[140:143], a[16:19], v[28:31]\n\t"
      "v_mfma_f32_16x16x32_bf16 v[28:31], v[156:159], a[20:23], v[28:31]\n\t"
      "v_mfma_f32_16x16x32_bf16 v[32:35], a[60:63], a[0:3], 0\n\t"
      "v_mfma_f32_16x16x32_bf16 v[32:35], a[68:71], a[4:7], v[32:35]\n\t"
      "s_waitcnt lgkmcnt(2)\n\t"
      "v_mfma_f32_16x16x32_bf16 v[32:35], a[76:79], a[8:11], v[32:35]\n\t"
      "buffer_store_dwordx4 a[48:51], v9, s[44:47], s49 offen sc1\n\t"
      "v_mfma_f32_16x16x32_bf16 v[32:35], a[84:87], a[12:15], v[32:35]\n\t"
      "v_mfma_f32_16x16x32_bf16 v[32:35], a[92:95], a[16:19], v[32:35]\n\t"
      "v_mfma_f32_16x16x32_bf16 v[32:35], a[100:103], a[20:23], v[32:35]\n\t"
      "s_add_u32 m0, s18, s50\n\t"
      "v_mfma_f32_16x16x32_bf16 v[36:39], a[64:67], a[0:3], 0\n\t"
      "buffer_load_dwordx4 v5, s[40:43], s48 offen nt lds\n\t"
      "v_mfma_f32_16x16x32_bf16 v[36:39], a[72:75], a[4:7], v[36:39]\n\t"
      "v_mfma_f32_16x16x32_bf16 v[36:39], a[80:83], a[8:11], v[36:39]\n\t"
      "v_mfma_f32_16x16x32_bf16 v[36:39], a[88:91], a[12:15], v[36:39]\n\t"
      "s_waitcnt lgkmcnt(1)\n\t"
      "v_mfma_f32_16x16x32_bf16 v[36:39], a[96:99], a[16:19], v[36:39]\n\t"
      "buffer_store_dwordx4 a[52:55], v10, s[44:47], s49 offen offset:1024 sc1\n\t"
      "v_mfma_f32_16x16x32_bf16 v[36:39], a[104:107], a[20:23], v[36:39]\n\t"
      "v_mfma_f32_16x16x32_bf16 v[16:19], v[160:163], a[24:27], v[16:19]\n\t"
      "ds_read_b128 a[0:3], v56 offset:0\n\t"
      "v_mfma_f32_16x16x32_bf16 v[16:19], v[176:179], a[28:31], v[16:19]\n\t"
      "ds_read_b128 a[4:7], v57 offset:0\n\t"
      "v_mfma_f32_16x16x32_bf16 v[16:19], v[192:195], a[32:35], v[16:19]\n\t"
      "ds_read_b128 a[8:11], v58 offset:0\n\t"
      "v_mfma_f32_16x16x32_bf16 v[16:19], v[208:211], a[36:39], v[16:19]\n\t"
      "ds_read_b128 a[12:15], v59 offset:0\n\t"
      "v_mfma_f32_16x16x32_bf16 v[16:19], v[224:227], a[40:43], v[16:19]\n\t"
      "ds_read_b128 a[16:19], v56 offset:256\n\t"
      "v_mfma_f32_16x16x32_bf16 v[16:19], v[240:243], a[44:47], v[16:19]\n\t"
      "ds_read_b128 a[20:23], v57 offset:256\n\t"
      "v_mfma_f32_16x16x32_bf16 v[20:23], v[164:167], a[24:27], v[20:23]\n\t"
      "s_add_u32 s51, s51, 12288\n\t"
      "s_cmp_eq_u32 s51, 160256\n\t"
      "s_cselect_b32 s51, 25088, s51\n\t"
      "v_mfma_f32_16x16x32_bf16 v[20:23], v[180:183], a[28:31], v[20:23]\n\t"
      "s_waitcnt lgkmcnt(6)\n\t"
      "v_mfma_f32_16x16x32_bf16 v[20:23], v[196:199], a[32:35], v[20:23]\n\t"
      "buffer_store_dwordx4 a[56:59], v11, s[44:47], s49 offen offset:2048 sc1\n\t"
      "v_mfma_f32_16x16x32_bf16 v[20:23], v[212:215], a[36:39], v[20:23]\n\t"
      "v_mfma_f32_16x16x32_bf16 v[20:23], v[228:231], a[40:43], v[20:23]\n\t"
      "s_waitcnt lgkmcnt(0)\n\t"
      "v_mfma_f32_16x16x32_bf16 v[20:23], v[244:247], a[44:47], v[20:23]\n\t"
      "v_mfma_f32_16x16x32_bf16 v[24:27], v[168:171], a[24:27], v[24:27]\n\t"
      "v_mfma_f32_16x16x32_bf16 v[24:27], v[184:187], a[28:31], v[24:27]\n\t"
      "v_cvt_pk_bf16_f32 v40, v16, v17\n\t"
      "v_cvt_pk_bf16_f32 v41, v18, v19\n\t"
      "v_mfma_f32_16x16x32_bf16 v[24:27], v[200:203], a[32:35], v[24:27]\n\t"
      "ds_write_b64 v0, v[40:41] offset:12544\n\t"
      "v_mfma_f32_16x16x32_bf16 v[24:27], v[216:219], a[36:39], v[24:27]\n\t"
      "buffer_load_dwordx4 v6, s[40:43], s48 offen offset:1024 nt lds\n\t"
      "v_mfma_f32_16x16x32_bf16 v[24:27], v[232:235], a[40:43], v[24:27]\n\t"
      "v_mfma_f32_16x16x32_bf16 v[24:27], v[248:251], a[44:47], v[24:27]\n\t"
      "v_mfma_f32_16x16x32_bf16 v[28:31], v[172:175], a[24:27], v[28:31]\n\t"
      "v_mfma_f32_16x16x32_bf16 v[28:31], v[188:191], a[28:31], v[28:31]\n\t"
      "v_cvt_pk_bf16_f32 v42, v20, v21\n\t"
      "v_cvt_pk_bf16_f32 v43, v22, v23\n\t"
      "v_mfma_f32_16x16x32_bf16 v[28:31], v[204:207], a[32:35], v[28:31]\n\t"
      "ds_write_b64 v0, v[42:43] offset:12576\n\t"
      "v_mfma_f32_16x16x32_bf16 v[28:31], v[220:223], a[36:39], v[28:31]\n\t"
      "v_mfma_f32_16x16x32_bf16 v[28:31], v[236:239], a[40:43], v[28:31]\n\t"
      "v_mfma_f32_16x16x32_bf16 v[28:31], v[252:255], a[44:47], v[28:31]\n\t"
      "v_mfma_f32_16x16x32_bf16 v[32:35], a[108:111], a[24:27], v[32:35]\n\t"
      "v_mfma_f32_16x16x32_bf16 v[32:35], a[116:119], a[28:31], v[32:35]\n\t"
      "v_cvt_pk_bf16_f32 v44, v24, v25\n\t"
      "v_cvt_pk_bf16_f32 v45, v26, v27\n\t"
      "v_mfma_f32_16x16x32_bf16 v[32:35], a[124:127], a[32:35], v[32:35]\n\t"
      "ds_write_b64 v0, v[44:45] offset:12608\n\t"
      "v_mfma_f32_16x16x32_bf16 v[32:35], a[132:135], a[36:39], v[32:35]\n\t"
      "buffer_load_dwordx4 v7, s[40:43], s48 offen offset:2048 nt lds\n\t"
      "v_mfma_f32_16x16x32_bf16 v[32:35], a[140:143], a[40:43], v[32:35]\n\t"
      "s_add_u32 s50, s50, 12288\n\t"
      "v_mfma_f32_16x16x32_bf16 v[32:35], a[148:151], a[44:47], v[32:35]\n\t"
      "v_cvt_pk_bf16_f32 v46, v28, v29\n\t"
      "v_cvt_pk_bf16_f32 v47, v30, v31\n\t"
      "v_mfma_f32_16x16x32_bf16 v[36:39], a[112:115], a[24:27], v[36:39]\n\t"
      "ds_write_b64 v0, v[46:47] offset:12640\n\t"
      "v_mfma_f32_16x16x32_bf16 v[36:39], a[120:123], a[28:31], v[36:39]\n\t"
      "s_cmp_eq_u32 s50, 160256\n\t"
      "s_cselect_b32 s50, 25088, s50\n\t"
      "v_mfma_f32_16x16x32_bf16 v[36:39], a[128:131], a[32:35], v[36:39]\n\t"
      "s_sub_u32 s52, s52, 1\n\t"
      "s_cmp_lg_u32 s52, 0\n\t"
      "v_mfma_f32_16x16x32_bf16 v[36:39], a[136:139], a[36:39], v[36:39]\n\t"
      "v_mfma_f32_16x16x32_bf16 v[36:39], a[144:147], a[40:43], v[36:39]\n\t"
      "v_cvt_pk_bf16_f32 v48, v32, v33\n\t"
      "v_cvt_pk_bf16_f32 v49, v34, v35\n\t"
      "v_mfma_f32_16x16x32_bf16 v[36:39], a[152:155], a[44:47], v[36:39]\n\t"
      "ds_write_b64 v0, v[48:49] offset:12672\n\t"
      "s_cbranch_scc1 .Lloop1_%=\n\t"
      "s_branch .Lendc_%=\n\t"
      ".Lloop2_%=:\n\t"
      "v_mfma_f32_16x16x32_bf16 v[16:19], v[64:67], a[0:3], 0\n\t"
      "ds_read_b128 a[24:27], v58 offset:256\n\t"
      "v_mfma_f32_16x16x32_bf16 v[16:19], v[80:83], a[4:7], v[16:19]\n\t"
      "ds_read_b128 a[28:31], v59 offset:256\n\t"
      "v_mfma_f32_16x16x32_bf16 v[16:19], v[96:99], a[8:11], v[16:19]\n\t"
      "ds_read_b128 a[32:35], v56 offset:512\n\t"
      "v_mfma_f32_16x16x32_bf16 v[16:19], v[112:115], a[12:15], v[16:19]\n\t"
      "ds_read_b128 a[36:39], v57 offset:512\n\t"
      "v_mfma_f32_16x16x32_bf16 v[16:19], v[128:131], a[16:19], v[16:19]\n\t"
      "ds_read_b128 a[40:43], v58 offset:512\n\t"
      "v_mfma_f32_16x16x32_bf16 v[16:19], v[144:147], a[20:23], v[16:19]\n\t"
      "ds_read_b128 a[44:47], v59 offset:512\n\t"
      "v_mfma_f32_16x16x32_bf16 v[20:23], v[68:71], a[0:3], 0\n\t"
      "v_cvt_pk_bf16_f32 v50, v36, v37\n\t"
      "v_cvt_pk_bf16_f32 v51, v38, v39\n\t"
      "v_mfma_f32_16x16x32_bf16 v[20:23], v[84:87], a[4:7], v[20:23]\n\t"
      "ds_write_b64 v0, v[50:51] offset:12704\n\t"
      "v_mfma_f32_16x16x32_bf16 v[20:23], v[100:103], a[8:11], v[20:23]\n\t"
      "v_add_u32 v52, s51, v15\n\t"
      "v_xor_b32 v53, 64, v52\n\t"
      "v_mfma_f32_16x16x32_bf16 v[20:23], v[116:119], a[12:15], v[20:23]\n\t"
      "v_xor_b32 v54, 128, v52\n\t"
      "v_xor_b32 v55, 192, v52\n\t"
      "v_mfma_f32_16x16x32_bf16 v[20:23], v[132:135], a[16:19], v[20:23]\n\t"
      "s_add_u32 s56, s56, 786432\n\t"
      "s_lshr_b32 s57, s57, 1\n\t"
      "v_mfma_f32_16x16x32_bf16 v[20:23], v[148:151], a[20:23], v[20:23]\n\t"
      "s_bitcmp1_b32 s57, 0\n\t"
      "s_cselect_b32 s48, 24576, 0\n\t"
      "s_add_u32 s48, s48, s56\n\t"
      "v_mfma_f32_16x16x32_bf16 v[24:27], v[72:75], a[0:3], 0\n\t"
      "v_mfma_f32_16x16x32_bf16 v[24:27], v[88:91], a[4:7], v[24:27]\n\t"
      "v_mfma_f32_16x16x32_bf16 v[24:27], v[104:107], a[8:11], v[24:27]\n\t"
      "v_mfma_f32_16x16x32_bf16 v[24:27], v[120:123], a[12:15], v[24:27]\n\t"
      "v_mfma_f32_16x16x32_bf16 v[24:27], v[136:139], a[16:19], v[24:27]\n\t"
      "s_waitcnt vmcnt(12)\n\t"
      "s_waitcnt lgkmcnt(0)\n\t"
      "s_barrier\n\t"
      "v_mfma_f32_16x16x32_bf16 v[24:27], v[152:155], a[20:23], v[24:27]\n\t"
      "v_mfma_f32_16x16x32_bf16 v[28:31], v[76:79], a[0:3], 0\n\t"
      "ds_read_b128 a[48:51], v8 offset:12544\n\t"
      "v_mfma_f32_16x16x32_bf16 v[28:31], v[92:95], a[4:7], v[28:31]\n\t"
      "ds_read_b128 a[52:55], v60 offset:13568\n\t"
      "v_mfma_f32_16x16x32_bf16 v[28:31], v[108:111], a[8:11], v[28:31]\n\t"
      "ds_read_b128 a[56:59], v61 offset:14592\n\t"
      "v_mfma_f32_16x16x32_bf16 v[28:31], v[124:127], a[12:15], v[28:31]\n\t"
      "v_mfma_f32_16x16x32_bf16 v[28:31], v[140:143], a[16:19], v[28:31]\n\t"
      "v_mfma_f32_16x16x32_bf16 v[28:31], v[156:159], a[20:23], v[28:31]\n\t"
      "v_mfma_f32_16x16x32_bf16 v[32:35], a[60:63], a[0:3], 0\n\t"
      "v_mfma_f32_16x16x32_bf16 v[32:35], a[68:71], a[4:7], v[32:35]\n\t"
      "s_waitcnt lgkmcnt(2)\n\t"
      "v_mfma_f32_16x16x32_bf16 v[32:35], a[76:79], a[8:11], v[32:35]\n\t"
      "buffer_store_dwordx4 a[48:51], v12, s[44:47], s49 offen sc1\n\t"
      "v_mfma_f32_16x16x32_bf16 v[32:35], a[84:87], a[12:15], v[32:35]\n\t"
      "v_mfma_f32_16x16x32_bf16 v[32:35], a[92:95], a[16:19], v[32:35]\n\t"
      "v_mfma_f32_16x16x32_bf16 v[32:35], a[100:103], a[20:23], v[32:35]\n\t"
      "s_add_u32 m0, s18, s50\n\t"
      "v_mfma_f32_16x16x32_bf16 v[36:39], a[64:67], a[0:3], 0\n\t"
      "buffer_load_dwordx4 v2, s[40:43], s48 offen nt lds\n\t"
      "v_mfma_f32_16x16x32_bf16 v[36:39], a[72:75], a[4:7], v[36:39]\n\t"
      "v_mfma_f32_16x16x32_bf16 v[36:39], a[80:83], a[8:11], v[36:39]\n\t"
      "v_mfma_f32_16x16x32_bf16 v[36:39], a[88:91], a[12:15], v[36:39]\n\t"
      "s_waitcnt lgkmcnt(1)\n\t"
      "v_mfma_f32_16x16x32_bf16 v[36:39], a[96:99], a[16:19], v[36:39]\n\t"
      "buffer_store_dwordx4 a[52:55], v13, s[44:47], s49 offen offset:1024 sc1\n\t"
      "v_mfma_f32_16x16x32_bf16 v[36:39], a[104:107], a[20:23], v[36:39]\n\t"
      "v_mfma_f32_16x16x32_bf16 v[16:19], v[160:163], a[24:27], v[16:19]\n\t"
      "ds_read_b128 a[0:3], v52 offset:0\n\t"
      "v_mfma_f32_16x16x32_bf16 v[16:19], v[176:179], a[28:31], v[16:19]\n\t"
      "ds_read_b128 a[4:7], v53 offset:0\n\t"
      "v_mfma_f32_16x16x32_bf16 v[16:19], v[192:195], a[32:35], v[16:19]\n\t"
      "ds_read_b128 a[8:11], v54 offset:0\n\t"
      "v_mfma_f32_16x16x32_bf16 v[16:19], v[208:211], a[36:39], v[16:19]\n\t"
      "ds_read_b128 a[12:15], v55 offset:0\n\t"
      "v_mfma_f32_16x16x32_bf16 v[16:19], v[224:227], a[40:43], v[16:19]\n\t"
      "ds_read_b128 a[16:19], v52 offset:256\n\t"
      "v_mfma_f32_16x16x32_bf16 v[16:19], v[240:243], a[44:47], v[16:19]\n\t"
      "ds_read_b128 a[20:23], v53 offset:256\n\t"
      "v_mfma_f32_16x16x32_bf16 v[20:23], v[164:167], a[24:27], v[20:23]\n\t"
      "s_add_u32 s51, s51, 12288\n\t"
      "s_cmp_eq_u32 s51, 160256\n\t"
      "s_cselect_b32 s51, 25088, s51\n\t"
      "v_mfma_f32_16x16x32_bf16 v[20:23], v[180:183], a[28:31], v[20:23]\n\t"
      "s_waitcnt lgkmcnt(6)\n\t"
      "v_mfma_f32_16x16x32_bf16 v[20:23], v[196:199], a[32:35], v[20:23]\n\t"
      "buffer_store_dwordx4 a[56:59], v14, s[44:47], s49 offen offset:2048 sc1\n\t"
      "v_mfma_f32_16x16x32_bf16 v[20:23], v[212:215], a[36:39], v[20:23]\n\t"
      "v_mfma_f32_16x16x32_bf16 v[20:23], v[228:231], a[40:43], v[20:23]\n\t"
      "s_waitcnt lgkmcnt(0)\n\t"
      "v_mfma_f32_16x16x32_bf16 v[20:23], v[244:247], a[44:47], v[20:23]\n\t"
      "v_mfma_f32_16x16x32_bf16 v[24:27], v[168:171], a[24:27], v[24:27]\n\t"
      "v_mfma_f32_16x16x32_bf16 v[24:27], v[184:187], a[28:31], v[24:27]\n\t"
      "v_cvt_pk_bf16_f32 v40, v16, v17\n\t"
      "v_cvt_pk_bf16_f32 v41, v18, v19\n\t"
      "v_mfma_f32_16x16x32_bf16 v[24:27], v[200:203], a[32:35], v[24:27]\n\t"
      "ds_write_b64 v0, v[40:41] offset:0\n\t"
      "v_mfma_f32_16x16x32_bf16 v[24:27], v[216:219], a[36:39], v[24:27]\n\t"
      "buffer_load_dwordx4 v3, s[40:43], s48 offen offset:1024 nt lds\n\t"
      "v_mfma_f32_16x16x32_bf16 v[24:27], v[232:235], a[40:43], v[24:27]\n\t"
      "s_add_u32 s58, s58, 786432\n\t"
      "s_lshr_b32 s59, s59, 1\n\t"
      "v_mfma_f32_16x16x32_bf16 v[24:27], v[248:251], a[44:47], v[24:27]\n\t"
      "s_bitcmp1_b32 s59, 0\n\t"
      "s_cselect_b32 s49, 24576, 0\n\t"
      "s_add_u32 s49, s49, s58\n\t"
      "v_mfma_f32_16x16x32_bf16 v[28:31], v[172:175], a[24:27], v[28:31]\n\t"
      "v_mfma_f32_16x16x32_bf16 v[28:31], v[188:191], a[28:31], v[28:31]\n\t"
      "v_cvt_pk_bf16_f32 v42, v20, v21\n\t"
      "v_cvt_pk_bf16_f32 v43, v22, v23\n\t"
      "v_mfma_f32_16x16x32_bf16 v[28:31], v[204:207], a[32:35], v[28:31]\n\t"
      "ds_write_b64 v0, v[42:43] offset:32\n\t"
      "v_mfma_f32_16x16x32_bf16 v[28:31], v[220:223], a[36:39], v[28:31]\n\t"
      "v_mfma_f32_16x16x32_bf16 v[28:31], v[236:239], a[40:43], v[28:31]\n\t"
      "v_mfma_f32_16x16x32_bf16 v[28:31], v[252:255], a[44:47], v[28:31]\n\t"
      "v_mfma_f32_16x16x32_bf16 v[32:35], a[108:111], a[24:27], v[32:35]\n\t"
      "v_mfma_f32_16x16x32_bf16 v[32:35], a[116:119], a[28:31], v[32:35]\n\t"
      "v_cvt_pk_bf16_f32 v44, v24, v25\n\t"
      "v_cvt_pk_bf16_f32 v45, v26, v27\n\t"
      "v_mfma_f32_16x16x32_bf16 v[32:35], a[124:127], a[32:35], v[32:35]\n\t"
      "ds_write_b64 v0, v[44:45] offset:64\n\t"
      "v_mfma_f32_16x16x32_bf16 v[32:35], a[132:135], a[36:39], v[32:35]\n\t"
      "buffer_load_dwordx4 v4, s[40:43], s48 offen offset:2048 nt lds\n\t"
      "v_mfma_f32_16x16x32_bf16 v[32:35], a[140:143], a[40:43], v[32:35]\n\t"
      "s_add_u32 s50, s50, 12288\n\t"
      "v_mfma_f32_16x16x32_bf16 v[32:35], a[148:151], a[44:47], v[32:35]\n\t"
      "v_cvt_pk_bf16_f32 v46, v28, v29\n\t"
      "v_cvt_pk_bf16_f32 v47, v30, v31\n\t"
      "v_mfma_f32_16x16x32_bf16 v[36:39], a[112:115], a[24:27], v[36:39]\n\t"
      "ds_write_b64 v0, v[46:47] offset:96\n\t"
      "v_mfma_f32_16x16x32_bf16 v[36:39], a[120:123], a[28:31], v[36:39]\n\t"
      "s_cmp_eq_u32 s50, 160256\n\t"
      "s_cselect_b32 s50, 25088, s50\n\t"
      "v_mfma_f32_16x16x32_bf16 v[36:39], a[128:131], a[32:35], v[36:39]\n\t"
      "v_mfma_f32_16x16x32_bf16 v[36:39], a[136:139], a[36:39], v[36:39]\n\t"
      "v_mfma_f32_16x16x32_bf16 v[36:39], a[144:147], a[40:43], v[36:39]\n\t"
      "v_cvt_pk_bf16_f32 v48, v32, v33\n\t"
      "v_cvt_pk_bf16_f32 v49, v34, v35\n\t"
      "v_mfma_f32_16x16x32_bf16 v[36:39], a[152:155], a[44:47], v[36:39]\n\t"
      "ds_write_b64 v0, v[48:49] offset:128\n\t"
      ".Lmid2_%=:\n\t"
      "v_mfma_f32_16x16x32_bf16 v[16:19], v[64:67], a[0:3], 0\n\t"
      "ds_read_b128 a[24:27], v54 offset:256\n\t"
      "v_mfma_f32_16x16x32_bf16 v[16:19], v[80:83], a[4:7], v[16:19]\n\t"
      "ds_read_b128 a[28:31], v55 offset:256\n\t"
      "v_mfma_f32_16x16x32_bf16 v[16:19], v[96:99], a[8:11], v[16:19]\n\t"
      "ds_read_b128 a[32:35], v52 offset:512\n\t"
      "v_mfma_f32_16x16x32_bf16 v[16:19], v[112:115], a[12:15], v[16:19]\n\t"
      "ds_read_b128 a[36:39], v53 offset:512\n\t"
      "v_mfma_f32_16x16x32_bf16 v[16:19], v[128:131], a[16:19], v[16:19]\n\t"
      "ds_read_b128 a[40:43], v54 offset:512\n\t"
      "v_mfma_f32_16x16x32_bf16 v[16:19], v[144:147], a[20:23], v[16:19]\n\t"
      "ds_read_b128 a[44:47], v55 offset:512\n\t"
      "v_mfma_f32_16x16x32_bf16 v[20:23], v[68:71], a[0:3], 0\n\t"
      "v_cvt_pk_bf16_f32 v50, v36, v37\n\t"
      "v_cvt_pk_bf16_f32 v51, v38, v39\n\t"
      "v_mfma_f32_16x16x32_bf16 v[20:23], v[84:87], a[4:7], v[20:23]\n\t"
      "ds_write_b64 v0, v[50:51] offset:160\n\t"
      "v_mfma_f32_16x16x32_bf16 v[20:23], v[100:103], a[8:11], v[20:23]\n\t"
      "v_add_u32 v56, s51, v15\n\t"
      "v_xor_b32 v57, 64, v56\n\t"
      "v_mfma_f32_16x16x32_bf16 v[20:23], v[116:119], a[12:15], v[20:23]\n\t"
      "v_xor_b32 v58, 128, v56\n\t"
      "v_xor_b32 v59, 192, v56\n\t"
      "v_mfma_f32_16x16x32_bf16 v[20:23], v[132:135], a[16:19], v[20:23]\n\t"
      "v_mfma_f32_16x16x32_bf16 v[20:23], v[148:151], a[20:23], v[20:23]\n\t"
      "v_mfma_f32_16x16x32_bf16 v[24:27], v[72:75], a[0:3], 0\n\t"
      "v_mfma_f32_16x16x32_bf16 v[24:27], v[88:91], a[4:7], v[24:27]\n\t"
      "v_mfma_f32_16x16x32_bf16 v[24:27], v[104:107], a[8:11], v[24:27]\n\t"
      "v_mfma_f32_16x16x32_bf16 v[24:27], v[120:123], a[12:15], v[24:27]\n\t"
      "v_mfma_f32_16x16x32_bf16 v[24:27], v[136:139], a[16:19], v[24:27]\n\t"
      "s_waitcnt vmcnt(12)\n\t"
      "s_waitcnt lgkmcnt(0)\n\t"
      "s_barrier\n\t"
      "v_mfma_f32_16x16x32_bf16 v[24:27], v[152:155], a[20:23], v[24:27]\n\t"
      "v_mfma_f32_16x16x32_bf16 v[28:31], v[76:79], a[0:3], 0\n\t"
      "ds_read_b128 a[48:51], v8 offset:0\n\t"
      "v_mfma_f32_16x16x32_bf16 v[28:31], v[92:95], a[4:7], v[28:31]\n\t"
      "ds_read_b128 a[52:55], v60 offset:1024\n\t"
      "v_mfma_f32_16x16x32_bf16 v[28:31], v[108:111], a[8:11], v[28:31]\n\t"
      "ds_read_b128 a[56:59], v61 offset:2048\n\t"
      "v_mfma_f32_16x16x32_bf16 v[28:31], v[124:127], a[12:15], v[28:31]\n\t"
      "v_mfma_f32_16x16x32_bf16 v[28:31], v[140:143], a[16:19], v[28:31]\n\t"
      "v_mfma_f32_16x16x32_bf16 v[28:31], v[156:159], a[20:23], v[28:31]\n\t"
      "v_mfma_f32_16x16x32_bf16 v[32:35], a[60:63], a[0:3], 0\n\t"
      "v_mfma_f32_16x16x32_bf16 v[32:35], a[68:71], a[4:7], v[32:35]\n\t"
      "s_waitcnt lgkmcnt(2)\n\t"
      "v_mfma_f32_16x16x32_bf16 v[32:35], a[76:79], a[8:11], v[32:35]\n\t"
      "buffer_store_dwordx4 a[48:51], v9, s[44:47], s49 offen sc1\n\t"
      "v_mfma_f32_16x16x32_bf16 v[32:35], a[84:87], a[12:15], v[32:35]\n\t"
      "v_mfma_f32_16x16x32_bf16 v[32:35], a[92:95], a[16:19], v[32:35]\n\t"
      "v_mfma_f32_16x16x32_bf16 v[32:35], a[100:103], a[20:23], v[32:35]\n\t"
      "s_add_u32 m0, s18, s50\n\t"
      "v_mfma_f32_16x16x32_bf16 v[36:39], a[64:67], a[0:3], 0\n\t"
      "buffer_load_dwordx4 v5, s[40:43], s48 offen nt lds\n\t"
      "v_mfma_f32_16x16x32_bf16 v[36:39], a[72:75], a[4:7], v[36:39]\n\t"
      "v_mfma_f32_16x16x32_bf16 v[36:39], a[80:83], a[8:11], v[36:39]\n\t"
      "v_mfma_f32_16x16x32_bf16 v[36:39], a[88:91], a[12:15], v[36:39]\n\t"
      "s_waitcnt lgkmcnt(1)\n\t"
      "v_mfma_f32_16x16x32_bf16 v[36:39], a[96:99], a[16:19], v[36:39]\n\t"
      "buffer_store_dwordx4 a[52:55], v10, s[44:47], s49 offen offset:1024 sc1\n\t"
      "v_mfma_f32_16x16x32_bf16 v[36:39], a[104:107], a[20:23], v[36:39]\n\t"
      "v_mfma_f32_16x16x32_bf16 v[16:19], v[160:163], a[24:27], v[16:19]\n\t"
      "ds_read_b128 a[0:3], v56 offset:0\n\t"
      "v_mfma_f32_16x16x32_bf16 v[16:19], v[176:179], a[28:31], v[16:19]\n\t"
      "ds_read_b128 a[4:7], v57 offset:0\n\t"
      "v_mfma_f32_16x16x32_bf16 v[16:19], v[192:195], a[32:35], v[16:19]\n\t"
      "ds_read_b128 a[8:11], v58 offset:0\n\t"
      "v_mfma_f32_16x16x32_bf16 v[16:19], v[208:211], a[36:39], v[16:19]\n\t"
      "ds_read_b128 a[12:15], v59 offset:0\n\t"
      "v_mfma_f32_16x16x32_bf16 v[16:19], v[224:227], a[40:43], v[16:19]\n\t"
      "ds_read_b128 a[16:19], v56 offset:256\n\t"
      "v_mfma_f32_16x16x32_bf16 v[16:19], v[240:243], a[44:47], v[16:19]\n\t"
      "ds_read_b128 a[20:23], v57 offset:256\n\t"
      "v_mfma_f32_16x16x32_bf16 v[20:23], v[164:167], a[24:27], v[20:23]\n\t"
      "s_add_u32 s51, s51, 12288\n\t"
      "s_cmp_eq_u32 s51, 160256\n\t"
      "s_cselect_b32 s51, 25088, s51\n\t"
      "v_mfma_f32_16x16x32_bf16 v[20:23], v[180:183], a[28:31], v[20:23]\n\t"
      "s_waitcnt lgkmcnt(6)\n\t"
      "v_mfma_f32_16x16x32_bf16 v[20:23], v[196:199], a[32:35], v[20:23]\n\t"
      "buffer_store_dwordx4 a[56:59], v11, s[44:47], s49 offen offset:2048 sc1\n\t"
      "v_mfma_f32_16x16x32_bf16 v[20:23], v[212:215], a[36:39], v[20:23]\n\t"
      "v_mfma_f32_16x16x32_bf16 v[20:23], v[228:231], a[40:43], v[20:23]\n\t"
      "s_waitcnt lgkmcnt(0)\n\t"
      "v_mfma_f32_16x16x32_bf16 v[20:23], v[244:247], a[44:47], v[20:23]\n\t"
      "v_mfma_f32_16x16x32_bf16 v[24:27], v[168:171], a[24:27], v[24:27]\n\t"
      "v_mfma_f32_16x16x32_bf16 v[24:27], v[184:187], a[28:31], v[24:27]\n\t"
      "v_cvt_pk_bf16_f32 v40, v16, v17\n\t"
      "v_cvt_pk_bf16_f32 v41, v18, v19\n\t"
      "v_mfma_f32_16x16x32_bf16 v[24:27], v[200:203], a[32:35], v[24:27]\n\t"
      "ds_write_b64 v0, v[40:41] offset:12544\n\t"
      "v_mfma_f32_16x16x32_bf16 v[24:27], v[216:219], a[36:39], v[24:27]\n\t"
      "buffer_load_dwordx4 v6, s[40:43], s48 offen offset:1024 nt lds\n\t"
      "v_mfma_f32_16x16x32_bf16 v[24:27], v[232:235], a[40:43], v[24:27]\n\t"
      "v_mfma_f32_16x16x32_bf16 v[24:27], v[248:251], a[44:47], v[24:27]\n\t"
      "v_mfma_f32_16x16x32_bf16 v[28:31], v[172:175], a[24:27], v[28:31]\n\t"
      "v_mfma_f32_16x16x32_bf16 v[28:31], v[188:191], a[28:31], v[28:31]\n\t"
      "v_cvt_pk_bf16_f32 v42, v20, v21\n\t"
      "v_cvt_pk_bf16_f32 v43, v22, v23\n\t"
      "v_mfma_f32_16x16x32_bf16 v[28:31], v[204:207], a[32:35], v[28:31]\n\t"
      "ds_write_b64 v0, v[42:43] offset:12576\n\t"
      "v_mfma_f32_16x16x32_bf16 v[28:31], v[220:223], a[36:39], v[28:31]\n\t"
      "v_mfma_f32_16x16x32_bf16 v[28:31], v[236:239], a[40:43], v[28:31]\n\t"
      "v_mfma_f32_16x16x32_bf16 v[28:31], v[252:255], a[44:47], v[28:31]\n\t"
      "v_mfma_f32_16x16x32_bf16 v[32:35], a[108:111], a[24:27], v[32:35]\n\t"
      "v_mfma_f32_16x16x32_bf16 v[32:35], a[116:119], a[28:31], v[32:35]\n\t"
      "v_cvt_pk_bf16_f32 v44, v24, v25\n\t"
      "v_cvt_pk_bf16_f32 v45, v26, v27\n\t"
      "v_mfma_f32_16x16x32_bf16 v[32:35], a[124:127], a[32:35], v[32:35]\n\t"
      "ds_write_b64 v0, v[44:45] offset:12608\n\t"
      "v_mfma_f32_16x16x32_bf16 v[32:35], a[132:135], a[36:39], v[32:35]\n\t"
      "buffer_load_dwordx4 v7, s[40:43], s48 offen offset:2048 nt lds\n\t"
      "v_mfma_f32_16x16x32_bf16 v[32:35], a[140:143], a[40:43], v[32:35]\n\t"
      "s_add_u32 s50, s50, 12288\n\t"
      "v_mfma_f32_16x16x32_bf16 v[32:35], a[148:151], a[44:47], v[32:35]\n\t"
      "v_cvt_pk_bf16_f32 v46, v28, v29\n\t"
      "v_cvt_pk_bf16_f32 v47, v30, v31\n\t"
      "v_mfma_f32_16x16x32_bf16 v[36:39], a[112:115], a[24:27], v[36:39]\n\t"
      "ds_write_b64 v0, v[46:47] offset:12640\n\t"
      "v_mfma_f32_16x16x32_bf16 v[36:39], a[120:123], a[28:31], v[36:39]\n\t"
      "s_cmp_eq_u32 s50, 160256\n\t"
      "s_cselect_b32 s50, 25088, s50\n\t"
      "v_mfma_f32_16x16x32_bf16 v[36:39], a[128:131], a[32:35], v[36:39]\n\t"
      "s_sub_u32 s52, s52, 1\n\t"
      "s_cmp_lg_u32 s52, 0\n\t"
      "v_mfma_f32_16x16x32_bf16 v[36:39], a[136:139], a[36:39], v[36:39]\n\t"
      "v_mfma_f32_16x16x32_bf16 v[36:39], a[144:147], a[40:43], v[36:39]\n\t"
      "v_cvt_pk_bf16_f32 v48, v32, v33\n\t"
      "v_cvt_pk_bf16_f32 v49, v34, v35\n\t"
      "v_mfma_f32_16x16x32_bf16 v[36:39], a[152:155], a[44:47], v[36:39]\n\t"
      "ds_write_b64 v0, v[48:49] offset:12672\n\t"
      "s_cbranch_scc1 .Lloop2_%=\n\t"
      "s_branch .Lendc_%=\n\t"
      ".Lloop3_%=:\n\t"
      "v_mfma_f32_16x16x32_bf16 v[16:19], v[64:67], a[0:3], 0\n\t"
      "ds_read_b128 a[24:27], v58 offset:256\n\t"
      "v_mfma_f32_16x16x32_bf16 v[16:19], v[80:83], a[4:7], v[16:19]\n\t"
      "ds_read_b128 a[28:31], v59 offset:256\n\t"
      "v_mfma_f32_16x16x32_bf16 v[16:19], v[96:99], a[8:11], v[16:19]\n\t"
      "ds_read_b128 a[32:35], v56 offset:512\n\t"
      "v_mfma_f32_16x16x32_bf16 v[16:19], v[112:115], a[12:15], v[16:19]\n\t"
      "ds_read_b128 a[36:39], v57 offset:512\n\t"
      "v_mfma_f32_16x16x32_bf16 v[16:19], v[128:131], a[16:19], v[16:19]\n\t"
      "ds_read_b128 a[40:43], v58 offset:512\n\t"
      "v_mfma_f32_16x16x32_bf16 v[16:19], v[144:147], a[20:23], v[16:19]\n\t"
      "ds_read_b128 a[44:47], v59 offset:512\n\t"
      "v_mfma_f32_16x16x32_bf16 v[20:23], v[68:71], a[0:3], 0\n\t"
      "v_cvt_pk_bf16_f32 v50, v36, v37\n\t"
      "v_cvt_pk_bf16_f32 v51, v38, v39\n\t"
      "v_mfma_f32_16x16x32_bf16 v[20:23], v[84:87], a[4:7], v[20:23]\n\t"
      "ds_write_b64 v0, v[50:51] offset:12704\n\t"
      "v_mfma_f32_16x16x32_bf16 v[20:23], v[100:103], a[8:11], v[20:23]\n\t"
      "v_add_u32 v52, s51, v15\n\t"
      "v_xor_b32 v53, 64, v52\n\t"
      "v_mfma_f32_16x16x32_bf16 v[20:23], v[116:119], a[12:15], v[20:23]\n\t"
      "v_xor_b32 v54, 128, v52\n\t"
      "v_xor_b32 v55, 192, v52\n\t"
      "v_mfma_f32_16x16x32_bf16 v[20:23], v[132:135], a[16:19], v[20:23]\n\t"
      "s_add_u32 s56, s56, 786432\n\t"
      "s_lshr_b32 s57, s57, 1\n\t"
      "v_mfma_f32_16x16x32_bf16 v[20:23], v[148:151], a[20:23], v[20:23]\n\t"
      "s_bitcmp1_b32 s57, 0\n\t"
      "s_cselect_b32 s48, 24576, 0\n\t"
      "s_add_u32 s48, s48, s56\n\t"
      "v_mfma_f32_16x16x32_bf16 v[24:27], v[72:75], a[0:3], 0\n\t"
      "v_mfma_f32_16x16x32_bf16 v[24:27], v[88:91], a[4:7], v[24:27]\n\t"
      "v_mfma_f32_16x16x32_bf16 v[24:27], v[104:107], a[8:11], v[24:27]\n\t"
      "v_mfma_f32_16x16x32_bf16 v[24:27], v[120:123], a[12:15], v[24:27]\n\t"
      "v_mfma_f32_16x16x32_bf16 v[24:27], v[136:139], a[16:19], v[24:27]\n\t"
      "v_mfma_f32_16x16x32_bf16 v[24:27], v[152:155], a[20:23], v[24:27]\n\t"
      "s_waitcnt vmcnt(12)\n\t"
      "s_waitcnt lgkmcnt(0)\n\t"
      "s_barrier\n\t"
      "v_mfma_f32_16x16x32_bf16 v[28:31], v[76:79], a[0:3], 0\n\t"
      "ds_read_b128 a[48:51], v8 offset:12544\n\t"
      "v_mfma_f32_16x16x32_bf16 v[28:31], v[92:95], a[4:7], v[28:31]\n\t"
      "ds_read_b128 a[52:55], v60 offset:13568\n\t"
      "v_mfma_f32_16x16x32_bf16 v[28:31], v[108:111], a[8:11], v[28:31]\n\t"
      "ds_read_b128 a[56:59], v61 offset:14592\n\t"
      "v_mfma_f32_16x16x32_bf16 v[28:31], v[124:127], a[12:15], v[28:31]\n\t"
      "v_mfma_f32_16x16x32_bf16 v[28:31], v[140:143], a[16:19], v[28:31]\n\t"
      "v_mfma_f32_16x16x32_bf16 v[28:31], v[156:159], a[20:23], v[28:31]\n\t"
      "v_mfma_f32_16x16x32_bf16 v[32:35], a[60:63], a[0:3], 0\n\t"
      "v_mfma_f32_16x16x32_bf16 v[32:35], a[68:71], a[4:7], v[32:35]\n\t"
      "s_waitcnt lgkmcnt(2)\n\t"
      "v_mfma_f32_16x16x32_bf16 v[32:35], a[76:79], a[8:11], v[32:35]\n\t"
      "buffer_store_dwordx4 a[48:51], v12, s[44:47], s49 offen sc1\n\t"
      "v_mfma_f32_16x16x32_bf16 v[32:35], a[84:87], a[12:15], v[32:35]\n\t"
      "v_mfma_f32_16x16x32_bf16 v[32:35], a[92:95], a[16:19], v[32:35]\n\t"
      "v_mfma_f32_16x16x32_bf16 v[32:35], a[100:103], a[20:23], v[32:35]\n\t"
      "s_add_u32 m0, s18, s50\n\t"
      "v_mfma_f32_16x16x32_bf16 v[36:39], a[64:67], a[0:3], 0\n\t"
      "buffer_load_dwordx4 v2, s[40:43], s48 offen nt lds\n\t"
      "v_mfma_f32_16x16x32_bf16 v[36:39], a[72:75], a[4:7], v[36:39]\n\t"
      "v_mfma_f32_16x16x32_bf16 v[36:39], a[80:83], a[8:11], v[36:39]\n\t"
      "v_mfma_f32_16x16x32_bf16 v[36:39], a[88:91], a[12:15], v[36:39]\n\t"
      "s_waitcnt lgkmcnt(1)\n\t"
      "v_mfma_f32_16x16x32_bf16 v[36:39], a[96:99], a[16:19], v[36:39]\n\t"
      "buffer_store_dwordx4 a[52:55], v13, s[44:47], s49 offen offset:1024 sc1\n\t"
      "v_mfma_f32_16x16x32_bf16 v[36:39], a[104:107], a[20:23], v[36:39]\n\t"
      "v_mfma_f32_16x16x32_bf16 v[16:19], v[160:163], a[24:27], v[16:19]\n\t"
      "ds_read_b128 a[0:3], v52 offset:0\n\t"
      "v_mfma_f32_16x16x32_bf16 v[16:19], v[176:179], a[28:31], v[16:19]\n\t"
      "ds_read_b128 a[4:7], v53 offset:0\n\t"
      "v_mfma_f32_16x16x32_bf16 v[16:19], v[192:195], a[32:35], v[16:19]\n\t"
      "ds_read_b128 a[8:11], v54 offset:0\n\t"
      "v_mfma_f32_16x16x32_bf16 v[16:19], v[208:211], a[36:39], v[16:19]\n\t"
      "ds_read_b128 a[12:15], v55 offset:0\n\t"
      "v_mfma_f32_16x16x32_bf16 v[16:19], v[224:227], a[40:43], v[16:19]\n\t"
      "ds_read_b128 a[16:19], v52 offset:256\n\t"
      "v_mfma_f32_16x16x32_bf16 v[16:19], v[240:243], a[44:47], v[16:19]\n\t"
      "ds_read_b128 a[20:23], v53 offset:256\n\t"
      "v_mfma_f32_16x16x32_bf16 v[20:23], v[164:167], a[24:27], v[20:23]\n\t"
      "s_add_u32 s51, s51, 12288\n\t"
      "s_cmp_eq_u32 s51, 160256\n\t"
      "s_cselect_b32 s51, 25088, s51\n\t"
      "v_mfma_f32_16x16x32_bf16 v[20:23], v[180:183], a[28:31], v[20:23]\n\t"
      "s_waitcnt lgkmcnt(6)\n\t"
      "v_mfma_f32_16x16x32_bf16 v[20:23], v[196:199], a[32:35], v[20:23]\n\t"
      "buffer_store_dwordx4 a[56:59], v14, s[44:47], s49 offen offset:2048 sc1\n\t"
      "v_mfma_f32_16x16x32_bf16 v[20:23], v[212:215], a[36:39], v[20:23]\n\t"
      "v_mfma_f32_16x16x32_bf16 v[20:23], v[228:231], a[40:43], v[20:23]\n\t"
      "s_waitcnt lgkmcnt(0)\n\t"
      "v_mfma_f32_16x16x32_bf16 v[20:23], v[244:247], a[44:47], v[20:23]\n\t"
      "v_mfma_f32_16x16x32_bf16 v[24:27], v[168:171], a[24:27], v[24:27]\n\t"
      "v_mfma_f32_16x16x32_bf16 v[24:27], v[184:187], a[28:31], v[24:27]\n\t"
      "v_cvt_pk_bf16_f32 v40, v16, v17\n\t"
      "v_cvt_pk_bf16_f32 v41, v18, v19\n\t"
      "v_mfma_f32_16x16x32_bf16 v[24:27], v[200:203], a[32:35], v[24:27]\n\t"
      "ds_write_b64 v0, v[40:41] offset:0\n\t"
      "v_mfma_f32_16x16x32_bf16 v[24:27], v[216:219], a[36:39], v[24:27]\n\t"
      "buffer_load_dwordx4 v3, s[40:43], s48 offen offset:1024 nt lds\n\t"
      "v_mfma_f32_16x16x32_bf16 v[24:27], v[232:235], a[40:43], v[24:27]\n\t"
      "s_add_u32 s58, s58, 786432\n\t"
      "s_lshr_b32 s59, s59, 1\n\t"
      "v_mfma_f32_16x16x32_bf16 v[24:27], v[248:251], a[44:47], v[24:27]\n\t"
      "s_bitcmp1_b32 s59, 0\n\t"
      "s_cselect_b32 s49, 24576, 0\n\t"
      "s_add_u32 s49, s49, s58\n\t"
      "v_mfma_f32_16x16x32_bf16 v[28:31], v[172:175], a[24:27], v[28:31]\n\t"
      "v_mfma_f32_16x16x32_bf16 v[28:31], v[188:191], a[28:31], v[28:31]\n\t"
      "v_cvt_pk_bf16_f32 v42, v20, v21\n\t"
      "v_cvt_pk_bf16_f32 v43, v22, v23\n\t"
      "v_mfma_f32_16x16x32_bf16 v[28:31], v[204:207], a[32:35], v[28:31]\n\t"
      "ds_write_b64 v0, v[42:43] offset:32\n\t"
      "v_mfma_f32_16x16x32_bf16 v[28:31], v[220:223], a[36:39], v[28:31]\n\t"
      "v_mfma_f32_16x16x32_bf16 v[28:31], v[236:239], a[40:43], v[28:31]\n\t"
      "v_mfma_f32_16x16x32_bf16 v[28:31], v[252:255], a[44:47], v[28:31]\n\t"
      "v_mfma_f32_16x16x32_bf16 v[32:35], a[108:111], a[24:27], v[32:35]\n\t"
      "v_mfma_f32_16x16x32_bf16 v[32:35], a[116:119], a[28:31], v[32:35]\n\t"
      "v_cvt_pk_bf16_f32 v44, v24, v25\n\t"
      "v_cvt_pk_bf16_f32 v45, v26, v27\n\t"
      "v_mfma_f32_16x16x32_bf16 v[32:35], a[124:127], a[32:35], v[32:35]\n\t"
      "ds_write_b64 v0, v[44:45] offset:64\n\t"
      "v_mfma_f32_16x16x32_bf16 v[32:35], a[132:135], a[36:39], v[32:35]\n\t"
      "buffer_load_dwordx4 v4, s[40:43], s48 offen offset:2048 nt lds\n\t"
      "v_mfma_f32_16x16x32_bf16 v[32:35], a[140:143], a[40:43], v[32:35]\n\t"
      "s_add_u32 s50, s50, 12288\n\t"
      "v_mfma_f32_16x16x32_bf16 v[32:35], a[148:151], a[44:47], v[32:35]\n\t"
      "v_cvt_pk_bf16_f32 v46, v28, v29\n\t"
      "v_cvt_pk_bf16_f32 v47, v30, v31\n\t"
      "v_mfma_f32_16x16x32_bf16 v[36:39], a[112:115], a[24:27], v[36:39]\n\t"
      "ds_write_b64 v0, v[46:47] offset:96\n\t"
      "v_mfma_f32_16x16x32_bf16 v[36:39], a[120:123], a[28:31], v[36:39]\n\t"
      "s_cmp_eq_u32 s50, 160256\n\t"
      "s_cselect_b32 s50, 25088, s50\n\t"
      "v_mfma_f32_16x16x32_bf16 v[36:39], a[128:131], a[32:35], v[36:39]\n\t"
      "v_mfma_f32_16x16x32_bf16 v[36:39], a[136:139], a[36:39], v[36:39]\n\t"
      "v_mfma_f32_16x16x32_bf16 v[36:39], a[144:147], a[40:43], v[36:39]\n\t"
      "v_cvt_pk_bf16_f32 v48, v32, v33\n\t"
      "v_cvt_pk_bf16_f32 v49, v34, v35\n\t"
      "v_mfma_f32_16x16x32_bf16 v[36:39], a[152:155], a[44:47], v[36:39]\n\t"
      "ds_write_b64 v0, v[48:49] offset:128\n\t"
      ".Lmid3_%=:\n\t"
      "v_mfma_f32_16x16x32_bf16 v[16:19], v[64:67], a[0:3], 0\n\t"
      "ds_read_b128 a[24:27], v54 offset:256\n\t"
      "v_mfma_f32_16x16x32_bf16 v[16:19], v[80:83], a[4:7], v[16:19]\n\t"
      "ds_read_b128 a[28:31], v55 offset:256\n\t"
      "v_mfma_f32_16x16x32_bf16 v[16:19], v[96:99], a[8:11], v[16:19]\n\t"
      "ds_read_b128 a[32:35], v52 offset:512\n\t"
      "v_mfma_f32_16x16x32_bf16 v[16:19], v[112:115], a[12:15], v[16:19]\n\t"
      "ds_read_b128 a[36:39], v53 offset:512\n\t"
      "v_mfma_f32_16x16x32_bf16 v[16:19], v[128:131], a[16:19], v[16:19]\n\t"
      "ds_read_b128 a[40:43], v54 offset:512\n\t"
      "v_mfma_f32_16x16x32_bf16 v[16:19], v[144:147], a[20:23], v[16:19]\n\t"
      "ds_read_b128 a[44:47], v55 offset:512\n\t"
      "v_mfma_f32_16x16x32_bf16 v[20:23], v[68:71], a[0:3], 0\n\t"
      "v_cvt_pk_bf16_f32 v50, v36, v37\n\t"
      "v_cvt_pk_bf16_f32 v51, v38, v39\n\t"
      "v_mfma_f32_16x16x32_bf16 v[20:23], v[84:87], a[4:7], v[20:23]\n\t"
      "ds_write_b64 v0, v[50:51] offset:160\n\t"
      "v_mfma_f32_16x16x32_bf16 v[20:23], v[100:103], a[8:11], v[20:23]\n\t"
      "v_add_u32 v56, s51, v15\n\t"
      "v_xor_b32 v57, 64, v56\n\t"
      "v_mfma_f32_16x16x32_bf16 v[20:23], v[116:119], a[12:15], v[20:23]\n\t"
      "v_xor_b32 v58, 128, v56\n\t"
      "v_xor_b32 v59, 192, v56\n\t"
      "v_mfma_f32_16x16x32_bf16 v[20:23], v[132:135], a[16:19], v[20:23]\n\t"
      "v_mfma_f32_16x16x32_bf16 v[20:23], v[148:151], a[20:23], v[20:23]\n\t"
      "v_mfma_f32_16x16x32_bf16 v[24:27], v[72:75], a[0:3], 0\n\t"
      "v_mfma_f32_16x16x32_bf16 v[24:27], v[88:91], a[4:7], v[24:27]\n\t"
      "v_mfma_f32_16x16x32_bf16 v[24:27], v[104:107], a[8:11], v[24:27]\n\t"
      "v_mfma_f32_16x16x32_bf16 v[24:27], v[120:123], a[12:15], v[24:27]\n\t"
      "v_mfma_f32_16x16x32_bf16 v[24:27], v[136:139], a[16:19], v[24:27]\n\t"
      "v_mfma_f32_16x16x32_bf16 v[24:27], v[152:155], a[20:23], v[24:27]\n\t"
      "s_waitcnt vmcnt(12)\n\t"
      "s_waitcnt lgkmcnt(0)\n\t"
      "s_barrier\n\t"
      "v_mfma_f32_16x16x32_bf16 v[28:31], v[76:79], a[0:3], 0\n\t"
      "ds_read_b128 a[48:51], v8 offset:0\n\t"
      "v_mfma_f32_16x16x32_bf16 v[28:31], v[92:95], a[4:7], v[28:31]\n\t"
      "ds_read_b128 a[52:55], v60 offset:1024\n\t"
      "v_mfma_f32_16x16x32_bf16 v[28:31], v[108:111], a[8:11], v[28:31]\n\t"
      "ds_read_b128 a[56:59], v61 offset:2048\n\t"
      "v_mfma_f32_16x16x32_bf16 v[28:31], v[124:127], a[12:15], v[28:31]\n\t"
      "v_mfma_f32_16x16x32_bf16 v[28:31], v[140:143], a[16:19], v[28:31]\n\t"
      "v_mfma_f32_16x16x32_bf16 v[28:31], v[156:159], a[20:23], v[28:31]\n\t"
      "v_mfma_f32_16x16x32_bf16 v[32:35], a[60:63], a[0:3], 0\n\t"
      "v_mfma_f32_16x16x32_bf16 v[32:35], a[68:71], a[4:7], v[32:35]\n\t"
      "s_waitcnt lgkmcnt(2)\n\t"
      "v_mfma_f32_16x16x32_bf16 v[32:35], a[76:79], a[8:11], v[32:35]\n\t"
      "buffer_store_dwordx4 a[48:51], v9, s[44:47], s49 offen sc1\n\t"
      "v_mfma_f32_16x16x32_bf16 v[32:35], a[84:87], a[12:15], v[32:35]\n\t"
      "v_mfma_f32_16x16x32_bf16 v[32:35], a[92:95], a[16:19], v[32:35]\n\t"
      "v_mfma_f32_16x16x32_bf16 v[32:35], a[100:103], a[20:23], v[32:35]\n\t"
      "s_add_u32 m0, s18, s50\n\t"
      "v_mfma_f32_16x16x32_bf16 v[36:39], a[64:67], a[0:3], 0\n\t"
      "buffer_load_dwordx4 v5, s[40:43], s48 offen nt lds\n\t"
      "v_mfma_f32_16x16x32_bf16 v[36:39], a[72:75], a[4:7], v[36:39]\n\t"
      "v_mfma_f32_16x16x32_bf16 v[36:39], a[80:83], a[8:11], v[36:39]\n\t"
      "v_mfma_f32_16x16x32_bf16 v[36:39], a[88:91], a[12:15], v[36:39]\n\t"
      "s_waitcnt lgkmcnt(1)\n\t"
      "v_mfma_f32_16x16x32_bf16 v[36:39], a[96:99], a[16:19], v[36:39]\n\t"
      "buffer_store_dwordx4 a[52:55], v10, s[44:47], s49 offen offset:1024 sc1\n\t"
      "v_mfma_f32_16x16x32_bf16 v[36:39], a[104:107], a[20:23], v[36:39]\n\t"
      "v_mfma_f32_16x16x32_bf16 v[16:19], v[160:163], a[24:27], v[16:19]\n\t"
      "ds_read_b128 a[0:3], v56 offset:0\n\t"
      "v_mfma_f32_16x16x32_bf16 v[16:19], v[176:179], a[28:31], v[16:19]\n\t"
      "ds_read_b128 a[4:7], v57 offset:0\n\t"
      "v_mfma_f32_16x16x32_bf16 v[16:19], v[192:195], a[32:35], v[16:19]\n\t"
      "ds_read_b128 a[8:11], v58 offset:0\n\t"
      "v_mfma_f32_16x16x32_bf16 v[16:19], v[208:211], a[36:39], v[16:19]\n\t"
      "ds_read_b128 a[12:15], v59 offset:0\n\t"
      "v_mfma_f32_16x16x32_bf16 v[16:19], v[224:227], a[40:43], v[16:19]\n\t"
      "ds_read_b128 a[16:19], v56 offset:256\n\t"
      "v_mfma_f32_16x16x32_bf16 v[16:19], v[240:243], a[44:47], v[16:19]\n\t"
      "ds_read_b128 a[20:23], v57 offset:256\n\t"
      "v_mfma_f32_16x16x32_bf16 v[20:23], v[164:167], a[24:27], v[20:23]\n\t"
      "s_add_u32 s51, s51, 12288\n\t"
      "s_cmp_eq_u32 s51, 160256\n\t"
      "s_cselect_b32 s51, 25088, s51\n\t"
      "v_mfma_f32_16x16x32_bf16 v[20:23], v[180:183], a[28:31], v[20:23]\n\t"
      "s_waitcnt lgkmcnt(6)\n\t"
      "v_mfma_f32_16x16x32_bf16 v[20:23], v[196:199], a[32:35], v[20:23]\n\t"
      "buffer_store_dwordx4 a[56:59], v11, s[44:47], s49 offen offset:2048 sc1\n\t"
      "v_mfma_f32_16x16x32_bf16 v[20:23], v[212:215], a[36:39], v[20:23]\n\t"
      "v_mfma_f32_16x16x32_bf16 v[20:23], v[228:231], a[40:43], v[20:23]\n\t"
      "s_waitcnt lgkmcnt(0)\n\t"
      "v_mfma_f32_16x16x32_bf16 v[20:23], v[244:247], a[44:47], v[20:23]\n\t"
      "v_mfma_f32_16x16x32_bf16 v[24:27], v[168:171], a[24:27], v[24:27]\n\t"
      "v_mfma_f32_16x16x32_bf16 v[24:27], v[184:187], a[28:31], v[24:27]\n\t"
      "v_cvt_pk_bf16_f32 v40, v16, v17\n\t"
      "v_cvt_pk_bf16_f32 v41, v18, v19\n\t"
      "v_mfma_f32_16x16x32_bf16 v[24:27], v[200:203], a[32:35], v[24:27]\n\t"
      "ds_write_b64 v0, v[40:41] offset:12544\n\t"
      "v_mfma_f32_16x16x32_bf16 v[24:27], v[216:219], a[36:39], v[24:27]\n\t"
      "buffer_load_dwordx4 v6, s[40:43], s48 offen offset:1024 nt lds\n\t"
      "v_mfma_f32_16x16x32_bf16 v[24:27], v[232:235], a[40:43], v[24:27]\n\t"
      "v_mfma_f32_16x16x32_bf16 v[24:27], v[248:251], a[44:47], v[24:27]\n\t"
      "v_mfma_f32_16x16x32_bf16 v[28:31], v[172:175], a[24:27], v[28:31]\n\t"
      "v_mfma_f32_16x16x32_bf16 v[28:31], v[188:191], a[28:31], v[28:31]\n\t"
      "v_cvt_pk_bf16_f32 v42, v20, v21\n\t"
      "v_cvt_pk_bf16_f32 v43, v22, v23\n\t"
      "v_mfma_f32_16x16x32_bf16 v[28:31], v[204:207], a[32:35], v[28:31]\n\t"
      "ds_write_b64 v0, v[42:43] offset:12576\n\t"
      "v_mfma_f32_16x16x32_bf16 v[28:31], v[220:223], a[36:39], v[28:31]\n\t"
      "v_mfma_f32_16x16x32_bf16 v[28:31], v[236:239], a[40:43], v[28:31]\n\t"
      "v_mfma_f32_16x16x32_bf16 v[28:31], v[252:255], a[44:47], v[28:31]\n\t"
      "v_mfma_f32_16x16x32_bf16 v[32:35], a[108:111], a[24:27], v[32:35]\n\t"
      "v_mfma_f32_16x16x32_bf16 v[32:35], a[116:119], a[28:31], v[32:35]\n\t"
      "v_cvt_pk_bf16_f32 v44, v24, v25\n\t"
      "v_cvt_pk_bf16_f32 v45, v26, v27\n\t"
      "v_mfma_f32_16x16x32_bf16 v[32:35], a[124:127], a[32:35], v[32:35]\n\t"
      "ds_write_b64 v0, v[44:45] offset:12608\n\t"
      "v_mfma_f32_16x16x32_bf16 v[32:35], a[132:135], a[36:39], v[32:35]\n\t"
      "buffer_load_dwordx4 v7, s[40:43], s48 offen offset:2048 nt lds\n\t"
      "v_mfma_f32_16x16x32_bf16 v[32:35], a[140:143], a[40:43], v[32:35]\n\t"
      "s_add_u32 s50, s50, 12288\n\t"
      "v_mfma_f32_16x16x32_bf16 v[32:35], a[148:151], a[44:47], v[32:35]\n\t"
      "v_cvt_pk_bf16_f32 v46, v28, v29\n\t"
      "v_cvt_pk_bf16_f32 v47, v30, v31\n\t"
      "v_mfma_f32_16x16x32_bf16 v[36:39], a[112:115], a[24:27], v[36:39]\n\t"
      "ds_write_b64 v0, v[46:47] offset:12640\n\t"
      "v_mfma_f32_16x16x32_bf16 v[36:39], a[120:123], a[28:31], v[36:39]\n\t"
      "s_cmp_eq_u32 s50, 160256\n\t"
      "s_cselect_b32 s50, 25088, s50\n\t"
      "v_mfma_f32_16x16x32_bf16 v[36:39], a[128:131], a[32:35], v[36:39]\n\t"
      "s_sub_u32 s52, s52, 1\n\t"
      "s_cmp_lg_u32 s52, 0\n\t"
      "v_mfma_f32_16x16x32_bf16 v[36:39], a[136:139], a[36:39], v[36:39]\n\t"
      "v_mfma_f32_16x16x32_bf16 v[36:39], a[144:147], a[40:43], v[36:39]\n\t"
      "v_cvt_pk_bf16_f32 v48, v32, v33\n\t"
      "v_cvt_pk_bf16_f32 v49, v34, v35\n\t"
      "v_mfma_f32_16x16x32_bf16 v[36:39], a[152:155], a[44:47], v[36:39]\n\t"
      "ds_write_b64 v0, v[48:49] offset:12672\n\t"
      "s_cbranch_scc1 .Lloop3_%=\n\t"
      ".Lendc_%=:\n\t"
      "s_cmp_eq_u32 s48, 113221632\n\t"
      "s_cbranch_scc1 .Lwrap3_%=\n\t"
      ".Lwrapped3_%=:\n\t"
      "v_mfma_f32_16x16x32_bf16 v[16:19], v[64:67], a[0:3], 0\n\t"
      "ds_read_b128 a[24:27], v58 offset:256\n\t"
      "v_mfma_f32_16x16x32_bf16 v[16:19], v[80:83], a[4:7], v[16:19]\n\t"
      "ds_read_b128 a[28:31], v59 offset:256\n\t"
      "v_mfma_f32_16x16x32_bf16 v[16:19], v[96:99], a[8:11], v[16:19]\n\t"
      "ds_read_b128 a[32:35], v56 offset:512\n\t"
      "v_mfma_f32_16x16x32_bf16 v[16:19], v[112:115], a[12:15], v[16:19]\n\t"
      "ds_read_b128 a[36:39], v57 offset:512\n\t"
      "v_mfma_f32_16x16x32_bf16 v[16:19], v[128:131], a[16:19], v[16:19]\n\t"
      "ds_read_b128 a[40:43], v58 offset:512\n\t"
      "v_mfma_f32_16x16x32_bf16 v[16:19], v[144:147], a[20:23], v[16:19]\n\t"
      "ds_read_b128 a[44:47], v59 offset:512\n\t"
      "v_mfma_f32_16x16x32_bf16 v[20:23], v[68:71], a[0:3], 0\n\t"
      "v_cvt_pk_bf16_f32 v50, v36, v37\n\t"
      "v_cvt_pk_bf16_f32 v51, v38, v39\n\t"
      "v_mfma_f32_16x16x32_bf16 v[20:23], v[84:87], a[4:7], v[20:23]\n\t"
      "ds_write_b64 v0, v[50:51] offset:12704\n\t"
      "v_mfma_f32_16x16x32_bf16 v[20:23], v[100:103], a[8:11], v[20:23]\n\t"
      "v_add_u32 v52, s51, v15\n\t"
      "v_xor_b32 v53, 64, v52\n\t"
      "v_mfma_f32_16x16x32_bf16 v[20:23], v[116:119], a[12:15], v[20:23]\n\t"
      "v_xor_b32 v54, 128, v52\n\t"
      "v_xor_b32 v55, 192, v52\n\t"
      "v_mfma_f32_16x16x32_bf16 v[20:23], v[132:135], a[16:19], v[20:23]\n\t"
      "s_add_u32 s58, s58, 786432\n\t"
      "s_lshr_b32 s59, s59, 1\n\t"
      "v_mfma_f32_16x16x32_bf16 v[20:23], v[148:151], a[20:23], v[20:23]\n\t"
      "s_bitcmp1_b32 s59, 0\n\t"
      "s_cselect_b32 s84, 24576, 0\n\t"
      "s_add_u32 s84, s84, s58\n\t"
      "v_mfma_f32_16x16x32_bf16 v[24:27], v[72:75], a[0:3], 0\n\t"
      "s_add_u32 s58, s58, 786432\n\t"
      "s_lshr_b32 s59, s59, 1\n\t"
      "v_mfma_f32_16x16x32_bf16 v[24:27], v[88:91], a[4:7], v[24:27]\n\t"
      "s_bitcmp1_b32 s59, 0\n\t"
      "s_cselect_b32 s85, 24576, 0\n\t"
      "s_add_u32 s85, s85, s58\n\t"
      "v_mfma_f32_16x16x32_bf16 v[24:27], v[104:107], a[8:11], v[24:27]\n\t"
      "s_waitcnt vmcnt(12)\n\t"
      "s_waitcnt lgkmcnt(0)\n\t"
      "s_barrier\n\t"
      "v_mfma_f32_16x16x32_bf16 v[24:27], v[120:123], a[12:15], v[24:27]\n\t"
      "v_mfma_f32_16x16x32_bf16 v[24:27], v[136:139], a[16:19], v[24:27]\n\t"
      "v_mfma_f32_16x16x32_bf16 v[24:27], v[152:155], a[20:23], v[24:27]\n\t"
      "v_mfma_f32_16x16x32_bf16 v[28:31], v[76:79], a[0:3], 0\n\t"
      "ds_read_b128 a[188:191], v8 offset:12544\n\t"
      "v_mfma_f32_16x16x32_bf16 v[28:31], v[92:95], a[4:7], v[28:31]\n\t"
      "ds_read_b128 a[192:195], v60 offset:13568\n\t"
      "v_mfma_f32_16x16x32_bf16 v[28:31], v[108:111], a[8:11], v[28:31]\n\t"
      "ds_read_b128 a[196:199], v61 offset:14592\n\t"
      "v_mfma_f32_16x16x32_bf16 v[28:31], v[124:127], a[12:15], v[28:31]\n\t"
      "v_mfma_f32_16x16x32_bf16 v[28:31], v[140:143], a[16:19], v[28:31]\n\t"
      "v_mfma_f32_16x16x32_bf16 v[28:31], v[156:159], a[20:23], v[28:31]\n\t"
      "v_mfma_f32_16x16x32_bf16 v[32:35], a[60:63], a[0:3], 0\n\t"
      "v_mfma_f32_16x16x32_bf16 v[32:35], a[68:71], a[4:7], v[32:35]\n\t"
      "v_mfma_f32_16x16x32_bf16 v[32:35], a[76:79], a[8:11], v[32:35]\n\t"
      "v_mfma_f32_16x16x32_bf16 v[32:35], a[84:87], a[12:15], v[32:35]\n\t"
      "v_mfma_f32_16x16x32_bf16 v[32:35], a[92:95], a[16:19], v[32:35]\n\t"
      "v_mfma_f32_16x16x32_bf16 v[32:35], a[100:103], a[20:23], v[32:35]\n\t"
      "v_mfma_f32_16x16x32_bf16 v[36:39], a[64:67], a[0:3], 0\n\t"
      "v_mfma_f32_16x16x32_bf16 v[36:39], a[72:75], a[4:7], v[36:39]\n\t"
      "v_mfma_f32_16x16x32_bf16 v[36:39], a[80:83], a[8:11], v[36:39]\n\t"
      "v_mfma_f32_16x16x32_bf16 v[36:39], a[88:91], a[12:15], v[36:39]\n\t"
      "v_mfma_f32_16x16x32_bf16 v[36:39], a[96:99], a[16:19], v[36:39]\n\t"
      "v_mfma_f32_16x16x32_bf16 v[36:39], a[104:107], a[20:23], v[36:39]\n\t"
      "v_mfma_f32_16x16x32_bf16 v[16:19], v[160:163], a[24:27], v[16:19]\n\t"
      "ds_read_b128 a[0:3], v52 offset:0\n\t"
      "v_mfma_f32_16x16x32_bf16 v[16:19], v[176:179], a[28:31], v[16:19]\n\t"
      "ds_read_b128 a[4:7], v53 offset:0\n\t"
      "v_mfma_f32_16x16x32_bf16 v[16:19], v[192:195], a[32:35], v[16:19]\n\t"
      "ds_read_b128 a[8:11], v54 offset:0\n\t"
      "v_mfma_f32_16x16x32_bf16 v[16:19], v[208:211], a[36:39], v[16:19]\n\t"
      "ds_read_b128 a[12:15], v55 offset:0\n\t"
      "v_mfma_f32_16x16x32_bf16 v[16:19], v[224:227], a[40:43], v[16:19]\n\t"
      "ds_read_b128 a[16:19], v52 offset:256\n\t"
      "v_mfma_f32_16x16x32_bf16 v[16:19], v[240:243], a[44:47], v[16:19]\n\t"
      "ds_read_b128 a[20:23], v53 offset:256\n\t"
      "v_mfma_f32_16x16x32_bf16 v[20:23], v[164:167], a[24:27], v[20:23]\n\t"
      "s_add_u32 s51, s51, 12288\n\t"
      "s_cmp_eq_u32 s51, 160256\n\t"
      "s_cselect_b32 s51, 25088, s51\n\t"
      "v_mfma_f32_16x16x32_bf16 v[20:23], v[180:183], a[28:31], v[20:23]\n\t"
      "v_mfma_f32_16x16x32_bf16 v[20:23], v[196:199], a[32:35], v[20:23]\n\t"
      "v_mfma_f32_16x16x32_bf16 v[20:23], v[212:215], a[36:39], v[20:23]\n\t"
      "v_mfma_f32_16x16x32_bf16 v[20:23], v[228:231], a[40:43], v[20:23]\n\t"
      "s_waitcnt lgkmcnt(0)\n\t"
      "v_mfma_f32_16x16x32_bf16 v[20:23], v[244:247], a[44:47], v[20:23]\n\t"
      "v_mfma_f32_16x16x32_bf16 v[24:27], v[168:171], a[24:27], v[24:27]\n\t"
      "v_mfma_f32_16x16x32_bf16 v[24:27], v[184:187], a[28:31], v[24:27]\n\t"
      "v_cvt_pk_bf16_f32 v40, v16, v17\n\t"
      "v_cvt_pk_bf16_f32 v41, v18, v19\n\t"
      "v_mfma_f32_16x16x32_bf16 v[24:27], v[200:203], a[32:35], v[24:27]\n\t"
      "ds_write_b64 v0, v[40:41] offset:0\n\t"
      "v_mfma_f32_16x16x32_bf16 v[24:27], v[216:219], a[36:39], v[24:27]\n\t"
      "v_mfma_f32_16x16x32_bf16 v[24:27], v[232:235], a[40:43], v[24:27]\n\t"
      "v_mfma_f32_16x16x32_bf16 v[24:27], v[248:251], a[44:47], v[24:27]\n\t"
      "v_mfma_f32_16x16x32_bf16 v[28:31], v[172:175], a[24:27], v[28:31]\n\t"
      "v_mfma_f32_16x16x32_bf16 v[28:31], v[188:191], a[28:31], v[28:31]\n\t"
      "v_cvt_pk_bf16_f32 v42, v20, v21\n\t"
      "v_cvt_pk_bf16_f32 v43, v22, v23\n\t"
      "v_mfma_f32_16x16x32_bf16 v[28:31], v[204:207], a[32:35], v[28:31]\n\t"
      "ds_write_b64 v0, v[42:43] offset:32\n\t"
      "v_mfma_f32_16x16x32_bf16 v[28:31], v[220:223], a[36:39], v[28:31]\n\t"
      "v_mfma_f32_16x16x32_bf16 v[28:31], v[236:239], a[40:43], v[28:31]\n\t"
      "v_mfma_f32_16x16x32_bf16 v[28:31], v[252:255], a[44:47], v[28:31]\n\t"
      "v_mfma_f32_16x16x32_bf16 v[32:35], a[108:111], a[24:27], v[32:35]\n\t"
      "v_mfma_f32_16x16x32_bf16 v[32:35], a[116:119], a[28:31], v[32:35]\n\t"
      "v_cvt_pk_bf16_f32 v44, v24, v25\n\t"
      "v_cvt_pk_bf16_f32 v45, v26, v27\n\t"
      "v_mfma_f32_16x16x32_bf16 v[32:35], a[124:127], a[32:35], v[32:35]\n\t"
      "ds_write_b64 v0, v[44:45] offset:64\n\t"
      "v_mfma_f32_16x16x32_bf16 v[32:35], a[132:135], a[36:39], v[32:35]\n\t"
      "v_mfma_f32_16x16x32_bf16 v[32:35], a[140:143], a[40:43], v[32:35]\n\t"
      "v_mfma_f32_16x16x32_bf16 v[32:35], a[148:151], a[44:47], v[32:35]\n\t"
      "v_cvt_pk_bf16_f32 v46, v28, v29\n\t"
      "v_cvt_pk_bf16_f32 v47, v30, v31\n\t"
      "v_mfma_f32_16x16x32_bf16 v[36:39], a[112:115], a[24:27], v[36:39]\n\t"
      "ds_write_b64 v0, v[46:47] offset:96\n\t"
      "v_mfma_f32_16x16x32_bf16 v[36:39], a[120:123], a[28:31], v[36:39]\n\t"
      "v_mfma_f32_16x16x32_bf16 v[36:39], a[128:131], a[32:35], v[36:39]\n\t"
      "v_mfma_f32_16x16x32_bf16 v[36:39], a[136:139], a[36:39], v[36:39]\n\t"
      "v_mfma_f32_16x16x32_bf16 v[36:39], a[144:147], a[40:43], v[36:39]\n\t"
      "v_cvt_pk_bf16_f32 v48, v32, v33\n\t"
      "v_cvt_pk_bf16_f32 v49, v34, v35\n\t"
      "v_mfma_f32_16x16x32_bf16 v[36:39], a[152:155], a[44:47], v[36:39]\n\t"
      "ds_write_b64 v0, v[48:49] offset:128\n\t"
      "v_mfma_f32_16x16x32_bf16 v[16:19], v[64:67], a[0:3], 0\n\t"
      "ds_read_b128 a[24:27], v54 offset:256\n\t"
      "v_mfma_f32_16x16x32_bf16 v[16:19], v[80:83], a[4:7], v[16:19]\n\t"
      "ds_read_b128 a[28:31], v55 offset:256\n\t"
      "v_mfma_f32_16x16x32_bf16 v[16:19], v[96:99], a[8:11], v[16:19]\n\t"
      "ds_read_b128 a[32:35], v52 offset:512\n\t"
      "v_mfma_f32_16x16x32_bf16 v[16:19], v[112:115], a[12:15], v[16:19]\n\t"
      "ds_read_b128 a[36:39], v53 offset:512\n\t"
      "v_mfma_f32_16x16x32_bf16 v[16:19], v[128:131], a[16:19], v[16:19]\n\t"
      "ds_read_b128 a[40:43], v54 offset:512\n\t"
      "v_mfma_f32_16x16x32_bf16 v[16:19], v[144:147], a[20:23], v[16:19]\n\t"
      "ds_read_b128 a[44:47], v55 offset:512\n\t"
      "v_mfma_f32_16x16x32_bf16 v[20:23], v[68:71], a[0:3], 0\n\t"
      "v_cvt_pk_bf16_f32 v50, v36, v37\n\t"
      "v_cvt_pk_bf16_f32 v51, v38, v39\n\t"
      "v_mfma_f32_16x16x32_bf16 v[20:23], v[84:87], a[4:7], v[20:23]\n\t"
      "ds_write_b64 v0, v[50:51] offset:160\n\t"
      "v_mfma_f32_16x16x32_bf16 v[20:23], v[100:103], a[8:11], v[20:23]\n\t"
      "v_add_u32 v56, s51, v15\n\t"
      "v_xor_b32 v57, 64, v56\n\t"
      "v_mfma_f32_16x16x32_bf16 v[20:23], v[116:119], a[12:15], v[20:23]\n\t"
      "v_xor_b32 v58, 128, v56\n\t"
      "v_xor_b32 v59, 192, v56\n\t"
      "v_mfma_f32_16x16x32_bf16 v[20:23], v[132:135], a[16:19], v[20:23]\n\t"
      "v_mfma_f32_16x16x32_bf16 v[20:23], v[148:151], a[20:23], v[20:23]\n\t"
      "v_mfma_f32_16x16x32_bf16 v[24:27], v[72:75], a[0:3], 0\n\t"
      "v_mfma_f32_16x16x32_bf16 v[24:27], v[88:91], a[4:7], v[24:27]\n\t"
      "v_mfma_f32_16x16x32_bf16 v[24:27], v[104:107], a[8:11], v[24:27]\n\t"
      "s_waitcnt vmcnt(6)\n\t"
      "s_waitcnt lgkmcnt(0)\n\t"
      "s_barrier\n\t"
      "v_mfma_f32_16x16x32_bf16 v[24:27], v[120:123], a[12:15], v[24:27]\n\t"
      "v_mfma_f32_16x16x32_bf16 v[24:27], v[136:139], a[16:19], v[24:27]\n\t"
      "v_mfma_f32_16x16x32_bf16 v[24:27], v[152:155], a[20:23], v[24:27]\n\t"
      "v_mfma_f32_16x16x32_bf16 v[28:31], v[76:79], a[0:3], 0\n\t"
      "ds_read_b128 a[200:203], v8 offset:0\n\t"
      "v_mfma_f32_16x16x32_bf16 v[28:31], v[92:95], a[4:7], v[28:31]\n\t"
      "ds_read_b128 a[204:207], v60 offset:1024\n\t"
      "v_mfma_f32_16x16x32_bf16 v[28:31], v[108:111], a[8:11], v[28:31]\n\t"
      "ds_read_b128 a[208:211], v61 offset:2048\n\t"
      "v_mfma_f32_16x16x32_bf16 v[28:31], v[124:127], a[12:15], v[28:31]\n\t"
      "v_mfma_f32_16x16x32_bf16 v[28:31], v[140:143], a[16:19], v[28:31]\n\t"
      "v_mfma_f32_16x16x32_bf16 v[28:31], v[156:159], a[20:23], v[28:31]\n\t"
      "v_mfma_f32_16x16x32_bf16 v[32:35], a[60:63], a[0:3], 0\n\t"
      "v_mfma_f32_16x16x32_bf16 v[32:35], a[68:71], a[4:7], v[32:35]\n\t"
      "v_mfma_f32_16x16x32_bf16 v[32:35], a[76:79], a[8:11], v[32:35]\n\t"
      "v_mfma_f32_16x16x32_bf16 v[32:35], a[84:87], a[12:15], v[32:35]\n\t"
      "v_mfma_f32_16x16x32_bf16 v[32:35], a[92:95], a[16:19], v[32:35]\n\t"
      "v_mfma_f32_16x16x32_bf16 v[32:35], a[100:103], a[20:23], v[32:35]\n\t"
      "v_mfma_f32_16x16x32_bf16 v[36:39], a[64:67], a[0:3], 0\n\t"
      "v_mfma_f32_16x16x32_bf16 v[36:39], a[72:75], a[4:7], v[36:39]\n\t"
      "v_mfma_f32_16x16x32_bf16 v[36:39], a[80:83], a[8:11], v[36:39]\n\t"
      "v_mfma_f32_16x16x32_bf16 v[36:39], a[88:91], a[12:15], v[36:39]\n\t"
      "v_mfma_f32_16x16x32_bf16 v[36:39], a[96:99], a[16:19], v[36:39]\n\t"
      "v_mfma_f32_16x16x32_bf16 v[36:39], a[104:107], a[20:23], v[36:39]\n\t"
      "v_mfma_f32_16x16x32_bf16 v[16:19], v[160:163], a[24:27], v[16:19]\n\t"
      "ds_read_b128 a[0:3], v56 offset:0\n\t"
      "v_mfma_f32_16x16x32_bf16 v[16:19], v[176:179], a[28:31], v[16:19]\n\t"
      "ds_read_b128 a[4:7], v57 offset:0\n\t"
      "v_mfma_f32_16x16x32_bf16 v[16:19], v[192:195], a[32:35], v[16:19]\n\t"
      "ds_read_b128 a[8:11], v58 offset:0\n\t"
      "v_mfma_f32_16x16x32_bf16 v[16:19], v[208:211], a[36:39], v[16:19]\n\t"
      "ds_read_b128 a[12:15], v59 offset:0\n\t"
      "v_mfma_f32_16x16x32_bf16 v[16:19], v[224:227], a[40:43], v[16:19]\n\t"
      "ds_read_b128 a[16:19], v56 offset:256\n\t"
      "v_mfma_f32_16x16x32_bf16 v[16:19], v[240:243], a[44:47], v[16:19]\n\t"
      "ds_read_b128 a[20:23], v57 offset:256\n\t"
      "v_mfma_f32_16x16x32_bf16 v[20:23], v[164:167], a[24:27], v[20:23]\n\t"
      "s_add_u32 s51, s51, 12288\n\t"
      "s_cmp_eq_u32 s51, 160256\n\t"
      "s_cselect_b32 s51, 25088, s51\n\t"
      "v_mfma_f32_16x16x32_bf16 v[20:23], v[180:183], a[28:31], v[20:23]\n\t"
      "v_mfma_f32_16x16x32_bf16 v[20:23], v[196:199], a[32:35], v[20:23]\n\t"
      "buffer_store_dwordx4 a[188:191], v12, s[44:47], s49 offen sc1\n\t"
      "v_mfma_f32_16x16x32_bf16 v[20:23], v[212:215], a[36:39], v[20:23]\n\t"
      "v_mfma_f32_16x16x32_bf16 v[20:23], v[228:231], a[40:43], v[20:23]\n\t"
      "s_waitcnt lgkmcnt(0)\n\t"
      "v_mfma_f32_16x16x32_bf16 v[20:23], v[244:247], a[44:47], v[20:23]\n\t"
      "v_mfma_f32_16x16x32_bf16 v[24:27], v[168:171], a[24:27], v[24:27]\n\t"
      "v_mfma_f32_16x16x32_bf16 v[24:27], v[184:187], a[28:31], v[24:27]\n\t"
      "v_cvt_pk_bf16_f32 v40, v16, v17\n\t"
      "v_cvt_pk_bf16_f32 v41, v18, v19\n\t"
      "v_mfma_f32_16x16x32_bf16 v[24:27], v[200:203], a[32:35], v[24:27]\n\t"
      "ds_write_b64 v0, v[40:41] offset:12544\n\t"
      "v_mfma_f32_16x16x32_bf16 v[24:27], v[216:219], a[36:39], v[24:27]\n\t"
      "v_mfma_f32_16x16x32_bf16 v[24:27], v[232:235], a[40:43], v[24:27]\n\t"
      "v_mfma_f32_16x16x32_bf16 v[24:27], v[248:251], a[44:47], v[24:27]\n\t"
      "v_mfma_f32_16x16x32_bf16 v[28:31], v[172:175], a[24:27], v[28:31]\n\t"
      "v_mfma_f32_16x16x32_bf16 v[28:31], v[188:191], a[28:31], v[28:31]\n\t"
      "v_cvt_pk_bf16_f32 v42, v20, v21\n\t"
      "v_cvt_pk_bf16_f32 v43, v22, v23\n\t"
      "v_mfma_f32_16x16x32_bf16 v[28:31], v[204:207], a[32:35], v[28:31]\n\t"
      "ds_write_b64 v0, v[42:43] offset:12576\n\t"
      "v_mfma_f32_16x16x32_bf16 v[28:31], v[220:223], a[36:39], v[28:31]\n\t"
      "v_mfma_f32_16x16x32_bf16 v[28:31], v[236:239], a[40:43], v[28:31]\n\t"
      "buffer_store_dwordx4 a[192:195], v13, s[44:47], s49 offen offset:1024 sc1\n\t"
      "v_mfma_f32_16x16x32_bf16 v[28:31], v[252:255], a[44:47], v[28:31]\n\t"
      "v_mfma_f32_16x16x32_bf16 v[32:35], a[108:111], a[24:27], v[32:35]\n\t"
      "v_mfma_f32_16x16x32_bf16 v[32:35], a[116:119], a[28:31], v[32:35]\n\t"
      "v_cvt_pk_bf16_f32 v44, v24, v25\n\t"
      "v_cvt_pk_bf16_f32 v45, v26, v27\n\t"
      "v_mfma_f32_16x16x32_bf16 v[32:35], a[124:127], a[32:35], v[32:35]\n\t"
      "ds_write_b64 v0, v[44:45] offset:12608\n\t"
      "v_mfma_f32_16x16x32_bf16 v[32:35], a[132:135], a[36:39], v[32:35]\n\t"
      "v_mfma_f32_16x16x32_bf16 v[32:35], a[140:143], a[40:43], v[32:35]\n\t"
      "v_mfma_f32_16x16x32_bf16 v[32:35], a[148:151], a[44:47], v[32:35]\n\t"
      "v_cvt_pk_bf16_f32 v46, v28, v29\n\t"
      "v_cvt_pk_bf16_f32 v47, v30, v31\n\t"
      "v_mfma_f32_16x16x32_bf16 v[36:39], a[112:115], a[24:27], v[36:39]\n\t"
      "ds_write_b64 v0, v[46:47] offset:12640\n\t"
      "v_mfma_f32_16x16x32_bf16 v[36:39], a[120:123], a[28:31], v[36:39]\n\t"
      "v_mfma_f32_16x16x32_bf16 v[36:39], a[128:131], a[32:35], v[36:39]\n\t"
      "v_mfma_f32_16x16x32_bf16 v[36:39], a[136:139], a[36:39], v[36:39]\n\t"
      "v_mfma_f32_16x16x32_bf16 v[36:39], a[144:147], a[40:43], v[36:39]\n\t"
      "v_cvt_pk_bf16_f32 v48, v32, v33\n\t"
      "v_cvt_pk_bf16_f32 v49, v34, v35\n\t"
      "v_mfma_f32_16x16x32_bf16 v[36:39], a[152:155], a[44:47], v[36:39]\n\t"
      "ds_write_b64 v0, v[48:49] offset:12672\n\t"
      "v_mfma_f32_16x16x32_bf16 v[16:19], v[64:67], a[0:3], 0\n\t"
      "ds_read_b128 a[24:27], v58 offset:256\n\t"
      "v_mfma_f32_16x16x32_bf16 v[16:19], v[80:83], a[4:7], v[16:19]\n\t"
      "ds_read_b128 a[28:31], v59 offset:256\n\t"
      "v_mfma_f32_16x16x32_bf16 v[16:19], v[96:99], a[8:11], v[16:19]\n\t"
      "ds_read_b128 a[32:35], v56 offset:512\n\t"
      "v_mfma_f32_16x16x32_bf16 v[16:19], v[112:115], a[12:15], v[16:19]\n\t"
      "ds_read_b128 a[36:39], v57 offset:512\n\t"
      "buffer_store_dwordx4 a[196:199], v14, s[44:47], s49 offen offset:2048 sc1\n\t"
      "v_mfma_f32_16x16x32_bf16 v[16:19], v[128:131], a[16:19], v[16:19]\n\t"
      "ds_read_b128 a[40:43], v58 offset:512\n\t"
      "v_mfma_f32_16x16x32_bf16 v[16:19], v[144:147], a[20:23], v[16:19]\n\t"
      "ds_read_b128 a[44:47], v59 offset:512\n\t"
      "v_mfma_f32_16x16x32_bf16 v[20:23], v[68:71], a[0:3], 0\n\t"
      "v_cvt_pk_bf16_f32 v50, v36, v37\n\t"
      "v_cvt_pk_bf16_f32 v51, v38, v39\n\t"
      "v_mfma_f32_16x16x32_bf16 v[20:23], v[84:87], a[4:7], v[20:23]\n\t"
      "ds_write_b64 v0, v[50:51] offset:12704\n\t"
      "v_mfma_f32_16x16x32_bf16 v[20:23], v[100:103], a[8:11], v[20:23]\n\t"
      "v_add_u32 v52, s51, v15\n\t"
      "v_xor_b32 v53, 64, v52\n\t"
      "v_mfma_f32_16x16x32_bf16 v[20:23], v[116:119], a[12:15], v[20:23]\n\t"
      "v_xor_b32 v54, 128, v52\n\t"
      "v_xor_b32 v55, 192, v52\n\t"
      "v_mfma_f32_16x16x32_bf16 v[20:23], v[132:135], a[16:19], v[20:23]\n\t"
      "v_mfma_f32_16x16x32_bf16 v[20:23], v[148:151], a[20:23], v[20:23]\n\t"
      "v_mfma_f32_16x16x32_bf16 v[24:27], v[72:75], a[0:3], 0\n\t"
      "buffer_store_dwordx4 a[200:203], v9, s[44:47], s84 offen sc1\n\t"
      "v_mfma_f32_16x16x32_bf16 v[24:27], v[88:91], a[4:7], v[24:27]\n\t"
      "v_mfma_f32_16x16x32_bf16 v[24:27], v[104:107], a[8:11], v[24:27]\n\t"
      "s_waitcnt vmcnt(4)\n\t"
      "s_waitcnt lgkmcnt(0)\n\t"
      "s_barrier\n\t"
      "v_mfma_f32_16x16x32_bf16 v[24:27], v[120:123], a[12:15], v[24:27]\n\t"
      "v_mfma_f32_16x16x32_bf16 v[24:27], v[136:139], a[16:19], v[24:27]\n\t"
      "v_mfma_f32_16x16x32_bf16 v[24:27], v[152:155], a[20:23], v[24:27]\n\t"
      "v_mfma_f32_16x16x32_bf16 v[28:31], v[76:79], a[0:3], 0\n\t"
      "ds_read_b128 a[212:215], v8 offset:12544\n\t"
      "v_mfma_f32_16x16x32_bf16 v[28:31], v[92:95], a[4:7], v[28:31]\n\t"
      "ds_read_b128 a[216:219], v60 offset:13568\n\t"
      "v_mfma_f32_16x16x32_bf16 v[28:31], v[108:111], a[8:11], v[28:31]\n\t"
      "ds_read_b128 a[220:223], v61 offset:14592\n\t"
      "v_mfma_f32_16x16x32_bf16 v[28:31], v[124:127], a[12:15], v[28:31]\n\t"
      "v_mfma_f32_16x16x32_bf16 v[28:31], v[140:143], a[16:19], v[28:31]\n\t"
      "v_mfma_f32_16x16x32_bf16 v[28:31], v[156:159], a[20:23], v[28:31]\n\t"
      "v_mfma_f32_16x16x32_bf16 v[32:35], a[60:63], a[0:3], 0\n\t"
      "buffer_store_dwordx4 a[204:207], v10, s[44:47], s84 offen offset:1024 sc1\n\t"
      "v_mfma_f32_16x16x32_bf16 v[32:35], a[68:71], a[4:7], v[32:35]\n\t"
      "v_mfma_f32_16x16x32_bf16 v[32:35], a[76:79], a[8:11], v[32:35]\n\t"
      "v_mfma_f32_16x16x32_bf16 v[32:35], a[84:87], a[12:15], v[32:35]\n\t"
      "v_mfma_f32_16x16x32_bf16 v[32:35], a[92:95], a[16:19], v[32:35]\n\t"
      "v_mfma_f32_16x16x32_bf16 v[32:35], a[100:103], a[20:23], v[32:35]\n\t"
      "v_mfma_f32_16x16x32_bf16 v[36:39], a[64:67], a[0:3], 0\n\t"
      "v_mfma_f32_16x16x32_bf16 v[36:39], a[72:75], a[4:7], v[36:39]\n\t"
      "v_mfma_f32_16x16x32_bf16 v[36:39], a[80:83], a[8:11], v[36:39]\n\t"
      "v_mfma_f32_16x16x32_bf16 v[36:39], a[88:91], a[12:15], v[36:39]\n\t"
      "v_mfma_f32_16x16x32_bf16 v[36:39], a[96:99], a[16:19], v[36:39]\n\t"
      "buffer_store_dwordx4 a[208:211], v11, s[44:47], s84 offen offset:2048 sc1\n\t"
      "v_mfma_f32_16x16x32_bf16 v[36:39], a[104:107], a[20:23], v[36:39]\n\t"
      "v_mfma_f32_16x16x32_bf16 v[16:19], v[160:163], a[24:27], v[16:19]\n\t"
      "ds_read_b128 a[0:3], v52 offset:0\n\t"
      "v_mfma_f32_16x16x32_bf16 v[16:19], v[176:179], a[28:31], v[16:19]\n\t"
      "ds_read_b128 a[4:7], v53 offset:0\n\t"
      "v_mfma_f32_16x16x32_bf16 v[16:19], v[192:195], a[32:35], v[16:19]\n\t"
      "ds_read_b128 a[8:11], v54 offset:0\n\t"
      "v_mfma_f32_16x16x32_bf16 v[16:19], v[208:211], a[36:39], v[16:19]\n\t"
      "ds_read_b128 a[12:15], v55 offset:0\n\t"
      "v_mfma_f32_16x16x32_bf16 v[16:19], v[224:227], a[40:43], v[16:19]\n\t"
      "ds_read_b128 a[16:19], v52 offset:256\n\t"
      "v_mfma_f32_16x16x32_bf16 v[16:19], v[240:243], a[44:47], v[16:19]\n\t"
      "ds_read_b128 a[20:23], v53 offset:256\n\t"
      "v_mfma_f32_16x16x32_bf16 v[20:23], v[164:167], a[24:27], v[20:23]\n\t"
      "s_add_u32 s51, s51, 12288\n\t"
      "s_cmp_eq_u32 s51, 160256\n\t"
      "s_cselect_b32 s51, 25088, s51\n\t"
      "v_mfma_f32_16x16x32_bf16 v[20:23], v[180:183], a[28:31], v[20:23]\n\t"
      "s_waitcnt lgkmcnt(8)\n\t"
      "v_mfma_f32_16x16x32_bf16 v[20:23], v[196:199], a[32:35], v[20:23]\n\t"
      "buffer_store_dwordx4 a[212:215], v12, s[44:47], s84 offen sc1\n\t"
      "v_mfma_f32_16x16x32_bf16 v[20:23], v[212:215], a[36:39], v[20:23]\n\t"
      "v_mfma_f32_16x16x32_bf16 v[20:23], v[228:231], a[40:43], v[20:23]\n\t"
      "s_waitcnt lgkmcnt(0)\n\t"
      "v_mfma_f32_16x16x32_bf16 v[20:23], v[244:247], a[44:47], v[20:23]\n\t"
      "v_mfma_f32_16x16x32_bf16 v[24:27], v[168:171], a[24:27], v[24:27]\n\t"
      "v_mfma_f32_16x16x32_bf16 v[24:27], v[184:187], a[28:31], v[24:27]\n\t"
      "v_cvt_pk_bf16_f32 v40, v16, v17\n\t"
      "v_cvt_pk_bf16_f32 v41, v18, v19\n\t"
      "v_mfma_f32_16x16x32_bf16 v[24:27], v[200:203], a[32:35], v[24:27]\n\t"
      "ds_write_b64 v0, v[40:41] offset:0\n\t"
      "v_mfma_f32_16x16x32_bf16 v[24:27], v[216:219], a[36:39], v[24:27]\n\t"
      "v_mfma_f32_16x16x32_bf16 v[24:27], v[232:235], a[40:43], v[24:27]\n\t"
      "v_mfma_f32_16x16x32_bf16 v[24:27], v[248:251], a[44:47], v[24:27]\n\t"
      "v_mfma_f32_16x16x32_bf16 v[28:31], v[172:175], a[24:27], v[28:31]\n\t"
      "v_mfma_f32_16x16x32_bf16 v[28:31], v[188:191], a[28:31], v[28:31]\n\t"
      "v_cvt_pk_bf16_f32 v42, v20, v21\n\t"
      "v_cvt_pk_bf16_f32 v43, v22, v23\n\t"
      "v_mfma_f32_16x16x32_bf16 v[28:31], v[204:207], a[32:35], v[28:31]\n\t"
      "ds_write_b64 v0, v[42:43] offset:32\n\t"
      "v_mfma_f32_16x16x32_bf16 v[28:31], v[220:223], a[36:39], v[28:31]\n\t"
      "v_mfma_f32_16x16x32_bf16 v[28:31], v[236:239], a[40:43], v[28:31]\n\t"
      "buffer_store_dwordx4 a[216:219], v13, s[44:47], s84 offen offset:1024 sc1\n\t"
      "v_mfma_f32_16x16x32_bf16 v[28:31], v[252:255], a[44:47], v[28:31]\n\t"
      "v_mfma_f32_16x16x32_bf16 v[32:35], a[108:111], a[24:27], v[32:35]\n\t"
      "v_mfma_f32_16x16x32_bf16 v[32:35], a[116:119], a[28:31], v[32:35]\n\t"
      "v_cvt_pk_bf16_f32 v44, v24, v25\n\t"
      "v_cvt_pk_bf16_f32 v45, v26, v27\n\t"
      "v_mfma_f32_16x16x32_bf16 v[32:35], a[124:127], a[32:35], v[32:35]\n\t"
      "ds_write_b64 v0, v[44:45] offset:64\n\t"
      "v_mfma_f32_16x16x32_bf16 v[32:35], a[132:135], a[36:39], v[32:35]\n\t"
      "v_mfma_f32_16x16x32_bf16 v[32:35], a[140:143], a[40:43], v[32:35]\n\t"
      "v_mfma_f32_16x16x32_bf16 v[32:35], a[148:151], a[44:47], v[32:35]\n\t"
      "v_cvt_pk_bf16_f32 v46, v28, v29\n\t"
      "v_cvt_pk_bf16_f32 v47, v30, v31\n\t"
      "v_mfma_f32_16x16x32_bf16 v[36:39], a[112:115], a[24:27], v[36:39]\n\t"
      "ds_write_b64 v0, v[46:47] offset:96\n\t"
      "v_mfma_f32_16x16x32_bf16 v[36:39], a[120:123], a[28:31], v[36:39]\n\t"
      "v_mfma_f32_16x16x32_bf16 v[36:39], a[128:131], a[32:35], v[36:39]\n\t"
      "v_mfma_f32_16x16x32_bf16 v[36:39], a[136:139], a[36:39], v[36:39]\n\t"
      "v_mfma_f32_16x16x32_bf16 v[36:39], a[144:147], a[40:43], v[36:39]\n\t"
      "v_cvt_pk_bf16_f32 v48, v32, v33\n\t"
      "v_cvt_pk_bf16_f32 v49, v34, v35\n\t"
      "v_mfma_f32_16x16x32_bf16 v[36:39], a[152:155], a[44:47], v[36:39]\n\t"
      "ds_write_b64 v0, v[48:49] offset:128\n\t"
      "v_mfma_f32_16x16x32_bf16 v[16:19], v[64:67], a[0:3], 0\n\t"
      "ds_read_b128 a[24:27], v54 offset:256\n\t"
      "v_mfma_f32_16x16x32_bf16 v[16:19], v[80:83], a[4:7], v[16:19]\n\t"
      "ds_read_b128 a[28:31], v55 offset:256\n\t"
      "v_mfma_f32_16x16x32_bf16 v[16:19], v[96:99], a[8:11], v[16:19]\n\t"
      "ds_read_b128 a[32:35], v52 offset:512\n\t"
      "v_mfma_f32_16x16x32_bf16 v[16:19], v[112:115], a[12:15], v[16:19]\n\t"
      "ds_read_b128 a[36:39], v53 offset:512\n\t"
      "v_mfma_f32_16x16x32_bf16 v[16:19], v[128:131], a[16:19], v[16:19]\n\t"
      "ds_read_b128 a[40:43], v54 offset:512\n\t"
      "v_mfma_f32_16x16x32_bf16 v[16:19], v[144:147], a[20:23], v[16:19]\n\t"
      "ds_read_b128 a[44:47], v55 offset:512\n\t"
      "v_mfma_f32_16x16x32_bf16 v[20:23], v[68:71], a[0:3], 0\n\t"
      "v_cvt_pk_bf16_f32 v50, v36, v37\n\t"
      "v_cvt_pk_bf16_f32 v51, v38, v39\n\t"
      "v_mfma_f32_16x16x32_bf16 v[20:23], v[84:87], a[4:7], v[20:23]\n\t"
      "ds_write_b64 v0, v[50:51] offset:160\n\t"
      "v_mfma_f32_16x16x32_bf16 v[20:23], v[100:103], a[8:11], v[20:23]\n\t"
      "v_mfma_f32_16x16x32_bf16 v[20:23], v[116:119], a[12:15], v[20:23]\n\t"
      "v_mfma_f32_16x16x32_bf16 v[20:23], v[132:135], a[16:19], v[20:23]\n\t"
      "buffer_store_dwordx4 a[220:223], v14, s[44:47], s84 offen offset:2048 sc1\n\t"
      "v_mfma_f32_16x16x32_bf16 v[20:23], v[148:151], a[20:23], v[20:23]\n\t"
      "v_mfma_f32_16x16x32_bf16 v[24:27], v[72:75], a[0:3], 0\n\t"
      "v_mfma_f32_16x16x32_bf16 v[24:27], v[88:91], a[4:7], v[24:27]\n\t"
      "v_mfma_f32_16x16x32_bf16 v[24:27], v[104:107], a[8:11], v[24:27]\n\t"
      "s_waitcnt lgkmcnt(0)\n\t"
      "s_barrier\n\t"
      "v_mfma_f32_16x16x32_bf16 v[24:27], v[120:123], a[12:15], v[24:27]\n\t"
      "v_mfma_f32_16x16x32_bf16 v[24:27], v[136:139], a[16:19], v[24:27]\n\t"
      "v_mfma_f32_16x16x32_bf16 v[24:27], v[152:155], a[20:23], v[24:27]\n\t"
      "v_mfma_f32_16x16x32_bf16 v[28:31], v[76:79], a[0:3], 0\n\t"
      "ds_read_b128 a[224:227], v8 offset:0\n\t"
      "v_mfma_f32_16x16x32_bf16 v[28:31], v[92:95], a[4:7], v[28:31]\n\t"
      "ds_read_b128 a[228:231], v60 offset:1024\n\t"
      "v_mfma_f32_16x16x32_bf16 v[28:31], v[108:111], a[8:11], v[28:31]\n\t"
      "ds_read_b128 a[232:235], v61 offset:2048\n\t"
      "v_mfma_f32_16x16x32_bf16 v[28:31], v[124:127], a[12:15], v[28:31]\n\t"
      "v_mfma_f32_16x16x32_bf16 v[28:31], v[140:143], a[16:19], v[28:31]\n\t"
      "v_mfma_f32_16x16x32_bf16 v[28:31], v[156:159], a[20:23], v[28:31]\n\t"
      "v_mfma_f32_16x16x32_bf16 v[32:35], a[60:63], a[0:3], 0\n\t"
      "v_mfma_f32_16x16x32_bf16 v[32:35], a[68:71], a[4:7], v[32:35]\n\t"
      "s_waitcnt lgkmcnt(2)\n\t"
      "v_mfma_f32_16x16x32_bf16 v[32:35], a[76:79], a[8:11], v[32:35]\n\t"
      "buffer_store_dwordx4 a[224:227], v9, s[44:47], s85 offen sc1\n\t"
      "v_mfma_f32_16x16x32_bf16 v[32:35], a[84:87], a[12:15], v[32:35]\n\t"
      "v_mfma_f32_16x16x32_bf16 v[32:35], a[92:95], a[16:19], v[32:35]\n\t"
      "v_mfma_f32_16x16x32_bf16 v[32:35], a[100:103], a[20:23], v[32:35]\n\t"
      "v_mfma_f32_16x16x32_bf16 v[36:39], a[64:67], a[0:3], 0\n\t"
      "v_mfma_f32_16x16x32_bf16 v[36:39], a[72:75], a[4:7], v[36:39]\n\t"
      "v_mfma_f32_16x16x32_bf16 v[36:39], a[80:83], a[8:11], v[36:39]\n\t"
      "v_mfma_f32_16x16x32_bf16 v[36:39], a[88:91], a[12:15], v[36:39]\n\t"
      "s_waitcnt lgkmcnt(1)\n\t"
      "v_mfma_f32_16x16x32_bf16 v[36:39], a[96:99], a[16:19], v[36:39]\n\t"
      "buffer_store_dwordx4 a[228:231], v10, s[44:47], s85 offen offset:1024 sc1\n\t"
      "v_mfma_f32_16x16x32_bf16 v[36:39], a[104:107], a[20:23], v[36:39]\n\t"
      "v_mfma_f32_16x16x32_bf16 v[16:19], v[160:163], a[24:27], v[16:19]\n\t"
      "v_mfma_f32_16x16x32_bf16 v[16:19], v[176:179], a[28:31], v[16:19]\n\t"
      "v_mfma_f32_16x16x32_bf16 v[16:19], v[192:195], a[32:35], v[16:19]\n\t"
      "v_mfma_f32_16x16x32_bf16 v[16:19], v[208:211], a[36:39], v[16:19]\n\t"
      "v_mfma_f32_16x16x32_bf16 v[16:19], v[224:227], a[40:43], v[16:19]\n\t"
      "v_mfma_f32_16x16x32_bf16 v[16:19], v[240:243], a[44:47], v[16:19]\n\t"
      "v_mfma_f32_16x16x32_bf16 v[20:23], v[164:167], a[24:27], v[20:23]\n\t"
      "v_mfma_f32_16x16x32_bf16 v[20:23], v[180:183], a[28:31], v[20:23]\n\t"
      "s_waitcnt lgkmcnt(0)\n\t"
      "v_mfma_f32_16x16x32_bf16 v[20:23], v[196:199], a[32:35], v[20:23]\n\t"
      "buffer_store_dwordx4 a[232:235], v11, s[44:47], s85 offen offset:2048 sc1\n\t"
      "v_mfma_f32_16x16x32_bf16 v[20:23], v[212:215], a[36:39], v[20:23]\n\t"
      "v_mfma_f32_16x16x32_bf16 v[20:23], v[228:231], a[40:43], v[20:23]\n\t"
      "v_mfma_f32_16x16x32_bf16 v[20:23], v[244:247], a[44:47], v[20:23]\n\t"
      "v_mfma_f32_16x16x32_bf16 v[24:27], v[168:171], a[24:27], v[24:27]\n\t"
      "v_mfma_f32_16x16x32_bf16 v[24:27], v[184:187], a[28:31], v[24:27]\n\t"
      "v_cvt_pk_bf16_f32 v40, v16, v17\n\t"
      "v_cvt_pk_bf16_f32 v41, v18, v19\n\t"
      "v_mfma_f32_16x16x32_bf16 v[24:27], v[200:203], a[32:35], v[24:27]\n\t"
      "ds_write_b64 v0, v[40:41] offset:12544\n\t"
      "v_mfma_f32_16x16x32_bf16 v[24:27], v[216:219], a[36:39], v[24:27]\n\t"
      "v_mfma_f32_16x16x32_bf16 v[24:27], v[232:235], a[40:43], v[24:27]\n\t"
      "v_mfma_f32_16x16x32_bf16 v[24:27], v[248:251], a[44:47], v[24:27]\n\t"
      "v_mfma_f32_16x16x32_bf16 v[28:31], v[172:175], a[24:27], v[28:31]\n\t"
      "v_mfma_f32_16x16x32_bf16 v[28:31], v[188:191], a[28:31], v[28:31]\n\t"
      "v_cvt_pk_bf16_f32 v42, v20, v21\n\t"
      "v_cvt_pk_bf16_f32 v43, v22, v23\n\t"
      "v_mfma_f32_16x16x32_bf16 v[28:31], v[204:207], a[32:35], v[28:31]\n\t"
      "ds_write_b64 v0, v[42:43] offset:12576\n\t"
      "v_mfma_f32_16x16x32_bf16 v[28:31], v[220:223], a[36:39], v[28:31]\n\t"
      "v_mfma_f32_16x16x32_bf16 v[28:31], v[236:239], a[40:43], v[28:31]\n\t"
      "v_mfma_f32_16x16x32_bf16 v[28:31], v[252:255], a[44:47], v[28:31]\n\t"
      "v_mfma_f32_16x16x32_bf16 v[32:35], a[108:111], a[24:27], v[32:35]\n\t"
      "v_mfma_f32_16x16x32_bf16 v[32:35], a[116:119], a[28:31], v[32:35]\n\t"
      "v_cvt_pk_bf16_f32 v44, v24, v25\n\t"
      "v_cvt_pk_bf16_f32 v45, v26, v27\n\t"
      "v_mfma_f32_16x16x32_bf16 v[32:35], a[124:127], a[32:35], v[32:35]\n\t"
      "ds_write_b64 v0, v[44:45] offset:12608\n\t"
      "v_mfma_f32_16x16x32_bf16 v[32:35], a[132:135], a[36:39], v[32:35]\n\t"
      "v_mfma_f32_16x16x32_bf16 v[32:35], a[140:143], a[40:43], v[32:35]\n\t"
      "v_mfma_f32_16x16x32_bf16 v[32:35], a[148:151], a[44:47], v[32:35]\n\t"
      "v_cvt_pk_bf16_f32 v46, v28, v29\n\t"
      "v_cvt_pk_bf16_f32 v47, v30, v31\n\t"
      "v_mfma_f32_16x16x32_bf16 v[36:39], a[112:115], a[24:27], v[36:39]\n\t"
      "ds_write_b64 v0, v[46:47] offset:12640\n\t"
      "v_mfma_f32_16x16x32_bf16 v[36:39], a[120:123], a[28:31], v[36:39]\n\t"
      "v_mfma_f32_16x16x32_bf16 v[36:39], a[128:131], a[32:35], v[36:39]\n\t"
      "v_mfma_f32_16x16x32_bf16 v[36:39], a[136:139], a[36:39], v[36:39]\n\t"
      "v_mfma_f32_16x16x32_bf16 v[36:39], a[144:147], a[40:43], v[36:39]\n\t"
      "v_cvt_pk_bf16_f32 v48, v32, v33\n\t"
      "v_cvt_pk_bf16_f32 v49, v34, v35\n\t"
      "v_mfma_f32_16x16x32_bf16 v[36:39], a[152:155], a[44:47], v[36:39]\n\t"
      "ds_write_b64 v0, v[48:49] offset:12672\n\t"
      ".Ltail_%=:\n\t"
      "s_nop 6\n\t"
      "v_cvt_pk_bf16_f32 v50, v36, v37\n\t"
      "v_cvt_pk_bf16_f32 v51, v38, v39\n\t"
      "ds_write_b64 v0, v[50:51] offset:12704\n\t"
      "s_cmp_eq_u32 s85, 113221632\n\t"
      "s_cbranch_scc1 .Lwrapo_%=\n\t"
      ".Lwrappedo_%=:\n\t"
      "s_waitcnt lgkmcnt(0)\n\t"
      "s_barrier\n\t"
      "ds_read_b128 a[48:51], v8 offset:12544\n\t"
      "ds_read_b128 a[52:55], v60 offset:13568\n\t"
      "ds_read_b128 a[56:59], v61 offset:14592\n\t"
      "s_waitcnt lgkmcnt(2)\n\t"
      "buffer_store_dwordx4 a[48:51], v12, s[44:47], s85 offen sc1\n\t"
      "s_waitcnt lgkmcnt(1)\n\t"
      "buffer_store_dwordx4 a[52:55], v13, s[44:47], s85 offen offset:1024 sc1\n\t"
      "s_waitcnt lgkmcnt(0)\n\t"
      "buffer_store_dwordx4 a[56:59], v14, s[44:47], s85 offen offset:2048 sc1\n\t"
      "s_cmp_lt_i32 s91, 0\n\t"
      "s_cselect_b32 s30, -1, 0\n\t"
      "s_xor_b32 s16, s91, 0x3ffff\n\t"
      "s_and_b32 s16, s16, s30\n\t"
      "s_cbranch_scc0 .Lend_%=\n\t"
      "s_waitcnt vmcnt(0) lgkmcnt(0)\n\t"
      "s_barrier\n\t"
      "s_lshl1_add_u32 s30, s14, 24576\n\t"
      "s_sub_u32 s15, s30, s15\n\t"
      "s_branch .Lstart_%=\n\t"
      ".Lwrap3_%=:\n\t"
      "v_add_u32 v5, s48, v5\n\t"
      "v_subrev_u32 v62, 113246208, v5\n\t"
      "v_min_u32 v5, v5, v62\n\t"
      "v_add_u32 v6, s48, v6\n\t"
      "v_subrev_u32 v62, 113246208, v6\n\t"
      "v_min_u32 v6, v6, v62\n\t"
      "v_add_u32 v7, s48, v7\n\t"
      "v_subrev_u32 v62, 113246208, v7\n\t"
      "v_min_u32 v7, v7, v62\n\t"
      "s_mov_b32 s48, 0\n\t"
      "buffer_load_dwordx4 v5, s[40:43], s48 offen nt lds\n\t"
      "buffer_load_dwordx4 v6, s[40:43], s48 offen offset:1024 nt lds\n\t"
      "buffer_load_dwordx4 v7, s[40:43], s48 offen offset:2048 nt lds\n\t"
      "s_branch .Lwrapped3_%=\n\t"
      ".Lwrapo_%=:\n\t"
      "v_add_u32 v12, s85, v12\n\t"
      "v_subrev_u32 v62, 113246208, v12\n\t"
      "v_min_u32 v12, v12, v62\n\t"
      "v_add_u32 v13, s85, v13\n\t"
      "v_subrev_u32 v62, 113246208, v13\n\t"
      "v_min_u32 v13, v13, v62\n\t"
      "v_add_u32 v14, s85, v14\n\t"
      "v_subrev_u32 v62, 113246208, v14\n\t"
      "v_min_u32 v14, v14, v62\n\t"
      "s_mov_b32 s85, 0\n\t"
      "s_branch .Lwrappedo_%=\n\t"
      ".Lwait_%=:\n\t"
      "s_sub_u32 s52, s52, 1\n\t"
      "s_cbranch_scc1 .Lwait2_%=\n\t"
      "s_add_u32 s90, s90, 0x2000000\n\t"
      "v_mov_b32 v54, s90\n\t"
      "v_mov_b32 v55, s19\n\t"
      "global_store_dword v55, v54, s[12:13] sc1\n\t"
      "s_sleep 16\n\t"
      "global_load_dword v40, v62, s[12:13] sc1\n\t"
      "s_waitcnt vmcnt(0)\n\t"
      "s_branch .Lcheck_%=\n\t"
      ".Lwait2_%=:\n\t"
      "s_brev_b32 s91, 1\n\t"
      "s_branch .Lwaited_%=\n\t"
      ".Lspinf_%=:\n\t"
      "s_memrealtime s[36:37]\n\t"
      "s_waitcnt lgkmcnt(0)\n\t"
      "s_sub_u32 s53, s36, s38\n\t"
      "s_cmp_lt_i32 s53, 0\n\t"
      "s_cbranch_scc1 .Lspinf_%=\n\t"
      "s_branch .Lspunf_%=\n\t"
      ".Lspin0_%=:\n\t"
      "s_memrealtime s[36:37]\n\t"
      "s_waitcnt lgkmcnt(0)\n\t"
      "s_sub_u32 s53, s36, s38\n\t"
      "s_cmp_lt_i32 s53, 0\n\t"
      "s_cbranch_scc1 .Lspin0_%=\n\t"
      "s_branch .Lspun0_%=\n\t"
      ".Lspin1_%=:\n\t"
      "s_memrealtime s[36:37]\n\t"
      "s_waitcnt lgkmcnt(0)\n\t"
      "s_sub_u32 s53, s36, s38\n\t"
      "s_cmp_lt_i32 s53, 0\n\t"
      "s_cbranch_scc1 .Lspin1_%=\n\t"
      "s_branch .Lspun1_%=\n\t"
      ".Lend_%=:\n\t"
      : [tid] "+v"(tid)
      : [karg] "s"(__builtin_amdgcn_kernarg_segment_ptr()), [blk] "s"(blockIdx.x), [lds] "s"((unsigned)(size_t)lds)
      : "v1", "v2", "v3", "v4", "v5", "v6", "v7", "v8", "v9", "v10", "v11", "v12", "v13", "v14", "v15", "v16", "v17", "v18", "v19", "v20", "v21", "v22", "v23", "v24", "v25", "v26", "v27", "v28", "v29", "v30", "v31", "v32", "v33", "v34", "v35", "v36", "v37", "v38", "v39", "v40", "v41", "v42", "v43", "v44", "v45", "v46", "v47", "v48", "v49", "v50", "v51", "v52", "v53", "v54", "v55", "v56", "v57", "v58", "v59", "v60", "v61", "v62", "v63", "v64", "v65", "v66", "v67", "v68", "v69", "v70", "v71", "v72", "v73", "v74", "v75", "v76", "v77", "v78", "v79", "v80", "v81", "v82", "v83", "v84", "v85", "v86", "v87", "v88", "v89", "v90", "v91", "v92", "v93", "v94", "v95", "v96", "v97", "v98", "v99", "v100", "v101", "v102", "v103", "v104", "v105", "v106", "v107", "v108", "v109", "v110", "v111", "v112", "v113", "v114", "v115", "v116", "v117", "v118", "v119", "v120", "v121", "v122", "v123", "v124", "v125", "v126", "v127", "v128", "v129", "v130", "v131", "v132", "v133", "v134", "v135", "v136", "v137", "v138", "v139", "v140", "v141", "v142", "v143", "v144", "v145", "v146", "v147", "v148", "v149", "v150", "v151", "v152", "v153", "v154", "v155", "v156", "v157", "v158", "v159", "v160", "v161", "v162", "v163", "v164", "v165", "v166", "v167", "v168", "v169", "v170", "v171", "v172", "v173", "v174", "v175", "v176", "v177", "v178", "v179", "v180", "v181", "v182", "v183", "v184", "v185", "v186", "v187", "v188", "v189", "v190", "v191", "v192", "v193", "v194", "v195", "v196", "v197", "v198", "v199", "v200", "v201", "v202", "v203", "v204", "v205", "v206", "v207", "v208", "v209", "v210", "v211", "v212", "v213", "v214", "v215", "v216", "v217", "v218", "v219", "v220", "v221", "v222", "v223", "v224", "v225", "v226", "v227", "v228", "v229", "v230", "v231", "v232", "v233", "v234", "v235", "v236", "v237", "v238", "v239", "v240", "v241", "v242", "v243", "v244", "v245", "v246", "v247", "v248", "v249", "v250", "v251", "v252", "v253", "v254", "v255", "a0", "a1", "a2", "a3", "a4", "a5", "a6", "a7", "a8", "a9", "a10", "a11", "a12", "a13", "a14", "a15", "a16", "a17", "a18", "a19", "a20", "a21", "a22", "a23", "a24", "a25", "a26", "a27", "a28", "a29", "a30", "a31", "a32", "a33", "a34", "a35", "a36", "a37", "a38", "a39", "a40", "a41", "a42", "a43", "a44", "a45", "a46", "a47", "a48", "a49", "a50", "a51", "a52", "a53", "a54", "a55", "a56", "a57", "a58", "a59", "a60", "a61", "a62", "a63", "a64", "a65", "a66", "a67", "a68", "a69", "a70", "a71", "a72", "a73", "a74", "a75", "a76", "a77", "a78", "a79", "a80", "a81", "a82", "a83", "a84", "a85", "a86", "a87", "a88", "a89", "a90", "a91", "a92", "a93", "a94", "a95", "a96", "a97", "a98", "a99", "a100", "a101", "a102", "a103", "a104", "a105", "a106", "a107", "a108", "a109", "a110", "a111", "a112", "a113", "a114", "a115", "a116", "a117", "a118", "a119", "a120", "a121", "a122", "a123", "a124", "a125", "a126", "a127", "a128", "a129", "a130", "a131", "a132", "a133", "a134", "a135", "a136", "a137", "a138", "a139", "a140", "a141", "a142", "a143", "a144", "a145", "a146", "a147", "a148", "a149", "a150", "a151", "a152", "a153", "a154", "a155", "a156", "a157", "a158", "a159", "a160", "a161", "a162", "a163", "a164", "a165", "a166", "a167", "a168", "a169", "a170", "a171", "a172", "a173", "a174", "a175", "a176", "a177", "a178", "a179", "a180", "a181", "a182", "a183", "a184", "a185", "a186", "a187", "a188", "a189", "a190", "a191", "a192", "a193", "a194", "a195", "a196", "a197", "a198", "a199", "a200", "a201", "a202", "a203", "a204", "a205", "a206", "a207", "a208", "a209", "a210", "a211", "a212", "a213", "a214", "a215", "a216", "a217", "a218", "a219", "a220", "a221", "a222", "a223", "a224", "a225", "a226", "a227", "a228", "a229", "a230", "a231", "a232", "a233", "a234", "a235", "a236", "a237", "a238", "a239", "a240", "a241", "a242", "a243", "a244", "a245", "a246", "a247", "a248", "a249", "a250", "a251", "a252", "a253", "a254", "a255", "s4", "s5", "s6", "s7", "s8", "s9", "s10", "s11", "s12", "s13", "s14", "s15", "s16", "s17", "s18", "s19", "s20", "s21", "s22", "s23", "s24", "s25", "s26", "s27", "s28", "s29", "s30", "s31", "s32", "s33", "s34", "s35", "s36", "s37", "s38", "s39", "s40", "s41", "s42", "s43", "s44", "s45", "s46", "s47", "s48", "s49", "s50", "s51", "s52", "s53", "s54", "s55", "s56", "s57", "s58", "s59", "s60", "s61", "s62", "s63", "s64", "s65", "s66", "s67", "s68", "s69", "s70", "s71", "s72", "s73", "s74", "s75", "s76", "s77", "s78", "s79", "s80", "s81", "s82", "s83", "s84", "s85", "s86", "s87", "s88", "s89", "s90", "s91", "vcc", "scc", "memory");
}

__global__ __launch_bounds__(384) void dwsep_plain(const unsigned short* __restrict__ x, const unsigned short* __restrict__ dw,
                                                   const unsigned short* __restrict__ pw, unsigned short* __restrict__ out) {
  __shared__ float xs[384];
  const unsigned n = threadIdx.x;
  const size_t row = (size_t)blockIdx.x * 384u;
  xs[n] = __uint_as_float((unsigned)x[row + n] << 16) * __uint_as_float((unsigned)dw[n] << 16);
  __syncthreads();
  float acc = 0.f;
  for (int k = 0; k < 384; ++k) acc += xs[k] * __uint_as_float((unsigned)pw[k * 384 + n] << 16);
  const __bf16 o = (__bf16)acc;
  out[row + n] = __builtin_bit_cast(unsigned short, o);
}

extern "C" void kernel_launch(void* const* d_in, const int* in_sizes, int n_in, void* d_out, int out_size,
                              void* d_ws, size_t ws_size, hipStream_t stream) {
  const unsigned short* x = (const unsigned short*)d_in[0];
  const unsigned short* dw = (const unsigned short*)d_in[1];
  const unsigned short* pw = (const unsigned short*)d_in[2];
  unsigned short* out = (unsigned short*)d_out;
  if (in_sizes[0] == 56623104) dwsep_kernel<<<NWG, 256, 0, stream>>>(x, dw, pw, out, (unsigned*)d_ws);
  else dwsep_plain<<<in_sizes[0] / 384, 384, 0, stream>>>(x, dw, pw, out);
}
```

```cpp
#include <hip/hip_runtime.h>

constexpr unsigned ROWB = 768, TILEB = 12288, NWG = 256;

__global__ __launch_bounds__(256, 1) void dwsep_kernel(const unsigned short* __restrict__ x,
                                                       const unsigned short* __restrict__ dw,
                                                       const unsigned short* __restrict__ pw,
                                                       unsigned short* __restrict__ out, unsigned* ws) {
  __shared__ __attribute__((aligned(256))) char lds[163392];
  unsigned tid = threadIdx.x;
  asm volatile(
      "s_memtime s[32:33]\n\t"
      "s_load_dwordx8 s[4:11], %[karg], 0\n\t"
      "s_load_dwordx2 s[12:13], %[karg], 0x20\n\t"
      "v_readfirstlane_b32 s30, %[tid]\n\t"
      "s_lshr_b32 s31, %[blk], 1\n\t"
      "s_and_b32 s31, s31, 12\n\t"
      "s_and_b32 s34, %[blk], 3\n\t"
      "s_or_b32 s31, s31, s34\n\t"
      "s_lshr_b32 s30, s30, 6\n\t"
      "s_lshr_b32 s34, %[blk], 5\n\t"
      "s_bfe_u32 s35, %[blk], 0x10002\n\t"
      "s_mul_i32 s17, s30, 6144\n\t"
      "s_mul_i32 s18, s30, 3072\n\t"
      "s_mul_i32 s14, s34, 14155776\n\t"
      "s_mul_i32 s36, s31, 49152\n\t"
      "s_add_u32 s14, s14, s36\n\t"
      "s_mul_i32 s36, s35, 24576\n\t"
      "s_add_u32 s15, s14, s36\n\t"
      "s_mov_b32 s16, 0\n\t"
      "s_lshr_b32 s36, %[blk], 3\n\t"
      "s_lshl2_add_u32 s24, s36, s30\n\t"
      "s_sub_u32 s36, s24, s36\n\t"
      "s_sub_u32 s36, s36, 1\n\t"
      "s_mul_i32 s23, s36, 3072\n\t"
      "s_lshl_b32 s20, s34, 3\n\t"
      "s_mul_i32 s36, s34, 27\n\t"
      "s_lshr_b32 s36, s36, 2\n\t"
      "s_lshr_b32 s37, s31, 1\n\t"
      "s_add_u32 s19, s37, s20\n\t"
      "s_add_u32 s37, s37, s36\n\t"
      "s_min_u32 s37, s37, 53\n\t"
      "s_lshl_b32 s22, s37, 21\n\t"
      "s_lshl_b32 s36, s36, 21\n\t"
      "s_sub_u32 s21, s14, s36\n\t"
      "s_add_u32 s21, s21, 24576\n\t"
      "s_lshl_b32 s19, s19, 8\n\t"
      "s_and_b32 s31, s31, 1\n\t"
      "s_lshl1_add_u32 s31, s31, s35\n\t"
      "s_lshl2_add_u32 s19, s31, s19\n\t"
      ".Lstart_%=:\n\t"
      "v_mbcnt_lo_u32_b32 v233, -1, 0\n\t"
      "v_mbcnt_hi_u32_b32 v233, -1, v233\n\t"
      "s_mul_i32 s30, s17, 683\n\t"
      "s_lshr_b32 s30, s30, 16\n\t"
      "s_and_b32 s30, s30, 0xc0\n\t"
      "s_brev_b32 s34, 8\n\t"
      "s_mov_b32 s35, 0x20201010\n\t"
      "s_mov_b32 s36, 0x30303020\n\t"
      "v_lshl_add_u32 v1, v233, 4, s18\n\t"
      "v_lshrrev_b32 v16, 1, v233\n\t"
      "v_and_b32 v16, 24, v16\n\t"
      "v_bfe_u32 v17, s34, v16, 8\n\t"
      "v_or_b32 v17, s30, v17\n\t"
      "v_xor_b32 v18, v17, v1\n\t"
      "v_add_u32 v19, 80, v17\n\t"
      "v_add_u32 v17, 160, v17\n\t"
      "v_and_b32 v19, 256, v19\n\t"
      "v_and_b32 v17, 256, v17\n\t"
      "v_mad_u32_u24 v2, v19, 30, v18\n\t"
      "v_mad_u32_u24 v5, v17, 33, v18\n\t"
      "v_add_u32 v5, 19968, v5\n\t"
      "v_bfe_u32 v17, s35, v16, 8\n\t"
      "v_or_b32 v17, s30, v17\n\t"
      "v_xor_b32 v18, v17, v1\n\t"
      "v_add_u32 v19, 80, v17\n\t"
      "v_add_u32 v17, 160, v17\n\t"
      "v_and_b32 v19, 256, v19\n\t"
      "v_and_b32 v17, 256, v17\n\t"
      "v_mad_u32_u24 v3, v19, 30, v18\n\t"
      "v_mad_u32_u24 v6, v17, 33, v18\n\t"
      "v_add_u32 v6, 19968, v6\n\t"
      "v_bfe_u32 v17, s36, v16, 8\n\t"
      "v_or_b32 v17, s30, v17\n\t"
      "v_xor_b32 v18, v17, v1\n\t"
      "v_add_u32 v19, 80, v17\n\t"
      "v_add_u32 v17, 160, v17\n\t"
      "v_and_b32 v19, 256, v19\n\t"
      "v_and_b32 v17, 256, v17\n\t"
      "v_mad_u32_u24 v4, v19, 30, v18\n\t"
      "v_mad_u32_u24 v7, v17, 33, v18\n\t"
      "v_add_u32 v7, 19968, v7\n\t"
      "s_sub_u32 s31, s30, 64\n\t"
      "v_add_u32 v17, s31, v233\n\t"
      "v_min_u32 v17, 193, v17\n\t"
      "v_lshlrev_b32 v18, 2, v17\n\t"
      "v_lshlrev_b32 v17, 1, v17\n\t"
      "v_and_b32 v18, -16, v18\n\t"
      "v_and_or_b32 v8, v17, 6, v18\n\t"
      "s_mov_b32 s86, 294912\n\t"
      "s_mov_b32 s87, 0x00020000\n\t"
      "s_mov_b32 s42, 113246208\n\t"
      "s_mov_b32 s43, 0x00020000\n\t"
      "s_cmp_lg_u32 s17, 0\n\t"
      "s_cbranch_scc1 .Lno0_%=\n\t"
      "s_movk_i32 s31, -61\n\t"
      "v_lshrrev_b32 v16, 2, v233\n\t"
      "v_add_u32 v17, s20, v16\n\t"
      "v_lshl_add_u32 v18, v16, 3, s31\n\t"
      "v_cmp_gt_u32 vcc, 32, v233\n\t"
      "v_and_b32 v19, 3, v233\n\t"
      "v_lshlrev_b32 v19, 2, v19\n\t"
      "v_cndmask_b32 v17, v18, v17, vcc\n\t"
      "v_lshl_add_u32 v62, v17, 8, v19\n\t"
      "s_waitcnt lgkmcnt(2)\n\t"
      "s_add_u32 s32, s32, %[blk]\n\t"
      "s_mul_i32 s32, s32, 0x9e3779b1\n\t"
      "s_lshr_b32 s32, s32, 9\n\t"
      "s_and_b32 s32, s32, 0x1fdf80\n\t"
      "s_or_b32 s22, s22, s32\n\t"
      "v_mov_b32 v234, s22\n\t"
      "v_add_u32 v235, 8192, v234\n\t"
      "s_movk_i32 s64, 120\n\t"
      "s_mov_b64 vcc, 0\n\t"
      ".Lsweep_%=:\n\t"
      "s_getreg_b32 s30, hwreg(HW_REG_IB_STS, 0, 12)\n\t"
      "s_and_b32 s31, s30, 15\n\t"
      "s_lshr_b32 s30, s30, 8\n\t"
      "s_cmp_eq_u32 s31, 8\n\t"
      "s_addc_u32 s62, s62, 0\n\t"
      "s_cmp_eq_u32 s30, 1\n\t"
      "s_addc_u32 s63, s63, 0\n\t"
      "s_cmp_gt_u32 s31, 7\n\t"
      "s_addc_u32 s30, s30, 0\n\t"
      "s_sub_u32 s64, s64, 1\n\t"
      "s_min_u32 s30, s30, s64\n\t"
      "s_cmp_lg_u32 s30, 0\n\t"
      "s_cbranch_scc1 .Lsweep_%=\n\t"
      "s_cbranch_vccnz .Lswept_%=\n\t"
      "s_waitcnt lgkmcnt(0)\n\t"
      "global_load_dword v236, v234, s[4:5]\n\t"
      "global_load_dword v237, v235, s[4:5]\n\t"
      "s_memrealtime s[28:29]\n\t"
      "s_load_dword s60, s[10:11], s22\n\t"
      "s_load_dword s61, s[10:11], s22 offset:8192\n\t"
      "global_load_dword v63, v62, s[12:13] sc1\n\t"
      "s_mov_b64 s[40:41], s[4:5]\n\t"
      "s_add_u32 m0, s18, 25088\n\t"
      "s_add_u32 s48, s15, 0\n\t"
      "buffer_load_dwordx4 v2, s[40:43], s48 offen lds\n\t"
      "buffer_load_dwordx4 v3, s[40:43], s48 offen offset:1024 lds\n\t"
      "buffer_load_dwordx4 v4, s[40:43], s48 offen offset:2048 lds\n\t"
      "s_add_u32 m0, s18, 37376\n\t"
      "s_add_u32 s48, s15, 0\n\t"
      "buffer_load_dwordx4 v5, s[40:43], s48 offen lds\n\t"
      "buffer_load_dwordx4 v6, s[40:43], s48 offen offset:1024 lds\n\t"
      "buffer_load_dwordx4 v7, s[40:43], s48 offen offset:2048 lds\n\t"
      "s_barrier\n\t"
      "s_mov_b64 s[62:63], 0\n\t"
      "s_movk_i32 s64, 120\n\t"
      "s_mov_b64 vcc, -1\n\t"
      "s_branch .Lsweep_%=\n\t"
      ".Lswept_%=:\n\t"
      "s_min_u32 s62, s62, 7\n\t"
      "s_min_u32 s63, s63, 7\n\t"
      "s_lshl_b32 s63, s63, 8\n\t"
      "s_or_b32 s30, s62, s63\n\t"
      "s_sub_u32 s31, 0x808, s30\n\t"
      "s_add_u32 s30, s30, 0x10808\n\t"
      "s_getreg_b32 s62, hwreg(HW_REG_XCC_ID, 2, 1)\n\t"
      "s_cmp_lg_u32 s62, 0\n\t"
      "s_cselect_b32 s91, s30, s31\n\t"
      "s_mov_b64 s[84:85], s[8:9]\n\t"
      "v_mul_u32_u24 v16, 49152, v1\n\t"
      "v_add_u32 v16, s21, v16\n\t"
      "v_lshrrev_b32 v16, 21, v16\n\t"
      "v_lshlrev_b32 v16, 2, v16\n\t"
      "v_accvgpr_write_b32 a248, v16\n\t"
      "s_branch .Lgo0_%=\n\t"
      ".Lno0_%=:\n\t"
      "s_movk_i32 s82, 768\n\t"
      "s_mov_b32 s83, 0x00020000\n\t"
      "v_and_b32 v234, 0x3f0, v1\n\t"
      "s_waitcnt lgkmcnt(0)\n\t"
      "s_mov_b64 s[84:85], s[8:9]\n\t"
      "s_mov_b64 s[80:81], s[6:7]\n\t"
      "s_mov_b64 s[40:41], s[4:5]\n\t"
      "s_barrier\n\t"
      "buffer_load_dwordx4 a[200:203], v234, s[84:87], s23 offen offset:0\n\t"
      "buffer_load_dwordx4 a[204:207], v234, s[84:87], s23 offen offset:1024\n\t"
      "buffer_load_dwordx4 a[208:211], v234, s[84:87], s23 offen offset:2048\n\t"
      "buffer_load_ushort v224, v8, s[80:83], 0 offen\n\t"
      "buffer_load_ushort v225, v8, s[80:83], 0 offen offset:8\n\t"
      "s_add_u32 m0, s18, 25088\n\t"
      "s_add_u32 s48, s15, 0\n\t"
      "buffer_load_dwordx4 v2, s[40:43], s48 offen lds\n\t"
      "buffer_load_dwordx4 v3, s[40:43], s48 offen offset:1024 lds\n\t"
      "buffer_load_dwordx4 v4, s[40:43], s48 offen offset:2048 lds\n\t"
      "s_add_u32 m0, s18, 37376\n\t"
      "s_add_u32 s48, s15, 0\n\t"
      "buffer_load_dwordx4 v5, s[40:43], s48 offen lds\n\t"
      "buffer_load_dwordx4 v6, s[40:43], s48 offen offset:1024 lds\n\t"
      "buffer_load_dwordx4 v7, s[40:43], s48 offen offset:2048 lds\n\t"
      ".Lgo0_%=:\n\t"
      "s_getpc_b64 s[36:37]\n\t"
      ".Lpc_%=:\n\t"
      "s_lshr_b32 s30, s17, 5\n\t"
      "v_lshlrev_b32 v16, 4, v233\n\t"
      "v_add_u32 v17, 0, v233\n\t"
      "v_mul_u32_u24 v17, 171, v17\n\t"
      "v_lshrrev_b32 v17, 11, v17\n\t"
      "v_lshlrev_b32 v18, 2, v17\n\t"
      "v_and_b32 v18, 32, v18\n\t"
      "v_xor_b32 v18, v18, v16\n\t"
      "v_mul_u32_u24 v17, 576, v17\n\t"
      "v_add3_u32 v9, v17, s30, v18\n\t"
      "v_add_u32 v17, 64, v233\n\t"
      "v_mul_u32_u24 v17, 171, v17\n\t"
      "v_lshrrev_b32 v17, 11, v17\n\t"
      "v_lshlrev_b32 v18, 2, v17\n\t"
      "v_and_b32 v18, 32, v18\n\t"
      "v_xor_b32 v18, v18, v16\n\t"
      "v_mul_u32_u24 v17, 576, v17\n\t"
      "v_add3_u32 v10, v17, s30, v18\n\t"
      "v_add_u32 v17, 128, v233\n\t"
      "v_mul_u32_u24 v17, 171, v17\n\t"
      "v_lshrrev_b32 v17, 11, v17\n\t"
      "v_lshlrev_b32 v18, 2, v17\n\t"
      "v_and_b32 v18, 32, v18\n\t"
      "v_xor_b32 v18, v18, v16\n\t"
      "v_mul_u32_u24 v17, 576, v17\n\t"
      "v_add3_u32 v11, v17, s30, v18\n\t"
      "s_mov_b64 s[44:45], s[10:11]\n\t"
      "s_mov_b32 s46, 0\n\t"
      "s_mov_b32 s47, 0x00020000\n\t"
      "s_lshl_b32 s30, s24, 6\n\t"
      "s_add_u32 s31, s30, 0\n\t"
      "s_min_u32 s31, s31, .Lend_%=-.Lpc_%=-4\n\t"
      "s_load_dword s35, s[36:37], s31\n\t"
      "s_add_u32 s31, s30, 8192\n\t"
      "s_min_u32 s31, s31, .Lend_%=-.Lpc_%=-4\n\t"
      "s_load_dword s35, s[36:37], s31\n\t"
      "s_add_u32 s31, s30, 16384\n\t"
      "s_min_u32 s31, s31, .Lend_%=-.Lpc_%=-4\n\t"
      "s_load_dword s35, s[36:37], s31\n\t"
      "s_add_u32 s31, s30, 24576\n\t"
      "s_min_u32 s31, s31, .Lend_%=-.Lpc_%=-4\n\t"
      "s_load_dword s35, s[36:37], s31\n\t"
      "s_and_b32 s31, s24, 63\n\t"
      "s_lshl_b32 s31, s31, 21\n\t"
      "s_min_u32 s31, s31, 113246144\n\t"
      "s_bitcmp1_b32 s24, 6\n\t"
      "s_cselect_b64 s[32:33], s[44:45], s[40:41]\n\t"
      "s_load_dword s35, s[32:33], s31\n\t"
      "s_lshr_b32 s30, s17, 5\n\t"
      "v_lshrrev_b32 v16, 4, v233\n\t"
      "v_and_b32 v17, 15, v233\n\t"
      "v_and_b32 v18, 3, v233\n\t"
      "v_bfe_u32 v19, v233, 2, 2\n\t"
      "v_lshl_add_u32 v19, v16, 3, v19\n\t"
      "v_mul_u32_u24 v19, 192, v19\n\t"
      "v_lshl_add_u32 v19, v18, 3, v19\n\t"
      "v_lshlrev_b32 v20, 1, v233\n\t"
      "v_and_b32 v20, 32, v20\n\t"
      "v_add3_u32 v12, v19, v20, s17\n\t"
      "v_xor_b32 v13, 32, v12\n\t"
      "v_xor_b32 v19, v16, v17\n\t"
      "v_lshlrev_b32 v19, 4, v19\n\t"
      "v_mul_u32_u24 v20, 768, v17\n\t"
      "v_add_u32 v15, v19, v20\n\t"
      "v_mul_u32_u24 v20, 784, v17\n\t"
      "v_lshl_add_u32 v20, v16, 3, v20\n\t"
      "v_add_u32 v0, s30, v20\n\t"
      "v_lshlrev_b32 v19, 6, v16\n\t"
      "v_lshl_add_u32 v19, v18, 4, v19\n\t"
      "v_add_u32 v14, 160256, v19\n\t"
      "v_and_b32 v226, 0xfffffff0, v8\n\t"
      "v_and_b32 v227, 6, v8\n\t"
      "v_lshlrev_b32 v226, 2, v226\n\t"
      "v_lshl_add_u32 v226, v227, 3, v226\n\t"
      "v_add_u32 v226, 160256, v226\n\t"
      "v_and_b32 v232, 2, v8\n\t"
      "v_lshlrev_b32 v232, 3, v232\n\t"
      "v_cmp_gt_u32 vcc, 4, v227\n\t"
      "v_mov_b32 v56, v15\n\t"
      "v_xor_b32 v57, 64, v15\n\t"
      "v_xor_b32 v58, 128, v15\n\t"
      "v_xor_b32 v59, 192, v15\n\t"
      "v_add_u32 v60, 86528, v12\n\t"
      "v_add_u32 v61, 86528, v13\n\t"
      "v_xor_b32 v16, v2, v1\n\t"
      "v_and_b32 v16, 0xf0, v16\n\t"
      "v_xor_b32 v17, v16, v2\n\t"
      "v_xor_b32 v18, v16, v5\n\t"
      "v_add_u32 v19, v16, v1\n\t"
      "v_accvgpr_write_b32 a213, v17\n\t"
      "v_accvgpr_write_b32 a216, v18\n\t"
      "v_accvgpr_write_b32 a219, v19\n\t"
      "v_xor_b32 v16, v3, v1\n\t"
      "v_and_b32 v16, 0xf0, v16\n\t"
      "v_xor_b32 v17, v16, v3\n\t"
      "v_xor_b32 v18, v16, v6\n\t"
      "v_add_u32 v19, v16, v1\n\t"
      "v_accvgpr_write_b32 a214, v17\n\t"
      "v_accvgpr_write_b32 a217, v18\n\t"
      "v_accvgpr_write_b32 a220, v19\n\t"
      "v_xor_b32 v16, v4, v1\n\t"
      "v_and_b32 v16, 0xf0, v16\n\t"
      "v_xor_b32 v17, v16, v4\n\t"
      "v_xor_b32 v18, v16, v7\n\t"
      "v_add_u32 v19, v16, v1\n\t"
      "v_accvgpr_write_b32 a215, v17\n\t"
      "v_accvgpr_write_b32 a218, v18\n\t"
      "v_accvgpr_write_b32 a221, v19\n\t"
      "s_waitcnt vmcnt(6)\n\t"
      "s_bfe_u32 s30, s19, 0x30008\n\t"
      "s_bfe_u32 s31, s19, 0x20002\n\t"
      "s_lshl2_add_u32 s30, s30, s31\n\t"
      "v_readlane_b32 s90, v63, s30\n\t"
      "v_lshlrev_b32 v224, v232, v224\n\t"
      "v_lshlrev_b32 v225, v232, v225\n\t"
      "v_cndmask_b32 v228, 0, v224, vcc\n\t"
      "v_cndmask_b32 v230, 0, v225, vcc\n\t"
      "v_sub_u32 v229, v224, v228\n\t"
      "v_sub_u32 v231, v225, v230\n\t"
      "ds_write_b128 v226, v[228:231]\n\t"
      "s_add_u32 s90, s90, 0x2000000\n\t"
      "s_andn2_b32 s90, s90, 0x1ffffff\n\t"
      "s_or_b32 s90, s90, s91\n\t"
      "s_or_b32 s30, s17, s16\n\t"
      "s_cbranch_scc1 .Lno2_%=\n\t"
      "v_mov_b32 v234, s90\n\t"
      "v_mov_b32 v235, s19\n\t"
      "global_store_dword v235, v234, s[12:13] sc1\n\t"
      ".Lno2_%=:\n\t"
      "s_add_u32 m0, s17, 0\n\t"
      "s_mov_b32 s54, 0\n\t"
      "buffer_load_dwordx4 v9, s[84:87], s54 offen lds\n\t"
      "buffer_load_dwordx4 v10, s[84:87], s54 offen offset:1024 lds\n\t"
      "buffer_load_dwordx4 v11, s[84:87], s54 offen offset:2048 lds\n\t"
      "s_add_u32 m0, s17, 3072\n\t"
      "s_mov_b32 s54, 12288\n\t"
      "buffer_load_dwordx4 v9, s[84:87], s54 offen lds\n\t"
      "buffer_load_dwordx4 v10, s[84:87], s54 offen offset:1024 lds\n\t"
      "buffer_load_dwordx4 v11, s[84:87], s54 offen offset:2048 lds\n\t"
      "s_add_u32 m0, s17, 86528\n\t"
      "s_mov_b32 s54, 24576\n\t"
      "buffer_load_dwordx4 v9, s[84:87], s54 offen lds\n\t"
      "buffer_load_dwordx4 v10, s[84:87], s54 offen offset:1024 lds\n\t"
      "buffer_load_dwordx4 v11, s[84:87], s54 offen offset:2048 lds\n\t"
      "s_add_u32 m0, s17, 89600\n\t"
      "s_mov_b32 s54, 36864\n\t"
      "buffer_load_dwordx4 v9, s[84:87], s54 offen lds\n\t"
      "buffer_load_dwordx4 v10, s[84:87], s54 offen offset:1024 lds\n\t"
      "buffer_load_dwordx4 v11, s[84:87], s54 offen offset:2048 lds\n\t"
      "s_add_u32 m0, s17, 111104\n\t"
      "s_mov_b32 s54, 49152\n\t"
      "buffer_load_dwordx4 v9, s[84:87], s54 offen lds\n\t"
      "buffer_load_dwordx4 v10, s[84:87], s54 offen offset:1024 lds\n\t"
      "buffer_load_dwordx4 v11, s[84:87], s54 offen offset:2048 lds\n\t"
      "s_add_u32 m0, s17, 114176\n\t"
      "s_mov_b32 s54, 61440\n\t"
      "buffer_load_dwordx4 v9, s[84:87], s54 offen lds\n\t"
      "buffer_load_dwordx4 v10, s[84:87], s54 offen offset:1024 lds\n\t"
      "buffer_load_dwordx4 v11, s[84:87], s54 offen offset:2048 lds\n\t"
      "s_waitcnt lgkmcnt(0)\n\t"
      "s_waitcnt vmcnt(18)\n\t"
      "s_barrier\n\t"
      "s_waitcnt vmcnt(12)\n\t"
      "ds_read_b128 a[180:183], v14 offset:0\n\t"
      "ds_read_b64_tr_b16 v[64:65], v12 offset:0\n\t"
      "ds_read_b64_tr_b16 v[66:67], v12 offset:768\n\t"
      "ds_read_b64_tr_b16 v[68:69], v13 offset:0\n\t"
      "ds_read_b64_tr_b16 v[70:71], v13 offset:768\n\t"
      "ds_read_b64_tr_b16 v[72:73], v12 offset:64\n\t"
      "ds_read_b64_tr_b16 v[74:75], v12 offset:832\n\t"
      "ds_read_b64_tr_b16 v[76:77], v13 offset:64\n\t"
      "ds_read_b64_tr_b16 v[78:79], v13 offset:832\n\t"
      "ds_read_b64_tr_b16 a[60:61], v12 offset:128\n\t"
      "ds_read_b64_tr_b16 a[62:63], v12 offset:896\n\t"
      "ds_read_b64_tr_b16 a[64:65], v13 offset:128\n\t"
      "ds_read_b64_tr_b16 a[66:67], v13 offset:896\n\t"
      "s_add_u32 m0, s17, 135680\n\t"
      "s_mov_b32 s54, 73728\n\t"
      "buffer_load_dwordx4 v9, s[84:87], s54 offen lds\n\t"
      "buffer_load_dwordx4 v10, s[84:87], s54 offen offset:1024 lds\n\t"
      "buffer_load_dwordx4 v11, s[84:87], s54 offen offset:2048 lds\n\t"
      "s_add_u32 m0, s17, 138752\n\t"
      "s_mov_b32 s54, 86016\n\t"
      "buffer_load_dwordx4 v9, s[84:87], s54 offen lds\n\t"
      "buffer_load_dwordx4 v10, s[84:87], s54 offen offset:1024 lds\n\t"
      "buffer_load_dwordx4 v11, s[84:87], s54 offen offset:2048 lds\n\t"
      "s_waitcnt vmcnt(12)\n\t"
      "s_waitcnt lgkmcnt(0)\n\t"
      "; step\n\t"
      "v_mfma_f32_4x4x4_16b_bf16 v[40:43], a[180:181], v[64:65], 0\n\t"
      "ds_read_b128 a[184:187], v14 offset:256\n\t"
      "v_mfma_f32_4x4x4_16b_bf16 v[44:47], a[182:183], v[66:67], 0\n\t"
      "ds_read_b64_tr_b16 v[80:81], v60 offset:0\n\t"
      "v_mfma_f32_4x4x4_16b_bf16 v[48:51], a[180:181], v[68:69], 0\n\t"
      "ds_read_b64_tr_b16 v[82:83], v60 offset:768\n\t"
      "v_mfma_f32_4x4x4_16b_bf16 v[52:55], a[182:183], v[70:71], 0\n\t"
      "ds_read_b64_tr_b16 v[84:85], v61 offset:0\n\t"
      "ds_read_b64_tr_b16 v[86:87], v61 offset:768\n\t"
      "s_add_u32 m0, s17, 0\n\t"
      "s_mov_b32 s54, 98304\n\t"
      "buffer_load_dwordx4 v9, s[84:87], s54 offen lds\n\t"
      "buffer_load_dwordx4 v10, s[84:87], s54 offen offset:1024 lds\n\t"
      "v_cvt_pk_bf16_f32 v64, v40, v41\n\t"
      "v_cvt_pk_bf16_f32 v65, v42, v43\n\t"
      "v_cvt_pk_bf16_f32 v66, v44, v45\n\t"
      "v_cvt_pk_bf16_f32 v67, v46, v47\n\t"
      "v_cvt_pk_bf16_f32 v68, v48, v49\n\t"
      "v_cvt_pk_bf16_f32 v69, v50, v51\n\t"
      "v_cvt_pk_bf16_f32 v70, v52, v53\n\t"
      "v_cvt_pk_bf16_f32 v71, v54, v55\n\t"
      "v_mfma_f32_4x4x4_16b_bf16 v[40:43], a[180:181], v[72:73], 0\n\t"
      "ds_read_b64_tr_b16 v[88:89], v60 offset:64\n\t"
      "v_mfma_f32_4x4x4_16b_bf16 v[44:47], a[182:183], v[74:75], 0\n\t"
      "ds_read_b64_tr_b16 v[90:91], v60 offset:832\n\t"
      "v_mfma_f32_4x4x4_16b_bf16 v[48:51], a[180:181], v[76:77], 0\n\t"
      "ds_read_b64_tr_b16 v[92:93], v61 offset:64\n\t"
      "v_mfma_f32_4x4x4_16b_bf16 v[52:55], a[182:183], v[78:79], 0\n\t"
      "ds_read_b64_tr_b16 v[94:95], v61 offset:832\n\t"
      "buffer_load_dwordx4 v11, s[84:87], s54 offen offset:2048 lds\n\t"
      "s_add_u32 m0, s17, 3072\n\t"
      "s_mov_b32 s54, 110592\n\t"
      "buffer_load_dwordx4 v9, s[84:87], s54 offen lds\n\t"
      "v_cvt_pk_bf16_f32 v72, v40, v41\n\t"
      "v_cvt_pk_bf16_f32 v73, v42, v43\n\t"
      "v_cvt_pk_bf16_f32 v74, v44, v45\n\t"
      "v_cvt_pk_bf16_f32 v75, v46, v47\n\t"
      "v_cvt_pk_bf16_f32 v76, v48, v49\n\t"
      "v_cvt_pk_bf16_f32 v77, v50, v51\n\t"
      "v_cvt_pk_bf16_f32 v78, v52, v53\n\t"
      "v_cvt_pk_bf16_f32 v79, v54, v55\n\t"
      "ds_read_b128 a[224:227], v56 offset:25088\n\t"
      "v_mfma_f32_4x4x4_16b_bf16 v[40:43], a[180:181], a[60:61], 0\n\t"
      "ds_read_b64_tr_b16 a[68:69], v60 offset:128\n\t"
      "v_mfma_f32_4x4x4_16b_bf16 v[44:47], a[182:183], a[62:63], 0\n\t"
      "ds_read_b64_tr_b16 a[70:71], v60 offset:896\n\t"
      "v_mfma_f32_4x4x4_16b_bf16 v[48:51], a[180:181], a[64:65], 0\n\t"
      "ds_read_b64_tr_b16 a[72:73], v61 offset:128\n\t"
      "v_mfma_f32_4x4x4_16b_bf16 v[52:55], a[182:183], a[66:67], 0\n\t"
      "ds_read_b64_tr_b16 a[74:75], v61 offset:896\n\t"
      "buffer_load_dwordx4 v10, s[84:87], s54 offen offset:1024 lds\n\t"
      "buffer_load_dwordx4 v11, s[84:87], s54 offen offset:2048 lds\n\t"
      "ds_read_b128 a[228:231], v56 offset:37376\n\t"
      "v_cvt_pk_bf16_f32 v40, v40, v41\n\t"
      "v_cvt_pk_bf16_f32 v41, v42, v43\n\t"
      "v_cvt_pk_bf16_f32 v44, v44, v45\n\t"
      "v_cvt_pk_bf16_f32 v45, v46, v47\n\t"
      "v_cvt_pk_bf16_f32 v48, v48, v49\n\t"
      "v_cvt_pk_bf16_f32 v49, v50, v51\n\t"
      "v_cvt_pk_bf16_f32 v52, v52, v53\n\t"
      "v_cvt_pk_bf16_f32 v53, v54, v55\n\t"
      "v_accvgpr_write_b32 a60, v40\n\t"
      "v_accvgpr_write_b32 a61, v41\n\t"
      "v_accvgpr_write_b32 a62, v44\n\t"
      "v_accvgpr_write_b32 a63, v45\n\t"
      "v_accvgpr_write_b32 a64, v48\n\t"
      "v_accvgpr_write_b32 a65, v49\n\t"
      "v_accvgpr_write_b32 a66, v52\n\t"
      "v_accvgpr_write_b32 a67, v53\n\t"
      "s_waitcnt vmcnt(12)\n\t"
      "s_waitcnt lgkmcnt(1)\n\t"
      "; step\n\t"
      "s_memtime s[64:65]\n\t"
      "s_memrealtime s[66:67]\n\t"
      "v_mfma_f32_4x4x4_16b_bf16 v[40:43], a[184:185], v[80:81], 0\n\t"
      "ds_read_b128 a[180:183], v14 offset:512\n\t"
      "v_mfma_f32_4x4x4_16b_bf16 v[44:47], a[186:187], v[82:83], 0\n\t"
      "ds_read_b64_tr_b16 v[96:97], v60 offset:24576\n\t"
      "v_mfma_f32_4x4x4_16b_bf16 v[48:51], a[184:185], v[84:85], 0\n\t"
      "ds_read_b64_tr_b16 v[98:99], v60 offset:25344\n\t"
      "v_mfma_f32_4x4x4_16b_bf16 v[52:55], a[186:187], v[86:87], 0\n\t"
      "ds_read_b64_tr_b16 v[100:101], v61 offset:24576\n\t"
      "ds_read_b64_tr_b16 v[102:103], v61 offset:25344\n\t"
      "s_add_u32 m0, s17, 86528\n\t"
      "s_mov_b32 s54, 122880\n\t"
      "buffer_load_dwordx4 v9, s[84:87], s54 offen lds\n\t"
      "buffer_load_dwordx4 v10, s[84:87], s54 offen offset:1024 lds\n\t"
      "v_mfma_f32_16x16x32_bf16 v[16:19], v[64:67], a[224:227], 0\n\t"
      "v_cvt_pk_bf16_f32 v80, v40, v41\n\t"
      "v_cvt_pk_bf16_f32 v81, v42, v43\n\t"
      "v_mfma_f32_16x16x32_bf16 v[20:23], v[68:71], a[224:227], 0\n\t"
      "v_cvt_pk_bf16_f32 v82, v44, v45\n\t"
      "v_cvt_pk_bf16_f32 v83, v46, v47\n\t"
      "v_mfma_f32_16x16x32_bf16 v[24:27], v[72:75], a[224:227], 0\n\t"
      "v_cvt_pk_bf16_f32 v84, v48, v49\n\t"
      "v_cvt_pk_bf16_f32 v85, v50, v51\n\t"
      "v_mfma_f32_16x16x32_bf16 v[28:31], v[76:79], a[224:227], 0\n\t"
      "v_cvt_pk_bf16_f32 v86, v52, v53\n\t"
      "v_cvt_pk_bf16_f32 v87, v54, v55\n\t"
      "v_mfma_f32_4x4x4_16b_bf16 v[40:43], a[184:185], v[88:89], 0\n\t"
      "ds_read_b64_tr_b16 v[104:105], v60 offset:24640\n\t"
      "v_mfma_f32_4x4x4_16b_bf16 v[44:47], a[186:187], v[90:91], 0\n\t"
      "ds_read_b64_tr_b16 v[106:107], v60 offset:25408\n\t"
      "v_mfma_f32_4x4x4_16b_bf16 v[48:51], a[184:185], v[92:93], 0\n\t"
      "ds_read_b64_tr_b16 v[108:109], v61 offset:24640\n\t"
      "v_mfma_f32_4x4x4_16b_bf16 v[52:55], a[186:187], v[94:95], 0\n\t"
      "ds_read_b64_tr_b16 v[110:111], v61 offset:25408\n\t"
      "buffer_load_dwordx4 v11, s[84:87], s54 offen offset:2048 lds\n\t"
      "s_add_u32 m0, s17, 89600\n\t"
      "s_mov_b32 s54, 135168\n\t"
      "buffer_load_dwordx4 v9, s[84:87], s54 offen lds\n\t"
      "v_mfma_f32_16x16x32_bf16 v[32:35], a[60:63], a[224:227], 0\n\t"
      "v_cvt_pk_bf16_f32 v88, v40, v41\n\t"
      "v_cvt_pk_bf16_f32 v89, v42, v43\n\t"
      "v_mfma_f32_16x16x32_bf16 v[36:39], a[64:67], a[224:227], 0\n\t"
      "v_cvt_pk_bf16_f32 v90, v44, v45\n\t"
      "v_cvt_pk_bf16_f32 v91, v46, v47\n\t"
      "s_waitcnt lgkmcnt(9)\n\t"
      "v_mfma_f32_16x16x32_bf16 a[156:159], v[64:67], a[228:231], 0\n\t"
      "v_cvt_pk_bf16_f32 v92, v48, v49\n\t"
      "v_cvt_pk_bf16_f32 v93, v50, v51\n\t"
      "v_mfma_f32_16x16x32_bf16 a[160:163], v[68:71], a[228:231], 0\n\t"
      "v_cvt_pk_bf16_f32 v94, v52, v53\n\t"
      "v_cvt_pk_bf16_f32 v95, v54, v55\n\t"
      "ds_read_b128 a[232:235], v57 offset:25088\n\t"
      "v_mfma_f32_4x4x4_16b_bf16 v[40:43], a[184:185], a[68:69], 0\n\t"
      "ds_read_b64_tr_b16 a[76:77], v60 offset:24704\n\t"
      "v_mfma_f32_4x4x4_16b_bf16 v[44:47], a[186:187], a[70:71], 0\n\t"
      "ds_read_b64_tr_b16 a[78:79], v60 offset:25472\n\t"
      "v_mfma_f32_4x4x4_16b_bf16 v[48:51], a[184:185], a[72:73], 0\n\t"
      "ds_read_b64_tr_b16 a[80:81], v61 offset:24704\n\t"
      "v_mfma_f32_4x4x4_16b_bf16 v[52:55], a[186:187], a[74:75], 0\n\t"
      "ds_read_b64_tr_b16 a[82:83], v61 offset:25472\n\t"
      "buffer_load_dwordx4 v10, s[84:87], s54 offen offset:1024 lds\n\t"
      "buffer_load_dwordx4 v11, s[84:87], s54 offen offset:2048 lds\n\t"
      "ds_read_b128 a[236:239], v57 offset:37376\n\t"
      "v_mfma_f32_16x16x32_bf16 a[164:167], v[72:75], a[228:231], 0\n\t"
      "v_cvt_pk_bf16_f32 v40, v40, v41\n\t"
      "v_cvt_pk_bf16_f32 v41, v42, v43\n\t"
      "v_mfma_f32_16x16x32_bf16 a[168:171], v[76:79], a[228:231], 0\n\t"
      "v_cvt_pk_bf16_f32 v44, v44, v45\n\t"
      "v_cvt_pk_bf16_f32 v45, v46, v47\n\t"
      "v_mfma_f32_16x16x32_bf16 a[172:175], a[60:63], a[228:231], 0\n\t"
      "v_cvt_pk_bf16_f32 v48, v48, v49\n\t"
      "v_cvt_pk_bf16_f32 v49, v50, v51\n\t"
      "v_mfma_f32_16x16x32_bf16 a[176:179], a[64:67], a[228:231], 0\n\t"
      "v_cvt_pk_bf16_f32 v52, v52, v53\n\t"
      "v_cvt_pk_bf16_f32 v53, v54, v55\n\t"
      "v_accvgpr_write_b32 a68, v40\n\t"
      "v_accvgpr_write_b32 a69, v41\n\t"
      "v_accvgpr_write_b32 a70, v44\n\t"
      "v_accvgpr_write_b32 a71, v45\n\t"
      "v_accvgpr_write_b32 a72, v48\n\t"
      "v_accvgpr_write_b32 a73, v49\n\t"
      "v_accvgpr_write_b32 a74, v52\n\t"
      "v_accvgpr_write_b32 a75, v53\n\t"
      "s_waitcnt vmcnt(12)\n\t"
      "s_waitcnt lgkmcnt(1)\n\t"
      "; step\n\t"
      "v_mfma_f32_4x4x4_16b_bf16 v[40:43], a[180:181], v[96:97], 0\n\t"
      "ds_read_b128 a[184:187], v14 offset:768\n\t"
      "v_mfma_f32_4x4x4_16b_bf16 v[44:47], a[182:183], v[98:99], 0\n\t"
      "ds_read_b64_tr_b16 v[112:113], v60 offset:49152\n\t"
      "v_mfma_f32_4x4x4_16b_bf16 v[48:51], a[180:181], v[100:101], 0\n\t"
      "ds_read_b64_tr_b16 v[114:115], v60 offset:49920\n\t"
      "v_mfma_f32_4x4x4_16b_bf16 v[52:55], a[182:183], v[102:103], 0\n\t"
      "ds_read_b64_tr_b16 v[116:117], v61 offset:49152\n\t"
      "ds_read_b64_tr_b16 v[118:119], v61 offset:49920\n\t"
      "s_add_u32 m0, s17, 111104\n\t"
      "s_mov_b32 s54, 147456\n\t"
      "buffer_load_dwordx4 v9, s[84:87], s54 offen lds\n\t"
      "buffer_load_dwordx4 v10, s[84:87], s54 offen offset:1024 lds\n\t"
      "v_mfma_f32_16x16x32_bf16 v[16:19], v[80:83], a[232:235], v[16:19]\n\t"
      "v_cvt_pk_bf16_f32 v96, v40, v41\n\t"
      "v_cvt_pk_bf16_f32 v97, v42, v43\n\t"
      "v_mfma_f32_16x16x32_bf16 v[20:23], v[84:87], a[232:235], v[20:23]\n\t"
      "v_cvt_pk_bf16_f32 v98, v44, v45\n\t"
      "v_cvt_pk_bf16_f32 v99, v46, v47\n\t"
      "v_mfma_f32_16x16x32_bf16 v[24:27], v[88:91], a[232:235], v[24:27]\n\t"
      "v_cvt_pk_bf16_f32 v100, v48, v49\n\t"
      "v_cvt_pk_bf16_f32 v101, v50, v51\n\t"
      "v_mfma_f32_16x16x32_bf16 v[28:31], v[92:95], a[232:235], v[28:31]\n\t"
      "v_cvt_pk_bf16_f32 v102, v52, v53\n\t"
      "v_cvt_pk_bf16_f32 v103, v54, v55\n\t"
      "v_mfma_f32_4x4x4_16b_bf16 v[40:43], a[180:181], v[104:105], 0\n\t"
      "ds_read_b64_tr_b16 v[120:121], v60 offset:49216\n\t"
      "v_mfma_f32_4x4x4_16b_bf16 v[44:47], a[182:183], v[106:107], 0\n\t"
      "ds_read_b64_tr_b16 v[122:123], v60 offset:49984\n\t"
      "v_mfma_f32_4x4x4_16b_bf16 v[48:51], a[180:181], v[108:109], 0\n\t"
      "ds_read_b64_tr_b16 v[124:125], v61 offset:49216\n\t"
      "v_mfma_f32_4x4x4_16b_bf16 v[52:55], a[182:183], v[110:111], 0\n\t"
      "ds_read_b64_tr_b16 v[126:127], v61 offset:49984\n\t"
      "buffer_load_dwordx4 v11, s[84:87], s54 offen offset:2048 lds\n\t"
      "s_add_u32 m0, s17, 114176\n\t"
      "s_mov_b32 s54, 159744\n\t"
      "buffer_load_dwordx4 v9, s[84:87], s54 offen lds\n\t"
      "v_mfma_f32_16x16x32_bf16 v[32:35], a[68:71], a[232:235], v[32:35]\n\t"
      "v_cvt_pk_bf16_f32 v104, v40, v41\n\t"
      "v_cvt_pk_bf16_f32 v105, v42, v43\n\t"
      "v_mfma_f32_16x16x32_bf16 v[36:39], a[72:75], a[232:235], v[36:39]\n\t"
      "v_cvt_pk_bf16_f32 v106, v44, v45\n\t"
      "v_cvt_pk_bf16_f32 v107, v46, v47\n\t"
      "s_waitcnt lgkmcnt(9)\n\t"
      "v_mfma_f32_16x16x32_bf16 a[156:159], v[80:83], a[236:239], a[156:159]\n\t"
      "v_cvt_pk_bf16_f32 v108, v48, v49\n\t"
      "v_cvt_pk_bf16_f32 v109, v50, v51\n\t"
      "v_mfma_f32_16x16x32_bf16 a[160:163], v[84:87], a[236:239], a[160:163]\n\t"
      "v_cvt_pk_bf16_f32 v110, v52, v53\n\t"
      "v_cvt_pk_bf16_f32 v111, v54, v55\n\t"
      "ds_read_b128 a[240:243], v58 offset:25088\n\t"
      "v_mfma_f32_4x4x4_16b_bf16 v[40:43], a[180:181], a[76:77], 0\n\t"
      "ds_read_b64_tr_b16 a[84:85], v60 offset:49280\n\t"
      "v_mfma_f32_4x4x4_16b_bf16 v[44:47], a[182:183], a[78:79], 0\n\t"
      "ds_read_b64_tr_b16 a[86:87], v60 offset:50048\n\t"
      "v_mfma_f32_4x4x4_16b_bf16 v[48:51], a[180:181], a[80:81], 0\n\t"
      "ds_read_b64_tr_b16 a[88:89], v61 offset:49280\n\t"
      "v_mfma_f32_4x4x4_16b_bf16 v[52:55], a[182:183], a[82:83], 0\n\t"
      "ds_read_b64_tr_b16 a[90:91], v61 offset:50048\n\t"
      "buffer_load_dwordx4 v10, s[84:87], s54 offen offset:1024 lds\n\t"
      "buffer_load_dwordx4 v11, s[84:87], s54 offen offset:2048 lds\n\t"
      "ds_read_b128 a[244:247], v58 offset:37376\n\t"
      "v_mfma_f32_16x16x32_bf16 a[164:167], v[88:91], a[236:239], a[164:167]\n\t"
      "v_cvt_pk_bf16_f32 v40, v40, v41\n\t"
      "v_cvt_pk_bf16_f32 v41, v42, v43\n\t"
      "v_mfma_f32_16x16x32_bf16 a[168:171], v[92:95], a[236:239], a[168:171]\n\t"
      "v_cvt_pk_bf16_f32 v44, v44, v45\n\t"
      "v_cvt_pk_bf16_f32 v45, v46, v47\n\t"
      "v_mfma_f32_16x16x32_bf16 a[172:175], a[68:71], a[236:239], a[172:175]\n\t"
      "v_cvt_pk_bf16_f32 v48, v48, v49\n\t"
      "v_cvt_pk_bf16_f32 v49, v50, v51\n\t"
      "v_mfma_f32_16x16x32_bf16 a[176:179], a[72:75], a[236:239], a[176:179]\n\t"
      "v_cvt_pk_bf16_f32 v52, v52, v53\n\t"
      "v_cvt_pk_bf16_f32 v53, v54, v55\n\t"
      "v_accvgpr_write_b32 a76, v40\n\t"
      "v_accvgpr_write_b32 a77, v41\n\t"
      "v_accvgpr_write_b32 a78, v44\n\t"
      "v_accvgpr_write_b32 a79, v45\n\t"
      "v_accvgpr_write_b32 a80, v48\n\t"
      "v_accvgpr_write_b32 a81, v49\n\t"
      "v_accvgpr_write_b32 a82, v52\n\t"
      "v_accvgpr_write_b32 a83, v53\n\t"
      "s_waitcnt vmcnt(12)\n\t"
      "s_waitcnt lgkmcnt(1)\n\t"
      "; step\n\t"
      "v_mfma_f32_4x4x4_16b_bf16 v[40:43], a[184:185], v[112:113], 0\n\t"
      "ds_read_b128 a[180:183], v14 offset:1024\n\t"
      "v_mfma_f32_4x4x4_16b_bf16 v[44:47], a[186:187], v[114:115], 0\n\t"
      "ds_read_b64_tr_b16 v[128:129], v12 offset:0\n\t"
      "v_mfma_f32_4x4x4_16b_bf16 v[48:51], a[184:185], v[116:117], 0\n\t"
      "ds_read_b64_tr_b16 v[130:131], v12 offset:768\n\t"
      "v_mfma_f32_4x4x4_16b_bf16 v[52:55], a[186:187], v[118:119], 0\n\t"
      "ds_read_b64_tr_b16 v[132:133], v13 offset:0\n\t"
      "ds_read_b64_tr_b16 v[134:135], v13 offset:768\n\t"
      "s_add_u32 m0, s17, 135680\n\t"
      "s_mov_b32 s54, 172032\n\t"
      "buffer_load_dwordx4 v9, s[84:87], s54 offen lds\n\t"
      "buffer_load_dwordx4 v10, s[84:87], s54 offen offset:1024 lds\n\t"
      "v_mfma_f32_16x16x32_bf16 v[16:19], v[96:99], a[240:243], v[16:19]\n\t"
      "v_cvt_pk_bf16_f32 v112, v40, v41\n\t"
      "v_cvt_pk_bf16_f32 v113, v42, v43\n\t"
      "v_mfma_f32_16x16x32_bf16 v[20:23], v[100:103], a[240:243], v[20:23]\n\t"
      "v_cvt_pk_bf16_f32 v114, v44, v45\n\t"
      "v_cvt_pk_bf16_f32 v115, v46, v47\n\t"
      "v_mfma_f32_16x16x32_bf16 v[24:27], v[104:107], a[240:243], v[24:27]\n\t"
      "v_cvt_pk_bf16_f32 v116, v48, v49\n\t"
      "v_cvt_pk_bf16_f32 v117, v50, v51\n\t"
      "v_mfma_f32_16x16x32_bf16 v[28:31], v[108:111], a[240:243], v[28:31]\n\t"
      "v_cvt_pk_bf16_f32 v118, v52, v53\n\t"
      "v_cvt_pk_bf16_f32 v119, v54, v55\n\t"
      "v_mfma_f32_4x4x4_16b_bf16 v[40:43], a[184:185], v[120:121], 0\n\t"
      "ds_read_b64_tr_b16 v[136:137], v12 offset:64\n\t"
      "v_mfma_f32_4x4x4_16b_bf16 v[44:47], a[186:187], v[122:123], 0\n\t"
      "ds_read_b64_tr_b16 v[138:139], v12 offset:832\n\t"
      "v_mfma_f32_4x4x4_16b_bf16 v[48:51], a[184:185], v[124:125], 0\n\t"
      "ds_read_b64_tr_b16 v[140:141], v13 offset:64\n\t"
      "v_mfma_f32_4x4x4_16b_bf16 v[52:55], a[186:187], v[126:127], 0\n\t"
      "ds_read_b64_tr_b16 v[142:143], v13 offset:832\n\t"
      "buffer_load_dwordx4 v11, s[84:87], s54 offen offset:2048 lds\n\t"
      "s_add_u32 m0, s17, 138752\n\t"
      "s_mov_b32 s54, 184320\n\t"
      "buffer_load_dwordx4 v9, s[84:87], s54 offen lds\n\t"
      "v_mfma_f32_16x16x32_bf16 v[32:35], a[76:79], a[240:243], v[32:35]\n\t"
      "v_cvt_pk_bf16_f32 v120, v40, v41\n\t"
      "v_cvt_pk_bf16_f32 v121, v42, v43\n\t"
      "v_mfma_f32_16x16x32_bf16 v[36:39], a[80:83], a[240:243], v[36:39]\n\t"
      "v_cvt_pk_bf16_f32 v122, v44, v45\n\t"
      "v_cvt_pk_bf16_f32 v123, v46, v47\n\t"
      "s_waitcnt lgkmcnt(9)\n\t"
      "v_mfma_f32_16x16x32_bf16 a[156:159], v[96:99], a[244:247], a[156:159]\n\t"
      "v_cvt_pk_bf16_f32 v124, v48, v49\n\t"
      "v_cvt_pk_bf16_f32 v125, v50, v51\n\t"
      "v_mfma_f32_16x16x32_bf16 a[160:163], v[100:103], a[244:247], a[160:163]\n\t"
      "v_cvt_pk_bf16_f32 v126, v52, v53\n\t"
      "v_cvt_pk_bf16_f32 v127, v54, v55\n\t"
      "ds_read_b128 a[224:227], v59 offset:25088\n\t"
      "v_mfma_f32_4x4x4_16b_bf16 v[40:43], a[184:185], a[84:85], 0\n\t"
      "ds_read_b64_tr_b16 a[92:93], v12 offset:128\n\t"
      "v_mfma_f32_4x4x4_16b_bf16 v[44:47], a[186:187], a[86:87], 0\n\t"
      "ds_read_b64_tr_b16 a[94:95], v12 offset:896\n\t"
      "v_mfma_f32_4x4x4_16b_bf16 v[48:51], a[184:185], a[88:89], 0\n\t"
      "ds_read_b64_tr_b16 a[96:97], v13 offset:128\n\t"
      "v_mfma_f32_4x4x4_16b_bf16 v[52:55], a[186:187], a[90:91], 0\n\t"
      "ds_read_b64_tr_b16 a[98:99], v13 offset:896\n\t"
      "buffer_load_dwordx4 v10, s[84:87], s54 offen offset:1024 lds\n\t"
      "buffer_load_dwordx4 v11, s[84:87], s54 offen offset:2048 lds\n\t"
      "ds_read_b128 a[228:231], v59 offset:37376\n\t"
      "v_mfma_f32_16x16x32_bf16 a[164:167], v[104:107], a[244:247], a[164:167]\n\t"
      "v_cvt_pk_bf16_f32 v40, v40, v41\n\t"
      "v_cvt_pk_bf16_f32 v41, v42, v43\n\t"
      "v_mfma_f32_16x16x32_bf16 a[168:171], v[108:111], a[244:247], a[168:171]\n\t"
      "v_cvt_pk_bf16_f32 v44, v44, v45\n\t"
      "v_cvt_pk_bf16_f32 v45, v46, v47\n\t"
      "v_mfma_f32_16x16x32_bf16 a[172:175], a[76:79], a[244:247], a[172:175]\n\t"
      "v_cvt_pk_bf16_f32 v48, v48, v49\n\t"
      "v_cvt_pk_bf16_f32 v49, v50, v51\n\t"
      "v_mfma_f32_16x16x32_bf16 a[176:179], a[80:83], a[244:247], a[176:179]\n\t"
      "v_cvt_pk_bf16_f32 v52, v52, v53\n\t"
      "v_cvt_pk_bf16_f32 v53, v54, v55\n\t"
      "v_accvgpr_write_b32 a84, v40\n\t"
      "v_accvgpr_write_b32 a85, v41\n\t"
      "v_accvgpr_write_b32 a86, v44\n\t"
      "v_accvgpr_write_b32 a87, v45\n\t"
      "v_accvgpr_write_b32 a88, v48\n\t"
      "v_accvgpr_write_b32 a89, v49\n\t"
      "v_accvgpr_write_b32 a90, v52\n\t"
      "v_accvgpr_write_b32 a91, v53\n\t"
      "s_waitcnt vmcnt(12)\n\t"
      "s_waitcnt lgkmcnt(1)\n\t"
      "; step\n\t"
      "v_mfma_f32_4x4x4_16b_bf16 v[40:43], a[180:181], v[128:129], 0\n\t"
      "ds_read_b128 a[184:187], v14 offset:1280\n\t"
      "v_mfma_f32_4x4x4_16b_bf16 v[44:47], a[182:183], v[130:131], 0\n\t"
      "ds_read_b64_tr_b16 v[144:145], v60 offset:0\n\t"
      "v_mfma_f32_4x4x4_16b_bf16 v[48:51], a[180:181], v[132:133], 0\n\t"
      "ds_read_b64_tr_b16 v[146:147], v60 offset:768\n\t"
      "v_mfma_f32_4x4x4_16b_bf16 v[52:55], a[182:183], v[134:135], 0\n\t"
      "ds_read_b64_tr_b16 v[148:149], v61 offset:0\n\t"
      "ds_read_b64_tr_b16 v[150:151], v61 offset:768\n\t"
      "s_add_u32 m0, s17, 0\n\t"
      "s_mov_b32 s54, 196608\n\t"
      "buffer_load_dwordx4 v9, s[84:87], s54 offen lds\n\t"
      "buffer_load_dwordx4 v10, s[84:87], s54 offen offset:1024 lds\n\t"
      "v_mfma_f32_16x16x32_bf16 v[16:19], v[112:115], a[224:227], v[16:19]\n\t"
      "v_cvt_pk_bf16_f32 v128, v40, v41\n\t"
      "v_cvt_pk_bf16_f32 v129, v42, v43\n\t"
      "v_mfma_f32_16x16x32_bf16 v[20:23], v[116:119], a[224:227], v[20:23]\n\t"
      "v_cvt_pk_bf16_f32 v130, v44, v45\n\t"
      "v_cvt_pk_bf16_f32 v131, v46, v47\n\t"
      "v_mfma_f32_16x16x32_bf16 v[24:27], v[120:123], a[224:227], v[24:27]\n\t"
      "v_cvt_pk_bf16_f32 v132, v48, v49\n\t"
      "v_cvt_pk_bf16_f32 v133, v50, v51\n\t"
      "v_mfma_f32_16x16x32_bf16 v[28:31], v[124:127], a[224:227], v[28:31]\n\t"
      "v_cvt_pk_bf16_f32 v134, v52, v53\n\t"
      "v_cvt_pk_bf16_f32 v135, v54, v55\n\t"
      "v_mfma_f32_4x4x4_16b_bf16 v[40:43], a[180:181], v[136:137], 0\n\t"
      "ds_read_b64_tr_b16 v[152:153], v60 offset:64\n\t"
      "v_mfma_f32_4x4x4_16b_bf16 v[44:47], a[182:183], v[138:139], 0\n\t"
      "ds_read_b64_tr_b16 v[154:155], v60 offset:832\n\t"
      "v_mfma_f32_4x4x4_16b_bf16 v[48:51], a[180:181], v[140:141], 0\n\t"
      "ds_read_b64_tr_b16 v[156:157], v61 offset:64\n\t"
      "v_mfma_f32_4x4x4_16b_bf16 v[52:55], a[182:183], v[142:143], 0\n\t"
      "ds_read_b64_tr_b16 v[158:159], v61 offset:832\n\t"
      "buffer_load_dwordx4 v11, s[84:87], s54 offen offset:2048 lds\n\t"
      "s_add_u32 m0, s17, 3072\n\t"
      "s_mov_b32 s54, 208896\n\t"
      "buffer_load_dwordx4 v9, s[84:87], s54 offen lds\n\t"
      "v_mfma_f32_16x16x32_bf16 v[32:35], a[84:87], a[224:227], v[32:35]\n\t"
      "v_cvt_pk_bf16_f32 v136, v40, v41\n\t"
      "v_cvt_pk_bf16_f32 v137, v42, v43\n\t"
      "v_mfma_f32_16x16x32_bf16 v[36:39], a[88:91], a[224:227], v[36:39]\n\t"
      "v_cvt_pk_bf16_f32 v138, v44, v45\n\t"
      "v_cvt_pk_bf16_f32 v139, v46, v47\n\t"
      "s_waitcnt lgkmcnt(9)\n\t"
      "v_mfma_f32_16x16x32_bf16 a[156:159], v[112:115], a[228:231], a[156:159]\n\t"
      "v_cvt_pk_bf16_f32 v140, v48, v49\n\t"
      "v_cvt_pk_bf16_f32 v141, v50, v51\n\t"
      "v_mfma_f32_16x16x32_bf16 a[160:163], v[116:119], a[228:231], a[160:163]\n\t"
      "v_cvt_pk_bf16_f32 v142, v52, v53\n\t"
      "v_cvt_pk_bf16_f32 v143, v54, v55\n\t"
      "ds_read_b128 a[232:235], v56 offset:25344\n\t"
      "v_mfma_f32_4x4x4_16b_bf16 v[40:43], a[180:181], a[92:93], 0\n\t"
      "ds_read_b64_tr_b16 a[100:101], v60 offset:128\n\t"
      "v_mfma_f32_4x4x4_16b_bf16 v[44:47], a[182:183], a[94:95], 0\n\t"
      "ds_read_b64_tr_b16 a[102:103], v60 offset:896\n\t"
      "v_mfma_f32_4x4x4_16b_bf16 v[48:51], a[180:181], a[96:97], 0\n\t"
      "ds_read_b64_tr_b16 a[104:105], v61 offset:128\n\t"
      "v_mfma_f32_4x4x4_16b_bf16 v[52:55], a[182:183], a[98:99], 0\n\t"
      "ds_read_b64_tr_b16 a[106:107], v61 offset:896\n\t"
      "buffer_load_dwordx4 v10, s[84:87], s54 offen offset:1024 lds\n\t"
      "buffer_load_dwordx4 v11, s[84:87], s54 offen offset:2048 lds\n\t"
      "ds_read_b128 a[236:239], v56 offset:37632\n\t"
      "v_mfma_f32_16x16x32_bf16 a[164:167], v[120:123], a[228:231], a[164:167]\n\t"
      "v_cvt_pk_bf16_f32 v40, v40, v41\n\t"
      "v_cvt_pk_bf16_f32 v41, v42, v43\n\t"
      "v_mfma_f32_16x16x32_bf16 a[168:171], v[124:127], a[228:231], a[168:171]\n\t"
      "v_cvt_pk_bf16_f32 v44, v44, v45\n\t"
      "v_cvt_pk_bf16_f32 v45, v46, v47\n\t"
      "v_mfma_f32_16x16x32_bf16 a[172:175], a[84:87], a[228:231], a[172:175]\n\t"
      "v_cvt_pk_bf16_f32 v48, v48, v49\n\t"
      "v_cvt_pk_bf16_f32 v49, v50, v51\n\t"
      "v_mfma_f32_16x16x32_bf16 a[176:179], a[88:91], a[228:231], a[176:179]\n\t"
      "v_cvt_pk_bf16_f32 v52, v52, v53\n\t"
      "v_cvt_pk_bf16_f32 v53, v54, v55\n\t"
      "v_accvgpr_write_b32 a92, v40\n\t"
      "v_accvgpr_write_b32 a93, v41\n\t"
      "v_accvgpr_write_b32 a94, v44\n\t"
      "v_accvgpr_write_b32 a95, v45\n\t"
      "v_accvgpr_write_b32 a96, v48\n\t"
      "v_accvgpr_write_b32 a97, v49\n\t"
      "v_accvgpr_write_b32 a98, v52\n\t"
      "v_accvgpr_write_b32 a99, v53\n\t"
      "s_waitcnt vmcnt(12)\n\t"
      "s_waitcnt lgkmcnt(1)\n\t"
      "; step\n\t"
      "v_mfma_f32_4x4x4_16b_bf16 v[40:43], a[184:185], v[144:145], 0\n\t"
      "ds_read_b128 a[180:183], v14 offset:1536\n\t"
      "v_mfma_f32_4x4x4_16b_bf16 v[44:47], a[186:187], v[146:147], 0\n\t"
      "ds_read_b64_tr_b16 v[160:161], v60 offset:24576\n\t"
      "v_mfma_f32_4x4x4_16b_bf16 v[48:51], a[184:185], v[148:149], 0\n\t"
      "ds_read_b64_tr_b16 v[162:163], v60 offset:25344\n\t"
      "v_mfma_f32_4x4x4_16b_bf16 v[52:55], a[186:187], v[150:151], 0\n\t"
      "ds_read_b64_tr_b16 v[164:165], v61 offset:24576\n\t"
      "ds_read_b64_tr_b16 v[166:167], v61 offset:25344\n\t"
      "s_add_u32 m0, s17, 86528\n\t"
      "s_mov_b32 s54, 221184\n\t"
      "buffer_load_dwordx4 v9, s[84:87], s54 offen lds\n\t"
      "buffer_load_dwordx4 v10, s[84:87], s54 offen offset:1024 lds\n\t"
      "v_mfma_f32_16x16x32_bf16 v[16:19], v[128:131], a[232:235], v[16:19]\n\t"
      "v_cvt_pk_bf16_f32 v144, v40, v41\n\t"
      "v_cvt_pk_bf16_f32 v145, v42, v43\n\t"
      "v_mfma_f32_16x16x32_bf16 v[20:23], v[132:135], a[232:235], v[20:23]\n\t"
      "v_cvt_pk_bf16_f32 v146, v44, v45\n\t"
      "v_cvt_pk_bf16_f32 v147, v46, v47\n\t"
      "v_mfma_f32_16x16x32_bf16 v[24:27], v[136:139], a[232:235], v[24:27]\n\t"
      "v_cvt_pk_bf16_f32 v148, v48, v49\n\t"
      "v_cvt_pk_bf16_f32 v149, v50, v51\n\t"
      "v_mfma_f32_16x16x32_bf16 v[28:31], v[140:143], a[232:235], v[28:31]\n\t"
      "v_cvt_pk_bf16_f32 v150, v52, v53\n\t"
      "v_cvt_pk_bf16_f32 v151, v54, v55\n\t"
      "v_mfma_f32_4x4x4_16b_bf16 v[40:43], a[184:185], v[152:153], 0\n\t"
      "ds_read_b64_tr_b16 v[168:169], v60 offset:24640\n\t"
      "v_mfma_f32_4x4x4_16b_bf16 v[44:47], a[186:187], v[154:155], 0\n\t"
      "ds_read_b64_tr_b16 v[170:171], v60 offset:25408\n\t"
      "v_mfma_f32_4x4x4_16b_bf16 v[48:51], a[184:185], v[156:157], 0\n\t"
      "ds_read_b64_tr_b16 v[172:173], v61 offset:24640\n\t"
      "v_mfma_f32_4x4x4_16b_bf16 v[52:55], a[186:187], v[158:159], 0\n\t"
      "ds_read_b64_tr_b16 v[174:175], v61 offset:25408\n\t"
      "buffer_load_dwordx4 v11, s[84:87], s54 offen offset:2048 lds\n\t"
      "s_add_u32 m0, s17, 89600\n\t"
      "s_mov_b32 s54, 233472\n\t"
      "buffer_load_dwordx4 v9, s[84:87], s54 offen lds\n\t"
      "v_mfma_f32_16x16x32_bf16 v[32:35], a[92:95], a[232:235], v[32:35]\n\t"
      "v_cvt_pk_bf16_f32 v152, v40, v41\n\t"
      "v_cvt_pk_bf16_f32 v153, v42, v43\n\t"
      "v_mfma_f32_16x16x32_bf16 v[36:39], a[96:99], a[232:235], v[36:39]\n\t"
      "v_cvt_pk_bf16_f32 v154, v44, v45\n\t"
      "v_cvt_pk_bf16_f32 v155, v46, v47\n\t"
      "s_waitcnt lgkmcnt(9)\n\t"
      "v_mfma_f32_16x16x32_bf16 a[156:159], v[128:131], a[236:239], a[156:159]\n\t"
      "v_cvt_pk_bf16_f32 v156, v48, v49\n\t"
      "v_cvt_pk_bf16_f32 v157, v50, v51\n\t"
      "v_mfma_f32_16x16x32_bf16 a[160:163], v[132:135], a[236:239], a[160:163]\n\t"
      "v_cvt_pk_bf16_f32 v158, v52, v53\n\t"
      "v_cvt_pk_bf16_f32 v159, v54, v55\n\t"
      "ds_read_b128 a[240:243], v57 offset:25344\n\t"
      "v_mfma_f32_4x4x4_16b_bf16 v[40:43], a[184:185], a[100:101], 0\n\t"
      "ds_read_b64_tr_b16 a[108:109], v60 offset:24704\n\t"
      "v_mfma_f32_4x4x4_16b_bf16 v[44:47], a[186:187], a[102:103], 0\n\t"
      "ds_read_b64_tr_b16 a[110:111], v60 offset:25472\n\t"
      "v_mfma_f32_4x4x4_16b_bf16 v[48:51], a[184:185], a[104:105], 0\n\t"
      "ds_read_b64_tr_b16 a[112:113], v61 offset:24704\n\t"
      "v_mfma_f32_4x4x4_16b_bf16 v[52:55], a[186:187], a[106:107], 0\n\t"
      "ds_read_b64_tr_b16 a[114:115], v61 offset:25472\n\t"
      "buffer_load_dwordx4 v10, s[84:87], s54 offen offset:1024 lds\n\t"
      "buffer_load_dwordx4 v11, s[84:87], s54 offen offset:2048 lds\n\t"
      "ds_read_b128 a[244:247], v57 offset:37632\n\t"
      "v_mfma_f32_16x16x32_bf16 a[164:167], v[136:139], a[236:239], a[164:167]\n\t"
      "v_cvt_pk_bf16_f32 v40, v40, v41\n\t"
      "v_cvt_pk_bf16_f32 v41, v42, v43\n\t"
      "v_mfma_f32_16x16x32_bf16 a[168:171], v[140:143], a[236:239], a[168:171]\n\t"
      "v_cvt_pk_bf16_f32 v44, v44, v45\n\t"
      "v_cvt_pk_bf16_f32 v45, v46, v47\n\t"
      "v_mfma_f32_16x16x32_bf16 a[172:175], a[92:95], a[236:239], a[172:175]\n\t"
      "v_cvt_pk_bf16_f32 v48, v48, v49\n\t"
      "v_cvt_pk_bf16_f32 v49, v50, v51\n\t"
      "v_mfma_f32_16x16x32_bf16 a[176:179], a[96:99], a[236:239], a[176:179]\n\t"
      "v_cvt_pk_bf16_f32 v52, v52, v53\n\t"
      "v_cvt_pk_bf16_f32 v53, v54, v55\n\t"
      "v_accvgpr_write_b32 a100, v40\n\t"
      "v_accvgpr_write_b32 a101, v41\n\t"
      "v_accvgpr_write_b32 a102, v44\n\t"
      "v_accvgpr_write_b32 a103, v45\n\t"
      "v_accvgpr_write_b32 a104, v48\n\t"
      "v_accvgpr_write_b32 a105, v49\n\t"
      "v_accvgpr_write_b32 a106, v52\n\t"
      "v_accvgpr_write_b32 a107, v53\n\t"
      "s_waitcnt vmcnt(12)\n\t"
      "s_waitcnt lgkmcnt(1)\n\t"
      "; step\n\t"
      "v_mfma_f32_4x4x4_16b_bf16 v[40:43], a[180:181], v[160:161], 0\n\t"
      "ds_read_b128 a[184:187], v14 offset:1792\n\t"
      "v_mfma_f32_4x4x4_16b_bf16 v[44:47], a[182:183], v[162:163], 0\n\t"
      "ds_read_b64_tr_b16 v[176:177], v60 offset:49152\n\t"
      "v_mfma_f32_4x4x4_16b_bf16 v[48:51], a[180:181], v[164:165], 0\n\t"
      "ds_read_b64_tr_b16 v[178:179], v60 offset:49920\n\t"
      "v_mfma_f32_4x4x4_16b_bf16 v[52:55], a[182:183], v[166:167], 0\n\t"
      "ds_read_b64_tr_b16 v[180:181], v61 offset:49152\n\t"
      "ds_read_b64_tr_b16 v[182:183], v61 offset:49920\n\t"
      "s_add_u32 m0, s17, 111104\n\t"
      "s_mov_b32 s54, 245760\n\t"
      "buffer_load_dwordx4 v9, s[84:87], s54 offen lds\n\t"
      "buffer_load_dwordx4 v10, s[84:87], s54 offen offset:1024 lds\n\t"
      "v_mfma_f32_16x16x32_bf16 v[16:19], v[144:147], a[240:243], v[16:19]\n\t"
      "v_cvt_pk_bf16_f32 v160, v40, v41\n\t"
      "v_cvt_pk_bf16_f32 v161, v42, v43\n\t"
      "v_mfma_f32_16x16x32_bf16 v[20:23], v[148:151], a[240:243], v[20:23]\n\t"
      "v_cvt_pk_bf16_f32 v162, v44, v45\n\t"
      "v_cvt_pk_bf16_f32 v163, v46, v47\n\t"
      "v_mfma_f32_16x16x32_bf16 v[24:27], v[152:155], a[240:243], v[24:27]\n\t"
      "v_cvt_pk_bf16_f32 v164, v48, v49\n\t"
      "v_cvt_pk_bf16_f32 v165, v50, v51\n\t"
      "v_mfma_f32_16x16x32_bf16 v[28:31], v[156:159], a[240:243], v[28:31]\n\t"
      "v_cvt_pk_bf16_f32 v166, v52, v53\n\t"
      "v_cvt_pk_bf16_f32 v167, v54, v55\n\t"
      "v_mfma_f32_4x4x4_16b_bf16 v[40:43], a[180:181], v[168:169], 0\n\t"
      "ds_read_b64_tr_b16 v[184:185], v60 offset:49216\n\t"
      "v_mfma_f32_4x4x4_16b_bf16 v[44:47], a[182:183], v[170:171], 0\n\t"
      "ds_read_b64_tr_b16 v[186:187], v60 offset:49984\n\t"
      "v_mfma_f32_4x4x4_16b_bf16 v[48:51], a[180:181], v[172:173], 0\n\t"
      "ds_read_b64_tr_b16 v[188:189], v61 offset:49216\n\t"
      "v_mfma_f32_4x4x4_16b_bf16 v[52:55], a[182:183], v[174:175], 0\n\t"
      "ds_read_b64_tr_b16 v[190:191], v61 offset:49984\n\t"
      "buffer_load_dwordx4 v11, s[84:87], s54 offen offset:2048 lds\n\t"
      "s_add_u32 m0, s17, 114176\n\t"
      "s_mov_b32 s54, 258048\n\t"
      "buffer_load_dwordx4 v9, s[84:87], s54 offen lds\n\t"
      "v_mfma_f32_16x16x32_bf16 v[32:35], a[100:103], a[240:243], v[32:35]\n\t"
      "v_cvt_pk_bf16_f32 v168, v40, v41\n\t"
      "v_cvt_pk_bf16_f32 v169, v42, v43\n\t"
      "v_mfma_f32_16x16x32_bf16 v[36:39], a[104:107], a[240:243], v[36:39]\n\t"
      "v_cvt_pk_bf16_f32 v170, v44, v45\n\t"
      "v_cvt_pk_bf16_f32 v171, v46, v47\n\t"
      "s_waitcnt lgkmcnt(9)\n\t"
      "v_mfma_f32_16x16x32_bf16 a[156:159], v[144:147], a[244:247], a[156:159]\n\t"
      "v_cvt_pk_bf16_f32 v172, v48, v49\n\t"
      "v_cvt_pk_bf16_f32 v173, v50, v51\n\t"
      "v_mfma_f32_16x16x32_bf16 a[160:163], v[148:151], a[244:247], a[160:163]\n\t"
      "v_cvt_pk_bf16_f32 v174, v52, v53\n\t"
      "v_cvt_pk_bf16_f32 v175, v54, v55\n\t"
      "ds_read_b128 a[224:227], v58 offset:25344\n\t"
      "v_mfma_f32_4x4x4_16b_bf16 v[40:43], a[180:181], a[108:109], 0\n\t"
      "ds_read_b64_tr_b16 a[116:117], v60 offset:49280\n\t"
      "v_mfma_f32_4x4x4_16b_bf16 v[44:47], a[182:183], a[110:111], 0\n\t"
      "ds_read_b64_tr_b16 a[118:119], v60 offset:50048\n\t"
      "v_mfma_f32_4x4x4_16b_bf16 v[48:51], a[180:181], a[112:113], 0\n\t"
      "ds_read_b64_tr_b16 a[120:121], v61 offset:49280\n\t"
      "v_mfma_f32_4x4x4_16b_bf16 v[52:55], a[182:183], a[114:115], 0\n\t"
      "ds_read_b64_tr_b16 a[122:123], v61 offset:50048\n\t"
      "buffer_load_dwordx4 v10, s[84:87], s54 offen offset:1024 lds\n\t"
      "buffer_load_dwordx4 v11, s[84:87], s54 offen offset:2048 lds\n\t"
      "ds_read_b128 a[228:231], v58 offset:37632\n\t"
      "v_mfma_f32_16x16x32_bf16 a[164:167], v[152:155], a[244:247], a[164:167]\n\t"
      "v_cvt_pk_bf16_f32 v40, v40, v41\n\t"
      "v_cvt_pk_bf16_f32 v41, v42, v43\n\t"
      "v_mfma_f32_16x16x32_bf16 a[168:171], v[156:159], a[244:247], a[168:171]\n\t"
      "v_cvt_pk_bf16_f32 v44, v44, v45\n\t"
      "v_cvt_pk_bf16_f32 v45, v46, v47\n\t"
      "v_mfma_f32_16x16x32_bf16 a[172:175], a[100:103], a[244:247], a[172:175]\n\t"
      "v_cvt_pk_bf16_f32 v48, v48, v49\n\t"
      "v_cvt_pk_bf16_f32 v49, v50, v51\n\t"
      "v_mfma_f32_16x16x32_bf16 a[176:179], a[104:107], a[244:247], a[176:179]\n\t"
      "v_cvt_pk_bf16_f32 v52, v52, v53\n\t"
      "v_cvt_pk_bf16_f32 v53, v54, v55\n\t"
      "v_accvgpr_write_b32 a108, v40\n\t"
      "v_accvgpr_write_b32 a109, v41\n\t"
      "v_accvgpr_write_b32 a110, v44\n\t"
      "v_accvgpr_write_b32 a111, v45\n\t"
      "v_accvgpr_write_b32 a112, v48\n\t"
      "v_accvgpr_write_b32 a113, v49\n\t"
      "v_accvgpr_write_b32 a114, v52\n\t"
      "v_accvgpr_write_b32 a115, v53\n\t"
      "s_waitcnt vmcnt(12)\n\t"
      "s_waitcnt lgkmcnt(1)\n\t"
      "; step\n\t"
      "v_mfma_f32_4x4x4_16b_bf16 v[40:43], a[184:185], v[176:177], 0\n\t"
      "ds_read_b128 a[180:183], v14 offset:2048\n\t"
      "v_mfma_f32_4x4x4_16b_bf16 v[44:47], a[186:187], v[178:179], 0\n\t"
      "ds_read_b64_tr_b16 v[192:193], v12 offset:0\n\t"
      "v_mfma_f32_4x4x4_16b_bf16 v[48:51], a[184:185], v[180:181], 0\n\t"
      "ds_read_b64_tr_b16 v[194:195], v12 offset:768\n\t"
      "v_mfma_f32_4x4x4_16b_bf16 v[52:55], a[186:187], v[182:183], 0\n\t"
      "ds_read_b64_tr_b16 v[196:197], v13 offset:0\n\t"
      "ds_read_b64_tr_b16 v[198:199], v13 offset:768\n\t"
      "s_add_u32 m0, s17, 135680\n\t"
      "s_mov_b32 s54, 270336\n\t"
      "buffer_load_dwordx4 v9, s[84:87], s54 offen lds\n\t"
      "buffer_load_dwordx4 v10, s[84:87], s54 offen offset:1024 lds\n\t"
      "v_mfma_f32_16x16x32_bf16 v[16:19], v[160:163], a[224:227], v[16:19]\n\t"
      "v_cvt_pk_bf16_f32 v176, v40, v41\n\t"
      "v_cvt_pk_bf16_f32 v177, v42, v43\n\t"
      "v_mfma_f32_16x16x32_bf16 v[20:23], v[164:167], a[224:227], v[20:23]\n\t"
      "v_cvt_pk_bf16_f32 v178, v44, v45\n\t"
      "v_cvt_pk_bf16_f32 v179, v46, v47\n\t"
      "v_mfma_f32_16x16x32_bf16 v[24:27], v[168:171], a[224:227], v[24:27]\n\t"
      "v_cvt_pk_bf16_f32 v180, v48, v49\n\t"
      "v_cvt_pk_bf16_f32 v181, v50, v51\n\t"
      "v_mfma_f32_16x16x32_bf16 v[28:31], v[172:175], a[224:227], v[28:31]\n\t"
      "v_cvt_pk_bf16_f32 v182, v52, v53\n\t"
      "v_cvt_pk_bf16_f32 v183, v54, v55\n\t"
      "v_mfma_f32_4x4x4_16b_bf16 v[40:43], a[184:185], v[184:185], 0\n\t"
      "ds_read_b64_tr_b16 v[200:201], v12 offset:64\n\t"
      "v_mfma_f32_4x4x4_16b_bf16 v[44:47], a[186:187], v[186:187], 0\n\t"
      "ds_read_b64_tr_b16 v[202:203], v12 offset:832\n\t"
      "v_mfma_f32_4x4x4_16b_bf16 v[48:51], a[184:185], v[188:189], 0\n\t"
      "ds_read_b64_tr_b16 v[204:205], v13 offset:64\n\t"
      "v_mfma_f32_4x4x4_16b_bf16 v[52:55], a[186:187], v[190:191], 0\n\t"
      "ds_read_b64_tr_b16 v[206:207], v13 offset:832\n\t"
      "buffer_load_dwordx4 v11, s[84:87], s54 offen offset:2048 lds\n\t"
      "s_add_u32 m0, s17, 138752\n\t"
      "s_mov_b32 s54, 282624\n\t"
      "buffer_load_dwordx4 v9, s[84:87], s54 offen lds\n\t"
      "v_mfma_f32_16x16x32_bf16 v[32:35], a[108:111], a[224:227], v[32:35]\n\t"
      "v_cvt_pk_bf16_f32 v184, v40, v41\n\t"
      "v_cvt_pk_bf16_f32 v185, v42, v43\n\t"
      "v_mfma_f32_16x16x32_bf16 v[36:39], a[112:115], a[224:227], v[36:39]\n\t"
      "v_cvt_pk_bf16_f32 v186, v44, v45\n\t"
      "v_cvt_pk_bf16_f32 v187, v46, v47\n\t"
      "s_waitcnt lgkmcnt(9)\n\t"
      "v_mfma_f32_16x16x32_bf16 a[156:159], v[160:163], a[228:231], a[156:159]\n\t"
      "v_cvt_pk_bf16_f32 v188, v48, v49\n\t"
      "v_cvt_pk_bf16_f32 v189, v50, v51\n\t"
      "v_mfma_f32_16x16x32_bf16 a[160:163], v[164:167], a[228:231], a[160:163]\n\t"
      "v_cvt_pk_bf16_f32 v190, v52, v53\n\t"
      "v_cvt_pk_bf16_f32 v191, v54, v55\n\t"
      "ds_read_b128 a[232:235], v59 offset:25344\n\t"
      "v_mfma_f32_4x4x4_16b_bf16 v[40:43], a[184:185], a[116:117], 0\n\t"
      "ds_read_b64_tr_b16 a[124:125], v12 offset:128\n\t"
      "v_mfma_f32_4x4x4_16b_bf16 v[44:47], a[186:187], a[118:119], 0\n\t"
      "ds_read_b64_tr_b16 a[126:127], v12 offset:896\n\t"
      "v_mfma_f32_4x4x4_16b_bf16 v[48:51], a[184:185], a[120:121], 0\n\t"
      "ds_read_b64_tr_b16 a[128:129], v13 offset:128\n\t"
      "v_mfma_f32_4x4x4_16b_bf16 v[52:55], a[186:187], a[122:123], 0\n\t"
      "ds_read_b64_tr_b16 a[130:131], v13 offset:896\n\t"
      "buffer_load_dwordx4 v10, s[84:87], s54 offen offset:1024 lds\n\t"
      "buffer_load_dwordx4 v11, s[84:87], s54 offen offset:2048 lds\n\t"
      "ds_read_b128 a[236:239], v59 offset:37632\n\t"
      "s_or_b32 s30, s17, s16\n\t"
      "s_cbranch_scc1 .Lno3_%=\n\t"
      "global_load_dword a252, v62, s[12:13] sc1\n\t"
      ".Lno3_%=:\n\t"
      "s_add_u32 m0, s18, 49664\n\t"
      "s_add_u32 s48, s15, 786432\n\t"
      "buffer_load_dwordx4 v2, s[40:43], s48 offen lds\n\t"
      "buffer_load_dwordx4 v3, s[40:43], s48 offen offset:1024 lds\n\t"
      "buffer_load_dwordx4 v4, s[40:43], s48 offen offset:2048 lds\n\t"
      "v_mfma_f32_16x16x32_bf16 a[164:167], v[168:171], a[228:231], a[164:167]\n\t"
      "v_cvt_pk_bf16_f32 v40, v40, v41\n\t"
      "v_cvt_pk_bf16_f32 v41, v42, v43\n\t"
      "v_mfma_f32_16x16x32_bf16 a[168:171], v[172:175], a[228:231], a[168:171]\n\t"
      "v_cvt_pk_bf16_f32 v44, v44, v45\n\t"
      "v_cvt_pk_bf16_f32 v45, v46, v47\n\t"
      "v_mfma_f32_16x16x32_bf16 a[172:175], a[108:111], a[228:231], a[172:175]\n\t"
      "v_cvt_pk_bf16_f32 v48, v48, v49\n\t"
      "v_cvt_pk_bf16_f32 v49, v50, v51\n\t"
      "v_mfma_f32_16x16x32_bf16 a[176:179], a[112:115], a[228:231], a[176:179]\n\t"
      "v_cvt_pk_bf16_f32 v52, v52, v53\n\t"
      "v_cvt_pk_bf16_f32 v53, v54, v55\n\t"
      "v_accvgpr_write_b32 a116, v40\n\t"
      "v_accvgpr_write_b32 a117, v41\n\t"
      "v_accvgpr_write_b32 a118, v44\n\t"
      "v_accvgpr_write_b32 a119, v45\n\t"
      "v_accvgpr_write_b32 a120, v48\n\t"
      "v_accvgpr_write_b32 a121, v49\n\t"
      "v_accvgpr_write_b32 a122, v52\n\t"
      "v_accvgpr_write_b32 a123, v53\n\t"
      "s_waitcnt vmcnt(15)\n\t"
      "s_waitcnt lgkmcnt(1)\n\t"
      "; step\n\t"
      "v_mfma_f32_4x4x4_16b_bf16 v[40:43], a[180:181], v[192:193], 0\n\t"
      "ds_read_b128 a[184:187], v14 offset:2304\n\t"
      "v_mfma_f32_4x4x4_16b_bf16 v[44:47], a[182:183], v[194:195], 0\n\t"
      "ds_read_b64_tr_b16 v[208:209], v60 offset:0\n\t"
      "v_mfma_f32_4x4x4_16b_bf16 v[48:51], a[180:181], v[196:197], 0\n\t"
      "ds_read_b64_tr_b16 v[210:211], v60 offset:768\n\t"
      "v_mfma_f32_4x4x4_16b_bf16 v[52:55], a[182:183], v[198:199], 0\n\t"
      "ds_read_b64_tr_b16 v[212:213], v61 offset:0\n\t"
      "ds_read_b64_tr_b16 v[214:215], v61 offset:768\n\t"
      "s_add_u32 m0, s18, 61952\n\t"
      "s_add_u32 s48, s15, 786432\n\t"
      "buffer_load_dwordx4 v5, s[40:43], s48 offen lds\n\t"
      "buffer_load_dwordx4 v6, s[40:43], s48 offen offset:1024 lds\n\t"
      "v_mfma_f32_16x16x32_bf16 v[16:19], v[176:179], a[232:235], v[16:19]\n\t"
      "v_cvt_pk_bf16_f32 v192, v40, v41\n\t"
      "v_cvt_pk_bf16_f32 v193, v42, v43\n\t"
      "v_mfma_f32_16x16x32_bf16 v[20:23], v[180:183], a[232:235], v[20:23]\n\t"
      "v_cvt_pk_bf16_f32 v194, v44, v45\n\t"
      "v_cvt_pk_bf16_f32 v195, v46, v47\n\t"
      "v_mfma_f32_16x16x32_bf16 v[24:27], v[184:187], a[232:235], v[24:27]\n\t"
      "v_cvt_pk_bf16_f32 v196, v48, v49\n\t"
      "v_cvt_pk_bf16_f32 v197, v50, v51\n\t"
      "v_mfma_f32_16x16x32_bf16 v[28:31], v[188:191], a[232:235], v[28:31]\n\t"
      "v_cvt_pk_bf16_f32 v198, v52, v53\n\t"
      "v_cvt_pk_bf16_f32 v199, v54, v55\n\t"
      "v_mfma_f32_4x4x4_16b_bf16 v[40:43], a[180:181], v[200:201], 0\n\t"
      "ds_read_b64_tr_b16 v[216:217], v60 offset:64\n\t"
      "v_mfma_f32_4x4x4_16b_bf16 v[44:47], a[182:183], v[202:203], 0\n\t"
      "ds_read_b64_tr_b16 v[218:219], v60 offset:832\n\t"
      "v_mfma_f32_4x4x4_16b_bf16 v[48:51], a[180:181], v[204:205], 0\n\t"
      "ds_read_b64_tr_b16 v[220:221], v61 offset:64\n\t"
      "v_mfma_f32_4x4x4_16b_bf16 v[52:55], a[182:183], v[206:207], 0\n\t"
      "ds_read_b64_tr_b16 v[222:223], v61 offset:832\n\t"
      "buffer_load_dwordx4 v7, s[40:43], s48 offen offset:2048 lds\n\t"
      "v_mfma_f32_16x16x32_bf16 v[32:35], a[116:119], a[232:235], v[32:35]\n\t"
      "v_cvt_pk_bf16_f32 v200, v40, v41\n\t"
      "v_cvt_pk_bf16_f32 v201, v42, v43\n\t"
      "v_mfma_f32_16x16x32_bf16 v[36:39], a[120:123], a[232:235], v[36:39]\n\t"
      "v_cvt_pk_bf16_f32 v202, v44, v45\n\t"
      "v_cvt_pk_bf16_f32 v203, v46, v47\n\t"
      "s_waitcnt lgkmcnt(9)\n\t"
      "v_mfma_f32_16x16x32_bf16 a[156:159], v[176:179], a[236:239], a[156:159]\n\t"
      "v_cvt_pk_bf16_f32 v204, v48, v49\n\t"
      "v_cvt_pk_bf16_f32 v205, v50, v51\n\t"
      "v_mfma_f32_16x16x32_bf16 a[160:163], v[180:183], a[236:239], a[160:163]\n\t"
      "v_cvt_pk_bf16_f32 v206, v52, v53\n\t"
      "v_cvt_pk_bf16_f32 v207, v54, v55\n\t"
      "ds_read_b128 a[240:243], v56 offset:25600\n\t"
      "v_mfma_f32_4x4x4_16b_bf16 v[40:43], a[180:181], a[124:125], 0\n\t"
      "ds_read_b64_tr_b16 a[132:133], v60 offset:128\n\t"
      "v_mfma_f32_4x4x4_16b_bf16 v[44:47], a[182:183], a[126:127], 0\n\t"
      "ds_read_b64_tr_b16 a[134:135], v60 offset:896\n\t"
      "v_mfma_f32_4x4x4_16b_bf16 v[48:51], a[180:181], a[128:129], 0\n\t"
      "ds_read_b64_tr_b16 a[136:137], v61 offset:128\n\t"
      "v_mfma_f32_4x4x4_16b_bf16 v[52:55], a[182:183], a[130:131], 0\n\t"
      "ds_read_b64_tr_b16 a[138:139], v61 offset:896\n\t"
      "ds_read_b128 a[244:247], v56 offset:37888\n\t"
      "v_mfma_f32_16x16x32_bf16 a[164:167], v[184:187], a[236:239], a[164:167]\n\t"
      "v_cvt_pk_bf16_f32 v40, v40, v41\n\t"
      "v_cvt_pk_bf16_f32 v41, v42, v43\n\t"
      "v_mfma_f32_16x16x32_bf16 a[168:171], v[188:191], a[236:239], a[168:171]\n\t"
      "v_cvt_pk_bf16_f32 v44, v44, v45\n\t"
      "v_cvt_pk_bf16_f32 v45, v46, v47\n\t"
      "v_mfma_f32_16x16x32_bf16 a[172:175], a[116:119], a[236:239], a[172:175]\n\t"
      "v_cvt_pk_bf16_f32 v48, v48, v49\n\t"
      "v_cvt_pk_bf16_f32 v49, v50, v51\n\t"
      "v_mfma_f32_16x16x32_bf16 a[176:179], a[120:123], a[236:239], a[176:179]\n\t"
      "v_cvt_pk_bf16_f32 v52, v52, v53\n\t"
      "v_cvt_pk_bf16_f32 v53, v54, v55\n\t"
      "v_accvgpr_write_b32 a124, v40\n\t"
      "v_accvgpr_write_b32 a125, v41\n\t"
      "v_accvgpr_write_b32 a126, v44\n\t"
      "v_accvgpr_write_b32 a127, v45\n\t"
      "v_accvgpr_write_b32 a128, v48\n\t"
      "v_accvgpr_write_b32 a129, v49\n\t"
      "v_accvgpr_write_b32 a130, v52\n\t"
      "v_accvgpr_write_b32 a131, v53\n\t"
      "s_waitcnt vmcnt(12)\n\t"
      "s_waitcnt lgkmcnt(1)\n\t"
      "; step\n\t"
      "v_mfma_f32_4x4x4_16b_bf16 v[40:43], a[184:185], v[208:209], 0\n\t"
      "ds_read_b128 a[180:183], v14 offset:2560\n\t"
      "v_mfma_f32_4x4x4_16b_bf16 v[44:47], a[186:187], v[210:211], 0\n\t"
      "ds_read_b64_tr_b16 v[224:225], v60 offset:24576\n\t"
      "v_mfma_f32_4x4x4_16b_bf16 v[48:51], a[184:185], v[212:213], 0\n\t"
      "ds_read_b64_tr_b16 v[226:227], v60 offset:25344\n\t"
      "v_mfma_f32_4x4x4_16b_bf16 v[52:55], a[186:187], v[214:215], 0\n\t"
      "ds_read_b64_tr_b16 v[228:229], v61 offset:24576\n\t"
      "ds_read_b64_tr_b16 v[230:231], v61 offset:25344\n\t"
      "v_mfma_f32_16x16x32_bf16 v[16:19], v[192:195], a[240:243], v[16:19]\n\t"
      "v_cvt_pk_bf16_f32 v208, v40, v41\n\t"
      "v_cvt_pk_bf16_f32 v209, v42, v43\n\t"
      "v_mfma_f32_16x16x32_bf16 v[20:23], v[196:199], a[240:243], v[20:23]\n\t"
      "v_cvt_pk_bf16_f32 v210, v44, v45\n\t"
      "v_cvt_pk_bf16_f32 v211, v46, v47\n\t"
      "v_mfma_f32_16x16x32_bf16 v[24:27], v[200:203], a[240:243], v[24:27]\n\t"
      "v_cvt_pk_bf16_f32 v212, v48, v49\n\t"
      "v_cvt_pk_bf16_f32 v213, v50, v51\n\t"
      "v_mfma_f32_16x16x32_bf16 v[28:31], v[204:207], a[240:243], v[28:31]\n\t"
      "v_cvt_pk_bf16_f32 v214, v52, v53\n\t"
      "v_cvt_pk_bf16_f32 v215, v54, v55\n\t"
      "v_mfma_f32_4x4x4_16b_bf16 v[40:43], a[184:185], v[216:217], 0\n\t"
      "ds_read_b64_tr_b16 v[232:233], v60 offset:24640\n\t"
      "v_mfma_f32_4x4x4_16b_bf16 v[44:47], a[186:187], v[218:219], 0\n\t"
      "ds_read_b64_tr_b16 v[234:235], v60 offset:25408\n\t"
      "v_mfma_f32_4x4x4_16b_bf16 v[48:51], a[184:185], v[220:221], 0\n\t"
      "ds_read_b64_tr_b16 v[236:237], v61 offset:24640\n\t"
      "v_mfma_f32_4x4x4_16b_bf16 v[52:55], a[186:187], v[222:223], 0\n\t"
      "ds_read_b64_tr_b16 v[238:239], v61 offset:25408\n\t"
      "v_mfma_f32_16x16x32_bf16 v[32:35], a[124:127], a[240:243], v[32:35]\n\t"
      "v_cvt_pk_bf16_f32 v216, v40, v41\n\t"
      "v_cvt_pk_bf16_f32 v217, v42, v43\n\t"
      "v_mfma_f32_16x16x32_bf16 v[36:39], a[128:131], a[240:243], v[36:39]\n\t"
      "v_cvt_pk_bf16_f32 v218, v44, v45\n\t"
      "v_cvt_pk_bf16_f32 v219, v46, v47\n\t"
      "s_waitcnt lgkmcnt(9)\n\t"
      "v_mfma_f32_16x16x32_bf16 a[156:159], v[192:195], a[244:247], a[156:159]\n\t"
      "v_cvt_pk_bf16_f32 v220, v48, v49\n\t"
      "v_cvt_pk_bf16_f32 v221, v50, v51\n\t"
      "v_mfma_f32_16x16x32_bf16 a[160:163], v[196:199], a[244:247], a[160:163]\n\t"
      "v_cvt_pk_bf16_f32 v222, v52, v53\n\t"
      "v_cvt_pk_bf16_f32 v223, v54, v55\n\t"
      "ds_read_b128 a[224:227], v57 offset:25600\n\t"
      "v_mfma_f32_4x4x4_16b_bf16 v[40:43], a[184:185], a[132:133], 0\n\t"
      "ds_read_b64_tr_b16 a[140:141], v60 offset:24704\n\t"
      "v_mfma_f32_4x4x4_16b_bf16 v[44:47], a[186:187], a[134:135], 0\n\t"
      "ds_read_b64_tr_b16 a[142:143], v60 offset:25472\n\t"
      "v_mfma_f32_4x4x4_16b_bf16 v[48:51], a[184:185], a[136:137], 0\n\t"
      "ds_read_b64_tr_b16 a[144:145], v61 offset:24704\n\t"
      "v_mfma_f32_4x4x4_16b_bf16 v[52:55], a[186:187], a[138:139], 0\n\t"
      "ds_read_b64_tr_b16 a[146:147], v61 offset:25472\n\t"
      "ds_read_b128 a[228:231], v57 offset:37888\n\t"
      "v_mfma_f32_16x16x32_bf16 a[164:167], v[200:203], a[244:247], a[164:167]\n\t"
      "v_cvt_pk_bf16_f32 v40, v40, v41\n\t"
      "v_cvt_pk_bf16_f32 v41, v42, v43\n\t"
      "v_mfma_f32_16x16x32_bf16 a[168:171], v[204:207], a[244:247], a[168:171]\n\t"
      "v_cvt_pk_bf16_f32 v44, v44, v45\n\t"
      "v_cvt_pk_bf16_f32 v45, v46, v47\n\t"
      "v_mfma_f32_16x16x32_bf16 a[172:175], a[124:127], a[244:247], a[172:175]\n\t"
      "v_cvt_pk_bf16_f32 v48, v48, v49\n\t"
      "v_cvt_pk_bf16_f32 v49, v50, v51\n\t"
      "v_mfma_f32_16x16x32_bf16 a[176:179], a[128:131], a[244:247], a[176:179]\n\t"
      "v_cvt_pk_bf16_f32 v52, v52, v53\n\t"
      "v_cvt_pk_bf16_f32 v53, v54, v55\n\t"
      "v_accvgpr_write_b32 a132, v40\n\t"
      "v_accvgpr_write_b32 a133, v41\n\t"
      "v_accvgpr_write_b32 a134, v44\n\t"
      "v_accvgpr_write_b32 a135, v45\n\t"
      "v_accvgpr_write_b32 a136, v48\n\t"
      "v_accvgpr_write_b32 a137, v49\n\t"
      "v_accvgpr_write_b32 a138, v52\n\t"
      "v_accvgpr_write_b32 a139, v53\n\t"
      "s_waitcnt vmcnt(6)\n\t"
      "s_waitcnt lgkmcnt(1)\n\t"
      "; step\n\t"
      "s_memtime s[68:69]\n\t"
      "s_memrealtime s[70:71]\n\t"
      "v_mfma_f32_4x4x4_16b_bf16 v[40:43], a[180:181], v[224:225], 0\n\t"
      "ds_read_b128 a[184:187], v14 offset:2816\n\t"
      "v_mfma_f32_4x4x4_16b_bf16 v[44:47], a[182:183], v[226:227], 0\n\t"
      "ds_read_b64_tr_b16 v[240:241], v60 offset:49152\n\t"
      "v_mfma_f32_4x4x4_16b_bf16 v[48:51], a[180:181], v[228:229], 0\n\t"
      "ds_read_b64_tr_b16 v[242:243], v60 offset:49920\n\t"
      "v_mfma_f32_4x4x4_16b_bf16 v[52:55], a[182:183], v[230:231], 0\n\t"
      "ds_read_b64_tr_b16 v[244:245], v61 offset:49152\n\t"
      "ds_read_b64_tr_b16 v[246:247], v61 offset:49920\n\t"
      "v_mfma_f32_16x16x32_bf16 v[16:19], v[208:211], a[224:227], v[16:19]\n\t"
      "v_cvt_pk_bf16_f32 v224, v40, v41\n\t"
      "v_cvt_pk_bf16_f32 v225, v42, v43\n\t"
      "v_mfma_f32_16x16x32_bf16 v[20:23], v[212:215], a[224:227], v[20:23]\n\t"
      "v_cvt_pk_bf16_f32 v226, v44, v45\n\t"
      "v_cvt_pk_bf16_f32 v227, v46, v47\n\t"
      "v_mfma_f32_16x16x32_bf16 v[24:27], v[216:219], a[224:227], v[24:27]\n\t"
      "v_cvt_pk_bf16_f32 v228, v48, v49\n\t"
      "v_cvt_pk_bf16_f32 v229, v50, v51\n\t"
      "v_mfma_f32_16x16x32_bf16 v[28:31], v[220:223], a[224:227], v[28:31]\n\t"
      "v_cvt_pk_bf16_f32 v230, v52, v53\n\t"
      "v_cvt_pk_bf16_f32 v231, v54, v55\n\t"
      "v_mfma_f32_4x4x4_16b_bf16 v[40:43], a[180:181], v[232:233], 0\n\t"
      "ds_read_b64_tr_b16 v[248:249], v60 offset:49216\n\t"
      "v_mfma_f32_4x4x4_16b_bf16 v[44:47], a[182:183], v[234:235], 0\n\t"
      "ds_read_b64_tr_b16 v[250:251], v60 offset:49984\n\t"
      "v_mfma_f32_4x4x4_16b_bf16 v[48:51], a[180:181], v[236:237], 0\n\t"
      "ds_read_b64_tr_b16 v[252:253], v61 offset:49216\n\t"
      "v_mfma_f32_4x4x4_16b_bf16 v[52:55], a[182:183], v[238:239], 0\n\t"
      "ds_read_b64_tr_b16 v[254:255], v61 offset:49984\n\t"
      "v_mfma_f32_16x16x32_bf16 v[32:35], a[132:135], a[224:227], v[32:35]\n\t"
      "v_cvt_pk_bf16_f32 v232, v40, v41\n\t"
      "v_cvt_pk_bf16_f32 v233, v42, v43\n\t"
      "v_mfma_f32_16x16x32_bf16 v[36:39], a[136:139], a[224:227], v[36:39]\n\t"
      "v_cvt_pk_bf16_f32 v234, v44, v45\n\t"
      "v_cvt_pk_bf16_f32 v235, v46, v47\n\t"
      "s_waitcnt lgkmcnt(9)\n\t"
      "v_mfma_f32_16x16x32_bf16 a[156:159], v[208:211], a[228:231], a[156:159]\n\t"
      "v_cvt_pk_bf16_f32 v236, v48, v49\n\t"
      "v_cvt_pk_bf16_f32 v237, v50, v51\n\t"
      "v_mfma_f32_16x16x32_bf16 a[160:163], v[212:215], a[228:231], a[160:163]\n\t"
      "v_cvt_pk_bf16_f32 v238, v52, v53\n\t"
      "v_cvt_pk_bf16_f32 v239, v54, v55\n\t"
      "ds_read_b128 a[232:235], v58 offset:25600\n\t"
      "v_mfma_f32_4x4x4_16b_bf16 v[40:43], a[180:181], a[140:141], 0\n\t"
      "ds_read_b64_tr_b16 a[148:149], v60 offset:49280\n\t"
      "v_mfma_f32_4x4x4_16b_bf16 v[44:47], a[182:183], a[142:143], 0\n\t"
      "ds_read_b64_tr_b16 a[150:151], v60 offset:50048\n\t"
      "v_mfma_f32_4x4x4_16b_bf16 v[48:51], a[180:181], a[144:145], 0\n\t"
      "ds_read_b64_tr_b16 a[152:153], v61 offset:49280\n\t"
      "v_mfma_f32_4x4x4_16b_bf16 v[52:55], a[182:183], a[146:147], 0\n\t"
      "ds_read_b64_tr_b16 a[154:155], v61 offset:50048\n\t"
      "v_mfma_f32_16x16x32_bf16 a[164:167], v[216:219], a[228:231], a[164:167]\n\t"
      "v_cvt_pk_bf16_f32 v40, v40, v41\n\t"
      "v_cvt_pk_bf16_f32 v41, v42, v43\n\t"
      "v_mfma_f32_16x16x32_bf16 a[168:171], v[220:223], a[228:231], a[168:171]\n\t"
      "v_cvt_pk_bf16_f32 v44, v44, v45\n\t"
      "v_cvt_pk_bf16_f32 v45, v46, v47\n\t"
      "v_mfma_f32_16x16x32_bf16 a[172:175], a[132:135], a[228:231], a[172:175]\n\t"
      "v_cvt_pk_bf16_f32 v48, v48, v49\n\t"
      "v_cvt_pk_bf16_f32 v49, v50, v51\n\t"
      "v_mfma_f32_16x16x32_bf16 a[176:179], a[136:139], a[228:231], a[176:179]\n\t"
      "v_cvt_pk_bf16_f32 v52, v52, v53\n\t"
      "v_cvt_pk_bf16_f32 v53, v54, v55\n\t"
      "v_accvgpr_write_b32 a140, v40\n\t"
      "v_accvgpr_write_b32 a141, v41\n\t"
      "v_accvgpr_write_b32 a142, v44\n\t"
      "v_accvgpr_write_b32 a143, v45\n\t"
      "v_accvgpr_write_b32 a144, v48\n\t"
      "v_accvgpr_write_b32 a145, v49\n\t"
      "v_accvgpr_write_b32 a146, v52\n\t"
      "v_accvgpr_write_b32 a147, v53\n\t"
      "s_waitcnt lgkmcnt(0)\n\t"
      "; step\n\t"
      "v_mfma_f32_4x4x4_16b_bf16 v[40:43], a[184:185], v[240:241], 0\n\t"
      "v_mfma_f32_4x4x4_16b_bf16 v[44:47], a[186:187], v[242:243], 0\n\t"
      "v_mfma_f32_4x4x4_16b_bf16 v[48:51], a[184:185], v[244:245], 0\n\t"
      "v_mfma_f32_4x4x4_16b_bf16 v[52:55], a[186:187], v[246:247], 0\n\t"
      "v_mfma_f32_16x16x32_bf16 v[16:19], v[224:227], a[232:235], v[16:19]\n\t"
      "s_nop 0\n\t"
      "v_cvt_pk_bf16_f32 v240, v40, v41\n\t"
      "v_cvt_pk_bf16_f32 v241, v42, v43\n\t"
      "v_mfma_f32_16x16x32_bf16 v[20:23], v[228:231], a[232:235], v[20:23]\n\t"
      "v_cvt_pk_bf16_f32 v242, v44, v45\n\t"
      "v_cvt_pk_bf16_f32 v243, v46, v47\n\t"
      "v_cvt_pk_bf16_f32 v244, v48, v49\n\t"
      "v_cvt_pk_bf16_f32 v245, v50, v51\n\t"
      "v_cvt_pk_bf16_f32 v246, v52, v53\n\t"
      "v_cvt_pk_bf16_f32 v247, v54, v55\n\t"
      "v_mfma_f32_4x4x4_16b_bf16 v[40:43], a[184:185], v[248:249], 0\n\t"
      "v_mfma_f32_4x4x4_16b_bf16 v[44:47], a[186:187], v[250:251], 0\n\t"
      "v_mfma_f32_4x4x4_16b_bf16 v[48:51], a[184:185], v[252:253], 0\n\t"
      "v_mfma_f32_4x4x4_16b_bf16 v[52:55], a[186:187], v[254:255], 0\n\t"
      "v_mfma_f32_16x16x32_bf16 v[24:27], v[232:235], a[232:235], v[24:27]\n\t"
      "s_nop 0\n\t"
      "v_cvt_pk_bf16_f32 v248, v40, v41\n\t"
      "v_cvt_pk_bf16_f32 v249, v42, v43\n\t"
      "v_mfma_f32_16x16x32_bf16 v[28:31], v[236:239], a[232:235], v[28:31]\n\t"
      "v_cvt_pk_bf16_f32 v250, v44, v45\n\t"
      "v_cvt_pk_bf16_f32 v251, v46, v47\n\t"
      "v_cvt_pk_bf16_f32 v252, v48, v49\n\t"
      "v_cvt_pk_bf16_f32 v253, v50, v51\n\t"
      "v_cvt_pk_bf16_f32 v254, v52, v53\n\t"
      "v_cvt_pk_bf16_f32 v255, v54, v55\n\t"
      "ds_read_b128 a[236:239], v59 offset:25600\n\t"
      "v_mfma_f32_4x4x4_16b_bf16 v[40:43], a[184:185], a[148:149], 0\n\t"
      "v_mfma_f32_4x4x4_16b_bf16 v[44:47], a[186:187], a[150:151], 0\n\t"
      "v_mfma_f32_4x4x4_16b_bf16 v[48:51], a[184:185], a[152:153], 0\n\t"
      "v_mfma_f32_4x4x4_16b_bf16 v[52:55], a[186:187], a[154:155], 0\n\t"
      "ds_read_b128 a[240:243], v58 offset:37888\n\t"
      "ds_read_b128 a[244:247], v59 offset:37888\n\t"
      "v_mfma_f32_16x16x32_bf16 v[32:35], a[140:143], a[232:235], v[32:35]\n\t"
      "v_cvt_pk_bf16_f32 v40, v40, v41\n\t"
      "v_cvt_pk_bf16_f32 v41, v42, v43\n\t"
      "v_mfma_f32_16x16x32_bf16 v[36:39], a[144:147], a[232:235], v[36:39]\n\t"
      "v_cvt_pk_bf16_f32 v44, v44, v45\n\t"
      "v_cvt_pk_bf16_f32 v45, v46, v47\n\t"
      "v_cvt_pk_bf16_f32 v48, v48, v49\n\t"
      "v_cvt_pk_bf16_f32 v49, v50, v51\n\t"
      "v_cvt_pk_bf16_f32 v52, v52, v53\n\t"
      "v_cvt_pk_bf16_f32 v53, v54, v55\n\t"
      "v_accvgpr_write_b32 a148, v40\n\t"
      "v_accvgpr_write_b32 a149, v41\n\t"
      "v_accvgpr_write_b32 a150, v44\n\t"
      "v_accvgpr_write_b32 a151, v45\n\t"
      "v_accvgpr_write_b32 a152, v48\n\t"
      "v_accvgpr_write_b32 a153, v49\n\t"
      "v_accvgpr_write_b32 a154, v52\n\t"
      "v_accvgpr_write_b32 a155, v53\n\t"
      "s_waitcnt lgkmcnt(2)\n\t"
      "v_mfma_f32_16x16x32_bf16 v[16:19], v[240:243], a[236:239], v[16:19]\n\t"
      "s_add_u32 s49, s15, 0\n\t"
      "s_mov_b32 s50, 98816\n\t"
      "s_mov_b32 s51, 61952\n\t"
      "v_mfma_f32_16x16x32_bf16 v[20:23], v[244:247], a[236:239], v[20:23]\n\t"
      "s_mov_b32 s58, s14\n\t"
      "s_add_u32 s56, s14, 2359296\n\t"
      "v_mfma_f32_16x16x32_bf16 v[24:27], v[248:251], a[236:239], v[24:27]\n\t"
      "s_movk_i32 s39, 96\n\t"
      "s_movk_i32 s29, 71\n\t"
      "s_mov_b32 s32, 1947275679\n\t"
      "v_mfma_f32_16x16x32_bf16 v[28:31], v[252:255], a[236:239], v[28:31]\n\t"
      "s_mov_b32 s33, 143165559\n\t"
      "s_mov_b32 s34, 1950421356\n\t"
      "s_mov_b32 s35, 8947847\n\t"
      "v_mfma_f32_16x16x32_bf16 v[32:35], a[148:151], a[236:239], v[32:35]\n\t"
      "v_mov_b32 v52, 163328\n\t"
      "v_mfma_f32_16x16x32_bf16 v[36:39], a[152:155], a[236:239], v[36:39]\n\t"
      "s_cmp_lg_u32 s17, 0\n\t"
      "s_cselect_b32 s39, 0, s39\n\t"
      "s_cbranch_scc1 .Lno4_%=\n\t"
      "v_accvgpr_read_b32 v41, a248\n\t"
      "v_mov_b32 v51, 0x4040\n\t"
      "s_movk_i32 s52, 2000\n\t"
      "s_mov_b32 s91, 0\n\t"
      "s_waitcnt vmcnt(6) \n\t"
      "v_accvgpr_read_b32 v40, a252\n\t"
      ".Lcheck_%=:\n\t"
      "v_cmp_ne_u32 s[54:55], v40, v63\n\t"
      "s_cmp_lg_u32 s16, 0\n\t"
      "v_add_u32_dpp v42, v40, v40 quad_perm:[2,3,0,1] row_mask:0xf bank_mask:0xf\n\t"
      "s_cselect_b32 s30, -1, s54\n\t"
      "s_cmp_lg_u32 s30, -1\n\t"
      "s_cbranch_scc1 .Lwait_%=\n\t"
      ".Lwaited_%=:\n\t"
      "v_add_u32_dpp v42, v42, v42 quad_perm:[1,0,3,2] row_mask:0xf bank_mask:0xf\n\t"
      "s_bfe_u32 s30, s19, 0x30008\n\t"
      "s_bfe_u32 s31, s19, 0x10003\n\t"
      "s_lshl1_add_u32 s30, s30, s31\n\t"
      "s_lshl_b32 s30, s30, 1\n\t"
      "v_mov_b32 v43, v42\n\t"
      "v_subrev_u32_dpp v43, v42, v51 row_half_mirror row_mask:0x3 bank_mask:0xf\n\t"
      "v_lshl_add_u32 v42, v42, 1, v43\n\t"
      "v_readlane_b32 s31, v40, s30\n\t"
      "v_add_u32 v42, 0x1f1f, v42\n\t"
      "v_and_b32 v43, 128, v42\n\t"
      "v_and_b32 v44, 32768, v42\n\t"
      "v_cmp_ne_u32 s[88:89], 0, v43\n\t"
      "v_cmp_ne_u32 s[36:37], 0, v44\n\t"
      "s_bitcmp1_b32 s19, 2\n\t"
      "s_cselect_b32 s30, 0x3ffff, 0\n\t"
      "s_bitcmp1_b32 s31, 16\n\t"
      "s_cselect_b32 s31, 262140, 0\n\t"
      "s_xor_b32 s30, s30, s31\n\t"
      "s_xor_b32 s72, s36, s88\n\t"
      "s_and_b32 s72, s72, 0x11111111\n\t"
      "s_bcnt1_i32_b32 s72, s72\n\t"
      "s_bfe_u32 s31, %[blk], 0x20003\n\t"
      "s_lshr_b32 s31, 1118481, s31\n\t"
      "s_and_b32 s31, s31, 262140\n\t"
      "s_cmp_ge_u32 s72, 7\n\t"
      "s_cselect_b32 s31, s31, 0\n\t"
      "s_xor_b32 s30, s30, s31\n\t"
      "v_lshrrev_b32 v41, v41, s88\n\t"
      "v_and_b32 v41, 1, v41\n\t"
      "v_cmp_ne_u32 vcc, 0, v41\n\t"
      "s_nop 0\n\t"
      "s_and_b32 s31, vcc_lo, 262140\n\t"
      "s_xor_b32 s31, s31, s30\n\t"
      "s_or_b32 s31, s31, s91\n\t"
      "s_and_b32 s30, s16, 0x3ffff\n\t"
      "s_cmp_lg_u32 s16, 0\n\t"
      "s_cselect_b32 s31, s30, s31\n\t"
      "v_mov_b32 v53, s31\n\t"
      "ds_write_b32 v52, v53\n\t"
      "s_xor_b32 s36, s37, s89\n\t"
      "s_lshr_b32 s30, s55, 1\n\t"
      "s_and_b32 s55, s55, s30\n\t"
      "s_lshr_b32 s30, s55, 2\n\t"
      "s_and_b32 s55, s55, s30\n\t"
      "s_and_b32 s36, s36, s55\n\t"
      "s_and_b32 s36, s36, 0x11111111\n\t"
      "s_bcnt1_i32_b32 s36, s36\n\t"
      "s_lshl_b32 s30, s36, 2\n\t"
      "s_mov_b32 s54, 1394741248\n\t"
      "s_mov_b32 s55, 6\n\t"
      "s_lshr_b64 s[54:55], s[54:55], s30\n\t"
      "s_and_b32 s30, s54, 15\n\t"
      "s_add_u32 s29, s29, s30\n\t"
      ".Lno4_%=:\n\t"
      "v_accvgpr_read_b32 v9, a213\n\t"
      "v_accvgpr_read_b32 v10, a214\n\t"
      "v_accvgpr_read_b32 v11, a215\n\t"
      "v_accvgpr_read_b32 v12, a216\n\t"
      "v_accvgpr_read_b32 v13, a217\n\t"
      "v_accvgpr_read_b32 v14, a218\n\t"
      "v_accvgpr_read_b32 v8, a219\n\t"
      "v_accvgpr_read_b32 v60, a220\n\t"
      "v_accvgpr_read_b32 v61, a221\n\t"
      "v_add_u32 v56, 49664, v56\n\t"
      "v_add_u32 v57, 49664, v57\n\t"
      "v_add_u32 v58, 49664, v58\n\t"
      "v_add_u32 v59, 49664, v59\n\t"
      "v_cvt_pk_bf16_f32 v40, v16, v17\n\t"
      "v_cvt_pk_bf16_f32 v41, v18, v19\n\t"
      "v_cvt_pk_bf16_f32 v42, v20, v21\n\t"
      "v_cvt_pk_bf16_f32 v43, v22, v23\n\t"
      "v_cvt_pk_bf16_f32 v44, v24, v25\n\t"
      "v_cvt_pk_bf16_f32 v45, v26, v27\n\t"
      "v_cvt_pk_bf16_f32 v46, v28, v29\n\t"
      "v_cvt_pk_bf16_f32 v47, v30, v31\n\t"
      "v_cvt_pk_bf16_f32 v48, v32, v33\n\t"
      "v_cvt_pk_bf16_f32 v49, v34, v35\n\t"
      "v_cvt_pk_bf16_f32 v50, v36, v37\n\t"
      "v_cvt_pk_bf16_f32 v51, v38, v39\n\t"
      "s_mov_b32 s46, 113246208\n\t"
      "s_mov_b32 s52, 15\n\t"
      "s_add_u32 s90, s90, 0x2000000\n\t"
      "s_waitcnt vmcnt(3)\n\t"
      "s_waitcnt lgkmcnt(0)\n\t"
      "s_barrier\n\t"
      "v_mfma_f32_16x16x32_bf16 a[156:159], v[224:227], a[240:243], a[156:159]\n\t"
      "ds_read_b32 v63, v52\n\t"
      "ds_read_b128 a[0:3], v56 offset:0\n\t"
      "s_memrealtime s[36:37]\n\t"
      "v_mfma_f32_16x16x32_bf16 a[160:163], v[228:231], a[240:243], a[160:163]\n\t"
      "ds_read_b128 a[4:7], v57 offset:0\n\t"
      "ds_read_b128 a[8:11], v58 offset:0\n\t"
      "v_mfma_f32_16x16x32_bf16 a[164:167], v[232:235], a[240:243], a[164:167]\n\t"
      "ds_read_b128 a[12:15], v59 offset:0\n\t"
      "ds_read_b128 a[16:19], v56 offset:256\n\t"
      "v_mfma_f32_16x16x32_bf16 a[168:171], v[236:239], a[240:243], a[168:171]\n\t"
      "ds_read_b128 a[20:23], v57 offset:256\n\t"
      "ds_write_b64 v0, v[40:41] offset:0\n\t"
      "v_mfma_f32_16x16x32_bf16 a[172:175], a[140:143], a[240:243], a[172:175]\n\t"
      "ds_write_b64 v0, v[42:43] offset:32\n\t"
      "v_mfma_f32_16x16x32_bf16 a[176:179], a[144:147], a[240:243], a[176:179]\n\t"
      "s_waitcnt lgkmcnt(8)\n\t"
      "v_readfirstlane_b32 s91, v63\n\t"
      "v_mfma_f32_16x16x32_bf16 a[156:159], v[240:243], a[244:247], a[156:159]\n\t"
      "ds_write_b64 v0, v[44:45] offset:64\n\t"
      "s_add_u32 m0, s18, 74240\n\t"
      "v_mfma_f32_16x16x32_bf16 a[160:163], v[244:247], a[244:247], a[160:163]\n\t"
      "s_bitcmp1_b32 s91, 2\n\t"
      "s_cselect_b32 s48, 24576, 0\n\t"
      "s_add_u32 s48, s48, s14\n\t"
      "v_mfma_f32_16x16x32_bf16 a[164:167], v[248:251], a[244:247], a[164:167]\n\t"
      "s_add_u32 s48, s48, 1572864\n\t"
      "buffer_load_dwordx4 v2, s[40:43], s48 offen lds\n\t"
      "s_mov_b32 s59, s91\n\t"
      "v_mfma_f32_16x16x32_bf16 a[168:171], v[252:255], a[244:247], a[168:171]\n\t"
      "ds_write_b64 v0, v[46:47] offset:96\n\t"
      "buffer_load_dwordx4 v3, s[40:43], s48 offen offset:1024 lds\n\t"
      "v_mfma_f32_16x16x32_bf16 a[172:175], a[148:151], a[244:247], a[172:175]\n\t"
      "ds_write_b64 v0, v[48:49] offset:128\n\t"
      "buffer_load_dwordx4 v4, s[40:43], s48 offen offset:2048 lds\n\t"
      "v_mfma_f32_16x16x32_bf16 a[176:179], a[152:155], a[244:247], a[176:179]\n\t"
      "ds_write_b64 v0, v[50:51] offset:160\n\t"
      "s_lshr_b32 s57, s91, 3\n\t"
      "s_bitcmp1_b32 s91, 3\n\t"
      "s_cselect_b32 s48, 24576, 0\n\t"
      "s_add_u32 s48, s48, s14\n\t"
      "s_add_u32 s48, s48, 2359296\n\t"
      "s_or_b32 s30, s17, s16\n\t"
      "s_cbranch_scc1 .Lno5_%=\n\t"
      "s_sub_u32 s68, s68, s64\n\t"
      "s_sub_u32 s70, s70, s66\n\t"
      "s_mul_i32 s64, s68, 100\n\t"
      "s_mul_i32 s66, s70, 1750\n\t"
      "s_sub_i32 s66, s64, s66\n\t"
      "s_ashr_i32 s66, s66, 9\n\t"
      "s_add_i32 s66, s66, 128\n\t"
      "s_max_i32 s66, s66, 1\n\t"
      "s_min_i32 s66, s66, 255\n\t"
      "s_lshl_b32 s66, s66, 17\n\t"
      "s_or_b32 s90, s90, s66\n\t"
      "v_mov_b32 v54, s90\n\t"
      "v_mov_b32 v55, s19\n\t"
      "global_store_dword v55, v54, s[12:13] sc1\n\t"
      ".Lno5_%=:\n\t"
      "s_waitcnt lgkmcnt(11)\n\t"
      "v_mfma_f32_16x16x32_bf16 v[16:19], v[64:67], a[0:3], 0\n\t"
      "ds_read_b128 a[24:27], v58 offset:256\n\t"
      "ds_read_b128 a[28:31], v59 offset:256\n\t"
      "s_waitcnt lgkmcnt(12)\n\t"
      "v_mfma_f32_16x16x32_bf16 v[16:19], v[80:83], a[4:7], v[16:19]\n\t"
      "ds_read_b128 a[32:35], v56 offset:512\n\t"
      "ds_read_b128 a[36:39], v57 offset:512\n\t"
      "s_waitcnt lgkmcnt(13)\n\t"
      "v_mfma_f32_16x16x32_bf16 v[16:19], v[96:99], a[8:11], v[16:19]\n\t"
      "ds_read_b128 a[40:43], v58 offset:512\n\t"
      "ds_read_b128 a[44:47], v59 offset:512\n\t"
      "s_waitcnt lgkmcnt(14)\n\t"
      "v_mfma_f32_16x16x32_bf16 v[16:19], v[112:115], a[12:15], v[16:19]\n\t"
      "v_accvgpr_read_b32 v52, a156\n\t"
      "v_accvgpr_read_b32 v53, a157\n\t"
      "s_waitcnt lgkmcnt(13)\n\t"
      "v_mfma_f32_16x16x32_bf16 v[16:19], v[128:131], a[16:19], v[16:19]\n\t"
      "v_accvgpr_read_b32 v54, a158\n\t"
      "v_accvgpr_read_b32 v55, a159\n\t"
      "s_waitcnt lgkmcnt(12)\n\t"
      "v_mfma_f32_16x16x32_bf16 v[16:19], v[144:147], a[20:23], v[16:19]\n\t"
      "v_cvt_pk_bf16_f32 v40, v52, v53\n\t"
      "v_cvt_pk_bf16_f32 v41, v54, v55\n\t"
      "v_mfma_f32_16x16x32_bf16 v[20:23], v[68:71], a[0:3], 0\n\t"
      "ds_write_b64 v0, v[40:41] offset:12544\n\t"
      "v_mfma_f32_16x16x32_bf16 v[20:23], v[84:87], a[4:7], v[20:23]\n\t"
      "s_waitcnt lgkmcnt(7)\n\t"
      "s_barrier\n\t"
      "v_mfma_f32_16x16x32_bf16 v[20:23], v[100:103], a[8:11], v[20:23]\n\t"
      "ds_read_b128 a[48:51], v8 offset:0\n\t"
      "ds_read_b128 a[52:55], v60 offset:1024\n\t"
      "ds_read_b128 a[56:59], v61 offset:2048\n\t"
      "v_mfma_f32_16x16x32_bf16 v[20:23], v[116:119], a[12:15], v[20:23]\n\t"
      "v_accvgpr_read_b32 v52, a160\n\t"
      "v_accvgpr_read_b32 v53, a161\n\t"
      "v_mfma_f32_16x16x32_bf16 v[20:23], v[132:135], a[16:19], v[20:23]\n\t"
      "v_accvgpr_read_b32 v54, a162\n\t"
      "v_accvgpr_read_b32 v55, a163\n\t"
      "v_mfma_f32_16x16x32_bf16 v[20:23], v[148:151], a[20:23], v[20:23]\n\t"
      "v_cvt_pk_bf16_f32 v42, v52, v53\n\t"
      "v_cvt_pk_bf16_f32 v43, v54, v55\n\t"
      "v_mfma_f32_16x16x32_bf16 v[24:27], v[72:75], a[0:3], 0\n\t"
      "ds_write_b64 v0, v[42:43] offset:12576\n\t"
      "s_waitcnt lgkmcnt(3)\n\t"
      "v_mfma_f32_16x16x32_bf16 v[24:27], v[88:91], a[4:7], v[24:27]\n\t"
      "buffer_store_dwordx4 a[48:51], v9, s[44:47], s49 offen sc1\n\t"
      "v_mfma_f32_16x16x32_bf16 v[24:27], v[104:107], a[8:11], v[24:27]\n\t"
      "v_accvgpr_read_b32 v52, a164\n\t"
      "v_accvgpr_read_b32 v53, a165\n\t"
      "v_mfma_f32_16x16x32_bf16 v[24:27], v[120:123], a[12:15], v[24:27]\n\t"
      "v_accvgpr_read_b32 v54, a166\n\t"
      "v_accvgpr_read_b32 v55, a167\n\t"
      "v_mfma_f32_16x16x32_bf16 v[24:27], v[136:139], a[16:19], v[24:27]\n\t"
      "v_cvt_pk_bf16_f32 v44, v52, v53\n\t"
      "v_cvt_pk_bf16_f32 v45, v54, v55\n\t"
      "v_mfma_f32_16x16x32_bf16 v[24:27], v[152:155], a[20:23], v[24:27]\n\t"
      "ds_write_b64 v0, v[44:45] offset:12608\n\t"
      "s_waitcnt lgkmcnt(3)\n\t"
      "v_mfma_f32_16x16x32_bf16 v[28:31], v[76:79], a[0:3], 0\n\t"
      "buffer_store_dwordx4 a[52:55], v10, s[44:47], s49 offen offset:1024 sc1\n\t"
      "v_mfma_f32_16x16x32_bf16 v[28:31], v[92:95], a[4:7], v[28:31]\n\t"
      "v_accvgpr_read_b32 v52, a168\n\t"
      "v_accvgpr_read_b32 v53, a169\n\t"
      "v_mfma_f32_16x16x32_bf16 v[28:31], v[108:111], a[8:11], v[28:31]\n\t"
      "v_accvgpr_read_b32 v54, a170\n\t"
      "v_accvgpr_read_b32 v55, a171\n\t"
      "v_mfma_f32_16x16x32_bf16 v[28:31], v[124:127], a[12:15], v[28:31]\n\t"
      "v_cvt_pk_bf16_f32 v46, v52, v53\n\t"
      "v_cvt_pk_bf16_f32 v47, v54, v55\n\t"
      "v_mfma_f32_16x16x32_bf16 v[28:31], v[140:143], a[16:19], v[28:31]\n\t"
      "ds_write_b64 v0, v[46:47] offset:12640\n\t"
      "s_waitcnt lgkmcnt(3)\n\t"
      "v_mfma_f32_16x16x32_bf16 v[28:31], v[156:159], a[20:23], v[28:31]\n\t"
      "buffer_store_dwordx4 a[56:59], v11, s[44:47], s49 offen offset:2048 sc1\n\t"
      "v_mfma_f32_16x16x32_bf16 v[32:35], a[60:63], a[0:3], 0\n\t"
      "v_accvgpr_read_b32 v52, a172\n\t"
      "v_accvgpr_read_b32 v53, a173\n\t"
      "v_mfma_f32_16x16x32_bf16 v[32:35], a[68:71], a[4:7], v[32:35]\n\t"
      "v_accvgpr_read_b32 v54, a174\n\t"
      "v_accvgpr_read_b32 v55, a175\n\t"
      "v_mfma_f32_16x16x32_bf16 v[32:35], a[76:79], a[8:11], v[32:35]\n\t"
      "v_cvt_pk_bf16_f32 v48, v52, v53\n\t"
      "v_cvt_pk_bf16_f32 v49, v54, v55\n\t"
      "v_mfma_f32_16x16x32_bf16 v[32:35], a[84:87], a[12:15], v[32:35]\n\t"
      "ds_write_b64 v0, v[48:49] offset:12672\n\t"
      "v_mfma_f32_16x16x32_bf16 v[32:35], a[92:95], a[16:19], v[32:35]\n\t"
      "v_accvgpr_read_b32 v52, a176\n\t"
      "v_accvgpr_read_b32 v53, a177\n\t"
      "v_mfma_f32_16x16x32_bf16 v[32:35], a[100:103], a[20:23], v[32:35]\n\t"
      "v_accvgpr_read_b32 v54, a178\n\t"
      "v_accvgpr_read_b32 v55, a179\n\t"
      "v_mfma_f32_16x16x32_bf16 v[36:39], a[64:67], a[0:3], 0\n\t"
      "v_cvt_pk_bf16_f32 v50, v52, v53\n\t"
      "v_cvt_pk_bf16_f32 v51, v54, v55\n\t"
      "v_mfma_f32_16x16x32_bf16 v[36:39], a[72:75], a[4:7], v[36:39]\n\t"
      "ds_write_b64 v0, v[50:51] offset:12704\n\t"
      "v_mfma_f32_16x16x32_bf16 v[36:39], a[80:83], a[8:11], v[36:39]\n\t"
      "v_add_u32 v52, s51, v15\n\t"
      "v_xor_b32 v53, 64, v52\n\t"
      "v_mfma_f32_16x16x32_bf16 v[36:39], a[88:91], a[12:15], v[36:39]\n\t"
      "v_xor_b32 v54, 128, v52\n\t"
      "v_xor_b32 v55, 192, v52\n\t"
      "v_mfma_f32_16x16x32_bf16 v[36:39], a[96:99], a[16:19], v[36:39]\n\t"
      "s_waitcnt lgkmcnt(0)\n\t"
      "s_add_u32 s38, s36, s39\n\t"
      "s_sub_u32 s28, s36, s28\n\t"
      "s_sub_i32 s28, s28, 515\n\t"
      "s_max_i32 s28, s28, 0\n\t"
      "s_min_i32 s28, s28, 60\n\t"
      "s_and_b32 s39, s32, 15\n\t"
      "s_cselect_b32 s53, s29, 0\n\t"
      "s_memrealtime s[36:37]\n\t"
      "v_mfma_f32_16x16x32_bf16 v[36:39], a[104:107], a[20:23], v[36:39]\n\t"
      "s_add_u32 s39, s39, s53\n\t"
      "s_lshr_b64 s[32:33], s[32:33], 4\n\t"
      "s_add_u32 m0, s18, 86528\n\t"
      "v_mfma_f32_16x16x32_bf16 v[16:19], v[160:163], a[24:27], v[16:19]\n\t"
      "s_bitcmp1_b32 s91, 2\n\t"
      "s_cselect_b32 s54, 24576, 0\n\t"
      "s_add_u32 s54, s54, s14\n\t"
      "s_add_u32 s54, s54, 1572864\n\t"
      "v_mfma_f32_16x16x32_bf16 v[16:19], v[176:179], a[28:31], v[16:19]\n\t"
      "s_waitcnt vmcnt(6)\n\t"
      "s_barrier\n\t"
      "v_mfma_f32_16x16x32_bf16 v[16:19], v[192:195], a[32:35], v[16:19]\n\t"
      "ds_read_b128 a[0:3], v52 offset:0\n\t"
      "ds_read_b128 a[188:191], v8 offset:12544\n\t"
      "buffer_load_dwordx4 v5, s[40:43], s54 offen lds\n\t"
      "v_mfma_f32_16x16x32_bf16 v[16:19], v[208:211], a[36:39], v[16:19]\n\t"
      "ds_read_b128 a[4:7], v53 offset:0\n\t"
      "ds_read_b128 a[192:195], v60 offset:13568\n\t"
      "v_mfma_f32_16x16x32_bf16 v[16:19], v[224:227], a[40:43], v[16:19]\n\t"
      "ds_read_b128 a[8:11], v54 offset:0\n\t"
      "ds_read_b128 a[196:199], v61 offset:14592\n\t"
      "buffer_load_dwordx4 v6, s[40:43], s54 offen offset:1024 lds\n\t"
      "v_mfma_f32_16x16x32_bf16 v[16:19], v[240:243], a[44:47], v[16:19]\n\t"
      "ds_read_b128 a[12:15], v55 offset:0\n\t"
      "v_mfma_f32_16x16x32_bf16 v[20:23], v[164:167], a[24:27], v[20:23]\n\t"
      "ds_read_b128 a[16:19], v52 offset:256\n\t"
      "buffer_load_dwordx4 v7, s[40:43], s54 offen offset:2048 lds\n\t"
      "v_mfma_f32_16x16x32_bf16 v[20:23], v[180:183], a[28:31], v[20:23]\n\t"
      "ds_read_b128 a[20:23], v53 offset:256\n\t"
      "s_add_u32 m0, s18, s50\n\t"
      "v_mfma_f32_16x16x32_bf16 v[20:23], v[196:199], a[32:35], v[20:23]\n\t"
      "s_add_u32 s51, s51, 12288\n\t"
      "s_cmp_eq_u32 s51, 160256\n\t"
      "s_cselect_b32 s51, 25088, s51\n\t"
      "s_waitcnt lgkmcnt(7)\n\t"
      "v_mfma_f32_16x16x32_bf16 v[20:23], v[212:215], a[36:39], v[20:23]\n\t"
      "buffer_store_dwordx4 a[188:191], v12, s[44:47], s49 offen sc1\n\t"
      "v_mfma_f32_16x16x32_bf16 v[20:23], v[228:231], a[40:43], v[20:23]\n\t"
      "s_mov_b32 s87, 0\n\t"
      "v_mfma_f32_16x16x32_bf16 v[20:23], v[244:247], a[44:47], v[20:23]\n\t"
      "s_waitcnt lgkmcnt(0)\n\t"
      "s_sub_u32 s53, s36, s38\n\t"
      "s_cmp_lt_i32 s53, 0\n\t"
      "v_mfma_f32_16x16x32_bf16 v[24:27], v[168:171], a[24:27], v[24:27]\n\t"
      "s_cbranch_scc1 .Lspinf_%=\n\t"
      ".Lspunf_%=:\n\t"
      "s_sub_i32 s53, s53, 30\n\t"
      "s_max_i32 s53, s53, 0\n\t"
      "v_mfma_f32_16x16x32_bf16 v[24:27], v[184:187], a[28:31], v[24:27]\n\t"
      "s_add_u32 s38, s38, s53\n\t"
      "s_add_u32 s38, s38, s39\n\t"
      "s_add_u32 s28, s28, s53\n\t"
      "v_mfma_f32_16x16x32_bf16 v[24:27], v[200:203], a[32:35], v[24:27]\n\t"
      "v_cvt_pk_bf16_f32 v40, v16, v17\n\t"
      "v_cvt_pk_bf16_f32 v41, v18, v19\n\t"
      "v_mfma_f32_16x16x32_bf16 v[24:27], v[216:219], a[36:39], v[24:27]\n\t"
      "ds_write_b32 v0, v40 offset:0\n\t"
      "v_mfma_f32_16x16x32_bf16 v[24:27], v[232:235], a[40:43], v[24:27]\n\t"
      "ds_write_b32 v0, v41 offset:4\n\t"
      "v_mfma_f32_16x16x32_bf16 v[24:27], v[248:251], a[44:47], v[24:27]\n\t"
      "buffer_load_dwordx4 v2, s[40:43], s48 offen lds\n\t"
      "v_mfma_f32_16x16x32_bf16 v[28:31], v[172:175], a[24:27], v[28:31]\n\t"
      "v_cvt_pk_bf16_f32 v42, v20, v21\n\t"
      "v_cvt_pk_bf16_f32 v43, v22, v23\n\t"
      "v_mfma_f32_16x16x32_bf16 v[28:31], v[188:191], a[28:31], v[28:31]\n\t"
      "ds_write_b32 v0, v42 offset:32\n\t"
      "v_mfma_f32_16x16x32_bf16 v[28:31], v[204:207], a[32:35], v[28:31]\n\t"
      "ds_write_b32 v0, v43 offset:36\n\t"
      "v_mfma_f32_16x16x32_bf16 v[28:31], v[220:223], a[36:39], v[28:31]\n\t"
      "buffer_store_dwordx4 a[192:195], v13, s[44:47], s49 offen offset:1024 sc1\n\t"
      "v_mfma_f32_16x16x32_bf16 v[28:31], v[236:239], a[40:43], v[28:31]\n\t"
      "buffer_load_dwordx4 v3, s[40:43], s48 offen offset:1024 lds\n\t"
      "v_mfma_f32_16x16x32_bf16 v[28:31], v[252:255], a[44:47], v[28:31]\n\t"
      "v_cvt_pk_bf16_f32 v44, v24, v25\n\t"
      "v_cvt_pk_bf16_f32 v45, v26, v27\n\t"
      "v_mfma_f32_16x16x32_bf16 v[32:35], a[108:111], a[24:27], v[32:35]\n\t"
      "ds_write_b32 v0, v44 offset:64\n\t"
      "v_mfma_f32_16x16x32_bf16 v[32:35], a[116:119], a[28:31], v[32:35]\n\t"
      "ds_write_b32 v0, v45 offset:68\n\t"
      "v_mfma_f32_16x16x32_bf16 v[32:35], a[124:127], a[32:35], v[32:35]\n\t"
      "buffer_store_dwordx4 a[196:199], v14, s[44:47], s49 offen offset:2048 sc1\n\t"
      "v_mfma_f32_16x16x32_bf16 v[32:35], a[132:135], a[36:39], v[32:35]\n\t"
      "s_add_u32 s58, s58, 786432\n\t"
      "s_lshr_b32 s59, s59, 1\n\t"
      "v_mfma_f32_16x16x32_bf16 v[32:35], a[140:143], a[40:43], v[32:35]\n\t"
      "s_bitcmp1_b32 s59, 0\n\t"
      "s_cselect_b32 s49, 24576, 0\n\t"
      "s_add_u32 s49, s49, s58\n\t"
      "v_mfma_f32_16x16x32_bf16 v[32:35], a[148:151], a[44:47], v[32:35]\n\t"
      "v_cvt_pk_bf16_f32 v46, v28, v29\n\t"
      "v_cvt_pk_bf16_f32 v47, v30, v31\n\t"
      "v_mfma_f32_16x16x32_bf16 v[36:39], a[112:115], a[24:27], v[36:39]\n\t"
      "ds_write_b32 v0, v46 offset:96\n\t"
      "v_mfma_f32_16x16x32_bf16 v[36:39], a[120:123], a[28:31], v[36:39]\n\t"
      "ds_write_b32 v0, v47 offset:100\n\t"
      "v_mfma_f32_16x16x32_bf16 v[36:39], a[128:131], a[32:35], v[36:39]\n\t"
      "buffer_load_dwordx4 v4, s[40:43], s48 offen offset:2048 lds\n\t"
      "s_add_u32 s50, s50, 12288\n\t"
      "v_mfma_f32_16x16x32_bf16 v[36:39], a[136:139], a[36:39], v[36:39]\n\t"
      "s_cmp_eq_u32 s50, 160256\n\t"
      "s_cselect_b32 s50, 25088, s50\n\t"
      "v_mfma_f32_16x16x32_bf16 v[36:39], a[144:147], a[40:43], v[36:39]\n\t"
      "v_cvt_pk_bf16_f32 v48, v32, v33\n\t"
      "v_cvt_pk_bf16_f32 v49, v34, v35\n\t"
      "v_mfma_f32_16x16x32_bf16 v[36:39], a[152:155], a[44:47], v[36:39]\n\t"
      "s_cmp_eq_u32 s17, 6144\n\t"
      "s_cbranch_scc1 .Lmid1_%=\n\t"
      "s_cmp_eq_u32 s17, 12288\n\t"
      "s_cbranch_scc1 .Lmid2_%=\n\t"
      "s_cmp_eq_u32 s17, 18432\n\t"
      "s_cbranch_scc1 .Lmid3_%=\n\t"
      "global_load_dword v63, v62, s[12:13] sc1\n\t"
      "s_branch .Lmid0_%=\n\t"
      ".Lloop0_%=:\n\t"
      "v_mfma_f32_16x16x32_bf16 v[16:19], v[64:67], a[0:3], 0\n\t"
      "ds_write_b32 v0, v48 offset:12672\n\t"
      "v_mfma_f32_16x16x32_bf16 v[16:19], v[80:83], a[4:7], v[16:19]\n\t"
      "ds_write_b32 v0, v49 offset:12676\n\t"
      "v_mfma_f32_16x16x32_bf16 v[16:19], v[96:99], a[8:11], v[16:19]\n\t"
      "ds_read_b128 a[24:27], v58 offset:256\n\t"
      "v_mfma_f32_16x16x32_bf16 v[16:19], v[112:115], a[12:15], v[16:19]\n\t"
      "ds_read_b128 a[28:31], v59 offset:256\n\t"
      "v_mfma_f32_16x16x32_bf16 v[16:19], v[128:131], a[16:19], v[16:19]\n\t"
      "ds_read_b128 a[32:35], v56 offset:512\n\t"
      "v_mfma_f32_16x16x32_bf16 v[16:19], v[144:147], a[20:23], v[16:19]\n\t"
      "ds_read_b128 a[36:39], v57 offset:512\n\t"
      "v_mfma_f32_16x16x32_bf16 v[20:23], v[68:71], a[0:3], 0\n\t"
      "v_cvt_pk_bf16_f32 v50, v36, v37\n\t"
      "v_cvt_pk_bf16_f32 v51, v38, v39\n\t"
      "v_mfma_f32_16x16x32_bf16 v[20:23], v[84:87], a[4:7], v[20:23]\n\t"
      "ds_write_b32 v0, v50 offset:12704\n\t"
      "v_mfma_f32_16x16x32_bf16 v[20:23], v[100:103], a[8:11], v[20:23]\n\t"
      "ds_write_b32 v0, v51 offset:12708\n\t"
      "v_mfma_f32_16x16x32_bf16 v[20:23], v[116:119], a[12:15], v[20:23]\n\t"
      "ds_read_b128 a[40:43], v58 offset:512\n\t"
      "v_mfma_f32_16x16x32_bf16 v[20:23], v[132:135], a[16:19], v[20:23]\n\t"
      "ds_read_b128 a[44:47], v59 offset:512\n\t"
      "v_mfma_f32_16x16x32_bf16 v[20:23], v[148:151], a[20:23], v[20:23]\n\t"
      "s_add_u32 s56, s56, 786432\n\t"
      "s_lshr_b32 s57, s57, 1\n\t"
      "v_mfma_f32_16x16x32_bf16 v[24:27], v[72:75], a[0:3], 0\n\t"
      "s_bitcmp1_b32 s57, 0\n\t"
      "s_cselect_b32 s48, 24576, 0\n\t"
      "s_add_u32 s48, s48, s56\n\t"
      "v_mfma_f32_16x16x32_bf16 v[24:27], v[88:91], a[4:7], v[24:27]\n\t"
      "v_mfma_f32_16x16x32_bf16 v[24:27], v[104:107], a[8:11], v[24:27]\n\t"
      "v_mfma_f32_16x16x32_bf16 v[24:27], v[120:123], a[12:15], v[24:27]\n\t"
      "s_waitcnt vmcnt(12)\n\t"
      "s_waitcnt lgkmcnt(0)\n\t"
      "s_barrier\n\t"
      "v_mfma_f32_16x16x32_bf16 v[24:27], v[136:139], a[16:19], v[24:27]\n\t"
      "v_mfma_f32_16x16x32_bf16 v[24:27], v[152:155], a[20:23], v[24:27]\n\t"
      "v_mfma_f32_16x16x32_bf16 v[28:31], v[76:79], a[0:3], 0\n\t"
      "v_mfma_f32_16x16x32_bf16 v[28:31], v[92:95], a[4:7], v[28:31]\n\t"
      "ds_read_b128 a[48:51], v8 offset:12544\n\t"
      "v_mfma_f32_16x16x32_bf16 v[28:31], v[108:111], a[8:11], v[28:31]\n\t"
      "ds_read_b128 a[52:55], v60 offset:13568\n\t"
      "v_mfma_f32_16x16x32_bf16 v[28:31], v[124:127], a[12:15], v[28:31]\n\t"
      "ds_read_b128 a[56:59], v61 offset:14592\n\t"
      "v_mfma_f32_16x16x32_bf16 v[28:31], v[140:143], a[16:19], v[28:31]\n\t"
      "s_and_b32 s39, s32, 15\n\t"
      "s_cselect_b32 s53, s29, 0\n\t"
      "v_mfma_f32_16x16x32_bf16 v[28:31], v[156:159], a[20:23], v[28:31]\n\t"
      "s_add_u32 s39, s39, s53\n\t"
      "s_lshr_b64 s[32:33], s[32:33], 4\n\t"
      "v_mfma_f32_16x16x32_bf16 v[32:35], a[60:63], a[0:3], 0\n\t"
      "s_cmp_le_u32 s52, 12\n\t"
      "s_cselect_b32 s53, 3, 0\n\t"
      "s_min_u32 s53, s28, s53\n\t"
      "v_mfma_f32_16x16x32_bf16 v[32:35], a[68:71], a[4:7], v[32:35]\n\t"
      "s_sub_u32 s28, s28, s53\n\t"
      "s_sub_u32 s39, s39, s53\n\t"
      "v_mfma_f32_16x16x32_bf16 v[32:35], a[76:79], a[8:11], v[32:35]\n\t"
      "s_waitcnt lgkmcnt(2)\n\t"
      "v_mfma_f32_16x16x32_bf16 v[32:35], a[84:87], a[12:15], v[32:35]\n\t"
      "buffer_store_dwordx4 a[48:51], v12, s[44:47], s49 offen sc1\n\t"
      "v_mfma_f32_16x16x32_bf16 v[32:35], a[92:95], a[16:19], v[32:35]\n\t"
      "v_add_u32 v52, s51, v15\n\t"
      "v_xor_b32 v53, 64, v52\n\t"
      "v_mfma_f32_16x16x32_bf16 v[32:35], a[100:103], a[20:23], v[32:35]\n\t"
      "v_xor_b32 v54, 128, v52\n\t"
      "v_xor_b32 v55, 192, v52\n\t"
      "v_mfma_f32_16x16x32_bf16 v[36:39], a[64:67], a[0:3], 0\n\t"
      "s_add_u32 m0, s18, s50\n\t"
      "v_mfma_f32_16x16x32_bf16 v[36:39], a[72:75], a[4:7], v[36:39]\n\t"
      "buffer_load_dwordx4 v2, s[40:43], s48 offen nt lds\n\t"
      "v_mfma_f32_16x16x32_bf16 v[36:39], a[80:83], a[8:11], v[36:39]\n\t"
      "s_memrealtime s[36:37]\n\t"
      "v_mfma_f32_16x16x32_bf16 v[36:39], a[88:91], a[12:15], v[36:39]\n\t"
      "s_waitcnt lgkmcnt(1)\n\t"
      "v_mfma_f32_16x16x32_bf16 v[36:39], a[96:99], a[16:19], v[36:39]\n\t"
      "buffer_store_dwordx4 a[52:55], v13, s[44:47], s49 offen offset:1024 sc1\n\t"
      "v_mfma_f32_16x16x32_bf16 v[36:39], a[104:107], a[20:23], v[36:39]\n\t"
      "v_mfma_f32_16x16x32_bf16 v[16:19], v[160:163], a[24:27], v[16:19]\n\t"
      "ds_read_b128 a[0:3], v52 offset:0\n\t"
      "v_mfma_f32_16x16x32_bf16 v[16:19], v[176:179], a[28:31], v[16:19]\n\t"
      "ds_read_b128 a[4:7], v53 offset:0\n\t"
      "v_mfma_f32_16x16x32_bf16 v[16:19], v[192:195], a[32:35], v[16:19]\n\t"
      "ds_read_b128 a[8:11], v54 offset:0\n\t"
      "v_mfma_f32_16x16x32_bf16 v[16:19], v[208:211], a[36:39], v[16:19]\n\t"
      "ds_read_b128 a[12:15], v55 offset:0\n\t"
      "v_mfma_f32_16x16x32_bf16 v[16:19], v[224:227], a[40:43], v[16:19]\n\t"
      "ds_read_b128 a[16:19], v52 offset:256\n\t"
      "v_mfma_f32_16x16x32_bf16 v[16:19], v[240:243], a[44:47], v[16:19]\n\t"
      "ds_read_b128 a[20:23], v53 offset:256\n\t"
      "v_mfma_f32_16x16x32_bf16 v[20:23], v[164:167], a[24:27], v[20:23]\n\t"
      "s_add_u32 s51, s51, 12288\n\t"
      "s_cmp_eq_u32 s51, 160256\n\t"
      "s_cselect_b32 s51, 25088, s51\n\t"
      "v_mfma_f32_16x16x32_bf16 v[20:23], v[180:183], a[28:31], v[20:23]\n\t"
      "s_waitcnt lgkmcnt(6)\n\t"
      "v_mfma_f32_16x16x32_bf16 v[20:23], v[196:199], a[32:35], v[20:23]\n\t"
      "buffer_store_dwordx4 a[56:59], v14, s[44:47], s49 offen offset:2048 sc1\n\t"
      "v_mfma_f32_16x16x32_bf16 v[20:23], v[212:215], a[36:39], v[20:23]\n\t"
      "s_cmp_eq_u32 s52, 14\n\t"
      "s_cbranch_scc1 .Lshb_%=\n\t"
      ".Lshbr_%=:\n\t"
      "v_mfma_f32_16x16x32_bf16 v[20:23], v[228:231], a[40:43], v[20:23]\n\t"
      "s_waitcnt lgkmcnt(0)\n\t"
      "s_sub_u32 s53, s36, s38\n\t"
      "s_cmp_lt_i32 s53, 0\n\t"
      "v_mfma_f32_16x16x32_bf16 v[20:23], v[244:247], a[44:47], v[20:23]\n\t"
      "s_cbranch_scc1 .Lspin0_%=\n\t"
      ".Lspun0_%=:\n\t"
      "s_sub_i32 s53, s53, 30\n\t"
      "s_max_i32 s53, s53, 0\n\t"
      "v_mfma_f32_16x16x32_bf16 v[24:27], v[168:171], a[24:27], v[24:27]\n\t"
      "s_add_u32 s38, s38, s53\n\t"
      "s_add_u32 s38, s38, s39\n\t"
      "s_add_u32 s28, s28, s53\n\t"
      "v_mfma_f32_16x16x32_bf16 v[24:27], v[184:187], a[28:31], v[24:27]\n\t"
      "v_cvt_pk_bf16_f32 v40, v16, v17\n\t"
      "v_cvt_pk_bf16_f32 v41, v18, v19\n\t"
      "v_mfma_f32_16x16x32_bf16 v[24:27], v[200:203], a[32:35], v[24:27]\n\t"
      "ds_write_b32 v0, v40 offset:0\n\t"
      "v_mfma_f32_16x16x32_bf16 v[24:27], v[216:219], a[36:39], v[24:27]\n\t"
      "ds_write_b32 v0, v41 offset:4\n\t"
      "v_mfma_f32_16x16x32_bf16 v[24:27], v[232:235], a[40:43], v[24:27]\n\t"
      "buffer_load_dwordx4 v3, s[40:43], s48 offen offset:1024 nt lds\n\t"
      "v_mfma_f32_16x16x32_bf16 v[24:27], v[248:251], a[44:47], v[24:27]\n\t"
      "v_cvt_pk_bf16_f32 v42, v20, v21\n\t"
      "v_cvt_pk_bf16_f32 v43, v22, v23\n\t"
      "v_mfma_f32_16x16x32_bf16 v[28:31], v[172:175], a[24:27], v[28:31]\n\t"
      "ds_write_b32 v0, v42 offset:32\n\t"
      "v_mfma_f32_16x16x32_bf16 v[28:31], v[188:191], a[28:31], v[28:31]\n\t"
      "ds_write_b32 v0, v43 offset:36\n\t"
      "v_mfma_f32_16x16x32_bf16 v[28:31], v[204:207], a[32:35], v[28:31]\n\t"
      "s_add_u32 s58, s58, 786432\n\t"
      "s_lshr_b32 s59, s59, 1\n\t"
      "v_mfma_f32_16x16x32_bf16 v[28:31], v[220:223], a[36:39], v[28:31]\n\t"
      "s_bitcmp1_b32 s59, 0\n\t"
      "s_cselect_b32 s49, 24576, 0\n\t"
      "s_add_u32 s49, s49, s58\n\t"
      "v_mfma_f32_16x16x32_bf16 v[28:31], v[236:239], a[40:43], v[28:31]\n\t"
      "s_sub_i32 s87, s87, 6\n\t"
      "s_max_i32 s87, s87, 0\n\t"
      "v_mfma_f32_16x16x32_bf16 v[28:31], v[252:255], a[44:47], v[28:31]\n\t"
      "v_cvt_pk_bf16_f32 v44, v24, v25\n\t"
      "v_cvt_pk_bf16_f32 v45, v26, v27\n\t"
      "v_mfma_f32_16x16x32_bf16 v[32:35], a[108:111], a[24:27], v[32:35]\n\t"
      "ds_write_b32 v0, v44 offset:64\n\t"
      "v_mfma_f32_16x16x32_bf16 v[32:35], a[116:119], a[28:31], v[32:35]\n\t"
      "ds_write_b32 v0, v45 offset:68\n\t"
      "v_mfma_f32_16x16x32_bf16 v[32:35], a[124:127], a[32:35], v[32:35]\n\t"
      "s_cmp_gt_u32 s28, 300\n\t"
      "s_cselect_b32 s53, s53, 0\n\t"
      "v_mfma_f32_16x16x32_bf16 v[32:35], a[132:135], a[36:39], v[32:35]\n\t"
      "buffer_load_dwordx4 v4, s[40:43], s48 offen offset:2048 nt lds\n\t"
      "s_add_u32 s50, s50, 12288\n\t"
      "v_mfma_f32_16x16x32_bf16 v[32:35], a[140:143], a[40:43], v[32:35]\n\t"
      "s_cmp_eq_u32 s50, 160256\n\t"
      "s_cselect_b32 s50, 25088, s50\n\t"
      "v_mfma_f32_16x16x32_bf16 v[32:35], a[148:151], a[44:47], v[32:35]\n\t"
      "v_cvt_pk_bf16_f32 v46, v28, v29\n\t"
      "v_cvt_pk_bf16_f32 v47, v30, v31\n\t"
      "v_mfma_f32_16x16x32_bf16 v[36:39], a[112:115], a[24:27], v[36:39]\n\t"
      "ds_write_b32 v0, v46 offset:96\n\t"
      "v_mfma_f32_16x16x32_bf16 v[36:39], a[120:123], a[28:31], v[36:39]\n\t"
      "ds_write_b32 v0, v47 offset:100\n\t"
      "v_mfma_f32_16x16x32_bf16 v[36:39], a[128:131], a[32:35], v[36:39]\n\t"
      "s_cmp_gt_u32 s53, 10\n\t"
      "s_cselect_b32 s87, 44, s87\n\t"
      "s_add_u32 s38, s38, s87\n\t"
      "v_mfma_f32_16x16x32_bf16 v[36:39], a[136:139], a[36:39], v[36:39]\n\t"
      "v_mfma_f32_16x16x32_bf16 v[36:39], a[144:147], a[40:43], v[36:39]\n\t"
      "v_cvt_pk_bf16_f32 v48, v32, v33\n\t"
      "v_cvt_pk_bf16_f32 v49, v34, v35\n\t"
      "v_mfma_f32_16x16x32_bf16 v[36:39], a[152:155], a[44:47], v[36:39]\n\t"
      ".Lmid0_%=:\n\t"
      "v_mfma_f32_16x16x32_bf16 v[16:19], v[64:67], a[0:3], 0\n\t"
      "ds_write_b32 v0, v48 offset:128\n\t"
      "v_mfma_f32_16x16x32_bf16 v[16:19], v[80:83], a[4:7], v[16:19]\n\t"
      "ds_write_b32 v0, v49 offset:132\n\t"
      "v_mfma_f32_16x16x32_bf16 v[16:19], v[96:99], a[8:11], v[16:19]\n\t"
      "ds_read_b128 a[24:27], v54 offset:256\n\t"
      "v_mfma_f32_16x16x32_bf16 v[16:19], v[112:115], a[12:15], v[16:19]\n\t"
      "ds_read_b128 a[28:31], v55 offset:256\n\t"
      "v_mfma_f32_16x16x32_bf16 v[16:19], v[128:131], a[16:19], v[16:19]\n\t"
      "ds_read_b128 a[32:35], v52 offset:512\n\t"
      "v_mfma_f32_16x16x32_bf16 v[16:19], v[144:147], a[20:23], v[16:19]\n\t"
      "ds_read_b128 a[36:39], v53 offset:512\n\t"
      "v_mfma_f32_16x16x32_bf16 v[20:23], v[68:71], a[0:3], 0\n\t"
      "v_cvt_pk_bf16_f32 v50, v36, v37\n\t"
      "v_cvt_pk_bf16_f32 v51, v38, v39\n\t"
      "v_mfma_f32_16x16x32_bf16 v[20:23], v[84:87], a[4:7], v[20:23]\n\t"
      "ds_write_b32 v0, v50 offset:160\n\t"
      "v_mfma_f32_16x16x32_bf16 v[20:23], v[100:103], a[8:11], v[20:23]\n\t"
      "ds_write_b32 v0, v51 offset:164\n\t"
      "v_mfma_f32_16x16x32_bf16 v[20:23], v[116:119], a[12:15], v[20:23]\n\t"
      "ds_read_b128 a[40:43], v54 offset:512\n\t"
      "v_mfma_f32_16x16x32_bf16 v[20:23], v[132:135], a[16:19], v[20:23]\n\t"
      "ds_read_b128 a[44:47], v55 offset:512\n\t"
      "v_mfma_f32_16x16x32_bf16 v[20:23], v[148:151], a[20:23], v[20:23]\n\t"
      "v_mfma_f32_16x16x32_bf16 v[24:27], v[72:75], a[0:3], 0\n\t"
      "v_mfma_f32_16x16x32_bf16 v[24:27], v[88:91], a[4:7], v[24:27]\n\t"
      "v_mfma_f32_16x16x32_bf16 v[24:27], v[104:107], a[8:11], v[24:27]\n\t"
      "v_mfma_f32_16x16x32_bf16 v[24:27], v[120:123], a[12:15], v[24:27]\n\t"
      "s_waitcnt vmcnt(12)\n\t"
      "s_waitcnt lgkmcnt(0)\n\t"
      "s_barrier\n\t"
      "v_mfma_f32_16x16x32_bf16 v[24:27], v[136:139], a[16:19], v[24:27]\n\t"
      "v_mfma_f32_16x16x32_bf16 v[24:27], v[152:155], a[20:23], v[24:27]\n\t"
      "v_mfma_f32_16x16x32_bf16 v[28:31], v[76:79], a[0:3], 0\n\t"
      "v_mfma_f32_16x16x32_bf16 v[28:31], v[92:95], a[4:7], v[28:31]\n\t"
      "ds_read_b128 a[48:51], v8 offset:0\n\t"
      "v_mfma_f32_16x16x32_bf16 v[28:31], v[108:111], a[8:11], v[28:31]\n\t"
      "ds_read_b128 a[52:55], v60 offset:1024\n\t"
      "v_mfma_f32_16x16x32_bf16 v[28:31], v[124:127], a[12:15], v[28:31]\n\t"
      "ds_read_b128 a[56:59], v61 offset:2048\n\t"
      "v_mfma_f32_16x16x32_bf16 v[28:31], v[140:143], a[16:19], v[28:31]\n\t"
      "s_and_b32 s39, s34, 15\n\t"
      "s_cselect_b32 s53, s29, 0\n\t"
      "v_mfma_f32_16x16x32_bf16 v[28:31], v[156:159], a[20:23], v[28:31]\n\t"
      "s_add_u32 s39, s39, s53\n\t"
      "s_lshr_b64 s[34:35], s[34:35], 4\n\t"
      "v_mfma_f32_16x16x32_bf16 v[32:35], a[60:63], a[0:3], 0\n\t"
      "s_cmp_le_u32 s52, 12\n\t"
      "s_cselect_b32 s53, 3, 0\n\t"
      "s_min_u32 s53, s28, s53\n\t"
      "v_mfma_f32_16x16x32_bf16 v[32:35], a[68:71], a[4:7], v[32:35]\n\t"
      "s_sub_u32 s28, s28, s53\n\t"
      "s_sub_u32 s39, s39, s53\n\t"
      "v_mfma_f32_16x16x32_bf16 v[32:35], a[76:79], a[8:11], v[32:35]\n\t"
      "s_waitcnt lgkmcnt(2)\n\t"
      "v_mfma_f32_16x16x32_bf16 v[32:35], a[84:87], a[12:15], v[32:35]\n\t"
      "buffer_store_dwordx4 a[48:51], v9, s[44:47], s49 offen sc1\n\t"
      "v_mfma_f32_16x16x32_bf16 v[32:35], a[92:95], a[16:19], v[32:35]\n\t"
      "v_add_u32 v56, s51, v15\n\t"
      "v_xor_b32 v57, 64, v56\n\t"
      "v_mfma_f32_16x16x32_bf16 v[32:35], a[100:103], a[20:23], v[32:35]\n\t"
      "v_xor_b32 v58, 128, v56\n\t"
      "v_xor_b32 v59, 192, v56\n\t"
      "v_mfma_f32_16x16x32_bf16 v[36:39], a[64:67], a[0:3], 0\n\t"
      "s_add_u32 m0, s18, s50\n\t"
      "v_mfma_f32_16x16x32_bf16 v[36:39], a[72:75], a[4:7], v[36:39]\n\t"
      "buffer_load_dwordx4 v5, s[40:43], s48 offen nt lds\n\t"
      "v_mfma_f32_16x16x32_bf16 v[36:39], a[80:83], a[8:11], v[36:39]\n\t"
      "s_memrealtime s[36:37]\n\t"
      "v_mfma_f32_16x16x32_bf16 v[36:39], a[88:91], a[12:15], v[36:39]\n\t"
      "s_waitcnt lgkmcnt(1)\n\t"
      "v_mfma_f32_16x16x32_bf16 v[36:39], a[96:99], a[16:19], v[36:39]\n\t"
      "buffer_store_dwordx4 a[52:55], v10, s[44:47], s49 offen offset:1024 sc1\n\t"
      "v_mfma_f32_16x16x32_bf16 v[36:39], a[104:107], a[20:23], v[36:39]\n\t"
      "v_mfma_f32_16x16x32_bf16 v[16:19], v[160:163], a[24:27], v[16:19]\n\t"
      "ds_read_b128 a[0:3], v56 offset:0\n\t"
      "v_mfma_f32_16x16x32_bf16 v[16:19], v[176:179], a[28:31], v[16:19]\n\t"
      "ds_read_b128 a[4:7], v57 offset:0\n\t"
      "v_mfma_f32_16x16x32_bf16 v[16:19], v[192:195], a[32:35], v[16:19]\n\t"
      "ds_read_b128 a[8:11], v58 offset:0\n\t"
      "v_mfma_f32_16x16x32_bf16 v[16:19], v[208:211], a[36:39], v[16:19]\n\t"
      "ds_read_b128 a[12:15], v59 offset:0\n\t"
      "v_mfma_f32_16x16x32_bf16 v[16:19], v[224:227], a[40:43], v[16:19]\n\t"
      "ds_read_b128 a[16:19], v56 offset:256\n\t"
      "v_mfma_f32_16x16x32_bf16 v[16:19], v[240:243], a[44:47], v[16:19]\n\t"
      "ds_read_b128 a[20:23], v57 offset:256\n\t"
      "v_mfma_f32_16x16x32_bf16 v[20:23], v[164:167], a[24:27], v[20:23]\n\t"
      "s_add_u32 s51, s51, 12288\n\t"
      "s_cmp_eq_u32 s51, 160256\n\t"
      "s_cselect_b32 s51, 25088, s51\n\t"
      "v_mfma_f32_16x16x32_bf16 v[20:23], v[180:183], a[28:31], v[20:23]\n\t"
      "s_waitcnt lgkmcnt(6)\n\t"
      "v_mfma_f32_16x16x32_bf16 v[20:23], v[196:199], a[32:35], v[20:23]\n\t"
      "buffer_store_dwordx4 a[56:59], v11, s[44:47], s49 offen offset:2048 sc1\n\t"
      "v_mfma_f32_16x16x32_bf16 v[20:23], v[212:215], a[36:39], v[20:23]\n\t"
      "v_mfma_f32_16x16x32_bf16 v[20:23], v[228:231], a[40:43], v[20:23]\n\t"
      "s_waitcnt lgkmcnt(0)\n\t"
      "s_sub_u32 s53, s36, s38\n\t"
      "s_cmp_lt_i32 s53, 0\n\t"
      "v_mfma_f32_16x16x32_bf16 v[20:23], v[244:247], a[44:47], v[20:23]\n\t"
      "s_cbranch_scc1 .Lspin1_%=\n\t"
      ".Lspun1_%=:\n\t"
      "s_sub_i32 s53, s53, 30\n\t"
      "s_max_i32 s53, s53, 0\n\t"
      "v_mfma_f32_16x16x32_bf16 v[24:27], v[168:171], a[24:27], v[24:27]\n\t"
      "s_add_u32 s38, s38, s53\n\t"
      "s_add_u32 s38, s38, s39\n\t"
      "s_add_u32 s28, s28, s53\n\t"
      "v_mfma_f32_16x16x32_bf16 v[24:27], v[184:187], a[28:31], v[24:27]\n\t"
      "v_cvt_pk_bf16_f32 v40, v16, v17\n\t"
      "v_cvt_pk_bf16_f32 v41, v18, v19\n\t"
      "v_mfma_f32_16x16x32_bf16 v[24:27], v[200:203], a[32:35], v[24:27]\n\t"
      "ds_write_b32 v0, v40 offset:12544\n\t"
      "v_mfma_f32_16x16x32_bf16 v[24:27], v[216:219], a[36:39], v[24:27]\n\t"
      "ds_write_b32 v0, v41 offset:12548\n\t"
      "v_mfma_f32_16x16x32_bf16 v[24:27], v[232:235], a[40:43], v[24:27]\n\t"
      "buffer_load_dwordx4 v6, s[40:43], s48 offen offset:1024 nt lds\n\t"
      "v_mfma_f32_16x16x32_bf16 v[24:27], v[248:251], a[44:47], v[24:27]\n\t"
      "v_cvt_pk_bf16_f32 v42, v20, v21\n\t"
      "v_cvt_pk_bf16_f32 v43, v22, v23\n\t"
      "v_mfma_f32_16x16x32_bf16 v[28:31], v[172:175], a[24:27], v[28:31]\n\t"
      "ds_write_b32 v0, v42 offset:12576\n\t"
      "v_mfma_f32_16x16x32_bf16 v[28:31], v[188:191], a[28:31], v[28:31]\n\t"
      "ds_write_b32 v0, v43 offset:12580\n\t"
      "v_mfma_f32_16x16x32_bf16 v[28:31], v[204:207], a[32:35], v[28:31]\n\t"
      "v_mfma_f32_16x16x32_bf16 v[28:31], v[220:223], a[36:39], v[28:31]\n\t"
      "v_mfma_f32_16x16x32_bf16 v[28:31], v[236:239], a[40:43], v[28:31]\n\t"
      "s_sub_i32 s87, s87, 6\n\t"
      "s_max_i32 s87, s87, 0\n\t"
      "v_mfma_f32_16x16x32_bf16 v[28:31], v[252:255], a[44:47], v[28:31]\n\t"
      "v_cvt_pk_bf16_f32 v44, v24, v25\n\t"
      "v_cvt_pk_bf16_f32 v45, v26, v27\n\t"
      "v_mfma_f32_16x16x32_bf16 v[32:35], a[108:111], a[24:27], v[32:35]\n\t"
      "ds_write_b32 v0, v44 offset:12608\n\t"
      "v_mfma_f32_16x16x32_bf16 v[32:35], a[116:119], a[28:31], v[32:35]\n\t"
      "ds_write_b32 v0, v45 offset:12612\n\t"
      "v_mfma_f32_16x16x32_bf16 v[32:35], a[124:127], a[32:35], v[32:35]\n\t"
      "s_cmp_gt_u32 s28, 300\n\t"
      "s_cselect_b32 s53, s53, 0\n\t"
      "v_mfma_f32_16x16x32_bf16 v[32:35], a[132:135], a[36:39], v[32:35]\n\t"
      "buffer_load_dwordx4 v7, s[40:43], s48 offen offset:2048 nt lds\n\t"
      "s_add_u32 s50, s50, 12288\n\t"
      "v_mfma_f32_16x16x32_bf16 v[32:35], a[140:143], a[40:43], v[32:35]\n\t"
      "s_cmp_eq_u32 s50, 160256\n\t"
      "s_cselect_b32 s50, 25088, s50\n\t"
      "v_mfma_f32_16x16x32_bf16 v[32:35], a[148:151], a[44:47], v[32:35]\n\t"
      "v_cvt_pk_bf16_f32 v46, v28, v29\n\t"
      "v_cvt_pk_bf16_f32 v47, v30, v31\n\t"
      "v_mfma_f32_16x16x32_bf16 v[36:39], a[112:115], a[24:27], v[36:39]\n\t"
      "ds_write_b32 v0, v46 offset:12640\n\t"
      "v_mfma_f32_16x16x32_bf16 v[36:39], a[120:123], a[28:31], v[36:39]\n\t"
      "ds_write_b32 v0, v47 offset:12644\n\t"
      "v_mfma_f32_16x16x32_bf16 v[36:39], a[128:131], a[32:35], v[36:39]\n\t"
      "s_cmp_gt_u32 s53, 10\n\t"
      "s_cselect_b32 s87, 44, s87\n\t"
      "s_add_u32 s38, s38, s87\n\t"
      "v_mfma_f32_16x16x32_bf16 v[36:39], a[136:139], a[36:39], v[36:39]\n\t"
      "s_sub_u32 s52, s52, 1\n\t"
      "s_cmp_lg_u32 s52, 0\n\t"
      "v_mfma_f32_16x16x32_bf16 v[36:39], a[144:147], a[40:43], v[36:39]\n\t"
      "v_cvt_pk_bf16_f32 v48, v32, v33\n\t"
      "v_cvt_pk_bf16_f32 v49, v34, v35\n\t"
      "v_mfma_f32_16x16x32_bf16 v[36:39], a[152:155], a[44:47], v[36:39]\n\t"
      "s_cbranch_scc1 .Lloop0_%=\n\t"
      "s_branch .Lendc_%=\n\t"
      ".Lshb_%=:\n\t"
      "s_waitcnt vmcnt(9)\n\t"
      "v_bfe_u32 v63, v63, 17, 8\n\t"
      "v_min_u32 v62, 1, v63\n\t"
      "v_lshl_add_u32 v63, v62, 16, v63\n\t"
      "s_nop 1\n\t"
      "v_add_u32_dpp v63, v63, v63 row_shr:1 row_mask:0xf bank_mask:0xf bound_ctrl:0\n\t"
      "s_nop 1\n\t"
      "v_add_u32_dpp v63, v63, v63 row_shr:2 row_mask:0xf bank_mask:0xf bound_ctrl:0\n\t"
      "s_nop 1\n\t"
      "v_add_u32_dpp v63, v63, v63 row_shr:4 row_mask:0xf bank_mask:0xf bound_ctrl:0\n\t"
      "s_nop 1\n\t"
      "v_add_u32_dpp v63, v63, v63 row_shr:8 row_mask:0xf bank_mask:0xf bound_ctrl:0\n\t"
      "s_nop 0\n\t"
      "v_readlane_b32 s64, v63, 15\n\t"
      "v_readlane_b32 s65, v63, 31\n\t"
      "s_nop 1\n\t"
      "s_add_u32 s64, s64, s65\n\t"
      "s_lshr_b32 s65, s64, 16\n\t"
      "s_and_b32 s64, s64, 0xffff\n\t"
      "s_mov_b32 s66, 0\n\t"
      "s_mul_i32 s67, s65, 934\n\t"
      "s_lshl_b32 s69, s64, 3\n\t"
      "s_cmp_lt_u32 s69, s67\n\t"
      "s_cselect_b32 s66, 3, s66\n\t"
      "s_add_u32 s29, s29, s66\n\t"
      "s_mul_i32 s67, s65, 737\n\t"
      "s_cmp_lt_u32 s69, s67\n\t"
      "s_cbranch_scc0 .Lshbr_%=\n\t"
      "s_movk_i32 s29, 79\n\t"
      "s_mov_b32 s32, 0x11111111\n\t"
      "s_mov_b32 s33, 0x11111111\n\t"
      "s_mov_b32 s34, 0x11111111\n\t"
      "s_mov_b32 s35, 0x11111111\n\t"
      "s_sub_i32 s28, s28, 30\n\t"
      "s_min_i32 s66, s28, 0\n\t"
      "s_sub_u32 s38, s38, s66\n\t"
      "s_max_i32 s28, s28, 0\n\t"
      "s_branch .Lshbr_%=\n\t"
      ".Lloop1_%=:\n\t"
      "v_mfma_f32_16x16x32_bf16 v[16:19], v[64:67], a[0:3], 0\n\t"
      "ds_write_b32 v0, v48 offset:12672\n\t"
      "v_mfma_f32_16x16x32_bf16 v[16:19], v[80:83], a[4:7], v[16:19]\n\t"
      "ds_write_b32 v0, v49 offset:12676\n\t"
      "v_mfma_f32_16x16x32_bf16 v[16:19], v[96:99], a[8:11], v[16:19]\n\t"
      "ds_read_b128 a[24:27], v58 offset:256\n\t"
      "v_mfma_f32_16x16x32_bf16 v[16:19], v[112:115], a[12:15], v[16:19]\n\t"
      "ds_read_b128 a[28:31], v59 offset:256\n\t"
      "v_mfma_f32_16x16x32_bf16 v[16:19], v[128:131], a[16:19], v[16:19]\n\t"
      "ds_read_b128 a[32:35], v56 offset:512\n\t"
      "v_mfma_f32_16x16x32_bf16 v[16:19], v[144:147], a[20:23], v[16:19]\n\t"
      "ds_read_b128 a[36:39], v57 offset:512\n\t"
      "v_mfma_f32_16x16x32_bf16 v[20:23], v[68:71], a[0:3], 0\n\t"
      "v_cvt_pk_bf16_f32 v50, v36, v37\n\t"
      "v_cvt_pk_bf16_f32 v51, v38, v39\n\t"
      "v_mfma_f32_16x16x32_bf16 v[20:23], v[84:87], a[4:7], v[20:23]\n\t"
      "ds_write_b32 v0, v50 offset:12704\n\t"
      "v_mfma_f32_16x16x32_bf16 v[20:23], v[100:103], a[8:11], v[20:23]\n\t"
      "ds_write_b32 v0, v51 offset:12708\n\t"
      "v_mfma_f32_16x16x32_bf16 v[20:23], v[116:119], a[12:15], v[20:23]\n\t"
      "ds_read_b128 a[40:43], v58 offset:512\n\t"
      "v_mfma_f32_16x16x32_bf16 v[20:23], v[132:135], a[16:19], v[20:23]\n\t"
      "ds_read_b128 a[44:47], v59 offset:512\n\t"
      "v_mfma_f32_16x16x32_bf16 v[20:23], v[148:151], a[20:23], v[20:23]\n\t"
      "s_add_u32 s56, s56, 786432\n\t"
      "s_lshr_b32 s57, s57, 1\n\t"
      "v_mfma_f32_16x16x32_bf16 v[24:27], v[72:75], a[0:3], 0\n\t"
      "s_bitcmp1_b32 s57, 0\n\t"
      "s_cselect_b32 s48, 24576, 0\n\t"
      "s_add_u32 s48, s48, s56\n\t"
      "v_mfma_f32_16x16x32_bf16 v[24:27], v[88:91], a[4:7], v[24:27]\n\t"
      "v_mfma_f32_16x16x32_bf16 v[24:27], v[104:107], a[8:11], v[24:27]\n\t"
      "v_mfma_f32_16x16x32_bf16 v[24:27], v[120:123], a[12:15], v[24:27]\n\t"
      "v_mfma_f32_16x16x32_bf16 v[24:27], v[136:139], a[16:19], v[24:27]\n\t"
      "s_waitcnt vmcnt(12)\n\t"
      "s_waitcnt lgkmcnt(0)\n\t"
      "s_barrier\n\t"
      "v_mfma_f32_16x16x32_bf16 v[24:27], v[152:155], a[20:23], v[24:27]\n\t"
      "v_mfma_f32_16x16x32_bf16 v[28:31], v[76:79], a[0:3], 0\n\t"
      "v_mfma_f32_16x16x32_bf16 v[28:31], v[92:95], a[4:7], v[28:31]\n\t"
      "ds_read_b128 a[48:51], v8 offset:12544\n\t"
      "v_mfma_f32_16x16x32_bf16 v[28:31], v[108:111], a[8:11], v[28:31]\n\t"
      "ds_read_b128 a[52:55], v60 offset:13568\n\t"
      "v_mfma_f32_16x16x32_bf16 v[28:31], v[124:127], a[12:15], v[28:31]\n\t"
      "ds_read_b128 a[56:59], v61 offset:14592\n\t"
      "v_mfma_f32_16x16x32_bf16 v[28:31], v[140:143], a[16:19], v[28:31]\n\t"
      "v_mfma_f32_16x16x32_bf16 v[28:31], v[156:159], a[20:23], v[28:31]\n\t"
      "v_mfma_f32_16x16x32_bf16 v[32:35], a[60:63], a[0:3], 0\n\t"
      "v_mfma_f32_16x16x32_bf16 v[32:35], a[68:71], a[4:7], v[32:35]\n\t"
      "v_mfma_f32_16x16x32_bf16 v[32:35], a[76:79], a[8:11], v[32:35]\n\t"
      "s_waitcnt lgkmcnt(2)\n\t"
      "v_mfma_f32_16x16x32_bf16 v[32:35], a[84:87], a[12:15], v[32:35]\n\t"
      "buffer_store_dwordx4 a[48:51], v12, s[44:47], s49 offen sc1\n\t"
      "v_mfma_f32_16x16x32_bf16 v[32:35], a[92:95], a[16:19], v[32:35]\n\t"
      "v_add_u32 v52, s51, v15\n\t"
      "v_xor_b32 v53, 64, v52\n\t"
      "v_mfma_f32_16x16x32_bf16 v[32:35], a[100:103], a[20:23], v[32:35]\n\t"
      "v_xor_b32 v54, 128, v52\n\t"
      "v_xor_b32 v55, 192, v52\n\t"
      "v_mfma_f32_16x16x32_bf16 v[36:39], a[64:67], a[0:3], 0\n\t"
      "s_add_u32 m0, s18, s50\n\t"
      "v_mfma_f32_16x16x32_bf16 v[36:39], a[72:75], a[4:7], v[36:39]\n\t"
      "buffer_load_dwordx4 v2, s[40:43], s48 offen nt lds\n\t"
      "v_mfma_f32_16x16x32_bf16 v[36:39], a[80:83], a[8:11], v[36:39]\n\t"
      "v_mfma_f32_16x16x32_bf16 v[36:39], a[88:91], a[12:15], v[36:39]\n\t"
      "s_waitcnt lgkmcnt(1)\n\t"
      "v_mfma_f32_16x16x32_bf16 v[36:39], a[96:99], a[16:19], v[36:39]\n\t"
      "buffer_store_dwordx4 a[52:55], v13, s[44:47], s49 offen offset:1024 sc1\n\t"
      "v_mfma_f32_16x16x32_bf16 v[36:39], a[104:107], a[20:23], v[36:39]\n\t"
      "v_mfma_f32_16x16x32_bf16 v[16:19], v[160:163], a[24:27], v[16:19]\n\t"
      "ds_read_b128 a[0:3], v52 offset:0\n\t"
      "v_mfma_f32_16x16x32_bf16 v[16:19], v[176:179], a[28:31], v[16:19]\n\t"
      "ds_read_b128 a[4:7], v53 offset:0\n\t"
      "v_mfma_f32_16x16x32_bf16 v[16:19], v[192:195], a[32:35], v[16:19]\n\t"
      "ds_read_b128 a[8:11], v54 offset:0\n\t"
      "v_mfma_f32_16x16x32_bf16 v[16:19], v[208:211], a[36:39], v[16:19]\n\t"
      "ds_read_b128 a[12:15], v55 offset:0\n\t"
      "v_mfma_f32_16x16x32_bf16 v[16:19], v[224:227], a[40:43], v[16:19]\n\t"
      "ds_read_b128 a[16:19], v52 offset:256\n\t"
      "v_mfma_f32_16x16x32_bf16 v[16:19], v[240:243], a[44:47], v[16:19]\n\t"
      "ds_read_b128 a[20:23], v53 offset:256\n\t"
      "v_mfma_f32_16x16x32_bf16 v[20:23], v[164:167], a[24:27], v[20:23]\n\t"
      "s_add_u32 s51, s51, 12288\n\t"
      "s_cmp_eq_u32 s51, 160256\n\t"
      "s_cselect_b32 s51, 25088, s51\n\t"
      "v_mfma_f32_16x16x32_bf16 v[20:23], v[180:183], a[28:31], v[20:23]\n\t"
      "s_waitcnt lgkmcnt(6)\n\t"
      "v_mfma_f32_16x16x32_bf16 v[20:23], v[196:199], a[32:35], v[20:23]\n\t"
      "buffer_store_dwordx4 a[56:59], v14, s[44:47], s49 offen offset:2048 sc1\n\t"
      "v_mfma_f32_16x16x32_bf16 v[20:23], v[212:215], a[36:39], v[20:23]\n\t"
      "v_mfma_f32_16x16x32_bf16 v[20:23], v[228:231], a[40:43], v[20:23]\n\t"
      "s_waitcnt lgkmcnt(0)\n\t"
      "v_mfma_f32_16x16x32_bf16 v[20:23], v[244:247], a[44:47], v[20:23]\n\t"
      "v_mfma_f32_16x16x32_bf16 v[24:27], v[168:171], a[24:27], v[24:27]\n\t"
      "v_mfma_f32_16x16x32_bf16 v[24:27], v[184:187], a[28:31], v[24:27]\n\t"
      "v_cvt_pk_bf16_f32 v40, v16, v17\n\t"
      "v_cvt_pk_bf16_f32 v41, v18, v19\n\t"
      "v_mfma_f32_16x16x32_bf16 v[24:27], v[200:203], a[32:35], v[24:27]\n\t"
      "ds_write_b32 v0, v40 offset:0\n\t"
      "v_mfma_f32_16x16x32_bf16 v[24:27], v[216:219], a[36:39], v[24:27]\n\t"
      "ds_write_b32 v0, v41 offset:4\n\t"
      "v_mfma_f32_16x16x32_bf16 v[24:27], v[232:235], a[40:43], v[24:27]\n\t"
      "buffer_load_dwordx4 v3, s[40:43], s48 offen offset:1024 nt lds\n\t"
      "v_mfma_f32_16x16x32_bf16 v[24:27], v[248:251], a[44:47], v[24:27]\n\t"
      "v_cvt_pk_bf16_f32 v42, v20, v21\n\t"
      "v_cvt_pk_bf16_f32 v43, v22, v23\n\t"
      "v_mfma_f32_16x16x32_bf16 v[28:31], v[172:175], a[24:27], v[28:31]\n\t"
      "ds_write_b32 v0, v42 offset:32\n\t"
      "v_mfma_f32_16x16x32_bf16 v[28:31], v[188:191], a[28:31], v[28:31]\n\t"
      "ds_write_b32 v0, v43 offset:36\n\t"
      "v_mfma_f32_16x16x32_bf16 v[28:31], v[204:207], a[32:35], v[28:31]\n\t"
      "s_add_u32 s58, s58, 786432\n\t"
      "s_lshr_b32 s59, s59, 1\n\t"
      "v_mfma_f32_16x16x32_bf16 v[28:31], v[220:223], a[36:39], v[28:31]\n\t"
      "s_bitcmp1_b32 s59, 0\n\t"
      "s_cselect_b32 s49, 24576, 0\n\t"
      "s_add_u32 s49, s49, s58\n\t"
      "v_mfma_f32_16x16x32_bf16 v[28:31], v[236:239], a[40:43], v[28:31]\n\t"
      "v_mfma_f32_16x16x32_bf16 v[28:31], v[252:255], a[44:47], v[28:31]\n\t"
      "v_cvt_pk_bf16_f32 v44, v24, v25\n\t"
      "v_cvt_pk_bf16_f32 v45, v26, v27\n\t"
      "v_mfma_f32_16x16x32_bf16 v[32:35], a[108:111], a[24:27], v[32:35]\n\t"
      "ds_write_b32 v0, v44 offset:64\n\t"
      "v_mfma_f32_16x16x32_bf16 v[32:35], a[116:119], a[28:31], v[32:35]\n\t"
      "ds_write_b32 v0, v45 offset:68\n\t"
      "v_mfma_f32_16x16x32_bf16 v[32:35], a[124:127], a[32:35], v[32:35]\n\t"
      "v_mfma_f32_16x16x32_bf16 v[32:35], a[132:135], a[36:39], v[32:35]\n\t"
      "buffer_load_dwordx4 v4, s[40:43], s48 offen offset:2048 nt lds\n\t"
      "s_add_u32 s50, s50, 12288\n\t"
      "v_mfma_f32_16x16x32_bf16 v[32:35], a[140:143], a[40:43], v[32:35]\n\t"
      "s_cmp_eq_u32 s50, 160256\n\t"
      "s_cselect_b32 s50, 25088, s50\n\t"
      "v_mfma_f32_16x16x32_bf16 v[32:35], a[148:151], a[44:47], v[32:35]\n\t"
      "v_cvt_pk_bf16_f32 v46, v28, v29\n\t"
      "v_cvt_pk_bf16_f32 v47, v30, v31\n\t"
      "v_mfma_f32_16x16x32_bf16 v[36:39], a[112:115], a[24:27], v[36:39]\n\t"
      "ds_write_b32 v0, v46 offset:96\n\t"
      "v_mfma_f32_16x16x32_bf16 v[36:39], a[120:123], a[28:31], v[36:39]\n\t"
      "ds_write_b32 v0, v47 offset:100\n\t"
      "v_mfma_f32_16x16x32_bf16 v[36:39], a[128:131], a[32:35], v[36:39]\n\t"
      "v_mfma_f32_16x16x32_bf16 v[36:39], a[136:139], a[36:39], v[36:39]\n\t"
      "v_mfma_f32_16x16x32_bf16 v[36:39], a[144:147], a[40:43], v[36:39]\n\t"
      "v_cvt_pk_bf16_f32 v48, v32, v33\n\t"
      "v_cvt_pk_bf16_f32 v49, v34, v35\n\t"
      "v_mfma_f32_16x16x32_bf16 v[36:39], a[152:155], a[44:47], v[36:39]\n\t"
      ".Lmid1_%=:\n\t"
      "v_mfma_f32_16x16x32_bf16 v[16:19], v[64:67], a[0:3], 0\n\t"
      "ds_write_b32 v0, v48 offset:128\n\t"
      "v_mfma_f32_16x16x32_bf16 v[16:19], v[80:83], a[4:7], v[16:19]\n\t"
      "ds_write_b32 v0, v49 offset:132\n\t"
      "v_mfma_f32_16x16x32_bf16 v[16:19], v[96:99], a[8:11], v[16:19]\n\t"
      "ds_read_b128 a[24:27], v54 offset:256\n\t"
      "v_mfma_f32_16x16x32_bf16 v[16:19], v[112:115], a[12:15], v[16:19]\n\t"
      "ds_read_b128 a[28:31], v55 offset:256\n\t"
      "v_mfma_f32_16x16x32_bf16 v[16:19], v[128:131], a[16:19], v[16:19]\n\t"
      "ds_read_b128 a[32:35], v52 offset:512\n\t"
      "v_mfma_f32_16x16x32_bf16 v[16:19], v[144:147], a[20:23], v[16:19]\n\t"
      "ds_read_b128 a[36:39], v53 offset:512\n\t"
      "v_mfma_f32_16x16x32_bf16 v[20:23], v[68:71], a[0:3], 0\n\t"
      "v_cvt_pk_bf16_f32 v50, v36, v37\n\t"
      "v_cvt_pk_bf16_f32 v51, v38, v39\n\t"
      "v_mfma_f32_16x16x32_bf16 v[20:23], v[84:87], a[4:7], v[20:23]\n\t"
      "ds_write_b32 v0, v50 offset:160\n\t"
      "v_mfma_f32_16x16x32_bf16 v[20:23], v[100:103], a[8:11], v[20:23]\n\t"
      "ds_write_b32 v0, v51 offset:164\n\t"
      "v_mfma_f32_16x16x32_bf16 v[20:23], v[116:119], a[12:15], v[20:23]\n\t"
      "ds_read_b128 a[40:43], v54 offset:512\n\t"
      "v_mfma_f32_16x16x32_bf16 v[20:23], v[132:135], a[16:19], v[20:23]\n\t"
      "ds_read_b128 a[44:47], v55 offset:512\n\t"
      "v_mfma_f32_16x16x32_bf16 v[20:23], v[148:151], a[20:23], v[20:23]\n\t"
      "v_mfma_f32_16x16x32_bf16 v[24:27], v[72:75], a[0:3], 0\n\t"
      "v_mfma_f32_16x16x32_bf16 v[24:27], v[88:91], a[4:7], v[24:27]\n\t"
      "v_mfma_f32_16x16x32_bf16 v[24:27], v[104:107], a[8:11], v[24:27]\n\t"
      "v_mfma_f32_16x16x32_bf16 v[24:27], v[120:123], a[12:15], v[24:27]\n\t"
      "v_mfma_f32_16x16x32_bf16 v[24:27], v[136:139], a[16:19], v[24:27]\n\t"
      "s_waitcnt vmcnt(12)\n\t"
      "s_waitcnt lgkmcnt(0)\n\t"
      "s_barrier\n\t"
      "v_mfma_f32_16x16x32_bf16 v[24:27], v[152:155], a[20:23], v[24:27]\n\t"
      "v_mfma_f32_16x16x32_bf16 v[28:31], v[76:79], a[0:3], 0\n\t"
      "v_mfma_f32_16x16x32_bf16 v[28:31], v[92:95], a[4:7], v[28:31]\n\t"
      "ds_read_b128 a[48:51], v8 offset:0\n\t"
      "v_mfma_f32_16x16x32_bf16 v[28:31], v[108:111], a[8:11], v[28:31]\n\t"
      "ds_read_b128 a[52:55], v60 offset:1024\n\t"
      "v_mfma_f32_16x16x32_bf16 v[28:31], v[124:127], a[12:15], v[28:31]\n\t"
      "ds_read_b128 a[56:59], v61 offset:2048\n\t"
      "v_mfma_f32_16x16x32_bf16 v[28:31], v[140:143], a[16:19], v[28:31]\n\t"
      "v_mfma_f32_16x16x32_bf16 v[28:31], v[156:159], a[20:23], v[28:31]\n\t"
      "v_mfma_f32_16x16x32_bf16 v[32:35], a[60:63], a[0:3], 0\n\t"
      "v_mfma_f32_16x16x32_bf16 v[32:35], a[68:71], a[4:7], v[32:35]\n\t"
      "v_mfma_f32_16x16x32_bf16 v[32:35], a[76:79], a[8:11], v[32:35]\n\t"
      "s_waitcnt lgkmcnt(2)\n\t"
      "v_mfma_f32_16x16x32_bf16 v[32:35], a[84:87], a[12:15], v[32:35]\n\t"
      "buffer_store_dwordx4 a[48:51], v9, s[44:47], s49 offen sc1\n\t"
      "v_mfma_f32_16x16x32_bf16 v[32:35], a[92:95], a[16:19], v[32:35]\n\t"
      "v_add_u32 v56, s51, v15\n\t"
      "v_xor_b32 v57, 64, v56\n\t"
      "v_mfma_f32_16x16x32_bf16 v[32:35], a[100:103], a[20:23], v[32:35]\n\t"
      "v_xor_b32 v58, 128, v56\n\t"
      "v_xor_b32 v59, 192, v56\n\t"
      "v_mfma_f32_16x16x32_bf16 v[36:39], a[64:67], a[0:3], 0\n\t"
      "s_add_u32 m0, s18, s50\n\t"
      "v_mfma_f32_16x16x32_bf16 v[36:39], a[72:75], a[4:7], v[36:39]\n\t"
      "buffer_load_dwordx4 v5, s[40:43], s48 offen nt lds\n\t"
      "v_mfma_f32_16x16x32_bf16 v[36:39], a[80:83], a[8:11], v[36:39]\n\t"
      "v_mfma_f32_16x16x32_bf16 v[36:39], a[88:91], a[12:15], v[36:39]\n\t"
      "s_waitcnt lgkmcnt(1)\n\t"
      "v_mfma_f32_16x16x32_bf16 v[36:39], a[96:99], a[16:19], v[36:39]\n\t"
      "buffer_store_dwordx4 a[52:55], v10, s[44:47], s49 offen offset:1024 sc1\n\t"
      "v_mfma_f32_16x16x32_bf16 v[36:39], a[104:107], a[20:23], v[36:39]\n\t"
      "v_mfma_f32_16x16x32_bf16 v[16:19], v[160:163], a[24:27], v[16:19]\n\t"
      "ds_read_b128 a[0:3], v56 offset:0\n\t"
      "v_mfma_f32_16x16x32_bf16 v[16:19], v[176:179], a[28:31], v[16:19]\n\t"
      "ds_read_b128 a[4:7], v57 offset:0\n\t"
      "v_mfma_f32_16x16x32_bf16 v[16:19], v[192:195], a[32:35], v[16:19]\n\t"
      "ds_read_b128 a[8:11], v58 offset:0\n\t"
      "v_mfma_f32_16x16x32_bf16 v[16:19], v[208:211], a[36:39], v[16:19]\n\t"
      "ds_read_b128 a[12:15], v59 offset:0\n\t"
      "v_mfma_f32_16x16x32_bf16 v[16:19], v[224:227], a[40:43], v[16:19]\n\t"
      "ds_read_b128 a[16:19], v56 offset:256\n\t"
      "v_mfma_f32_16x16x32_bf16 v[16:19], v[240:243], a[44:47], v[16:19]\n\t"
      "ds_read_b128 a[20:23], v57 offset:256\n\t"
      "v_mfma_f32_16x16x32_bf16 v[20:23], v[164:167], a[24:27], v[20:23]\n\t"
      "s_add_u32 s51, s51, 12288\n\t"
      "s_cmp_eq_u32 s51, 160256\n\t"
      "s_cselect_b32 s51, 25088, s51\n\t"
      "v_mfma_f32_16x16x32_bf16 v[20:23], v[180:183], a[28:31], v[20:23]\n\t"
      "s_waitcnt lgkmcnt(6)\n\t"
      "v_mfma_f32_16x16x32_bf16 v[20:23], v[196:199], a[32:35], v[20:23]\n\t"
      "buffer_store_dwordx4 a[56:59], v11, s[44:47], s49 offen offset:2048 sc1\n\t"
      "v_mfma_f32_16x16x32_bf16 v[20:23], v[212:215], a[36:39], v[20:23]\n\t"
      "v_mfma_f32_16x16x32_bf16 v[20:23], v[228:231], a[40:43], v[20:23]\n\t"
      "s_waitcnt lgkmcnt(0)\n\t"
      "v_mfma_f32_16x16x32_bf16 v[20:23], v[244:247], a[44:47], v[20:23]\n\t"
      "v_mfma_f32_16x16x32_bf16 v[24:27], v[168:171], a[24:27], v[24:27]\n\t"
      "v_mfma_f32_16x16x32_bf16 v[24:27], v[184:187], a[28:31], v[24:27]\n\t"
      "v_cvt_pk_bf16_f32 v40, v16, v17\n\t"
      "v_cvt_pk_bf16_f32 v41, v18, v19\n\t"
      "v_mfma_f32_16x16x32_bf16 v[24:27], v[200:203], a[32:35], v[24:27]\n\t"
      "ds_write_b32 v0, v40 offset:12544\n\t"
      "v_mfma_f32_16x16x32_bf16 v[24:27], v[216:219], a[36:39], v[24:27]\n\t"
      "ds_write_b32 v0, v41 offset:12548\n\t"
      "v_mfma_f32_16x16x32_bf16 v[24:27], v[232:235], a[40:43], v[24:27]\n\t"
      "buffer_load_dwordx4 v6, s[40:43], s48 offen offset:1024 nt lds\n\t"
      "v_mfma_f32_16x16x32_bf16 v[24:27], v[248:251], a[44:47], v[24:27]\n\t"
      "v_cvt_pk_bf16_f32 v42, v20, v21\n\t"
      "v_cvt_pk_bf16_f32 v43, v22, v23\n\t"
      "v_mfma_f32_16x16x32_bf16 v[28:31], v[172:175], a[24:27], v[28:31]\n\t"
      "ds_write_b32 v0, v42 offset:12576\n\t"
      "v_mfma_f32_16x16x32_bf16 v[28:31], v[188:191], a[28:31], v[28:31]\n\t"
      "ds_write_b32 v0, v43 offset:12580\n\t"
      "v_mfma_f32_16x16x32_bf16 v[28:31], v[204:207], a[32:35], v[28:31]\n\t"
      "v_mfma_f32_16x16x32_bf16 v[28:31], v[220:223], a[36:39], v[28:31]\n\t"
      "v_mfma_f32_16x16x32_bf16 v[28:31], v[236:239], a[40:43], v[28:31]\n\t"
      "v_mfma_f32_16x16x32_bf16 v[28:31], v[252:255], a[44:47], v[28:31]\n\t"
      "v_cvt_pk_bf16_f32 v44, v24, v25\n\t"
      "v_cvt_pk_bf16_f32 v45, v26, v27\n\t"
      "v_mfma_f32_16x16x32_bf16 v[32:35], a[108:111], a[24:27], v[32:35]\n\t"
      "ds_write_b32 v0, v44 offset:12608\n\t"
      "v_mfma_f32_16x16x32_bf16 v[32:35], a[116:119], a[28:31], v[32:35]\n\t"
      "ds_write_b32 v0, v45 offset:12612\n\t"
      "v_mfma_f32_16x16x32_bf16 v[32:35], a[124:127], a[32:35], v[32:35]\n\t"
      "v_mfma_f32_16x16x32_bf16 v[32:35], a[132:135], a[36:39], v[32:35]\n\t"
      "buffer_load_dwordx4 v7, s[40:43], s48 offen offset:2048 nt lds\n\t"
      "s_add_u32 s50, s50, 12288\n\t"
      "v_mfma_f32_16x16x32_bf16 v[32:35], a[140:143], a[40:43], v[32:35]\n\t"
      "s_cmp_eq_u32 s50, 160256\n\t"
      "s_cselect_b32 s50, 25088, s50\n\t"
      "v_mfma_f32_16x16x32_bf16 v[32:35], a[148:151], a[44:47], v[32:35]\n\t"
      "v_cvt_pk_bf16_f32 v46, v28, v29\n\t"
      "v_cvt_pk_bf16_f32 v47, v30, v31\n\t"
      "v_mfma_f32_16x16x32_bf16 v[36:39], a[112:115], a[24:27], v[36:39]\n\t"
      "ds_write_b32 v0, v46 offset:12640\n\t"
      "v_mfma_f32_16x16x32_bf16 v[36:39], a[120:123], a[28:31], v[36:39]\n\t"
      "ds_write_b32 v0, v47 offset:12644\n\t"
      "v_mfma_f32_16x16x32_bf16 v[36:39], a[128:131], a[32:35], v[36:39]\n\t"
      "v_mfma_f32_16x16x32_bf16 v[36:39], a[136:139], a[36:39], v[36:39]\n\t"
      "s_sub_u32 s52, s52, 1\n\t"
      "s_cmp_lg_u32 s52, 0\n\t"
      "v_mfma_f32_16x16x32_bf16 v[36:39], a[144:147], a[40:43], v[36:39]\n\t"
      "v_cvt_pk_bf16_f32 v48, v32, v33\n\t"
      "v_cvt_pk_bf16_f32 v49, v34, v35\n\t"
      "v_mfma_f32_16x16x32_bf16 v[36:39], a[152:155], a[44:47], v[36:39]\n\t"
      "s_cbranch_scc1 .Lloop1_%=\n\t"
      "s_branch .Lendc_%=\n\t"
      ".Lloop2_%=:\n\t"
      "v_mfma_f32_16x16x32_bf16 v[16:19], v[64:67], a[0:3], 0\n\t"
      "ds_write_b32 v0, v48 offset:12672\n\t"
      "v_mfma_f32_16x16x32_bf16 v[16:19], v[80:83], a[4:7], v[16:19]\n\t"
      "ds_write_b32 v0, v49 offset:12676\n\t"
      "v_mfma_f32_16x16x32_bf16 v[16:19], v[96:99], a[8:11], v[16:19]\n\t"
      "ds_read_b128 a[24:27], v58 offset:256\n\t"
      "v_mfma_f32_16x16x32_bf16 v[16:19], v[112:115], a[12:15], v[16:19]\n\t"
      "ds_read_b128 a[28:31], v59 offset:256\n\t"
      "v_mfma_f32_16x16x32_bf16 v[16:19], v[128:131], a[16:19], v[16:19]\n\t"
      "ds_read_b128 a[32:35], v56 offset:512\n\t"
      "v_mfma_f32_16x16x32_bf16 v[16:19], v[144:147], a[20:23], v[16:19]\n\t"
      "ds_read_b128 a[36:39], v57 offset:512\n\t"
      "v_mfma_f32_16x16x32_bf16 v[20:23], v[68:71], a[0:3], 0\n\t"
      "v_cvt_pk_bf16_f32 v50, v36, v37\n\t"
      "v_cvt_pk_bf16_f32 v51, v38, v39\n\t"
      "v_mfma_f32_16x16x32_bf16 v[20:23], v[84:87], a[4:7], v[20:23]\n\t"
      "ds_write_b32 v0, v50 offset:12704\n\t"
      "v_mfma_f32_16x16x32_bf16 v[20:23], v[100:103], a[8:11], v[20:23]\n\t"
      "ds_write_b32 v0, v51 offset:12708\n\t"
      "v_mfma_f32_16x16x32_bf16 v[20:23], v[116:119], a[12:15], v[20:23]\n\t"
      "ds_read_b128 a[40:43], v58 offset:512\n\t"
      "v_mfma_f32_16x16x32_bf16 v[20:23], v[132:135], a[16:19], v[20:23]\n\t"
      "ds_read_b128 a[44:47], v59 offset:512\n\t"
      "v_mfma_f32_16x16x32_bf16 v[20:23], v[148:151], a[20:23], v[20:23]\n\t"
      "s_add_u32 s56, s56, 786432\n\t"
      "s_lshr_b32 s57, s57, 1\n\t"
      "v_mfma_f32_16x16x32_bf16 v[24:27], v[72:75], a[0:3], 0\n\t"
      "s_bitcmp1_b32 s57, 0\n\t"
      "s_cselect_b32 s48, 24576, 0\n\t"
      "s_add_u32 s48, s48, s56\n\t"
      "v_mfma_f32_16x16x32_bf16 v[24:27], v[88:91], a[4:7], v[24:27]\n\t"
      "v_mfma_f32_16x16x32_bf16 v[24:27], v[104:107], a[8:11], v[24:27]\n\t"
      "v_mfma_f32_16x16x32_bf16 v[24:27], v[120:123], a[12:15], v[24:27]\n\t"
      "v_mfma_f32_16x16x32_bf16 v[24:27], v[136:139], a[16:19], v[24:27]\n\t"
      "v_mfma_f32_16x16x32_bf16 v[24:27], v[152:155], a[20:23], v[24:27]\n\t"
      "s_waitcnt vmcnt(12)\n\t"
      "s_waitcnt lgkmcnt(0)\n\t"
      "s_barrier\n\t"
      "v_mfma_f32_16x16x32_bf16 v[28:31], v[76:79], a[0:3], 0\n\t"
      "v_mfma_f32_16x16x32_bf16 v[28:31], v[92:95], a[4:7], v[28:31]\n\t"
      "ds_read_b128 a[48:51], v8 offset:12544\n\t"
      "v_mfma_f32_16x16x32_bf16 v[28:31], v[108:111], a[8:11], v[28:31]\n\t"
      "ds_read_b128 a[52:55], v60 offset:13568\n\t"
      "v_mfma_f32_16x16x32_bf16 v[28:31], v[124:127], a[12:15], v[28:31]\n\t"
      "ds_read_b128 a[56:59], v61 offset:14592\n\t"
      "v_mfma_f32_16x16x32_bf16 v[28:31], v[140:143], a[16:19], v[28:31]\n\t"
      "v_mfma_f32_16x16x32_bf16 v[28:31], v[156:159], a[20:23], v[28:31]\n\t"
      "v_mfma_f32_16x16x32_bf16 v[32:35], a[60:63], a[0:3], 0\n\t"
      "v_mfma_f32_16x16x32_bf16 v[32:35], a[68:71], a[4:7], v[32:35]\n\t"
      "v_mfma_f32_16x16x32_bf16 v[32:35], a[76:79], a[8:11], v[32:35]\n\t"
      "s_waitcnt lgkmcnt(2)\n\t"
      "v_mfma_f32_16x16x32_bf16 v[32:35], a[84:87], a[12:15], v[32:35]\n\t"
      "buffer_store_dwordx4 a[48:51], v12, s[44:47], s49 offen sc1\n\t"
      "v_mfma_f32_16x16x32_bf16 v[32:35], a[92:95], a[16:19], v[32:35]\n\t"
      "v_add_u32 v52, s51, v15\n\t"
      "v_xor_b32 v53, 64, v52\n\t"
      "v_mfma_f32_16x16x32_bf16 v[32:35], a[100:103], a[20:23], v[32:35]\n\t"
      "v_xor_b32 v54, 128, v52\n\t"
      "v_xor_b32 v55, 192, v52\n\t"
      "v_mfma_f32_16x16x32_bf16 v[36:39], a[64:67], a[0:3], 0\n\t"
      "s_add_u32 m0, s18, s50\n\t"
      "v_mfma_f32_16x16x32_bf16 v[36:39], a[72:75], a[4:7], v[36:39]\n\t"
      "buffer_load_dwordx4 v2, s[40:43], s48 offen nt lds\n\t"
      "v_mfma_f32_16x16x32_bf16 v[36:39], a[80:83], a[8:11], v[36:39]\n\t"
      "v_mfma_f32_16x16x32_bf16 v[36:39], a[88:91], a[12:15], v[36:39]\n\t"
      "s_waitcnt lgkmcnt(1)\n\t"
      "v_mfma_f32_16x16x32_bf16 v[36:39], a[96:99], a[16:19], v[36:39]\n\t"
      "buffer_store_dwordx4 a[52:55], v13, s[44:47], s49 offen offset:1024 sc1\n\t"
      "v_mfma_f32_16x16x32_bf16 v[36:39], a[104:107], a[20:23], v[36:39]\n\t"
      "v_mfma_f32_16x16x32_bf16 v[16:19], v[160:163], a[24:27], v[16:19]\n\t"
      "ds_read_b128 a[0:3], v52 offset:0\n\t"
      "v_mfma_f32_16x16x32_bf16 v[16:19], v[176:179], a[28:31], v[16:19]\n\t"
      "ds_read_b128 a[4:7], v53 offset:0\n\t"
      "v_mfma_f32_16x16x32_bf16 v[16:19], v[192:195], a[32:35], v[16:19]\n\t"
      "ds_read_b128 a[8:11], v54 offset:0\n\t"
      "v_mfma_f32_16x16x32_bf16 v[16:19], v[208:211], a[36:39], v[16:19]\n\t"
      "ds_read_b128 a[12:15], v55 offset:0\n\t"
      "v_mfma_f32_16x16x32_bf16 v[16:19], v[224:227], a[40:43], v[16:19]\n\t"
      "ds_read_b128 a[16:19], v52 offset:256\n\t"
      "v_mfma_f32_16x16x32_bf16 v[16:19], v[240:243], a[44:47], v[16:19]\n\t"
      "ds_read_b128 a[20:23], v53 offset:256\n\t"
      "v_mfma_f32_16x16x32_bf16 v[20:23], v[164:167], a[24:27], v[20:23]\n\t"
      "s_add_u32 s51, s51, 12288\n\t"
      "s_cmp_eq_u32 s51, 160256\n\t"
      "s_cselect_b32 s51, 25088, s51\n\t"
      "v_mfma_f32_16x16x32_bf16 v[20:23], v[180:183], a[28:31], v[20:23]\n\t"
      "s_waitcnt lgkmcnt(6)\n\t"
      "v_mfma_f32_16x16x32_bf16 v[20:23], v[196:199], a[32:35], v[20:23]\n\t"
      "buffer_store_dwordx4 a[56:59], v14, s[44:47], s49 offen offset:2048 sc1\n\t"
      "v_mfma_f32_16x16x32_bf16 v[20:23], v[212:215], a[36:39], v[20:23]\n\t"
      "v_mfma_f32_16x16x32_bf16 v[20:23], v[228:231], a[40:43], v[20:23]\n\t"
      "s_waitcnt lgkmcnt(0)\n\t"
      "v_mfma_f32_16x16x32_bf16 v[20:23], v[244:247], a[44:47], v[20:23]\n\t"
      "v_mfma_f32_16x16x32_bf16 v[24:27], v[168:171], a[24:27], v[24:27]\n\t"
      "v_mfma_f32_16x16x32_bf16 v[24:27], v[184:187], a[28:31], v[24:27]\n\t"
      "v_cvt_pk_bf16_f32 v40, v16, v17\n\t"
      "v_cvt_pk_bf16_f32 v41, v18, v19\n\t"
      "v_mfma_f32_16x16x32_bf16 v[24:27], v[200:203], a[32:35], v[24:27]\n\t"
      "ds_write_b32 v0, v40 offset:0\n\t"
      "v_mfma_f32_16x16x32_bf16 v[24:27], v[216:219], a[36:39], v[24:27]\n\t"
      "ds_write_b32 v0, v41 offset:4\n\t"
      "v_mfma_f32_16x16x32_bf16 v[24:27], v[232:235], a[40:43], v[24:27]\n\t"
      "buffer_load_dwordx4 v3, s[40:43], s48 offen offset:1024 nt lds\n\t"
      "v_mfma_f32_16x16x32_bf16 v[24:27], v[248:251], a[44:47], v[24:27]\n\t"
      "v_cvt_pk_bf16_f32 v42, v20, v21\n\t"
      "v_cvt_pk_bf16_f32 v43, v22, v23\n\t"
      "v_mfma_f32_16x16x32_bf16 v[28:31], v[172:175], a[24:27], v[28:31]\n\t"
      "ds_write_b32 v0, v42 offset:32\n\t"
      "v_mfma_f32_16x16x32_bf16 v[28:31], v[188:191], a[28:31], v[28:31]\n\t"
      "ds_write_b32 v0, v43 offset:36\n\t"
      "v_mfma_f32_16x16x32_bf16 v[28:31], v[204:207], a[32:35], v[28:31]\n\t"
      "s_add_u32 s58, s58, 786432\n\t"
      "s_lshr_b32 s59, s59, 1\n\t"
      "v_mfma_f32_16x16x32_bf16 v[28:31], v[220:223], a[36:39], v[28:31]\n\t"
      "s_bitcmp1_b32 s59, 0\n\t"
      "s_cselect_b32 s49, 24576, 0\n\t"
      "s_add_u32 s49, s49, s58\n\t"
      "v_mfma_f32_16x16x32_bf16 v[28:31], v[236:239], a[40:43], v[28:31]\n\t"
      "v_mfma_f32_16x16x32_bf16 v[28:31], v[252:255], a[44:47], v[28:31]\n\t"
      "v_cvt_pk_bf16_f32 v44, v24, v25\n\t"
      "v_cvt_pk_bf16_f32 v45, v26, v27\n\t"
      "v_mfma_f32_16x16x32_bf16 v[32:35], a[108:111], a[24:27], v[32:35]\n\t"
      "ds_write_b32 v0, v44 offset:64\n\t"
      "v_mfma_f32_16x16x32_bf16 v[32:35], a[116:119], a[28:31], v[32:35]\n\t"
      "ds_write_b32 v0, v45 offset:68\n\t"
      "v_mfma_f32_16x16x32_bf16 v[32:35], a[124:127], a[32:35], v[32:35]\n\t"
      "v_mfma_f32_16x16x32_bf16 v[32:35], a[132:135], a[36:39], v[32:35]\n\t"
      "buffer_load_dwordx4 v4, s[40:43], s48 offen offset:2048 nt lds\n\t"
      "s_add_u32 s50, s50, 12288\n\t"
      "v_mfma_f32_16x16x32_bf16 v[32:35], a[140:143], a[40:43], v[32:35]\n\t"
      "s_cmp_eq_u32 s50, 160256\n\t"
      "s_cselect_b32 s50, 25088, s50\n\t"
      "v_mfma_f32_16x16x32_bf16 v[32:35], a[148:151], a[44:47], v[32:35]\n\t"
      "v_cvt_pk_bf16_f32 v46, v28, v29\n\t"
      "v_cvt_pk_bf16_f32 v47, v30, v31\n\t"
      "v_mfma_f32_16x16x32_bf16 v[36:39], a[112:115], a[24:27], v[36:39]\n\t"
      "ds_write_b32 v0, v46 offset:96\n\t"
      "v_mfma_f32_16x16x32_bf16 v[36:39], a[120:123], a[28:31], v[36:39]\n\t"
      "ds_write_b32 v0, v47 offset:100\n\t"
      "v_mfma_f32_16x16x32_bf16 v[36:39], a[128:131], a[32:35], v[36:39]\n\t"
      "v_mfma_f32_16x16x32_bf16 v[36:39], a[136:139], a[36:39], v[36:39]\n\t"
      "v_mfma_f32_16x16x32_bf16 v[36:39], a[144:147], a[40:43], v[36:39]\n\t"
      "v_cvt_pk_bf16_f32 v48, v32, v33\n\t"
      "v_cvt_pk_bf16_f32 v49, v34, v35\n\t"
      "v_mfma_f32_16x16x32_bf16 v[36:39], a[152:155], a[44:47], v[36:39]\n\t"
      ".Lmid2_%=:\n\t"
      "v_mfma_f32_16x16x32_bf16 v[16:19], v[64:67], a[0:3], 0\n\t"
      "ds_write_b32 v0, v48 offset:128\n\t"
      "v_mfma_f32_16x16x32_bf16 v[16:19], v[80:83], a[4:7], v[16:19]\n\t"
      "ds_write_b32 v0, v49 offset:132\n\t"
      "v_mfma_f32_16x16x32_bf16 v[16:19], v[96:99], a[8:11], v[16:19]\n\t"
      "ds_read_b128 a[24:27], v54 offset:256\n\t"
      "v_mfma_f32_16x16x32_bf16 v[16:19], v[112:115], a[12:15], v[16:19]\n\t"
      "ds_read_b128 a[28:31], v55 offset:256\n\t"
      "v_mfma_f32_16x16x32_bf16 v[16:19], v[128:131], a[16:19], v[16:19]\n\t"
      "ds_read_b128 a[32:35], v52 offset:512\n\t"
      "v_mfma_f32_16x16x32_bf16 v[16:19], v[144:147], a[20:23], v[16:19]\n\t"
      "ds_read_b128 a[36:39], v53 offset:512\n\t"
      "v_mfma_f32_16x16x32_bf16 v[20:23], v[68:71], a[0:3], 0\n\t"
      "v_cvt_pk_bf16_f32 v50, v36, v37\n\t"
      "v_cvt_pk_bf16_f32 v51, v38, v39\n\t"
      "v_mfma_f32_16x16x32_bf16 v[20:23], v[84:87], a[4:7], v[20:23]\n\t"
      "ds_write_b32 v0, v50 offset:160\n\t"
      "v_mfma_f32_16x16x32_bf16 v[20:23], v[100:103], a[8:11], v[20:23]\n\t"
      "ds_write_b32 v0, v51 offset:164\n\t"
      "v_mfma_f32_16x16x32_bf16 v[20:23], v[116:119], a[12:15], v[20:23]\n\t"
      "ds_read_b128 a[40:43], v54 offset:512\n\t"
      "v_mfma_f32_16x16x32_bf16 v[20:23], v[132:135], a[16:19], v[20:23]\n\t"
      "ds_read_b128 a[44:47], v55 offset:512\n\t"
      "v_mfma_f32_16x16x32_bf16 v[20:23], v[148:151], a[20:23], v[20:23]\n\t"
      "v_mfma_f32_16x16x32_bf16 v[24:27], v[72:75], a[0:3], 0\n\t"
      "v_mfma_f32_16x16x32_bf16 v[24:27], v[88:91], a[4:7], v[24:27]\n\t"
      "v_mfma_f32_16x16x32_bf16 v[24:27], v[104:107], a[8:11], v[24:27]\n\t"
      "v_mfma_f32_16x16x32_bf16 v[24:27], v[120:123], a[12:15], v[24:27]\n\t"
      "v_mfma_f32_16x16x32_bf16 v[24:27], v[136:139], a[16:19], v[24:27]\n\t"
      "v_mfma_f32_16x16x32_bf16 v[24:27], v[152:155], a[20:23], v[24:27]\n\t"
      "s_waitcnt vmcnt(12)\n\t"
      "s_waitcnt lgkmcnt(0)\n\t"
      "s_barrier\n\t"
      "v_mfma_f32_16x16x32_bf16 v[28:31], v[76:79], a[0:3], 0\n\t"
      "v_mfma_f32_16x16x32_bf16 v[28:31], v[92:95], a[4:7], v[28:31]\n\t"
      "ds_read_b128 a[48:51], v8 offset:0\n\t"
      "v_mfma_f32_16x16x32_bf16 v[28:31], v[108:111], a[8:11], v[28:31]\n\t"
      "ds_read_b128 a[52:55], v60 offset:1024\n\t"
      "v_mfma_f32_16x16x32_bf16 v[28:31], v[124:127], a[12:15], v[28:31]\n\t"
      "ds_read_b128 a[56:59], v61 offset:2048\n\t"
      "v_mfma_f32_16x16x32_bf16 v[28:31], v[140:143], a[16:19], v[28:31]\n\t"
      "v_mfma_f32_16x16x32_bf16 v[28:31], v[156:159], a[20:23], v[28:31]\n\t"
      "v_mfma_f32_16x16x32_bf16 v[32:35], a[60:63], a[0:3], 0\n\t"
      "v_mfma_f32_16x16x32_bf16 v[32:35], a[68:71], a[4:7], v[32:35]\n\t"
      "v_mfma_f32_16x16x32_bf16 v[32:35], a[76:79], a[8:11], v[32:35]\n\t"
      "s_waitcnt lgkmcnt(2)\n\t"
      "v_mfma_f32_16x16x32_bf16 v[32:35], a[84:87], a[12:15], v[32:35]\n\t"
      "buffer_store_dwordx4 a[48:51], v9, s[44:47], s49 offen sc1\n\t"
      "v_mfma_f32_16x16x32_bf16 v[32:35], a[92:95], a[16:19], v[32:35]\n\t"
      "v_add_u32 v56, s51, v15\n\t"
      "v_xor_b32 v57, 64, v56\n\t"
      "v_mfma_f32_16x16x32_bf16 v[32:35], a[100:103], a[20:23], v[32:35]\n\t"
      "v_xor_b32 v58, 128, v56\n\t"
      "v_xor_b32 v59, 192, v56\n\t"
      "v_mfma_f32_16x16x32_bf16 v[36:39], a[64:67], a[0:3], 0\n\t"
      "s_add_u32 m0, s18, s50\n\t"
      "v_mfma_f32_16x16x32_bf16 v[36:39], a[72:75], a[4:7], v[36:39]\n\t"
      "buffer_load_dwordx4 v5, s[40:43], s48 offen nt lds\n\t"
      "v_mfma_f32_16x16x32_bf16 v[36:39], a[80:83], a[8:11], v[36:39]\n\t"
      "v_mfma_f32_16x16x32_bf16 v[36:39], a[88:91], a[12:15], v[36:39]\n\t"
      "s_waitcnt lgkmcnt(1)\n\t"
      "v_mfma_f32_16x16x32_bf16 v[36:39], a[96:99], a[16:19], v[36:39]\n\t"
      "buffer_store_dwordx4 a[52:55], v10, s[44:47], s49 offen offset:1024 sc1\n\t"
      "v_mfma_f32_16x16x32_bf16 v[36:39], a[104:107], a[20:23], v[36:39]\n\t"
      "v_mfma_f32_16x16x32_bf16 v[16:19], v[160:163], a[24:27], v[16:19]\n\t"
      "ds_read_b128 a[0:3], v56 offset:0\n\t"
      "v_mfma_f32_16x16x32_bf16 v[16:19], v[176:179], a[28:31], v[16:19]\n\t"
      "ds_read_b128 a[4:7], v57 offset:0\n\t"
      "v_mfma_f32_16x16x32_bf16 v[16:19], v[192:195], a[32:35], v[16:19]\n\t"
      "ds_read_b128 a[8:11], v58 offset:0\n\t"
      "v_mfma_f32_16x16x32_bf16 v[16:19], v[208:211], a[36:39], v[16:19]\n\t"
      "ds_read_b128 a[12:15], v59 offset:0\n\t"
      "v_mfma_f32_16x16x32_bf16 v[16:19], v[224:227], a[40:43], v[16:19]\n\t"
      "ds_read_b128 a[16:19], v56 offset:256\n\t"
      "v_mfma_f32_16x16x32_bf16 v[16:19], v[240:243], a[44:47], v[16:19]\n\t"
      "ds_read_b128 a[20:23], v57 offset:256\n\t"
      "v_mfma_f32_16x16x32_bf16 v[20:23], v[164:167], a[24:27], v[20:23]\n\t"
      "s_add_u32 s51, s51, 12288\n\t"
      "s_cmp_eq_u32 s51, 160256\n\t"
      "s_cselect_b32 s51, 25088, s51\n\t"
      "v_mfma_f32_16x16x32_bf16 v[20:23], v[180:183], a[28:31], v[20:23]\n\t"
      "s_waitcnt lgkmcnt(6)\n\t"
      "v_mfma_f32_16x16x32_bf16 v[20:23], v[196:199], a[32:35], v[20:23]\n\t"
      "buffer_store_dwordx4 a[56:59], v11, s[44:47], s49 offen offset:2048 sc1\n\t"
      "v_mfma_f32_16x16x32_bf16 v[20:23], v[212:215], a[36:39], v[20:23]\n\t"
      "v_mfma_f32_16x16x32_bf16 v[20:23], v[228:231], a[40:43], v[20:23]\n\t"
      "s_waitcnt lgkmcnt(0)\n\t"
      "v_mfma_f32_16x16x32_bf16 v[20:23], v[244:247], a[44:47], v[20:23]\n\t"
      "v_mfma_f32_16x16x32_bf16 v[24:27], v[168:171], a[24:27], v[24:27]\n\t"
      "v_mfma_f32_16x16x32_bf16 v[24:27], v[184:187], a[28:31], v[24:27]\n\t"
      "v_cvt_pk_bf16_f32 v40, v16, v17\n\t"
      "v_cvt_pk_bf16_f32 v41, v18, v19\n\t"
      "v_mfma_f32_16x16x32_bf16 v[24:27], v[200:203], a[32:35], v[24:27]\n\t"
      "ds_write_b32 v0, v40 offset:12544\n\t"
      "v_mfma_f32_16x16x32_bf16 v[24:27], v[216:219], a[36:39], v[24:27]\n\t"
      "ds_write_b32 v0, v41 offset:12548\n\t"
      "v_mfma_f32_16x16x32_bf16 v[24:27], v[232:235], a[40:43], v[24:27]\n\t"
      "buffer_load_dwordx4 v6, s[40:43], s48 offen offset:1024 nt lds\n\t"
      "v_mfma_f32_16x16x32_bf16 v[24:27], v[248:251], a[44:47], v[24:27]\n\t"
      "v_cvt_pk_bf16_f32 v42, v20, v21\n\t"
      "v_cvt_pk_bf16_f32 v43, v22, v23\n\t"
      "v_mfma_f32_16x16x32_bf16 v[28:31], v[172:175], a[24:27], v[28:31]\n\t"
      "ds_write_b32 v0, v42 offset:12576\n\t"
      "v_mfma_f32_16x16x32_bf16 v[28:31], v[188:191], a[28:31], v[28:31]\n\t"
      "ds_write_b32 v0, v43 offset:12580\n\t"
      "v_mfma_f32_16x16x32_bf16 v[28:31], v[204:207], a[32:35], v[28:31]\n\t"
      "v_mfma_f32_16x16x32_bf16 v[28:31], v[220:223], a[36:39], v[28:31]\n\t"
      "v_mfma_f32_16x16x32_bf16 v[28:31], v[236:239], a[40:43], v[28:31]\n\t"
      "v_mfma_f32_16x16x32_bf16 v[28:31], v[252:255], a[44:47], v[28:31]\n\t"
      "v_cvt_pk_bf16_f32 v44, v24, v25\n\t"
      "v_cvt_pk_bf16_f32 v45, v26, v27\n\t"
      "v_mfma_f32_16x16x32_bf16 v[32:35], a[108:111], a[24:27], v[32:35]\n\t"
      "ds_write_b32 v0, v44 offset:12608\n\t"
      "v_mfma_f32_16x16x32_bf16 v[32:35], a[116:119], a[28:31], v[32:35]\n\t"
      "ds_write_b32 v0, v45 offset:12612\n\t"
      "v_mfma_f32_16x16x32_bf16 v[32:35], a[124:127], a[32:35], v[32:35]\n\t"
      "v_mfma_f32_16x16x32_bf16 v[32:35], a[132:135], a[36:39], v[32:35]\n\t"
      "buffer_load_dwordx4 v7, s[40:43], s48 offen offset:2048 nt lds\n\t"
      "s_add_u32 s50, s50, 12288\n\t"
      "v_mfma_f32_16x16x32_bf16 v[32:35], a[140:143], a[40:43], v[32:35]\n\t"
      "s_cmp_eq_u32 s50, 160256\n\t"
      "s_cselect_b32 s50, 25088, s50\n\t"
      "v_mfma_f32_16x16x32_bf16 v[32:35], a[148:151], a[44:47], v[32:35]\n\t"
      "v_cvt_pk_bf16_f32 v46, v28, v29\n\t"
      "v_cvt_pk_bf16_f32 v47, v30, v31\n\t"
      "v_mfma_f32_16x16x32_bf16 v[36:39], a[112:115], a[24:27], v[36:39]\n\t"
      "ds_write_b32 v0, v46 offset:12640\n\t"
      "v_mfma_f32_16x16x32_bf16 v[36:39], a[120:123], a[28:31], v[36:39]\n\t"
      "ds_write_b32 v0, v47 offset:12644\n\t"
      "v_mfma_f32_16x16x32_bf16 v[36:39], a[128:131], a[32:35], v[36:39]\n\t"
      "v_mfma_f32_16x16x32_bf16 v[36:39], a[136:139], a[36:39], v[36:39]\n\t"
      "s_sub_u32 s52, s52, 1\n\t"
      "s_cmp_lg_u32 s52, 0\n\t"
      "v_mfma_f32_16x16x32_bf16 v[36:39], a[144:147], a[40:43], v[36:39]\n\t"
      "v_cvt_pk_bf16_f32 v48, v32, v33\n\t"
      "v_cvt_pk_bf16_f32 v49, v34, v35\n\t"
      "v_mfma_f32_16x16x32_bf16 v[36:39], a[152:155], a[44:47], v[36:39]\n\t"
      "s_cbranch_scc1 .Lloop2_%=\n\t"
      "s_branch .Lendc_%=\n\t"
      ".Lloop3_%=:\n\t"
      "v_mfma_f32_16x16x32_bf16 v[16:19], v[64:67], a[0:3], 0\n\t"
      "ds_write_b32 v0, v48 offset:12672\n\t"
      "v_mfma_f32_16x16x32_bf16 v[16:19], v[80:83], a[4:7], v[16:19]\n\t"
      "ds_write_b32 v0, v49 offset:12676\n\t"
      "v_mfma_f32_16x16x32_bf16 v[16:19], v[96:99], a[8:11], v[16:19]\n\t"
      "ds_read_b128 a[24:27], v58 offset:256\n\t"
      "v_mfma_f32_16x16x32_bf16 v[16:19], v[112:115], a[12:15], v[16:19]\n\t"
      "ds_read_b128 a[28:31], v59 offset:256\n\t"
      "v_mfma_f32_16x16x32_bf16 v[16:19], v[128:131], a[16:19], v[16:19]\n\t"
      "ds_read_b128 a[32:35], v56 offset:512\n\t"
      "v_mfma_f32_16x16x32_bf16 v[16:19], v[144:147], a[20:23], v[16:19]\n\t"
      "ds_read_b128 a[36:39], v57 offset:512\n\t"
      "v_mfma_f32_16x16x32_bf16 v[20:23], v[68:71], a[0:3], 0\n\t"
      "v_cvt_pk_bf16_f32 v50, v36, v37\n\t"
      "v_cvt_pk_bf16_f32 v51, v38, v39\n\t"
      "v_mfma_f32_16x16x32_bf16 v[20:23], v[84:87], a[4:7], v[20:23]\n\t"
      "ds_write_b32 v0, v50 offset:12704\n\t"
      "v_mfma_f32_16x16x32_bf16 v[20:23], v[100:103], a[8:11], v[20:23]\n\t"
      "ds_write_b32 v0, v51 offset:12708\n\t"
      "v_mfma_f32_16x16x32_bf16 v[20:23], v[116:119], a[12:15], v[20:23]\n\t"
      "ds_read_b128 a[40:43], v58 offset:512\n\t"
      "v_mfma_f32_16x16x32_bf16 v[20:23], v[132:135], a[16:19], v[20:23]\n\t"
      "ds_read_b128 a[44:47], v59 offset:512\n\t"
      "v_mfma_f32_16x16x32_bf16 v[20:23], v[148:151], a[20:23], v[20:23]\n\t"
      "s_add_u32 s56, s56, 786432\n\t"
      "s_lshr_b32 s57, s57, 1\n\t"
      "v_mfma_f32_16x16x32_bf16 v[24:27], v[72:75], a[0:3], 0\n\t"
      "s_bitcmp1_b32 s57, 0\n\t"
      "s_cselect_b32 s48, 24576, 0\n\t"
      "s_add_u32 s48, s48, s56\n\t"
      "v_mfma_f32_16x16x32_bf16 v[24:27], v[88:91], a[4:7], v[24:27]\n\t"
      "v_mfma_f32_16x16x32_bf16 v[24:27], v[104:107], a[8:11], v[24:27]\n\t"
      "v_mfma_f32_16x16x32_bf16 v[24:27], v[120:123], a[12:15], v[24:27]\n\t"
      "v_mfma_f32_16x16x32_bf16 v[24:27], v[136:139], a[16:19], v[24:27]\n\t"
      "v_mfma_f32_16x16x32_bf16 v[24:27], v[152:155], a[20:23], v[24:27]\n\t"
      "v_mfma_f32_16x16x32_bf16 v[28:31], v[76:79], a[0:3], 0\n\t"
      "s_waitcnt vmcnt(12)\n\t"
      "s_waitcnt lgkmcnt(0)\n\t"
      "s_barrier\n\t"
      "v_mfma_f32_16x16x32_bf16 v[28:31], v[92:95], a[4:7], v[28:31]\n\t"
      "ds_read_b128 a[48:51], v8 offset:12544\n\t"
      "v_mfma_f32_16x16x32_bf16 v[28:31], v[108:111], a[8:11], v[28:31]\n\t"
      "ds_read_b128 a[52:55], v60 offset:13568\n\t"
      "v_mfma_f32_16x16x32_bf16 v[28:31], v[124:127], a[12:15], v[28:31]\n\t"
      "ds_read_b128 a[56:59], v61 offset:14592\n\t"
      "v_mfma_f32_16x16x32_bf16 v[28:31], v[140:143], a[16:19], v[28:31]\n\t"
      "v_mfma_f32_16x16x32_bf16 v[28:31], v[156:159], a[20:23], v[28:31]\n\t"
      "v_mfma_f32_16x16x32_bf16 v[32:35], a[60:63], a[0:3], 0\n\t"
      "v_mfma_f32_16x16x32_bf16 v[32:35], a[68:71], a[4:7], v[32:35]\n\t"
      "v_mfma_f32_16x16x32_bf16 v[32:35], a[76:79], a[8:11], v[32:35]\n\t"
      "s_waitcnt lgkmcnt(2)\n\t"
      "v_mfma_f32_16x16x32_bf16 v[32:35], a[84:87], a[12:15], v[32:35]\n\t"
      "buffer_store_dwordx4 a[48:51], v12, s[44:47], s49 offen sc1\n\t"
      "v_mfma_f32_16x16x32_bf16 v[32:35], a[92:95], a[16:19], v[32:35]\n\t"
      "v_add_u32 v52, s51, v15\n\t"
      "v_xor_b32 v53, 64, v52\n\t"
      "v_mfma_f32_16x16x32_bf16 v[32:35], a[100:103], a[20:23], v[32:35]\n\t"
      "v_xor_b32 v54, 128, v52\n\t"
      "v_xor_b32 v55, 192, v52\n\t"
      "v_mfma_f32_16x16x32_bf16 v[36:39], a[64:67], a[0:3], 0\n\t"
      "s_add_u32 m0, s18, s50\n\t"
      "v_mfma_f32_16x16x32_bf16 v[36:39], a[72:75], a[4:7], v[36:39]\n\t"
      "buffer_load_dwordx4 v2, s[40:43], s48 offen nt lds\n\t"
      "v_mfma_f32_16x16x32_bf16 v[36:39], a[80:83], a[8:11], v[36:39]\n\t"
      "v_mfma_f32_16x16x32_bf16 v[36:39], a[88:91], a[12:15], v[36:39]\n\t"
      "s_waitcnt lgkmcnt(1)\n\t"
      "v_mfma_f32_16x16x32_bf16 v[36:39], a[96:99], a[16:19], v[36:39]\n\t"
      "buffer_store_dwordx4 a[52:55], v13, s[44:47], s49 offen offset:1024 sc1\n\t"
      "v_mfma_f32_16x16x32_bf16 v[36:39], a[104:107], a[20:23], v[36:39]\n\t"
      "v_mfma_f32_16x16x32_bf16 v[16:19], v[160:163], a[24:27], v[16:19]\n\t"
      "ds_read_b128 a[0:3], v52 offset:0\n\t"
      "v_mfma_f32_16x16x32_bf16 v[16:19], v[176:179], a[28:31], v[16:19]\n\t"
      "ds_read_b128 a[4:7], v53 offset:0\n\t"
      "v_mfma_f32_16x16x32_bf16 v[16:19], v[192:195], a[32:35], v[16:19]\n\t"
      "ds_read_b128 a[8:11], v54 offset:0\n\t"
      "v_mfma_f32_16x16x32_bf16 v[16:19], v[208:211], a[36:39], v[16:19]\n\t"
      "ds_read_b128 a[12:15], v55 offset:0\n\t"
      "v_mfma_f32_16x16x32_bf16 v[16:19], v[224:227], a[40:43], v[16:19]\n\t"
      "ds_read_b128 a[16:19], v52 offset:256\n\t"
      "v_mfma_f32_16x16x32_bf16 v[16:19], v[240:243], a[44:47], v[16:19]\n\t"
      "ds_read_b128 a[20:23], v53 offset:256\n\t"
      "v_mfma_f32_16x16x32_bf16 v[20:23], v[164:167], a[24:27], v[20:23]\n\t"
      "s_add_u32 s51, s51, 12288\n\t"
      "s_cmp_eq_u32 s51, 160256\n\t"
      "s_cselect_b32 s51, 25088, s51\n\t"
      "v_mfma_f32_16x16x32_bf16 v[20:23], v[180:183], a[28:31], v[20:23]\n\t"
      "s_waitcnt lgkmcnt(6)\n\t"
      "v_mfma_f32_16x16x32_bf16 v[20:23], v[196:199], a[32:35], v[20:23]\n\t"
      "buffer_store_dwordx4 a[56:59], v14, s[44:47], s49 offen offset:2048 sc1\n\t"
      "v_mfma_f32_16x16x32_bf16 v[20:23], v[212:215], a[36:39], v[20:23]\n\t"
      "v_mfma_f32_16x16x32_bf16 v[20:23], v[228:231], a[40:43], v[20:23]\n\t"
      "s_waitcnt lgkmcnt(0)\n\t"
      "v_mfma_f32_16x16x32_bf16 v[20:23], v[244:247], a[44:47], v[20:23]\n\t"
      "v_mfma_f32_16x16x32_bf16 v[24:27], v[168:171], a[24:27], v[24:27]\n\t"
      "v_mfma_f32_16x16x32_bf16 v[24:27], v[184:187], a[28:31], v[24:27]\n\t"
      "v_cvt_pk_bf16_f32 v40, v16, v17\n\t"
      "v_cvt_pk_bf16_f32 v41, v18, v19\n\t"
      "v_mfma_f32_16x16x32_bf16 v[24:27], v[200:203], a[32:35], v[24:27]\n\t"
      "ds_write_b32 v0, v40 offset:0\n\t"
      "v_mfma_f32_16x16x32_bf16 v[24:27], v[216:219], a[36:39], v[24:27]\n\t"
      "ds_write_b32 v0, v41 offset:4\n\t"
      "v_mfma_f32_16x16x32_bf16 v[24:27], v[232:235], a[40:43], v[24:27]\n\t"
      "buffer_load_dwordx4 v3, s[40:43], s48 offen offset:1024 nt lds\n\t"
      "v_mfma_f32_16x16x32_bf16 v[24:27], v[248:251], a[44:47], v[24:27]\n\t"
      "v_cvt_pk_bf16_f32 v42, v20, v21\n\t"
      "v_cvt_pk_bf16_f32 v43, v22, v23\n\t"
      "v_mfma_f32_16x16x32_bf16 v[28:31], v[172:175], a[24:27], v[28:31]\n\t"
      "ds_write_b32 v0, v42 offset:32\n\t"
      "v_mfma_f32_16x16x32_bf16 v[28:31], v[188:191], a[28:31], v[28:31]\n\t"
      "ds_write_b32 v0, v43 offset:36\n\t"
      "v_mfma_f32_16x16x32_bf16 v[28:31], v[204:207], a[32:35], v[28:31]\n\t"
      "s_add_u32 s58, s58, 786432\n\t"
      "s_lshr_b32 s59, s59, 1\n\t"
      "v_mfma_f32_16x16x32_bf16 v[28:31], v[220:223], a[36:39], v[28:31]\n\t"
      "s_bitcmp1_b32 s59, 0\n\t"
      "s_cselect_b32 s49, 24576, 0\n\t"
      "s_add_u32 s49, s49, s58\n\t"
      "v_mfma_f32_16x16x32_bf16 v[28:31], v[236:239], a[40:43], v[28:31]\n\t"
      "v_mfma_f32_16x16x32_bf16 v[28:31], v[252:255], a[44:47], v[28:31]\n\t"
      "v_cvt_pk_bf16_f32 v44, v24, v25\n\t"
      "v_cvt_pk_bf16_f32 v45, v26, v27\n\t"
      "v_mfma_f32_16x16x32_bf16 v[32:35], a[108:111], a[24:27], v[32:35]\n\t"
      "ds_write_b32 v0, v44 offset:64\n\t"
      "v_mfma_f32_16x16x32_bf16 v[32:35], a[116:119], a[28:31], v[32:35]\n\t"
      "ds_write_b32 v0, v45 offset:68\n\t"
      "v_mfma_f32_16x16x32_bf16 v[32:35], a[124:127], a[32:35], v[32:35]\n\t"
      "v_mfma_f32_16x16x32_bf16 v[32:35], a[132:135], a[36:39], v[32:35]\n\t"
      "buffer_load_dwordx4 v4, s[40:43], s48 offen offset:2048 nt lds\n\t"
      "s_add_u32 s50, s50, 12288\n\t"
      "v_mfma_f32_16x16x32_bf16 v[32:35], a[140:143], a[40:43], v[32:35]\n\t"
      "s_cmp_eq_u32 s50, 160256\n\t"
      "s_cselect_b32 s50, 25088, s50\n\t"
      "v_mfma_f32_16x16x32_bf16 v[32:35], a[148:151], a[44:47], v[32:35]\n\t"
      "v_cvt_pk_bf16_f32 v46, v28, v29\n\t"
      "v_cvt_pk_bf16_f32 v47, v30, v31\n\t"
      "v_mfma_f32_16x16x32_bf16 v[36:39], a[112:115], a[24:27], v[36:39]\n\t"
      "ds_write_b32 v0, v46 offset:96\n\t"
      "v_mfma_f32_16x16x32_bf16 v[36:39], a[120:123], a[28:31], v[36:39]\n\t"
      "ds_write_b32 v0, v47 offset:100\n\t"
      "v_mfma_f32_16x16x32_bf16 v[36:39], a[128:131], a[32:35], v[36:39]\n\t"
      "v_mfma_f32_16x16x32_bf16 v[36:39], a[136:139], a[36:39], v[36:39]\n\t"
      "v_mfma_f32_16x16x32_bf16 v[36:39], a[144:147], a[40:43], v[36:39]\n\t"
      "v_cvt_pk_bf16_f32 v48, v32, v33\n\t"
      "v_cvt_pk_bf16_f32 v49, v34, v35\n\t"
      "v_mfma_f32_16x16x32_bf16 v[36:39], a[152:155], a[44:47], v[36:39]\n\t"
      ".Lmid3_%=:\n\t"
      "v_mfma_f32_16x16x32_bf16 v[16:19], v[64:67], a[0:3], 0\n\t"
      "ds_write_b32 v0, v48 offset:128\n\t"
      "v_mfma_f32_16x16x32_bf16 v[16:19], v[80:83], a[4:7], v[16:19]\n\t"
      "ds_write_b32 v0, v49 offset:132\n\t"
      "v_mfma_f32_16x16x32_bf16 v[16:19], v[96:99], a[8:11], v[16:19]\n\t"
      "ds_read_b128 a[24:27], v54 offset:256\n\t"
      "v_mfma_f32_16x16x32_bf16 v[16:19], v[112:115], a[12:15], v[16:19]\n\t"
      "ds_read_b128 a[28:31], v55 offset:256\n\t"
      "v_mfma_f32_16x16x32_bf16 v[16:19], v[128:131], a[16:19], v[16:19]\n\t"
      "ds_read_b128 a[32:35], v52 offset:512\n\t"
      "v_mfma_f32_16x16x32_bf16 v[16:19], v[144:147], a[20:23], v[16:19]\n\t"
      "ds_read_b128 a[36:39], v53 offset:512\n\t"
      "v_mfma_f32_16x16x32_bf16 v[20:23], v[68:71], a[0:3], 0\n\t"
      "v_cvt_pk_bf16_f32 v50, v36, v37\n\t"
      "v_cvt_pk_bf16_f32 v51, v38, v39\n\t"
      "v_mfma_f32_16x16x32_bf16 v[20:23], v[84:87], a[4:7], v[20:23]\n\t"
      "ds_write_b32 v0, v50 offset:160\n\t"
      "v_mfma_f32_16x16x32_bf16 v[20:23], v[100:103], a[8:11], v[20:23]\n\t"
      "ds_write_b32 v0, v51 offset:164\n\t"
      "v_mfma_f32_16x16x32_bf16 v[20:23], v[116:119], a[12:15], v[20:23]\n\t"
      "ds_read_b128 a[40:43], v54 offset:512\n\t"
      "v_mfma_f32_16x16x32_bf16 v[20:23], v[132:135], a[16:19], v[20:23]\n\t"
      "ds_read_b128 a[44:47], v55 offset:512\n\t"
      "v_mfma_f32_16x16x32_bf16 v[20:23], v[148:151], a[20:23], v[20:23]\n\t"
      "v_mfma_f32_16x16x32_bf16 v[24:27], v[72:75], a[0:3], 0\n\t"
      "v_mfma_f32_16x16x32_bf16 v[24:27], v[88:91], a[4:7], v[24:27]\n\t"
      "v_mfma_f32_16x16x32_bf16 v[24:27], v[104:107], a[8:11], v[24:27]\n\t"
      "v_mfma_f32_16x16x32_bf16 v[24:27], v[120:123], a[12:15], v[24:27]\n\t"
      "v_mfma_f32_16x16x32_bf16 v[24:27], v[136:139], a[16:19], v[24:27]\n\t"
      "v_mfma_f32_16x16x32_bf16 v[24:27], v[152:155], a[20:23], v[24:27]\n\t"
      "v_mfma_f32_16x16x32_bf16 v[28:31], v[76:79], a[0:3], 0\n\t"
      "s_waitcnt vmcnt(12)\n\t"
      "s_waitcnt lgkmcnt(0)\n\t"
      "s_barrier\n\t"
      "v_mfma_f32_16x16x32_bf16 v[28:31], v[92:95], a[4:7], v[28:31]\n\t"
      "ds_read_b128 a[48:51], v8 offset:0\n\t"
      "v_mfma_f32_16x16x32_bf16 v[28:31], v[108:111], a[8:11], v[28:31]\n\t"
      "ds_read_b128 a[52:55], v60 offset:1024\n\t"
      "v_mfma_f32_16x16x32_bf16 v[28:31], v[124:127], a[12:15], v[28:31]\n\t"
      "ds_read_b128 a[56:59], v61 offset:2048\n\t"
      "v_mfma_f32_16x16x32_bf16 v[28:31], v[140:143], a[16:19], v[28:31]\n\t"
      "v_mfma_f32_16x16x32_bf16 v[28:31], v[156:159], a[20:23], v[28:31]\n\t"
      "v_mfma_f32_16x16x32_bf16 v[32:35], a[60:63], a[0:3], 0\n\t"
      "v_mfma_f32_16x16x32_bf16 v[32:35], a[68:71], a[4:7], v[32:35]\n\t"
      "v_mfma_f32_16x16x32_bf16 v[32:35], a[76:79], a[8:11], v[32:35]\n\t"
      "s_waitcnt lgkmcnt(2)\n\t"
      "v_mfma_f32_16x16x32_bf16 v[32:35], a[84:87], a[12:15], v[32:35]\n\t"
      "buffer_store_dwordx4 a[48:51], v9, s[44:47], s49 offen sc1\n\t"
      "v_mfma_f32_16x16x32_bf16 v[32:35], a[92:95], a[16:19], v[32:35]\n\t"
      "v_add_u32 v56, s51, v15\n\t"
      "v_xor_b32 v57, 64, v56\n\t"
      "v_mfma_f32_16x16x32_bf16 v[32:35], a[100:103], a[20:23], v[32:35]\n\t"
      "v_xor_b32 v58, 128, v56\n\t"
      "v_xor_b32 v59, 192, v56\n\t"
      "v_mfma_f32_16x16x32_bf16 v[36:39], a[64:67], a[0:3], 0\n\t"
      "s_add_u32 m0, s18, s50\n\t"
      "v_mfma_f32_16x16x32_bf16 v[36:39], a[72:75], a[4:7], v[36:39]\n\t"
      "buffer_load_dwordx4 v5, s[40:43], s48 offen nt lds\n\t"
      "v_mfma_f32_16x16x32_bf16 v[36:39], a[80:83], a[8:11], v[36:39]\n\t"
      "v_mfma_f32_16x16x32_bf16 v[36:39], a[88:91], a[12:15], v[36:39]\n\t"
      "s_waitcnt lgkmcnt(1)\n\t"
      "v_mfma_f32_16x16x32_bf16 v[36:39], a[96:99], a[16:19], v[36:39]\n\t"
      "buffer_store_dwordx4 a[52:55], v10, s[44:47], s49 offen offset:1024 sc1\n\t"
      "v_mfma_f32_16x16x32_bf16 v[36:39], a[104:107], a[20:23], v[36:39]\n\t"
      "v_mfma_f32_16x16x32_bf16 v[16:19], v[160:163], a[24:27], v[16:19]\n\t"
      "ds_read_b128 a[0:3], v56 offset:0\n\t"
      "v_mfma_f32_16x16x32_bf16 v[16:19], v[176:179], a[28:31], v[16:19]\n\t"
      "ds_read_b128 a[4:7], v57 offset:0\n\t"
      "v_mfma_f32_16x16x32_bf16 v[16:19], v[192:195], a[32:35], v[16:19]\n\t"
      "ds_read_b128 a[8:11], v58 offset:0\n\t"
      "v_mfma_f32_16x16x32_bf16 v[16:19], v[208:211], a[36:39], v[16:19]\n\t"
      "ds_read_b128 a[12:15], v59 offset:0\n\t"
      "v_mfma_f32_16x16x32_bf16 v[16:19], v[224:227], a[40:43], v[16:19]\n\t"
      "ds_read_b128 a[16:19], v56 offset:256\n\t"
      "v_mfma_f32_16x16x32_bf16 v[16:19], v[240:243], a[44:47], v[16:19]\n\t"
      "ds_read_b128 a[20:23], v57 offset:256\n\t"
      "v_mfma_f32_16x16x32_bf16 v[20:23], v[164:167], a[24:27], v[20:23]\n\t"
      "s_add_u32 s51, s51, 12288\n\t"
      "s_cmp_eq_u32 s51, 160256\n\t"
      "s_cselect_b32 s51, 25088, s51\n\t"
      "v_mfma_f32_16x16x32_bf16 v[20:23], v[180:183], a[28:31], v[20:23]\n\t"
      "s_waitcnt lgkmcnt(6)\n\t"
      "v_mfma_f32_16x16x32_bf16 v[20:23], v[196:199], a[32:35], v[20:23]\n\t"
      "buffer_store_dwordx4 a[56:59], v11, s[44:47], s49 offen offset:2048 sc1\n\t"
      "v_mfma_f32_16x16x32_bf16 v[20:23], v[212:215], a[36:39], v[20:23]\n\t"
      "v_mfma_f32_16x16x32_bf16 v[20:23], v[228:231], a[40:43], v[20:23]\n\t"
      "s_waitcnt lgkmcnt(0)\n\t"
      "v_mfma_f32_16x16x32_bf16 v[20:23], v[244:247], a[44:47], v[20:23]\n\t"
      "v_mfma_f32_16x16x32_bf16 v[24:27], v[168:171], a[24:27], v[24:27]\n\t"
      "v_mfma_f32_16x16x32_bf16 v[24:27], v[184:187], a[28:31], v[24:27]\n\t"
      "v_cvt_pk_bf16_f32 v40, v16, v17\n\t"
      "v_cvt_pk_bf16_f32 v41, v18, v19\n\t"
      "v_mfma_f32_16x16x32_bf16 v[24:27], v[200:203], a[32:35], v[24:27]\n\t"
      "ds_write_b32 v0, v40 offset:12544\n\t"
      "v_mfma_f32_16x16x32_bf16 v[24:27], v[216:219], a[36:39], v[24:27]\n\t"
      "ds_write_b32 v0, v41 offset:12548\n\t"
      "v_mfma_f32_16x16x32_bf16 v[24:27], v[232:235], a[40:43], v[24:27]\n\t"
      "buffer_load_dwordx4 v6, s[40:43], s48 offen offset:1024 nt lds\n\t"
      "v_mfma_f32_16x16x32_bf16 v[24:27], v[248:251], a[44:47], v[24:27]\n\t"
      "v_cvt_pk_bf16_f32 v42, v20, v21\n\t"
      "v_cvt_pk_bf16_f32 v43, v22, v23\n\t"
      "v_mfma_f32_16x16x32_bf16 v[28:31], v[172:175], a[24:27], v[28:31]\n\t"
      "ds_write_b32 v0, v42 offset:12576\n\t"
      "v_mfma_f32_16x16x32_bf16 v[28:31], v[188:191], a[28:31], v[28:31]\n\t"
      "ds_write_b32 v0, v43 offset:12580\n\t"
      "v_mfma_f32_16x16x32_bf16 v[28:31], v[204:207], a[32:35], v[28:31]\n\t"
      "v_mfma_f32_16x16x32_bf16 v[28:31], v[220:223], a[36:39], v[28:31]\n\t"
      "v_mfma_f32_16x16x32_bf16 v[28:31], v[236:239], a[40:43], v[28:31]\n\t"
      "v_mfma_f32_16x16x32_bf16 v[28:31], v[252:255], a[44:47], v[28:31]\n\t"
      "v_cvt_pk_bf16_f32 v44, v24, v25\n\t"
      "v_cvt_pk_bf16_f32 v45, v26, v27\n\t"
      "v_mfma_f32_16x16x32_bf16 v[32:35], a[108:111], a[24:27], v[32:35]\n\t"
      "ds_write_b32 v0, v44 offset:12608\n\t"
      "v_mfma_f32_16x16x32_bf16 v[32:35], a[116:119], a[28:31], v[32:35]\n\t"
      "ds_write_b32 v0, v45 offset:12612\n\t"
      "v_mfma_f32_16x16x32_bf16 v[32:35], a[124:127], a[32:35], v[32:35]\n\t"
      "v_mfma_f32_16x16x32_bf16 v[32:35], a[132:135], a[36:39], v[32:35]\n\t"
      "buffer_load_dwordx4 v7, s[40:43], s48 offen offset:2048 nt lds\n\t"
      "s_add_u32 s50, s50, 12288\n\t"
      "v_mfma_f32_16x16x32_bf16 v[32:35], a[140:143], a[40:43], v[32:35]\n\t"
      "s_cmp_eq_u32 s50, 160256\n\t"
      "s_cselect_b32 s50, 25088, s50\n\t"
      "v_mfma_f32_16x16x32_bf16 v[32:35], a[148:151], a[44:47], v[32:35]\n\t"
      "v_cvt_pk_bf16_f32 v46, v28, v29\n\t"
      "v_cvt_pk_bf16_f32 v47, v30, v31\n\t"
      "v_mfma_f32_16x16x32_bf16 v[36:39], a[112:115], a[24:27], v[36:39]\n\t"
      "ds_write_b32 v0, v46 offset:12640\n\t"
      "v_mfma_f32_16x16x32_bf16 v[36:39], a[120:123], a[28:31], v[36:39]\n\t"
      "ds_write_b32 v0, v47 offset:12644\n\t"
      "v_mfma_f32_16x16x32_bf16 v[36:39], a[128:131], a[32:35], v[36:39]\n\t"
      "v_mfma_f32_16x16x32_bf16 v[36:39], a[136:139], a[36:39], v[36:39]\n\t"
      "s_sub_u32 s52, s52, 1\n\t"
      "s_cmp_lg_u32 s52, 0\n\t"
      "v_mfma_f32_16x16x32_bf16 v[36:39], a[144:147], a[40:43], v[36:39]\n\t"
      "v_cvt_pk_bf16_f32 v48, v32, v33\n\t"
      "v_cvt_pk_bf16_f32 v49, v34, v35\n\t"
      "v_mfma_f32_16x16x32_bf16 v[36:39], a[152:155], a[44:47], v[36:39]\n\t"
      "s_cbranch_scc1 .Lloop3_%=\n\t"
      ".Lendc_%=:\n\t"
      "s_cmp_eq_u32 s48, 113221632\n\t"
      "s_cbranch_scc1 .Lwrap3_%=\n\t"
      ".Lwrapped3_%=:\n\t"
      "v_mfma_f32_16x16x32_bf16 v[16:19], v[64:67], a[0:3], 0\n\t"
      "ds_write_b32 v0, v48 offset:12672\n\t"
      "v_mfma_f32_16x16x32_bf16 v[16:19], v[80:83], a[4:7], v[16:19]\n\t"
      "ds_write_b32 v0, v49 offset:12676\n\t"
      "v_mfma_f32_16x16x32_bf16 v[16:19], v[96:99], a[8:11], v[16:19]\n\t"
      "ds_read_b128 a[24:27], v58 offset:256\n\t"
      "v_mfma_f32_16x16x32_bf16 v[16:19], v[112:115], a[12:15], v[16:19]\n\t"
      "ds_read_b128 a[28:31], v59 offset:256\n\t"
      "v_mfma_f32_16x16x32_bf16 v[16:19], v[128:131], a[16:19], v[16:19]\n\t"
      "ds_read_b128 a[32:35], v56 offset:512\n\t"
      "v_mfma_f32_16x16x32_bf16 v[16:19], v[144:147], a[20:23], v[16:19]\n\t"
      "ds_read_b128 a[36:39], v57 offset:512\n\t"
      "v_mfma_f32_16x16x32_bf16 v[20:23], v[68:71], a[0:3], 0\n\t"
      "v_cvt_pk_bf16_f32 v50, v36, v37\n\t"
      "v_cvt_pk_bf16_f32 v51, v38, v39\n\t"
      "v_mfma_f32_16x16x32_bf16 v[20:23], v[84:87], a[4:7], v[20:23]\n\t"
      "ds_write_b32 v0, v50 offset:12704\n\t"
      "v_mfma_f32_16x16x32_bf16 v[20:23], v[100:103], a[8:11], v[20:23]\n\t"
      "ds_write_b32 v0, v51 offset:12708\n\t"
      "v_mfma_f32_16x16x32_bf16 v[20:23], v[116:119], a[12:15], v[20:23]\n\t"
      "ds_read_b128 a[40:43], v58 offset:512\n\t"
      "v_mfma_f32_16x16x32_bf16 v[20:23], v[132:135], a[16:19], v[20:23]\n\t"
      "ds_read_b128 a[44:47], v59 offset:512\n\t"
      "v_mfma_f32_16x16x32_bf16 v[20:23], v[148:151], a[20:23], v[20:23]\n\t"
      "s_add_u32 s58, s58, 786432\n\t"
      "s_lshr_b32 s59, s59, 1\n\t"
      "v_mfma_f32_16x16x32_bf16 v[24:27], v[72:75], a[0:3], 0\n\t"
      "s_bitcmp1_b32 s59, 0\n\t"
      "s_cselect_b32 s84, 24576, 0\n\t"
      "s_add_u32 s84, s84, s58\n\t"
      "v_mfma_f32_16x16x32_bf16 v[24:27], v[88:91], a[4:7], v[24:27]\n\t"
      "s_add_u32 s58, s58, 786432\n\t"
      "s_lshr_b32 s59, s59, 1\n\t"
      "v_mfma_f32_16x16x32_bf16 v[24:27], v[104:107], a[8:11], v[24:27]\n\t"
      "s_bitcmp1_b32 s59, 0\n\t"
      "s_cselect_b32 s85, 24576, 0\n\t"
      "s_add_u32 s85, s85, s58\n\t"
      "v_mfma_f32_16x16x32_bf16 v[24:27], v[120:123], a[12:15], v[24:27]\n\t"
      "s_waitcnt vmcnt(12)\n\t"
      "s_waitcnt lgkmcnt(0)\n\t"
      "s_barrier\n\t"
      "v_mfma_f32_16x16x32_bf16 v[24:27], v[136:139], a[16:19], v[24:27]\n\t"
      "v_mfma_f32_16x16x32_bf16 v[24:27], v[152:155], a[20:23], v[24:27]\n\t"
      "v_mfma_f32_16x16x32_bf16 v[28:31], v[76:79], a[0:3], 0\n\t"
      "v_mfma_f32_16x16x32_bf16 v[28:31], v[92:95], a[4:7], v[28:31]\n\t"
      "ds_read_b128 a[188:191], v8 offset:12544\n\t"
      "v_mfma_f32_16x16x32_bf16 v[28:31], v[108:111], a[8:11], v[28:31]\n\t"
      "ds_read_b128 a[192:195], v60 offset:13568\n\t"
      "v_mfma_f32_16x16x32_bf16 v[28:31], v[124:127], a[12:15], v[28:31]\n\t"
      "ds_read_b128 a[196:199], v61 offset:14592\n\t"
      "v_mfma_f32_16x16x32_bf16 v[28:31], v[140:143], a[16:19], v[28:31]\n\t"
      "v_mfma_f32_16x16x32_bf16 v[28:31], v[156:159], a[20:23], v[28:31]\n\t"
      "v_mfma_f32_16x16x32_bf16 v[32:35], a[60:63], a[0:3], 0\n\t"
      "v_mfma_f32_16x16x32_bf16 v[32:35], a[68:71], a[4:7], v[32:35]\n\t"
      "v_mfma_f32_16x16x32_bf16 v[32:35], a[76:79], a[8:11], v[32:35]\n\t"
      "v_mfma_f32_16x16x32_bf16 v[32:35], a[84:87], a[12:15], v[32:35]\n\t"
      "v_mfma_f32_16x16x32_bf16 v[32:35], a[92:95], a[16:19], v[32:35]\n\t"
      "v_add_u32 v52, s51, v15\n\t"
      "v_xor_b32 v53, 64, v52\n\t"
      "v_mfma_f32_16x16x32_bf16 v[32:35], a[100:103], a[20:23], v[32:35]\n\t"
      "v_xor_b32 v54, 128, v52\n\t"
      "v_xor_b32 v55, 192, v52\n\t"
      "v_mfma_f32_16x16x32_bf16 v[36:39], a[64:67], a[0:3], 0\n\t"
      "v_mfma_f32_16x16x32_bf16 v[36:39], a[72:75], a[4:7], v[36:39]\n\t"
      "v_mfma_f32_16x16x32_bf16 v[36:39], a[80:83], a[8:11], v[36:39]\n\t"
      "v_mfma_f32_16x16x32_bf16 v[36:39], a[88:91], a[12:15], v[36:39]\n\t"
      "v_mfma_f32_16x16x32_bf16 v[36:39], a[96:99], a[16:19], v[36:39]\n\t"
      "v_mfma_f32_16x16x32_bf16 v[36:39], a[104:107], a[20:23], v[36:39]\n\t"
      "v_mfma_f32_16x16x32_bf16 v[16:19], v[160:163], a[24:27], v[16:19]\n\t"
      "ds_read_b128 a[0:3], v52 offset:0\n\t"
      "v_mfma_f32_16x16x32_bf16 v[16:19], v[176:179], a[28:31], v[16:19]\n\t"
      "ds_read_b128 a[4:7], v53 offset:0\n\t"
      "v_mfma_f32_16x16x32_bf16 v[16:19], v[192:195], a[32:35], v[16:19]\n\t"
      "ds_read_b128 a[8:11], v54 offset:0\n\t"
      "v_mfma_f32_16x16x32_bf16 v[16:19], v[208:211], a[36:39], v[16:19]\n\t"
      "ds_read_b128 a[12:15], v55 offset:0\n\t"
      "v_mfma_f32_16x16x32_bf16 v[16:19], v[224:227], a[40:43], v[16:19]\n\t"
      "ds_read_b128 a[16:19], v52 offset:256\n\t"
      "v_mfma_f32_16x16x32_bf16 v[16:19], v[240:243], a[44:47], v[16:19]\n\t"
      "ds_read_b128 a[20:23], v53 offset:256\n\t"
      "v_mfma_f32_16x16x32_bf16 v[20:23], v[164:167], a[24:27], v[20:23]\n\t"
      "s_add_u32 s51, s51, 12288\n\t"
      "s_cmp_eq_u32 s51, 160256\n\t"
      "s_cselect_b32 s51, 25088, s51\n\t"
      "v_mfma_f32_16x16x32_bf16 v[20:23], v[180:183], a[28:31], v[20:23]\n\t"
      "v_mfma_f32_16x16x32_bf16 v[20:23], v[196:199], a[32:35], v[20:23]\n\t"
      "v_mfma_f32_16x16x32_bf16 v[20:23], v[212:215], a[36:39], v[20:23]\n\t"
      "v_mfma_f32_16x16x32_bf16 v[20:23], v[228:231], a[40:43], v[20:23]\n\t"
      "s_waitcnt lgkmcnt(0)\n\t"
      "v_mfma_f32_16x16x32_bf16 v[20:23], v[244:247], a[44:47], v[20:23]\n\t"
      "v_mfma_f32_16x16x32_bf16 v[24:27], v[168:171], a[24:27], v[24:27]\n\t"
      "v_mfma_f32_16x16x32_bf16 v[24:27], v[184:187], a[28:31], v[24:27]\n\t"
      "v_cvt_pk_bf16_f32 v40, v16, v17\n\t"
      "v_cvt_pk_bf16_f32 v41, v18, v19\n\t"
      "v_mfma_f32_16x16x32_bf16 v[24:27], v[200:203], a[32:35], v[24:27]\n\t"
      "ds_write_b32 v0, v40 offset:0\n\t"
      "v_mfma_f32_16x16x32_bf16 v[24:27], v[216:219], a[36:39], v[24:27]\n\t"
      "ds_write_b32 v0, v41 offset:4\n\t"
      "v_mfma_f32_16x16x32_bf16 v[24:27], v[232:235], a[40:43], v[24:27]\n\t"
      "v_mfma_f32_16x16x32_bf16 v[24:27], v[248:251], a[44:47], v[24:27]\n\t"
      "v_cvt_pk_bf16_f32 v42, v20, v21\n\t"
      "v_cvt_pk_bf16_f32 v43, v22, v23\n\t"
      "v_mfma_f32_16x16x32_bf16 v[28:31], v[172:175], a[24:27], v[28:31]\n\t"
      "ds_write_b32 v0, v42 offset:32\n\t"
      "v_mfma_f32_16x16x32_bf16 v[28:31], v[188:191], a[28:31], v[28:31]\n\t"
      "ds_write_b32 v0, v43 offset:36\n\t"
      "v_mfma_f32_16x16x32_bf16 v[28:31], v[204:207], a[32:35], v[28:31]\n\t"
      "v_mfma_f32_16x16x32_bf16 v[28:31], v[220:223], a[36:39], v[28:31]\n\t"
      "v_mfma_f32_16x16x32_bf16 v[28:31], v[236:239], a[40:43], v[28:31]\n\t"
      "v_mfma_f32_16x16x32_bf16 v[28:31], v[252:255], a[44:47], v[28:31]\n\t"
      "v_cvt_pk_bf16_f32 v44, v24, v25\n\t"
      "v_cvt_pk_bf16_f32 v45, v26, v27\n\t"
      "v_mfma_f32_16x16x32_bf16 v[32:35], a[108:111], a[24:27], v[32:35]\n\t"
      "ds_write_b32 v0, v44 offset:64\n\t"
      "v_mfma_f32_16x16x32_bf16 v[32:35], a[116:119], a[28:31], v[32:35]\n\t"
      "ds_write_b32 v0, v45 offset:68\n\t"
      "v_mfma_f32_16x16x32_bf16 v[32:35], a[124:127], a[32:35], v[32:35]\n\t"
      "v_mfma_f32_16x16x32_bf16 v[32:35], a[132:135], a[36:39], v[32:35]\n\t"
      "v_mfma_f32_16x16x32_bf16 v[32:35], a[140:143], a[40:43], v[32:35]\n\t"
      "v_mfma_f32_16x16x32_bf16 v[32:35], a[148:151], a[44:47], v[32:35]\n\t"
      "v_cvt_pk_bf16_f32 v46, v28, v29\n\t"
      "v_cvt_pk_bf16_f32 v47, v30, v31\n\t"
      "v_mfma_f32_16x16x32_bf16 v[36:39], a[112:115], a[24:27], v[36:39]\n\t"
      "ds_write_b32 v0, v46 offset:96\n\t"
      "v_mfma_f32_16x16x32_bf16 v[36:39], a[120:123], a[28:31], v[36:39]\n\t"
      "ds_write_b32 v0, v47 offset:100\n\t"
      "v_mfma_f32_16x16x32_bf16 v[36:39], a[128:131], a[32:35], v[36:39]\n\t"
      "v_mfma_f32_16x16x32_bf16 v[36:39], a[136:139], a[36:39], v[36:39]\n\t"
      "v_mfma_f32_16x16x32_bf16 v[36:39], a[144:147], a[40:43], v[36:39]\n\t"
      "v_cvt_pk_bf16_f32 v48, v32, v33\n\t"
      "v_cvt_pk_bf16_f32 v49, v34, v35\n\t"
      "v_mfma_f32_16x16x32_bf16 v[36:39], a[152:155], a[44:47], v[36:39]\n\t"
      "v_mfma_f32_16x16x32_bf16 v[16:19], v[64:67], a[0:3], 0\n\t"
      "ds_write_b32 v0, v48 offset:128\n\t"
      "v_mfma_f32_16x16x32_bf16 v[16:19], v[80:83], a[4:7], v[16:19]\n\t"
      "ds_write_b32 v0, v49 offset:132\n\t"
      "v_mfma_f32_16x16x32_bf16 v[16:19], v[96:99], a[8:11], v[16:19]\n\t"
      "ds_read_b128 a[24:27], v54 offset:256\n\t"
      "v_mfma_f32_16x16x32_bf16 v[16:19], v[112:115], a[12:15], v[16:19]\n\t"
      "ds_read_b128 a[28:31], v55 offset:256\n\t"
      "v_mfma_f32_16x16x32_bf16 v[16:19], v[128:131], a[16:19], v[16:19]\n\t"
      "ds_read_b128 a[32:35], v52 offset:512\n\t"
      "v_mfma_f32_16x16x32_bf16 v[16:19], v[144:147], a[20:23], v[16:19]\n\t"
      "ds_read_b128 a[36:39], v53 offset:512\n\t"
      "v_mfma_f32_16x16x32_bf16 v[20:23], v[68:71], a[0:3], 0\n\t"
      "v_cvt_pk_bf16_f32 v50, v36, v37\n\t"
      "v_cvt_pk_bf16_f32 v51, v38, v39\n\t"
      "v_mfma_f32_16x16x32_bf16 v[20:23], v[84:87], a[4:7], v[20:23]\n\t"
      "ds_write_b32 v0, v50 offset:160\n\t"
      "v_mfma_f32_16x16x32_bf16 v[20:23], v[100:103], a[8:11], v[20:23]\n\t"
      "ds_write_b32 v0, v51 offset:164\n\t"
      "v_mfma_f32_16x16x32_bf16 v[20:23], v[116:119], a[12:15], v[20:23]\n\t"
      "ds_read_b128 a[40:43], v54 offset:512\n\t"
      "v_mfma_f32_16x16x32_bf16 v[20:23], v[132:135], a[16:19], v[20:23]\n\t"
      "ds_read_b128 a[44:47], v55 offset:512\n\t"
      "v_mfma_f32_16x16x32_bf16 v[20:23], v[148:151], a[20:23], v[20:23]\n\t"
      "v_mfma_f32_16x16x32_bf16 v[24:27], v[72:75], a[0:3], 0\n\t"
      "v_mfma_f32_16x16x32_bf16 v[24:27], v[88:91], a[4:7], v[24:27]\n\t"
      "v_mfma_f32_16x16x32_bf16 v[24:27], v[104:107], a[8:11], v[24:27]\n\t"
      "v_mfma_f32_16x16x32_bf16 v[24:27], v[120:123], a[12:15], v[24:27]\n\t"
      "s_waitcnt vmcnt(6)\n\t"
      "s_waitcnt lgkmcnt(0)\n\t"
      "s_barrier\n\t"
      "v_mfma_f32_16x16x32_bf16 v[24:27], v[136:139], a[16:19], v[24:27]\n\t"
      "v_mfma_f32_16x16x32_bf16 v[24:27], v[152:155], a[20:23], v[24:27]\n\t"
      "v_mfma_f32_16x16x32_bf16 v[28:31], v[76:79], a[0:3], 0\n\t"
      "v_mfma_f32_16x16x32_bf16 v[28:31], v[92:95], a[4:7], v[28:31]\n\t"
      "ds_read_b128 a[200:203], v8 offset:0\n\t"
      "v_mfma_f32_16x16x32_bf16 v[28:31], v[108:111], a[8:11], v[28:31]\n\t"
      "ds_read_b128 a[204:207], v60 offset:1024\n\t"
      "v_mfma_f32_16x16x32_bf16 v[28:31], v[124:127], a[12:15], v[28:31]\n\t"
      "ds_read_b128 a[208:211], v61 offset:2048\n\t"
      "v_mfma_f32_16x16x32_bf16 v[28:31], v[140:143], a[16:19], v[28:31]\n\t"
      "v_mfma_f32_16x16x32_bf16 v[28:31], v[156:159], a[20:23], v[28:31]\n\t"
      "v_mfma_f32_16x16x32_bf16 v[32:35], a[60:63], a[0:3], 0\n\t"
      "v_mfma_f32_16x16x32_bf16 v[32:35], a[68:71], a[4:7], v[32:35]\n\t"
      "v_mfma_f32_16x16x32_bf16 v[32:35], a[76:79], a[8:11], v[32:35]\n\t"
      "v_mfma_f32_16x16x32_bf16 v[32:35], a[84:87], a[12:15], v[32:35]\n\t"
      "v_mfma_f32_16x16x32_bf16 v[32:35], a[92:95], a[16:19], v[32:35]\n\t"
      "v_add_u32 v56, s51, v15\n\t"
      "v_xor_b32 v57, 64, v56\n\t"
      "v_mfma_f32_16x16x32_bf16 v[32:35], a[100:103], a[20:23], v[32:35]\n\t"
      "v_xor_b32 v58, 128, v56\n\t"
      "v_xor_b32 v59, 192, v56\n\t"
      "v_mfma_f32_16x16x32_bf16 v[36:39], a[64:67], a[0:3], 0\n\t"
      "v_mfma_f32_16x16x32_bf16 v[36:39], a[72:75], a[4:7], v[36:39]\n\t"
      "v_mfma_f32_16x16x32_bf16 v[36:39], a[80:83], a[8:11], v[36:39]\n\t"
      "v_mfma_f32_16x16x32_bf16 v[36:39], a[88:91], a[12:15], v[36:39]\n\t"
      "v_mfma_f32_16x16x32_bf16 v[36:39], a[96:99], a[16:19], v[36:39]\n\t"
      "v_mfma_f32_16x16x32_bf16 v[36:39], a[104:107], a[20:23], v[36:39]\n\t"
      "v_mfma_f32_16x16x32_bf16 v[16:19], v[160:163], a[24:27], v[16:19]\n\t"
      "ds_read_b128 a[0:3], v56 offset:0\n\t"
      "v_mfma_f32_16x16x32_bf16 v[16:19], v[176:179], a[28:31], v[16:19]\n\t"
      "ds_read_b128 a[4:7], v57 offset:0\n\t"
      "v_mfma_f32_16x16x32_bf16 v[16:19], v[192:195], a[32:35], v[16:19]\n\t"
      "ds_read_b128 a[8:11], v58 offset:0\n\t"
      "v_mfma_f32_16x16x32_bf16 v[16:19], v[208:211], a[36:39], v[16:19]\n\t"
      "ds_read_b128 a[12:15], v59 offset:0\n\t"
      "v_mfma_f32_16x16x32_bf16 v[16:19], v[224:227], a[40:43], v[16:19]\n\t"
      "ds_read_b128 a[16:19], v56 offset:256\n\t"
      "v_mfma_f32_16x16x32_bf16 v[16:19], v[240:243], a[44:47], v[16:19]\n\t"
      "ds_read_b128 a[20:23], v57 offset:256\n\t"
      "v_mfma_f32_16x16x32_bf16 v[20:23], v[164:167], a[24:27], v[20:23]\n\t"
      "s_add_u32 s51, s51, 12288\n\t"
      "s_cmp_eq_u32 s51, 160256\n\t"
      "s_cselect_b32 s51, 25088, s51\n\t"
      "v_mfma_f32_16x16x32_bf16 v[20:23], v[180:183], a[28:31], v[20:23]\n\t"
      "v_mfma_f32_16x16x32_bf16 v[20:23], v[196:199], a[32:35], v[20:23]\n\t"
      "buffer_store_dwordx4 a[188:191], v12, s[44:47], s49 offen sc1\n\t"
      "v_mfma_f32_16x16x32_bf16 v[20:23], v[212:215], a[36:39], v[20:23]\n\t"
      "v_mfma_f32_16x16x32_bf16 v[20:23], v[228:231], a[40:43], v[20:23]\n\t"
      "s_waitcnt lgkmcnt(0)\n\t"
      "v_mfma_f32_16x16x32_bf16 v[20:23], v[244:247], a[44:47], v[20:23]\n\t"
      "v_mfma_f32_16x16x32_bf16 v[24:27], v[168:171], a[24:27], v[24:27]\n\t"
      "v_mfma_f32_16x16x32_bf16 v[24:27], v[184:187], a[28:31], v[24:27]\n\t"
      "v_cvt_pk_bf16_f32 v40, v16, v17\n\t"
      "v_cvt_pk_bf16_f32 v41, v18, v19\n\t"
      "v_mfma_f32_16x16x32_bf16 v[24:27], v[200:203], a[32:35], v[24:27]\n\t"
      "ds_write_b32 v0, v40 offset:12544\n\t"
      "v_mfma_f32_16x16x32_bf16 v[24:27], v[216:219], a[36:39], v[24:27]\n\t"
      "ds_write_b32 v0, v41 offset:12548\n\t"
      "v_mfma_f32_16x16x32_bf16 v[24:27], v[232:235], a[40:43], v[24:27]\n\t"
      "v_mfma_f32_16x16x32_bf16 v[24:27], v[248:251], a[44:47], v[24:27]\n\t"
      "v_cvt_pk_bf16_f32 v42, v20, v21\n\t"
      "v_cvt_pk_bf16_f32 v43, v22, v23\n\t"
      "v_mfma_f32_16x16x32_bf16 v[28:31], v[172:175], a[24:27], v[28:31]\n\t"
      "ds_write_b32 v0, v42 offset:12576\n\t"
      "v_mfma_f32_16x16x32_bf16 v[28:31], v[188:191], a[28:31], v[28:31]\n\t"
      "ds_write_b32 v0, v43 offset:12580\n\t"
      "v_mfma_f32_16x16x32_bf16 v[28:31], v[204:207], a[32:35], v[28:31]\n\t"
      "v_mfma_f32_16x16x32_bf16 v[28:31], v[220:223], a[36:39], v[28:31]\n\t"
      "v_mfma_f32_16x16x32_bf16 v[28:31], v[236:239], a[40:43], v[28:31]\n\t"
      "buffer_store_dwordx4 a[192:195], v13, s[44:47], s49 offen offset:1024 sc1\n\t"
      "v_mfma_f32_16x16x32_bf16 v[28:31], v[252:255], a[44:47], v[28:31]\n\t"
      "v_cvt_pk_bf16_f32 v44, v24, v25\n\t"
      "v_cvt_pk_bf16_f32 v45, v26, v27\n\t"
      "v_mfma_f32_16x16x32_bf16 v[32:35], a[108:111], a[24:27], v[32:35]\n\t"
      "ds_write_b32 v0, v44 offset:12608\n\t"
      "v_mfma_f32_16x16x32_bf16 v[32:35], a[116:119], a[28:31], v[32:35]\n\t"
      "ds_write_b32 v0, v45 offset:12612\n\t"
      "v_mfma_f32_16x16x32_bf16 v[32:35], a[124:127], a[32:35], v[32:35]\n\t"
      "v_mfma_f32_16x16x32_bf16 v[32:35], a[132:135], a[36:39], v[32:35]\n\t"
      "v_mfma_f32_16x16x32_bf16 v[32:35], a[140:143], a[40:43], v[32:35]\n\t"
      "v_mfma_f32_16x16x32_bf16 v[32:35], a[148:151], a[44:47], v[32:35]\n\t"
      "v_cvt_pk_bf16_f32 v46, v28, v29\n\t"
      "v_cvt_pk_bf16_f32 v47, v30, v31\n\t"
      "v_mfma_f32_16x16x32_bf16 v[36:39], a[112:115], a[24:27], v[36:39]\n\t"
      "ds_write_b32 v0, v46 offset:12640\n\t"
      "v_mfma_f32_16x16x32_bf16 v[36:39], a[120:123], a[28:31], v[36:39]\n\t"
      "ds_write_b32 v0, v47 offset:12644\n\t"
      "v_mfma_f32_16x16x32_bf16 v[36:39], a[128:131], a[32:35], v[36:39]\n\t"
      "v_mfma_f32_16x16x32_bf16 v[36:39], a[136:139], a[36:39], v[36:39]\n\t"
      "v_mfma_f32_16x16x32_bf16 v[36:39], a[144:147], a[40:43], v[36:39]\n\t"
      "v_cvt_pk_bf16_f32 v48, v32, v33\n\t"
      "v_cvt_pk_bf16_f32 v49, v34, v35\n\t"
      "v_mfma_f32_16x16x32_bf16 v[36:39], a[152:155], a[44:47], v[36:39]\n\t"
      "v_mfma_f32_16x16x32_bf16 v[16:19], v[64:67], a[0:3], 0\n\t"
      "ds_write_b32 v0, v48 offset:12672\n\t"
      "v_mfma_f32_16x16x32_bf16 v[16:19], v[80:83], a[4:7], v[16:19]\n\t"
      "ds_write_b32 v0, v49 offset:12676\n\t"
      "v_mfma_f32_16x16x32_bf16 v[16:19], v[96:99], a[8:11], v[16:19]\n\t"
      "ds_read_b128 a[24:27], v58 offset:256\n\t"
      "v_mfma_f32_16x16x32_bf16 v[16:19], v[112:115], a[12:15], v[16:19]\n\t"
      "ds_read_b128 a[28:31], v59 offset:256\n\t"
      "buffer_store_dwordx4 a[196:199], v14, s[44:47], s49 offen offset:2048 sc1\n\t"
      "v_mfma_f32_16x16x32_bf16 v[16:19], v[128:131], a[16:19], v[16:19]\n\t"
      "ds_read_b128 a[32:35], v56 offset:512\n\t"
      "v_mfma_f32_16x16x32_bf16 v[16:19], v[144:147], a[20:23], v[16:19]\n\t"
      "ds_read_b128 a[36:39], v57 offset:512\n\t"
      "v_mfma_f32_16x16x32_bf16 v[20:23], v[68:71], a[0:3], 0\n\t"
      "v_cvt_pk_bf16_f32 v50, v36, v37\n\t"
      "v_cvt_pk_bf16_f32 v51, v38, v39\n\t"
      "v_mfma_f32_16x16x32_bf16 v[20:23], v[84:87], a[4:7], v[20:23]\n\t"
      "ds_write_b32 v0, v50 offset:12704\n\t"
      "v_mfma_f32_16x16x32_bf16 v[20:23], v[100:103], a[8:11], v[20:23]\n\t"
      "ds_write_b32 v0, v51 offset:12708\n\t"
      "v_mfma_f32_16x16x32_bf16 v[20:23], v[116:119], a[12:15], v[20:23]\n\t"
      "ds_read_b128 a[40:43], v58 offset:512\n\t"
      "v_mfma_f32_16x16x32_bf16 v[20:23], v[132:135], a[16:19], v[20:23]\n\t"
      "ds_read_b128 a[44:47], v59 offset:512\n\t"
      "v_mfma_f32_16x16x32_bf16 v[20:23], v[148:151], a[20:23], v[20:23]\n\t"
      "v_mfma_f32_16x16x32_bf16 v[24:27], v[72:75], a[0:3], 0\n\t"
      "buffer_store_dwordx4 a[200:203], v9, s[44:47], s84 offen sc1\n\t"
      "v_mfma_f32_16x16x32_bf16 v[24:27], v[88:91], a[4:7], v[24:27]\n\t"
      "v_mfma_f32_16x16x32_bf16 v[24:27], v[104:107], a[8:11], v[24:27]\n\t"
      "v_mfma_f32_16x16x32_bf16 v[24:27], v[120:123], a[12:15], v[24:27]\n\t"
      "s_waitcnt vmcnt(4)\n\t"
      "s_waitcnt lgkmcnt(0)\n\t"
      "s_barrier\n\t"
      "v_mfma_f32_16x16x32_bf16 v[24:27], v[136:139], a[16:19], v[24:27]\n\t"
      "v_mfma_f32_16x16x32_bf16 v[24:27], v[152:155], a[20:23], v[24:27]\n\t"
      "v_mfma_f32_16x16x32_bf16 v[28:31], v[76:79], a[0:3], 0\n\t"
      "v_mfma_f32_16x16x32_bf16 v[28:31], v[92:95], a[4:7], v[28:31]\n\t"
      "ds_read_b128 a[212:215], v8 offset:12544\n\t"
      "v_mfma_f32_16x16x32_bf16 v[28:31], v[108:111], a[8:11], v[28:31]\n\t"
      "ds_read_b128 a[216:219], v60 offset:13568\n\t"
      "v_mfma_f32_16x16x32_bf16 v[28:31], v[124:127], a[12:15], v[28:31]\n\t"
      "ds_read_b128 a[220:223], v61 offset:14592\n\t"
      "v_mfma_f32_16x16x32_bf16 v[28:31], v[140:143], a[16:19], v[28:31]\n\t"
      "v_mfma_f32_16x16x32_bf16 v[28:31], v[156:159], a[20:23], v[28:31]\n\t"
      "v_mfma_f32_16x16x32_bf16 v[32:35], a[60:63], a[0:3], 0\n\t"
      "buffer_store_dwordx4 a[204:207], v10, s[44:47], s84 offen offset:1024 sc1\n\t"
      "v_mfma_f32_16x16x32_bf16 v[32:35], a[68:71], a[4:7], v[32:35]\n\t"
      "v_mfma_f32_16x16x32_bf16 v[32:35], a[76:79], a[8:11], v[32:35]\n\t"
      "v_mfma_f32_16x16x32_bf16 v[32:35], a[84:87], a[12:15], v[32:35]\n\t"
      "v_mfma_f32_16x16x32_bf16 v[32:35], a[92:95], a[16:19], v[32:35]\n\t"
      "v_add_u32 v52, s51, v15\n\t"
      "v_xor_b32 v53, 64, v52\n\t"
      "v_mfma_f32_16x16x32_bf16 v[32:35], a[100:103], a[20:23], v[32:35]\n\t"
      "v_xor_b32 v54, 128, v52\n\t"
      "v_xor_b32 v55, 192, v52\n\t"
      "v_mfma_f32_16x16x32_bf16 v[36:39], a[64:67], a[0:3], 0\n\t"
      "v_mfma_f32_16x16x32_bf16 v[36:39], a[72:75], a[4:7], v[36:39]\n\t"
      "v_mfma_f32_16x16x32_bf16 v[36:39], a[80:83], a[8:11], v[36:39]\n\t"
      "v_mfma_f32_16x16x32_bf16 v[36:39], a[88:91], a[12:15], v[36:39]\n\t"
      "v_mfma_f32_16x16x32_bf16 v[36:39], a[96:99], a[16:19], v[36:39]\n\t"
      "buffer_store_dwordx4 a[208:211], v11, s[44:47], s84 offen offset:2048 sc1\n\t"
      "v_mfma_f32_16x16x32_bf16 v[36:39], a[104:107], a[20:23], v[36:39]\n\t"
      "v_mfma_f32_16x16x32_bf16 v[16:19], v[160:163], a[24:27], v[16:19]\n\t"
      "ds_read_b128 a[0:3], v52 offset:0\n\t"
      "v_mfma_f32_16x16x32_bf16 v[16:19], v[176:179], a[28:31], v[16:19]\n\t"
      "ds_read_b128 a[4:7], v53 offset:0\n\t"
      "v_mfma_f32_16x16x32_bf16 v[16:19], v[192:195], a[32:35], v[16:19]\n\t"
      "ds_read_b128 a[8:11], v54 offset:0\n\t"
      "v_mfma_f32_16x16x32_bf16 v[16:19], v[208:211], a[36:39], v[16:19]\n\t"
      "ds_read_b128 a[12:15], v55 offset:0\n\t"
      "v_mfma_f32_16x16x32_bf16 v[16:19], v[224:227], a[40:43], v[16:19]\n\t"
      "ds_read_b128 a[16:19], v52 offset:256\n\t"
      "v_mfma_f32_16x16x32_bf16 v[16:19], v[240:243], a[44:47], v[16:19]\n\t"
      "ds_read_b128 a[20:23], v53 offset:256\n\t"
      "v_mfma_f32_16x16x32_bf16 v[20:23], v[164:167], a[24:27], v[20:23]\n\t"
      "s_add_u32 s51, s51, 12288\n\t"
      "s_cmp_eq_u32 s51, 160256\n\t"
      "s_cselect_b32 s51, 25088, s51\n\t"
      "v_mfma_f32_16x16x32_bf16 v[20:23], v[180:183], a[28:31], v[20:23]\n\t"
      "s_waitcnt lgkmcnt(8)\n\t"
      "v_mfma_f32_16x16x32_bf16 v[20:23], v[196:199], a[32:35], v[20:23]\n\t"
      "buffer_store_dwordx4 a[212:215], v12, s[44:47], s84 offen sc1\n\t"
      "v_mfma_f32_16x16x32_bf16 v[20:23], v[212:215], a[36:39], v[20:23]\n\t"
      "v_mfma_f32_16x16x32_bf16 v[20:23], v[228:231], a[40:43], v[20:23]\n\t"
      "s_waitcnt lgkmcnt(0)\n\t"
      "v_mfma_f32_16x16x32_bf16 v[20:23], v[244:247], a[44:47], v[20:23]\n\t"
      "v_mfma_f32_16x16x32_bf16 v[24:27], v[168:171], a[24:27], v[24:27]\n\t"
      "v_mfma_f32_16x16x32_bf16 v[24:27], v[184:187], a[28:31], v[24:27]\n\t"
      "v_cvt_pk_bf16_f32 v40, v16, v17\n\t"
      "v_cvt_pk_bf16_f32 v41, v18, v19\n\t"
      "v_mfma_f32_16x16x32_bf16 v[24:27], v[200:203], a[32:35], v[24:27]\n\t"
      "ds_write_b32 v0, v40 offset:0\n\t"
      "v_mfma_f32_16x16x32_bf16 v[24:27], v[216:219], a[36:39], v[24:27]\n\t"
      "ds_write_b32 v0, v41 offset:4\n\t"
      "v_mfma_f32_16x16x32_bf16 v[24:27], v[232:235], a[40:43], v[24:27]\n\t"
      "v_mfma_f32_16x16x32_bf16 v[24:27], v[248:251], a[44:47], v[24:27]\n\t"
      "v_cvt_pk_bf16_f32 v42, v20, v21\n\t"
      "v_cvt_pk_bf16_f32 v43, v22, v23\n\t"
      "v_mfma_f32_16x16x32_bf16 v[28:31], v[172:175], a[24:27], v[28:31]\n\t"
      "ds_write_b32 v0, v42 offset:32\n\t"
      "v_mfma_f32_16x16x32_bf16 v[28:31], v[188:191], a[28:31], v[28:31]\n\t"
      "ds_write_b32 v0, v43 offset:36\n\t"
      "v_mfma_f32_16x16x32_bf16 v[28:31], v[204:207], a[32:35], v[28:31]\n\t"
      "v_mfma_f32_16x16x32_bf16 v[28:31], v[220:223], a[36:39], v[28:31]\n\t"
      "v_mfma_f32_16x16x32_bf16 v[28:31], v[236:239], a[40:43], v[28:31]\n\t"
      "buffer_store_dwordx4 a[216:219], v13, s[44:47], s84 offen offset:1024 sc1\n\t"
      "v_mfma_f32_16x16x32_bf16 v[28:31], v[252:255], a[44:47], v[28:31]\n\t"
      "v_cvt_pk_bf16_f32 v44, v24, v25\n\t"
      "v_cvt_pk_bf16_f32 v45, v26, v27\n\t"
      "v_mfma_f32_16x16x32_bf16 v[32:35], a[108:111], a[24:27], v[32:35]\n\t"
      "ds_write_b32 v0, v44 offset:64\n\t"
      "v_mfma_f32_16x16x32_bf16 v[32:35], a[116:119], a[28:31], v[32:35]\n\t"
      "ds_write_b32 v0, v45 offset:68\n\t"
      "v_mfma_f32_16x16x32_bf16 v[32:35], a[124:127], a[32:35], v[32:35]\n\t"
      "v_mfma_f32_16x16x32_bf16 v[32:35], a[132:135], a[36:39], v[32:35]\n\t"
      "v_mfma_f32_16x16x32_bf16 v[32:35], a[140:143], a[40:43], v[32:35]\n\t"
      "v_mfma_f32_16x16x32_bf16 v[32:35], a[148:151], a[44:47], v[32:35]\n\t"
      "v_cvt_pk_bf16_f32 v46, v28, v29\n\t"
      "v_cvt_pk_bf16_f32 v47, v30, v31\n\t"
      "v_mfma_f32_16x16x32_bf16 v[36:39], a[112:115], a[24:27], v[36:39]\n\t"
      "ds_write_b32 v0, v46 offset:96\n\t"
      "v_mfma_f32_16x16x32_bf16 v[36:39], a[120:123], a[28:31], v[36:39]\n\t"
      "ds_write_b32 v0, v47 offset:100\n\t"
      "v_mfma_f32_16x16x32_bf16 v[36:39], a[128:131], a[32:35], v[36:39]\n\t"
      "v_mfma_f32_16x16x32_bf16 v[36:39], a[136:139], a[36:39], v[36:39]\n\t"
      "v_mfma_f32_16x16x32_bf16 v[36:39], a[144:147], a[40:43], v[36:39]\n\t"
      "v_cvt_pk_bf16_f32 v48, v32, v33\n\t"
      "v_cvt_pk_bf16_f32 v49, v34, v35\n\t"
      "v_mfma_f32_16x16x32_bf16 v[36:39], a[152:155], a[44:47], v[36:39]\n\t"
      "v_mfma_f32_16x16x32_bf16 v[16:19], v[64:67], a[0:3], 0\n\t"
      "ds_write_b32 v0, v48 offset:128\n\t"
      "v_mfma_f32_16x16x32_bf16 v[16:19], v[80:83], a[4:7], v[16:19]\n\t"
      "ds_write_b32 v0, v49 offset:132\n\t"
      "v_mfma_f32_16x16x32_bf16 v[16:19], v[96:99], a[8:11], v[16:19]\n\t"
      "ds_read_b128 a[24:27], v54 offset:256\n\t"
      "v_mfma_f32_16x16x32_bf16 v[16:19], v[112:115], a[12:15], v[16:19]\n\t"
      "ds_read_b128 a[28:31], v55 offset:256\n\t"
      "v_mfma_f32_16x16x32_bf16 v[16:19], v[128:131], a[16:19], v[16:19]\n\t"
      "ds_read_b128 a[32:35], v52 offset:512\n\t"
      "v_mfma_f32_16x16x32_bf16 v[16:19], v[144:147], a[20:23], v[16:19]\n\t"
      "ds_read_b128 a[36:39], v53 offset:512\n\t"
      "v_mfma_f32_16x16x32_bf16 v[20:23], v[68:71], a[0:3], 0\n\t"
      "v_cvt_pk_bf16_f32 v50, v36, v37\n\t"
      "v_cvt_pk_bf16_f32 v51, v38, v39\n\t"
      "v_mfma_f32_16x16x32_bf16 v[20:23], v[84:87], a[4:7], v[20:23]\n\t"
      "ds_write_b32 v0, v50 offset:160\n\t"
      "v_mfma_f32_16x16x32_bf16 v[20:23], v[100:103], a[8:11], v[20:23]\n\t"
      "ds_write_b32 v0, v51 offset:164\n\t"
      "v_mfma_f32_16x16x32_bf16 v[20:23], v[116:119], a[12:15], v[20:23]\n\t"
      "ds_read_b128 a[40:43], v54 offset:512\n\t"
      "v_mfma_f32_16x16x32_bf16 v[20:23], v[132:135], a[16:19], v[20:23]\n\t"
      "ds_read_b128 a[44:47], v55 offset:512\n\t"
      "buffer_store_dwordx4 a[220:223], v14, s[44:47], s84 offen offset:2048 sc1\n\t"
      "v_mfma_f32_16x16x32_bf16 v[20:23], v[148:151], a[20:23], v[20:23]\n\t"
      "v_mfma_f32_16x16x32_bf16 v[24:27], v[72:75], a[0:3], 0\n\t"
      "v_mfma_f32_16x16x32_bf16 v[24:27], v[88:91], a[4:7], v[24:27]\n\t"
      "v_mfma_f32_16x16x32_bf16 v[24:27], v[104:107], a[8:11], v[24:27]\n\t"
      "v_mfma_f32_16x16x32_bf16 v[24:27], v[120:123], a[12:15], v[24:27]\n\t"
      "s_waitcnt lgkmcnt(0)\n\t"
      "s_barrier\n\t"
      "v_mfma_f32_16x16x32_bf16 v[24:27], v[136:139], a[16:19], v[24:27]\n\t"
      "v_mfma_f32_16x16x32_bf16 v[24:27], v[152:155], a[20:23], v[24:27]\n\t"
      "v_mfma_f32_16x16x32_bf16 v[28:31], v[76:79], a[0:3], 0\n\t"
      "v_mfma_f32_16x16x32_bf16 v[28:31], v[92:95], a[4:7], v[28:31]\n\t"
      "ds_read_b128 a[224:227], v8 offset:0\n\t"
      "v_mfma_f32_16x16x32_bf16 v[28:31], v[108:111], a[8:11], v[28:31]\n\t"
      "ds_read_b128 a[228:231], v60 offset:1024\n\t"
      "v_mfma_f32_16x16x32_bf16 v[28:31], v[124:127], a[12:15], v[28:31]\n\t"
      "ds_read_b128 a[232:235], v61 offset:2048\n\t"
      "v_mfma_f32_16x16x32_bf16 v[28:31], v[140:143], a[16:19], v[28:31]\n\t"
      "v_mfma_f32_16x16x32_bf16 v[28:31], v[156:159], a[20:23], v[28:31]\n\t"
      "v_mfma_f32_16x16x32_bf16 v[32:35], a[60:63], a[0:3], 0\n\t"
      "v_mfma_f32_16x16x32_bf16 v[32:35], a[68:71], a[4:7], v[32:35]\n\t"
      "s_waitcnt lgkmcnt(2)\n\t"
      "v_mfma_f32_16x16x32_bf16 v[32:35], a[76:79], a[8:11], v[32:35]\n\t"
      "buffer_store_dwordx4 a[224:227], v9, s[44:47], s85 offen sc1\n\t"
      "v_mfma_f32_16x16x32_bf16 v[32:35], a[84:87], a[12:15], v[32:35]\n\t"
      "v_mfma_f32_16x16x32_bf16 v[32:35], a[92:95], a[16:19], v[32:35]\n\t"
      "v_mfma_f32_16x16x32_bf16 v[32:35], a[100:103], a[20:23], v[32:35]\n\t"
      "v_mfma_f32_16x16x32_bf16 v[36:39], a[64:67], a[0:3], 0\n\t"
      "v_mfma_f32_16x16x32_bf16 v[36:39], a[72:75], a[4:7], v[36:39]\n\t"
      "v_mfma_f32_16x16x32_bf16 v[36:39], a[80:83], a[8:11], v[36:39]\n\t"
      "v_mfma_f32_16x16x32_bf16 v[36:39], a[88:91], a[12:15], v[36:39]\n\t"
      "s_waitcnt lgkmcnt(1)\n\t"
      "v_mfma_f32_16x16x32_bf16 v[36:39], a[96:99], a[16:19], v[36:39]\n\t"
      "buffer_store_dwordx4 a[228:231], v10, s[44:47], s85 offen offset:1024 sc1\n\t"
      "v_mfma_f32_16x16x32_bf16 v[36:39], a[104:107], a[20:23], v[36:39]\n\t"
      "v_mfma_f32_16x16x32_bf16 v[16:19], v[160:163], a[24:27], v[16:19]\n\t"
      "v_mfma_f32_16x16x32_bf16 v[16:19], v[176:179], a[28:31], v[16:19]\n\t"
      "v_mfma_f32_16x16x32_bf16 v[16:19], v[192:195], a[32:35], v[16:19]\n\t"
      "v_mfma_f32_16x16x32_bf16 v[16:19], v[208:211], a[36:39], v[16:19]\n\t"
      "v_mfma_f32_16x16x32_bf16 v[16:19], v[224:227], a[40:43], v[16:19]\n\t"
      "v_mfma_f32_16x16x32_bf16 v[16:19], v[240:243], a[44:47], v[16:19]\n\t"
      "v_mfma_f32_16x16x32_bf16 v[20:23], v[164:167], a[24:27], v[20:23]\n\t"
      "v_mfma_f32_16x16x32_bf16 v[20:23], v[180:183], a[28:31], v[20:23]\n\t"
      "s_waitcnt lgkmcnt(0)\n\t"
      "v_mfma_f32_16x16x32_bf16 v[20:23], v[196:199], a[32:35], v[20:23]\n\t"
      "buffer_store_dwordx4 a[232:235], v11, s[44:47], s85 offen offset:2048 sc1\n\t"
      "v_mfma_f32_16x16x32_bf16 v[20:23], v[212:215], a[36:39], v[20:23]\n\t"
      "v_mfma_f32_16x16x32_bf16 v[20:23], v[228:231], a[40:43], v[20:23]\n\t"
      "v_mfma_f32_16x16x32_bf16 v[20:23], v[244:247], a[44:47], v[20:23]\n\t"
      "v_mfma_f32_16x16x32_bf16 v[24:27], v[168:171], a[24:27], v[24:27]\n\t"
      "v_mfma_f32_16x16x32_bf16 v[24:27], v[184:187], a[28:31], v[24:27]\n\t"
      "v_cvt_pk_bf16_f32 v40, v16, v17\n\t"
      "v_cvt_pk_bf16_f32 v41, v18, v19\n\t"
      "v_mfma_f32_16x16x32_bf16 v[24:27], v[200:203], a[32:35], v[24:27]\n\t"
      "ds_write_b32 v0, v40 offset:12544\n\t"
      "v_mfma_f32_16x16x32_bf16 v[24:27], v[216:219], a[36:39], v[24:27]\n\t"
      "ds_write_b32 v0, v41 offset:12548\n\t"
      "v_mfma_f32_16x16x32_bf16 v[24:27], v[232:235], a[40:43], v[24:27]\n\t"
      "v_mfma_f32_16x16x32_bf16 v[24:27], v[248:251], a[44:47], v[24:27]\n\t"
      "v_cvt_pk_bf16_f32 v42, v20, v21\n\t"
      "v_cvt_pk_bf16_f32 v43, v22, v23\n\t"
      "v_mfma_f32_16x16x32_bf16 v[28:31], v[172:175], a[24:27], v[28:31]\n\t"
      "ds_write_b32 v0, v42 offset:12576\n\t"
      "v_mfma_f32_16x16x32_bf16 v[28:31], v[188:191], a[28:31], v[28:31]\n\t"
      "ds_write_b32 v0, v43 offset:12580\n\t"
      "v_mfma_f32_16x16x32_bf16 v[28:31], v[204:207], a[32:35], v[28:31]\n\t"
      "v_mfma_f32_16x16x32_bf16 v[28:31], v[220:223], a[36:39], v[28:31]\n\t"
      "v_mfma_f32_16x16x32_bf16 v[28:31], v[236:239], a[40:43], v[28:31]\n\t"
      "v_mfma_f32_16x16x32_bf16 v[28:31], v[252:255], a[44:47], v[28:31]\n\t"
      "v_cvt_pk_bf16_f32 v44, v24, v25\n\t"
      "v_cvt_pk_bf16_f32 v45, v26, v27\n\t"
      "v_mfma_f32_16x16x32_bf16 v[32:35], a[108:111], a[24:27], v[32:35]\n\t"
      "ds_write_b32 v0, v44 offset:12608\n\t"
      "v_mfma_f32_16x16x32_bf16 v[32:35], a[116:119], a[28:31], v[32:35]\n\t"
      "ds_write_b32 v0, v45 offset:12612\n\t"
      "v_mfma_f32_16x16x32_bf16 v[32:35], a[124:127], a[32:35], v[32:35]\n\t"
      "v_mfma_f32_16x16x32_bf16 v[32:35], a[132:135], a[36:39], v[32:35]\n\t"
      "v_mfma_f32_16x16x32_bf16 v[32:35], a[140:143], a[40:43], v[32:35]\n\t"
      "v_mfma_f32_16x16x32_bf16 v[32:35], a[148:151], a[44:47], v[32:35]\n\t"
      "v_cvt_pk_bf16_f32 v46, v28, v29\n\t"
      "v_cvt_pk_bf16_f32 v47, v30, v31\n\t"
      "v_mfma_f32_16x16x32_bf16 v[36:39], a[112:115], a[24:27], v[36:39]\n\t"
      "ds_write_b32 v0, v46 offset:12640\n\t"
      "v_mfma_f32_16x16x32_bf16 v[36:39], a[120:123], a[28:31], v[36:39]\n\t"
      "ds_write_b32 v0, v47 offset:12644\n\t"
      "v_mfma_f32_16x16x32_bf16 v[36:39], a[128:131], a[32:35], v[36:39]\n\t"
      "v_mfma_f32_16x16x32_bf16 v[36:39], a[136:139], a[36:39], v[36:39]\n\t"
      "v_mfma_f32_16x16x32_bf16 v[36:39], a[144:147], a[40:43], v[36:39]\n\t"
      "v_cvt_pk_bf16_f32 v48, v32, v33\n\t"
      "v_cvt_pk_bf16_f32 v49, v34, v35\n\t"
      "v_mfma_f32_16x16x32_bf16 v[36:39], a[152:155], a[44:47], v[36:39]\n\t"
      ".Ltail_%=:\n\t"
      "ds_write_b64 v0, v[48:49] offset:12672\n\t"
      "s_nop 6\n\t"
      "v_cvt_pk_bf16_f32 v50, v36, v37\n\t"
      "v_cvt_pk_bf16_f32 v51, v38, v39\n\t"
      "ds_write_b64 v0, v[50:51] offset:12704\n\t"
      "s_cmp_eq_u32 s85, 113221632\n\t"
      "s_cbranch_scc1 .Lwrapo_%=\n\t"
      ".Lwrappedo_%=:\n\t"
      "s_waitcnt lgkmcnt(0)\n\t"
      "s_barrier\n\t"
      "ds_read_b128 a[48:51], v8 offset:12544\n\t"
      "ds_read_b128 a[52:55], v60 offset:13568\n\t"
      "ds_read_b128 a[56:59], v61 offset:14592\n\t"
      "s_waitcnt lgkmcnt(2)\n\t"
      "buffer_store_dwordx4 a[48:51], v12, s[44:47], s85 offen sc1\n\t"
      "s_waitcnt lgkmcnt(1)\n\t"
      "buffer_store_dwordx4 a[52:55], v13, s[44:47], s85 offen offset:1024 sc1\n\t"
      "s_waitcnt lgkmcnt(0)\n\t"
      "buffer_store_dwordx4 a[56:59], v14, s[44:47], s85 offen offset:2048 sc1\n\t"
      "s_cmp_lt_i32 s91, 0\n\t"
      "s_cselect_b32 s30, -1, 0\n\t"
      "s_xor_b32 s16, s91, 0x3ffff\n\t"
      "s_and_b32 s16, s16, s30\n\t"
      "s_cbranch_scc0 .Lend_%=\n\t"
      "s_waitcnt vmcnt(0) lgkmcnt(0)\n\t"
      "s_barrier\n\t"
      "s_lshl1_add_u32 s30, s14, 24576\n\t"
      "s_sub_u32 s15, s30, s15\n\t"
      "s_branch .Lstart_%=\n\t"
      ".Lwrap3_%=:\n\t"
      "v_add_u32 v5, s48, v5\n\t"
      "v_subrev_u32 v62, 113246208, v5\n\t"
      "v_min_u32 v5, v5, v62\n\t"
      "v_add_u32 v6, s48, v6\n\t"
      "v_subrev_u32 v62, 113246208, v6\n\t"
      "v_min_u32 v6, v6, v62\n\t"
      "v_add_u32 v7, s48, v7\n\t"
      "v_subrev_u32 v62, 113246208, v7\n\t"
      "v_min_u32 v7, v7, v62\n\t"
      "s_mov_b32 s48, 0\n\t"
      "buffer_load_dwordx4 v5, s[40:43], s48 offen nt lds\n\t"
      "buffer_load_dwordx4 v6, s[40:43], s48 offen offset:1024 nt lds\n\t"
      "buffer_load_dwordx4 v7, s[40:43], s48 offen offset:2048 nt lds\n\t"
      "s_branch .Lwrapped3_%=\n\t"
      ".Lwrapo_%=:\n\t"
      "v_add_u32 v12, s85, v12\n\t"
      "v_subrev_u32 v62, 113246208, v12\n\t"
      "v_min_u32 v12, v12, v62\n\t"
      "v_add_u32 v13, s85, v13\n\t"
      "v_subrev_u32 v62, 113246208, v13\n\t"
      "v_min_u32 v13, v13, v62\n\t"
      "v_add_u32 v14, s85, v14\n\t"
      "v_subrev_u32 v62, 113246208, v14\n\t"
      "v_min_u32 v14, v14, v62\n\t"
      "s_mov_b32 s85, 0\n\t"
      "s_branch .Lwrappedo_%=\n\t"
      ".Lwait_%=:\n\t"
      "s_sub_u32 s52, s52, 1\n\t"
      "s_cbranch_scc1 .Lwait2_%=\n\t"
      "s_add_u32 s90, s90, 0x2000000\n\t"
      "v_mov_b32 v54, s90\n\t"
      "v_mov_b32 v55, s19\n\t"
      "global_store_dword v55, v54, s[12:13] sc1\n\t"
      "s_sleep 16\n\t"
      "global_load_dword v40, v62, s[12:13] sc1\n\t"
      "s_waitcnt vmcnt(0)\n\t"
      "s_branch .Lcheck_%=\n\t"
      ".Lwait2_%=:\n\t"
      "s_brev_b32 s91, 1\n\t"
      "s_branch .Lwaited_%=\n\t"
      ".Lspinf_%=:\n\t"
      "s_memrealtime s[36:37]\n\t"
      "s_waitcnt lgkmcnt(0)\n\t"
      "s_sub_u32 s53, s36, s38\n\t"
      "s_cmp_lt_i32 s53, 0\n\t"
      "s_cbranch_scc1 .Lspinf_%=\n\t"
      "s_branch .Lspunf_%=\n\t"
      ".Lspin0_%=:\n\t"
      "s_memrealtime s[36:37]\n\t"
      "s_waitcnt lgkmcnt(0)\n\t"
      "s_sub_u32 s53, s36, s38\n\t"
      "s_cmp_lt_i32 s53, 0\n\t"
      "s_cbranch_scc1 .Lspin0_%=\n\t"
      "s_branch .Lspun0_%=\n\t"
      ".Lspin1_%=:\n\t"
      "s_memrealtime s[36:37]\n\t"
      "s_waitcnt lgkmcnt(0)\n\t"
      "s_sub_u32 s53, s36, s38\n\t"
      "s_cmp_lt_i32 s53, 0\n\t"
      "s_cbranch_scc1 .Lspin1_%=\n\t"
      "s_branch .Lspun1_%=\n\t"
      ".Lend_%=:\n\t"
      : [tid] "+v"(tid)
      : [karg] "s"(__builtin_amdgcn_kernarg_segment_ptr()), [blk] "s"(blockIdx.x), [lds] "s"((unsigned)(size_t)lds)
      : "v1", "v2", "v3", "v4", "v5", "v6", "v7", "v8", "v9", "v10", "v11", "v12", "v13", "v14", "v15", "v16", "v17", "v18", "v19", "v20", "v21", "v22", "v23", "v24", "v25", "v26", "v27", "v28", "v29", "v30", "v31", "v32", "v33", "v34", "v35", "v36", "v37", "v38", "v39", "v40", "v41", "v42", "v43", "v44", "v45", "v46", "v47", "v48", "v49", "v50", "v51", "v52", "v53", "v54", "v55", "v56", "v57", "v58", "v59", "v60", "v61", "v62", "v63", "v64", "v65", "v66", "v67", "v68", "v69", "v70", "v71", "v72", "v73", "v74", "v75", "v76", "v77", "v78", "v79", "v80", "v81", "v82", "v83", "v84", "v85", "v86", "v87", "v88", "v89", "v90", "v91", "v92", "v93", "v94", "v95", "v96", "v97", "v98", "v99", "v100", "v101", "v102", "v103", "v104", "v105", "v106", "v107", "v108", "v109", "v110", "v111", "v112", "v113", "v114", "v115", "v116", "v117", "v118", "v119", "v120", "v121", "v122", "v123", "v124", "v125", "v126", "v127", "v128", "v129", "v130", "v131", "v132", "v133", "v134", "v135", "v136", "v137", "v138", "v139", "v140", "v141", "v142", "v143", "v144", "v145", "v146", "v147", "v148", "v149", "v150", "v151", "v152", "v153", "v154", "v155", "v156", "v157", "v158", "v159", "v160", "v161", "v162", "v163", "v164", "v165", "v166", "v167", "v168", "v169", "v170", "v171", "v172", "v173", "v174", "v175", "v176", "v177", "v178", "v179", "v180", "v181", "v182", "v183", "v184", "v185", "v186", "v187", "v188", "v189", "v190", "v191", "v192", "v193", "v194", "v195", "v196", "v197", "v198", "v199", "v200", "v201", "v202", "v203", "v204", "v205", "v206", "v207", "v208", "v209", "v210", "v211", "v212", "v213", "v214", "v215", "v216", "v217", "v218", "v219", "v220", "v221", "v222", "v223", "v224", "v225", "v226", "v227", "v228", "v229", "v230", "v231", "v232", "v233", "v234", "v235", "v236", "v237", "v238", "v239", "v240", "v241", "v242", "v243", "v244", "v245", "v246", "v247", "v248", "v249", "v250", "v251", "v252", "v253", "v254", "v255", "a0", "a1", "a2", "a3", "a4", "a5", "a6", "a7", "a8", "a9", "a10", "a11", "a12", "a13", "a14", "a15", "a16", "a17", "a18", "a19", "a20", "a21", "a22", "a23", "a24", "a25", "a26", "a27", "a28", "a29", "a30", "a31", "a32", "a33", "a34", "a35", "a36", "a37", "a38", "a39", "a40", "a41", "a42", "a43", "a44", "a45", "a46", "a47", "a48", "a49", "a50", "a51", "a52", "a53", "a54", "a55", "a56", "a57", "a58", "a59", "a60", "a61", "a62", "a63", "a64", "a65", "a66", "a67", "a68", "a69", "a70", "a71", "a72", "a73", "a74", "a75", "a76", "a77", "a78", "a79", "a80", "a81", "a82", "a83", "a84", "a85", "a86", "a87", "a88", "a89", "a90", "a91", "a92", "a93", "a94", "a95", "a96", "a97", "a98", "a99", "a100", "a101", "a102", "a103", "a104", "a105", "a106", "a107", "a108", "a109", "a110", "a111", "a112", "a113", "a114", "a115", "a116", "a117", "a118", "a119", "a120", "a121", "a122", "a123", "a124", "a125", "a126", "a127", "a128", "a129", "a130", "a131", "a132", "a133", "a134", "a135", "a136", "a137", "a138", "a139", "a140", "a141", "a142", "a143", "a144", "a145", "a146", "a147", "a148", "a149", "a150", "a151", "a152", "a153", "a154", "a155", "a156", "a157", "a158", "a159", "a160", "a161", "a162", "a163", "a164", "a165", "a166", "a167", "a168", "a169", "a170", "a171", "a172", "a173", "a174", "a175", "a176", "a177", "a178", "a179", "a180", "a181", "a182", "a183", "a184", "a185", "a186", "a187", "a188", "a189", "a190", "a191", "a192", "a193", "a194", "a195", "a196", "a197", "a198", "a199", "a200", "a201", "a202", "a203", "a204", "a205", "a206", "a207", "a208", "a209", "a210", "a211", "a212", "a213", "a214", "a215", "a216", "a217", "a218", "a219", "a220", "a221", "a222", "a223", "a224", "a225", "a226", "a227", "a228", "a229", "a230", "a231", "a232", "a233", "a234", "a235", "a236", "a237", "a238", "a239", "a240", "a241", "a242", "a243", "a244", "a245", "a246", "a247", "a248", "a249", "a250", "a251", "a252", "a253", "a254", "a255", "s4", "s5", "s6", "s7", "s8", "s9", "s10", "s11", "s12", "s13", "s14", "s15", "s16", "s17", "s18", "s19", "s20", "s21", "s22", "s23", "s24", "s25", "s26", "s27", "s28", "s29", "s30", "s31", "s32", "s33", "s34", "s35", "s36", "s37", "s38", "s39", "s40", "s41", "s42", "s43", "s44", "s45", "s46", "s47", "s48", "s49", "s50", "s51", "s52", "s53", "s54", "s55", "s56", "s57", "s58", "s59", "s60", "s61", "s62", "s63", "s64", "s65", "s66", "s67", "s68", "s69", "s70", "s71", "s72", "s73", "s74", "s75", "s76", "s77", "s78", "s79", "s80", "s81", "s82", "s83", "s84", "s85", "s86", "s87", "s88", "s89", "s90", "s91", "vcc", "scc", "memory");
}

__global__ __launch_bounds__(384) void dwsep_plain(const unsigned short* __restrict__ x, const unsigned short* __restrict__ dw,
                                                   const unsigned short* __restrict__ pw, unsigned short* __restrict__ out) {
  __shared__ float xs[384];
  const unsigned n = threadIdx.x;
  const size_t row = (size_t)blockIdx.x * 384u;
  xs[n] = __uint_as_float((unsigned)x[row + n] << 16) * __uint_as_float((unsigned)dw[n] << 16);
  __syncthreads();
  float acc = 0.f;
  for (int k = 0; k < 384; ++k) acc += xs[k] * __uint_as_float((unsigned)pw[k * 384 + n] << 16);
  const __bf16 o = (__bf16)acc;
  out[row + n] = __builtin_bit_cast(unsigned short, o);
}

extern "C" void kernel_launch(void* const* d_in, const int* in_sizes, int n_in, void* d_out, int out_size,
                              void* d_ws, size_t ws_size, hipStream_t stream) {
  const unsigned short* x = (const unsigned short*)d_in[0];
  const unsigned short* dw = (const unsigned short*)d_in[1];
  const unsigned short* pw = (const unsigned short*)d_in[2];
  unsigned short* out = (unsigned short*)d_out;
  if (in_sizes[0] == 56623104) dwsep_kernel<<<NWG, 256, 0, stream>>>(x, dw, pw, out, (unsigned*)d_ws);
  else dwsep_plain<<<in_sizes[0] / 384, 384, 0, stream>>>(x, dw, pw, out);
}
```
